```python
import torch

def _softmax(x, axis=-1):
    return torch.softmax(x, dim=axis)
def _gelu(x):
    return torch.nn.functional.gelu(x, approximate="tanh")
def _silu(x):
    return torch.nn.functional.silu(x)
def _sigmoid(x):
    return torch.sigmoid(x)
def _layernorm(x, axis=-1, eps=1e-5):
    m = x.mean(dim=axis, keepdim=True)
    v = x.var(dim=axis, keepdim=True, unbiased=False)
    return (x - m) / torch.sqrt(v + eps)
def _scatter_add(tgt, idx, src, axis=0):
    return tgt.clone().index_add_(axis, idx, src)
def _segment_sum(src, seg, n):
    out = torch.zeros((n,) + src.shape[1:], dtype=src.dtype)
    return out.index_add_(0, seg, src)
def _pad(x, pw):
    flat = tuple(v for pair in reversed(pw) for v in pair)
    return torch.nn.functional.pad(x, flat)

kernel_name = 'interleave_layers__sliding_local_dilated__dense_activation_squared_relu__prompt'

def setup_inputs():

    _w = (lambda a: (lambda t: t.to(torch.bfloat16) if t.is_floating_point() else t)(torch.as_tensor(a)))
    def _A_setup():
        B, S, D = 1, 2048, 1024
        return {
            "x": _w((lambda *s: torch.randn(*s, dtype=torch.bfloat16) * 0.05)(B, S, D)),
            "wqkv": _w((lambda *s: torch.randn(*s, dtype=torch.bfloat16) * 0.05)(D, 3 * D)),
            "wo": _w((lambda *s: torch.randn(*s, dtype=torch.bfloat16) * 0.05)(D, D)),
        }
    _A_in = _A_setup()

    def _B_setup():
        B, S, D, F = 1, 2048, 1024, 3584
        return {
            "x": _w((lambda *s: torch.randn(*s, dtype=torch.bfloat16) * 0.05)(B, S, D)),
            "w1": _w((lambda *s: torch.randn(*s, dtype=torch.bfloat16) * 0.05)(D, F)),
            "b1": _w((lambda *s: torch.randn(*s, dtype=torch.bfloat16) * 0.2)(F)),
            "w2": _w((lambda *s: torch.randn(*s, dtype=torch.bfloat16) * 0.05)(F, D)),
            "b2": _w((lambda *s: torch.randn(*s, dtype=torch.bfloat16) * 1.6)(D)),
        }
    _B_in = _B_setup()

    return {
        "A_x": _A_in["x"],
        "A_wqkv": _A_in["wqkv"],
        "A_wo": _A_in["wo"],
        "B_x": _B_in["x"],
        "B_w1": _B_in["w1"],
        "B_b1": _B_in["b1"],
        "B_w2": _B_in["w2"],
        "B_b2": _B_in["b2"],
    }

def reference(A_x, A_wqkv, A_wo, B_x, B_w1, B_b1, B_w2, B_b2):
    def _A_ref(x, wqkv, wo):
        B, S, D = x.shape
        H = 32; Dh = D // H
        W, dil = 8, 2
        qkv = (lambda x, *d: x.permute(*d))((x @ wqkv).reshape(B, S, 3, H, Dh), 2, 0, 3, 1, 4)
        q, k, v = qkv[0], qkv[1], qkv[2]
        scale = Dh ** -0.5
        pos = torch.arange(S)
        off = torch.arange(W) * dil
        idx = pos[:, None] - off[None, :]
        mask = idx >= 0
        idx_c = torch.where(mask, idx, 0).reshape(-1)
        kg = k[:, :, idx_c, :].reshape(B, H, S, W, Dh)
        vg = v[:, :, idx_c, :].reshape(B, H, S, W, Dh)
        logits = torch.einsum("bhsd,bhswd->bhsw", q, kg) * scale
        logits = torch.where(mask[None, None, :, :], logits, -1e9)
        attn = _softmax(logits, axis=-1)
        o = torch.einsum("bhsw,bhswd->bhsd", attn, vg)
        return (lambda x, *d: x.permute(*d))(o, 0, 2, 1, 3).reshape(B, S, D) @ wo

    def _B_ref(x, w1, b1, w2, b2):
        h = torch.einsum("bsd,df->bsf", x, w1) + b1
        zero = torch.zeros_like(h)
        r = torch.where(h > zero, h, zero)
        h = r * r
        out = torch.einsum("bsf,fd->bsd", h, w2) + b2
        return out

    x = A_x
    x = x + _A_ref(_layernorm(x, axis=-1), A_wqkv, A_wo)
    x = x + _B_ref(_layernorm(x, axis=-1), B_w1, B_b1, B_w2, B_b2)
    x = x + _A_ref(_layernorm(x, axis=-1), A_wqkv, A_wo)
    x = x + _B_ref(_layernorm(x, axis=-1), B_w1, B_b1, B_w2, B_b2)
    return x
```

```cpp
#include <hip/hip_runtime.h>
#include <hip/hip_bf16.h>
#include <stdint.h>

typedef __attribute__((ext_vector_type(8))) short bf16x8;
typedef __attribute__((ext_vector_type(4))) short s16x4;
typedef __attribute__((ext_vector_type(4))) float f32x4;
typedef __attribute__((ext_vector_type(2))) float f32x2;
typedef __attribute__((ext_vector_type(4))) unsigned u32x4;
typedef __attribute__((ext_vector_type(2))) unsigned u32x2;
typedef __attribute__((ext_vector_type(2))) __bf16 bf2_t;
typedef unsigned short u16;
typedef __amdgpu_buffer_rsrc_t rsrc_t;

#define S_LEN 2048
#define D_MODEL 1024
#define F_DIM 3584
#define N_QKV 3072
#define NLAYERS 2

constexpr unsigned WS_EP = 0;
constexpr unsigned WS_IF = 4096;
constexpr unsigned WS_PC = WS_IF + 2 * 4096;
constexpr unsigned WS_L0 = 1u << 20;
static_assert(WS_PC + 16 * 416 * 64 <= WS_L0, "workspace");
constexpr unsigned XB = S_LEN * D_MODEL * 2, STB = S_LEN * 128;
constexpr unsigned L_XIN = 0;
constexpr unsigned L_STIN = L_XIN + XB;
constexpr unsigned L_OB = L_STIN + STB;
constexpr unsigned L_HALO = L_OB + XB;
constexpr unsigned L_FH = L_HALO + 256 * 4096;
constexpr unsigned L_CN = L_FH + 256 * 8 * 128;
#define CNS 1024
constexpr unsigned CNB = 16 * CNS;
constexpr unsigned L_X1 = L_CN + 3 * CNB;
constexpr unsigned L_ST1 = L_X1 + XB;
constexpr unsigned L_HB = L_ST1 + STB;
constexpr unsigned L_P = L_HB + S_LEN * F_DIM * 2;
constexpr unsigned LSTR = L_P + 256 * 49152;
constexpr unsigned WS_END = WS_L0 + NLAYERS * LSTR;
static_assert((size_t)WS_END * 2 <= (256u << 20), "workspace");

constexpr int HALF = 81920;
constexpr int LDS_BYTES = 2 * HALF;
constexpr int TAB_OFF = 15 * 4096;
constexpr int XCH_OFF = 16 * 4096;
constexpr int KIM_OFF = 13 * 4096;

__device__ __forceinline__ float bf2f(u16 v) { return __uint_as_float(((unsigned)v) << 16); }
__device__ __forceinline__ unsigned pk2(float a, float b) {
  return __builtin_bit_cast(unsigned, __builtin_convertvector((f32x2){a, b}, bf2_t));
}
__device__ __forceinline__ unsigned pk2v(f32x2 v) { return __builtin_bit_cast(unsigned, __builtin_convertvector(v, bf2_t)); }
__device__ __forceinline__ float lo2f(unsigned v) { return __uint_as_float(v << 16); }
__device__ __forceinline__ float hi2f(unsigned v) { return __uint_as_float(v & 0xffff0000u); }
__device__ __forceinline__ float rows_sum(float v) {
  const auto a = __builtin_amdgcn_permlane16_swap(__float_as_uint(v), __float_as_uint(v), false, false);
  const float m = __uint_as_float(a[0]) + __uint_as_float(a[1]);
  const auto b = __builtin_amdgcn_permlane32_swap(__float_as_uint(m), __float_as_uint(m), false, false);
  return __uint_as_float(b[0]) + __uint_as_float(b[1]);
}

template <int D> __device__ __forceinline__ float half_sum(float v) {
  if constexpr (D <= 8) return v + __int_as_float(__builtin_amdgcn_update_dpp(0, __float_as_int(v), 0x120 + D, 0xF, 0xF, true));
  else {
    const auto a = D == 16 ? __builtin_amdgcn_permlane16_swap(__float_as_uint(v), __float_as_uint(v), false, false)
                           : __builtin_amdgcn_permlane32_swap(__float_as_uint(v), __float_as_uint(v), false, false);
    return __uint_as_float(a[0]) + __uint_as_float(a[1]);
  }
}

#define PIN(x) asm volatile("" : "+v"(x))
#define PINS(x) asm volatile("" : "+s"(x))
#define WLG(n) __builtin_amdgcn_s_waitcnt(0xC07F | ((n) << 8))
#define WVM(n) __builtin_amdgcn_s_waitcnt(0x0F70 | ((n) & 15) | (((n) >> 4) << 14))
#define BAR() asm volatile("s_barrier" ::: "memory")
#define PRIO(n) __builtin_amdgcn_s_setprio(n)
#define NOP(n) asm volatile("s_nop %0" ::"n"(n))
#define DMA(r, vw, sb, off, ldoff) do { unsigned t_; asm volatile("s_add_u32 m0, %1, %2\n\ts_add_u32 %0, %3, %4\n\tbuffer_load_dwordx4 %5, %6, %0 offen lds" \
    : "=&s"(t_) : "s"(sld), "n"(ldoff), "s"(sb), "n"(off), "v"(vw), "s"(r) : "memory", "scc"); } while (0)
#define GLX(d, vo, r, sb, off) u32x4 d; do { unsigned t_; asm volatile("s_add_u32 %1, %2, %3\n\tbuffer_load_dwordx4 %0, %4, %5, %1 offen" \
    : "=v"(d), "=&s"(t_) : "s"(sb), "n"(off), "v"(vo), "s"(r) : "memory", "scc"); } while (0)
#define TR2(xo, yo, x, y, QP, MASK) u32x4 xo, yo; { unsigned a0_, a1_, a2_, a3_, b0_, b1_, b2_, b3_; \
    asm volatile("s_mov_b32 vcc_lo, " MASK "\n\ts_mov_b32 vcc_hi, " MASK "\n\t" \
      "v_cndmask_b32_dpp %4, %8, %12, vcc quad_perm:" QP " row_mask:0xf bank_mask:0xf\n\tv_cndmask_b32_dpp %5, %9, %13, vcc quad_perm:" QP " row_mask:0xf bank_mask:0xf\n\t" \
      "v_cndmask_b32_dpp %6, %10, %14, vcc quad_perm:" QP " row_mask:0xf bank_mask:0xf\n\tv_cndmask_b32_dpp %7, %11, %15, vcc quad_perm:" QP " row_mask:0xf bank_mask:0xf\n\t" \
      "s_not_b64 vcc, vcc\n\t" \
      "v_cndmask_b32_dpp %0, %12, %8, vcc quad_perm:" QP " row_mask:0xf bank_mask:0xf\n\tv_cndmask_b32_dpp %1, %13, %9, vcc quad_perm:" QP " row_mask:0xf bank_mask:0xf\n\t" \
      "v_cndmask_b32_dpp %2, %14, %10, vcc quad_perm:" QP " row_mask:0xf bank_mask:0xf\n\tv_cndmask_b32_dpp %3, %15, %11, vcc quad_perm:" QP " row_mask:0xf bank_mask:0xf\n\ts_nop 1" \
      : "=&v"(a0_), "=&v"(a1_), "=&v"(a2_), "=&v"(a3_), "=&v"(b0_), "=&v"(b1_), "=&v"(b2_), "=&v"(b3_) \
      : "v"(x[0]), "v"(x[1]), "v"(x[2]), "v"(x[3]), "v"(y[0]), "v"(y[1]), "v"(y[2]), "v"(y[3]) : "vcc", "scc"); \
    xo = (u32x4){a0_, a1_, a2_, a3_}; yo = (u32x4){b0_, b1_, b2_, b3_}; }
#define SWP(op, x, y) { unsigned a0_ = x[0], a1_ = x[1], a2_ = x[2], a3_ = x[3], b0_ = y[0], b1_ = y[1], b2_ = y[2], b3_ = y[3]; \
    asm volatile(op " %0, %4\n\t" op " %1, %5\n\t" op " %2, %6\n\t" op " %3, %7\n\ts_nop 1" \
      : "+v"(a0_), "+v"(a1_), "+v"(a2_), "+v"(a3_), "+v"(b0_), "+v"(b1_), "+v"(b2_), "+v"(b3_)); \
    x = (u32x4){a0_, a1_, a2_, a3_}; y = (u32x4){b0_, b1_, b2_, b3_}; }
#define SW16(x, y) SWP("v_permlane16_swap_b32", x, y)
#define SW32(x, y) SWP("v_permlane32_swap_b32", x, y)
#define TRA(xo, yo, x, y) TR2(xo, yo, x, y, "[1,0,3,2]", "0xaaaaaaaa")
#define TRB(xo, yo, x, y) TR2(xo, yo, x, y, "[2,3,0,1]", "0xcccccccc")
#define TOUCH(r, vo) asm volatile("s_mov_b32 m0, %0\n\ts_nop 4\n\tbuffer_load_dword %1, %2, 0 offen lds" :: "s"(lds0 + TAB_OFF + 2048), "v"(vo), "s"(r) : "memory")
#define RDT(d, base, off) s16x4 d; asm volatile("ds_read_b64_tr_b16 %0, %1 offset:%2" : "=v"(d) : "v"(base), "n"(off))
#define RDN(d, base, off) asm volatile("ds_read_b64_tr_b16 %0, %1 offset:%2" : "=v"(d) : "v"(base), "n"(off))
#define RDF(d, base, off) f32x4 d; asm volatile("ds_read_b128 %0, %1 offset:%2" : "=v"(d) : "v"(base), "n"(off))
#define RD8(d, base, off) u32x2 d; asm volatile("ds_read_b64 %0, %1 offset:%2" : "=v"(d) : "v"(base), "n"(off))
#define RDB(d, base, off) bf16x8 d; asm volatile("ds_read_b128 %0, %1 offset:%2" : "=v"(d) : "v"(base), "n"(off))
#define WR(d, base, off) asm volatile("ds_write_b128 %0, %1 offset:%2" ::"v"(base), "v"(d), "n"(off) : "memory")
#define WR1(d, base, off) asm volatile("ds_write_b32 %0, %1 offset:%2" ::"v"(base), "v"(d), "n"(off) : "memory")
#define CAT8(l, h) ((bf16x8){l[0], l[1], l[2], l[3], h[0], h[1], h[2], h[3]})
#define MF0(c, l, h, a) f32x4 c; asm volatile("v_mfma_f32_16x16x32_bf16 %0, %1, %2, 0" : "=v"(c) : "v"(CAT8(l, h)), "v"(a))
#define MF(c, l, h, a) asm volatile("v_mfma_f32_16x16x32_bf16 %0, %1, %2, %0" : "+v"(c) : "v"(CAT8(l, h)), "v"(a))
#define GLD(d, vo, sb, off) u32x4 d; do { unsigned t_; asm volatile("s_add_u32 %1, %2, %3\n\tbuffer_load_dwordx4 %0, %4, %5, %1 offen sc1" \
    : "=v"(d), "=&s"(t_) : "s"(sb), "n"(off), "v"(vo), "s"(rS) : "memory", "scc"); } while (0)
#define GRL(d, vo, sb, off) do { unsigned t_; asm volatile("s_add_u32 %1, %2, %3\n\tbuffer_load_dwordx4 %0, %4, %5, %1 offen sc1" \
    : "+v"(d), "=&s"(t_) : "s"(sb), "n"(off), "v"(vo), "s"(rS) : "memory", "scc"); } while (0)
#define GLA(d, vo, r, sb, off) u32x4 d; asm volatile("buffer_load_dwordx4 %0, %1, %2, %3 offen offset:%4" : "=v"(d) : "v"(vo), "s"(r), "s"(sb), "n"(off) : "memory")
#define GLW(d, vo, r, so) u32x4 d; asm volatile("s_nop 4\n\tbuffer_load_dwordx4 %0, %1, %2, %3 offen" : "=v"(d) : "v"(vo), "s"(r), "s"(so) : "memory")
#define GLH(d, vo, r) unsigned d; asm volatile("s_nop 4\n\tbuffer_load_ushort %0, %1, %2, 0 offen" : "=v"(d) : "v"(vo), "s"(r) : "memory")
#define GLD2(d, vo, r, sb) u32x2 d; asm volatile("s_nop 4\n\tbuffer_load_dwordx2 %0, %1, %2, %3 offen sc1" : "=v"(d) : "v"(vo), "s"(r), "s"(sb) : "memory")
#define GRL2(d, vo, r, sb) asm volatile("s_nop 4\n\tbuffer_load_dwordx2 %0, %1, %2, %3 offen sc1" : "+v"(d) : "v"(vo), "s"(r), "s"(sb) : "memory")
#define GTAG(vo, sb) do { u32x2 v_ = {tag0, tag1}; PIN(v_); asm volatile("s_mov_b64 exec, 3\n\ts_nop 4\n\tbuffer_store_dwordx2 %0, %1, %2, %3 offen sc1\n\ts_mov_b64 exec, -1" :: "v"(v_), "v"(vo), "s"(rS), "s"(sb) : "memory"); } while (0)
#define SETUP_DONE(k) { IDS(); LANE(l_); const unsigned vo_ = l_ ? 2048 + (ct * 16 + panel) * 8 : tile * 8, st_ = wid == 0 ? WS_IF + (k) * 4096 : WS_END; GTAG(vo_, st_); }
#define CHKT(f, vo, sb) PIN(f); if (__builtin_expect(wid0 == 0 && ANY(f[0] != tag0 || f[1] != tag1), 0)) { \
    _Pragma("nounroll") for (int n_ = 0; n_ < SPIN_CAP; ++n_) { RETRY(8); __builtin_amdgcn_s_sleep(8); GRL2(f, vo, rS, sb); WVM(0); PIN(f); if (!ANY(f[0] != tag0 || f[1] != tag1)) break; } }
#define GL8(d, vo, sb, off) f32x2 d; do { unsigned t_; asm volatile("s_add_u32 %1, %2, %3\n\tbuffer_load_dwordx2 %0, %4, %5, %1 offen sc1" \
    : "=v"(d), "=&s"(t_) : "s"(sb), "n"(off), "v"(vo), "s"(rS) : "memory", "scc"); } while (0)
#define GLD1(d, vo, sb, off) unsigned d; do { unsigned t_; asm volatile("s_add_u32 %1, %2, %3\n\tbuffer_load_dword %0, %4, %5, %1 offen sc1" \
    : "=v"(d), "=&s"(t_) : "s"(sb), "n"(off), "v"(vo), "s"(rS) : "memory", "scc"); } while (0)
#define GRL1(d, vo, sb, off) do { unsigned t_; asm volatile("s_add_u32 %1, %2, %3\n\tbuffer_load_dword %0, %4, %5, %1 offen sc1" \
    : "+v"(d), "=&s"(t_) : "s"(sb), "n"(off), "v"(vo), "s"(rS) : "memory", "scc"); } while (0)
#define GST(d, vo, sb, off) do { unsigned t_; asm volatile("s_add_u32 %0, %1, %2\n\tbuffer_store_dwordx4 %3, %4, %5, %0 offen sc1\n\ts_nop 1" \
    : "=&s"(t_) : "s"(sb), "n"(off), "v"(d), "v"(vo), "s"(rS) : "memory", "scc"); } while (0)
#define GMK(vo, sb, off) do { u32x4 m_ = {~0u, ~0u, ~0u, ~0u}; PIN(m_); GST(m_, vo, sb, off); } while (0)
#define GST2(d, vo, sb, off) do { unsigned t_; asm volatile("s_add_u32 %0, %1, %2\n\ts_mov_b64 exec, 0xffff\n\tbuffer_store_dwordx2 %3, %4, %5, %0 offen sc1\n\ts_mov_b64 exec, -1" \
    : "=&s"(t_) : "s"(sb), "n"(off), "v"(d), "v"(vo), "s"(rS) : "memory", "scc"); } while (0)
#define GST1(d, sb, off) do { unsigned t_; asm volatile("s_add_u32 %0, %1, %2\n\ts_mov_b64 exec, 1\n\tbuffer_store_dwordx2 %3, off, %4, %0 sc1\n\ts_mov_b64 exec, -1" \
    : "=&s"(t_) : "s"(sb), "n"(off), "v"(d), "s"(rS) : "memory", "scc"); } while (0)
#define GSIG(val, sb, off) do { unsigned t_, v_; asm volatile("v_mov_b32 %1, %4\n\ts_add_u32 %0, %2, %3\n\ts_mov_b64 exec, 1\n\tbuffer_store_dword %1, off, %5, %0 sc1\n\ts_mov_b64 exec, -1" \
    : "=&s"(t_), "=&v"(v_) : "s"(sb), "n"(off), "n"(val), "s"(rS) : "memory", "scc"); } while (0)
#define V3(op, d, a, b, c) asm volatile(op " %0, %1, %2, %3" : "=v"(d) : "v"(a), "v"(b), "v"(c))
#define V2(op, d, a, b) asm volatile(op " %0, %1, %2" : "=v"(d) : "v"(a), "v"(b))
#define FMA(d, a, b, c) do { float r_; V3("v_fma_f32", r_, a, b, c); d = r_; } while (0)
#define MUL(d, a, b) do { float r_; V2("v_mul_f32", r_, a, b); d = r_; } while (0)
#define ADD(d, a, b) do { float r_; V2("v_add_f32", r_, a, b); d = r_; } while (0)
#define H0(v) ((f32x2){v[0], v[1]})
#define H1(v) ((f32x2){v[2], v[3]})
#define PFMA(d, a, b, c) V3("v_pk_fma_f32", d, a, b, c)
#define PMUL(d, a, b) V2("v_pk_mul_f32", d, a, b)
#define PADD(d, a, b) V2("v_pk_add_f32", d, a, b)
#define SQA(d, a) do { float r_; asm volatile("v_mul_f32_e64 %0, %1, |%1|" : "=v"(r_) : "v"(a)); d = r_; } while (0)
#define CVT(d, a, b) V2("v_cvt_pk_bf16_f32", d, a, b)
#define CLP(d) asm volatile("v_min_u32 %0, -2, %0" : "+v"(d))
#define RLU(d) asm volatile("v_pk_max_i16 %0, %0, 0" : "+v"(d))
#define ULO(d, a) asm volatile("v_lshlrev_b32 %0, 16, %1" : "=v"(d) : "v"(a))
#define UHI(d, a) asm volatile("v_and_b32 %0, 0xffff0000, %1" : "=v"(d) : "v"(a))
#define MX3(d, a, b, c) V3("v_max3_u32", d, a, b, c)
#define MX2(d, a, b) V2("v_max_u32", d, a, b)
#define SPIN_CAP 30000
#define ANY(c) (__builtin_amdgcn_ballot_w64(c) != 0ull)
__device__ __forceinline__ unsigned mx8(const u32x4& a, const u32x4& b) {
  return max(max(max(a[0], a[1]), max(a[2], a[3])), max(max(b[0], b[1]), max(b[2], b[3])));
}
#define CHK2(t, a, b, vo, sa, oa, sb, ob, id) if (__builtin_expect(ANY(t == ~0u), 0)) { \
    _Pragma("nounroll") for (int n_ = 0; n_ < SPIN_CAP; ++n_) { RETRY(id); __builtin_amdgcn_s_sleep(8); GRL(a, vo, sa, oa); GRL(b, vo, sb, ob); WVM(0); PIN(a); PIN(b); if (!ANY(mx8(a, b) == ~0u)) break; } \
    NOP(1); }
#define CHKF(f, need, vo, sb, off) PIN(f); if (__builtin_expect(ANY(f < need), 0)) { \
    _Pragma("nounroll") for (int n_ = 0; n_ < SPIN_CAP; ++n_) { RETRY(10 + need); __builtin_amdgcn_s_sleep(8); GRL1(f, vo, sb, off); WVM(0); PIN(f); if (!ANY(f < need)) break; } }
#define GADD(sb, off) do { unsigned t_, v_; asm volatile("v_mov_b32 %1, 1\n\ts_add_u32 %0, %2, %3\n\ts_mov_b64 exec, 1\n\tbuffer_atomic_add %1, off, %4, %0\n\ts_mov_b64 exec, -1" \
    : "=&s"(t_), "=&v"(v_) : "s"(sb), "n"(off), "s"(rS) : "memory", "scc"); } while (0)
#define GLC(d, sb, off) do { unsigned t_; asm volatile("s_add_u32 %1, %2, %3\n\tbuffer_load_dword %0, off, %4, %1 sc1\n\ts_waitcnt vmcnt(0)" \
    : "=v"(d), "=&s"(t_) : "s"(sb), "n"(off), "s"(rS) : "memory", "scc"); } while (0)
#define ARRIVE(off) if (wid0 == 0) GADD(scn, sL + off)
#define MEET(off) if (wid0 == 0) { unsigned c_; GLC(c_, scn, sL + off); \
    _Pragma("nounroll") for (int n_ = 0; n_ < SPIN_CAP && ANY(c_ < 16u); ++n_) { __builtin_amdgcn_s_sleep(PSL); GLC(c_, scn, sL + off); } }
#define PSL 2

#define STP(i)
#define STR(i)
#define STB(i)
#define RETRY(i)

__device__ __forceinline__ u32x4 clamp4(u32x4 v) {
  return (u32x4){min(v[0], 0xfffffffeu), min(v[1], 0xfffffffeu), min(v[2], 0xfffffffeu), min(v[3], 0xfffffffeu)};
}
__device__ __forceinline__ float vmax2(float a, float b) { return __builtin_amdgcn_fmed3f(a, b, __builtin_inff()); }
template <bool MAX> __device__ __forceinline__ void rows2(float& a, float& b) {
  const auto x = __builtin_amdgcn_permlane16_swap(__float_as_uint(a), __float_as_uint(b), false, false);
  float m = MAX ? vmax2(__uint_as_float(x[0]), __uint_as_float(x[1])) : __uint_as_float(x[0]) + __uint_as_float(x[1]);
  const auto y = __builtin_amdgcn_permlane32_swap(__float_as_uint(m), __float_as_uint(m), false, false);
  m = MAX ? vmax2(__uint_as_float(y[0]), __uint_as_float(y[1])) : __builtin_amdgcn_rcpf(__uint_as_float(y[0]) + __uint_as_float(y[1]));
  const auto z = __builtin_amdgcn_permlane16_swap(__float_as_uint(m), __float_as_uint(m), false, false);
  a = __uint_as_float(z[0]); b = __uint_as_float(z[1]);
}
__device__ __forceinline__ void attn2(u32x4 (&o)[2], const u32x4 (&q)[2], const bf16x8 (&kp)[2], const bf16x8 (&ko)[2], const bf16x8 (&vt)[2][2],
                                      const bool par, const bool own0, const bool own1, const bool noprev) {
  const f32x4 z = {0.f, 0.f, 0.f, 0.f};
  f32x4 s0[2], s1[2];
#pragma unroll
  for (int h = 0; h < 2; ++h) {
    s0[h] = __builtin_amdgcn_mfma_f32_16x16x32_bf16(kp[h], __builtin_bit_cast(bf16x8, q[h]), z, 0, 0, 0);
    s1[h] = __builtin_amdgcn_mfma_f32_16x16x32_bf16(ko[h], __builtin_bit_cast(bf16x8, q[h]), z, 0, 0, 0);
  }
  float l0[2], l1[2], mx[2];
#pragma unroll
  for (int h = 0; h < 2; ++h) {
    const float a0 = own0 ? (par ? s1[h][1] : s1[h][0]) : (par ? s0[h][1] : s0[h][0]);
    const float a1 = own1 ? (par ? s1[h][3] : s1[h][2]) : (par ? s0[h][3] : s0[h][2]);
    const unsigned r = pk2(a0, a1);
    const unsigned u = pk2(lo2f(r) * 0.17677669529663687f, hi2f(r) * 0.17677669529663687f);
    l0[h] = (noprev && !own0) ? -3.0e38f : lo2f(u); l1[h] = (noprev && !own1) ? -3.0e38f : hi2f(u);
    mx[h] = vmax2(l0[h], l1[h]);
  }
  rows2<true>(mx[0], mx[1]);
  float inv[2];
#pragma unroll
  for (int h = 0; h < 2; ++h) { l0[h] = __expf(l0[h] - mx[h]); l1[h] = __expf(l1[h] - mx[h]); inv[h] = l0[h] + l1[h]; }
  rows2<false>(inv[0], inv[1]);
  f32x4 o0[2], o1[2];
#pragma unroll
  for (int h = 0; h < 2; ++h) {
    const unsigned p = pk2(l0[h] * inv[h], l1[h] * inv[h]);
    const unsigned w0 = par ? (p << 16) : (p & 0xffffu), w1 = par ? (p & 0xffff0000u) : (p >> 16);
    const u32x4 pw = {own0 ? 0u : w0, own1 ? 0u : w1, own0 ? w0 : 0u, own1 ? w1 : 0u};
    o0[h] = __builtin_amdgcn_mfma_f32_16x16x32_bf16(vt[h][0], __builtin_bit_cast(bf16x8, pw), z, 0, 0, 0);
    o1[h] = __builtin_amdgcn_mfma_f32_16x16x32_bf16(vt[h][1], __builtin_bit_cast(bf16x8, pw), z, 0, 0, 0);
  }
#pragma unroll
  for (int h = 0; h < 2; ++h) o[h] = (u32x4){pk2(o0[h][0], o0[h][1]), pk2(o0[h][2], o0[h][3]), pk2(o1[h][0], o1[h][1]), pk2(o1[h][2], o1[h][3])};
}
__device__ __forceinline__ u32x4 resid_add(const f32x4 a, const f32x4 b, const u32x4 x) {
  const u32x4 o = {pk2(a[0], a[1]), pk2(a[2], a[3]), pk2(b[0], b[1]), pk2(b[2], b[3])};
  u32x4 r;
#pragma unroll
  for (int e = 0; e < 4; ++e) r[e] = pk2(lo2f(x[e]) + lo2f(o[e]), hi2f(x[e]) + hi2f(o[e]));
  return r;
}
__device__ __forceinline__ void row_stats(u32x4 y, float& s, float& ss) {
  const bf2_t one2 = {(__bf16)1.0f, (__bf16)1.0f};
  asm volatile("" : "+v"(y));
#pragma unroll
  for (int e = 0; e < 4; ++e) {
    const unsigned w = y[e];
    const bf2_t ob = __builtin_bit_cast(bf2_t, w);
    s = __builtin_amdgcn_fdot2_f32_bf16(ob, one2, s, false);
    ss = __builtin_amdgcn_fdot2_f32_bf16(ob, ob, ss, false);
  }
}

template <int NC, class F>
__device__ __forceinline__ unsigned colsum_off(const int rowbytes, F colbyte, int row0, int wid, int lane) {
  constexpr int NCH = NC / 8;
  const int c = lane % NCH, r2 = lane / NCH;
  return r2 < 2 ? (unsigned)((row0 + wid * 8 + r2 * 4) * rowbytes + colbyte(c * 8)) : 0x80000000u;
}
template <int NC>
__device__ __forceinline__ void colsum_put(const u32x4 v0, const u32x4 v1, const u32x4 v2, const u32x4 v3, float* scr, int wid, int lane) {
  constexpr int NCH = NC / 8;
  const int c = lane % NCH, r2 = lane / NCH;
  f32x4 lo, hi;
#pragma unroll
  for (int e = 0; e < 4; ++e) {
    const float a = (lo2f(v0[e]) + lo2f(v1[e])) + (lo2f(v2[e]) + lo2f(v3[e])), b = (hi2f(v0[e]) + hi2f(v1[e])) + (hi2f(v2[e]) + hi2f(v3[e]));
    if (e < 2) { lo[2 * e] = a; lo[2 * e + 1] = b; } else { hi[2 * e - 4] = a; hi[2 * e - 3] = b; }
  }
  if (r2 < 2) {
    f32x4* d = reinterpret_cast<f32x4*>(scr + (wid * 2 + r2) * 232 + c * 8);
    d[0] = lo; d[1] = hi;
  }
  asm volatile("s_waitcnt lgkmcnt(0)" ::: "memory");
}
template <int NC>
__device__ __forceinline__ void colsum_out(const rsrc_t rS, const float* scr, int wid, int lane, unsigned dst) {
  const int td = (wid - 4) * 64 + lane;
  if (td >= 0 && td < NC) {
    float t = 0.f;
#pragma unroll
    for (int g = 0; g < 16; ++g) t += scr[g * 232 + td];
    __builtin_amdgcn_raw_buffer_store_b32(__float_as_uint(t), rS, td * 4, dst, 16);
  }
  asm volatile("s_waitcnt lgkmcnt(0)" ::: "memory");
}

struct MegaArgs {
  const u16 *x, *wqkv, *wo, *w1, *b1, *w2, *b2;
  u16* out;
  char* ws;
};

__global__ __launch_bounds__(512, 2) void mega_kernel(MegaArgs m) {
  extern __shared__ __attribute__((aligned(16))) char smem[];
  asm volatile(".Lmega_begin:");
  const int tid = threadIdx.x;
  int lane = tid & 63;
  const int wid0 = __builtin_amdgcn_readfirstlane(tid >> 6);
  const int bid = blockIdx.x, g_ = bid & 7, j_ = bid >> 3;
  const int tile0 = ((g_ & 1) * 8 + (j_ & 7)) * 16 + (g_ >> 1) * 4 + (j_ >> 3);
#define IDS() int wid = wid0, tile = tile0; PINS(wid); PINS(tile); \
  const int rg = wid & 3, kh = wid >> 2, panel = tile >> 4, ct = tile & 15, kq = ct >> 2, nq = ct & 3, mk = panel * 8 + rg * 2 + kh; \
  (void)rg; (void)kh; (void)panel; (void)ct; (void)kq; (void)nq; (void)mk
  const rsrc_t rS = __builtin_amdgcn_make_buffer_rsrc((void*)m.ws, 0, WS_END, 0x00020000);
  const rsrc_t rQ = __builtin_amdgcn_make_buffer_rsrc((void*)m.wqkv, 0, D_MODEL * N_QKV * 2, 0x00020000);
  const rsrc_t rO = __builtin_amdgcn_make_buffer_rsrc((void*)m.wo, 0, D_MODEL * D_MODEL * 2, 0x00020000);
  const rsrc_t r1 = __builtin_amdgcn_make_buffer_rsrc((void*)m.w1, 0, D_MODEL * F_DIM * 2, 0x00020000);
  const rsrc_t r2 = __builtin_amdgcn_make_buffer_rsrc((void*)m.w2, 0, F_DIM * D_MODEL * 2, 0x00020000);
  const unsigned lds0 = (unsigned)(size_t)smem;
  if (j_ < 4) {
    unsigned long long pc; int d0, d1;
    asm volatile("s_getpc_b64 %0\n.Lmega_pc:\n\ts_mov_b32 %1, .Lmega_begin-.Lmega_pc\n\ts_mov_b32 %2, .Lmega_end-.Lmega_begin" : "=s"(pc), "=s"(d0), "=s"(d1));
    const int off = min((j_ * 512 + tid) * 64, d1 - 4);
    asm volatile("s_mov_b32 m0, %0\n\ts_nop 0\n\tglobal_load_lds_dword %1, off" :: "s"(lds0 + TAB_OFF + 2048), "v"(((pc + (long long)d0) & ~3ull) + (unsigned long long)off) : "memory");
  }
  unsigned tag0, tag1;
  const unsigned sld = lds0 + (wid0 >> 2) * HALF + (wid0 & 3) * 1024, sld0 = sld;
  unsigned v16 = lane * 16;
  PIN(v16);
  int bb0, bb1, bb2, bb3, xw, xr, tb;
#define LBASES() { LANE(l_); const int lg = l_ >> 4, q = (l_ & 15) >> 2, p = l_ & 3, row = 4 * lg + q, sw = (row >> 1) & 3; \
    const int b = (int)lds0 + (wid0 >> 2) * HALF + row * 128 + p * 8; \
    bb0 = b + ((0 ^ sw) << 5); bb1 = b + ((1 ^ sw) << 5); bb2 = b + ((2 ^ sw) << 5); bb3 = b + ((3 ^ sw) << 5); \
    xw = (int)lds0 + (wid0 >> 2) * HALF + XCH_OFF + (wid0 & 3) * 4096 + l_ * 16; \
    xr = (int)lds0 + (1 - (wid0 >> 2)) * HALF + XCH_OFF + (wid0 & 3) * 4096 + l_ * 16; \
    tb = (int)lds0 + TAB_OFF + lg * 16; \
    PIN(bb0); PIN(bb1); PIN(bb2); PIN(bb3); PIN(xw); PIN(xr); PIN(tb); }
#define LANE(l) int l; asm volatile("v_lshrrev_b32 %0, 4, %1" : "=v"(l) : "v"(v16))
#define V8(n) unsigned n; asm volatile("v_lshrrev_b32 %0, 1, %1" : "=v"(n) : "v"(v16))
#define VW(name, RB) unsigned name; { LANE(l_); const int r_ = (wid0 & 3) * 8 + (l_ >> 3); name = r_ * (RB) + (((l_ & 7) ^ (((r_ >> 1) & 3) << 1)) << 4); PIN(name); }
#define VWP(name, RB, perm) unsigned name; { LANE(l_); const int r_ = (wid0 & 3) * 8 + (l_ >> 3), p_ = (perm) ? (perm) * ((r_ & 15) >> 2) + 4 * (r_ >> 4) + (r_ & 3) : r_; \
    name = p_ * (RB) + (((l_ & 7) ^ (((r_ >> 1) & 3) << 1)) << 4); PIN(name); }
#define VWN(name, RB) unsigned name; { LANE(l_); const int r_ = (wid0 & 3) * 16 + (l_ >> 2); name = r_ * (RB) + (((l_ & 3) ^ (((r_ >> 2) & 1) << 1)) << 4); PIN(name); }
#define SV(name, expr) unsigned name; { IDS(); name = (expr); }
  unsigned s_q, sa0, sa1, sst, so, s_o, s_1w, s_1n, s_20, s_21, s_22, s_23, kis, hso, hsi, st1, st2, shw, shn, sfo, sh0, sh1, sfi, sfp, sp0, sp1, sp2, spi, sx2, scn;
#define BASES() { IDS(); \
    sa0 = (mk * 32 + kh * 16) * 1024; sa1 = ((mk ^ 1) * 32 + kh * 16) * 1024; sst = mk * 2048; so = (mk * 32 + ct * 2) * 1024; \
    s_o = kh * (512 * D_MODEL * 2) + ct * 128; \
    s_1w = kh * (512 * F_DIM * 2) + (ct * 224 + (ct & 1) * 32) * 2; s_1n = kh * (512 * F_DIM * 2) + (ct * 224 + ((ct & 1) ? 0 : 192)) * 2; \
    const unsigned b2_ = (kq * 896 + kh * 448) * (D_MODEL * 2) + nq * 512; \
    s_20 = b2_ + ((kq + 1) & 3) * 128; s_21 = b2_ + ((kq + 2) & 3) * 128; s_22 = b2_ + ((kq + 3) & 3) * 128; s_23 = b2_ + kq * 128; \
    kis = lds0 + (rg >> 1) * HALF + KIM_OFF + ((rg * 2 + kh) & 3) * 2048; \
    hso = tile * 4096; hsi = (panel > 0 && wid == 0) ? (tile - 16) * 4096 : WS_END; \
    st1 = mk * 2048 + ct * 128; st2 = mk * 2048 + (nq * 4 + kq) * 128; \
    shw = (mk * 112 + ct * 7 + (ct & 1)) * 1024; shn = (mk * 112 + ct * 7 + ((ct & 1) ? 0 : 6)) * 1024; sfo = (tile * 8 + wid) * 128; \
    sh0 = (mk * 112 + kq * 28 + kh * 14) * 1024; sh1 = ((mk ^ 1) * 112 + kq * 28 + kh * 14) * 1024; \
    sfi = ((panel * 16 + kq * 4 + kh * 2) * 8 + rg) * 128; sfp = ((panel * 16 + nq) * 8 + wid) * 128; \
    const unsigned bp_ = (panel * 16 + nq) * 49152 + (rg * 2 + kh) * 2048; \
    sp0 = bp_ + ((kq + 1) & 3) * 196608; sp1 = bp_ + ((kq + 2) & 3) * 196608 + 16384; sp2 = bp_ + ((kq + 3) & 3) * 196608 + 32768; \
    spi = bp_ + kq * 196608; sx2 = (mk * 32 + nq * 8 + kq * 2) * 1024; scn = panel * CNS; \
    PINS(sa0); PINS(sa1); PINS(sst); PINS(so); PINS(s_o); PINS(s_1w); PINS(s_1n); PINS(s_20); PINS(s_21); PINS(s_22); PINS(s_23); PINS(kis); PINS(hso); PINS(hsi); \
    PINS(st1); PINS(st2); PINS(shw); PINS(shn); PINS(sfo); PINS(sh0); PINS(sh1); PINS(sfi); PINS(sfp); PINS(sp0); PINS(sp1); PINS(sp2); PINS(spi); PINS(sx2); PINS(scn); }
  { IDS(); s_q = kh * (512 * N_QKV * 2) + ct * 128; }
  s16x4 b176_00, b176_01, b176_10, b176_11, b176_20, b176_21, b176_30, b176_31, b177_00, b177_01, b177_10, b177_11, b177_20, b177_21, b177_30, b177_31, b178_00, b178_01, b178_10, b178_11, b178_20, b178_21, b178_30, b178_31, b179_00, b179_01, b179_10, b179_11, b179_20, b179_21, b179_30, b179_31, b180_00, b180_01, b180_10, b180_11, b180_20, b180_21, b180_30, b180_31, b181_00, b181_01, b181_10, b181_11, b181_20, b181_21, b181_30, b181_31;
  {
  constexpr int layer = 0; constexpr unsigned sL = WS_L0 + layer * LSTR; (void)layer;
  STR(30); STP(0);
  VWP(vwq, 6144, 32);
  const rsrc_t rA = __builtin_amdgcn_make_buffer_rsrc((void*)m.x, 0, XB, 0x00020000);
  DMA(rQ, vwq, s_q, 2048, 0);
  DMA(rQ, vwq, s_q, 4096, 4096);
  STP(1);
  SV(qas0, mk * 32768 + kh * 1024); SV(qas1, (mk ^ 1) * 32768 + kh * 1024);
  unsigned qva; { LANE(l_); qva = ((l_ >> 2) & 3) * 8192 + (l_ >> 4) * 64 + (l_ & 3) * 16; PIN(qva); }
  GLX(qr00_0, qva, rA, qas0, 0);
  GLX(qr01_0, qva, rA, qas0, 2048);
  GLX(qr02_0, qva, rA, qas0, 4096);
  GLX(qr03_0, qva, rA, qas0, 6144);
  GLX(qr00_1, qva, rA, qas1, 0);
  GLX(qr01_1, qva, rA, qas1, 2048);
  GLX(qr02_1, qva, rA, qas1, 4096);
  GLX(qr03_1, qva, rA, qas1, 6144);
  DMA(rQ, vwq, s_q, 51200, 8192);
  DMA(rQ, vwq, s_q, 53248, 12288);
  STP(28);
  unsigned cv_; SV(xs_, (panel * 128 + ct * 8 + wid) * 2048); SV(cs_, panel * 64 * (N_QKV * 2) + ct * 128);
  { LANE(l_); const int r_ = wid0 * 8 + (l_ >> 3); cv_ = r_ * (N_QKV * 2) + (((l_ & 7) ^ (((r_ >> 1) & 3) << 1)) << 4); PIN(cv_); }
  { const unsigned sld = lds0 + wid0 * 1024; DMA(rQ, cv_, cs_, 2048, 65536); }
  { const unsigned sld = lds0 + wid0 * 1024; DMA(rQ, cv_, cs_, 4096, 73728); }
  { const unsigned sld = lds0 + wid0 * 1024; DMA(rQ, cv_, cs_, 0, 147456); }
  GLA(xr0, v16, rA, xs_, 0);
  GLA(xr1, v16, rA, xs_, 1024);
  DMA(rQ, vwq, s_q, 100352, 16384);
  DMA(rQ, vwq, s_q, 102400, 20480);
  unsigned bv1_, bv2_, zo_ = WS_END, co_; int ca0_, ca1_;
  { IDS(); LANE(l_); const int td = wid * 64 + l_, i = td - 192;
    bv1_ = (td >= 192 && td < 416) ? (ct * 224 + (i < 192 ? (ct & 1) * 32 + i : ((ct & 1) ? 0 : 192) + i - 192)) * 2 : 0x80000000u;
    bv2_ = (td >= 416 && td < 480) ? (nq * 256 + kq * 64 + td - 416) * 2 : 0x80000000u;
    if (l_ < 2) zo_ = WS_L0 + l_ * LSTR + L_FH + (tile * 8 + wid) * 128;
    else if (l_ < 8 && ct == 0 && wid == 0) zo_ = WS_L0 + (l_ & 1) * LSTR + L_CN + ((l_ >> 1) - 1) * CNB + panel * CNS;
    const int lg = l_ >> 4, row = 4 * lg + ((l_ & 15) >> 2), sw = (row >> 1) & 3, sg = min(wid >> 1, 2);
    const int b = (int)lds0 + XCH_OFF + (sg < 2 ? sg * 8192 : HALF) + row * 128 + (l_ & 3) * 8;
    ca0_ = b + ((((wid & 1) * 2) ^ sw) << 5); ca1_ = b + ((((wid & 1) * 2 + 1) ^ sw) << 5);
    co_ = (wid < 6 && (l_ & 15) < 2) ? WS_PC + (ct * 16 + panel) * 1664 + (wid * 32 + (l_ & 1) * 16 + lg * 4) * 4 : WS_END;
    PIN(ca0_); PIN(ca1_); PIN(co_); }
  const rsrc_t rB1 = __builtin_amdgcn_make_buffer_rsrc((void*)m.b1, 0, F_DIM * 2, 0x00020000), rB2 = __builtin_amdgcn_make_buffer_rsrc((void*)m.b2, 0, D_MODEL * 2, 0x00020000);
  GLH(bi1, bv1_, rB1);
  GLH(bi2, bv2_, rB2);
  const unsigned ez_ = 0, es_ = WS_EP; GLD2(ep, ez_, rS, es_);
  PIN(zo_);
  LBASES(); BASES(); STP(29);
  unsigned tix; { IDS(); const int c0 = (3 - (int)((size_t)m.wqkv >> 7)) & 7; tix = (ct & 7) == c0 ? 0x80000000u : (panel * 14 + ct - (ct > c0) - (ct > c0 + 8)) * 8192; PINS(tix); }
  WVM(18);
  TRA(qs00_0, qs01_0, qr00_0, qr01_0);
  WVM(16);
  TRA(qs02_0, qs03_0, qr02_0, qr03_0);
  TRB(qa0_0, qa2_0, qs00_0, qs02_0);
  TRB(qa1_0, qa3_0, qs01_0, qs03_0);
  WVM(14);
  TRA(qs00_1, qs01_1, qr00_1, qr01_1);
  WVM(12);
  TRA(qs02_1, qs03_1, qr02_1, qr03_1);
  TRB(qa0_1, qa2_1, qs00_1, qs02_1);
  TRB(qa1_1, qa3_1, qs01_1, qs03_1);
  STP(20);
  WVM(10);
  BAR(); STB(0);
  PRIO(3);
  RDT(b0_00, bb0, 0);
  RDT(b0_01, bb0, 2048);
  RDT(b0_10, bb1, 0);
  RDT(b0_11, bb1, 2048);
  RDT(b0_20, bb2, 0);
  RDT(b0_21, bb2, 2048);
  RDT(b0_30, bb3, 0);
  RDT(b0_31, bb3, 2048);
  WLG(6);
  MF0(ck_00, b0_00, b0_01, qa0_0);
  RDT(b1_00, bb0, 4096);
  RDT(b1_01, bb0, 6144);
  DMA(rQ, vwq, s_q, 149504, 24576);
  STP(27);
  MF0(ck_10, b0_00, b0_01, qa0_1);
  WLG(6);
  MF0(ck_01, b0_10, b0_11, qa0_0);
  RDT(b1_10, bb1, 4096);
  RDT(b1_11, bb1, 6144);
  MF0(ck_11, b0_10, b0_11, qa0_1);
  WLG(4);
  MF0(ck_02, b0_20, b0_21, qa0_0);
  RDT(b1_20, bb2, 4096);
  RDT(b1_21, bb2, 6144);
  DMA(rQ, vwq, s_q, 151552, 28672);
  MF0(ck_12, b0_20, b0_21, qa0_1);
  MF0(ck_03, b0_30, b0_31, qa0_0);
  RDT(b1_30, bb3, 4096);
  RDT(b1_31, bb3, 6144);
  MF0(ck_13, b0_30, b0_31, qa0_1);
  GLX(qr10_0, qva, rA, qas0, 256);
  STP(23);
  WLG(4);
  MF0(cv_00, b1_00, b1_01, qa0_0);
  RDT(b2_00, bb0, 8192);
  RDT(b2_01, bb0, 10240);
  WVM(8);
  float xs0 = 0.f, xq0 = 0.f, xs1 = 0.f, xq1 = 0.f; row_stats(xr0, xs0, xq0); PIN(xs0); PIN(xq0);
  MF0(cv_10, b1_00, b1_01, qa0_1);
  row_stats(xr1, xs1, xq1); xs0 += xs1; xq0 += xq1; PIN(xs0); PIN(xq0);
  MF0(cv_01, b1_10, b1_11, qa0_0);
  RDT(b2_10, bb1, 8192);
  RDT(b2_11, bb1, 10240);
  GLX(qr11_0, qva, rA, qas0, 2304);
  xs0 = half_sum<16>(xs0); xq0 = half_sum<16>(xq0); PIN(xs0); PIN(xq0);
  MF0(cv_11, b1_10, b1_11, qa0_1);
  xs0 = half_sum<32>(xs0); xq0 = half_sum<32>(xq0); PIN(xs0); PIN(xq0);
  WLG(4);
  MF0(cv_02, b1_20, b1_21, qa0_0);
  RDT(b2_20, bb2, 8192);
  RDT(b2_21, bb2, 10240);
  xs0 = half_sum<8>(xs0); xq0 = half_sum<8>(xq0); PIN(xs0); PIN(xq0);
  MF0(cv_12, b1_20, b1_21, qa0_1);
  GLX(qr12_0, qva, rA, qas0, 4352);
  xs0 = half_sum<4>(xs0); xq0 = half_sum<4>(xq0); PIN(xs0); PIN(xq0);
  MF0(cv_03, b1_30, b1_31, qa0_0);
  RDT(b2_30, bb3, 8192);
  RDT(b2_31, bb3, 10240);
  xs0 = half_sum<2>(xs0); xq0 = half_sum<2>(xq0); PIN(xs0); PIN(xq0);
  MF0(cv_13, b1_30, b1_31, qa0_1);
  PRIO(2);
  xs0 = half_sum<1>(xs0); xq0 = half_sum<1>(xq0); PIN(xs0); PIN(xq0);
  WLG(4);
  MF(ck_00, b2_00, b2_01, qa1_0);
  RDT(b3_00, bb0, 12288);
  RDT(b3_01, bb0, 14336);
  GLX(qr13_0, qva, rA, qas0, 6400);
  { const u32x2 sv_ = {__float_as_uint(xs0), __float_as_uint(xq0)}; SV(sb_, (panel * 8 + (ct >> 1)) * 2048 + ((ct & 1) * 8 + wid) * 8); GST1(sv_, sb_, WS_L0 + L_STIN); }
  MF(ck_10, b2_00, b2_01, qa1_1);
  MF(ck_01, b2_10, b2_11, qa1_0);
  RDT(b3_10, bb1, 12288);
  RDT(b3_11, bb1, 14336);
  MF(ck_11, b2_10, b2_11, qa1_1);
  WLG(4);
  MF(ck_02, b2_20, b2_21, qa1_0);
  RDT(b3_20, bb2, 12288);
  RDT(b3_21, bb2, 14336);
  GLX(qr10_1, qva, rA, qas1, 256);
  MF(ck_12, b2_20, b2_21, qa1_1);
  MF(ck_03, b2_30, b2_31, qa1_0);
  RDT(b3_30, bb3, 12288);
  RDT(b3_31, bb3, 14336);
  MF(ck_13, b2_30, b2_31, qa1_1);
  PRIO(1);
  GLX(qr11_1, qva, rA, qas1, 2304);
  WLG(4);
  MF(cv_00, b3_00, b3_01, qa1_0);
  WVM(7);
  BAR(); STB(1);
  PRIO(3);
  RDT(b4_00, bb0, 16384);
  RDT(b4_01, bb0, 18432);
  MF(cv_10, b3_00, b3_01, qa1_1);
  __builtin_amdgcn_raw_buffer_store_b32(0u, rS, zo_, 0, 16); asm volatile("" ::: "memory");
  { SV(sm_, WS_L0 + kh * LSTR + L_HALO + tile * 4096 + rg * 1024); GMK(v16, sm_, 0); }
  MF(cv_01, b3_10, b3_11, qa1_0);
  RDT(b4_10, bb1, 16384);
  RDT(b4_11, bb1, 18432);
  GLX(qr12_1, qva, rA, qas1, 4352);
  RDT(cb000, ca0_, 0);
  RDT(cb001, ca0_, 2048);
  RDT(cb010, ca0_, 4096);
  RDT(cb011, ca0_, 6144);
  MF(cv_11, b3_10, b3_11, qa1_1);
  RDT(cb100, ca1_, 0);
  RDT(cb101, ca1_, 2048);
  RDT(cb110, ca1_, 4096);
  RDT(cb111, ca1_, 6144);
  WLG(12);
  MF(cv_02, b3_20, b3_21, qa1_0);
  RDT(b4_20, bb2, 16384);
  RDT(b4_21, bb2, 18432);
  u32x4 one_ = {0x3f803f80u, 0x3f803f80u, 0x3f803f80u, 0x3f803f80u}; PIN(one_);
  MF(cv_12, b3_20, b3_21, qa1_1);
  GLX(qr13_1, qva, rA, qas1, 6400);
  MF(cv_03, b3_30, b3_31, qa1_0);
  RDT(b4_30, bb3, 16384);
  RDT(b4_31, bb3, 18432);
  MF(cv_13, b3_30, b3_31, qa1_1);
  WLG(8);
  MF0(cc0, cb000, cb001, one_);
  MF(cc0, cb010, cb011, one_);
  MF(ck_00, b4_00, b4_01, qa2_0);
  RDT(b5_00, bb0, 20480);
  RDT(b5_01, bb0, 22528);
  DMA(rQ, vwq, s_q, 788480, 32768);
  WLG(6);
  MF0(cc1, cb100, cb101, one_);
  MF(cc1, cb110, cb111, one_);
  MF(ck_10, b4_00, b4_01, qa2_1);
  MF(ck_01, b4_10, b4_11, qa2_0);
  RDT(b5_10, bb1, 20480);
  RDT(b5_11, bb1, 22528);
  MF(ck_11, b4_10, b4_11, qa2_1);
  WLG(4);
  MF(ck_02, b4_20, b4_21, qa2_0);
  RDT(b5_20, bb2, 20480);
  RDT(b5_21, bb2, 22528);
  DMA(rQ, vwq, s_q, 790528, 36864);
  MF(ck_12, b4_20, b4_21, qa2_1);
  PIN(cc0); PIN(cc1); f32x4 cc_ = (v16 & 16) ? cc1 : cc0; PIN(cc_);
  MF(ck_03, b4_30, b4_31, qa2_0);
  RDT(b5_30, bb3, 20480);
  RDT(b5_31, bb3, 22528);
  { const unsigned z_ = 0; GST(cc_, co_, z_, 0); }
  MF(ck_13, b4_30, b4_31, qa2_1);
  DMA(rQ, vwq, s_q, 837632, 40960);
  PIN(bi1); PIN(bi2); PIN(ep); tag0 = __builtin_amdgcn_readfirstlane(ep[0]) + 1u; tag1 = __builtin_amdgcn_readfirstlane(ep[1]) + 0x9E3779B9u;
  unsigned bi_ = (bi1 | bi2) << 16; int ba_ = (int)lds0 + TAB_OFF + 2048 + (wid0 < 3 ? 0 : wid0 * 256) + (int)(v16 >> 2);
  WR1(bi_, ba_, 0);
  WLG(5);
  MF(cv_00, b5_00, b5_01, qa2_0);
  PRIO(2);
  RDT(b6_00, bb0, 24576);
  RDT(b6_01, bb0, 26624);
  MF(cv_10, b5_00, b5_01, qa2_1);
  MF(cv_01, b5_10, b5_11, qa2_0);
  RDT(b6_10, bb1, 24576);
  RDT(b6_11, bb1, 26624);
  DMA(rQ, vwq, s_q, 839680, 45056);
  MF(cv_11, b5_10, b5_11, qa2_1);
  WLG(5);
  MF(cv_02, b5_20, b5_21, qa2_0);
  RDT(b6_20, bb2, 24576);
  RDT(b6_21, bb2, 26624);
  MF(cv_12, b5_20, b5_21, qa2_1);
  DMA(rQ, vwq, s_q, 886784, 49152);
  MF(cv_03, b5_30, b5_31, qa2_0);
  RDT(b6_30, bb3, 24576);
  RDT(b6_31, bb3, 26624);
  MF(cv_13, b5_30, b5_31, qa2_1);
  WVM(15);
  TRA(qs10_0, qs11_0, qr10_0, qr11_0);
  WLG(4);
  MF(ck_00, b6_00, b6_01, qa3_0);
  PRIO(1);
  RDT(b7_00, bb0, 28672);
  RDT(b7_01, bb0, 30720);
  MF(ck_10, b6_00, b6_01, qa3_1);
  DMA(rQ, vwq, s_q, 888832, 53248);
  WVM(14);
  TRA(qs12_0, qs13_0, qr12_0, qr13_0);
  MF(ck_01, b6_10, b6_11, qa3_0);
  RDT(b7_10, bb1, 28672);
  RDT(b7_11, bb1, 30720);
  MF(ck_11, b6_10, b6_11, qa3_1);
  TRB(qa4_0, qa6_0, qs10_0, qs12_0);
  WLG(4);
  MF(ck_02, b6_20, b6_21, qa3_0);
  RDT(b7_20, bb2, 28672);
  RDT(b7_21, bb2, 30720);
  DMA(rQ, vwq, s_q, 935936, 57344);
  MF(ck_12, b6_20, b6_21, qa3_1);
  PRIO(0);
  TRB(qa5_0, qa7_0, qs11_0, qs13_0);
  MF(ck_03, b6_30, b6_31, qa3_0);
  RDT(b7_30, bb3, 28672);
  RDT(b7_31, bb3, 30720);
  MF(ck_13, b6_30, b6_31, qa3_1);
  DMA(rQ, vwq, s_q, 937984, 0);
  WVM(13);
  TRA(qs10_1, qs11_1, qr10_1, qr11_1);
  WLG(4);
  MF(cv_00, b7_00, b7_01, qa3_0);
  WVM(4);
  BAR(); STB(2);
  PRIO(3);
  RDT(b8_00, bb0, 32768);
  RDT(b8_01, bb0, 34816);
  SETUP_DONE(0);
  MF(cv_10, b7_00, b7_01, qa3_1);
  TRA(qs12_1, qs13_1, qr12_1, qr13_1);
  MF(cv_01, b7_10, b7_11, qa3_0);
  RDT(b8_10, bb1, 32768);
  RDT(b8_11, bb1, 34816);
  GLX(qr20_0, qva, rA, qas0, 512);
  MF(cv_11, b7_10, b7_11, qa3_1);
  TRB(qa4_1, qa6_1, qs10_1, qs12_1);
  WLG(4);
  MF(cv_02, b7_20, b7_21, qa3_0);
  RDT(b8_20, bb2, 32768);
  RDT(b8_21, bb2, 34816);
  MF(cv_12, b7_20, b7_21, qa3_1);
  GLX(qr21_0, qva, rA, qas0, 2560);
  TRB(qa5_1, qa7_1, qs11_1, qs13_1);
  MF(cv_03, b7_30, b7_31, qa3_0);
  RDT(b8_30, bb3, 32768);
  RDT(b8_31, bb3, 34816);
  MF(cv_13, b7_30, b7_31, qa3_1);
  WLG(4);
  MF(ck_00, b8_00, b8_01, qa4_0);
  RDT(b9_00, bb0, 36864);
  RDT(b9_01, bb0, 38912);
  MF(ck_10, b8_00, b8_01, qa4_1);
  GLX(qr22_0, qva, rA, qas0, 4608);
  MF(ck_01, b8_10, b8_11, qa4_0);
  RDT(b9_10, bb1, 36864);
  RDT(b9_11, bb1, 38912);
  MF(ck_11, b8_10, b8_11, qa4_1);
  WLG(4);
  MF(ck_02, b8_20, b8_21, qa4_0);
  RDT(b9_20, bb2, 36864);
  RDT(b9_21, bb2, 38912);
  GLX(qr23_0, qva, rA, qas0, 6656);
  MF(ck_12, b8_20, b8_21, qa4_1);
  MF(ck_03, b8_30, b8_31, qa4_0);
  RDT(b9_30, bb3, 36864);
  RDT(b9_31, bb3, 38912);
  MF(ck_13, b8_30, b8_31, qa4_1);
  GLX(qr20_1, qva, rA, qas1, 512);
  WLG(4);
  MF(cv_00, b9_00, b9_01, qa4_0);
  PRIO(2);
  RDT(b10_00, bb0, 40960);
  RDT(b10_01, bb0, 43008);
  MF(cv_10, b9_00, b9_01, qa4_1);
  MF(cv_01, b9_10, b9_11, qa4_0);
  RDT(b10_10, bb1, 40960);
  RDT(b10_11, bb1, 43008);
  GLX(qr21_1, qva, rA, qas1, 2560);
  MF(cv_11, b9_10, b9_11, qa4_1);
  WLG(4);
  MF(cv_02, b9_20, b9_21, qa4_0);
  RDT(b10_20, bb2, 40960);
  RDT(b10_21, bb2, 43008);
  MF(cv_12, b9_20, b9_21, qa4_1);
  GLX(qr22_1, qva, rA, qas1, 4608);
  MF(cv_03, b9_30, b9_31, qa4_0);
  RDT(b10_30, bb3, 40960);
  RDT(b10_31, bb3, 43008);
  MF(cv_13, b9_30, b9_31, qa4_1);
  WLG(4);
  MF(ck_00, b10_00, b10_01, qa5_0);
  PRIO(1);
  RDT(b11_00, bb0, 45056);
  RDT(b11_01, bb0, 47104);
  MF(ck_10, b10_00, b10_01, qa5_1);
  GLX(qr23_1, qva, rA, qas1, 6656);
  MF(ck_01, b10_10, b10_11, qa5_0);
  RDT(b11_10, bb1, 45056);
  RDT(b11_11, bb1, 47104);
  MF(ck_11, b10_10, b10_11, qa5_1);
  WLG(4);
  MF(ck_02, b10_20, b10_21, qa5_0);
  RDT(b11_20, bb2, 45056);
  RDT(b11_21, bb2, 47104);
  DMA(rQ, vwq, s_q, 1574912, 4096);
  MF(ck_12, b10_20, b10_21, qa5_1);
  PRIO(0);
  MF(ck_03, b10_30, b10_31, qa5_0);
  RDT(b11_30, bb3, 45056);
  RDT(b11_31, bb3, 47104);
  MF(ck_13, b10_30, b10_31, qa5_1);
  DMA(rQ, vwq, s_q, 1624064, 8192);
  WLG(4);
  MF(cv_00, b11_00, b11_01, qa5_0);
  WVM(11);
  BAR(); STB(3);
  PRIO(3);
  RDT(b12_00, bb0, 49152);
  RDT(b12_01, bb0, 51200);
  MF(cv_10, b11_00, b11_01, qa5_1);
  MF(cv_01, b11_10, b11_11, qa5_0);
  RDT(b12_10, bb1, 49152);
  RDT(b12_11, bb1, 51200);
  DMA(rQ, vwq, s_q, 1673216, 12288);
  MF(cv_11, b11_10, b11_11, qa5_1);
  WLG(4);
  MF(cv_02, b11_20, b11_21, qa5_0);
  RDT(b12_20, bb2, 49152);
  RDT(b12_21, bb2, 51200);
  MF(cv_12, b11_20, b11_21, qa5_1);
  DMA(rQ, vwq, s_q, 1722368, 16384);
  MF(cv_03, b11_30, b11_31, qa5_0);
  RDT(b12_30, bb3, 49152);
  RDT(b12_31, bb3, 51200);
  MF(cv_13, b11_30, b11_31, qa5_1);
  WLG(4);
  MF(ck_00, b12_00, b12_01, qa6_0);
  RDT(b13_00, bb0, 53248);
  RDT(b13_01, bb0, 55296);
  MF(ck_10, b12_00, b12_01, qa6_1);
  GLX(qr30_0, qva, rA, qas0, 768);
  MF(ck_01, b12_10, b12_11, qa6_0);
  RDT(b13_10, bb1, 53248);
  RDT(b13_11, bb1, 55296);
  MF(ck_11, b12_10, b12_11, qa6_1);
  WLG(4);
  MF(ck_02, b12_20, b12_21, qa6_0);
  RDT(b13_20, bb2, 53248);
  RDT(b13_21, bb2, 55296);
  GLX(qr31_0, qva, rA, qas0, 2816);
  MF(ck_12, b12_20, b12_21, qa6_1);
  MF(ck_03, b12_30, b12_31, qa6_0);
  RDT(b13_30, bb3, 53248);
  RDT(b13_31, bb3, 55296);
  MF(ck_13, b12_30, b12_31, qa6_1);
  GLX(qr32_0, qva, rA, qas0, 4864);
  WLG(4);
  MF(cv_00, b13_00, b13_01, qa6_0);
  PRIO(2);
  RDT(b14_00, bb0, 57344);
  RDT(b14_01, bb0, 59392);
  MF(cv_10, b13_00, b13_01, qa6_1);
  MF(cv_01, b13_10, b13_11, qa6_0);
  RDT(b14_10, bb1, 57344);
  RDT(b14_11, bb1, 59392);
  GLX(qr33_0, qva, rA, qas0, 6912);
  MF(cv_11, b13_10, b13_11, qa6_1);
  WLG(4);
  MF(cv_02, b13_20, b13_21, qa6_0);
  RDT(b14_20, bb2, 57344);
  RDT(b14_21, bb2, 59392);
  MF(cv_12, b13_20, b13_21, qa6_1);
  GLX(qr30_1, qva, rA, qas1, 768);
  MF(cv_03, b13_30, b13_31, qa6_0);
  RDT(b14_30, bb3, 57344);
  RDT(b14_31, bb3, 59392);
  MF(cv_13, b13_30, b13_31, qa6_1);
  WVM(15);
  TRA(qs20_0, qs21_0, qr20_0, qr21_0);
  WLG(4);
  MF(ck_00, b14_00, b14_01, qa7_0);
  PRIO(1);
  RDT(b15_00, bb0, 0);
  RDT(b15_01, bb0, 2048);
  MF(ck_10, b14_00, b14_01, qa7_1);
  GLX(qr31_1, qva, rA, qas1, 2816);
  WVM(14);
  TRA(qs22_0, qs23_0, qr22_0, qr23_0);
  MF(ck_01, b14_10, b14_11, qa7_0);
  RDT(b15_10, bb1, 0);
  RDT(b15_11, bb1, 2048);
  MF(ck_11, b14_10, b14_11, qa7_1);
  TRB(qa8_0, qa10_0, qs20_0, qs22_0);
  WLG(4);
  MF(ck_02, b14_20, b14_21, qa7_0);
  RDT(b15_20, bb2, 0);
  RDT(b15_21, bb2, 2048);
  GLX(qr32_1, qva, rA, qas1, 4864);
  unsigned vfq; { IDS(); LANE(l_); vfq = ((l_ & 16) ? 2048 + ct * 128 : panel * 128) + (l_ & 15) * 8; PIN(vfq); } const unsigned sfq = wid0 == 0 ? WS_IF + 0 * 4096 : WS_END;
  GLD2(ifq, vfq, rS, sfq);
  MF(ck_12, b14_20, b14_21, qa7_1);
  PRIO(0);
  TRB(qa9_0, qa11_0, qs21_0, qs23_0);
  MF(ck_03, b14_30, b14_31, qa7_0);
  RDT(b15_30, bb3, 0);
  RDT(b15_31, bb3, 2048);
  MF(ck_13, b14_30, b14_31, qa7_1);
  GLX(qr33_1, qva, rA, qas1, 6912);
  WVM(15);
  TRA(qs20_1, qs21_1, qr20_1, qr21_1);
  WLG(4);
  MF(cv_00, b15_00, b15_01, qa7_0);
  WVM(9);
  BAR(); STB(4);
  PRIO(3);
  RDT(b16_00, bb0, 4096);
  RDT(b16_01, bb0, 6144);
  MF(cv_10, b15_00, b15_01, qa7_1);
  TRA(qs22_1, qs23_1, qr22_1, qr23_1);
  MF(cv_01, b15_10, b15_11, qa7_0);
  RDT(b16_10, bb1, 4096);
  RDT(b16_11, bb1, 6144);
  DMA(rQ, vwq, s_q, 2361344, 20480);
  MF(cv_11, b15_10, b15_11, qa7_1);
  TRB(qa8_1, qa10_1, qs20_1, qs22_1);
  WLG(4);
  MF(cv_02, b15_20, b15_21, qa7_0);
  RDT(b16_20, bb2, 4096);
  RDT(b16_21, bb2, 6144);
  MF(cv_12, b15_20, b15_21, qa7_1);
  DMA(rQ, vwq, s_q, 2410496, 24576);
  TRB(qa9_1, qa11_1, qs21_1, qs23_1);
  MF(cv_03, b15_30, b15_31, qa7_0);
  RDT(b16_30, bb3, 4096);
  RDT(b16_31, bb3, 6144);
  MF(cv_13, b15_30, b15_31, qa7_1);
  STP(12);
  WLG(4);
  MF(ck_00, b16_00, b16_01, qa8_0);
  RDT(b17_00, bb0, 8192);
  RDT(b17_01, bb0, 10240);
  MF(ck_10, b16_00, b16_01, qa8_1);
  DMA(rQ, vwq, s_q, 2459648, 28672);
  MF(ck_01, b16_10, b16_11, qa8_0);
  RDT(b17_10, bb1, 8192);
  RDT(b17_11, bb1, 10240);
  MF(ck_11, b16_10, b16_11, qa8_1);
  WLG(4);
  MF(ck_02, b16_20, b16_21, qa8_0);
  RDT(b17_20, bb2, 8192);
  RDT(b17_21, bb2, 10240);
  DMA(rQ, vwq, s_q, 2508800, 32768);
  MF(ck_12, b16_20, b16_21, qa8_1);
  MF(ck_03, b16_30, b16_31, qa8_0);
  RDT(b17_30, bb3, 8192);
  RDT(b17_31, bb3, 10240);
  MF(ck_13, b16_30, b16_31, qa8_1);
  DMA(rQ, vwq, s_q, 1576960, 36864);
  WLG(4);
  MF(ck_00, b17_00, b17_01, qa9_0);
  PRIO(2);
  RDT(b18_00, bb0, 12288);
  RDT(b18_01, bb0, 14336);
  MF(ck_10, b17_00, b17_01, qa9_1);
  MF(ck_01, b17_10, b17_11, qa9_0);
  RDT(b18_10, bb1, 12288);
  RDT(b18_11, bb1, 14336);
  DMA(rQ, vwq, s_q, 1626112, 40960);
  MF(ck_11, b17_10, b17_11, qa9_1);
  WLG(4);
  MF(ck_02, b17_20, b17_21, qa9_0);
  RDT(b18_20, bb2, 12288);
  RDT(b18_21, bb2, 14336);
  { unsigned to_ = v16 * 8 + (wid0 < 2 ? tix + wid0 * 1835008u : 0x80000000u); TOUCH(rO, to_); }
  MF(ck_12, b17_20, b17_21, qa9_1);
  MF(ck_03, b17_30, b17_31, qa9_0);
  RDT(b18_30, bb3, 12288);
  RDT(b18_31, bb3, 14336);
  MF(ck_13, b17_30, b17_31, qa9_1);
  WVM(14);
  TRA(qs30_0, qs31_0, qr30_0, qr31_0);
  WLG(4);
  MF(ck_00, b18_00, b18_01, qa10_0);
  PRIO(1);
  RDT(b19_00, bb0, 16384);
  RDT(b19_01, bb0, 18432);
  DMA(rQ, vwq, s_q, 1675264, 45056);
  MF(ck_10, b18_00, b18_01, qa10_1);
  WVM(13);
  TRA(qs32_0, qs33_0, qr32_0, qr33_0);
  MF(ck_01, b18_10, b18_11, qa10_0);
  RDT(b19_10, bb1, 16384);
  RDT(b19_11, bb1, 18432);
  MF(ck_11, b18_10, b18_11, qa10_1);
  TRB(qa12_0, qa14_0, qs30_0, qs32_0);
  WLG(4);
  MF(ck_02, b18_20, b18_21, qa10_0);
  RDT(b19_20, bb2, 16384);
  RDT(b19_21, bb2, 18432);
  DMA(rQ, vwq, s_q, 1724416, 49152);
  MF(ck_12, b18_20, b18_21, qa10_1);
  PRIO(0);
  TRB(qa13_0, qa15_0, qs31_0, qs33_0);
  MF(ck_03, b18_30, b18_31, qa10_0);
  RDT(b19_30, bb3, 16384);
  RDT(b19_31, bb3, 18432);
  WVM(10);
  CHKT(ifq, vfq, sfq);
  MF(ck_13, b18_30, b18_31, qa10_1);
  TRA(qs30_1, qs31_1, qr30_1, qr31_1);
  WLG(4);
  MF(ck_00, b19_00, b19_01, qa11_0);
  WVM(5);
  BAR(); STB(5);
  PRIO(3);
  RDT(b20_00, bb0, 20480);
  RDT(b20_01, bb0, 22528);
  DMA(rQ, vwq, s_q, 2363392, 0);
  unsigned vcq; { IDS(); LANE(l_); const int td = wid * 64 + l_; vcq = td < 384 ? WS_PC + (ct * 16 + (l_ >> 3) * 2) * 1664 + 0 + wid * 128 + (l_ & 7) * 16 : WS_END; PIN(vcq); } const unsigned zq = 0;
  MF(ck_10, b19_00, b19_01, qa11_1);
  V8(v8q); v8q &= 127; PIN(v8q);
  GL8(stq3, v8q, sst, sL + L_STIN);
  TRA(qs32_1, qs33_1, qr32_1, qr33_1);
  MF(ck_01, b19_10, b19_11, qa11_0);
  RDT(b20_10, bb1, 20480);
  RDT(b20_11, bb1, 22528);
  MF(ck_11, b19_10, b19_11, qa11_1);
  GLD(pcq0, vcq, zq, 0);
  TRB(qa12_1, qa14_1, qs30_1, qs32_1);
  WLG(4);
  MF(ck_02, b19_20, b19_21, qa11_0);
  RDT(b20_20, bb2, 20480);
  RDT(b20_21, bb2, 22528);
  MF(ck_12, b19_20, b19_21, qa11_1);
  TRB(qa13_1, qa15_1, qs31_1, qs33_1);
  MF(ck_03, b19_30, b19_31, qa11_0);
  RDT(b20_30, bb3, 20480);
  RDT(b20_31, bb3, 22528);
  GLD(pcq1, vcq, zq, 1664);
  MF(ck_13, b19_30, b19_31, qa11_1);
  WLG(4);
  MF(ck_00, b20_00, b20_01, qa12_0);
  RDT(b21_00, bb0, 24576);
  RDT(b21_01, bb0, 26624);
  MF(ck_10, b20_00, b20_01, qa12_1);
  DMA(rQ, vwq, s_q, 2412544, 4096);
  MF(ck_01, b20_10, b20_11, qa12_0);
  RDT(b21_10, bb1, 24576);
  RDT(b21_11, bb1, 26624);
  MF(ck_11, b20_10, b20_11, qa12_1);
  WLG(4);
  MF(ck_02, b20_20, b20_21, qa12_0);
  RDT(b21_20, bb2, 24576);
  RDT(b21_21, bb2, 26624);
  DMA(rQ, vwq, s_q, 2461696, 8192);
  MF(ck_12, b20_20, b20_21, qa12_1);
  MF(ck_03, b20_30, b20_31, qa12_0);
  RDT(b21_30, bb3, 24576);
  RDT(b21_31, bb3, 26624);
  MF(ck_13, b20_30, b20_31, qa12_1);
  WLG(4);
  MF(ck_00, b21_00, b21_01, qa13_0);
  PRIO(2);
  RDT(b22_00, bb0, 28672);
  RDT(b22_01, bb0, 30720);
  DMA(rQ, vwq, s_q, 2510848, 12288);
  MF(ck_10, b21_00, b21_01, qa13_1);
  RDT(b23_00, bb0, 32768);
  RDT(b23_01, bb0, 34816);
  MF(ck_01, b21_10, b21_11, qa13_0);
  RDT(b22_10, bb1, 28672);
  RDT(b22_11, bb1, 30720);
  MF(ck_11, b21_10, b21_11, qa13_1);
  RDT(b23_10, bb1, 32768);
  RDT(b23_11, bb1, 34816);
  WLG(8);
  MF(ck_02, b21_20, b21_21, qa13_0);
  RDT(b22_20, bb2, 28672);
  RDT(b22_21, bb2, 30720);
  { unsigned to_ = v16 * 8 + (wid0 == 2 ? tix + 0u : 0x80000000u); TOUCH(r2, to_); }
  MF(ck_12, b21_20, b21_21, qa13_1);
  RDT(b23_20, bb2, 32768);
  RDT(b23_21, bb2, 34816);
  MF(ck_03, b21_30, b21_31, qa13_0);
  RDT(b22_30, bb3, 28672);
  RDT(b22_31, bb3, 30720);
  WVM(4);
  PIN(pcq0); PIN(pcq1); f32x4 csq = __builtin_bit_cast(f32x4, pcq0) + __builtin_bit_cast(f32x4, pcq1); PIN(csq);
  MF(ck_13, b21_30, b21_31, qa13_1);
  RDT(b23_30, bb3, 32768);
  RDT(b23_31, bb3, 34816);
  csq[0] = half_sum<8>(csq[0]); csq[1] = half_sum<8>(csq[1]); PIN(csq);
  WLG(8);
  MF(ck_10, b22_00, b22_01, qa14_1);
  PRIO(1);
  csq[2] = half_sum<8>(csq[2]); csq[3] = half_sum<8>(csq[3]); PIN(csq);
  MF(ck_10, b23_00, b23_01, qa15_1);
  csq[0] = half_sum<16>(csq[0]); csq[1] = half_sum<16>(csq[1]); PIN(csq);
  MF(ck_11, b22_10, b22_11, qa14_1);
  csq[2] = half_sum<16>(csq[2]); csq[3] = half_sum<16>(csq[3]); PIN(csq);
  MF(ck_11, b23_10, b23_11, qa15_1);
  csq[0] = half_sum<32>(csq[0]); csq[1] = half_sum<32>(csq[1]); PIN(csq);
  WLG(0);
  MF(ck_12, b22_20, b22_21, qa14_1);
  csq[2] = half_sum<32>(csq[2]); csq[3] = half_sum<32>(csq[3]); PIN(csq);
  NOP(3);
  WR(ck_10, xw, 0);
  MF(ck_12, b23_20, b23_21, qa15_1);
  PRIO(0);
  int taq; { IDS(); LANE(l_); const int td = wid * 64 + l_; taq = (int)lds0 + TAB_OFF + (td < 384 ? 0 + wid * 128 : 2048) + (l_ & 7) * 16; }
  WR(csq, taq, 0);
  MF(ck_13, b22_30, b22_31, qa14_1);
  PIN(stq3); float sq = stq3[0] * (1.0f / D_MODEL), tq = stq3[1]; PIN(sq);
  NOP(0);
  WR(ck_11, xw, 1024);
  MF(ck_13, b23_30, b23_31, qa15_1);
  tq = __builtin_amdgcn_rsqf(fmaxf(tq * (1.0f / D_MODEL) - sq * sq, 0.f) + 1e-5f); PIN(tq);
  MF(ck_00, b22_00, b22_01, qa14_0);
  sq = -sq * tq; PIN(sq); const f32x2 rsq = {tq, tq}, nmq = {sq, sq};
  NOP(1);
  WR(ck_12, xw, 2048);
  MF(ck_01, b22_10, b22_11, qa14_0);
  MF(ck_02, b22_20, b22_21, qa14_0);
  NOP(3);
  WR(ck_13, xw, 3072);
  MF(ck_03, b22_30, b22_31, qa14_0);
  WLG(0);
  BAR(); STB(6);
  PRIO(3);
  RDT(b24_00, bb0, 36864);
  RDT(b24_01, bb0, 38912);
  DMA(rQ, vwq, s_q, 0, 16384);
  MF(ck_00, b23_00, b23_01, qa15_0);
  RDT(b24_10, bb1, 36864);
  RDT(b24_11, bb1, 38912);
  MF(ck_01, b23_10, b23_11, qa15_0);
  RDT(b24_20, bb2, 36864);
  RDT(b24_21, bb2, 38912);
  MF(ck_02, b23_20, b23_21, qa15_0);
  RDT(b24_30, bb3, 36864);
  RDT(b24_31, bb3, 38912);
  DMA(rQ, vwq, s_q, 49152, 20480);
  MF(ck_03, b23_30, b23_31, qa15_0);
  WLG(4);
  MF(cv_00, b24_00, b24_01, qa8_0);
  RDT(b25_00, bb0, 40960);
  RDT(b25_01, bb0, 43008);
  RDF(ck_c0, tb, 0);
  MF(cv_10, b24_00, b24_01, qa8_1);
  DMA(rQ, vwq, s_q, 98304, 24576);
  MF(cv_01, b24_10, b24_11, qa8_0);
  RDT(b25_10, bb1, 40960);
  RDT(b25_11, bb1, 43008);
  MF(cv_11, b24_10, b24_11, qa8_1);
  f32x2 ck_t00, ck_t01;
  WLG(2);
  MUL(ck_t00[0], ck_c0[0], nmq[0]);
  MUL(ck_t00[1], ck_c0[1], nmq[0]);
  MF(cv_02, b24_20, b24_21, qa8_0);
  RDT(b25_20, bb2, 40960);
  RDT(b25_21, bb2, 43008);
  DMA(rQ, vwq, s_q, 147456, 28672);
  MUL(ck_t01[0], ck_c0[2], nmq[0]);
  MUL(ck_t01[1], ck_c0[3], nmq[0]);
  MF(cv_12, b24_20, b24_21, qa8_1);
  RDF(ck_r0, xr, 0);
  RDF(ck_r1, xr, 1024);
  FMA(ck_t00[0], ck_00[0], rsq[0], ck_t00[0]);
  MF(cv_03, b24_30, b24_31, qa8_0);
  RDT(b25_30, bb3, 40960);
  RDT(b25_31, bb3, 43008);
  FMA(ck_t00[1], ck_00[1], rsq[0], ck_t00[1]);
  FMA(ck_t01[0], ck_00[2], rsq[0], ck_t01[0]);
  MF(cv_13, b24_30, b24_31, qa8_1);
  DMA(rQ, vwq, s_q, 786432, 32768);
  FMA(ck_t01[1], ck_00[3], rsq[0], ck_t01[1]);
  RDF(ck_c1, tb, 64);
  MF(cv_00, b25_00, b25_01, qa9_0);
  RDT(b26_00, bb0, 45056);
  RDT(b26_01, bb0, 47104);
  MF(cv_10, b25_00, b25_01, qa9_1);
  WLG(6);
  FMA(ck_t00[0], ck_r0[0], rsq[0], ck_t00[0]);
  FMA(ck_t00[1], ck_r0[1], rsq[0], ck_t00[1]);
  MF(cv_01, b25_10, b25_11, qa9_0);
  RDT(b26_10, bb1, 45056);
  RDT(b26_11, bb1, 47104);
  FMA(ck_t01[0], ck_r0[2], rsq[0], ck_t01[0]);
  FMA(ck_t01[1], ck_r0[3], rsq[0], ck_t01[1]);
  MF(cv_11, b25_10, b25_11, qa9_1);
  PRIO(2);
  RDF(ck_r2, xr, 2048);
  RDF(ck_r3, xr, 3072);
  unsigned ck_p00; CVT(ck_p00, ck_t00[0], ck_t00[1]);
  MF(cv_02, b25_20, b25_21, qa9_0);
  RDT(b26_20, bb2, 45056);
  RDT(b26_21, bb2, 47104);
  { unsigned to_ = v16 * 8 + (wid0 == 3 ? tix + 1835008u : 0x80000000u); TOUCH(r2, to_); }
  unsigned ck_p01; CVT(ck_p01, ck_t01[0], ck_t01[1]);
  f32x2 ck_t10, ck_t11;
  WLG(8);
  MUL(ck_t10[0], ck_c1[0], nmq[0]);
  MF(cv_12, b25_20, b25_21, qa9_1);
  MUL(ck_t10[1], ck_c1[1], nmq[0]);
  MUL(ck_t11[0], ck_c1[2], nmq[0]);
  MF(cv_03, b25_30, b25_31, qa9_0);
  RDT(b26_30, bb3, 45056);
  RDT(b26_31, bb3, 47104);
  MUL(ck_t11[1], ck_c1[3], nmq[0]);
  FMA(ck_t10[0], ck_01[0], rsq[0], ck_t10[0]);
  MF(cv_13, b25_30, b25_31, qa9_1);
  FMA(ck_t10[1], ck_01[1], rsq[0], ck_t10[1]);
  FMA(ck_t11[0], ck_01[2], rsq[0], ck_t11[0]);
  WLG(6);
  MF(cv_00, b26_00, b26_01, qa10_0);
  RDT(b27_00, bb0, 49152);
  RDT(b27_01, bb0, 51200);
  FMA(ck_t11[1], ck_01[3], rsq[0], ck_t11[1]);
  RDF(ck_c2, tb, 128);
  MF(cv_10, b26_00, b26_01, qa10_1);
  MF(cv_01, b26_10, b26_11, qa10_0);
  RDT(b27_10, bb1, 49152);
  RDT(b27_11, bb1, 51200);
  FMA(ck_t10[0], ck_r1[0], rsq[0], ck_t10[0]);
  FMA(ck_t10[1], ck_r1[1], rsq[0], ck_t10[1]);
  MF(cv_11, b26_10, b26_11, qa10_1);
  PRIO(1);
  FMA(ck_t11[0], ck_r1[2], rsq[0], ck_t11[0]);
  FMA(ck_t11[1], ck_r1[3], rsq[0], ck_t11[1]);
  WLG(5);
  MF(cv_02, b26_20, b26_21, qa10_0);
  RDT(b27_20, bb2, 49152);
  RDT(b27_21, bb2, 51200);
  unsigned ck_p10; CVT(ck_p10, ck_t10[0], ck_t10[1]);
  unsigned ck_p11; CVT(ck_p11, ck_t11[0], ck_t11[1]);
  MF(cv_12, b26_20, b26_21, qa10_1);
  const u32x4 ck_o0 = {ck_p00, ck_p01, ck_p10, ck_p11};
  unsigned ck_ia = kis + v16; PIN(ck_ia);
  WR(ck_o0, ck_ia, 0);
  if (wid0 == 7) GST(clamp4(ck_o0), v16, hso, sL + L_HALO + 0);
  f32x2 ck_t20, ck_t21;
  WLG(5);
  MUL(ck_t20[0], ck_c2[0], nmq[0]);
  MF(cv_03, b26_30, b26_31, qa10_0);
  RDT(b27_30, bb3, 49152);
  RDT(b27_31, bb3, 51200);
  MUL(ck_t20[1], ck_c2[1], nmq[0]);
  MUL(ck_t21[0], ck_c2[2], nmq[0]);
  MF(cv_13, b26_30, b26_31, qa10_1);
  MUL(ck_t21[1], ck_c2[3], nmq[0]);
  FMA(ck_t20[0], ck_02[0], rsq[0], ck_t20[0]);
  MF(cv_00, b27_00, b27_01, qa11_0);
  PRIO(0);
  WVM(7);
  WLG(2);
  BAR(); STB(7);
  PRIO(3);
  RDT(b28_00, bb0, 0);
  RDT(b28_01, bb0, 2048);
  DMA(rQ, vwq, s_q, 835584, 36864);
  FMA(ck_t20[1], ck_02[1], rsq[0], ck_t20[1]);
  FMA(ck_t21[0], ck_02[2], rsq[0], ck_t21[0]);
  MF(cv_10, b27_00, b27_01, qa11_1);
  FMA(ck_t21[1], ck_02[3], rsq[0], ck_t21[1]);
  RDF(ck_c3, tb, 192);
  MF(cv_01, b27_10, b27_11, qa11_0);
  RDT(b28_10, bb1, 0);
  RDT(b28_11, bb1, 2048);
  MF(cv_11, b27_10, b27_11, qa11_1);
  DMA(rQ, vwq, s_q, 884736, 40960);
  FMA(ck_t20[0], ck_r2[0], rsq[0], ck_t20[0]);
  FMA(ck_t20[1], ck_r2[1], rsq[0], ck_t20[1]);
  MF(cv_02, b27_20, b27_21, qa11_0);
  RDT(b28_20, bb2, 0);
  RDT(b28_21, bb2, 2048);
  FMA(ck_t21[0], ck_r2[2], rsq[0], ck_t21[0]);
  FMA(ck_t21[1], ck_r2[3], rsq[0], ck_t21[1]);
  MF(cv_12, b27_20, b27_21, qa11_1);
  unsigned ck_p20; CVT(ck_p20, ck_t20[0], ck_t20[1]);
  unsigned ck_p21; CVT(ck_p21, ck_t21[0], ck_t21[1]);
  WLG(5);
  MF(cv_03, b27_30, b27_31, qa11_0);
  RDT(b28_30, bb3, 0);
  RDT(b28_31, bb3, 2048);
  DMA(rQ, vwq, s_q, 933888, 45056);
  f32x2 ck_t30, ck_t31;
  WLG(6);
  MUL(ck_t30[0], ck_c3[0], nmq[0]);
  MUL(ck_t30[1], ck_c3[1], nmq[0]);
  MF(cv_13, b27_30, b27_31, qa11_1);
  MUL(ck_t31[0], ck_c3[2], nmq[0]);
  MUL(ck_t31[1], ck_c3[3], nmq[0]);
  MF(cv_00, b28_00, b28_01, qa12_0);
  RDT(b29_00, bb0, 4096);
  RDT(b29_01, bb0, 6144);
  FMA(ck_t30[0], ck_03[0], rsq[0], ck_t30[0]);
  FMA(ck_t30[1], ck_03[1], rsq[0], ck_t30[1]);
  MF(cv_10, b28_00, b28_01, qa12_1);
  FMA(ck_t31[0], ck_03[2], rsq[0], ck_t31[0]);
  FMA(ck_t31[1], ck_03[3], rsq[0], ck_t31[1]);
  WLG(4);
  MF(cv_01, b28_10, b28_11, qa12_0);
  RDT(b29_10, bb1, 4096);
  RDT(b29_11, bb1, 6144);
  MF(cv_11, b28_10, b28_11, qa12_1);
  FMA(ck_t30[0], ck_r3[0], rsq[0], ck_t30[0]);
  FMA(ck_t30[1], ck_r3[1], rsq[0], ck_t30[1]);
  MF(cv_02, b28_20, b28_21, qa12_0);
  RDT(b29_20, bb2, 4096);
  RDT(b29_21, bb2, 6144);
  FMA(ck_t31[0], ck_r3[2], rsq[0], ck_t31[0]);
  FMA(ck_t31[1], ck_r3[3], rsq[0], ck_t31[1]);
  MF(cv_12, b28_20, b28_21, qa12_1);
  unsigned ck_p30; CVT(ck_p30, ck_t30[0], ck_t30[1]);
  unsigned ck_p31; CVT(ck_p31, ck_t31[0], ck_t31[1]);
  WLG(4);
  MF(cv_03, b28_30, b28_31, qa12_0);
  RDT(b29_30, bb3, 4096);
  RDT(b29_31, bb3, 6144);
  const u32x4 ck_o1 = {ck_p20, ck_p21, ck_p30, ck_p31};
  WR(ck_o1, ck_ia, 1024);
  if (wid0 == 7) GST(clamp4(ck_o1), v16, hso, sL + L_HALO + 1024);
  MF(cv_13, b28_30, b28_31, qa12_1);
  MF(cv_00, b29_00, b29_01, qa13_0);
  PRIO(2);
  RDT(b30_00, bb0, 8192);
  RDT(b30_01, bb0, 10240);
  MF(cv_10, b29_00, b29_01, qa13_1);
  RDT(b31_00, bb0, 12288);
  RDT(b31_01, bb0, 14336);
  WLG(7);
  MF(cv_01, b29_10, b29_11, qa13_0);
  RDT(b30_10, bb1, 8192);
  RDT(b30_11, bb1, 10240);
  MF(cv_11, b29_10, b29_11, qa13_1);
  RDT(b31_10, bb1, 12288);
  RDT(b31_11, bb1, 14336);
  MF(cv_02, b29_20, b29_21, qa13_0);
  RDT(b30_20, bb2, 8192);
  RDT(b30_21, bb2, 10240);
  { unsigned to_ = v16 * 8 + (wid0 == 4 ? tix + 3670016u : 0x80000000u); TOUCH(r2, to_); }
  MF(cv_12, b29_20, b29_21, qa13_1);
  RDT(b31_20, bb2, 12288);
  RDT(b31_21, bb2, 14336);
  WLG(8);
  MF(cv_03, b29_30, b29_31, qa13_0);
  RDT(b30_30, bb3, 8192);
  RDT(b30_31, bb3, 10240);
  MF(cv_13, b29_30, b29_31, qa13_1);
  RDT(b31_30, bb3, 12288);
  RDT(b31_31, bb3, 14336);
  MF(cv_10, b30_00, b30_01, qa14_1);
  PRIO(1);
  MF(cv_10, b31_00, b31_01, qa15_1);
  WLG(4);
  MF(cv_11, b30_10, b30_11, qa14_1);
  MF(cv_11, b31_10, b31_11, qa15_1);
  MF(cv_12, b30_20, b30_21, qa14_1);
  NOP(3);
  WR(cv_10, xw, 0);
  MF(cv_12, b31_20, b31_21, qa15_1);
  PRIO(0);
  WLG(1);
  MF(cv_13, b30_30, b30_31, qa14_1);
  NOP(1);
  WR(cv_11, xw, 1024);
  MF(cv_13, b31_30, b31_31, qa15_1);
  MF(cv_00, b30_00, b30_01, qa14_0);
  NOP(1);
  WR(cv_12, xw, 2048);
  MF(cv_01, b30_10, b30_11, qa14_0);
  MF(cv_02, b30_20, b30_21, qa14_0);
  VW(vwo, 2048);
  NOP(3);
  WR(cv_13, xw, 3072);
  MF(cv_03, b30_30, b30_31, qa14_0);
  WVM(6);
  WLG(0);
  BAR(); STB(8);
  PRIO(3);
  RDT(b32_00, bb0, 16384);
  RDT(b32_01, bb0, 18432);
  DMA(rQ, vwq, s_q, 1572864, 49152);
  MF(cv_00, b31_00, b31_01, qa15_0);
  RDT(b32_10, bb1, 16384);
  RDT(b32_11, bb1, 18432);
  MF(cv_01, b31_10, b31_11, qa15_0);
  RDT(b32_20, bb2, 16384);
  RDT(b32_21, bb2, 18432);
  MF(cv_02, b31_20, b31_21, qa15_0);
  RDT(b32_30, bb3, 16384);
  RDT(b32_31, bb3, 18432);
  DMA(rQ, vwq, s_q, 1622016, 0);
  MF(cv_03, b31_30, b31_31, qa15_0);
  STP(13);
  WLG(4);
  MF0(cq_00, b32_00, b32_01, qa0_0);
  RDT(b33_00, bb0, 20480);
  RDT(b33_01, bb0, 22528);
  RDF(cv_c0, tb, 256);
  MF0(cq_10, b32_00, b32_01, qa0_1);
  DMA(rQ, vwq, s_q, 1671168, 4096);
  MF0(cq_01, b32_10, b32_11, qa0_0);
  RDT(b33_10, bb1, 20480);
  RDT(b33_11, bb1, 22528);
  MF0(cq_11, b32_10, b32_11, qa0_1);
  f32x2 cv_t00, cv_t01;
  WLG(2);
  MUL(cv_t00[0], cv_c0[0], nmq[0]);
  MUL(cv_t00[1], cv_c0[1], nmq[0]);
  MF0(cq_02, b32_20, b32_21, qa0_0);
  RDT(b33_20, bb2, 20480);
  RDT(b33_21, bb2, 22528);
  DMA(rQ, vwq, s_q, 1720320, 8192);
  MUL(cv_t01[0], cv_c0[2], nmq[0]);
  MUL(cv_t01[1], cv_c0[3], nmq[0]);
  MF0(cq_12, b32_20, b32_21, qa0_1);
  RDF(cv_r0, xr, 0);
  RDF(cv_r1, xr, 1024);
  FMA(cv_t00[0], cv_00[0], rsq[0], cv_t00[0]);
  MF0(cq_03, b32_30, b32_31, qa0_0);
  RDT(b33_30, bb3, 20480);
  RDT(b33_31, bb3, 22528);
  FMA(cv_t00[1], cv_00[1], rsq[0], cv_t00[1]);
  FMA(cv_t01[0], cv_00[2], rsq[0], cv_t01[0]);
  MF0(cq_13, b32_30, b32_31, qa0_1);
  DMA(rQ, vwq, s_q, 2359296, 12288);
  FMA(cv_t01[1], cv_00[3], rsq[0], cv_t01[1]);
  RDF(cv_c1, tb, 320);
  MF(cq_00, b33_00, b33_01, qa1_0);
  RDT(b34_00, bb0, 24576);
  RDT(b34_01, bb0, 26624);
  MF(cq_10, b33_00, b33_01, qa1_1);
  WLG(6);
  FMA(cv_t00[0], cv_r0[0], rsq[0], cv_t00[0]);
  FMA(cv_t00[1], cv_r0[1], rsq[0], cv_t00[1]);
  MF(cq_01, b33_10, b33_11, qa1_0);
  RDT(b34_10, bb1, 24576);
  RDT(b34_11, bb1, 26624);
  FMA(cv_t01[0], cv_r0[2], rsq[0], cv_t01[0]);
  FMA(cv_t01[1], cv_r0[3], rsq[0], cv_t01[1]);
  MF(cq_11, b33_10, b33_11, qa1_1);
  PRIO(2);
  RDF(cv_r2, xr, 2048);
  RDF(cv_r3, xr, 3072);
  unsigned cv_p00; CVT(cv_p00, cv_t00[0], cv_t00[1]);
  MF(cq_02, b33_20, b33_21, qa1_0);
  RDT(b34_20, bb2, 24576);
  RDT(b34_21, bb2, 26624);
  { unsigned to_ = v16 * 8 + (wid0 == 5 ? tix + 5505024u : 0x80000000u); TOUCH(r2, to_); }
  unsigned cv_p01; CVT(cv_p01, cv_t01[0], cv_t01[1]);
  f32x2 cv_t10, cv_t11;
  WLG(8);
  MUL(cv_t10[0], cv_c1[0], nmq[0]);
  MF(cq_12, b33_20, b33_21, qa1_1);
  MUL(cv_t10[1], cv_c1[1], nmq[0]);
  MUL(cv_t11[0], cv_c1[2], nmq[0]);
  MF(cq_03, b33_30, b33_31, qa1_0);
  RDT(b34_30, bb3, 24576);
  RDT(b34_31, bb3, 26624);
  MUL(cv_t11[1], cv_c1[3], nmq[0]);
  FMA(cv_t10[0], cv_01[0], rsq[0], cv_t10[0]);
  MF(cq_13, b33_30, b33_31, qa1_1);
  FMA(cv_t10[1], cv_01[1], rsq[0], cv_t10[1]);
  FMA(cv_t11[0], cv_01[2], rsq[0], cv_t11[0]);
  WLG(6);
  MF(cq_00, b34_00, b34_01, qa2_0);
  RDT(b35_00, bb0, 28672);
  RDT(b35_01, bb0, 30720);
  FMA(cv_t11[1], cv_01[3], rsq[0], cv_t11[1]);
  RDF(cv_c2, tb, 384);
  MF(cq_10, b34_00, b34_01, qa2_1);
  MF(cq_01, b34_10, b34_11, qa2_0);
  RDT(b35_10, bb1, 28672);
  RDT(b35_11, bb1, 30720);
  FMA(cv_t10[0], cv_r1[0], rsq[0], cv_t10[0]);
  FMA(cv_t10[1], cv_r1[1], rsq[0], cv_t10[1]);
  MF(cq_11, b34_10, b34_11, qa2_1);
  PRIO(1);
  FMA(cv_t11[0], cv_r1[2], rsq[0], cv_t11[0]);
  FMA(cv_t11[1], cv_r1[3], rsq[0], cv_t11[1]);
  WLG(5);
  MF(cq_02, b34_20, b34_21, qa2_0);
  RDT(b35_20, bb2, 28672);
  RDT(b35_21, bb2, 30720);
  unsigned cv_p10; CVT(cv_p10, cv_t10[0], cv_t10[1]);
  unsigned cv_p11; CVT(cv_p11, cv_t11[0], cv_t11[1]);
  MF(cq_12, b34_20, b34_21, qa2_1);
  const u32x4 cv_o0 = {cv_p00, cv_p01, cv_p10, cv_p11};
  const int cv_ia = xr; int vao; { LANE(l_); vao = xr - l_ * 16 + ((l_ & 3) * 16 + 4 * (l_ >> 4) + ((l_ & 15) >> 2)) * 16; PIN(vao); }
  WR(cv_o0, cv_ia, 0);
  RDT(vo00, vao, 0);
  RDT(vo01, vao, 8);
  f32x2 cv_t20, cv_t21;
  WLG(7);
  MUL(cv_t20[0], cv_c2[0], nmq[0]);
  MF(cq_03, b34_30, b34_31, qa2_0);
  RDT(b35_30, bb3, 28672);
  RDT(b35_31, bb3, 30720);
  MUL(cv_t20[1], cv_c2[1], nmq[0]);
  MUL(cv_t21[0], cv_c2[2], nmq[0]);
  MF(cq_13, b34_30, b34_31, qa2_1);
  MUL(cv_t21[1], cv_c2[3], nmq[0]);
  FMA(cv_t20[0], cv_02[0], rsq[0], cv_t20[0]);
  MF(cq_00, b35_00, b35_01, qa3_0);
  PRIO(0);
  WVM(7);
  WLG(4);
  BAR(); STB(9);
  PRIO(3);
  RDT(b36_00, bb0, 32768);
  RDT(b36_01, bb0, 34816);
  DMA(rQ, vwq, s_q, 2408448, 16384);
  FMA(cv_t20[1], cv_02[1], rsq[0], cv_t20[1]);
  FMA(cv_t21[0], cv_02[2], rsq[0], cv_t21[0]);
  MF(cq_10, b35_00, b35_01, qa3_1);
  FMA(cv_t21[1], cv_02[3], rsq[0], cv_t21[1]);
  RDF(cv_c3, tb, 448);
  MF(cq_01, b35_10, b35_11, qa3_0);
  RDT(b36_10, bb1, 32768);
  RDT(b36_11, bb1, 34816);
  WLG(7);
  PIN(vo00); PIN(vo01); if (wid0 == 7) GST(clamp4(__builtin_bit_cast(u32x4, CAT8(vo00, vo01))), v16, hso, sL + L_HALO + 2048);
  MF(cq_11, b35_10, b35_11, qa3_1);
  DMA(rQ, vwq, s_q, 2457600, 20480);
  FMA(cv_t20[0], cv_r2[0], rsq[0], cv_t20[0]);
  FMA(cv_t20[1], cv_r2[1], rsq[0], cv_t20[1]);
  MF(cq_02, b35_20, b35_21, qa3_0);
  RDT(b36_20, bb2, 32768);
  RDT(b36_21, bb2, 34816);
  FMA(cv_t21[0], cv_r2[2], rsq[0], cv_t21[0]);
  FMA(cv_t21[1], cv_r2[3], rsq[0], cv_t21[1]);
  MF(cq_12, b35_20, b35_21, qa3_1);
  unsigned cv_p20; CVT(cv_p20, cv_t20[0], cv_t20[1]);
  unsigned cv_p21; CVT(cv_p21, cv_t21[0], cv_t21[1]);
  WLG(5);
  MF(cq_03, b35_30, b35_31, qa3_0);
  RDT(b36_30, bb3, 32768);
  RDT(b36_31, bb3, 34816);
  DMA(rQ, vwq, s_q, 2506752, 24576);
  f32x2 cv_t30, cv_t31;
  WLG(6);
  MUL(cv_t30[0], cv_c3[0], nmq[0]);
  MUL(cv_t30[1], cv_c3[1], nmq[0]);
  MF(cq_13, b35_30, b35_31, qa3_1);
  MUL(cv_t31[0], cv_c3[2], nmq[0]);
  MUL(cv_t31[1], cv_c3[3], nmq[0]);
  MF(cq_00, b36_00, b36_01, qa4_0);
  RDT(b37_00, bb0, 36864);
  RDT(b37_01, bb0, 38912);
  FMA(cv_t30[0], cv_03[0], rsq[0], cv_t30[0]);
  FMA(cv_t30[1], cv_03[1], rsq[0], cv_t30[1]);
  MF(cq_10, b36_00, b36_01, qa4_1);
  FMA(cv_t31[0], cv_03[2], rsq[0], cv_t31[0]);
  FMA(cv_t31[1], cv_03[3], rsq[0], cv_t31[1]);
  WLG(4);
  MF(cq_01, b36_10, b36_11, qa4_0);
  RDT(b37_10, bb1, 36864);
  RDT(b37_11, bb1, 38912);
  MF(cq_11, b36_10, b36_11, qa4_1);
  FMA(cv_t30[0], cv_r3[0], rsq[0], cv_t30[0]);
  FMA(cv_t30[1], cv_r3[1], rsq[0], cv_t30[1]);
  MF(cq_02, b36_20, b36_21, qa4_0);
  RDT(b37_20, bb2, 36864);
  RDT(b37_21, bb2, 38912);
  FMA(cv_t31[0], cv_r3[2], rsq[0], cv_t31[0]);
  FMA(cv_t31[1], cv_r3[3], rsq[0], cv_t31[1]);
  MF(cq_12, b36_20, b36_21, qa4_1);
  unsigned cv_p30; CVT(cv_p30, cv_t30[0], cv_t30[1]);
  unsigned cv_p31; CVT(cv_p31, cv_t31[0], cv_t31[1]);
  WLG(4);
  MF(cq_03, b36_30, b36_31, qa4_0);
  RDT(b37_30, bb3, 36864);
  RDT(b37_31, bb3, 38912);
  const u32x4 cv_o1 = {cv_p20, cv_p21, cv_p30, cv_p31};
  WR(cv_o1, cv_ia, 1024);
  RDT(vo10, vao, 1024);
  RDT(vo11, vao, 1032);
  MF(cq_13, b36_30, b36_31, qa4_1);
  MF(cq_00, b37_00, b37_01, qa5_0);
  PRIO(2);
  RDT(b38_00, bb0, 40960);
  RDT(b38_01, bb0, 43008);
  unsigned dv_; { IDS(); LANE(l_); dv_ = colsum_off<224>(F_DIM * 2, [&](int i) { return (ct * 224 + (i < 192 ? (ct & 1) * 32 + i : ((ct & 1) ? 0 : 192) + i - 192)) * 2; }, panel * 64, wid, l_); PIN(dv_); }
  MF(cq_10, b37_00, b37_01, qa5_1);
  WLG(7);
  MF(cq_01, b37_10, b37_11, qa5_0);
  RDT(b38_10, bb1, 40960);
  RDT(b38_11, bb1, 43008);
  GLW(dw0, dv_, r1, 0 * F_DIM * 2);
  MF(cq_11, b37_10, b37_11, qa5_1);
  WLG(4);
  PIN(vo10); PIN(vo11); if (wid0 == 7) GST(clamp4(__builtin_bit_cast(u32x4, CAT8(vo10, vo11))), v16, hso, sL + L_HALO + 3072);
  MF(cq_02, b37_20, b37_21, qa5_0);
  RDT(b38_20, bb2, 40960);
  RDT(b38_21, bb2, 43008);
  MF(cq_12, b37_20, b37_21, qa5_1);
  MF(cq_03, b37_30, b37_31, qa5_0);
  RDT(b38_30, bb3, 40960);
  RDT(b38_31, bb3, 43008);
  MF(cq_13, b37_30, b37_31, qa5_1);
  WLG(4);
  MF(cq_00, b38_00, b38_01, qa6_0);
  PRIO(1);
  RDT(b39_00, bb0, 45056);
  RDT(b39_01, bb0, 47104);
  MF(cq_10, b38_00, b38_01, qa6_1);
  MF(cq_01, b38_10, b38_11, qa6_0);
  RDT(b39_10, bb1, 45056);
  RDT(b39_11, bb1, 47104);
  int kao, kap, vap;
      { IDS(); const int mb = max(rg * 2 + kh - 1, 0);
        kao = kis + v16; kap = (int)lds0 + (mb >> 2) * HALF + (mb & 3) * 2048 + KIM_OFF + v16;
        vap = vao + (2 * kh - 1) * HALF + ((mb >> 1) - rg) * 4096;
        PIN(kao); PIN(kap); PIN(vap); }
  MF(cq_11, b38_10, b38_11, qa6_1);
  WLG(4);
  MF(cq_02, b38_20, b38_21, qa6_0);
  RDT(b39_20, bb2, 45056);
  RDT(b39_21, bb2, 47104);
  MF(cq_12, b38_20, b38_21, qa6_1);
  PRIO(0);
  MF(cq_03, b38_30, b38_31, qa6_0);
  RDT(b39_30, bb3, 45056);
  RDT(b39_31, bb3, 47104);
  MF(cq_13, b38_30, b38_31, qa6_1);
  WLG(4);
  MF(cq_00, b39_00, b39_01, qa7_0);
  WVM(6);
  BAR(); STB(10);
  PRIO(3);
  RDT(b40_00, bb0, 49152);
  RDT(b40_01, bb0, 51200);
  DMA(rO, vwo, s_o, 0, 28672);
  MF(cq_10, b39_00, b39_01, qa7_1);
  MF(cq_01, b39_10, b39_11, qa7_0);
  RDT(b40_10, bb1, 49152);
  RDT(b40_11, bb1, 51200);
  RDB(ko0, kao, 0);
  MF(cq_11, b39_10, b39_11, qa7_1);
  DMA(rO, vwo, s_o, 65536, 32768);
  RDB(kp0, kap, 0);
  WLG(6);
  MF(cq_02, b39_20, b39_21, qa7_0);
  RDT(b40_20, bb2, 49152);
  RDT(b40_21, bb2, 51200);
  RDT(vp00, vap, 0);
  MF(cq_12, b39_20, b39_21, qa7_1);
  RDT(vp01, vap, 8);
  MF(cq_03, b39_30, b39_31, qa7_0);
  RDT(b40_30, bb3, 49152);
  RDT(b40_31, bb3, 51200);
  GLW(dw1, dv_, r1, 1 * F_DIM * 2);
  RDB(ko1, kao, 1024);
  MF(cq_13, b39_30, b39_31, qa7_1);
  RDB(kp1, kap, 1024);
  WLG(10);
  MF(cq_00, b40_00, b40_01, qa8_0);
  RDT(b41_00, bb0, 0);
  RDT(b41_01, bb0, 2048);
  RDT(vp10, vap, 1024);
  MF(cq_10, b40_00, b40_01, qa8_1);
  DMA(rO, vwo, s_o, 131072, 36864);
  RDT(vp11, vap, 1032);
  MF(cq_01, b40_10, b40_11, qa8_0);
  RDT(b41_10, bb1, 0);
  RDT(b41_11, bb1, 2048);
  MF(cq_11, b40_10, b40_11, qa8_1);
  WLG(8);
  MF(cq_02, b40_20, b40_21, qa8_0);
  RDT(b41_20, bb2, 0);
  RDT(b41_21, bb2, 2048);
  DMA(rO, vwo, s_o, 196608, 40960);
  MF(cq_12, b40_20, b40_21, qa8_1);
  MF(cq_03, b40_30, b40_31, qa8_0);
  RDT(b41_30, bb3, 0);
  RDT(b41_31, bb3, 2048);
  MF(cq_13, b40_30, b40_31, qa8_1);
  WLG(4);
  MF(cq_00, b41_00, b41_01, qa9_0);
  PRIO(2);
  RDT(b42_00, bb0, 4096);
  RDT(b42_01, bb0, 6144);
  MF(cq_10, b41_00, b41_01, qa9_1);
  MF(cq_01, b41_10, b41_11, qa9_0);
  RDT(b42_10, bb1, 4096);
  RDT(b42_11, bb1, 6144);
  MF(cq_11, b41_10, b41_11, qa9_1);
  WLG(4);
  MF(cq_02, b41_20, b41_21, qa9_0);
  RDT(b42_20, bb2, 4096);
  RDT(b42_21, bb2, 6144);
  MF(cq_12, b41_20, b41_21, qa9_1);
  MF(cq_03, b41_30, b41_31, qa9_0);
  RDT(b42_30, bb3, 4096);
  RDT(b42_31, bb3, 6144);
  MF(cq_13, b41_30, b41_31, qa9_1);
  WLG(4);
  MF(cq_00, b42_00, b42_01, qa10_0);
  PRIO(1);
  RDT(b43_00, bb0, 8192);
  RDT(b43_01, bb0, 10240);
  MF(cq_10, b42_00, b42_01, qa10_1);
  MF(cq_01, b42_10, b42_11, qa10_0);
  RDT(b43_10, bb1, 8192);
  RDT(b43_11, bb1, 10240);
  GLW(dw2, dv_, r1, 2 * F_DIM * 2);
  MF(cq_11, b42_10, b42_11, qa10_1);
  WLG(4);
  MF(cq_02, b42_20, b42_21, qa10_0);
  RDT(b43_20, bb2, 8192);
  RDT(b43_21, bb2, 10240);
  MF(cq_12, b42_20, b42_21, qa10_1);
  PRIO(0);
  MF(cq_03, b42_30, b42_31, qa10_0);
  RDT(b43_30, bb3, 8192);
  RDT(b43_31, bb3, 10240);
  MF(cq_13, b42_30, b42_31, qa10_1);
  WLG(4);
  MF(cq_00, b43_00, b43_01, qa11_0);
  WVM(7);
  BAR(); STB(11);
  PRIO(3);
  RDT(b44_00, bb0, 12288);
  RDT(b44_01, bb0, 14336);
  DMA(rO, vwo, s_o, 262144, 53248);
  GLD(hl0, v16, hsi, sL + L_HALO + 0);
  RDF(cq_c0, tb, 512);
  RDF(cq_c1, tb, 576);
  RDF(cq_c2, tb, 640);
  RDF(cq_c3, tb, 704);
  MF(cq_10, b43_00, b43_01, qa11_1);
  GLD(hl1, v16, hsi, sL + L_HALO + 1024);
  bool par, own0, own1, nop;
      { IDS(); LANE(l_); nop = mk == 0; const int li = l_ & 15, lg = l_ >> 4;
        par = li & 1; own0 = li - 4 * lg - (li & 1) >= 0; own1 = li - 4 * lg - (li & 1) - 2 >= 0; }
  MF(cq_01, b43_10, b43_11, qa11_0);
  RDT(b44_10, bb1, 12288);
  RDT(b44_11, bb1, 14336);
  GLD(hl2, v16, hsi, sL + L_HALO + 2048);
  MF(cq_11, b43_10, b43_11, qa11_1);
  DMA(rO, vwo, s_o, 327680, 57344);
  GLD(hl3, v16, hsi, sL + L_HALO + 3072);
  WLG(8);
  MF(cq_02, b43_20, b43_21, qa11_0);
  RDT(b44_20, bb2, 12288);
  RDT(b44_21, bb2, 14336);
  WLG(7);
  PIN(cq_c0); f32x4 cq_u0 = cq_c0 * nmq[0]; PIN(cq_u0);
  MF(cq_12, b43_20, b43_21, qa11_1);
  WLG(6);
  PIN(cq_c1); f32x4 cq_u1 = cq_c1 * nmq[0]; PIN(cq_u1);
  MF(cq_03, b43_30, b43_31, qa11_0);
  RDT(b44_30, bb3, 12288);
  RDT(b44_31, bb3, 14336);
  DMA(rO, vwo, s_o, 393216, 45056);
  WLG(7);
  PIN(cq_c2); f32x4 cq_u2 = cq_c2 * nmq[0]; PIN(cq_u2);
  MF(cq_13, b43_30, b43_31, qa11_1);
  WLG(6);
  PIN(cq_c3); f32x4 cq_u3 = cq_c3 * nmq[0]; PIN(cq_u3);
  MF(cq_00, b44_00, b44_01, qa12_0);
  RDT(b45_00, bb0, 16384);
  RDT(b45_01, bb0, 18432);
  MF(cq_10, b44_00, b44_01, qa12_1);
  DMA(rO, vwo, s_o, 458752, 49152);
  WLG(4);
  MF(cq_01, b44_10, b44_11, qa12_0);
  RDT(b45_10, bb1, 16384);
  RDT(b45_11, bb1, 18432);
  MF(cq_11, b44_10, b44_11, qa12_1);
  MF(cq_02, b44_20, b44_21, qa12_0);
  RDT(b45_20, bb2, 16384);
  RDT(b45_21, bb2, 18432);
  DMA(rO, vwo, s_o, 524288, 0);
  MF(cq_12, b44_20, b44_21, qa12_1);
  WLG(4);
  MF(cq_03, b44_30, b44_31, qa12_0);
  RDT(b45_30, bb3, 16384);
  RDT(b45_31, bb3, 18432);
  MF(cq_13, b44_30, b44_31, qa12_1);
  MF(cq_00, b45_00, b45_01, qa13_0);
  PRIO(2);
  RDT(b46_00, bb0, 20480);
  RDT(b46_01, bb0, 22528);
  GLW(dw3, dv_, r1, 3 * F_DIM * 2);
  MF(cq_10, b45_00, b45_01, qa13_1);
  RDT(b47_00, bb0, 24576);
  RDT(b47_01, bb0, 26624);
  WLG(6);
  MF(cq_01, b45_10, b45_11, qa13_0);
  RDT(b46_10, bb1, 20480);
  RDT(b46_11, bb1, 22528);
  MF(cq_11, b45_10, b45_11, qa13_1);
  RDT(b47_10, bb1, 24576);
  RDT(b47_11, bb1, 26624);
  DMA(rO, vwo, s_o, 589824, 4096);
  MF(cq_02, b45_20, b45_21, qa13_0);
  RDT(b46_20, bb2, 20480);
  RDT(b46_21, bb2, 22528);
  MF(cq_12, b45_20, b45_21, qa13_1);
  RDT(b47_20, bb2, 24576);
  RDT(b47_21, bb2, 26624);
  WLG(8);
  MF(cq_03, b45_30, b45_31, qa13_0);
  RDT(b46_30, bb3, 20480);
  RDT(b46_31, bb3, 22528);
  MF(cq_13, b45_30, b45_31, qa13_1);
  RDT(b47_30, bb3, 24576);
  RDT(b47_31, bb3, 26624);
  MF(cq_10, b46_00, b46_01, qa14_1);
  PRIO(1);
  MF(cq_10, b47_00, b47_01, qa15_1);
  WLG(4);
  MF(cq_11, b46_10, b46_11, qa14_1);
  MF(cq_11, b47_10, b47_11, qa15_1);
  MF(cq_12, b46_20, b46_21, qa14_1);
  NOP(3);
  WR(cq_10, xw, 0);
  MF(cq_12, b47_20, b47_21, qa15_1);
  PRIO(0);
  WLG(1);
  MF(cq_13, b46_30, b46_31, qa14_1);
  NOP(1);
  WR(cq_11, xw, 1024);
  MF(cq_13, b47_30, b47_31, qa15_1);
  MF(cq_00, b46_00, b46_01, qa14_0);
  NOP(1);
  WR(cq_12, xw, 2048);
  MF(cq_01, b46_10, b46_11, qa14_0);
  MF(cq_02, b46_20, b46_21, qa14_0);
  NOP(3);
  WR(cq_13, xw, 3072);
  MF(cq_03, b46_30, b46_31, qa14_0);
  MF(cq_00, b47_00, b47_01, qa15_0);
  MF(cq_01, b47_10, b47_11, qa15_0);
  MF(cq_02, b47_20, b47_21, qa15_0);
  MF(cq_03, b47_30, b47_31, qa15_0);
  PRIO(0);
  STP(2);
  WVM(13);
  WLG(0);
  BAR(); STB(12);
  RDF(cq_r0, xr, 0);
  RDF(cq_r1, xr, 1024);
  RDF(cq_r2, xr, 2048);
  RDF(cq_r3, xr, 3072);
  PIN(kp0); PIN(kp1); PIN(vp00); PIN(vp01); PIN(vp10); PIN(vp11);
  WVM(5);
  PIN(hl0); PIN(hl1); PIN(hl2); PIN(hl3);
  if (wid0 == 0) {
    { unsigned t_ = mx8(hl0, hl1); CHK2(t_, hl0, hl1, v16, hsi, sL + L_HALO, hsi, sL + L_HALO + 1024, 2); }
    { unsigned t_ = mx8(hl2, hl3); CHK2(t_, hl2, hl3, v16, hsi, sL + L_HALO + 2048, hsi, sL + L_HALO + 3072, 2); }
    kp0 = __builtin_bit_cast(bf16x8, hl0); kp1 = __builtin_bit_cast(bf16x8, hl1);
    vp00 = __builtin_bit_cast(s16x4, (u32x2){hl2[0], hl2[1]}); vp01 = __builtin_bit_cast(s16x4, (u32x2){hl2[2], hl2[3]});
    vp10 = __builtin_bit_cast(s16x4, (u32x2){hl3[0], hl3[1]}); vp11 = __builtin_bit_cast(s16x4, (u32x2){hl3[2], hl3[3]});
  }
  WLG(0);
  PIN(cq_r0); PIN(cq_00);
  PIN(cq_r1); PIN(cq_01);
  PIN(cq_r2); PIN(cq_02);
  PIN(cq_r3); PIN(cq_03);
  const f32x4 cq_t0 = (cq_00 + cq_r0) * rsq[0] + cq_u0;
  const f32x4 cq_t1 = (cq_01 + cq_r1) * rsq[0] + cq_u1;
  const f32x4 cq_t2 = (cq_02 + cq_r2) * rsq[0] + cq_u2;
  const f32x4 cq_t3 = (cq_03 + cq_r3) * rsq[0] + cq_u3;
  u32x4 qf0 = {pk2(cq_t0[0], cq_t0[1]), pk2(cq_t0[2], cq_t0[3]), pk2(cq_t1[0], cq_t1[1]), pk2(cq_t1[2], cq_t1[3])}; PIN(qf0);
  u32x4 qf1 = {pk2(cq_t2[0], cq_t2[1]), pk2(cq_t2[2], cq_t2[3]), pk2(cq_t3[0], cq_t3[1]), pk2(cq_t3[2], cq_t3[3])}; PIN(qf1);
  PIN(kp0); PIN(kp1); PIN(ko0); PIN(ko1); PIN(vp00); PIN(vp01); PIN(vp10); PIN(vp11); PIN(vo00); PIN(vo01); PIN(vo10); PIN(vo11);
  u32x4 ao[2]; attn2(ao, {qf0, qf1}, {kp0, kp1}, {ko0, ko1}, {{CAT8(vp00, vo00), CAT8(vp01, vo01)}, {CAT8(vp10, vo10), CAT8(vp11, vo11)}}, par, own0, own1, nop);
  GST(ao[0], v16, so, sL + L_OB + 0);
  GST(ao[1], v16, so, sL + L_OB + 1024);
  STP(3);
  VW(vw1, 7168);
  RDT(b48_00, bb0, 28672);
  RDT(b48_01, bb0, 30720);
  RDT(b48_10, bb1, 28672);
  RDT(b48_11, bb1, 30720);
  RDT(b48_20, bb2, 28672);
  RDT(b48_21, bb2, 30720);
  RDT(b48_30, bb3, 28672);
  RDT(b48_31, bb3, 30720);
  RDT(b49_00, bb0, 32768);
  RDT(b49_01, bb0, 34816);
  RDT(b49_10, bb1, 32768);
  RDT(b49_11, bb1, 34816);
  RDT(b49_20, bb2, 32768);
  RDT(b49_21, bb2, 34816);
  RDT(b49_30, bb3, 32768);
  RDT(b49_31, bb3, 34816);
  RDT(b50_00, bb0, 36864);
  RDT(b50_01, bb0, 38912);
  RDT(b50_10, bb1, 36864);
  RDT(b50_11, bb1, 38912);
  RDT(b50_20, bb2, 36864);
  RDT(b50_21, bb2, 38912);
  RDT(b50_30, bb3, 36864);
  RDT(b50_31, bb3, 38912);
  WLG(0);
  WVM(0);
  BAR(); STB(13);
  ARRIVE(L_CN + CNB);
  { PIN(dw0); PIN(dw1); PIN(dw2); PIN(dw3);
    IDS(); LANE(l_); float* scr = reinterpret_cast<float*>(smem + XCH_OFF);
    colsum_put<224>(dw0, dw1, dw2, dw3, scr, wid, l_);
    BAR();
    colsum_out<224>(rS, scr, wid, l_, WS_PC + (ct * 16 + panel) * 1664 + 768);
  }
  DMA(rO, vwo, s_o, 655360, 8192);
  DMA(rO, vwo, s_o, 720896, 12288);
  DMA(rO, vwo, s_o, 786432, 16384);
  DMA(rO, vwo, s_o, 851968, 20480);
  DMA(rO, vwo, s_o, 917504, 24576);
  DMA(rO, vwo, s_o, 983040, 28672);
  DMA(r1, vw1, s_1w, 0, 32768);
  DMA(r1, vw1, s_1w, 128, 36864);
  RDT(b51_00, bb0, 40960);
  RDT(b51_01, bb0, 43008);
  RDT(b51_10, bb1, 40960);
  RDT(b51_11, bb1, 43008);
  RDT(b51_20, bb2, 40960);
  RDT(b51_21, bb2, 43008);
  RDT(b51_30, bb3, 40960);
  RDT(b51_31, bb3, 43008);
  RDT(b52_00, bb0, 53248);
  RDT(b52_01, bb0, 55296);
  RDT(b52_10, bb1, 53248);
  RDT(b52_11, bb1, 55296);
  RDT(b52_20, bb2, 53248);
  RDT(b52_21, bb2, 55296);
  RDT(b52_30, bb3, 53248);
  RDT(b52_31, bb3, 55296);
  RDT(b53_00, bb0, 57344);
  RDT(b53_01, bb0, 59392);
  RDT(b53_10, bb1, 57344);
  RDT(b53_11, bb1, 59392);
  RDT(b53_20, bb2, 57344);
  RDT(b53_21, bb2, 59392);
  RDT(b53_30, bb3, 57344);
  RDT(b53_31, bb3, 59392);
  WLG(0);
  MEET(L_CN + CNB);
  STP(21);
  BAR(); STB(14);
  STP(4);
  PRIO(3);
  GLD(oa0_0, v16, sa0, sL + L_OB + 0);
  GLD(oa0_1, v16, sa1, sL + L_OB + 0);
  PRIO(2);
  GLD(oa1_0, v16, sa0, sL + L_OB + 1024);
  GLD(oa1_1, v16, sa1, sL + L_OB + 1024);
  PRIO(1);
  GLD(oa2_0, v16, sa0, sL + L_OB + 2048);
  GLD(oa2_1, v16, sa1, sL + L_OB + 2048);
  PRIO(0);
  GLD(oa3_0, v16, sa0, sL + L_OB + 3072);
  GLD(oa3_1, v16, sa1, sL + L_OB + 3072);
  unsigned rv_; { LANE(l_); rv_ = (l_ >> 3) * 2048 + (l_ & 7) * 16; PIN(rv_); } SV(rs_, mk * 32768 + ct * 128); SV(rl_, rg * 3072);
  PRIO(3);
  WVM(6);
  MF0(co_00, b48_00, b48_01, oa0_0);
  GLD(oa4_0, v16, sa0, sL + L_OB + 4096);
  MF0(co_10, b48_00, b48_01, oa0_1);
  MF0(co_01, b48_10, b48_11, oa0_0);
  MF0(co_11, b48_10, b48_11, oa0_1);
  MF0(co_02, b48_20, b48_21, oa0_0);
  GLD(oa4_1, v16, sa1, sL + L_OB + 4096);
  MF0(co_12, b48_20, b48_21, oa0_1);
  MF0(co_03, b48_30, b48_31, oa0_0);
  WVM(6);
  MF0(co_13, b48_30, b48_31, oa0_1);
  STP(24);
  MF(co_00, b49_00, b49_01, oa1_0);
  GLD(oa5_0, v16, sa0, sL + L_OB + 5120);
  MF(co_10, b49_00, b49_01, oa1_1);
  MF(co_01, b49_10, b49_11, oa1_0);
  MF(co_11, b49_10, b49_11, oa1_1);
  MF(co_02, b49_20, b49_21, oa1_0);
  GLD(oa5_1, v16, sa1, sL + L_OB + 5120);
  MF(co_12, b49_20, b49_21, oa1_1);
  MF(co_03, b49_30, b49_31, oa1_0);
  WVM(6);
  MF(co_13, b49_30, b49_31, oa1_1);
  PRIO(2);
  MF(co_00, b50_00, b50_01, oa2_0);
  GLD(oa6_0, v16, sa0, sL + L_OB + 6144);
  MF(co_10, b50_00, b50_01, oa2_1);
  MF(co_01, b50_10, b50_11, oa2_0);
  MF(co_11, b50_10, b50_11, oa2_1);
  MF(co_02, b50_20, b50_21, oa2_0);
  GLD(oa6_1, v16, sa1, sL + L_OB + 6144);
  MF(co_12, b50_20, b50_21, oa2_1);
  MF(co_03, b50_30, b50_31, oa2_0);
  WVM(6);
  MF(co_13, b50_30, b50_31, oa2_1);
  PRIO(1);
  MF(co_00, b51_00, b51_01, oa3_0);
  GLD(oa7_0, v16, sa0, sL + L_OB + 7168);
  MF(co_10, b51_00, b51_01, oa3_1);
  MF(co_01, b51_10, b51_11, oa3_0);
  MF(co_11, b51_10, b51_11, oa3_1);
  MF(co_02, b51_20, b51_21, oa3_0);
  PRIO(0);
  GLD(oa7_1, v16, sa1, sL + L_OB + 7168);
  MF(co_12, b51_20, b51_21, oa3_1);
  MF(co_03, b51_30, b51_31, oa3_0);
  WVM(6);
  MF(co_13, b51_30, b51_31, oa3_1);
  MF(co_00, b52_00, b52_01, oa4_0);
  GLD(oa8_0, v16, sa0, sL + L_OB + 8192);
  MF(co_10, b52_00, b52_01, oa4_1);
  MF(co_01, b52_10, b52_11, oa4_0);
  MF(co_11, b52_10, b52_11, oa4_1);
  MF(co_02, b52_20, b52_21, oa4_0);
  GLD(oa8_1, v16, sa1, sL + L_OB + 8192);
  MF(co_12, b52_20, b52_21, oa4_1);
  MF(co_03, b52_30, b52_31, oa4_0);
  WVM(6);
  MF(co_13, b52_30, b52_31, oa4_1);
  MF(co_00, b53_00, b53_01, oa5_0);
  RDT(b54_00, bb0, 45056);
  RDT(b54_01, bb0, 47104);
  GLD(oa9_0, v16, sa0, sL + L_OB + 9216);
  MF(co_10, b53_00, b53_01, oa5_1);
  MF(co_01, b53_10, b53_11, oa5_0);
  RDT(b54_10, bb1, 45056);
  RDT(b54_11, bb1, 47104);
  MF(co_11, b53_10, b53_11, oa5_1);
  MF(co_02, b53_20, b53_21, oa5_0);
  RDT(b54_20, bb2, 45056);
  RDT(b54_21, bb2, 47104);
  GLD(oa9_1, v16, sa1, sL + L_OB + 9216);
  MF(co_12, b53_20, b53_21, oa5_1);
  MF(co_03, b53_30, b53_31, oa5_0);
  RDT(b54_30, bb3, 45056);
  RDT(b54_31, bb3, 47104);
  WVM(6);
  MF(co_13, b53_30, b53_31, oa5_1);
  WLG(4);
  MF(co_00, b54_00, b54_01, oa6_0);
  RDT(b55_00, bb0, 49152);
  RDT(b55_01, bb0, 51200);
  GLD(oa10_0, v16, sa0, sL + L_OB + 10240);
  MF(co_10, b54_00, b54_01, oa6_1);
  MF(co_01, b54_10, b54_11, oa6_0);
  RDT(b55_10, bb1, 49152);
  RDT(b55_11, bb1, 51200);
  MF(co_11, b54_10, b54_11, oa6_1);
  WLG(4);
  MF(co_02, b54_20, b54_21, oa6_0);
  RDT(b55_20, bb2, 49152);
  RDT(b55_21, bb2, 51200);
  GLD(oa10_1, v16, sa1, sL + L_OB + 10240);
  MF(co_12, b54_20, b54_21, oa6_1);
  MF(co_03, b54_30, b54_31, oa6_0);
  RDT(b55_30, bb3, 49152);
  RDT(b55_31, bb3, 51200);
  WVM(6);
  MF(co_13, b54_30, b54_31, oa6_1);
  WLG(4);
  MF(co_00, b55_00, b55_01, oa7_0);
  BAR(); STB(15);
  PRIO(3);
  RDT(b56_00, bb0, 0);
  RDT(b56_01, bb0, 2048);
  GLD(oa11_0, v16, sa0, sL + L_OB + 11264);
  MF(co_10, b55_00, b55_01, oa7_1);
  MF(co_01, b55_10, b55_11, oa7_0);
  RDT(b56_10, bb1, 0);
  RDT(b56_11, bb1, 2048);
  MF(co_11, b55_10, b55_11, oa7_1);
  WLG(4);
  MF(co_02, b55_20, b55_21, oa7_0);
  RDT(b56_20, bb2, 0);
  RDT(b56_21, bb2, 2048);
  GLD(oa11_1, v16, sa1, sL + L_OB + 11264);
  MF(co_12, b55_20, b55_21, oa7_1);
  MF(co_03, b55_30, b55_31, oa7_0);
  RDT(b56_30, bb3, 0);
  RDT(b56_31, bb3, 2048);
  WVM(6);
  MF(co_13, b55_30, b55_31, oa7_1);
  WLG(4);
  MF(co_00, b56_00, b56_01, oa8_0);
  RDT(b57_00, bb0, 4096);
  RDT(b57_01, bb0, 6144);
  GLD(oa12_0, v16, sa0, sL + L_OB + 12288);
  MF(co_10, b56_00, b56_01, oa8_1);
  MF(co_01, b56_10, b56_11, oa8_0);
  RDT(b57_10, bb1, 4096);
  RDT(b57_11, bb1, 6144);
  MF(co_11, b56_10, b56_11, oa8_1);
  WLG(4);
  MF(co_02, b56_20, b56_21, oa8_0);
  RDT(b57_20, bb2, 4096);
  RDT(b57_21, bb2, 6144);
  GLD(oa12_1, v16, sa1, sL + L_OB + 12288);
  MF(co_12, b56_20, b56_21, oa8_1);
  MF(co_03, b56_30, b56_31, oa8_0);
  RDT(b57_30, bb3, 4096);
  RDT(b57_31, bb3, 6144);
  WVM(6);
  MF(co_13, b56_30, b56_31, oa8_1);
  WLG(4);
  MF(co_00, b57_00, b57_01, oa9_0);
  PRIO(2);
  RDT(b58_00, bb0, 8192);
  RDT(b58_01, bb0, 10240);
  GLD(oa13_0, v16, sa0, sL + L_OB + 13312);
  MF(co_10, b57_00, b57_01, oa9_1);
  MF(co_01, b57_10, b57_11, oa9_0);
  RDT(b58_10, bb1, 8192);
  RDT(b58_11, bb1, 10240);
  MF(co_11, b57_10, b57_11, oa9_1);
  WLG(4);
  MF(co_02, b57_20, b57_21, oa9_0);
  RDT(b58_20, bb2, 8192);
  RDT(b58_21, bb2, 10240);
  GLD(oa13_1, v16, sa1, sL + L_OB + 13312);
  MF(co_12, b57_20, b57_21, oa9_1);
  MF(co_03, b57_30, b57_31, oa9_0);
  RDT(b58_30, bb3, 8192);
  RDT(b58_31, bb3, 10240);
  WVM(6);
  MF(co_13, b57_30, b57_31, oa9_1);
  WLG(4);
  MF(co_00, b58_00, b58_01, oa10_0);
  PRIO(1);
  RDT(b59_00, bb0, 12288);
  RDT(b59_01, bb0, 14336);
  { const unsigned sld = sld0 + rl_; DMA(rA, rv_, rs_, 0, 65536); }
  MF(co_10, b58_00, b58_01, oa10_1);
  MF(co_01, b58_10, b58_11, oa10_0);
  RDT(b59_10, bb1, 12288);
  RDT(b59_11, bb1, 14336);
  MF(co_11, b58_10, b58_11, oa10_1);
  WLG(4);
  MF(co_02, b58_20, b58_21, oa10_0);
  RDT(b59_20, bb2, 12288);
  RDT(b59_21, bb2, 14336);
  { const unsigned sld = sld0 + rl_; DMA(rA, rv_, rs_, 16384, 66560); }
  MF(co_12, b58_20, b58_21, oa10_1);
  PRIO(0);
  MF(co_03, b58_30, b58_31, oa10_0);
  RDT(b59_30, bb3, 12288);
  RDT(b59_31, bb3, 14336);
  WVM(6);
  MF(co_13, b58_30, b58_31, oa10_1);
  WLG(4);
  MF(co_00, b59_00, b59_01, oa11_0);
  BAR(); STB(16);
  PRIO(3);
  RDT(b60_00, bb0, 16384);
  RDT(b60_01, bb0, 18432);
  GLD(oa14_0, v16, sa0, sL + L_OB + 14336);
  MF(co_10, b59_00, b59_01, oa11_1);
  MF(co_01, b59_10, b59_11, oa11_0);
  RDT(b60_10, bb1, 16384);
  RDT(b60_11, bb1, 18432);
  MF(co_11, b59_10, b59_11, oa11_1);
  WLG(4);
  MF(co_02, b59_20, b59_21, oa11_0);
  RDT(b60_20, bb2, 16384);
  RDT(b60_21, bb2, 18432);
  GLD(oa14_1, v16, sa1, sL + L_OB + 14336);
  MF(co_12, b59_20, b59_21, oa11_1);
  MF(co_03, b59_30, b59_31, oa11_0);
  RDT(b60_30, bb3, 16384);
  RDT(b60_31, bb3, 18432);
  WVM(6);
  MF(co_13, b59_30, b59_31, oa11_1);
  WLG(4);
  MF(co_00, b60_00, b60_01, oa12_0);
  RDT(b61_00, bb0, 20480);
  RDT(b61_01, bb0, 22528);
  GLD(oa15_0, v16, sa0, sL + L_OB + 15360);
  MF(co_10, b60_00, b60_01, oa12_1);
  MF(co_01, b60_10, b60_11, oa12_0);
  RDT(b61_10, bb1, 20480);
  RDT(b61_11, bb1, 22528);
  MF(co_11, b60_10, b60_11, oa12_1);
  WLG(4);
  MF(co_02, b60_20, b60_21, oa12_0);
  RDT(b61_20, bb2, 20480);
  RDT(b61_21, bb2, 22528);
  GLD(oa15_1, v16, sa1, sL + L_OB + 15360);
  MF(co_12, b60_20, b60_21, oa12_1);
  MF(co_03, b60_30, b60_31, oa12_0);
  RDT(b61_30, bb3, 20480);
  RDT(b61_31, bb3, 22528);
  WVM(6);
  MF(co_13, b60_30, b60_31, oa12_1);
  WLG(4);
  MF(co_00, b61_00, b61_01, oa13_0);
  PRIO(2);
  RDT(b62_00, bb0, 24576);
  RDT(b62_01, bb0, 26624);
  DMA(r1, vw1, s_1w, 229376, 40960);
  MF(co_10, b61_00, b61_01, oa13_1);
  RDT(b63_00, bb0, 28672);
  RDT(b63_01, bb0, 30720);
  MF(co_01, b61_10, b61_11, oa13_0);
  RDT(b62_10, bb1, 24576);
  RDT(b62_11, bb1, 26624);
  WVM(5);
  int ra_; { LANE(l_); ra_ = xw - l_ * 16 + (l_ & 15) * 128 + (l_ >> 4) * 8; }
  RD8(rx00, ra_, 0);
  RD8(rx01, ra_, 32);
  RD8(rx10, ra_, 64);
  RD8(rx11, ra_, 96);
  MF(co_11, b61_10, b61_11, oa13_1);
  RDT(b63_10, bb1, 28672);
  RDT(b63_11, bb1, 30720);
  WLG(12);
  MF(co_02, b61_20, b61_21, oa13_0);
  RDT(b62_20, bb2, 24576);
  RDT(b62_21, bb2, 26624);
  DMA(r1, vw1, s_1w, 229504, 53248);
  MF(co_12, b61_20, b61_21, oa13_1);
  RDT(b63_20, bb2, 28672);
  RDT(b63_21, bb2, 30720);
  MF(co_03, b61_30, b61_31, oa13_0);
  RDT(b62_30, bb3, 24576);
  RDT(b62_31, bb3, 26624);
  WVM(4);
  MF(co_13, b61_30, b61_31, oa13_1);
  RDT(b63_30, bb3, 28672);
  RDT(b63_31, bb3, 30720);
  WVM(2);
  WLG(8);
  MF(co_10, b62_00, b62_01, oa14_1);
  PRIO(1);
  DMA(r1, vw1, s_1w, 458752, 57344);
  MF(co_10, b63_00, b63_01, oa15_1);
  MF(co_11, b62_10, b62_11, oa14_1);
  MF(co_11, b63_10, b63_11, oa15_1);
  WLG(0);
  MF(co_12, b62_20, b62_21, oa14_1);
  DMA(r1, vw1, s_1w, 458880, 45056);
  NOP(2);
  WR(co_10, xw, 0);
  MF(co_12, b63_20, b63_21, oa15_1);
  PRIO(0);
  MF(co_13, b62_30, b62_31, oa14_1);
  NOP(0);
  WR(co_11, xw, 1024);
  MF(co_13, b63_30, b63_31, oa15_1);
  MF(co_00, b62_00, b62_01, oa14_0);
  DMA(r1, vw1, s_1w, 688128, 49152);
  NOP(1);
  WR(co_12, xw, 2048);
  MF(co_01, b62_10, b62_11, oa14_0);
  MF(co_02, b62_20, b62_21, oa14_0);
  NOP(2);
  WR(co_13, xw, 3072);
  MF(co_03, b62_30, b62_31, oa14_0);
  MF(co_00, b63_00, b63_01, oa15_0);
  DMA(r1, vw1, s_1w, 688256, 0);
  MF(co_01, b63_10, b63_11, oa15_0);
  MF(co_02, b63_20, b63_21, oa15_0);
  MF(co_03, b63_30, b63_31, oa15_0);
  PRIO(0);
  STP(5);
  WLG(0);
  BAR(); STB(17);
  RDF(co_r0, xr, 0);
  RDF(co_r1, xr, 1024);
  RDF(co_r2, xr, 2048);
  RDF(co_r3, xr, 3072);
  WLG(0);
  PIN(rx00); PIN(rx01); PIN(rx10); PIN(rx11); const u32x4 rx0 = {rx00[0], rx00[1], rx01[0], rx01[1]}, rx1 = {rx10[0], rx10[1], rx11[0], rx11[1]};
  PIN(co_r0); PIN(co_00);
  PIN(co_r1); PIN(co_01);
  PIN(co_r2); PIN(co_02);
  PIN(co_r3); PIN(co_03);
  const u32x4 y0 = resid_add(co_00 + co_r0, co_01 + co_r1, rx0), y1 = resid_add(co_02 + co_r2, co_03 + co_r3, rx1);
  GST(y0, v16, so, sL + L_X1);
  GST(y1, v16, so, sL + L_X1 + 1024);
  { float s_ = 0.f, ss_ = 0.f; row_stats(y0, s_, ss_); row_stats(y1, s_, ss_);
  const u32x2 sv_ = {__float_as_uint(rows_sum(s_)), __float_as_uint(rows_sum(ss_))};
  V8(v8_); GST2(sv_, v8_, st1, sL + L_ST1); }
  STP(6);
  RDT(b64_00, bb0, 32768);
  RDT(b64_01, bb0, 34816);
  RDT(b64_10, bb1, 32768);
  RDT(b64_11, bb1, 34816);
  RDT(b64_20, bb2, 32768);
  RDT(b64_21, bb2, 34816);
  RDT(b64_30, bb3, 32768);
  RDT(b64_31, bb3, 34816);
  RDT(b65_00, bb0, 36864);
  RDT(b65_01, bb0, 38912);
  RDT(b65_10, bb1, 36864);
  RDT(b65_11, bb1, 38912);
  RDT(b65_20, bb2, 36864);
  RDT(b65_21, bb2, 38912);
  RDT(b65_30, bb3, 36864);
  RDT(b65_31, bb3, 38912);
  WLG(0);
  WVM(0);
  BAR(); STB(18);
  ARRIVE(L_CN + 2 * CNB);
  SETUP_DONE(1);
  DMA(r1, vw1, s_1w, 917504, 4096);
  DMA(r1, vw1, s_1w, 917632, 8192);
  DMA(r1, vw1, s_1w, 1146880, 12288);
  DMA(r1, vw1, s_1w, 1147008, 16384);
  DMA(r1, vw1, s_1w, 1376256, 20480);
  DMA(r1, vw1, s_1w, 1376384, 24576);
  DMA(r1, vw1, s_1w, 1605632, 28672);
  DMA(r1, vw1, s_1w, 1605760, 32768);
  DMA(r1, vw1, s_1w, 1835008, 36864);
  RDT(b66_00, bb0, 40960);
  RDT(b66_01, bb0, 43008);
  RDT(b66_10, bb1, 40960);
  RDT(b66_11, bb1, 43008);
  RDT(b66_20, bb2, 40960);
  RDT(b66_21, bb2, 43008);
  RDT(b66_30, bb3, 40960);
  RDT(b66_31, bb3, 43008);
  RDT(b67_00, bb0, 53248);
  RDT(b67_01, bb0, 55296);
  RDT(b67_10, bb1, 53248);
  RDT(b67_11, bb1, 55296);
  RDT(b67_20, bb2, 53248);
  RDT(b67_21, bb2, 55296);
  RDT(b67_30, bb3, 53248);
  RDT(b67_31, bb3, 55296);
  RDT(b68_00, bb0, 57344);
  RDT(b68_01, bb0, 59392);
  RDT(b68_10, bb1, 57344);
  RDT(b68_11, bb1, 59392);
  RDT(b68_20, bb2, 57344);
  RDT(b68_21, bb2, 59392);
  RDT(b68_30, bb3, 57344);
  RDT(b68_31, bb3, 59392);
  RDT(b69_00, bb0, 45056);
  RDT(b69_01, bb0, 47104);
  RDT(b69_10, bb1, 45056);
  RDT(b69_11, bb1, 47104);
  RDT(b69_20, bb2, 45056);
  RDT(b69_21, bb2, 47104);
  RDT(b69_30, bb3, 45056);
  RDT(b69_31, bb3, 47104);
  WLG(0);
  MEET(L_CN + 2 * CNB);
  STP(22);
  BAR(); STB(19);
  STP(7);
  PRIO(3);
  GLD(fa0_0, v16, sa0, sL + L_X1 + 0);
  GLD(fa0_1, v16, sa1, sL + L_X1 + 0);
  PRIO(2);
  GLD(fa1_0, v16, sa0, sL + L_X1 + 1024);
  GLD(fa1_1, v16, sa1, sL + L_X1 + 1024);
  PRIO(1);
  GLD(fa2_0, v16, sa0, sL + L_X1 + 2048);
  GLD(fa2_1, v16, sa1, sL + L_X1 + 2048);
  PRIO(0);
  GLD(fa3_0, v16, sa0, sL + L_X1 + 3072);
  GLD(fa3_1, v16, sa1, sL + L_X1 + 3072);
  PRIO(3);
  WVM(6);
  MF0(f0_00, b64_00, b64_01, fa0_0);
  GLD(fa4_0, v16, sa0, sL + L_X1 + 4096);
  MF0(f0_10, b64_00, b64_01, fa0_1);
  MF0(f0_01, b64_10, b64_11, fa0_0);
  MF0(f0_11, b64_10, b64_11, fa0_1);
  MF0(f0_02, b64_20, b64_21, fa0_0);
  MF0(f0_12, b64_20, b64_21, fa0_1);
  GLD(fa4_1, v16, sa1, sL + L_X1 + 4096);
  MF0(f0_03, b64_30, b64_31, fa0_0);
  MF0(f0_13, b64_30, b64_31, fa0_1);
  STP(25);
  MF0(f1_00, b65_00, b65_01, fa0_0);
  MF0(f1_10, b65_00, b65_01, fa0_1);
  MF0(f1_01, b65_10, b65_11, fa0_0);
  GLD(fa5_0, v16, sa0, sL + L_X1 + 5120);
  MF0(f1_11, b65_10, b65_11, fa0_1);
  MF0(f1_02, b65_20, b65_21, fa0_0);
  MF0(f1_12, b65_20, b65_21, fa0_1);
  MF0(f1_03, b65_30, b65_31, fa0_0);
  GLD(fa5_1, v16, sa1, sL + L_X1 + 5120);
  WVM(8);
  MF0(f1_13, b65_30, b65_31, fa0_1);
  PRIO(2);
  MF(f0_00, b66_00, b66_01, fa1_0);
  MF(f0_10, b66_00, b66_01, fa1_1);
  MF(f0_01, b66_10, b66_11, fa1_0);
  MF(f0_11, b66_10, b66_11, fa1_1);
  GLD(fa6_0, v16, sa0, sL + L_X1 + 6144);
  MF(f0_02, b66_20, b66_21, fa1_0);
  MF(f0_12, b66_20, b66_21, fa1_1);
  MF(f0_03, b66_30, b66_31, fa1_0);
  MF(f0_13, b66_30, b66_31, fa1_1);
  PRIO(1);
  GLD(fa6_1, v16, sa1, sL + L_X1 + 6144);
  MF(f1_00, b67_00, b67_01, fa1_0);
  MF(f1_10, b67_00, b67_01, fa1_1);
  MF(f1_01, b67_10, b67_11, fa1_0);
  MF(f1_11, b67_10, b67_11, fa1_1);
  MF(f1_02, b67_20, b67_21, fa1_0);
  PRIO(0);
  GLD(fa7_0, v16, sa0, sL + L_X1 + 7168);
  MF(f1_12, b67_20, b67_21, fa1_1);
  MF(f1_03, b67_30, b67_31, fa1_0);
  WVM(9);
  MF(f1_13, b67_30, b67_31, fa1_1);
  MF(f0_00, b68_00, b68_01, fa2_0);
  MF(f0_10, b68_00, b68_01, fa2_1);
  GLD(fa7_1, v16, sa1, sL + L_X1 + 7168);
  MF(f0_01, b68_10, b68_11, fa2_0);
  MF(f0_11, b68_10, b68_11, fa2_1);
  MF(f0_02, b68_20, b68_21, fa2_0);
  MF(f0_12, b68_20, b68_21, fa2_1);
  DMA(r1, vw1, s_1w, 2064384, 40960);
  MF(f0_03, b68_30, b68_31, fa2_0);
  MF(f0_13, b68_30, b68_31, fa2_1);
  MF(f1_00, b69_00, b69_01, fa2_0);
  RDT(b70_00, bb0, 49152);
  RDT(b70_01, bb0, 51200);
  MF(f1_10, b69_00, b69_01, fa2_1);
  MF(f1_01, b69_10, b69_11, fa2_0);
  RDT(b70_10, bb1, 49152);
  RDT(b70_11, bb1, 51200);
  DMA(r1, vw1, s_1w, 2293760, 53248);
  MF(f1_11, b69_10, b69_11, fa2_1);
  MF(f1_02, b69_20, b69_21, fa2_0);
  RDT(b70_20, bb2, 49152);
  RDT(b70_21, bb2, 51200);
  MF(f1_12, b69_20, b69_21, fa2_1);
  MF(f1_03, b69_30, b69_31, fa2_0);
  RDT(b70_30, bb3, 49152);
  RDT(b70_31, bb3, 51200);
  WVM(10);
  MF(f1_13, b69_30, b69_31, fa2_1);
  DMA(r1, vw1, s_1w, 2523136, 57344);
  WLG(4);
  MF(f0_00, b70_00, b70_01, fa3_0);
  RDT(b71_00, bb0, 0);
  RDT(b71_01, bb0, 2048);
  MF(f0_10, b70_00, b70_01, fa3_1);
  MF(f0_01, b70_10, b70_11, fa3_0);
  RDT(b71_10, bb1, 0);
  RDT(b71_11, bb1, 2048);
  MF(f0_11, b70_10, b70_11, fa3_1);
  GLD(fa8_0, v16, sa0, sL + L_X1 + 8192);
  WLG(4);
  MF(f0_02, b70_20, b70_21, fa3_0);
  RDT(b71_20, bb2, 0);
  RDT(b71_21, bb2, 2048);
  MF(f0_12, b70_20, b70_21, fa3_1);
  MF(f0_03, b70_30, b70_31, fa3_0);
  RDT(b71_30, bb3, 0);
  RDT(b71_31, bb3, 2048);
  MF(f0_13, b70_30, b70_31, fa3_1);
  WLG(4);
  MF(f1_00, b71_00, b71_01, fa3_0);
  BAR(); STB(20);
  PRIO(3);
  RDT(b72_00, bb0, 4096);
  RDT(b72_01, bb0, 6144);
  GLD(fa8_1, v16, sa1, sL + L_X1 + 8192);
  MF(f1_10, b71_00, b71_01, fa3_1);
  MF(f1_01, b71_10, b71_11, fa3_0);
  RDT(b72_10, bb1, 4096);
  RDT(b72_11, bb1, 6144);
  MF(f1_11, b71_10, b71_11, fa3_1);
  WLG(4);
  MF(f1_02, b71_20, b71_21, fa3_0);
  RDT(b72_20, bb2, 4096);
  RDT(b72_21, bb2, 6144);
  GLD(fa9_0, v16, sa0, sL + L_X1 + 9216);
  MF(f1_12, b71_20, b71_21, fa3_1);
  MF(f1_03, b71_30, b71_31, fa3_0);
  RDT(b72_30, bb3, 4096);
  RDT(b72_31, bb3, 6144);
  WVM(12);
  MF(f1_13, b71_30, b71_31, fa3_1);
  WLG(4);
  MF(f0_00, b72_00, b72_01, fa4_0);
  RDT(b73_00, bb0, 8192);
  RDT(b73_01, bb0, 10240);
  MF(f0_10, b72_00, b72_01, fa4_1);
  GLD(fa9_1, v16, sa1, sL + L_X1 + 9216);
  MF(f0_01, b72_10, b72_11, fa4_0);
  RDT(b73_10, bb1, 8192);
  RDT(b73_11, bb1, 10240);
  MF(f0_11, b72_10, b72_11, fa4_1);
  WLG(4);
  MF(f0_02, b72_20, b72_21, fa4_0);
  RDT(b73_20, bb2, 8192);
  RDT(b73_21, bb2, 10240);
  MF(f0_12, b72_20, b72_21, fa4_1);
  MF(f0_03, b72_30, b72_31, fa4_0);
  RDT(b73_30, bb3, 8192);
  RDT(b73_31, bb3, 10240);
  GLD(fa10_0, v16, sa0, sL + L_X1 + 10240);
  MF(f0_13, b72_30, b72_31, fa4_1);
  WLG(4);
  MF(f1_00, b73_00, b73_01, fa4_0);
  PRIO(2);
  RDT(b74_00, bb0, 12288);
  RDT(b74_01, bb0, 14336);
  MF(f1_10, b73_00, b73_01, fa4_1);
  MF(f1_01, b73_10, b73_11, fa4_0);
  RDT(b74_10, bb1, 12288);
  RDT(b74_11, bb1, 14336);
  GLD(fa10_1, v16, sa1, sL + L_X1 + 10240);
  MF(f1_11, b73_10, b73_11, fa4_1);
  WLG(4);
  MF(f1_02, b73_20, b73_21, fa4_0);
  RDT(b74_20, bb2, 12288);
  RDT(b74_21, bb2, 14336);
  unsigned vff; { IDS(); LANE(l_); vff = ((l_ & 16) ? 2048 + ct * 128 : panel * 128) + (l_ & 15) * 8; PIN(vff); } const unsigned sff = wid0 == 0 ? WS_IF + 1 * 4096 : WS_END;
  GLD2(iff, vff, rS, sff);
  MF(f1_12, b73_20, b73_21, fa4_1);
  MF(f1_03, b73_30, b73_31, fa4_0);
  RDT(b74_30, bb3, 12288);
  RDT(b74_31, bb3, 14336);
  WVM(14);
  MF(f1_13, b73_30, b73_31, fa4_1);
  GLD(fa11_0, v16, sa0, sL + L_X1 + 11264);
  WLG(4);
  MF(f0_00, b74_00, b74_01, fa5_0);
  PRIO(1);
  RDT(b75_00, bb0, 16384);
  RDT(b75_01, bb0, 18432);
  MF(f0_10, b74_00, b74_01, fa5_1);
  MF(f0_01, b74_10, b74_11, fa5_0);
  RDT(b75_10, bb1, 16384);
  RDT(b75_11, bb1, 18432);
  MF(f0_11, b74_10, b74_11, fa5_1);
  GLD(fa11_1, v16, sa1, sL + L_X1 + 11264);
  WLG(4);
  MF(f0_02, b74_20, b74_21, fa5_0);
  RDT(b75_20, bb2, 16384);
  RDT(b75_21, bb2, 18432);
  MF(f0_12, b74_20, b74_21, fa5_1);
  PRIO(0);
  MF(f0_03, b74_30, b74_31, fa5_0);
  RDT(b75_30, bb3, 16384);
  RDT(b75_31, bb3, 18432);
  MF(f0_13, b74_30, b74_31, fa5_1);
  WLG(4);
  MF(f1_00, b75_00, b75_01, fa5_0);
  BAR(); STB(21);
  PRIO(3);
  RDT(b76_00, bb0, 20480);
  RDT(b76_01, bb0, 22528);
  DMA(r1, vw1, s_1w, 2752512, 45056);
  MF(f1_10, b75_00, b75_01, fa5_1);
  MF(f1_01, b75_10, b75_11, fa5_0);
  RDT(b76_10, bb1, 20480);
  RDT(b76_11, bb1, 22528);
  MF(f1_11, b75_10, b75_11, fa5_1);
  WLG(4);
  MF(f1_02, b75_20, b75_21, fa5_0);
  RDT(b76_20, bb2, 20480);
  RDT(b76_21, bb2, 22528);
  MF(f1_12, b75_20, b75_21, fa5_1);
  DMA(r1, vw1, s_1w, 2981888, 49152);
  MF(f1_03, b75_30, b75_31, fa5_0);
  RDT(b76_30, bb3, 20480);
  RDT(b76_31, bb3, 22528);
  WVM(16);
  MF(f1_13, b75_30, b75_31, fa5_1);
  WLG(4);
  MF(f0_00, b76_00, b76_01, fa6_0);
  RDT(b77_00, bb0, 24576);
  RDT(b77_01, bb0, 26624);
  MF(f0_10, b76_00, b76_01, fa6_1);
  DMA(r1, vw1, s_1w, 3211264, 0);
  MF(f0_01, b76_10, b76_11, fa6_0);
  RDT(b77_10, bb1, 24576);
  RDT(b77_11, bb1, 26624);
  MF(f0_11, b76_10, b76_11, fa6_1);
  WLG(4);
  MF(f0_02, b76_20, b76_21, fa6_0);
  RDT(b77_20, bb2, 24576);
  RDT(b77_21, bb2, 26624);
  MF(f0_12, b76_20, b76_21, fa6_1);
  MF(f0_03, b76_30, b76_31, fa6_0);
  RDT(b77_30, bb3, 24576);
  RDT(b77_31, bb3, 26624);
  DMA(r1, vw1, s_1w, 3440640, 4096);
  MF(f0_13, b76_30, b76_31, fa6_1);
  WLG(4);
  MF(f1_00, b77_00, b77_01, fa6_0);
  PRIO(2);
  RDT(b78_00, bb0, 28672);
  RDT(b78_01, bb0, 30720);
  MF(f1_10, b77_00, b77_01, fa6_1);
  MF(f1_01, b77_10, b77_11, fa6_0);
  RDT(b78_10, bb1, 28672);
  RDT(b78_11, bb1, 30720);
  GLD(fa12_0, v16, sa0, sL + L_X1 + 12288);
  MF(f1_11, b77_10, b77_11, fa6_1);
  WLG(4);
  MF(f1_02, b77_20, b77_21, fa6_0);
  RDT(b78_20, bb2, 28672);
  RDT(b78_21, bb2, 30720);
  MF(f1_12, b77_20, b77_21, fa6_1);
  MF(f1_03, b77_30, b77_31, fa6_0);
  RDT(b78_30, bb3, 28672);
  RDT(b78_31, bb3, 30720);
  WVM(17);
  MF(f1_13, b77_30, b77_31, fa6_1);
  GLD(fa12_1, v16, sa1, sL + L_X1 + 12288);
  WLG(4);
  MF(f0_00, b78_00, b78_01, fa7_0);
  PRIO(1);
  RDT(b79_00, bb0, 32768);
  RDT(b79_01, bb0, 34816);
  MF(f0_10, b78_00, b78_01, fa7_1);
  MF(f0_01, b78_10, b78_11, fa7_0);
  RDT(b79_10, bb1, 32768);
  RDT(b79_11, bb1, 34816);
  MF(f0_11, b78_10, b78_11, fa7_1);
  WLG(4);
  MF(f0_02, b78_20, b78_21, fa7_0);
  RDT(b79_20, bb2, 32768);
  RDT(b79_21, bb2, 34816);
  GLD(fa13_0, v16, sa0, sL + L_X1 + 13312);
  MF(f0_12, b78_20, b78_21, fa7_1);
  PRIO(0);
  MF(f0_03, b78_30, b78_31, fa7_0);
  RDT(b79_30, bb3, 32768);
  RDT(b79_31, bb3, 34816);
  WVM(9);
  CHKT(iff, vff, sff);
  MF(f0_13, b78_30, b78_31, fa7_1);
  WLG(4);
  MF(f1_00, b79_00, b79_01, fa7_0);
  BAR(); STB(22);
  PRIO(3);
  RDT(b80_00, bb0, 36864);
  RDT(b80_01, bb0, 38912);
  GLD(fa13_1, v16, sa1, sL + L_X1 + 13312);
  unsigned vcf; { IDS(); LANE(l_); const int td = wid * 64 + l_; vcf = td < 448 ? WS_PC + (ct * 16 + (l_ >> 3) * 2) * 1664 + 768 + wid * 128 + (l_ & 7) * 16 : WS_END; PIN(vcf); } const unsigned zf = 0;
  MF(f1_10, b79_00, b79_01, fa7_1);
  MF(f1_01, b79_10, b79_11, fa7_0);
  RDT(b80_10, bb1, 36864);
  RDT(b80_11, bb1, 38912);
  MF(f1_11, b79_10, b79_11, fa7_1);
  WLG(4);
  MF(f1_02, b79_20, b79_21, fa7_0);
  RDT(b80_20, bb2, 36864);
  RDT(b80_21, bb2, 38912);
  MF(f1_12, b79_20, b79_21, fa7_1);
  GLD(pcf0, vcf, zf, 0);
  V8(v8f);
  MF(f1_03, b79_30, b79_31, fa7_0);
  RDT(b80_30, bb3, 36864);
  RDT(b80_31, bb3, 38912);
  MF(f1_13, b79_30, b79_31, fa7_1);
  STP(14);
  WLG(4);
  MF(f0_00, b80_00, b80_01, fa8_0);
  RDT(b81_00, bb0, 40960);
  RDT(b81_01, bb0, 43008);
  MF(f0_10, b80_00, b80_01, fa8_1);
  GLD(pcf1, vcf, zf, 1664);
  MF(f0_01, b80_10, b80_11, fa8_0);
  RDT(b81_10, bb1, 40960);
  RDT(b81_11, bb1, 43008);
  MF(f0_11, b80_10, b80_11, fa8_1);
  WLG(4);
  MF(f0_02, b80_20, b80_21, fa8_0);
  RDT(b81_20, bb2, 40960);
  RDT(b81_21, bb2, 43008);
  MF(f0_12, b80_20, b80_21, fa8_1);
  MF(f0_03, b80_30, b80_31, fa8_0);
  RDT(b81_30, bb3, 40960);
  RDT(b81_31, bb3, 43008);
  GLD(fa14_0, v16, sa0, sL + L_X1 + 14336);
  MF(f0_13, b80_30, b80_31, fa8_1);
  WLG(4);
  MF(f0_00, b81_00, b81_01, fa9_0);
  PRIO(2);
  RDT(b82_00, bb0, 53248);
  RDT(b82_01, bb0, 55296);
  MF(f0_10, b81_00, b81_01, fa9_1);
  MF(f0_01, b81_10, b81_11, fa9_0);
  RDT(b82_10, bb1, 53248);
  RDT(b82_11, bb1, 55296);
  MF(f0_11, b81_10, b81_11, fa9_1);
  GLD(fa14_1, v16, sa1, sL + L_X1 + 14336);
  WLG(4);
  MF(f0_02, b81_20, b81_21, fa9_0);
  RDT(b82_20, bb2, 53248);
  RDT(b82_21, bb2, 55296);
  MF(f0_12, b81_20, b81_21, fa9_1);
  MF(f0_03, b81_30, b81_31, fa9_0);
  RDT(b82_30, bb3, 53248);
  RDT(b82_31, bb3, 55296);
  MF(f0_13, b81_30, b81_31, fa9_1);
  GL8(stf0, v8f, sst, sL + L_ST1 + 0);
  GL8(stf1, v8f, sst, sL + L_ST1 + 512);
  WLG(4);
  MF(f0_00, b82_00, b82_01, fa10_0);
  PRIO(1);
  RDT(b83_00, bb0, 57344);
  RDT(b83_01, bb0, 59392);
  MF(f0_10, b82_00, b82_01, fa10_1);
  MF(f0_01, b82_10, b82_11, fa10_0);
  RDT(b83_10, bb1, 57344);
  RDT(b83_11, bb1, 59392);
  MF(f0_11, b82_10, b82_11, fa10_1);
  WLG(4);
  MF(f0_02, b82_20, b82_21, fa10_0);
  RDT(b83_20, bb2, 57344);
  RDT(b83_21, bb2, 59392);
  GL8(stf2, v8f, sst, sL + L_ST1 + 1024);
  GL8(stf3, v8f, sst, sL + L_ST1 + 1536);
  MF(f0_12, b82_20, b82_21, fa10_1);
  PRIO(0);
  MF(f0_03, b82_30, b82_31, fa10_0);
  RDT(b83_30, bb3, 57344);
  RDT(b83_31, bb3, 59392);
  GMK(v16, shn, sL + L_HB);
  WVM(17);
  MF(f0_13, b82_30, b82_31, fa10_1);
  WLG(4);
  MF(f0_00, b83_00, b83_01, fa11_0);
  WVM(13);
  BAR(); STB(23);
  PRIO(3);
  RDT(b84_00, bb0, 45056);
  RDT(b84_01, bb0, 47104);
  MF(f0_10, b83_00, b83_01, fa11_1);
  MF(f0_01, b83_10, b83_11, fa11_0);
  RDT(b84_10, bb1, 45056);
  RDT(b84_11, bb1, 47104);
  MF(f0_11, b83_10, b83_11, fa11_1);
  GLD(fa15_0, v16, sa0, sL + L_X1 + 15360);
  WLG(4);
  MF(f0_02, b83_20, b83_21, fa11_0);
  RDT(b84_20, bb2, 45056);
  RDT(b84_21, bb2, 47104);
  MF(f0_12, b83_20, b83_21, fa11_1);
  MF(f0_03, b83_30, b83_31, fa11_0);
  RDT(b84_30, bb3, 45056);
  RDT(b84_31, bb3, 47104);
  WVM(12);
  MF(f0_13, b83_30, b83_31, fa11_1);
  WLG(4);
  MF(f0_00, b84_00, b84_01, fa12_0);
  RDT(b85_00, bb0, 49152);
  RDT(b85_01, bb0, 51200);
  GLD(fa15_1, v16, sa1, sL + L_X1 + 15360);
  MF(f0_10, b84_00, b84_01, fa12_1);
  MF(f0_01, b84_10, b84_11, fa12_0);
  RDT(b85_10, bb1, 49152);
  RDT(b85_11, bb1, 51200);
  MF(f0_11, b84_10, b84_11, fa12_1);
  WLG(4);
  MF(f0_02, b84_20, b84_21, fa12_0);
  RDT(b85_20, bb2, 49152);
  RDT(b85_21, bb2, 51200);
  WVM(9);
  PIN(pcf0); PIN(pcf1); f32x4 csf = __builtin_bit_cast(f32x4, pcf0) + __builtin_bit_cast(f32x4, pcf1); PIN(csf);
  MF(f0_12, b84_20, b84_21, fa12_1);
  DMA(r1, vw1, s_1w, 1835136, 8192);
  csf[0] = half_sum<8>(csf[0]); csf[1] = half_sum<8>(csf[1]); PIN(csf);
  MF(f0_03, b84_30, b84_31, fa12_0);
  RDT(b85_30, bb3, 49152);
  RDT(b85_31, bb3, 51200);
  csf[2] = half_sum<8>(csf[2]); csf[3] = half_sum<8>(csf[3]); PIN(csf);
  MF(f0_13, b84_30, b84_31, fa12_1);
  csf[0] = half_sum<16>(csf[0]); csf[1] = half_sum<16>(csf[1]); PIN(csf);
  WLG(4);
  MF(f0_00, b85_00, b85_01, fa13_0);
  PRIO(2);
  RDT(b86_00, bb0, 0);
  RDT(b86_01, bb0, 2048);
  csf[2] = half_sum<16>(csf[2]); csf[3] = half_sum<16>(csf[3]); PIN(csf);
  MF(f0_10, b85_00, b85_01, fa13_1);
  RDT(b87_00, bb0, 4096);
  RDT(b87_01, bb0, 6144);
  DMA(r1, vw1, s_1w, 2064512, 12288);
  csf[0] = half_sum<32>(csf[0]); csf[1] = half_sum<32>(csf[1]); PIN(csf);
  MF(f0_01, b85_10, b85_11, fa13_0);
  RDT(b86_10, bb1, 0);
  RDT(b86_11, bb1, 2048);
  csf[2] = half_sum<32>(csf[2]); csf[3] = half_sum<32>(csf[3]); PIN(csf);
  MF(f0_11, b85_10, b85_11, fa13_1);
  RDT(b87_10, bb1, 4096);
  RDT(b87_11, bb1, 6144);
  int taf; { IDS(); LANE(l_); const int td = wid * 64 + l_; taf = (int)lds0 + TAB_OFF + (td < 448 ? 768 + wid * 128 : 2048) + (l_ & 7) * 16; }
  WR(csf, taf, 0);
  WLG(9);
  MF(f0_02, b85_20, b85_21, fa13_0);
  RDT(b86_20, bb2, 0);
  RDT(b86_21, bb2, 2048);
  MF(f0_12, b85_20, b85_21, fa13_1);
  RDT(b87_20, bb2, 4096);
  RDT(b87_21, bb2, 6144);
  MF(f0_03, b85_30, b85_31, fa13_0);
  RDT(b86_30, bb3, 0);
  RDT(b86_31, bb3, 2048);
  DMA(r1, vw1, s_1w, 2293888, 16384);
  WVM(10);
  MF(f0_13, b85_30, b85_31, fa13_1);
  RDT(b87_30, bb3, 4096);
  RDT(b87_31, bb3, 6144);
  WVM(3);
  WLG(9);
  MF(f0_10, b86_00, b86_01, fa14_1);
  PRIO(1);
  MF(f0_10, b87_00, b87_01, fa15_1);
  MF(f0_11, b86_10, b86_11, fa14_1);
  DMA(r1, vw1, s_1w, 2523264, 20480);
  MF(f0_11, b87_10, b87_11, fa15_1);
  WLG(0);
  MF(f0_12, b86_20, b86_21, fa14_1);
  PIN(stf0); PIN(stf1); PIN(stf2); PIN(stf3); float sf = (stf0[0] + stf1[0]) + (stf2[0] + stf3[0]), tf = (stf0[1] + stf1[1]) + (stf2[1] + stf3[1]); PIN(sf); PIN(tf);
  NOP(2);
  WR(f0_10, xw, 0);
  MF(f0_12, b87_20, b87_21, fa15_1);
  PRIO(0);
  sf = half_sum<16>(sf); tf = half_sum<16>(tf); PIN(sf); PIN(tf);
  MF(f0_13, b86_30, b86_31, fa14_1);
  sf = half_sum<32>(sf) * (1.0f / D_MODEL); tf = half_sum<32>(tf); PIN(sf); PIN(tf);
  NOP(1);
  WR(f0_11, xw, 1024);
  MF(f0_13, b87_30, b87_31, fa15_1);
  DMA(r1, vw1, s_1w, 2752640, 24576);
  tf = __builtin_amdgcn_rsqf(fmaxf(tf * (1.0f / D_MODEL) - sf * sf, 0.f) + 1e-5f); PIN(tf);
  MF(f0_00, b86_00, b86_01, fa14_0);
  sf = -sf * tf; PIN(sf); const f32x2 rsf = {tf, tf}, nmf = {sf, sf};
  NOP(1);
  WR(f0_12, xw, 2048);
  MF(f0_01, b86_10, b86_11, fa14_0);
  MF(f0_02, b86_20, b86_21, fa14_0);
  NOP(2);
  WR(f0_13, xw, 3072);
  MF(f0_03, b86_30, b86_31, fa14_0);
  WVM(1);
  WLG(0);
  BAR(); STB(24);
  PRIO(3);
  RDT(b88_00, bb0, 8192);
  RDT(b88_01, bb0, 10240);
  MF(f0_00, b87_00, b87_01, fa15_0);
  RDT(b88_10, bb1, 8192);
  RDT(b88_11, bb1, 10240);
  DMA(r1, vw1, s_1w, 2982016, 28672);
  MF(f0_01, b87_10, b87_11, fa15_0);
  RDT(b88_20, bb2, 8192);
  RDT(b88_21, bb2, 10240);
  MF(f0_02, b87_20, b87_21, fa15_0);
  RDT(b88_30, bb3, 8192);
  RDT(b88_31, bb3, 10240);
  MF(f0_03, b87_30, b87_31, fa15_0);
  WLG(4);
  MF(f1_00, b88_00, b88_01, fa8_0);
  RDT(b89_00, bb0, 12288);
  RDT(b89_01, bb0, 14336);
  DMA(r1, vw1, s_1w, 3211392, 32768);
  RDF(f0_c0, tb, 768);
  RDF(f0_b0, tb, 2816);
  MF(f1_10, b88_00, b88_01, fa8_1);
  MF(f1_01, b88_10, b88_11, fa8_0);
  RDT(b89_10, bb1, 12288);
  RDT(b89_11, bb1, 14336);
  MF(f1_11, b88_10, b88_11, fa8_1);
  f32x2 f0_t00, f0_t01;
  WLG(2);
  FMA(f0_t00[0], f0_c0[0], nmf[0], f0_b0[0]);
  FMA(f0_t00[1], f0_c0[1], nmf[0], f0_b0[1]);
  MF(f1_02, b88_20, b88_21, fa8_0);
  RDT(b89_20, bb2, 12288);
  RDT(b89_21, bb2, 14336);
  FMA(f0_t01[0], f0_c0[2], nmf[0], f0_b0[2]);
  FMA(f0_t01[1], f0_c0[3], nmf[0], f0_b0[3]);
  MF(f1_12, b88_20, b88_21, fa8_1);
  DMA(r1, vw1, s_1w, 3440768, 36864);
  RDF(f0_r0, xr, 0);
  RDF(f0_r1, xr, 1024);
  FMA(f0_t00[0], f0_00[0], rsf[0], f0_t00[0]);
  MF(f1_03, b88_30, b88_31, fa8_0);
  RDT(b89_30, bb3, 12288);
  RDT(b89_31, bb3, 14336);
  FMA(f0_t00[1], f0_00[1], rsf[0], f0_t00[1]);
  FMA(f0_t01[0], f0_00[2], rsf[0], f0_t01[0]);
  MF(f1_13, b88_30, b88_31, fa8_1);
  FMA(f0_t01[1], f0_00[3], rsf[0], f0_t01[1]);
  RDF(f0_c1, tb, 832);
  RDF(f0_b1, tb, 2880);
  MF(f1_00, b89_00, b89_01, fa9_0);
  RDT(b90_00, bb0, 16384);
  RDT(b90_01, bb0, 18432);
  MF(f1_10, b89_00, b89_01, fa9_1);
  DMA(r1, vw1, s_1w, 256, 40960);
  WLG(7);
  FMA(f0_t00[0], f0_r0[0], rsf[0], f0_t00[0]);
  FMA(f0_t00[1], f0_r0[1], rsf[0], f0_t00[1]);
  MF(f1_01, b89_10, b89_11, fa9_0);
  RDT(b90_10, bb1, 16384);
  RDT(b90_11, bb1, 18432);
  FMA(f0_t01[0], f0_r0[2], rsf[0], f0_t01[0]);
  FMA(f0_t01[1], f0_r0[3], rsf[0], f0_t01[1]);
  MF(f1_11, b89_10, b89_11, fa9_1);
  PRIO(2);
  RDF(f0_r2, xr, 2048);
  RDF(f0_r3, xr, 3072);
  SQA(f0_t00[0], f0_t00[0]);
  MF(f1_02, b89_20, b89_21, fa9_0);
  RDT(b90_20, bb2, 16384);
  RDT(b90_21, bb2, 18432);
  SQA(f0_t00[1], f0_t00[1]);
  SQA(f0_t01[0], f0_t01[0]);
  MF(f1_12, b89_20, b89_21, fa9_1);
  SQA(f0_t01[1], f0_t01[1]);
  unsigned f0_p00; CVT(f0_p00, f0_t00[0], f0_t00[1]);
  WLG(6);
  MF(f1_03, b89_30, b89_31, fa9_0);
  RDT(b90_30, bb3, 16384);
  RDT(b90_31, bb3, 18432);
  DMA(r1, vw1, s_1w, 229632, 53248);
  unsigned f0_p01; CVT(f0_p01, f0_t01[0], f0_t01[1]);
  RLU(f0_p00);
  MF(f1_13, b89_30, b89_31, fa9_1);
  RLU(f0_p01);
  f32x2 f0_t10, f0_t11;
  FMA(f0_t10[0], f0_c1[0], nmf[0], f0_b1[0]);
  MF(f1_00, b90_00, b90_01, fa10_0);
  RDT(b91_00, bb0, 20480);
  RDT(b91_01, bb0, 22528);
  FMA(f0_t10[1], f0_c1[1], nmf[0], f0_b1[1]);
  FMA(f0_t11[0], f0_c1[2], nmf[0], f0_b1[2]);
  MF(f1_10, b90_00, b90_01, fa10_1);
  FMA(f0_t11[1], f0_c1[3], nmf[0], f0_b1[3]);
  FMA(f0_t10[0], f0_01[0], rsf[0], f0_t10[0]);
  WLG(4);
  MF(f1_01, b90_10, b90_11, fa10_0);
  RDT(b91_10, bb1, 20480);
  RDT(b91_11, bb1, 22528);
  FMA(f0_t10[1], f0_01[1], rsf[0], f0_t10[1]);
  FMA(f0_t11[0], f0_01[2], rsf[0], f0_t11[0]);
  MF(f1_11, b90_10, b90_11, fa10_1);
  PRIO(1);
  DMA(r1, vw1, s_1w, 459008, 57344);
  FMA(f0_t11[1], f0_01[3], rsf[0], f0_t11[1]);
  RDF(f0_c2, tb, 896);
  RDF(f0_b2, tb, 2944);
  MF(f1_02, b90_20, b90_21, fa10_0);
  RDT(b91_20, bb2, 20480);
  RDT(b91_21, bb2, 22528);
  MF(f1_12, b90_20, b90_21, fa10_1);
  FMA(f0_t10[0], f0_r1[0], rsf[0], f0_t10[0]);
  FMA(f0_t10[1], f0_r1[1], rsf[0], f0_t10[1]);
  WLG(6);
  MF(f1_03, b90_30, b90_31, fa10_0);
  RDT(b91_30, bb3, 20480);
  RDT(b91_31, bb3, 22528);
  FMA(f0_t11[0], f0_r1[2], rsf[0], f0_t11[0]);
  FMA(f0_t11[1], f0_r1[3], rsf[0], f0_t11[1]);
  MF(f1_13, b90_30, b90_31, fa10_1);
  DMA(r1, vw1, s_1w, 688384, 45056);
  SQA(f0_t10[0], f0_t10[0]);
  SQA(f0_t10[1], f0_t10[1]);
  MF(f1_00, b91_00, b91_01, fa11_0);
  PRIO(0);
  WVM(4);
  BAR(); STB(25);
  PRIO(3);
  RDT(b92_00, bb0, 24576);
  RDT(b92_01, bb0, 26624);
  SQA(f0_t11[0], f0_t11[0]);
  SQA(f0_t11[1], f0_t11[1]);
  MF(f1_10, b91_00, b91_01, fa11_1);
  unsigned f0_p10; CVT(f0_p10, f0_t10[0], f0_t10[1]);
  unsigned f0_p11; CVT(f0_p11, f0_t11[0], f0_t11[1]);
  WLG(4);
  MF(f1_01, b91_10, b91_11, fa11_0);
  RDT(b92_10, bb1, 24576);
  RDT(b92_11, bb1, 26624);
  RLU(f0_p10);
  RLU(f0_p11);
  MF(f1_11, b91_10, b91_11, fa11_1);
  const u32x4 f0_o0 = {f0_p00, f0_p01, f0_p10, f0_p11};
  GST(f0_o0, v16, shw, sL + L_HB + 0);
  f32x2 f0_t20, f0_t21;
  FMA(f0_t20[0], f0_c2[0], nmf[0], f0_b2[0]);
  MF(f1_02, b91_20, b91_21, fa11_0);
  RDT(b92_20, bb2, 24576);
  RDT(b92_21, bb2, 26624);
  FMA(f0_t20[1], f0_c2[1], nmf[0], f0_b2[1]);
  FMA(f0_t21[0], f0_c2[2], nmf[0], f0_b2[2]);
  MF(f1_12, b91_20, b91_21, fa11_1);
  FMA(f0_t21[1], f0_c2[3], nmf[0], f0_b2[3]);
  FMA(f0_t20[0], f0_02[0], rsf[0], f0_t20[0]);
  WLG(4);
  MF(f1_03, b91_30, b91_31, fa11_0);
  RDT(b92_30, bb3, 24576);
  RDT(b92_31, bb3, 26624);
  FMA(f0_t20[1], f0_02[1], rsf[0], f0_t20[1]);
  FMA(f0_t21[0], f0_02[2], rsf[0], f0_t21[0]);
  MF(f1_13, b91_30, b91_31, fa11_1);
  FMA(f0_t21[1], f0_02[3], rsf[0], f0_t21[1]);
  RDF(f0_c3, tb, 960);
  RDF(f0_b3, tb, 3008);
  MF(f1_00, b92_00, b92_01, fa12_0);
  RDT(b93_00, bb0, 28672);
  RDT(b93_01, bb0, 30720);
  DMA(r1, vw1, s_1w, 917760, 49152);
  MF(f1_10, b92_00, b92_01, fa12_1);
  FMA(f0_t20[0], f0_r2[0], rsf[0], f0_t20[0]);
  FMA(f0_t20[1], f0_r2[1], rsf[0], f0_t20[1]);
  WLG(6);
  MF(f1_01, b92_10, b92_11, fa12_0);
  RDT(b93_10, bb1, 28672);
  RDT(b93_11, bb1, 30720);
  FMA(f0_t21[0], f0_r2[2], rsf[0], f0_t21[0]);
  FMA(f0_t21[1], f0_r2[3], rsf[0], f0_t21[1]);
  MF(f1_11, b92_10, b92_11, fa12_1);
  SQA(f0_t20[0], f0_t20[0]);
  SQA(f0_t20[1], f0_t20[1]);
  MF(f1_02, b92_20, b92_21, fa12_0);
  RDT(b93_20, bb2, 28672);
  RDT(b93_21, bb2, 30720);
  SQA(f0_t21[0], f0_t21[0]);
  SQA(f0_t21[1], f0_t21[1]);
  MF(f1_12, b92_20, b92_21, fa12_1);
  DMA(r1, vw1, s_1w, 1147136, 0);
  unsigned f0_p20; CVT(f0_p20, f0_t20[0], f0_t20[1]);
  unsigned f0_p21; CVT(f0_p21, f0_t21[0], f0_t21[1]);
  WLG(4);
  MF(f1_03, b92_30, b92_31, fa12_0);
  RDT(b93_30, bb3, 28672);
  RDT(b93_31, bb3, 30720);
  RLU(f0_p20);
  RLU(f0_p21);
  MF(f1_13, b92_30, b92_31, fa12_1);
  f32x2 f0_t30, f0_t31;
  FMA(f0_t30[0], f0_c3[0], nmf[0], f0_b3[0]);
  FMA(f0_t30[1], f0_c3[1], nmf[0], f0_b3[1]);
  MF(f1_00, b93_00, b93_01, fa13_0);
  PRIO(2);
  RDT(b94_00, bb0, 32768);
  RDT(b94_01, bb0, 34816);
  FMA(f0_t31[0], f0_c3[2], nmf[0], f0_b3[2]);
  FMA(f0_t31[1], f0_c3[3], nmf[0], f0_b3[3]);
  MF(f1_10, b93_00, b93_01, fa13_1);
  RDT(b95_00, bb0, 36864);
  RDT(b95_01, bb0, 38912);
  FMA(f0_t30[0], f0_03[0], rsf[0], f0_t30[0]);
  FMA(f0_t30[1], f0_03[1], rsf[0], f0_t30[1]);
  WLG(6);
  MF(f1_01, b93_10, b93_11, fa13_0);
  RDT(b94_10, bb1, 32768);
  RDT(b94_11, bb1, 34816);
  DMA(r1, vw1, s_1w, 1376512, 4096);
  FMA(f0_t31[0], f0_03[2], rsf[0], f0_t31[0]);
  FMA(f0_t31[1], f0_03[3], rsf[0], f0_t31[1]);
  MF(f1_11, b93_10, b93_11, fa13_1);
  RDT(b95_10, bb1, 36864);
  RDT(b95_11, bb1, 38912);
  MF(f1_02, b93_20, b93_21, fa13_0);
  RDT(b94_20, bb2, 32768);
  RDT(b94_21, bb2, 34816);
  FMA(f0_t30[0], f0_r3[0], rsf[0], f0_t30[0]);
  FMA(f0_t30[1], f0_r3[1], rsf[0], f0_t30[1]);
  VWN(vw1n, 7168);
  MF(f1_12, b93_20, b93_21, fa13_1);
  RDT(b95_20, bb2, 36864);
  RDT(b95_21, bb2, 38912);
  FMA(f0_t31[0], f0_r3[2], rsf[0], f0_t31[0]);
  FMA(f0_t31[1], f0_r3[3], rsf[0], f0_t31[1]);
  WLG(8);
  MF(f1_03, b93_30, b93_31, fa13_0);
  RDT(b94_30, bb3, 32768);
  RDT(b94_31, bb3, 34816);
  DMA(r1, vw1, s_1w, 1605888, 8192);
  SQA(f0_t30[0], f0_t30[0]);
  SQA(f0_t30[1], f0_t30[1]);
  MF(f1_13, b93_30, b93_31, fa13_1);
  RDT(b95_30, bb3, 36864);
  RDT(b95_31, bb3, 38912);
  SQA(f0_t31[0], f0_t31[0]);
  SQA(f0_t31[1], f0_t31[1]);
  MF(f1_10, b94_00, b94_01, fa14_1);
  PRIO(1);
  unsigned f0_p30; CVT(f0_p30, f0_t30[0], f0_t30[1]);
  unsigned f0_p31; CVT(f0_p31, f0_t31[0], f0_t31[1]);
  MF(f1_10, b95_00, b95_01, fa15_1);
  RLU(f0_p30);
  RLU(f0_p31);
  WLG(4);
  MF(f1_11, b94_10, b94_11, fa14_1);
  const u32x4 f0_o1 = {f0_p20, f0_p21, f0_p30, f0_p31};
  GST(f0_o1, v16, shw, sL + L_HB + 1024);
  MF(f1_11, b95_10, b95_11, fa15_1);
  MF(f1_12, b94_20, b94_21, fa14_1);
  NOP(1);
  WR(f1_10, xw, 0);
  MF(f1_12, b95_20, b95_21, fa15_1);
  PRIO(0);
  WLG(1);
  MF(f1_13, b94_30, b94_31, fa14_1);
  NOP(1);
  WR(f1_11, xw, 1024);
  MF(f1_13, b95_30, b95_31, fa15_1);
  DMA(r1, vw1, s_1w, 1835264, 12288);
  MF(f1_00, b94_00, b94_01, fa14_0);
  NOP(0);
  WR(f1_12, xw, 2048);
  MF(f1_01, b94_10, b94_11, fa14_0);
  MF(f1_02, b94_20, b94_21, fa14_0);
  NOP(2);
  WR(f1_13, xw, 3072);
  MF(f1_03, b94_30, b94_31, fa14_0);
  WVM(7);
  WLG(0);
  BAR(); STB(26);
  PRIO(3);
  RDT(b96_00, bb0, 40960);
  RDT(b96_01, bb0, 43008);
  MF(f1_00, b95_00, b95_01, fa15_0);
  RDT(b96_10, bb1, 40960);
  RDT(b96_11, bb1, 43008);
  DMA(r1, vw1, s_1w, 2064640, 16384);
  MF(f1_01, b95_10, b95_11, fa15_0);
  RDT(b96_20, bb2, 40960);
  RDT(b96_21, bb2, 43008);
  MF(f1_02, b95_20, b95_21, fa15_0);
  RDT(b96_30, bb3, 40960);
  RDT(b96_31, bb3, 43008);
  MF(f1_03, b95_30, b95_31, fa15_0);
  STP(15);
  WLG(4);
  MF0(f2_00, b96_00, b96_01, fa0_0);
  RDT(b97_00, bb0, 53248);
  RDT(b97_01, bb0, 55296);
  RDF(f1_c0, tb, 1024);
  RDF(f1_b0, tb, 3072);
  MF0(f2_10, b96_00, b96_01, fa0_1);
  DMA(r1, vw1, s_1w, 2294016, 20480);
  MF0(f2_01, b96_10, b96_11, fa0_0);
  RDT(b97_10, bb1, 53248);
  RDT(b97_11, bb1, 55296);
  MF0(f2_11, b96_10, b96_11, fa0_1);
  f32x2 f1_t00, f1_t01;
  WLG(2);
  FMA(f1_t00[0], f1_c0[0], nmf[0], f1_b0[0]);
  FMA(f1_t00[1], f1_c0[1], nmf[0], f1_b0[1]);
  MF0(f2_02, b96_20, b96_21, fa0_0);
  RDT(b97_20, bb2, 53248);
  RDT(b97_21, bb2, 55296);
  FMA(f1_t01[0], f1_c0[2], nmf[0], f1_b0[2]);
  FMA(f1_t01[1], f1_c0[3], nmf[0], f1_b0[3]);
  MF0(f2_12, b96_20, b96_21, fa0_1);
  DMA(r1, vw1, s_1w, 2523392, 24576);
  RDF(f1_r0, xr, 0);
  RDF(f1_r1, xr, 1024);
  FMA(f1_t00[0], f1_00[0], rsf[0], f1_t00[0]);
  MF0(f2_03, b96_30, b96_31, fa0_0);
  RDT(b97_30, bb3, 53248);
  RDT(b97_31, bb3, 55296);
  FMA(f1_t00[1], f1_00[1], rsf[0], f1_t00[1]);
  FMA(f1_t01[0], f1_00[2], rsf[0], f1_t01[0]);
  MF0(f2_13, b96_30, b96_31, fa0_1);
  FMA(f1_t01[1], f1_00[3], rsf[0], f1_t01[1]);
  RDF(f1_c1, tb, 1088);
  RDF(f1_b1, tb, 3136);
  MF(f2_00, b97_00, b97_01, fa1_0);
  RDT(b98_00, bb0, 57344);
  RDT(b98_01, bb0, 59392);
  MF(f2_10, b97_00, b97_01, fa1_1);
  WLG(7);
  FMA(f1_t00[0], f1_r0[0], rsf[0], f1_t00[0]);
  FMA(f1_t00[1], f1_r0[1], rsf[0], f1_t00[1]);
  MF(f2_01, b97_10, b97_11, fa1_0);
  RDT(b98_10, bb1, 57344);
  RDT(b98_11, bb1, 59392);
  DMA(r1, vw1, s_1w, 2752768, 28672);
  FMA(f1_t01[0], f1_r0[2], rsf[0], f1_t01[0]);
  FMA(f1_t01[1], f1_r0[3], rsf[0], f1_t01[1]);
  MF(f2_11, b97_10, b97_11, fa1_1);
  PRIO(2);
  RDF(f1_r2, xr, 2048);
  RDF(f1_r3, xr, 3072);
  SQA(f1_t00[0], f1_t00[0]);
  MF(f2_02, b97_20, b97_21, fa1_0);
  RDT(b98_20, bb2, 57344);
  RDT(b98_21, bb2, 59392);
  SQA(f1_t00[1], f1_t00[1]);
  SQA(f1_t01[0], f1_t01[0]);
  MF(f2_12, b97_20, b97_21, fa1_1);
  SQA(f1_t01[1], f1_t01[1]);
  unsigned f1_p00; CVT(f1_p00, f1_t00[0], f1_t00[1]);
  WLG(6);
  MF(f2_03, b97_30, b97_31, fa1_0);
  RDT(b98_30, bb3, 57344);
  RDT(b98_31, bb3, 59392);
  unsigned f1_p01; CVT(f1_p01, f1_t01[0], f1_t01[1]);
  RLU(f1_p00);
  MF(f2_13, b97_30, b97_31, fa1_1);
  DMA(r1, vw1, s_1w, 2982144, 32768);
  RLU(f1_p01);
  f32x2 f1_t10, f1_t11;
  FMA(f1_t10[0], f1_c1[0], nmf[0], f1_b1[0]);
  MF(f2_00, b98_00, b98_01, fa2_0);
  RDT(b99_00, bb0, 45056);
  RDT(b99_01, bb0, 47104);
  FMA(f1_t10[1], f1_c1[1], nmf[0], f1_b1[1]);
  FMA(f1_t11[0], f1_c1[2], nmf[0], f1_b1[2]);
  MF(f2_10, b98_00, b98_01, fa2_1);
  FMA(f1_t11[1], f1_c1[3], nmf[0], f1_b1[3]);
  FMA(f1_t10[0], f1_01[0], rsf[0], f1_t10[0]);
  WLG(4);
  MF(f2_01, b98_10, b98_11, fa2_0);
  RDT(b99_10, bb1, 45056);
  RDT(b99_11, bb1, 47104);
  FMA(f1_t10[1], f1_01[1], rsf[0], f1_t10[1]);
  FMA(f1_t11[0], f1_01[2], rsf[0], f1_t11[0]);
  MF(f2_11, b98_10, b98_11, fa2_1);
  PRIO(1);
  DMA(r1, vw1, s_1w, 3211520, 36864);
  FMA(f1_t11[1], f1_01[3], rsf[0], f1_t11[1]);
  RDF(f1_c2, tb, 1152);
  RDF(f1_b2, tb, 3200);
  MF(f2_02, b98_20, b98_21, fa2_0);
  RDT(b99_20, bb2, 45056);
  RDT(b99_21, bb2, 47104);
  MF(f2_12, b98_20, b98_21, fa2_1);
  FMA(f1_t10[0], f1_r1[0], rsf[0], f1_t10[0]);
  FMA(f1_t10[1], f1_r1[1], rsf[0], f1_t10[1]);
  WLG(6);
  MF(f2_03, b98_30, b98_31, fa2_0);
  RDT(b99_30, bb3, 45056);
  RDT(b99_31, bb3, 47104);
  FMA(f1_t11[0], f1_r1[2], rsf[0], f1_t11[0]);
  FMA(f1_t11[1], f1_r1[3], rsf[0], f1_t11[1]);
  MF(f2_13, b98_30, b98_31, fa2_1);
  SQA(f1_t10[0], f1_t10[0]);
  SQA(f1_t10[1], f1_t10[1]);
  MF(f2_00, b99_00, b99_01, fa3_0);
  PRIO(0);
  WVM(8);
  BAR(); STB(27);
  PRIO(3);
  RDT(b100_00, bb0, 49152);
  RDT(b100_01, bb0, 51200);
  DMA(r1, vw1, s_1w, 3440896, 40960);
  SQA(f1_t11[0], f1_t11[0]);
  SQA(f1_t11[1], f1_t11[1]);
  MF(f2_10, b99_00, b99_01, fa3_1);
  unsigned f1_p10; CVT(f1_p10, f1_t10[0], f1_t10[1]);
  unsigned f1_p11; CVT(f1_p11, f1_t11[0], f1_t11[1]);
  WLG(4);
  MF(f2_01, b99_10, b99_11, fa3_0);
  RDT(b100_10, bb1, 49152);
  RDT(b100_11, bb1, 51200);
  RLU(f1_p10);
  RLU(f1_p11);
  MF(f2_11, b99_10, b99_11, fa3_1);
  const u32x4 f1_o0 = {f1_p00, f1_p01, f1_p10, f1_p11};
  GST(f1_o0, v16, shw, sL + L_HB + 2048);
  f32x2 f1_t20, f1_t21;
  FMA(f1_t20[0], f1_c2[0], nmf[0], f1_b2[0]);
  MF(f2_02, b99_20, b99_21, fa3_0);
  RDT(b100_20, bb2, 49152);
  RDT(b100_21, bb2, 51200);
  FMA(f1_t20[1], f1_c2[1], nmf[0], f1_b2[1]);
  FMA(f1_t21[0], f1_c2[2], nmf[0], f1_b2[2]);
  MF(f2_12, b99_20, b99_21, fa3_1);
  FMA(f1_t21[1], f1_c2[3], nmf[0], f1_b2[3]);
  FMA(f1_t20[0], f1_02[0], rsf[0], f1_t20[0]);
  WLG(4);
  MF(f2_03, b99_30, b99_31, fa3_0);
  RDT(b100_30, bb3, 49152);
  RDT(b100_31, bb3, 51200);
  FMA(f1_t20[1], f1_02[1], rsf[0], f1_t20[1]);
  FMA(f1_t21[0], f1_02[2], rsf[0], f1_t21[0]);
  MF(f2_13, b99_30, b99_31, fa3_1);
  FMA(f1_t21[1], f1_02[3], rsf[0], f1_t21[1]);
  RDF(f1_c3, tb, 1216);
  RDF(f1_b3, tb, 3264);
  MF(f2_00, b100_00, b100_01, fa4_0);
  RDT(b101_00, bb0, 0);
  RDT(b101_01, bb0, 2048);
  DMA(r1, vw1n, s_1n, 0, 53248);
  MF(f2_10, b100_00, b100_01, fa4_1);
  FMA(f1_t20[0], f1_r2[0], rsf[0], f1_t20[0]);
  FMA(f1_t20[1], f1_r2[1], rsf[0], f1_t20[1]);
  WLG(6);
  MF(f2_01, b100_10, b100_11, fa4_0);
  RDT(b101_10, bb1, 0);
  RDT(b101_11, bb1, 2048);
  FMA(f1_t21[0], f1_r2[2], rsf[0], f1_t21[0]);
  FMA(f1_t21[1], f1_r2[3], rsf[0], f1_t21[1]);
  MF(f2_11, b100_10, b100_11, fa4_1);
  SQA(f1_t20[0], f1_t20[0]);
  SQA(f1_t20[1], f1_t20[1]);
  MF(f2_02, b100_20, b100_21, fa4_0);
  RDT(b101_20, bb2, 0);
  RDT(b101_21, bb2, 2048);
  SQA(f1_t21[0], f1_t21[0]);
  SQA(f1_t21[1], f1_t21[1]);
  MF(f2_12, b100_20, b100_21, fa4_1);
  DMA(r1, vw1n, s_1n, 458752, 57344);
  unsigned f1_p20; CVT(f1_p20, f1_t20[0], f1_t20[1]);
  unsigned f1_p21; CVT(f1_p21, f1_t21[0], f1_t21[1]);
  WLG(4);
  MF(f2_03, b100_30, b100_31, fa4_0);
  RDT(b101_30, bb3, 0);
  RDT(b101_31, bb3, 2048);
  RLU(f1_p20);
  RLU(f1_p21);
  MF(f2_13, b100_30, b100_31, fa4_1);
  f32x2 f1_t30, f1_t31;
  FMA(f1_t30[0], f1_c3[0], nmf[0], f1_b3[0]);
  FMA(f1_t30[1], f1_c3[1], nmf[0], f1_b3[1]);
  MF(f2_00, b101_00, b101_01, fa5_0);
  PRIO(2);
  RDT(b102_00, bb0, 4096);
  RDT(b102_01, bb0, 6144);
  FMA(f1_t31[0], f1_c3[2], nmf[0], f1_b3[2]);
  FMA(f1_t31[1], f1_c3[3], nmf[0], f1_b3[3]);
  MF(f2_10, b101_00, b101_01, fa5_1);
  FMA(f1_t30[0], f1_03[0], rsf[0], f1_t30[0]);
  FMA(f1_t30[1], f1_03[1], rsf[0], f1_t30[1]);
  WLG(4);
  MF(f2_01, b101_10, b101_11, fa5_0);
  RDT(b102_10, bb1, 4096);
  RDT(b102_11, bb1, 6144);
  FMA(f1_t31[0], f1_03[2], rsf[0], f1_t31[0]);
  FMA(f1_t31[1], f1_03[3], rsf[0], f1_t31[1]);
  MF(f2_11, b101_10, b101_11, fa5_1);
  MF(f2_02, b101_20, b101_21, fa5_0);
  RDT(b102_20, bb2, 4096);
  RDT(b102_21, bb2, 6144);
  FMA(f1_t30[0], f1_r3[0], rsf[0], f1_t30[0]);
  FMA(f1_t30[1], f1_r3[1], rsf[0], f1_t30[1]);
  VW(vw2, 2048);
  MF(f2_12, b101_20, b101_21, fa5_1);
  FMA(f1_t31[0], f1_r3[2], rsf[0], f1_t31[0]);
  FMA(f1_t31[1], f1_r3[3], rsf[0], f1_t31[1]);
  WLG(4);
  MF(f2_03, b101_30, b101_31, fa5_0);
  RDT(b102_30, bb3, 4096);
  RDT(b102_31, bb3, 6144);
  SQA(f1_t30[0], f1_t30[0]);
  SQA(f1_t30[1], f1_t30[1]);
  MF(f2_13, b101_30, b101_31, fa5_1);
  SQA(f1_t31[0], f1_t31[0]);
  SQA(f1_t31[1], f1_t31[1]);
  MF(f2_00, b102_00, b102_01, fa6_0);
  PRIO(1);
  RDT(b103_00, bb0, 8192);
  RDT(b103_01, bb0, 10240);
  unsigned f1_p30; CVT(f1_p30, f1_t30[0], f1_t30[1]);
  unsigned f1_p31; CVT(f1_p31, f1_t31[0], f1_t31[1]);
  MF(f2_10, b102_00, b102_01, fa6_1);
  RLU(f1_p30);
  RLU(f1_p31);
  WLG(4);
  MF(f2_01, b102_10, b102_11, fa6_0);
  RDT(b103_10, bb1, 8192);
  RDT(b103_11, bb1, 10240);
  const u32x4 f1_o1 = {f1_p20, f1_p21, f1_p30, f1_p31};
  GST(f1_o1, v16, shw, sL + L_HB + 3072);
  MF(f2_11, b102_10, b102_11, fa6_1);
  MF(f2_02, b102_20, b102_21, fa6_0);
  RDT(b103_20, bb2, 8192);
  RDT(b103_21, bb2, 10240);
  MF(f2_12, b102_20, b102_21, fa6_1);
  PRIO(0);
  WLG(4);
  MF(f2_03, b102_30, b102_31, fa6_0);
  RDT(b103_30, bb3, 8192);
  RDT(b103_31, bb3, 10240);
  MF(f2_13, b102_30, b102_31, fa6_1);
  MF(f2_00, b103_00, b103_01, fa7_0);
  WVM(8);
  BAR(); STB(28);
  PRIO(3);
  RDT(b104_00, bb0, 12288);
  RDT(b104_01, bb0, 14336);
  DMA(r1, vw1n, s_1n, 917504, 45056);
  GSIG(1, sfo, sL + L_FH);
  MF(f2_10, b103_00, b103_01, fa7_1);
  WLG(4);
  MF(f2_01, b103_10, b103_11, fa7_0);
  RDT(b104_10, bb1, 12288);
  RDT(b104_11, bb1, 14336);
  MF(f2_11, b103_10, b103_11, fa7_1);
  MF(f2_02, b103_20, b103_21, fa7_0);
  RDT(b104_20, bb2, 12288);
  RDT(b104_21, bb2, 14336);
  DMA(r1, vw1n, s_1n, 1376256, 49152);
  MF(f2_12, b103_20, b103_21, fa7_1);
  WLG(4);
  MF(f2_03, b103_30, b103_31, fa7_0);
  RDT(b104_30, bb3, 12288);
  RDT(b104_31, bb3, 14336);
  MF(f2_13, b103_30, b103_31, fa7_1);
  MF(f2_00, b104_00, b104_01, fa8_0);
  RDT(b105_00, bb0, 16384);
  RDT(b105_01, bb0, 18432);
  MF(f2_10, b104_00, b104_01, fa8_1);
  DMA(r1, vw1n, s_1n, 1835008, 0);
  WLG(4);
  MF(f2_01, b104_10, b104_11, fa8_0);
  RDT(b105_10, bb1, 16384);
  RDT(b105_11, bb1, 18432);
  MF(f2_11, b104_10, b104_11, fa8_1);
  MF(f2_02, b104_20, b104_21, fa8_0);
  RDT(b105_20, bb2, 16384);
  RDT(b105_21, bb2, 18432);
  MF(f2_12, b104_20, b104_21, fa8_1);
  DMA(r1, vw1n, s_1n, 2293760, 4096);
  WLG(4);
  MF(f2_03, b104_30, b104_31, fa8_0);
  RDT(b105_30, bb3, 16384);
  RDT(b105_31, bb3, 18432);
  MF(f2_13, b104_30, b104_31, fa8_1);
  MF(f2_00, b105_00, b105_01, fa9_0);
  PRIO(2);
  RDT(b106_00, bb0, 20480);
  RDT(b106_01, bb0, 22528);
  MF(f2_10, b105_00, b105_01, fa9_1);
  WLG(4);
  MF(f2_01, b105_10, b105_11, fa9_0);
  RDT(b106_10, bb1, 20480);
  RDT(b106_11, bb1, 22528);
  GMK(v16, sp2, sL + L_P);
  MF(f2_11, b105_10, b105_11, fa9_1);
  MF(f2_02, b105_20, b105_21, fa9_0);
  RDT(b106_20, bb2, 20480);
  RDT(b106_21, bb2, 22528);
  MF(f2_12, b105_20, b105_21, fa9_1);
  WLG(4);
  MF(f2_03, b105_30, b105_31, fa9_0);
  RDT(b106_30, bb3, 20480);
  RDT(b106_31, bb3, 22528);
  MF(f2_13, b105_30, b105_31, fa9_1);
  MF(f2_00, b106_00, b106_01, fa10_0);
  PRIO(1);
  RDT(b107_00, bb0, 24576);
  RDT(b107_01, bb0, 26624);
  MF(f2_10, b106_00, b106_01, fa10_1);
  WLG(4);
  MF(f2_01, b106_10, b106_11, fa10_0);
  RDT(b107_10, bb1, 24576);
  RDT(b107_11, bb1, 26624);
  MF(f2_11, b106_10, b106_11, fa10_1);
  MF(f2_02, b106_20, b106_21, fa10_0);
  RDT(b107_20, bb2, 24576);
  RDT(b107_21, bb2, 26624);
  int bn0, bn1; { LANE(l_); const int lg = l_ >> 4, b_ = (int)lds0 + (wid0 >> 2) * HALF + (4 * lg + ((l_ & 15) >> 2)) * 64 + (l_ & 3) * 8; bn0 = b_ + ((lg & 1) << 5); bn1 = b_ + (((lg & 1) ^ 1) << 5); PIN(bn0); PIN(bn1); }
  MF(f2_12, b106_20, b106_21, fa10_1);
  PRIO(0);
  WLG(4);
  MF(f2_03, b106_30, b106_31, fa10_0);
  RDT(b107_30, bb3, 24576);
  RDT(b107_31, bb3, 26624);
  MF(f2_13, b106_30, b106_31, fa10_1);
  MF(f2_00, b107_00, b107_01, fa11_0);
  WVM(10);
  BAR(); STB(29);
  PRIO(3);
  RDT(b108_00, bb0, 28672);
  RDT(b108_01, bb0, 30720);
  DMA(r1, vw1n, s_1n, 2752512, 8192);
  MF(f2_10, b107_00, b107_01, fa11_1);
  WLG(4);
  MF(f2_01, b107_10, b107_11, fa11_0);
  RDT(b108_10, bb1, 28672);
  RDT(b108_11, bb1, 30720);
  unsigned vfl; { LANE(l_); vfl = (l_ & 3) * 512; PIN(vfl); }
  GLD1(fl0, vfl, sfi, sL + L_FH);
  MF(f2_11, b107_10, b107_11, fa11_1);
  MF(f2_02, b107_20, b107_21, fa11_0);
  RDT(b108_20, bb2, 28672);
  RDT(b108_21, bb2, 30720);
  DMA(r1, vw1n, s_1n, 3211264, 12288);
  MF(f2_12, b107_20, b107_21, fa11_1);
  WLG(4);
  MF(f2_03, b107_30, b107_31, fa11_0);
  RDT(b108_30, bb3, 28672);
  RDT(b108_31, bb3, 30720);
  MF(f2_13, b107_30, b107_31, fa11_1);
  MF(f2_00, b108_00, b108_01, fa12_0);
  RDT(b109_00, bb0, 32768);
  RDT(b109_01, bb0, 34816);
  MF(f2_10, b108_00, b108_01, fa12_1);
  DMA(r2, vw2, s_20, 0, 16384);
  WLG(4);
  MF(f2_01, b108_10, b108_11, fa12_0);
  RDT(b109_10, bb1, 32768);
  RDT(b109_11, bb1, 34816);
  MF(f2_11, b108_10, b108_11, fa12_1);
  MF(f2_02, b108_20, b108_21, fa12_0);
  RDT(b109_20, bb2, 32768);
  RDT(b109_21, bb2, 34816);
  MF(f2_12, b108_20, b108_21, fa12_1);
  DMA(r2, vw2, s_21, 0, 20480);
  WLG(4);
  MF(f2_03, b108_30, b108_31, fa12_0);
  RDT(b109_30, bb3, 32768);
  RDT(b109_31, bb3, 34816);
  MF(f2_13, b108_30, b108_31, fa12_1);
  MF(f2_00, b109_00, b109_01, fa13_0);
  PRIO(2);
  RDT(b110_00, bb0, 36864);
  RDT(b110_01, bb0, 38912);
  MF(f2_10, b109_00, b109_01, fa13_1);
  RDT(b111_00, bb0, 40960);
  RDT(b111_01, bb0, 43008);
  WLG(6);
  MF(f2_01, b109_10, b109_11, fa13_0);
  RDT(b110_10, bb1, 36864);
  RDT(b110_11, bb1, 38912);
  MF(f2_11, b109_10, b109_11, fa13_1);
  RDT(b111_10, bb1, 40960);
  RDT(b111_11, bb1, 43008);
  MF(f2_02, b109_20, b109_21, fa13_0);
  RDT(b110_20, bb2, 36864);
  RDT(b110_21, bb2, 38912);
  MF(f2_12, b109_20, b109_21, fa13_1);
  RDT(b111_20, bb2, 40960);
  RDT(b111_21, bb2, 43008);
  WLG(8);
  MF(f2_03, b109_30, b109_31, fa13_0);
  RDT(b110_30, bb3, 36864);
  RDT(b110_31, bb3, 38912);
  GMK(v16, sp2, sL + L_P + 1024);
  MF(f2_13, b109_30, b109_31, fa13_1);
  RDT(b111_30, bb3, 40960);
  RDT(b111_31, bb3, 43008);
  MF(f2_10, b110_00, b110_01, fa14_1);
  PRIO(1);
  MF(f2_10, b111_00, b111_01, fa15_1);
  WLG(4);
  MF(f2_11, b110_10, b110_11, fa14_1);
  MF(f2_11, b111_10, b111_11, fa15_1);
  MF(f2_12, b110_20, b110_21, fa14_1);
  NOP(3);
  WR(f2_10, xw, 0);
  MF(f2_12, b111_20, b111_21, fa15_1);
  PRIO(0);
  WLG(1);
  MF(f2_13, b110_30, b110_31, fa14_1);
  NOP(1);
  WR(f2_11, xw, 1024);
  MF(f2_13, b111_30, b111_31, fa15_1);
  MF(f2_00, b110_00, b110_01, fa14_0);
  WVM(12);
  GSIG(2, sfo, sL + L_FH);
  NOP(0);
  WR(f2_12, xw, 2048);
  MF(f2_01, b110_10, b110_11, fa14_0);
  MF(f2_02, b110_20, b110_21, fa14_0);
  NOP(2);
  WR(f2_13, xw, 3072);
  MF(f2_03, b110_30, b110_31, fa14_0);
  WVM(10);
  WLG(0);
  BAR(); STB(30);
  PRIO(3);
  RDT(b112_00, bn0, 53248);
  RDT(b112_01, bn0, 54272);
  DMA(r2, vw2, s_20, 65536, 24576);
  MF(f2_00, b111_00, b111_01, fa15_0);
  RDT(b112_10, bn1, 53248);
  RDT(b112_11, bn1, 54272);
  MF(f2_01, b111_10, b111_11, fa15_0);
  RDT(b112_20, bn0, 55296);
  RDT(b112_21, bn0, 56320);
  MF(f2_02, b111_20, b111_21, fa15_0);
  RDT(b112_30, bn1, 55296);
  RDT(b112_31, bn1, 56320);
  MF(f2_03, b111_30, b111_31, fa15_0);
  DMA(r2, vw2, s_21, 65536, 28672);
  STP(16);
  WLG(4);
  MF0(f3_00, b112_00, b112_01, fa0_0);
  RDT(b113_00, bn0, 57344);
  RDT(b113_01, bn0, 58368);
  RDF(f2_c0, tb, 1280);
  RDF(f2_b0, tb, 3328);
  MF0(f3_10, b112_00, b112_01, fa0_1);
  MF0(f3_01, b112_10, b112_11, fa0_0);
  RDT(b113_10, bn1, 57344);
  RDT(b113_11, bn1, 58368);
  MF0(f3_11, b112_10, b112_11, fa0_1);
  f32x2 f2_t00, f2_t01;
  WLG(2);
  FMA(f2_t00[0], f2_c0[0], nmf[0], f2_b0[0]);
  FMA(f2_t00[1], f2_c0[1], nmf[0], f2_b0[1]);
  MF(f3_00, b112_20, b112_21, fa1_0);
  RDT(b113_20, bn0, 59392);
  RDT(b113_21, bn0, 60416);
  DMA(r2, vw2, s_20, 524288, 32768);
  FMA(f2_t01[0], f2_c0[2], nmf[0], f2_b0[2]);
  FMA(f2_t01[1], f2_c0[3], nmf[0], f2_b0[3]);
  MF(f3_10, b112_20, b112_21, fa1_1);
  RDF(f2_r0, xr, 0);
  RDF(f2_r1, xr, 1024);
  FMA(f2_t00[0], f2_00[0], rsf[0], f2_t00[0]);
  MF(f3_01, b112_30, b112_31, fa1_0);
  RDT(b113_30, bn1, 59392);
  RDT(b113_31, bn1, 60416);
  FMA(f2_t00[1], f2_00[1], rsf[0], f2_t00[1]);
  FMA(f2_t01[0], f2_00[2], rsf[0], f2_t01[0]);
  MF(f3_11, b112_30, b112_31, fa1_1);
  FMA(f2_t01[1], f2_00[3], rsf[0], f2_t01[1]);
  RDF(f2_c1, tb, 1344);
  RDF(f2_b1, tb, 3392);
  MF(f3_00, b113_00, b113_01, fa2_0);
  RDT(b114_00, bn0, 45056);
  RDT(b114_01, bn0, 46080);
  DMA(r2, vw2, s_21, 524288, 36864);
  WVM(9);
  CHKF(fl0, 1u, vfl, sfi, sL + L_FH);
  MF(f3_10, b113_00, b113_01, fa2_1);
  WLG(7);
  FMA(f2_t00[0], f2_r0[0], rsf[0], f2_t00[0]);
  FMA(f2_t00[1], f2_r0[1], rsf[0], f2_t00[1]);
  MF(f3_01, b113_10, b113_11, fa2_0);
  RDT(b114_10, bn1, 45056);
  RDT(b114_11, bn1, 46080);
  FMA(f2_t01[0], f2_r0[2], rsf[0], f2_t01[0]);
  FMA(f2_t01[1], f2_r0[3], rsf[0], f2_t01[1]);
  MF(f3_11, b113_10, b113_11, fa2_1);
  PRIO(2);
  RDF(f2_r2, xr, 2048);
  RDF(f2_r3, xr, 3072);
  SQA(f2_t00[0], f2_t00[0]);
  MF(f3_00, b113_20, b113_21, fa3_0);
  RDT(b114_20, bn0, 47104);
  RDT(b114_21, bn0, 48128);
  SQA(f2_t00[1], f2_t00[1]);
  SQA(f2_t01[0], f2_t01[0]);
  MF(f3_10, b113_20, b113_21, fa3_1);
  GLD(ha0_0, v16, sh0, sL + L_HB + 0);
  SQA(f2_t01[1], f2_t01[1]);
  unsigned f2_p00; CVT(f2_p00, f2_t00[0], f2_t00[1]);
  WLG(6);
  MF(f3_01, b113_30, b113_31, fa3_0);
  RDT(b114_30, bn1, 47104);
  RDT(b114_31, bn1, 48128);
  unsigned f2_p01; CVT(f2_p01, f2_t01[0], f2_t01[1]);
  RLU(f2_p00);
  MF(f3_11, b113_30, b113_31, fa3_1);
  RLU(f2_p01);
  f32x2 f2_t10, f2_t11;
  FMA(f2_t10[0], f2_c1[0], nmf[0], f2_b1[0]);
  MF(f3_00, b114_00, b114_01, fa4_0);
  RDT(b115_00, bn0, 49152);
  RDT(b115_01, bn0, 50176);
  FMA(f2_t10[1], f2_c1[1], nmf[0], f2_b1[1]);
  FMA(f2_t11[0], f2_c1[2], nmf[0], f2_b1[2]);
  MF(f3_10, b114_00, b114_01, fa4_1);
  GLD(ha0_1, v16, sh1, sL + L_HB + 0);
  FMA(f2_t11[1], f2_c1[3], nmf[0], f2_b1[3]);
  FMA(f2_t10[0], f2_01[0], rsf[0], f2_t10[0]);
  WLG(4);
  MF(f3_01, b114_10, b114_11, fa4_0);
  RDT(b115_10, bn1, 49152);
  RDT(b115_11, bn1, 50176);
  FMA(f2_t10[1], f2_01[1], rsf[0], f2_t10[1]);
  FMA(f2_t11[0], f2_01[2], rsf[0], f2_t11[0]);
  MF(f3_11, b114_10, b114_11, fa4_1);
  PRIO(1);
  FMA(f2_t11[1], f2_01[3], rsf[0], f2_t11[1]);
  RDF(f2_c2, tb, 1408);
  RDF(f2_b2, tb, 3456);
  MF(f3_00, b114_20, b114_21, fa5_0);
  RDT(b115_20, bn0, 51200);
  RDT(b115_21, bn0, 52224);
  MF(f3_10, b114_20, b114_21, fa5_1);
  FMA(f2_t10[0], f2_r1[0], rsf[0], f2_t10[0]);
  FMA(f2_t10[1], f2_r1[1], rsf[0], f2_t10[1]);
  WLG(6);
  MF(f3_01, b114_30, b114_31, fa5_0);
  RDT(b115_30, bn1, 51200);
  RDT(b115_31, bn1, 52224);
  GLD(ha1_0, v16, sh0, sL + L_HB + 1024);
  FMA(f2_t11[0], f2_r1[2], rsf[0], f2_t11[0]);
  FMA(f2_t11[1], f2_r1[3], rsf[0], f2_t11[1]);
  MF(f3_11, b114_30, b114_31, fa5_1);
  SQA(f2_t10[0], f2_t10[0]);
  SQA(f2_t10[1], f2_t10[1]);
  MF(f3_00, b115_00, b115_01, fa6_0);
  PRIO(0);
  WVM(11);
  BAR(); STB(31);
  PRIO(3);
  RDT(b116_00, bn0, 0);
  RDT(b116_01, bn0, 1024);
  SQA(f2_t11[0], f2_t11[0]);
  SQA(f2_t11[1], f2_t11[1]);
  MF(f3_10, b115_00, b115_01, fa6_1);
  unsigned f2_p10; CVT(f2_p10, f2_t10[0], f2_t10[1]);
  unsigned f2_p11; CVT(f2_p11, f2_t11[0], f2_t11[1]);
  WLG(4);
  MF(f3_01, b115_10, b115_11, fa6_0);
  RDT(b116_10, bn1, 0);
  RDT(b116_11, bn1, 1024);
  GLD(ha1_1, v16, sh1, sL + L_HB + 1024);
  RLU(f2_p10);
  RLU(f2_p11);
  MF(f3_11, b115_10, b115_11, fa6_1);
  const u32x4 f2_o0 = {f2_p00, f2_p01, f2_p10, f2_p11};
  GST(f2_o0, v16, shw, sL + L_HB + 4096);
  f32x2 f2_t20, f2_t21;
  FMA(f2_t20[0], f2_c2[0], nmf[0], f2_b2[0]);
  MF(f3_00, b115_20, b115_21, fa7_0);
  RDT(b116_20, bn0, 2048);
  RDT(b116_21, bn0, 3072);
  FMA(f2_t20[1], f2_c2[1], nmf[0], f2_b2[1]);
  FMA(f2_t21[0], f2_c2[2], nmf[0], f2_b2[2]);
  MF(f3_10, b115_20, b115_21, fa7_1);
  FMA(f2_t21[1], f2_c2[3], nmf[0], f2_b2[3]);
  FMA(f2_t20[0], f2_02[0], rsf[0], f2_t20[0]);
  WLG(4);
  MF(f3_01, b115_30, b115_31, fa7_0);
  RDT(b116_30, bn1, 2048);
  RDT(b116_31, bn1, 3072);
  FMA(f2_t20[1], f2_02[1], rsf[0], f2_t20[1]);
  FMA(f2_t21[0], f2_02[2], rsf[0], f2_t21[0]);
  MF(f3_11, b115_30, b115_31, fa7_1);
  FMA(f2_t21[1], f2_02[3], rsf[0], f2_t21[1]);
  RDF(f2_c3, tb, 1472);
  RDF(f2_b3, tb, 3520);
  MF(f3_00, b116_00, b116_01, fa8_0);
  RDT(b117_00, bn0, 4096);
  RDT(b117_01, bn0, 5120);
  DMA(r2, vw2, s_20, 589824, 40960);
  MF(f3_10, b116_00, b116_01, fa8_1);
  FMA(f2_t20[0], f2_r2[0], rsf[0], f2_t20[0]);
  FMA(f2_t20[1], f2_r2[1], rsf[0], f2_t20[1]);
  WLG(6);
  MF(f3_01, b116_10, b116_11, fa8_0);
  RDT(b117_10, bn1, 4096);
  RDT(b117_11, bn1, 5120);
  FMA(f2_t21[0], f2_r2[2], rsf[0], f2_t21[0]);
  FMA(f2_t21[1], f2_r2[3], rsf[0], f2_t21[1]);
  MF(f3_11, b116_10, b116_11, fa8_1);
  SQA(f2_t20[0], f2_t20[0]);
  SQA(f2_t20[1], f2_t20[1]);
  MF(f3_00, b116_20, b116_21, fa9_0);
  RDT(b117_20, bn0, 6144);
  RDT(b117_21, bn0, 7168);
  SQA(f2_t21[0], f2_t21[0]);
  SQA(f2_t21[1], f2_t21[1]);
  GLD1(fl1, vfl, sfi, sL + L_FH);
  MF(f3_10, b116_20, b116_21, fa9_1);
  DMA(r2, vw2, s_21, 589824, 53248);
  unsigned f2_p20; CVT(f2_p20, f2_t20[0], f2_t20[1]);
  unsigned f2_p21; CVT(f2_p21, f2_t21[0], f2_t21[1]);
  WLG(4);
  MF(f3_01, b116_30, b116_31, fa9_0);
  RDT(b117_30, bn1, 6144);
  RDT(b117_31, bn1, 7168);
  RLU(f2_p20);
  RLU(f2_p21);
  MF(f3_11, b116_30, b116_31, fa9_1);
  f32x2 f2_t30, f2_t31;
  FMA(f2_t30[0], f2_c3[0], nmf[0], f2_b3[0]);
  FMA(f2_t30[1], f2_c3[1], nmf[0], f2_b3[1]);
  MF(f3_00, b117_00, b117_01, fa10_0);
  PRIO(2);
  RDT(b118_00, bn0, 8192);
  RDT(b118_01, bn0, 9216);
  FMA(f2_t31[0], f2_c3[2], nmf[0], f2_b3[2]);
  FMA(f2_t31[1], f2_c3[3], nmf[0], f2_b3[3]);
  MF(f3_10, b117_00, b117_01, fa10_1);
  RDT(b118_20, bn0, 10240);
  RDT(b118_21, bn0, 11264);
  GLD(ha2_0, v16, sh0, sL + L_HB + 8192);
  FMA(f2_t30[0], f2_03[0], rsf[0], f2_t30[0]);
  FMA(f2_t30[1], f2_03[1], rsf[0], f2_t30[1]);
  WLG(6);
  MF(f3_01, b117_10, b117_11, fa10_0);
  RDT(b119_00, bn0, 12288);
  RDT(b119_01, bn0, 13312);
  FMA(f2_t31[0], f2_03[2], rsf[0], f2_t31[0]);
  FMA(f2_t31[1], f2_03[3], rsf[0], f2_t31[1]);
  MF(f3_11, b117_10, b117_11, fa10_1);
  RDT(b119_20, bn0, 14336);
  RDT(b119_21, bn0, 15360);
  MF(f3_00, b117_20, b117_21, fa11_0);
  RDT(b118_10, bn1, 8192);
  RDT(b118_11, bn1, 9216);
  FMA(f2_t30[0], f2_r3[0], rsf[0], f2_t30[0]);
  FMA(f2_t30[1], f2_r3[1], rsf[0], f2_t30[1]);
  MF(f3_10, b117_20, b117_21, fa11_1);
  RDT(b118_30, bn1, 10240);
  RDT(b118_31, bn1, 11264);
  FMA(f2_t31[0], f2_r3[2], rsf[0], f2_t31[0]);
  FMA(f2_t31[1], f2_r3[3], rsf[0], f2_t31[1]);
  WLG(8);
  MF(f3_01, b117_30, b117_31, fa11_0);
  RDT(b119_10, bn1, 12288);
  RDT(b119_11, bn1, 13312);
  GLD(ha2_1, v16, sh1, sL + L_HB + 8192);
  SQA(f2_t30[0], f2_t30[0]);
  SQA(f2_t30[1], f2_t30[1]);
  MF(f3_11, b117_30, b117_31, fa11_1);
  RDT(b119_30, bn1, 14336);
  RDT(b119_31, bn1, 15360);
  SQA(f2_t31[0], f2_t31[0]);
  SQA(f2_t31[1], f2_t31[1]);
  MF(f3_10, b118_00, b118_01, fa12_1);
  PRIO(1);
  unsigned f2_p30; CVT(f2_p30, f2_t30[0], f2_t30[1]);
  unsigned f2_p31; CVT(f2_p31, f2_t31[0], f2_t31[1]);
  MF(f3_10, b118_20, b118_21, fa13_1);
  RLU(f2_p30);
  RLU(f2_p31);
  WLG(4);
  MF(f3_10, b119_00, b119_01, fa14_1);
  GLD(ha3_0, v16, sh0, sL + L_HB + 9216);
  const u32x4 f2_o1 = {f2_p20, f2_p21, f2_p30, f2_p31};
  GST(f2_o1, v16, shw, sL + L_HB + 5120);
  MF(f3_10, b119_20, b119_21, fa15_1);
  MF(f3_11, b118_10, b118_11, fa12_1);
  MF(f3_11, b118_30, b118_31, fa13_1);
  PRIO(0);
  WLG(0);
  MF(f3_11, b119_10, b119_11, fa14_1);
  NOP(2);
  WR(f3_10, xw, 0);
  MF(f3_11, b119_30, b119_31, fa15_1);
  GLD(ha3_1, v16, sh1, sL + L_HB + 9216);
  MF(f3_00, b118_00, b118_01, fa12_0);
  MF(f3_01, b118_10, b118_11, fa12_0);
  MF(f3_00, b118_20, b118_21, fa13_0);
  NOP(3);
  WR(f3_11, xw, 1024);
  MF(f3_01, b118_30, b118_31, fa13_0);
  WVM(15);
  WLG(0);
  BAR(); STB(32);
  PRIO(3);
  RDT(b120_00, bb0, 16384);
  RDT(b120_01, bb0, 18432);
  MF(f3_00, b119_00, b119_01, fa14_0);
  RDT(b120_10, bb1, 16384);
  RDT(b120_11, bb1, 18432);
  DMA(r2, vw2, s_20, 131072, 57344);
  MF(f3_01, b119_10, b119_11, fa14_0);
  RDT(b120_20, bb2, 16384);
  RDT(b120_21, bb2, 18432);
  MF(f3_00, b119_20, b119_21, fa15_0);
  RDT(b120_30, bb3, 16384);
  RDT(b120_31, bb3, 18432);
  STP(8);
  MF(f3_01, b119_30, b119_31, fa15_0);
  STP(9);
  WVM(12);
  WLG(4);
  MF0(g0_00, b120_00, b120_01, ha0_0);
  RDT(b121_00, bb0, 20480);
  RDT(b121_01, bb0, 22528);
  DMA(r2, vw2, s_21, 131072, 45056);
  RDF(f3_c0, tb, 1536);
  RDF(f3_b0, tb, 3584);
  MF0(g0_10, b120_00, b120_01, ha0_1);
  MF0(g0_01, b120_10, b120_11, ha0_0);
  RDT(b121_10, bb1, 20480);
  RDT(b121_11, bb1, 22528);
  MF0(g0_11, b120_10, b120_11, ha0_1);
  f32x2 f3_t00, f3_t01;
  WLG(2);
  FMA(f3_t00[0], f3_c0[0], nmf[0], f3_b0[0]);
  FMA(f3_t00[1], f3_c0[1], nmf[0], f3_b0[1]);
  MF0(g0_02, b120_20, b120_21, ha0_0);
  RDT(b121_20, bb2, 20480);
  RDT(b121_21, bb2, 22528);
  FMA(f3_t01[0], f3_c0[2], nmf[0], f3_b0[2]);
  FMA(f3_t01[1], f3_c0[3], nmf[0], f3_b0[3]);
  MF0(g0_12, b120_20, b120_21, ha0_1);
  DMA(r2, vw2, s_20, 196608, 49152);
  RDF(f3_r0, xr, 0);
  RDF(f3_r1, xr, 1024);
  FMA(f3_t00[0], f3_00[0], rsf[0], f3_t00[0]);
  MF0(g0_03, b120_30, b120_31, ha0_0);
  RDT(b121_30, bb3, 20480);
  RDT(b121_31, bb3, 22528);
  FMA(f3_t00[1], f3_00[1], rsf[0], f3_t00[1]);
  FMA(f3_t01[0], f3_00[2], rsf[0], f3_t01[0]);
  MF0(g0_13, b120_30, b120_31, ha0_1);
  FMA(f3_t01[1], f3_00[3], rsf[0], f3_t01[1]);
  RDF(f3_c1, tb, 1600);
  RDF(f3_b1, tb, 3648);
  MF0(g1_00, b121_00, b121_01, ha0_0);
  RDT(b122_00, bb0, 24576);
  RDT(b122_01, bb0, 26624);
  MF0(g1_10, b121_00, b121_01, ha0_1);
  DMA(r2, vw2, s_21, 196608, 0);
  WLG(7);
  FMA(f3_t00[0], f3_r0[0], rsf[0], f3_t00[0]);
  FMA(f3_t00[1], f3_r0[1], rsf[0], f3_t00[1]);
  MF0(g1_01, b121_10, b121_11, ha0_0);
  RDT(b122_10, bb1, 24576);
  RDT(b122_11, bb1, 26624);
  FMA(f3_t01[0], f3_r0[2], rsf[0], f3_t01[0]);
  FMA(f3_t01[1], f3_r0[3], rsf[0], f3_t01[1]);
  MF0(g1_11, b121_10, b121_11, ha0_1);
  PRIO(2);
  SQA(f3_t00[0], f3_t00[0]);
  SQA(f3_t00[1], f3_t00[1]);
  MF0(g1_02, b121_20, b121_21, ha0_0);
  RDT(b122_20, bb2, 24576);
  RDT(b122_21, bb2, 26624);
  SQA(f3_t01[0], f3_t01[0]);
  SQA(f3_t01[1], f3_t01[1]);
  WVM(10);
  CHKF(fl1, 2u, vfl, sfi, sL + L_FH);
  MF0(g1_12, b121_20, b121_21, ha0_1);
  GLD(ha4_0, v16, sh0, sL + L_HB + 2048);
  unsigned f3_p00; CVT(f3_p00, f3_t00[0], f3_t00[1]);
  unsigned f3_p01; CVT(f3_p01, f3_t01[0], f3_t01[1]);
  WLG(4);
  MF0(g1_03, b121_30, b121_31, ha0_0);
  RDT(b122_30, bb3, 24576);
  RDT(b122_31, bb3, 26624);
  RLU(f3_p00);
  RLU(f3_p01);
  MF0(g1_13, b121_30, b121_31, ha0_1);
  f32x2 f3_t10, f3_t11;
  FMA(f3_t10[0], f3_c1[0], nmf[0], f3_b1[0]);
  FMA(f3_t10[1], f3_c1[1], nmf[0], f3_b1[1]);
  MF(g0_00, b122_00, b122_01, ha1_0);
  RDT(b123_00, bb0, 28672);
  RDT(b123_01, bb0, 30720);
  FMA(f3_t11[0], f3_c1[2], nmf[0], f3_b1[2]);
  FMA(f3_t11[1], f3_c1[3], nmf[0], f3_b1[3]);
  MF(g0_10, b122_00, b122_01, ha1_1);
  FMA(f3_t10[0], f3_01[0], rsf[0], f3_t10[0]);
  FMA(f3_t10[1], f3_01[1], rsf[0], f3_t10[1]);
  WLG(4);
  MF(g0_01, b122_10, b122_11, ha1_0);
  RDT(b123_10, bb1, 28672);
  RDT(b123_11, bb1, 30720);
  GLD(ha4_1, v16, sh1, sL + L_HB + 2048);
  FMA(f3_t11[0], f3_01[2], rsf[0], f3_t11[0]);
  FMA(f3_t11[1], f3_01[3], rsf[0], f3_t11[1]);
  MF(g0_11, b122_10, b122_11, ha1_1);
  PRIO(1);
  MF(g0_02, b122_20, b122_21, ha1_0);
  RDT(b123_20, bb2, 28672);
  RDT(b123_21, bb2, 30720);
  FMA(f3_t10[0], f3_r1[0], rsf[0], f3_t10[0]);
  FMA(f3_t10[1], f3_r1[1], rsf[0], f3_t10[1]);
  MF(g0_12, b122_20, b122_21, ha1_1);
  FMA(f3_t11[0], f3_r1[2], rsf[0], f3_t11[0]);
  FMA(f3_t11[1], f3_r1[3], rsf[0], f3_t11[1]);
  WLG(4);
  MF(g0_03, b122_30, b122_31, ha1_0);
  RDT(b123_30, bb3, 28672);
  RDT(b123_31, bb3, 30720);
  GLD(ha5_0, v16, sh0, sL + L_HB + 3072);
  SQA(f3_t10[0], f3_t10[0]);
  SQA(f3_t10[1], f3_t10[1]);
  MF(g0_13, b122_30, b122_31, ha1_1);
  SQA(f3_t11[0], f3_t11[0]);
  SQA(f3_t11[1], f3_t11[1]);
  MF(g1_00, b123_00, b123_01, ha1_0);
  PRIO(0);
  WVM(12);
  BAR(); STB(33);
  PRIO(3);
  RDT(b124_00, bb0, 32768);
  RDT(b124_01, bb0, 34816);
  unsigned f3_p10; CVT(f3_p10, f3_t10[0], f3_t10[1]);
  unsigned f3_p11; CVT(f3_p11, f3_t11[0], f3_t11[1]);
  MF(g1_10, b123_00, b123_01, ha1_1);
  RLU(f3_p10);
  RLU(f3_p11);
  WLG(4);
  MF(g1_01, b123_10, b123_11, ha1_0);
  RDT(b124_10, bb1, 32768);
  RDT(b124_11, bb1, 34816);
  const u32x4 f3_o0 = {f3_p00, f3_p01, f3_p10, f3_p11};
  GST(f3_o0, v16, shn, sL + L_HB);
  MF(g1_11, b123_10, b123_11, ha1_1);
  MF(g1_02, b123_20, b123_21, ha1_0);
  RDT(b124_20, bb2, 32768);
  RDT(b124_21, bb2, 34816);
  MF(g1_12, b123_20, b123_21, ha1_1);
  WLG(4);
  MF(g1_03, b123_30, b123_31, ha1_0);
  RDT(b124_30, bb3, 32768);
  RDT(b124_31, bb3, 34816);
  WVM(9);
  GSIG(3, sfo, sL + L_FH);
  MF(g1_13, b123_30, b123_31, ha1_1);
  GLD(ha5_1, v16, sh1, sL + L_HB + 3072);
  MF(g0_00, b124_00, b124_01, ha2_0);
  RDT(b125_00, bb0, 36864);
  RDT(b125_01, bb0, 38912);
  MF(g0_10, b124_00, b124_01, ha2_1);
  WLG(4);
  MF(g0_01, b124_10, b124_11, ha2_0);
  RDT(b125_10, bb1, 36864);
  RDT(b125_11, bb1, 38912);
  MF(g0_11, b124_10, b124_11, ha2_1);
  MF(g0_02, b124_20, b124_21, ha2_0);
  RDT(b125_20, bb2, 36864);
  RDT(b125_21, bb2, 38912);
  DMA(r2, vw2, s_20, 655360, 4096);
  MF(g0_12, b124_20, b124_21, ha2_1);
  WLG(4);
  MF(g0_03, b124_30, b124_31, ha2_0);
  RDT(b125_30, bb3, 36864);
  RDT(b125_31, bb3, 38912);
  MF(g0_13, b124_30, b124_31, ha2_1);
  MF(g1_00, b125_00, b125_01, ha2_0);
  PRIO(2);
  RDT(b126_00, bb0, 40960);
  RDT(b126_01, bb0, 43008);
  DMA(r2, vw2, s_21, 655360, 8192);
  MF(g1_10, b125_00, b125_01, ha2_1);
  WLG(4);
  MF(g1_01, b125_10, b125_11, ha2_0);
  RDT(b126_10, bb1, 40960);
  RDT(b126_11, bb1, 43008);
  MF(g1_11, b125_10, b125_11, ha2_1);
  MF(g1_02, b125_20, b125_21, ha2_0);
  RDT(b126_20, bb2, 40960);
  RDT(b126_21, bb2, 43008);
  MF(g1_12, b125_20, b125_21, ha2_1);
  DMA(r2, vw2, s_20, 720896, 12288);
  WLG(4);
  MF(g1_03, b125_30, b125_31, ha2_0);
  RDT(b126_30, bb3, 40960);
  RDT(b126_31, bb3, 43008);
  MF(g1_13, b125_30, b125_31, ha2_1);
  WVM(13);
  MF(g0_00, b126_00, b126_01, ha3_0);
  PRIO(1);
  RDT(b127_00, bb0, 53248);
  RDT(b127_01, bb0, 55296);
  MF(g0_10, b126_00, b126_01, ha3_1);
  DMA(r2, vw2, s_21, 720896, 16384);
  WLG(4);
  MF(g0_01, b126_10, b126_11, ha3_0);
  RDT(b127_10, bb1, 53248);
  RDT(b127_11, bb1, 55296);
  MF(g0_11, b126_10, b126_11, ha3_1);
  MF(g0_02, b126_20, b126_21, ha3_0);
  RDT(b127_20, bb2, 53248);
  RDT(b127_21, bb2, 55296);
  MF(g0_12, b126_20, b126_21, ha3_1);
  PRIO(0);
  GLD(ha6_0, v16, sh0, sL + L_HB + 10240);
  WLG(4);
  MF(g0_03, b126_30, b126_31, ha3_0);
  RDT(b127_30, bb3, 53248);
  RDT(b127_31, bb3, 55296);
  MF(g0_13, b126_30, b126_31, ha3_1);
  MF(g1_00, b127_00, b127_01, ha3_0);
  WVM(11);
  BAR(); STB(34);
  PRIO(3);
  RDT(b128_00, bb0, 57344);
  RDT(b128_01, bb0, 59392);
  MF(g1_10, b127_00, b127_01, ha3_1);
  WLG(4);
  MF(g1_01, b127_10, b127_11, ha3_0);
  RDT(b128_10, bb1, 57344);
  RDT(b128_11, bb1, 59392);
  GLD(ha6_1, v16, sh1, sL + L_HB + 10240);
  MF(g1_11, b127_10, b127_11, ha3_1);
  MF(g1_02, b127_20, b127_21, ha3_0);
  RDT(b128_20, bb2, 57344);
  RDT(b128_21, bb2, 59392);
  MF(g1_12, b127_20, b127_21, ha3_1);
  WLG(4);
  MF(g1_03, b127_30, b127_31, ha3_0);
  RDT(b128_30, bb3, 57344);
  RDT(b128_31, bb3, 59392);
  GLD(ha7_0, v16, sh0, sL + L_HB + 11264);
  MF(g1_13, b127_30, b127_31, ha3_1);
  WVM(11);
  MF(g0_00, b128_00, b128_01, ha4_0);
  RDT(b129_00, bb0, 45056);
  RDT(b129_01, bb0, 47104);
  MF(g0_10, b128_00, b128_01, ha4_1);
  WLG(4);
  MF(g0_01, b128_10, b128_11, ha4_0);
  RDT(b129_10, bb1, 45056);
  RDT(b129_11, bb1, 47104);
  MF(g0_11, b128_10, b128_11, ha4_1);
  GLD(ha7_1, v16, sh1, sL + L_HB + 11264);
  MF(g0_02, b128_20, b128_21, ha4_0);
  RDT(b129_20, bb2, 45056);
  RDT(b129_21, bb2, 47104);
  MF(g0_12, b128_20, b128_21, ha4_1);
  WLG(4);
  MF(g0_03, b128_30, b128_31, ha4_0);
  RDT(b129_30, bb3, 45056);
  RDT(b129_31, bb3, 47104);
  MF(g0_13, b128_30, b128_31, ha4_1);
  DMA(r2, vw2, s_20, 393216, 20480);
  MF(g1_00, b129_00, b129_01, ha4_0);
  PRIO(2);
  RDT(b130_00, bb0, 49152);
  RDT(b130_01, bb0, 51200);
  MF(g1_10, b129_00, b129_01, ha4_1);
  GLD1(fl2, vfl, sfi, sL + L_FH);
  WLG(4);
  MF(g1_01, b129_10, b129_11, ha4_0);
  RDT(b130_10, bb1, 49152);
  RDT(b130_11, bb1, 51200);
  MF(g1_11, b129_10, b129_11, ha4_1);
  DMA(r2, vw2, s_21, 393216, 24576);
  MF(g1_02, b129_20, b129_21, ha4_0);
  RDT(b130_20, bb2, 49152);
  RDT(b130_21, bb2, 51200);
  MF(g1_12, b129_20, b129_21, ha4_1);
  WLG(4);
  MF(g1_03, b129_30, b129_31, ha4_0);
  RDT(b130_30, bb3, 49152);
  RDT(b130_31, bb3, 51200);
  MF(g1_13, b129_30, b129_31, ha4_1);
  WVM(11);
  MF(g0_00, b130_00, b130_01, ha5_0);
  PRIO(1);
  RDT(b131_00, bb0, 0);
  RDT(b131_01, bb0, 2048);
  DMA(r2, vw2, s_20, 458752, 28672);
  MF(g0_10, b130_00, b130_01, ha5_1);
  WLG(4);
  MF(g0_01, b130_10, b130_11, ha5_0);
  RDT(b131_10, bb1, 0);
  RDT(b131_11, bb1, 2048);
  MF(g0_11, b130_10, b130_11, ha5_1);
  MF(g0_02, b130_20, b130_21, ha5_0);
  RDT(b131_20, bb2, 0);
  RDT(b131_21, bb2, 2048);
  DMA(r2, vw2, s_21, 458752, 32768);
  MF(g0_12, b130_20, b130_21, ha5_1);
  PRIO(0);
  WLG(4);
  MF(g0_03, b130_30, b130_31, ha5_0);
  RDT(b131_30, bb3, 0);
  RDT(b131_31, bb3, 2048);
  MF(g0_13, b130_30, b130_31, ha5_1);
  MF(g1_00, b131_00, b131_01, ha5_0);
  WVM(9);
  BAR(); STB(35);
  PRIO(3);
  RDT(b132_00, bb0, 4096);
  RDT(b132_01, bb0, 6144);
  MF(g1_10, b131_00, b131_01, ha5_1);
  GLD(ha8_0, v16, sh0, sL + L_HB + 6144);
  WLG(4);
  MF(g1_01, b131_10, b131_11, ha5_0);
  RDT(b132_10, bb1, 4096);
  RDT(b132_11, bb1, 6144);
  MF(g1_11, b131_10, b131_11, ha5_1);
  MF(g1_02, b131_20, b131_21, ha5_0);
  RDT(b132_20, bb2, 4096);
  RDT(b132_21, bb2, 6144);
  MF(g1_12, b131_20, b131_21, ha5_1);
  GLD(ha8_1, v16, sh1, sL + L_HB + 6144);
  WLG(4);
  MF(g1_03, b131_30, b131_31, ha5_0);
  RDT(b132_30, bb3, 4096);
  RDT(b132_31, bb3, 6144);
  MF(g1_13, b131_30, b131_31, ha5_1);
  WVM(9);
  MF(g0_00, b132_00, b132_01, ha6_0);
  RDT(b133_00, bb0, 8192);
  RDT(b133_01, bb0, 10240);
  MF(g0_10, b132_00, b132_01, ha6_1);
  GLD(ha9_0, v16, sh0, sL + L_HB + 7168);
  WLG(4);
  MF(g0_01, b132_10, b132_11, ha6_0);
  RDT(b133_10, bb1, 8192);
  RDT(b133_11, bb1, 10240);
  MF(g0_11, b132_10, b132_11, ha6_1);
  MF(g0_02, b132_20, b132_21, ha6_0);
  RDT(b133_20, bb2, 8192);
  RDT(b133_21, bb2, 10240);
  MF(g0_12, b132_20, b132_21, ha6_1);
  WLG(4);
  MF(g0_03, b132_30, b132_31, ha6_0);
  RDT(b133_30, bb3, 8192);
  RDT(b133_31, bb3, 10240);
  GLD(ha9_1, v16, sh1, sL + L_HB + 7168);
  MF(g0_13, b132_30, b132_31, ha6_1);
  MF(g1_00, b133_00, b133_01, ha6_0);
  PRIO(2);
  RDT(b134_00, bb0, 12288);
  RDT(b134_01, bb0, 14336);
  MF(g1_10, b133_00, b133_01, ha6_1);
  WLG(4);
  MF(g1_01, b133_10, b133_11, ha6_0);
  RDT(b134_10, bb1, 12288);
  RDT(b134_11, bb1, 14336);
  DMA(r2, vw2, s_20, 262144, 36864);
  MF(g1_11, b133_10, b133_11, ha6_1);
  MF(g1_02, b133_20, b133_21, ha6_0);
  RDT(b134_20, bb2, 12288);
  RDT(b134_21, bb2, 14336);
  WVM(8);
  CHKF(fl2, 3u, vfl, sfi, sL + L_FH);
  MF(g1_12, b133_20, b133_21, ha6_1);
  WLG(4);
  MF(g1_03, b133_30, b133_31, ha6_0);
  RDT(b134_30, bb3, 12288);
  RDT(b134_31, bb3, 14336);
  MF(g1_13, b133_30, b133_31, ha6_1);
  DMA(r2, vw2, s_20, 327680, 40960);
  MF(g0_00, b134_00, b134_01, ha7_0);
  PRIO(1);
  RDT(b135_00, bb0, 16384);
  RDT(b135_01, bb0, 18432);
  MF(g0_10, b134_00, b134_01, ha7_1);
  WLG(4);
  MF(g0_01, b134_10, b134_11, ha7_0);
  RDT(b135_10, bb1, 16384);
  RDT(b135_11, bb1, 18432);
  MF(g0_11, b134_10, b134_11, ha7_1);
  DMA(r2, vw2, s_20, 786432, 53248);
  MF(g0_02, b134_20, b134_21, ha7_0);
  RDT(b135_20, bb2, 16384);
  RDT(b135_21, bb2, 18432);
  MF(g0_12, b134_20, b134_21, ha7_1);
  PRIO(0);
  WLG(4);
  MF(g0_03, b134_30, b134_31, ha7_0);
  RDT(b135_30, bb3, 16384);
  RDT(b135_31, bb3, 18432);
  MF(g0_13, b134_30, b134_31, ha7_1);
  DMA(r2, vw2, s_20, 851968, 57344);
  MF(g1_00, b135_00, b135_01, ha7_0);
  WVM(8);
  BAR(); STB(36);
  PRIO(3);
  RDT(b136_00, bb0, 20480);
  RDT(b136_01, bb0, 22528);
  MF(g1_10, b135_00, b135_01, ha7_1);
  WLG(4);
  MF(g1_01, b135_10, b135_11, ha7_0);
  RDT(b136_10, bb1, 20480);
  RDT(b136_11, bb1, 22528);
  WVM(6);
  unsigned hat8, hat8b; MX3(hat8, ha8_0[0], ha8_0[1], ha8_0[2]);
  MF(g1_11, b135_10, b135_11, ha7_1);
  MX3(hat8b, ha8_1[0], ha8_1[1], ha8_1[2]);
  MF(g1_02, b135_20, b135_21, ha7_0);
  RDT(b136_20, bb2, 20480);
  RDT(b136_21, bb2, 22528);
  GLD(ha10_0, v16, sh0, sL + L_HB + 4096);
  MX3(hat8, hat8, ha8_0[3], ha8_1[3]);
  MF(g1_12, b135_20, b135_21, ha7_1);
  MX2(hat8, hat8, hat8b);
  WLG(4);
  MF(g1_03, b135_30, b135_31, ha7_0);
  RDT(b136_30, bb3, 20480);
  RDT(b136_31, bb3, 22528);
  CHK2(hat8, ha8_0, ha8_1, v16, sh0, sL + L_HB + 6144, sh1, sL + L_HB + 6144, 4);
  MF(g1_13, b135_30, b135_31, ha7_1);
  MF(g0_00, b136_00, b136_01, ha8_0);
  RDT(b137_00, bb0, 24576);
  RDT(b137_01, bb0, 26624);
  GLD(ha10_1, v16, sh1, sL + L_HB + 4096);
  MF(g0_10, b136_00, b136_01, ha8_1);
  WLG(4);
  MF(g0_01, b136_10, b136_11, ha8_0);
  RDT(b137_10, bb1, 24576);
  RDT(b137_11, bb1, 26624);
  MF(g0_11, b136_10, b136_11, ha8_1);
  MF(g0_02, b136_20, b136_21, ha8_0);
  RDT(b137_20, bb2, 24576);
  RDT(b137_21, bb2, 26624);
  MF(g0_12, b136_20, b136_21, ha8_1);
  GLD(ha11_0, v16, sh0, sL + L_HB + 5120);
  WLG(4);
  MF(g0_03, b136_30, b136_31, ha8_0);
  RDT(b137_30, bb3, 24576);
  RDT(b137_31, bb3, 26624);
  MF(g0_13, b136_30, b136_31, ha8_1);
  MF(g1_00, b137_00, b137_01, ha8_0);
  PRIO(2);
  RDT(b138_00, bb0, 28672);
  RDT(b138_01, bb0, 30720);
  MF(g1_10, b137_00, b137_01, ha8_1);
  GLD(ha11_1, v16, sh1, sL + L_HB + 5120);
  WLG(4);
  MF(g1_01, b137_10, b137_11, ha8_0);
  RDT(b138_10, bb1, 28672);
  RDT(b138_11, bb1, 30720);
  WVM(8);
  unsigned hat9, hat9b; MX3(hat9, ha9_0[0], ha9_0[1], ha9_0[2]);
  MF(g1_11, b137_10, b137_11, ha8_1);
  MX3(hat9b, ha9_1[0], ha9_1[1], ha9_1[2]);
  MF(g1_02, b137_20, b137_21, ha8_0);
  RDT(b138_20, bb2, 28672);
  RDT(b138_21, bb2, 30720);
  MX3(hat9, hat9, ha9_0[3], ha9_1[3]);
  MF(g1_12, b137_20, b137_21, ha8_1);
  GLD(ha12_0, v16, sh0, sL + L_HB + 12288);
  MX2(hat9, hat9, hat9b);
  WLG(4);
  MF(g1_03, b137_30, b137_31, ha8_0);
  RDT(b138_30, bb3, 28672);
  RDT(b138_31, bb3, 30720);
  CHK2(hat9, ha9_0, ha9_1, v16, sh0, sL + L_HB + 7168, sh1, sL + L_HB + 7168, 4);
  MF(g1_13, b137_30, b137_31, ha8_1);
  MF(g0_00, b138_00, b138_01, ha9_0);
  PRIO(1);
  RDT(b139_00, bb0, 32768);
  RDT(b139_01, bb0, 34816);
  MF(g0_10, b138_00, b138_01, ha9_1);
  WLG(4);
  MF(g0_01, b138_10, b138_11, ha9_0);
  RDT(b139_10, bb1, 32768);
  RDT(b139_11, bb1, 34816);
  GLD(ha12_1, v16, sh1, sL + L_HB + 12288);
  MF(g0_11, b138_10, b138_11, ha9_1);
  MF(g0_02, b138_20, b138_21, ha9_0);
  RDT(b139_20, bb2, 32768);
  RDT(b139_21, bb2, 34816);
  MF(g0_12, b138_20, b138_21, ha9_1);
  PRIO(0);
  WLG(4);
  MF(g0_03, b138_30, b138_31, ha9_0);
  RDT(b139_30, bb3, 32768);
  RDT(b139_31, bb3, 34816);
  GLD(ha13_0, v16, sh0, sL + L_HB + 13312);
  MF(g0_13, b138_30, b138_31, ha9_1);
  MF(g1_00, b139_00, b139_01, ha9_0);
  WVM(7);
  BAR(); STB(37);
  PRIO(3);
  RDT(b140_00, bb0, 36864);
  RDT(b140_01, bb0, 38912);
  MF(g1_10, b139_00, b139_01, ha9_1);
  WLG(4);
  MF(g1_01, b139_10, b139_11, ha9_0);
  RDT(b140_10, bb1, 36864);
  RDT(b140_11, bb1, 38912);
  MF(g1_11, b139_10, b139_11, ha9_1);
  GLD(ha13_1, v16, sh1, sL + L_HB + 13312);
  MF(g1_02, b139_20, b139_21, ha9_0);
  RDT(b140_20, bb2, 36864);
  RDT(b140_21, bb2, 38912);
  MF(g1_12, b139_20, b139_21, ha9_1);
  WLG(4);
  MF(g1_03, b139_30, b139_31, ha9_0);
  RDT(b140_30, bb3, 36864);
  RDT(b140_31, bb3, 38912);
  MF(g1_13, b139_30, b139_31, ha9_1);
  DMA(r2, vw2, s_21, 262144, 45056);
  WVM(7);
  MF(g0_00, b140_00, b140_01, ha10_0);
  RDT(b141_00, bb0, 40960);
  RDT(b141_01, bb0, 43008);
  MF(g0_10, b140_00, b140_01, ha10_1);
  WLG(4);
  MF(g0_01, b140_10, b140_11, ha10_0);
  RDT(b141_10, bb1, 40960);
  RDT(b141_11, bb1, 43008);
  MF(g0_11, b140_10, b140_11, ha10_1);
  DMA(r2, vw2, s_21, 327680, 49152);
  MF(g0_02, b140_20, b140_21, ha10_0);
  RDT(b141_20, bb2, 40960);
  RDT(b141_21, bb2, 43008);
  MF(g0_12, b140_20, b140_21, ha10_1);
  WLG(4);
  MF(g0_03, b140_30, b140_31, ha10_0);
  RDT(b141_30, bb3, 40960);
  RDT(b141_31, bb3, 43008);
  MF(g0_13, b140_30, b140_31, ha10_1);
  WVM(6);
  MF(g0_00, b141_00, b141_01, ha11_0);
  PRIO(2);
  RDT(b142_00, bb0, 53248);
  RDT(b142_01, bb0, 55296);
  DMA(r2, vw2, s_21, 786432, 0);
  MF(g0_10, b141_00, b141_01, ha11_1);
  RDT(b143_00, bb0, 57344);
  RDT(b143_01, bb0, 59392);
  WLG(6);
  MF(g0_01, b141_10, b141_11, ha11_0);
  RDT(b142_10, bb1, 53248);
  RDT(b142_11, bb1, 55296);
  MF(g0_11, b141_10, b141_11, ha11_1);
  RDT(b143_10, bb1, 57344);
  RDT(b143_11, bb1, 59392);
  MF(g0_02, b141_20, b141_21, ha11_0);
  RDT(b142_20, bb2, 53248);
  RDT(b142_21, bb2, 55296);
  DMA(r2, vw2, s_21, 851968, 4096);
  MF(g0_12, b141_20, b141_21, ha11_1);
  RDT(b143_20, bb2, 57344);
  RDT(b143_21, bb2, 59392);
  WLG(8);
  MF(g0_03, b141_30, b141_31, ha11_0);
  RDT(b142_30, bb3, 53248);
  RDT(b142_31, bb3, 55296);
  MF(g0_13, b141_30, b141_31, ha11_1);
  RDT(b143_30, bb3, 57344);
  RDT(b143_31, bb3, 59392);
  WVM(6);
  MF(g0_10, b142_00, b142_01, ha12_1);
  PRIO(1);
  WVM(4);
  MF(g0_10, b143_00, b143_01, ha13_1);
  DMA(r2, vw2, s_22, 0, 8192);
  WLG(4);
  MF(g0_11, b142_10, b142_11, ha12_1);
  MF(g0_11, b143_10, b143_11, ha13_1);
  MF(g0_12, b142_20, b142_21, ha12_1);
  NOP(2);
  unsigned g0_s00, g0_s01; CVT(g0_s00, g0_10[0], g0_10[1]); CVT(g0_s01, g0_10[2], g0_10[3]);
  MF(g0_12, b143_20, b143_21, ha13_1);
  PRIO(0);
  DMA(r2, vw2, s_22, 65536, 12288);
  WLG(0);
  MF(g0_13, b142_30, b142_31, ha12_1);
  NOP(1);
  unsigned g0_s10, g0_s11; CVT(g0_s10, g0_11[0], g0_11[1]); CVT(g0_s11, g0_11[2], g0_11[3]);
  WR(((u32x4){g0_s00, g0_s01, g0_s10, g0_s11}), xw, 1024);
  MF(g0_13, b143_30, b143_31, ha13_1);
  MF(g0_00, b142_00, b142_01, ha12_0);
  NOP(0);
  unsigned g0_s20, g0_s21; CVT(g0_s20, g0_12[0], g0_12[1]); CVT(g0_s21, g0_12[2], g0_12[3]);
  MF(g0_01, b142_10, b142_11, ha12_0);
  DMA(r2, vw2, s_22, 524288, 16384);
  MF(g0_02, b142_20, b142_21, ha12_0);
  NOP(3);
  unsigned g0_s30, g0_s31; CVT(g0_s30, g0_13[0], g0_13[1]); CVT(g0_s31, g0_13[2], g0_13[3]);
  WR(((u32x4){g0_s20, g0_s21, g0_s30, g0_s31}), xw, 3072);
  MF(g0_03, b142_30, b142_31, ha12_0);
  WVM(3);
  WLG(0);
  BAR(); STB(38);
  PRIO(3);
  RDT(b144_00, bb0, 45056);
  RDT(b144_01, bb0, 47104);
  MF(g0_00, b143_00, b143_01, ha13_0);
  RDT(b144_10, bb1, 45056);
  RDT(b144_11, bb1, 47104);
  MF(g0_01, b143_10, b143_11, ha13_0);
  RDT(b144_20, bb2, 45056);
  RDT(b144_21, bb2, 47104);
  MF(g0_02, b143_20, b143_21, ha13_0);
  RDT(b144_30, bb3, 45056);
  RDT(b144_31, bb3, 47104);
  DMA(r2, vw2, s_22, 589824, 20480);
  MF(g0_03, b143_30, b143_31, ha13_0);
  STP(17);
  WLG(4);
  MF(g1_00, b144_00, b144_01, ha10_0);
  RDT(b145_00, bb0, 49152);
  RDT(b145_01, bb0, 51200);
  RDF(g0_r1, xr, 1024);
  RDF(g0_r3, xr, 3072);
  MF(g1_10, b144_00, b144_01, ha10_1);
  MF(g1_01, b144_10, b144_11, ha10_0);
  RDT(b145_10, bb1, 49152);
  RDT(b145_11, bb1, 51200);
  DMA(r2, vw2, s_22, 131072, 24576);
  MF(g1_11, b144_10, b144_11, ha10_1);
  f32x2 g0_t00, g0_t01;
  WLG(3);
  { float u_; ULO(u_, g0_r1[0]); g0_t00[0] = u_; }
  { float u_; UHI(u_, g0_r1[0]); g0_t00[1] = u_; }
  MF(g1_02, b144_20, b144_21, ha10_0);
  RDT(b145_20, bb2, 49152);
  RDT(b145_21, bb2, 51200);
  { float u_; ULO(u_, g0_r1[1]); g0_t01[0] = u_; }
  { float u_; UHI(u_, g0_r1[1]); g0_t01[1] = u_; }
  MF(g1_12, b144_20, b144_21, ha10_1);
  ADD(g0_t00[0], g0_t00[0], g0_00[0]);
  ADD(g0_t00[1], g0_t00[1], g0_00[1]);
  MF(g1_03, b144_30, b144_31, ha10_0);
  RDT(b145_30, bb3, 49152);
  RDT(b145_31, bb3, 51200);
  ADD(g0_t01[0], g0_t01[0], g0_00[2]);
  ADD(g0_t01[1], g0_t01[1], g0_00[3]);
  MF(g1_13, b144_30, b144_31, ha10_1);
  DMA(r2, vw2, s_22, 196608, 28672);
  unsigned g0_p00; CVT(g0_p00, g0_t00[0], g0_t00[1]);
  unsigned g0_p01; CVT(g0_p01, g0_t01[0], g0_t01[1]);
  MF(g1_00, b145_00, b145_01, ha11_0);
  RDT(b146_00, bb0, 0);
  RDT(b146_01, bb0, 2048);
  f32x2 g0_t10, g0_t11;
  { float u_; ULO(u_, g0_r1[2]); g0_t10[0] = u_; }
  { float u_; UHI(u_, g0_r1[2]); g0_t10[1] = u_; }
  MF(g1_10, b145_00, b145_01, ha11_1);
  RDT(b147_00, bb0, 4096);
  RDT(b147_01, bb0, 6144);
  { float u_; ULO(u_, g0_r1[3]); g0_t11[0] = u_; }
  { float u_; UHI(u_, g0_r1[3]); g0_t11[1] = u_; }
  WLG(6);
  MF(g1_01, b145_10, b145_11, ha11_0);
  RDT(b146_10, bb1, 0);
  RDT(b146_11, bb1, 2048);
  ADD(g0_t10[0], g0_t10[0], g0_01[0]);
  ADD(g0_t10[1], g0_t10[1], g0_01[1]);
  MF(g1_11, b145_10, b145_11, ha11_1);
  PRIO(2);
  RDT(b147_10, bb1, 4096);
  RDT(b147_11, bb1, 6144);
  DMA(r2, vw2, s_22, 655360, 32768);
  ADD(g0_t11[0], g0_t11[0], g0_01[2]);
  ADD(g0_t11[1], g0_t11[1], g0_01[3]);
  MF(g1_02, b145_20, b145_21, ha11_0);
  RDT(b146_20, bb2, 0);
  RDT(b146_21, bb2, 2048);
  unsigned g0_p10; CVT(g0_p10, g0_t10[0], g0_t10[1]);
  unsigned g0_p11; CVT(g0_p11, g0_t11[0], g0_t11[1]);
  MF(g1_12, b145_20, b145_21, ha11_1);
  RDT(b147_20, bb2, 4096);
  RDT(b147_21, bb2, 6144);
  const u32x4 g0_o0 = {g0_p00, g0_p01, g0_p10, g0_p11};
  GST(g0_o0, v16, sp0, sL + L_P + 0);
  f32x2 g0_t20, g0_t21;
  { float u_; ULO(u_, g0_r3[0]); g0_t20[0] = u_; }
  WLG(8);
  MF(g1_03, b145_30, b145_31, ha11_0);
  RDT(b146_30, bb3, 0);
  RDT(b146_31, bb3, 2048);
  { float u_; UHI(u_, g0_r3[0]); g0_t20[1] = u_; }
  { float u_; ULO(u_, g0_r3[1]); g0_t21[0] = u_; }
  MF(g1_13, b145_30, b145_31, ha11_1);
  RDT(b147_30, bb3, 4096);
  RDT(b147_31, bb3, 6144);
  { float u_; UHI(u_, g0_r3[1]); g0_t21[1] = u_; }
  ADD(g0_t20[0], g0_t20[0], g0_02[0]);
  MF(g1_10, b146_00, b146_01, ha12_1);
  ADD(g0_t20[1], g0_t20[1], g0_02[1]);
  ADD(g0_t21[0], g0_t21[0], g0_02[2]);
  MF(g1_10, b147_00, b147_01, ha13_1);
  ADD(g0_t21[1], g0_t21[1], g0_02[3]);
  unsigned g0_p20; CVT(g0_p20, g0_t20[0], g0_t20[1]);
  WLG(4);
  MF(g1_11, b146_10, b146_11, ha12_1);
  DMA(r2, vw2, s_22, 720896, 36864);
  unsigned g0_p21; CVT(g0_p21, g0_t21[0], g0_t21[1]);
  f32x2 g0_t30, g0_t31;
  { float u_; ULO(u_, g0_r3[2]); g0_t30[0] = u_; }
  MF(g1_11, b147_10, b147_11, ha13_1);
  PRIO(1);
  { float u_; UHI(u_, g0_r3[2]); g0_t30[1] = u_; }
  { float u_; ULO(u_, g0_r3[3]); g0_t31[0] = u_; }
  MF(g1_12, b146_20, b146_21, ha12_1);
  { float u_; UHI(u_, g0_r3[3]); g0_t31[1] = u_; }
  ADD(g0_t30[0], g0_t30[0], g0_03[0]);
  unsigned g1_s00, g1_s01; CVT(g1_s00, g1_10[0], g1_10[1]); CVT(g1_s01, g1_10[2], g1_10[3]);
  MF(g1_12, b147_20, b147_21, ha13_1);
  ADD(g0_t30[1], g0_t30[1], g0_03[1]);
  ADD(g0_t31[0], g0_t31[0], g0_03[2]);
  WLG(0);
  MF(g1_13, b146_30, b146_31, ha12_1);
  ADD(g0_t31[1], g0_t31[1], g0_03[3]);
  unsigned g0_p30; CVT(g0_p30, g0_t30[0], g0_t30[1]);
  unsigned g1_s10, g1_s11; CVT(g1_s10, g1_11[0], g1_11[1]); CVT(g1_s11, g1_11[2], g1_11[3]);
  WR(((u32x4){g1_s00, g1_s01, g1_s10, g1_s11}), xw, 0);
  MF(g1_13, b147_30, b147_31, ha13_1);
  DMA(r2, vw2, s_22, 393216, 40960);
  unsigned g0_p31; CVT(g0_p31, g0_t31[0], g0_t31[1]);
  const u32x4 g0_o1 = {g0_p20, g0_p21, g0_p30, g0_p31};
  GST(g0_o1, v16, sp0, sL + L_P + 1024);
  MF(g1_00, b146_00, b146_01, ha12_0);
  PRIO(0);
  unsigned g1_s20, g1_s21; CVT(g1_s20, g1_12[0], g1_12[1]); CVT(g1_s21, g1_12[2], g1_12[3]);
  MF(g1_01, b146_10, b146_11, ha12_0);
  MF(g1_02, b146_20, b146_21, ha12_0);
  NOP(2);
  unsigned g1_s30, g1_s31; CVT(g1_s30, g1_13[0], g1_13[1]); CVT(g1_s31, g1_13[2], g1_13[3]);
  WR(((u32x4){g1_s20, g1_s21, g1_s30, g1_s31}), xw, 2048);
  MF(g1_03, b146_30, b146_31, ha12_0);
  WVM(7);
  WLG(0);
  BAR(); STB(39);
  PRIO(3);
  RDT(b148_00, bb0, 8192);
  RDT(b148_01, bb0, 10240);
  MF(g1_00, b147_00, b147_01, ha13_0);
  RDT(b148_10, bb1, 8192);
  RDT(b148_11, bb1, 10240);
  DMA(r2, vw2, s_22, 458752, 53248);
  MF(g1_01, b147_10, b147_11, ha13_0);
  RDT(b148_20, bb2, 8192);
  RDT(b148_21, bb2, 10240);
  MF(g1_02, b147_20, b147_21, ha13_0);
  RDT(b148_30, bb3, 8192);
  RDT(b148_31, bb3, 10240);
  MF(g1_03, b147_30, b147_31, ha13_0);
  STP(18);
  WLG(4);
  MF0(g2_00, b148_00, b148_01, ha0_0);
  RDT(b149_00, bb0, 12288);
  RDT(b149_01, bb0, 14336);
  RDF(g1_r1, xr, 0);
  RDF(g1_r3, xr, 2048);
  MF0(g2_10, b148_00, b148_01, ha0_1);
  DMA(r2, vw2, s_22, 262144, 57344);
  MF0(g2_01, b148_10, b148_11, ha0_0);
  RDT(b149_10, bb1, 12288);
  RDT(b149_11, bb1, 14336);
  MF0(g2_11, b148_10, b148_11, ha0_1);
  f32x2 g1_t00, g1_t01;
  WLG(3);
  { float u_; ULO(u_, g1_r1[0]); g1_t00[0] = u_; }
  { float u_; UHI(u_, g1_r1[0]); g1_t00[1] = u_; }
  MF0(g2_02, b148_20, b148_21, ha0_0);
  RDT(b149_20, bb2, 12288);
  RDT(b149_21, bb2, 14336);
  { float u_; ULO(u_, g1_r1[1]); g1_t01[0] = u_; }
  { float u_; UHI(u_, g1_r1[1]); g1_t01[1] = u_; }
  MF0(g2_12, b148_20, b148_21, ha0_1);
  DMA(r2, vw2, s_22, 327680, 45056);
  ADD(g1_t00[0], g1_t00[0], g1_00[0]);
  ADD(g1_t00[1], g1_t00[1], g1_00[1]);
  MF0(g2_03, b148_30, b148_31, ha0_0);
  RDT(b149_30, bb3, 12288);
  RDT(b149_31, bb3, 14336);
  ADD(g1_t01[0], g1_t01[0], g1_00[2]);
  ADD(g1_t01[1], g1_t01[1], g1_00[3]);
  MF0(g2_13, b148_30, b148_31, ha0_1);
  unsigned g1_p00; CVT(g1_p00, g1_t00[0], g1_t00[1]);
  unsigned g1_p01; CVT(g1_p01, g1_t01[0], g1_t01[1]);
  MF(g2_00, b149_00, b149_01, ha1_0);
  RDT(b150_00, bb0, 16384);
  RDT(b150_01, bb0, 18432);
  f32x2 g1_t10, g1_t11;
  { float u_; ULO(u_, g1_r1[2]); g1_t10[0] = u_; }
  { float u_; UHI(u_, g1_r1[2]); g1_t10[1] = u_; }
  MF(g2_10, b149_00, b149_01, ha1_1);
  { float u_; ULO(u_, g1_r1[3]); g1_t11[0] = u_; }
  { float u_; UHI(u_, g1_r1[3]); g1_t11[1] = u_; }
  WLG(4);
  MF(g2_01, b149_10, b149_11, ha1_0);
  RDT(b150_10, bb1, 16384);
  RDT(b150_11, bb1, 18432);
  DMA(r2, vw2, s_22, 786432, 49152);
  ADD(g1_t10[0], g1_t10[0], g1_01[0]);
  ADD(g1_t10[1], g1_t10[1], g1_01[1]);
  MF(g2_11, b149_10, b149_11, ha1_1);
  PRIO(2);
  ADD(g1_t11[0], g1_t11[0], g1_01[2]);
  ADD(g1_t11[1], g1_t11[1], g1_01[3]);
  MF(g2_02, b149_20, b149_21, ha1_0);
  RDT(b150_20, bb2, 16384);
  RDT(b150_21, bb2, 18432);
  unsigned g1_p10; CVT(g1_p10, g1_t10[0], g1_t10[1]);
  unsigned g1_p11; CVT(g1_p11, g1_t11[0], g1_t11[1]);
  MF(g2_12, b149_20, b149_21, ha1_1);
  const u32x4 g1_o0 = {g1_p00, g1_p01, g1_p10, g1_p11};
  GST(g1_o0, v16, sp1, sL + L_P + 0);
  f32x2 g1_t20, g1_t21;
  { float u_; ULO(u_, g1_r3[0]); g1_t20[0] = u_; }
  WLG(4);
  MF(g2_03, b149_30, b149_31, ha1_0);
  RDT(b150_30, bb3, 16384);
  RDT(b150_31, bb3, 18432);
  { float u_; UHI(u_, g1_r3[0]); g1_t20[1] = u_; }
  { float u_; ULO(u_, g1_r3[1]); g1_t21[0] = u_; }
  MF(g2_13, b149_30, b149_31, ha1_1);
  { float u_; UHI(u_, g1_r3[1]); g1_t21[1] = u_; }
  ADD(g1_t20[0], g1_t20[0], g1_02[0]);
  MF(g2_00, b150_00, b150_01, ha2_0);
  RDT(b151_00, bb0, 20480);
  RDT(b151_01, bb0, 22528);
  ADD(g1_t20[1], g1_t20[1], g1_02[1]);
  ADD(g1_t21[0], g1_t21[0], g1_02[2]);
  MF(g2_10, b150_00, b150_01, ha2_1);
  ADD(g1_t21[1], g1_t21[1], g1_02[3]);
  unsigned g1_p20; CVT(g1_p20, g1_t20[0], g1_t20[1]);
  WLG(4);
  MF(g2_01, b150_10, b150_11, ha2_0);
  RDT(b151_10, bb1, 20480);
  RDT(b151_11, bb1, 22528);
  DMA(r2, vw2, s_22, 851968, 0);
  unsigned g1_p21; CVT(g1_p21, g1_t21[0], g1_t21[1]);
  f32x2 g1_t30, g1_t31;
  { float u_; ULO(u_, g1_r3[2]); g1_t30[0] = u_; }
  MF(g2_11, b150_10, b150_11, ha2_1);
  PRIO(1);
  { float u_; UHI(u_, g1_r3[2]); g1_t30[1] = u_; }
  { float u_; ULO(u_, g1_r3[3]); g1_t31[0] = u_; }
  MF(g2_02, b150_20, b150_21, ha2_0);
  RDT(b151_20, bb2, 20480);
  RDT(b151_21, bb2, 22528);
  { float u_; UHI(u_, g1_r3[3]); g1_t31[1] = u_; }
  ADD(g1_t30[0], g1_t30[0], g1_03[0]);
  MF(g2_12, b150_20, b150_21, ha2_1);
  ADD(g1_t30[1], g1_t30[1], g1_03[1]);
  ADD(g1_t31[0], g1_t31[0], g1_03[2]);
  WLG(4);
  MF(g2_03, b150_30, b150_31, ha2_0);
  RDT(b151_30, bb3, 20480);
  RDT(b151_31, bb3, 22528);
  ADD(g1_t31[1], g1_t31[1], g1_03[3]);
  unsigned g1_p30; CVT(g1_p30, g1_t30[0], g1_t30[1]);
  MF(g2_13, b150_30, b150_31, ha2_1);
  DMA(r2, vw2, s_23, 0, 4096);
  unsigned g1_p31; CVT(g1_p31, g1_t31[0], g1_t31[1]);
  const u32x4 g1_o1 = {g1_p20, g1_p21, g1_p30, g1_p31};
  GST(g1_o1, v16, sp1, sL + L_P + 1024);
  MF(g2_00, b151_00, b151_01, ha3_0);
  PRIO(0);
  WVM(10);
  BAR(); STB(40);
  PRIO(3);
  RDT(b152_00, bb0, 24576);
  RDT(b152_01, bb0, 26624);
  MF(g2_10, b151_00, b151_01, ha3_1);
  WLG(4);
  MF(g2_01, b151_10, b151_11, ha3_0);
  RDT(b152_10, bb1, 24576);
  RDT(b152_11, bb1, 26624);
  MF(g2_11, b151_10, b151_11, ha3_1);
  MF(g2_02, b151_20, b151_21, ha3_0);
  RDT(b152_20, bb2, 24576);
  RDT(b152_21, bb2, 26624);
  DMA(r2, vw2, s_23, 65536, 8192);
  MF(g2_12, b151_20, b151_21, ha3_1);
  WLG(4);
  MF(g2_03, b151_30, b151_31, ha3_0);
  RDT(b152_30, bb3, 24576);
  RDT(b152_31, bb3, 26624);
  MF(g2_13, b151_30, b151_31, ha3_1);
  MF(g2_00, b152_00, b152_01, ha4_0);
  RDT(b153_00, bb0, 28672);
  RDT(b153_01, bb0, 30720);
  MF(g2_10, b152_00, b152_01, ha4_1);
  DMA(r2, vw2, s_23, 524288, 12288);
  WLG(4);
  MF(g2_01, b152_10, b152_11, ha4_0);
  RDT(b153_10, bb1, 28672);
  RDT(b153_11, bb1, 30720);
  MF(g2_11, b152_10, b152_11, ha4_1);
  MF(g2_02, b152_20, b152_21, ha4_0);
  RDT(b153_20, bb2, 28672);
  RDT(b153_21, bb2, 30720);
  MF(g2_12, b152_20, b152_21, ha4_1);
  DMA(r2, vw2, s_23, 589824, 16384);
  WLG(4);
  MF(g2_03, b152_30, b152_31, ha4_0);
  RDT(b153_30, bb3, 28672);
  RDT(b153_31, bb3, 30720);
  MF(g2_13, b152_30, b152_31, ha4_1);
  MF(g2_00, b153_00, b153_01, ha5_0);
  PRIO(2);
  RDT(b154_00, bb0, 32768);
  RDT(b154_01, bb0, 34816);
  MF(g2_10, b153_00, b153_01, ha5_1);
  WLG(4);
  MF(g2_01, b153_10, b153_11, ha5_0);
  RDT(b154_10, bb1, 32768);
  RDT(b154_11, bb1, 34816);
  MF(g2_11, b153_10, b153_11, ha5_1);
  MF(g2_02, b153_20, b153_21, ha5_0);
  RDT(b154_20, bb2, 32768);
  RDT(b154_21, bb2, 34816);
  MF(g2_12, b153_20, b153_21, ha5_1);
  WLG(4);
  MF(g2_03, b153_30, b153_31, ha5_0);
  RDT(b154_30, bb3, 32768);
  RDT(b154_31, bb3, 34816);
  MF(g2_13, b153_30, b153_31, ha5_1);
  MF(g2_00, b154_00, b154_01, ha6_0);
  PRIO(1);
  RDT(b155_00, bb0, 36864);
  RDT(b155_01, bb0, 38912);
  MF(g2_10, b154_00, b154_01, ha6_1);
  WLG(4);
  MF(g2_01, b154_10, b154_11, ha6_0);
  RDT(b155_10, bb1, 36864);
  RDT(b155_11, bb1, 38912);
  MF(g2_11, b154_10, b154_11, ha6_1);
  MF(g2_02, b154_20, b154_21, ha6_0);
  RDT(b155_20, bb2, 36864);
  RDT(b155_21, bb2, 38912);
  MF(g2_12, b154_20, b154_21, ha6_1);
  PRIO(0);
  WLG(4);
  MF(g2_03, b154_30, b154_31, ha6_0);
  RDT(b155_30, bb3, 36864);
  RDT(b155_31, bb3, 38912);
  MF(g2_13, b154_30, b154_31, ha6_1);
  MF(g2_00, b155_00, b155_01, ha7_0);
  WVM(9);
  BAR(); STB(41);
  PRIO(3);
  RDT(b156_00, bb0, 40960);
  RDT(b156_01, bb0, 43008);
  DMA(r2, vw2, s_23, 131072, 20480);
  MF(g2_10, b155_00, b155_01, ha7_1);
  WLG(4);
  MF(g2_01, b155_10, b155_11, ha7_0);
  RDT(b156_10, bb1, 40960);
  RDT(b156_11, bb1, 43008);
  MF(g2_11, b155_10, b155_11, ha7_1);
  MF(g2_02, b155_20, b155_21, ha7_0);
  RDT(b156_20, bb2, 40960);
  RDT(b156_21, bb2, 43008);
  DMA(r2, vw2, s_23, 196608, 24576);
  MF(g2_12, b155_20, b155_21, ha7_1);
  WLG(4);
  MF(g2_03, b155_30, b155_31, ha7_0);
  RDT(b156_30, bb3, 40960);
  RDT(b156_31, bb3, 43008);
  MF(g2_13, b155_30, b155_31, ha7_1);
  MF(g2_00, b156_00, b156_01, ha8_0);
  RDT(b157_00, bb0, 53248);
  RDT(b157_01, bb0, 55296);
  DMA(r2, vw2, s_23, 655360, 28672);
  MF(g2_10, b156_00, b156_01, ha8_1);
  WLG(4);
  MF(g2_01, b156_10, b156_11, ha8_0);
  RDT(b157_10, bb1, 53248);
  RDT(b157_11, bb1, 55296);
  MF(g2_11, b156_10, b156_11, ha8_1);
  MF(g2_02, b156_20, b156_21, ha8_0);
  RDT(b157_20, bb2, 53248);
  RDT(b157_21, bb2, 55296);
  MF(g2_12, b156_20, b156_21, ha8_1);
  DMA(r2, vw2, s_23, 720896, 32768);
  WLG(4);
  MF(g2_03, b156_30, b156_31, ha8_0);
  RDT(b157_30, bb3, 53248);
  RDT(b157_31, bb3, 55296);
  MF(g2_13, b156_30, b156_31, ha8_1);
  MF(g2_00, b157_00, b157_01, ha9_0);
  PRIO(2);
  RDT(b158_00, bb0, 57344);
  RDT(b158_01, bb0, 59392);
  MF(g2_10, b157_00, b157_01, ha9_1);
  WLG(4);
  MF(g2_01, b157_10, b157_11, ha9_0);
  RDT(b158_10, bb1, 57344);
  RDT(b158_11, bb1, 59392);
  WVM(7);
  GSIG(4, sfo, sL + L_FH);
  MF(g2_11, b157_10, b157_11, ha9_1);
  MF(g2_02, b157_20, b157_21, ha9_0);
  RDT(b158_20, bb2, 57344);
  RDT(b158_21, bb2, 59392);
  VW(vwqn, 6144);
  MF(g2_12, b157_20, b157_21, ha9_1);
  WLG(4);
  MF(g2_03, b157_30, b157_31, ha9_0);
  RDT(b158_30, bb3, 57344);
  RDT(b158_31, bb3, 59392);
  MF(g2_13, b157_30, b157_31, ha9_1);
  MF(g2_00, b158_00, b158_01, ha10_0);
  PRIO(1);
  BAR(); STB(42);
  PRIO(3);
  RDT(b159_00, bb0, 45056);
  RDT(b159_01, bb0, 47104);
  DMA(r2, vw2, s_23, 393216, 36864);
  MF(g2_10, b158_00, b158_01, ha10_1);
  WLG(4);
  MF(g2_01, b158_10, b158_11, ha10_0);
  RDT(b159_10, bb1, 45056);
  RDT(b159_11, bb1, 47104);
  MF(g2_11, b158_10, b158_11, ha10_1);
  MF(g2_02, b158_20, b158_21, ha10_0);
  RDT(b159_20, bb2, 45056);
  RDT(b159_21, bb2, 47104);
  DMA(r2, vw2, s_23, 458752, 40960);
  MF(g2_12, b158_20, b158_21, ha10_1);
  WLG(4);
  MF(g2_03, b158_30, b158_31, ha10_0);
  RDT(b159_30, bb3, 45056);
  RDT(b159_31, bb3, 47104);
  MF(g2_13, b158_30, b158_31, ha10_1);
  MF(g2_00, b159_00, b159_01, ha11_0);
  RDT(b160_00, bb0, 49152);
  RDT(b160_01, bb0, 51200);
  DMA(r2, vw2, s_23, 262144, 53248);
  MF(g2_10, b159_00, b159_01, ha11_1);
  RDT(b161_00, bb0, 0);
  RDT(b161_01, bb0, 2048);
  WLG(6);
  MF(g2_01, b159_10, b159_11, ha11_0);
  RDT(b160_10, bb1, 49152);
  RDT(b160_11, bb1, 51200);
  MF(g2_11, b159_10, b159_11, ha11_1);
  RDT(b161_10, bb1, 0);
  RDT(b161_11, bb1, 2048);
  MF(g2_02, b159_20, b159_21, ha11_0);
  RDT(b160_20, bb2, 49152);
  RDT(b160_21, bb2, 51200);
  MF(g2_12, b159_20, b159_21, ha11_1);
  RDT(b161_20, bb2, 0);
  RDT(b161_21, bb2, 2048);
  WLG(8);
  MF(g2_03, b159_30, b159_31, ha11_0);
  RDT(b160_30, bb3, 49152);
  RDT(b160_31, bb3, 51200);
  MF(g2_13, b159_30, b159_31, ha11_1);
  RDT(b161_30, bb3, 0);
  RDT(b161_31, bb3, 2048);
  MF(g2_10, b160_00, b160_01, ha12_1);
  PRIO(2);
  MF(g2_10, b161_00, b161_01, ha13_1);
  WLG(4);
  MF(g2_11, b160_10, b160_11, ha12_1);
  MF(g2_11, b161_10, b161_11, ha13_1);
  MF(g2_12, b160_20, b160_21, ha12_1);
  NOP(3);
  unsigned g2_s00, g2_s01; CVT(g2_s00, g2_10[0], g2_10[1]); CVT(g2_s01, g2_10[2], g2_10[3]);
  MF(g2_12, b161_20, b161_21, ha13_1);
  WLG(0);
  MF(g2_13, b160_30, b160_31, ha12_1);
  NOP(3);
  unsigned g2_s10, g2_s11; CVT(g2_s10, g2_11[0], g2_11[1]); CVT(g2_s11, g2_11[2], g2_11[3]);
  WR(((u32x4){g2_s00, g2_s01, g2_s10, g2_s11}), xw, 1024);
  MF(g2_13, b161_30, b161_31, ha13_1);
  MF(g2_00, b160_00, b160_01, ha12_0);
  PRIO(1);
  NOP(1);
  unsigned g2_s20, g2_s21; CVT(g2_s20, g2_12[0], g2_12[1]); CVT(g2_s21, g2_12[2], g2_12[3]);
  MF(g2_01, b160_10, b160_11, ha12_0);
  MF(g2_02, b160_20, b160_21, ha12_0);
  NOP(3);
  unsigned g2_s30, g2_s31; CVT(g2_s30, g2_13[0], g2_13[1]); CVT(g2_s31, g2_13[2], g2_13[3]);
  WR(((u32x4){g2_s20, g2_s21, g2_s30, g2_s31}), xw, 3072);
  MF(g2_03, b160_30, b160_31, ha12_0);
  WVM(8);
  WLG(0);
  BAR(); STB(43);
  PRIO(3);
  RDT(b162_00, bb0, 4096);
  RDT(b162_01, bb0, 6144);
  DMA(r2, vw2, s_23, 327680, 57344);
  MF(g2_00, b161_00, b161_01, ha13_0);
  RDT(b162_10, bb1, 4096);
  RDT(b162_11, bb1, 6144);
  MF(g2_01, b161_10, b161_11, ha13_0);
  RDT(b162_20, bb2, 4096);
  RDT(b162_21, bb2, 6144);
  MF(g2_02, b161_20, b161_21, ha13_0);
  RDT(b162_30, bb3, 4096);
  RDT(b162_31, bb3, 6144);
  MF(g2_03, b161_30, b161_31, ha13_0);
  DMA(r2, vw2, s_23, 786432, 45056);
  WLG(4);
  MF0(g3_00, b162_00, b162_01, ha0_0);
  RDT(b163_00, bb0, 8192);
  RDT(b163_01, bb0, 10240);
  RDF(g2_r1, xr, 1024);
  RDF(g2_r3, xr, 3072);
  unsigned vfl2 = vfl * 8; PIN(vfl2);
  GLD1(fl3, vfl2, sfp, sL + L_FH);
  MF0(g3_10, b162_00, b162_01, ha0_1);
  MF0(g3_01, b162_10, b162_11, ha0_0);
  RDT(b163_10, bb1, 8192);
  RDT(b163_11, bb1, 10240);
  MF0(g3_11, b162_10, b162_11, ha0_1);
  DMA(r2, vw2, s_23, 851968, 49152);
  f32x2 g2_t00, g2_t01;
  WLG(3);
  { float u_; ULO(u_, g2_r1[0]); g2_t00[0] = u_; }
  { float u_; UHI(u_, g2_r1[0]); g2_t00[1] = u_; }
  MF0(g3_02, b162_20, b162_21, ha0_0);
  RDT(b163_20, bb2, 8192);
  RDT(b163_21, bb2, 10240);
  { float u_; ULO(u_, g2_r1[1]); g2_t01[0] = u_; }
  { float u_; UHI(u_, g2_r1[1]); g2_t01[1] = u_; }
  MF0(g3_12, b162_20, b162_21, ha0_1);
  ADD(g2_t00[0], g2_t00[0], g2_00[0]);
  ADD(g2_t00[1], g2_t00[1], g2_00[1]);
  MF0(g3_03, b162_30, b162_31, ha0_0);
  RDT(b163_30, bb3, 8192);
  RDT(b163_31, bb3, 10240);
  ADD(g2_t01[0], g2_t01[0], g2_00[2]);
  ADD(g2_t01[1], g2_t01[1], g2_00[3]);
  MF0(g3_13, b162_30, b162_31, ha0_1);
  unsigned g2_p00; CVT(g2_p00, g2_t00[0], g2_t00[1]);
  unsigned g2_p01; CVT(g2_p01, g2_t01[0], g2_t01[1]);
  MF(g3_00, b163_00, b163_01, ha1_0);
  RDT(b164_00, bb0, 12288);
  RDT(b164_01, bb0, 14336);
  DMA(rQ, vwqn, s_q, 2048, 0);
  CLP(g2_p00);
  CLP(g2_p01);
  MF(g3_10, b163_00, b163_01, ha1_1);
  f32x2 g2_t10, g2_t11;
  { float u_; ULO(u_, g2_r1[2]); g2_t10[0] = u_; }
  { float u_; UHI(u_, g2_r1[2]); g2_t10[1] = u_; }
  WLG(4);
  MF(g3_01, b163_10, b163_11, ha1_0);
  RDT(b164_10, bb1, 12288);
  RDT(b164_11, bb1, 14336);
  { float u_; ULO(u_, g2_r1[3]); g2_t11[0] = u_; }
  { float u_; UHI(u_, g2_r1[3]); g2_t11[1] = u_; }
  MF(g3_11, b163_10, b163_11, ha1_1);
  PRIO(2);
  ADD(g2_t10[0], g2_t10[0], g2_01[0]);
  ADD(g2_t10[1], g2_t10[1], g2_01[1]);
  MF(g3_02, b163_20, b163_21, ha1_0);
  RDT(b164_20, bb2, 12288);
  RDT(b164_21, bb2, 14336);
  ADD(g2_t11[0], g2_t11[0], g2_01[2]);
  ADD(g2_t11[1], g2_t11[1], g2_01[3]);
  MF(g3_12, b163_20, b163_21, ha1_1);
  unsigned g2_p10; CVT(g2_p10, g2_t10[0], g2_t10[1]);
  unsigned g2_p11; CVT(g2_p11, g2_t11[0], g2_t11[1]);
  WLG(4);
  MF(g3_03, b163_30, b163_31, ha1_0);
  RDT(b164_30, bb3, 12288);
  RDT(b164_31, bb3, 14336);
  CLP(g2_p10);
  CLP(g2_p11);
  MF(g3_13, b163_30, b163_31, ha1_1);
  const u32x4 g2_o0 = {g2_p00, g2_p01, g2_p10, g2_p11};
  GST(g2_o0, v16, sp2, sL + L_P + 0);
  f32x2 g2_t20, g2_t21;
  { float u_; ULO(u_, g2_r3[0]); g2_t20[0] = u_; }
  MF(g3_00, b164_00, b164_01, ha2_0);
  RDT(b165_00, bb0, 16384);
  RDT(b165_01, bb0, 18432);
  { float u_; UHI(u_, g2_r3[0]); g2_t20[1] = u_; }
  { float u_; ULO(u_, g2_r3[1]); g2_t21[0] = u_; }
  MF(g3_10, b164_00, b164_01, ha2_1);
  { float u_; UHI(u_, g2_r3[1]); g2_t21[1] = u_; }
  ADD(g2_t20[0], g2_t20[0], g2_02[0]);
  WLG(4);
  MF(g3_01, b164_10, b164_11, ha2_0);
  RDT(b165_10, bb1, 16384);
  RDT(b165_11, bb1, 18432);
  ADD(g2_t20[1], g2_t20[1], g2_02[1]);
  ADD(g2_t21[0], g2_t21[0], g2_02[2]);
  MF(g3_11, b164_10, b164_11, ha2_1);
  PRIO(1);
  ADD(g2_t21[1], g2_t21[1], g2_02[3]);
  unsigned g2_p20; CVT(g2_p20, g2_t20[0], g2_t20[1]);
  MF(g3_02, b164_20, b164_21, ha2_0);
  RDT(b165_20, bb2, 16384);
  RDT(b165_21, bb2, 18432);
  unsigned g2_p21; CVT(g2_p21, g2_t21[0], g2_t21[1]);
  CLP(g2_p20);
  MF(g3_12, b164_20, b164_21, ha2_1);
  GLD(ry0, v16, sx2, sL + L_X1 + 0);
  CLP(g2_p21);
  f32x2 g2_t30, g2_t31;
  { float u_; ULO(u_, g2_r3[2]); g2_t30[0] = u_; }
  WLG(4);
  MF(g3_03, b164_30, b164_31, ha2_0);
  RDT(b165_30, bb3, 16384);
  RDT(b165_31, bb3, 18432);
  { float u_; UHI(u_, g2_r3[2]); g2_t30[1] = u_; }
  { float u_; ULO(u_, g2_r3[3]); g2_t31[0] = u_; }
  MF(g3_13, b164_30, b164_31, ha2_1);
  { float u_; UHI(u_, g2_r3[3]); g2_t31[1] = u_; }
  ADD(g2_t30[0], g2_t30[0], g2_03[0]);
  MF(g3_00, b165_00, b165_01, ha3_0);
  PRIO(0);
  WVM(11);
  BAR(); STB(44);
  PRIO(3);
  RDT(b166_00, bb0, 20480);
  RDT(b166_01, bb0, 22528);
  ADD(g2_t30[1], g2_t30[1], g2_03[1]);
  ADD(g2_t31[0], g2_t31[0], g2_03[2]);
  MF(g3_10, b165_00, b165_01, ha3_1);
  GLD(ry1, v16, sx2, sL + L_X1 + 1024);
  ADD(g2_t31[1], g2_t31[1], g2_03[3]);
  unsigned g2_p30; CVT(g2_p30, g2_t30[0], g2_t30[1]);
  WLG(4);
  MF(g3_01, b165_10, b165_11, ha3_0);
  RDT(b166_10, bb1, 20480);
  RDT(b166_11, bb1, 22528);
  unsigned g2_p31; CVT(g2_p31, g2_t31[0], g2_t31[1]);
  CLP(g2_p30);
  MF(g3_11, b165_10, b165_11, ha3_1);
  CLP(g2_p31);
  const u32x4 g2_o1 = {g2_p20, g2_p21, g2_p30, g2_p31};
  GST(g2_o1, v16, sp2, sL + L_P + 1024);
  MF(g3_02, b165_20, b165_21, ha3_0);
  RDT(b166_20, bb2, 20480);
  RDT(b166_21, bb2, 22528);
  MF(g3_12, b165_20, b165_21, ha3_1);
  WLG(4);
  MF(g3_03, b165_30, b165_31, ha3_0);
  RDT(b166_30, bb3, 20480);
  RDT(b166_31, bb3, 22528);
  MF(g3_13, b165_30, b165_31, ha3_1);
  MF(g3_00, b166_00, b166_01, ha4_0);
  RDT(b167_00, bb0, 24576);
  RDT(b167_01, bb0, 26624);
  DMA(rQ, vwqn, s_q, 4096, 4096);
  MF(g3_10, b166_00, b166_01, ha4_1);
  WLG(4);
  MF(g3_01, b166_10, b166_11, ha4_0);
  RDT(b167_10, bb1, 24576);
  RDT(b167_11, bb1, 26624);
  MF(g3_11, b166_10, b166_11, ha4_1);
  MF(g3_02, b166_20, b166_21, ha4_0);
  RDT(b167_20, bb2, 24576);
  RDT(b167_21, bb2, 26624);
  MF(g3_12, b166_20, b166_21, ha4_1);
  DMA(rQ, vwqn, s_q, 198656, 8192);
  WLG(4);
  MF(g3_03, b166_30, b166_31, ha4_0);
  RDT(b167_30, bb3, 24576);
  RDT(b167_31, bb3, 26624);
  WVM(8);
  CHKF(fl3, 4u, vfl2, sfp, sL + L_FH);
  MF(g3_13, b166_30, b166_31, ha4_1);
  MF(g3_00, b167_00, b167_01, ha5_0);
  PRIO(2);
  RDT(b168_00, bb0, 28672);
  RDT(b168_01, bb0, 30720);
  MF(g3_10, b167_00, b167_01, ha5_1);
  GLD(pi00, v16, spi, sL + L_P + 0);
  WLG(4);
  MF(g3_01, b167_10, b167_11, ha5_0);
  RDT(b168_10, bb1, 28672);
  RDT(b168_11, bb1, 30720);
  MF(g3_11, b167_10, b167_11, ha5_1);
  MF(g3_02, b167_20, b167_21, ha5_0);
  RDT(b168_20, bb2, 28672);
  RDT(b168_21, bb2, 30720);
  MF(g3_12, b167_20, b167_21, ha5_1);
  WLG(4);
  MF(g3_03, b167_30, b167_31, ha5_0);
  RDT(b168_30, bb3, 28672);
  RDT(b168_31, bb3, 30720);
  GLD(pi01, v16, spi, sL + L_P + 1024);
  MF(g3_13, b167_30, b167_31, ha5_1);
  MF(g3_00, b168_00, b168_01, ha6_0);
  PRIO(1);
  RDT(b169_00, bb0, 32768);
  RDT(b169_01, bb0, 34816);
  MF(g3_10, b168_00, b168_01, ha6_1);
  WLG(4);
  MF(g3_01, b168_10, b168_11, ha6_0);
  RDT(b169_10, bb1, 32768);
  RDT(b169_11, bb1, 34816);
  GLD(pi10, v16, spi, sL + L_P + 16384);
  MF(g3_11, b168_10, b168_11, ha6_1);
  MF(g3_02, b168_20, b168_21, ha6_0);
  RDT(b169_20, bb2, 32768);
  RDT(b169_21, bb2, 34816);
  MF(g3_12, b168_20, b168_21, ha6_1);
  PRIO(0);
  WLG(4);
  MF(g3_03, b168_30, b168_31, ha6_0);
  RDT(b169_30, bb3, 32768);
  RDT(b169_31, bb3, 34816);
  GLD(pi11, v16, spi, sL + L_P + 17408);
  MF(g3_13, b168_30, b168_31, ha6_1);
  MF(g3_00, b169_00, b169_01, ha7_0);
  BAR(); STB(45);
  PRIO(3);
  RDT(b170_00, bb0, 36864);
  RDT(b170_01, bb0, 38912);
  MF(g3_10, b169_00, b169_01, ha7_1);
  WLG(4);
  MF(g3_01, b169_10, b169_11, ha7_0);
  RDT(b170_10, bb1, 36864);
  RDT(b170_11, bb1, 38912);
  MF(g3_11, b169_10, b169_11, ha7_1);
  DMA(rQ, vwqn, s_q, 200704, 12288);
  MF(g3_02, b169_20, b169_21, ha7_0);
  RDT(b170_20, bb2, 36864);
  RDT(b170_21, bb2, 38912);
  MF(g3_12, b169_20, b169_21, ha7_1);
  WLG(4);
  MF(g3_03, b169_30, b169_31, ha7_0);
  RDT(b170_30, bb3, 36864);
  RDT(b170_31, bb3, 38912);
  MF(g3_13, b169_30, b169_31, ha7_1);
  DMA(rQ, vwqn, s_q, 395264, 16384);
  MF(g3_00, b170_00, b170_01, ha8_0);
  RDT(b171_00, bb0, 40960);
  RDT(b171_01, bb0, 43008);
  MF(g3_10, b170_00, b170_01, ha8_1);
  WLG(4);
  MF(g3_01, b170_10, b170_11, ha8_0);
  RDT(b171_10, bb1, 40960);
  RDT(b171_11, bb1, 43008);
  MF(g3_11, b170_10, b170_11, ha8_1);
  MF(g3_02, b170_20, b170_21, ha8_0);
  RDT(b171_20, bb2, 40960);
  RDT(b171_21, bb2, 43008);
  DMA(rQ, vwqn, s_q, 397312, 20480);
  MF(g3_12, b170_20, b170_21, ha8_1);
  WLG(4);
  MF(g3_03, b170_30, b170_31, ha8_0);
  RDT(b171_30, bb3, 40960);
  RDT(b171_31, bb3, 43008);
  MF(g3_13, b170_30, b170_31, ha8_1);
  MF(g3_00, b171_00, b171_01, ha9_0);
  PRIO(2);
  RDT(b172_00, bb0, 53248);
  RDT(b172_01, bb0, 55296);
  DMA(rQ, vwqn, s_q, 591872, 24576);
  RDF(g3_b0, tb, 3712);
  RDF(g3_b1, tb, 3776);
  RDF(g3_b2, tb, 3840);
  RDF(g3_b3, tb, 3904);
  MF(g3_10, b171_00, b171_01, ha9_1);
  WLG(8);
  MF(g3_01, b171_10, b171_11, ha9_0);
  RDT(b172_10, bb1, 53248);
  RDT(b172_11, bb1, 55296);
  MF(g3_11, b171_10, b171_11, ha9_1);
  MF(g3_02, b171_20, b171_21, ha9_0);
  RDT(b172_20, bb2, 53248);
  RDT(b172_21, bb2, 55296);
  DMA(rQ, vwqn, s_q, 593920, 28672);
  MF(g3_12, b171_20, b171_21, ha9_1);
  WLG(8);
  MF(g3_03, b171_30, b171_31, ha9_0);
  RDT(b172_30, bb3, 53248);
  RDT(b172_31, bb3, 55296);
  MF(g3_13, b171_30, b171_31, ha9_1);
  MF(g3_00, b172_00, b172_01, ha10_0);
  PRIO(1);
  WVM(16);
  BAR(); STB(46);
  PRIO(3);
  RDT(b173_00, bb0, 57344);
  RDT(b173_01, bb0, 59392);
  MF(g3_10, b172_00, b172_01, ha10_1);
  DMA(rQ, vwqn, s_q, 788480, 32768);
  WLG(4);
  MF(g3_01, b172_10, b172_11, ha10_0);
  RDT(b173_10, bb1, 57344);
  RDT(b173_11, bb1, 59392);
  MF(g3_11, b172_10, b172_11, ha10_1);
  MF(g3_02, b172_20, b172_21, ha10_0);
  RDT(b173_20, bb2, 57344);
  RDT(b173_21, bb2, 59392);
  MF(g3_12, b172_20, b172_21, ha10_1);
  GLD(pi20, v16, spi, sL + L_P + 32768);
  WVM(7);
  PIN(pi00); PIN(pi01); PIN(pi10); PIN(pi11);
  WLG(4);
  MF(g3_03, b172_30, b172_31, ha10_0);
  RDT(b173_30, bb3, 57344);
  RDT(b173_31, bb3, 59392);
  PIN(g3_b0); f32x4 g3_u0 = ((f32x4){lo2f(pi00[0]), hi2f(pi00[0]), lo2f(pi00[1]), hi2f(pi00[1])} + (f32x4){lo2f(pi10[0]), hi2f(pi10[0]), lo2f(pi10[1]), hi2f(pi10[1])}) + g3_b0; PIN(g3_u0);
  MF(g3_13, b172_30, b172_31, ha10_1);
  MF(g3_00, b173_00, b173_01, ha11_0);
  RDT(b174_00, bb0, 45056);
  RDT(b174_01, bb0, 47104);
  MF(g3_10, b173_00, b173_01, ha11_1);
  RDT(b175_00, bb0, 49152);
  RDT(b175_01, bb0, 51200);
  PIN(g3_b1); f32x4 g3_u1 = ((f32x4){lo2f(pi00[2]), hi2f(pi00[2]), lo2f(pi00[3]), hi2f(pi00[3])} + (f32x4){lo2f(pi10[2]), hi2f(pi10[2]), lo2f(pi10[3]), hi2f(pi10[3])}) + g3_b1; PIN(g3_u1);
  WLG(6);
  MF(g3_01, b173_10, b173_11, ha11_0);
  RDT(b174_10, bb1, 45056);
  RDT(b174_11, bb1, 47104);
  GLD(pi21, v16, spi, sL + L_P + 33792);
  MF(g3_11, b173_10, b173_11, ha11_1);
  RDT(b175_10, bb1, 49152);
  RDT(b175_11, bb1, 51200);
  MF(g3_02, b173_20, b173_21, ha11_0);
  RDT(b174_20, bb2, 45056);
  RDT(b174_21, bb2, 47104);
  PIN(g3_b2); f32x4 g3_u2 = ((f32x4){lo2f(pi01[0]), hi2f(pi01[0]), lo2f(pi01[1]), hi2f(pi01[1])} + (f32x4){lo2f(pi11[0]), hi2f(pi11[0]), lo2f(pi11[1]), hi2f(pi11[1])}) + g3_b2; PIN(g3_u2);
  MF(g3_12, b173_20, b173_21, ha11_1);
  RDT(b175_20, bb2, 49152);
  RDT(b175_21, bb2, 51200);
  WLG(8);
  MF(g3_03, b173_30, b173_31, ha11_0);
  RDT(b174_30, bb3, 45056);
  RDT(b174_31, bb3, 47104);
  DMA(rQ, vwqn, s_q, 790528, 36864);
  MF(g3_13, b173_30, b173_31, ha11_1);
  RDT(b175_30, bb3, 49152);
  RDT(b175_31, bb3, 51200);
  PIN(g3_b3); f32x4 g3_u3 = ((f32x4){lo2f(pi01[2]), hi2f(pi01[2]), lo2f(pi01[3]), hi2f(pi01[3])} + (f32x4){lo2f(pi11[2]), hi2f(pi11[2]), lo2f(pi11[3]), hi2f(pi11[3])}) + g3_b3; PIN(g3_u3);
  MF(g3_10, b174_00, b174_01, ha12_1);
  PRIO(2);
  MF(g3_10, b175_00, b175_01, ha13_1);
  WLG(4);
  MF(g3_11, b174_10, b174_11, ha12_1);
  DMA(rQ, vwqn, s_q, 985088, 40960);
  MF(g3_11, b175_10, b175_11, ha13_1);
  MF(g3_12, b174_20, b174_21, ha12_1);
  NOP(2);
  WR(g3_10, xw, 0);
  MF(g3_12, b175_20, b175_21, ha13_1);
  WLG(1);
  MF(g3_13, b174_30, b174_31, ha12_1);
  NOP(2);
  WR(g3_11, xw, 1024);
  MF(g3_13, b175_30, b175_31, ha13_1);
  MF(g3_00, b174_00, b174_01, ha12_0);
  PRIO(1);
  NOP(1);
  WR(g3_12, xw, 2048);
  MF(g3_01, b174_10, b174_11, ha12_0);
  MF(g3_02, b174_20, b174_21, ha12_0);
  NOP(2);
  WR(g3_13, xw, 3072);
  MF(g3_03, b174_30, b174_31, ha12_0);
  MF(g3_00, b175_00, b175_01, ha13_0);
  MF(g3_01, b175_10, b175_11, ha13_0);
  PRIO(0);
  MF(g3_02, b175_20, b175_21, ha13_0);
  MF(g3_03, b175_30, b175_31, ha13_0);
  PRIO(0);
  STP(10);
  WLG(0);
  BAR(); STB(47);
  RDF(g3_r0, xr, 0);
  RDF(g3_r1, xr, 1024);
  RDF(g3_r2, xr, 2048);
  RDF(g3_r3, xr, 3072);
  WVM(2);
  PIN(ry0); PIN(ry1); PIN(pi20); PIN(pi21);
  { unsigned t_ = mx8(pi20, pi21); CHK2(t_, pi20, pi21, v16, spi, sL + L_P + 32768, spi, sL + L_P + 33792, 3); }
  WLG(0);
  PIN(g3_r0); PIN(g3_00);
  PIN(g3_r1); PIN(g3_01);
  PIN(g3_r2); PIN(g3_02);
  PIN(g3_r3); PIN(g3_03);
  const f32x4 g3_t0 = (g3_00 + g3_r0) + ((f32x4){lo2f(pi20[0]), hi2f(pi20[0]), lo2f(pi20[1]), hi2f(pi20[1])} + g3_u0);
  const f32x4 g3_t1 = (g3_01 + g3_r1) + ((f32x4){lo2f(pi20[2]), hi2f(pi20[2]), lo2f(pi20[3]), hi2f(pi20[3])} + g3_u1);
  const f32x4 g3_t2 = (g3_02 + g3_r2) + ((f32x4){lo2f(pi21[0]), hi2f(pi21[0]), lo2f(pi21[1]), hi2f(pi21[1])} + g3_u2);
  const f32x4 g3_t3 = (g3_03 + g3_r3) + ((f32x4){lo2f(pi21[2]), hi2f(pi21[2]), lo2f(pi21[3]), hi2f(pi21[3])} + g3_u3);
  const u32x4 z0 = resid_add(g3_t0, g3_t1, ry0), z1 = resid_add(g3_t2, g3_t3, ry1);
  GST(z0, v16, sx2, sL + LSTR + L_XIN + 0);
  GST(z1, v16, sx2, sL + LSTR + L_XIN + 1024);
  { float s_ = 0.f, ss_ = 0.f; row_stats(z0, s_, ss_); row_stats(z1, s_, ss_);
  const u32x2 sv_ = {__float_as_uint(rows_sum(s_)), __float_as_uint(rows_sum(ss_))};
  V8(v8_); GST2(sv_, v8_, st2, sL + LSTR + L_STIN); }
  STP(11);
  RDN(b176_00, bb0, 0);
  RDN(b176_01, bb0, 2048);
  RDN(b176_10, bb1, 0);
  RDN(b176_11, bb1, 2048);
  RDN(b176_20, bb2, 0);
  RDN(b176_21, bb2, 2048);
  RDN(b176_30, bb3, 0);
  RDN(b176_31, bb3, 2048);
  RDN(b177_00, bb0, 4096);
  RDN(b177_01, bb0, 6144);
  RDN(b177_10, bb1, 4096);
  RDN(b177_11, bb1, 6144);
  RDN(b177_20, bb2, 4096);
  RDN(b177_21, bb2, 6144);
  RDN(b177_30, bb3, 4096);
  RDN(b177_31, bb3, 6144);
  RDN(b178_00, bb0, 8192);
  RDN(b178_01, bb0, 10240);
  RDN(b178_10, bb1, 8192);
  RDN(b178_11, bb1, 10240);
  RDN(b178_20, bb2, 8192);
  RDN(b178_21, bb2, 10240);
  RDN(b178_30, bb3, 8192);
  RDN(b178_31, bb3, 10240);
  WLG(0);
  WVM(0);
  BAR(); STB(48);
  ARRIVE(L_CN);
  if (tile0 == 0 && wid0 == 1) {
    LANE(l_); bool ok = false;
    _Pragma("nounroll") for (int n_ = 0; n_ < SPIN_CAP && !ok; ++n_) {
      bool bad = false;
      _Pragma("unroll") for (int i = 0; i < 4; ++i) { const u32x2 f = __builtin_amdgcn_raw_buffer_load_b64(rS, l_ * 8, WS_IF + i * 512, 16); bad = bad || f[0] != tag0 || f[1] != tag1; }
      ok = !ANY(bad);
    }
    const unsigned se_ = WS_EP, vz_ = 0; GTAG(vz_, se_);
  }
  DMA(rQ, vwqn, s_q, 987136, 53248);
  DMA(rQ, vwqn, s_q, 1181696, 57344);
  DMA(rQ, vwqn, s_q, 1183744, 45056);
  DMA(rQ, vwqn, s_q, 1378304, 49152);
  DMA(rQ, vwqn, s_q, 1380352, 0);
  DMA(rQ, vwqn, s_q, 1574912, 4096);
  DMA(rQ, vwqn, s_q, 1771520, 8192);
  RDN(b179_00, bb0, 12288);
  RDN(b179_01, bb0, 14336);
  RDN(b179_10, bb1, 12288);
  RDN(b179_11, bb1, 14336);
  RDN(b179_20, bb2, 12288);
  RDN(b179_21, bb2, 14336);
  RDN(b179_30, bb3, 12288);
  RDN(b179_31, bb3, 14336);
  RDN(b180_00, bb0, 16384);
  RDN(b180_01, bb0, 18432);
  RDN(b180_10, bb1, 16384);
  RDN(b180_11, bb1, 18432);
  RDN(b180_20, bb2, 16384);
  RDN(b180_21, bb2, 18432);
  RDN(b180_30, bb3, 16384);
  RDN(b180_31, bb3, 18432);
  RDN(b181_00, bb0, 20480);
  RDN(b181_01, bb0, 22528);
  RDN(b181_10, bb1, 20480);
  RDN(b181_11, bb1, 22528);
  RDN(b181_20, bb2, 20480);
  RDN(b181_21, bb2, 22528);
  RDN(b181_30, bb3, 20480);
  RDN(b181_31, bb3, 22528);
  WLG(0);
  MEET(L_CN);
  STP(19);
  BAR(); STB(49);
  STR(31);
  }
  {
  constexpr int layer = 1; constexpr unsigned sL = WS_L0 + layer * LSTR; (void)layer;
  STR(30); STP(0);
  VWP(vwq, 6144, 0);
  STP(1);
  PRIO(3);
  GLD(qa0_0, v16, sa0, sL + L_XIN + 0);
  GLD(qa0_1, v16, sa1, sL + L_XIN + 0);
  PRIO(2);
  GLD(qa1_0, v16, sa0, sL + L_XIN + 1024);
  GLD(qa1_1, v16, sa1, sL + L_XIN + 1024);
  PRIO(1);
  GLD(qa2_0, v16, sa0, sL + L_XIN + 2048);
  GLD(qa2_1, v16, sa1, sL + L_XIN + 2048);
  PRIO(0);
  GLD(qa3_0, v16, sa0, sL + L_XIN + 3072);
  GLD(qa3_1, v16, sa1, sL + L_XIN + 3072);
  PRIO(3);
  WVM(6);
  MF0(ck_00, b176_00, b176_01, qa0_0);
  GLD(qa4_0, v16, sa0, sL + L_XIN + 4096);
  MF0(ck_10, b176_00, b176_01, qa0_1);
  MF0(ck_01, b176_10, b176_11, qa0_0);
  MF0(ck_11, b176_10, b176_11, qa0_1);
  MF0(ck_02, b176_20, b176_21, qa0_0);
  MF0(ck_12, b176_20, b176_21, qa0_1);
  GLD(qa4_1, v16, sa1, sL + L_XIN + 4096);
  MF0(ck_03, b176_30, b176_31, qa0_0);
  MF0(ck_13, b176_30, b176_31, qa0_1);
  STP(23);
  MF0(cv_00, b177_00, b177_01, qa0_0);
  MF0(cv_10, b177_00, b177_01, qa0_1);
  MF0(cv_01, b177_10, b177_11, qa0_0);
  GLD(qa5_0, v16, sa0, sL + L_XIN + 5120);
  MF0(cv_11, b177_10, b177_11, qa0_1);
  MF0(cv_02, b177_20, b177_21, qa0_0);
  MF0(cv_12, b177_20, b177_21, qa0_1);
  MF0(cv_03, b177_30, b177_31, qa0_0);
  GLD(qa5_1, v16, sa1, sL + L_XIN + 5120);
  WVM(8);
  MF0(cv_13, b177_30, b177_31, qa0_1);
  PRIO(2);
  MF(ck_00, b178_00, b178_01, qa1_0);
  MF(ck_10, b178_00, b178_01, qa1_1);
  MF(ck_01, b178_10, b178_11, qa1_0);
  MF(ck_11, b178_10, b178_11, qa1_1);
  GLD(qa6_0, v16, sa0, sL + L_XIN + 6144);
  MF(ck_02, b178_20, b178_21, qa1_0);
  MF(ck_12, b178_20, b178_21, qa1_1);
  MF(ck_03, b178_30, b178_31, qa1_0);
  MF(ck_13, b178_30, b178_31, qa1_1);
  PRIO(1);
  GLD(qa6_1, v16, sa1, sL + L_XIN + 6144);
  MF(cv_00, b179_00, b179_01, qa1_0);
  MF(cv_10, b179_00, b179_01, qa1_1);
  MF(cv_01, b179_10, b179_11, qa1_0);
  MF(cv_11, b179_10, b179_11, qa1_1);
  MF(cv_02, b179_20, b179_21, qa1_0);
  PRIO(0);
  GLD(qa7_0, v16, sa0, sL + L_XIN + 7168);
  MF(cv_12, b179_20, b179_21, qa1_1);
  MF(cv_03, b179_30, b179_31, qa1_0);
  WVM(9);
  MF(cv_13, b179_30, b179_31, qa1_1);
  MF(ck_00, b180_00, b180_01, qa2_0);
  MF(ck_10, b180_00, b180_01, qa2_1);
  GLD(qa7_1, v16, sa1, sL + L_XIN + 7168);
  MF(ck_01, b180_10, b180_11, qa2_0);
  MF(ck_11, b180_10, b180_11, qa2_1);
  MF(ck_02, b180_20, b180_21, qa2_0);
  MF(ck_12, b180_20, b180_21, qa2_1);
  DMA(rQ, vwq, s_q, 1968128, 12288);
  MF(ck_03, b180_30, b180_31, qa2_0);
  MF(ck_13, b180_30, b180_31, qa2_1);
  MF(cv_00, b181_00, b181_01, qa2_0);
  RDT(b182_00, bb0, 24576);
  RDT(b182_01, bb0, 26624);
  MF(cv_10, b181_00, b181_01, qa2_1);
  MF(cv_01, b181_10, b181_11, qa2_0);
  RDT(b182_10, bb1, 24576);
  RDT(b182_11, bb1, 26624);
  DMA(rQ, vwq, s_q, 2164736, 16384);
  MF(cv_11, b181_10, b181_11, qa2_1);
  MF(cv_02, b181_20, b181_21, qa2_0);
  RDT(b182_20, bb2, 24576);
  RDT(b182_21, bb2, 26624);
  MF(cv_12, b181_20, b181_21, qa2_1);
  MF(cv_03, b181_30, b181_31, qa2_0);
  RDT(b182_30, bb3, 24576);
  RDT(b182_31, bb3, 26624);
  WVM(10);
  MF(cv_13, b181_30, b181_31, qa2_1);
  GLD(qa8_0, v16, sa0, sL + L_XIN + 8192);
  WLG(4);
  MF(ck_00, b182_00, b182_01, qa3_0);
  RDT(b183_00, bb0, 28672);
  RDT(b183_01, bb0, 30720);
  MF(ck_10, b182_00, b182_01, qa3_1);
  MF(ck_01, b182_10, b182_11, qa3_0);
  RDT(b183_10, bb1, 28672);
  RDT(b183_11, bb1, 30720);
  MF(ck_11, b182_10, b182_11, qa3_1);
  GLD(qa8_1, v16, sa1, sL + L_XIN + 8192);
  WLG(4);
  MF(ck_02, b182_20, b182_21, qa3_0);
  RDT(b183_20, bb2, 28672);
  RDT(b183_21, bb2, 30720);
  MF(ck_12, b182_20, b182_21, qa3_1);
  MF(ck_03, b182_30, b182_31, qa3_0);
  RDT(b183_30, bb3, 28672);
  RDT(b183_31, bb3, 30720);
  MF(ck_13, b182_30, b182_31, qa3_1);
  WLG(4);
  MF(cv_00, b183_00, b183_01, qa3_0);
  BAR(); STB(1);
  PRIO(3);
  RDT(b184_00, bb0, 32768);
  RDT(b184_01, bb0, 34816);
  GLD(qa9_0, v16, sa0, sL + L_XIN + 9216);
  MF(cv_10, b183_00, b183_01, qa3_1);
  MF(cv_01, b183_10, b183_11, qa3_0);
  RDT(b184_10, bb1, 32768);
  RDT(b184_11, bb1, 34816);
  MF(cv_11, b183_10, b183_11, qa3_1);
  WLG(4);
  MF(cv_02, b183_20, b183_21, qa3_0);
  RDT(b184_20, bb2, 32768);
  RDT(b184_21, bb2, 34816);
  GLD(qa9_1, v16, sa1, sL + L_XIN + 9216);
  MF(cv_12, b183_20, b183_21, qa3_1);
  MF(cv_03, b183_30, b183_31, qa3_0);
  RDT(b184_30, bb3, 32768);
  RDT(b184_31, bb3, 34816);
  WVM(12);
  MF(cv_13, b183_30, b183_31, qa3_1);
  WLG(4);
  MF(ck_00, b184_00, b184_01, qa4_0);
  RDT(b185_00, bb0, 36864);
  RDT(b185_01, bb0, 38912);
  MF(ck_10, b184_00, b184_01, qa4_1);
  GLD(qa10_0, v16, sa0, sL + L_XIN + 10240);
  MF(ck_01, b184_10, b184_11, qa4_0);
  RDT(b185_10, bb1, 36864);
  RDT(b185_11, bb1, 38912);
  MF(ck_11, b184_10, b184_11, qa4_1);
  WLG(4);
  MF(ck_02, b184_20, b184_21, qa4_0);
  RDT(b185_20, bb2, 36864);
  RDT(b185_21, bb2, 38912);
  MF(ck_12, b184_20, b184_21, qa4_1);
  MF(ck_03, b184_30, b184_31, qa4_0);
  RDT(b185_30, bb3, 36864);
  RDT(b185_31, bb3, 38912);
  GLD(qa10_1, v16, sa1, sL + L_XIN + 10240);
  MF(ck_13, b184_30, b184_31, qa4_1);
  WLG(4);
  MF(cv_00, b185_00, b185_01, qa4_0);
  PRIO(2);
  RDT(b186_00, bb0, 40960);
  RDT(b186_01, bb0, 43008);
  MF(cv_10, b185_00, b185_01, qa4_1);
  MF(cv_01, b185_10, b185_11, qa4_0);
  RDT(b186_10, bb1, 40960);
  RDT(b186_11, bb1, 43008);
  GLD(qa11_0, v16, sa0, sL + L_XIN + 11264);
  MF(cv_11, b185_10, b185_11, qa4_1);
  WLG(4);
  MF(cv_02, b185_20, b185_21, qa4_0);
  RDT(b186_20, bb2, 40960);
  RDT(b186_21, bb2, 43008);
  MF(cv_12, b185_20, b185_21, qa4_1);
  MF(cv_03, b185_30, b185_31, qa4_0);
  RDT(b186_30, bb3, 40960);
  RDT(b186_31, bb3, 43008);
  WVM(13);
  MF(cv_13, b185_30, b185_31, qa4_1);
  GLD(qa11_1, v16, sa1, sL + L_XIN + 11264);
  WLG(4);
  MF(ck_00, b186_00, b186_01, qa5_0);
  PRIO(1);
  RDT(b187_00, bb0, 53248);
  RDT(b187_01, bb0, 55296);
  MF(ck_10, b186_00, b186_01, qa5_1);
  MF(ck_01, b186_10, b186_11, qa5_0);
  RDT(b187_10, bb1, 53248);
  RDT(b187_11, bb1, 55296);
  MF(ck_11, b186_10, b186_11, qa5_1);
  DMA(rQ, vwq, s_q, 2361344, 20480);
  WLG(4);
  MF(ck_02, b186_20, b186_21, qa5_0);
  RDT(b187_20, bb2, 53248);
  RDT(b187_21, bb2, 55296);
  MF(ck_12, b186_20, b186_21, qa5_1);
  PRIO(0);
  MF(ck_03, b186_30, b186_31, qa5_0);
  RDT(b187_30, bb3, 53248);
  RDT(b187_31, bb3, 55296);
  MF(ck_13, b186_30, b186_31, qa5_1);
  WLG(4);
  MF(cv_00, b187_00, b187_01, qa5_0);
  BAR(); STB(2);
  PRIO(3);
  RDT(b188_00, bb0, 57344);
  RDT(b188_01, bb0, 59392);
  DMA(rQ, vwq, s_q, 2557952, 24576);
  MF(cv_10, b187_00, b187_01, qa5_1);
  MF(cv_01, b187_10, b187_11, qa5_0);
  RDT(b188_10, bb1, 57344);
  RDT(b188_11, bb1, 59392);
  MF(cv_11, b187_10, b187_11, qa5_1);
  WLG(4);
  MF(cv_02, b187_20, b187_21, qa5_0);
  RDT(b188_20, bb2, 57344);
  RDT(b188_21, bb2, 59392);
  MF(cv_12, b187_20, b187_21, qa5_1);
  DMA(rQ, vwq, s_q, 2754560, 28672);
  MF(cv_03, b187_30, b187_31, qa5_0);
  RDT(b188_30, bb3, 57344);
  RDT(b188_31, bb3, 59392);
  WVM(15);
  MF(cv_13, b187_30, b187_31, qa5_1);
  WLG(4);
  MF(ck_00, b188_00, b188_01, qa6_0);
  RDT(b189_00, bb0, 45056);
  RDT(b189_01, bb0, 47104);
  MF(ck_10, b188_00, b188_01, qa6_1);
  DMA(rQ, vwq, s_q, 2951168, 32768);
  MF(ck_01, b188_10, b188_11, qa6_0);
  RDT(b189_10, bb1, 45056);
  RDT(b189_11, bb1, 47104);
  MF(ck_11, b188_10, b188_11, qa6_1);
  WLG(4);
  MF(ck_02, b188_20, b188_21, qa6_0);
  RDT(b189_20, bb2, 45056);
  RDT(b189_21, bb2, 47104);
  MF(ck_12, b188_20, b188_21, qa6_1);
  MF(ck_03, b188_30, b188_31, qa6_0);
  RDT(b189_30, bb3, 45056);
  RDT(b189_31, bb3, 47104);
  GLD(qa12_0, v16, sa0, sL + L_XIN + 12288);
  MF(ck_13, b188_30, b188_31, qa6_1);
  WLG(4);
  MF(cv_00, b189_00, b189_01, qa6_0);
  PRIO(2);
  RDT(b190_00, bb0, 49152);
  RDT(b190_01, bb0, 51200);
  MF(cv_10, b189_00, b189_01, qa6_1);
  MF(cv_01, b189_10, b189_11, qa6_0);
  RDT(b190_10, bb1, 49152);
  RDT(b190_11, bb1, 51200);
  GLD(qa12_1, v16, sa1, sL + L_XIN + 12288);
  MF(cv_11, b189_10, b189_11, qa6_1);
  WLG(4);
  MF(cv_02, b189_20, b189_21, qa6_0);
  RDT(b190_20, bb2, 49152);
  RDT(b190_21, bb2, 51200);
  MF(cv_12, b189_20, b189_21, qa6_1);
  MF(cv_03, b189_30, b189_31, qa6_0);
  RDT(b190_30, bb3, 49152);
  RDT(b190_31, bb3, 51200);
  WVM(16);
  MF(cv_13, b189_30, b189_31, qa6_1);
  GLD(qa13_0, v16, sa0, sL + L_XIN + 13312);
  WLG(4);
  MF(ck_00, b190_00, b190_01, qa7_0);
  PRIO(1);
  RDT(b191_00, bb0, 0);
  RDT(b191_01, bb0, 2048);
  MF(ck_10, b190_00, b190_01, qa7_1);
  MF(ck_01, b190_10, b190_11, qa7_0);
  RDT(b191_10, bb1, 0);
  RDT(b191_11, bb1, 2048);
  MF(ck_11, b190_10, b190_11, qa7_1);
  WLG(4);
  MF(ck_02, b190_20, b190_21, qa7_0);
  RDT(b191_20, bb2, 0);
  RDT(b191_21, bb2, 2048);
  GLD(qa13_1, v16, sa1, sL + L_XIN + 13312);
  MF(ck_12, b190_20, b190_21, qa7_1);
  PRIO(0);
  MF(ck_03, b190_30, b190_31, qa7_0);
  RDT(b191_30, bb3, 0);
  RDT(b191_31, bb3, 2048);
  MF(ck_13, b190_30, b190_31, qa7_1);
  WLG(4);
  MF(cv_00, b191_00, b191_01, qa7_0);
  WVM(16);
  BAR(); STB(3);
  PRIO(3);
  RDT(b192_00, bb0, 4096);
  RDT(b192_01, bb0, 6144);
  GLD(qa14_0, v16, sa0, sL + L_XIN + 14336);
  MF(cv_10, b191_00, b191_01, qa7_1);
  MF(cv_01, b191_10, b191_11, qa7_0);
  RDT(b192_10, bb1, 4096);
  RDT(b192_11, bb1, 6144);
  MF(cv_11, b191_10, b191_11, qa7_1);
  WLG(4);
  MF(cv_02, b191_20, b191_21, qa7_0);
  RDT(b192_20, bb2, 4096);
  RDT(b192_21, bb2, 6144);
  MF(cv_12, b191_20, b191_21, qa7_1);
  GLD(qa14_1, v16, sa1, sL + L_XIN + 14336);
  V8(v8q);
  MF(cv_03, b191_30, b191_31, qa7_0);
  RDT(b192_30, bb3, 4096);
  RDT(b192_31, bb3, 6144);
  WVM(16);
  MF(cv_13, b191_30, b191_31, qa7_1);
  STP(12);
  WLG(4);
  MF(ck_00, b192_00, b192_01, qa8_0);
  RDT(b193_00, bb0, 8192);
  RDT(b193_01, bb0, 10240);
  MF(ck_10, b192_00, b192_01, qa8_1);
  GL8(stq0, v8q, sst, sL + L_STIN + 0);
  GL8(stq1, v8q, sst, sL + L_STIN + 512);
  MF(ck_01, b192_10, b192_11, qa8_0);
  RDT(b193_10, bb1, 8192);
  RDT(b193_11, bb1, 10240);
  MF(ck_11, b192_10, b192_11, qa8_1);
  WLG(4);
  MF(ck_02, b192_20, b192_21, qa8_0);
  RDT(b193_20, bb2, 8192);
  RDT(b193_21, bb2, 10240);
  MF(ck_12, b192_20, b192_21, qa8_1);
  MF(ck_03, b192_30, b192_31, qa8_0);
  RDT(b193_30, bb3, 8192);
  RDT(b193_31, bb3, 10240);
  GL8(stq2, v8q, sst, sL + L_STIN + 1024);
  GL8(stq3, v8q, sst, sL + L_STIN + 1536);
  WVM(18);
  MF(ck_13, b192_30, b192_31, qa8_1);
  WLG(4);
  MF(ck_00, b193_00, b193_01, qa9_0);
  PRIO(2);
  RDT(b194_00, bb0, 12288);
  RDT(b194_01, bb0, 14336);
  MF(ck_10, b193_00, b193_01, qa9_1);
  MF(ck_01, b193_10, b193_11, qa9_0);
  RDT(b194_10, bb1, 12288);
  RDT(b194_11, bb1, 14336);
  MF(ck_11, b193_10, b193_11, qa9_1);
  GLD(qa15_0, v16, sa0, sL + L_XIN + 15360);
  WLG(4);
  MF(ck_02, b193_20, b193_21, qa9_0);
  RDT(b194_20, bb2, 12288);
  RDT(b194_21, bb2, 14336);
  MF(ck_12, b193_20, b193_21, qa9_1);
  MF(ck_03, b193_30, b193_31, qa9_0);
  RDT(b194_30, bb3, 12288);
  RDT(b194_31, bb3, 14336);
  WVM(17);
  MF(ck_13, b193_30, b193_31, qa9_1);
  GLD(qa15_1, v16, sa1, sL + L_XIN + 15360);
  WLG(4);
  MF(ck_00, b194_00, b194_01, qa10_0);
  PRIO(1);
  RDT(b195_00, bb0, 16384);
  RDT(b195_01, bb0, 18432);
  MF(ck_10, b194_00, b194_01, qa10_1);
  MF(ck_01, b194_10, b194_11, qa10_0);
  RDT(b195_10, bb1, 16384);
  RDT(b195_11, bb1, 18432);
  MF(ck_11, b194_10, b194_11, qa10_1);
  WLG(4);
  MF(ck_02, b194_20, b194_21, qa10_0);
  RDT(b195_20, bb2, 16384);
  RDT(b195_21, bb2, 18432);
  DMA(rQ, vwq, s_q, 1576960, 36864);
  MF(ck_12, b194_20, b194_21, qa10_1);
  PRIO(0);
  MF(ck_03, b194_30, b194_31, qa10_0);
  RDT(b195_30, bb3, 16384);
  RDT(b195_31, bb3, 18432);
  WVM(17);
  MF(ck_13, b194_30, b194_31, qa10_1);
  WLG(4);
  MF(ck_00, b195_00, b195_01, qa11_0);
  WVM(13);
  BAR(); STB(4);
  PRIO(3);
  RDT(b196_00, bb0, 20480);
  RDT(b196_01, bb0, 22528);
  DMA(rQ, vwq, s_q, 1773568, 40960);
  MF(ck_10, b195_00, b195_01, qa11_1);
  MF(ck_01, b195_10, b195_11, qa11_0);
  RDT(b196_10, bb1, 20480);
  RDT(b196_11, bb1, 22528);
  MF(ck_11, b195_10, b195_11, qa11_1);
  WLG(4);
  MF(ck_02, b195_20, b195_21, qa11_0);
  RDT(b196_20, bb2, 20480);
  RDT(b196_21, bb2, 22528);
  MF(ck_12, b195_20, b195_21, qa11_1);
  DMA(rQ, vwq, s_q, 1970176, 45056);
  MF(ck_03, b195_30, b195_31, qa11_0);
  RDT(b196_30, bb3, 20480);
  RDT(b196_31, bb3, 22528);
  WVM(13);
  MF(ck_13, b195_30, b195_31, qa11_1);
  WLG(4);
  MF(ck_00, b196_00, b196_01, qa12_0);
  RDT(b197_00, bb0, 24576);
  RDT(b197_01, bb0, 26624);
  MF(ck_10, b196_00, b196_01, qa12_1);
  MF(ck_01, b196_10, b196_11, qa12_0);
  RDT(b197_10, bb1, 24576);
  RDT(b197_11, bb1, 26624);
  DMA(rQ, vwq, s_q, 2166784, 49152);
  MF(ck_11, b196_10, b196_11, qa12_1);
  WLG(4);
  MF(ck_02, b196_20, b196_21, qa12_0);
  RDT(b197_20, bb2, 24576);
  RDT(b197_21, bb2, 26624);
  MF(ck_12, b196_20, b196_21, qa12_1);
  MF(ck_03, b196_30, b196_31, qa12_0);
  RDT(b197_30, bb3, 24576);
  RDT(b197_31, bb3, 26624);
  DMA(rQ, vwq, s_q, 2363392, 0);
  WVM(13);
  MF(ck_13, b196_30, b196_31, qa12_1);
  WLG(4);
  MF(ck_00, b197_00, b197_01, qa13_0);
  PRIO(2);
  RDT(b198_00, bb0, 28672);
  RDT(b198_01, bb0, 30720);
  MF(ck_10, b197_00, b197_01, qa13_1);
  RDT(b199_00, bb0, 32768);
  RDT(b199_01, bb0, 34816);
  MF(ck_01, b197_10, b197_11, qa13_0);
  RDT(b198_10, bb1, 28672);
  RDT(b198_11, bb1, 30720);
  MF(ck_11, b197_10, b197_11, qa13_1);
  RDT(b199_10, bb1, 32768);
  RDT(b199_11, bb1, 34816);
  DMA(rQ, vwq, s_q, 2560000, 4096);
  WLG(8);
  MF(ck_02, b197_20, b197_21, qa13_0);
  RDT(b198_20, bb2, 28672);
  RDT(b198_21, bb2, 30720);
  MF(ck_12, b197_20, b197_21, qa13_1);
  RDT(b199_20, bb2, 32768);
  RDT(b199_21, bb2, 34816);
  MF(ck_03, b197_30, b197_31, qa13_0);
  RDT(b198_30, bb3, 28672);
  RDT(b198_31, bb3, 30720);
  WVM(12);
  MF(ck_13, b197_30, b197_31, qa13_1);
  RDT(b199_30, bb3, 32768);
  RDT(b199_31, bb3, 34816);
  DMA(rQ, vwq, s_q, 2756608, 8192);
  WVM(7);
  WLG(8);
  MF(ck_10, b198_00, b198_01, qa14_1);
  PRIO(1);
  MF(ck_10, b199_00, b199_01, qa15_1);
  MF(ck_11, b198_10, b198_11, qa14_1);
  MF(ck_11, b199_10, b199_11, qa15_1);
  WLG(0);
  MF(ck_12, b198_20, b198_21, qa14_1);
  DMA(rQ, vwq, s_q, 2953216, 12288);
  PIN(stq0); PIN(stq1); PIN(stq2); PIN(stq3); float sq = (stq0[0] + stq1[0]) + (stq2[0] + stq3[0]), tq = (stq0[1] + stq1[1]) + (stq2[1] + stq3[1]); PIN(sq); PIN(tq);
  NOP(2);
  WR(ck_10, xw, 0);
  MF(ck_12, b199_20, b199_21, qa15_1);
  PRIO(0);
  sq = half_sum<16>(sq); tq = half_sum<16>(tq); PIN(sq); PIN(tq);
  MF(ck_13, b198_30, b198_31, qa14_1);
  sq = half_sum<32>(sq) * (1.0f / D_MODEL); tq = half_sum<32>(tq); PIN(sq); PIN(tq);
  NOP(0);
  WR(ck_11, xw, 1024);
  MF(ck_13, b199_30, b199_31, qa15_1);
  tq = __builtin_amdgcn_rsqf(fmaxf(tq * (1.0f / D_MODEL) - sq * sq, 0.f) + 1e-5f); PIN(tq);
  MF(ck_00, b198_00, b198_01, qa14_0);
  sq = -sq * tq; PIN(sq); const f32x2 rsq = {tq, tq}, nmq = {sq, sq};
  NOP(2);
  WR(ck_12, xw, 2048);
  MF(ck_01, b198_10, b198_11, qa14_0);
  MF(ck_02, b198_20, b198_21, qa14_0);
  NOP(3);
  WR(ck_13, xw, 3072);
  MF(ck_03, b198_30, b198_31, qa14_0);
  WVM(4);
  WLG(0);
  BAR(); STB(5);
  PRIO(3);
  RDT(b200_00, bb0, 36864);
  RDT(b200_01, bb0, 38912);
  DMA(rQ, vwq, s_q, 0, 16384);
  MF(ck_00, b199_00, b199_01, qa15_0);
  RDT(b200_10, bb1, 36864);
  RDT(b200_11, bb1, 38912);
  MF(ck_01, b199_10, b199_11, qa15_0);
  RDT(b200_20, bb2, 36864);
  RDT(b200_21, bb2, 38912);
  MF(ck_02, b199_20, b199_21, qa15_0);
  RDT(b200_30, bb3, 36864);
  RDT(b200_31, bb3, 38912);
  MF(ck_03, b199_30, b199_31, qa15_0);
  DMA(rQ, vwq, s_q, 196608, 20480);
  WLG(4);
  MF(cv_00, b200_00, b200_01, qa8_0);
  RDT(b201_00, bb0, 40960);
  RDT(b201_01, bb0, 43008);
  RDF(ck_c0, tb, 0);
  MF(cv_10, b200_00, b200_01, qa8_1);
  MF(cv_01, b200_10, b200_11, qa8_0);
  RDT(b201_10, bb1, 40960);
  RDT(b201_11, bb1, 43008);
  MF(cv_11, b200_10, b200_11, qa8_1);
  f32x2 ck_t00, ck_t01;
  WLG(2);
  MUL(ck_t00[0], ck_c0[0], nmq[0]);
  MUL(ck_t00[1], ck_c0[1], nmq[0]);
  MF(cv_02, b200_20, b200_21, qa8_0);
  RDT(b201_20, bb2, 40960);
  RDT(b201_21, bb2, 43008);
  DMA(rQ, vwq, s_q, 393216, 24576);
  MUL(ck_t01[0], ck_c0[2], nmq[0]);
  MUL(ck_t01[1], ck_c0[3], nmq[0]);
  MF(cv_12, b200_20, b200_21, qa8_1);
  RDF(ck_r0, xr, 0);
  RDF(ck_r1, xr, 1024);
  FMA(ck_t00[0], ck_00[0], rsq[0], ck_t00[0]);
  MF(cv_03, b200_30, b200_31, qa8_0);
  RDT(b201_30, bb3, 40960);
  RDT(b201_31, bb3, 43008);
  FMA(ck_t00[1], ck_00[1], rsq[0], ck_t00[1]);
  FMA(ck_t01[0], ck_00[2], rsq[0], ck_t01[0]);
  MF(cv_13, b200_30, b200_31, qa8_1);
  FMA(ck_t01[1], ck_00[3], rsq[0], ck_t01[1]);
  RDF(ck_c1, tb, 64);
  MF(cv_00, b201_00, b201_01, qa9_0);
  RDT(b202_00, bb0, 45056);
  RDT(b202_01, bb0, 47104);
  DMA(rQ, vwq, s_q, 589824, 28672);
  MF(cv_10, b201_00, b201_01, qa9_1);
  WLG(6);
  FMA(ck_t00[0], ck_r0[0], rsq[0], ck_t00[0]);
  FMA(ck_t00[1], ck_r0[1], rsq[0], ck_t00[1]);
  MF(cv_01, b201_10, b201_11, qa9_0);
  RDT(b202_10, bb1, 45056);
  RDT(b202_11, bb1, 47104);
  FMA(ck_t01[0], ck_r0[2], rsq[0], ck_t01[0]);
  FMA(ck_t01[1], ck_r0[3], rsq[0], ck_t01[1]);
  MF(cv_11, b201_10, b201_11, qa9_1);
  PRIO(2);
  RDF(ck_r2, xr, 2048);
  RDF(ck_r3, xr, 3072);
  unsigned ck_p00; CVT(ck_p00, ck_t00[0], ck_t00[1]);
  MF(cv_02, b201_20, b201_21, qa9_0);
  RDT(b202_20, bb2, 45056);
  RDT(b202_21, bb2, 47104);
  unsigned ck_p01; CVT(ck_p01, ck_t01[0], ck_t01[1]);
  f32x2 ck_t10, ck_t11;
  WLG(8);
  MUL(ck_t10[0], ck_c1[0], nmq[0]);
  MF(cv_12, b201_20, b201_21, qa9_1);
  DMA(rQ, vwq, s_q, 786432, 32768);
  MUL(ck_t10[1], ck_c1[1], nmq[0]);
  MUL(ck_t11[0], ck_c1[2], nmq[0]);
  MF(cv_03, b201_30, b201_31, qa9_0);
  RDT(b202_30, bb3, 45056);
  RDT(b202_31, bb3, 47104);
  MUL(ck_t11[1], ck_c1[3], nmq[0]);
  FMA(ck_t10[0], ck_01[0], rsq[0], ck_t10[0]);
  MF(cv_13, b201_30, b201_31, qa9_1);
  FMA(ck_t10[1], ck_01[1], rsq[0], ck_t10[1]);
  FMA(ck_t11[0], ck_01[2], rsq[0], ck_t11[0]);
  WLG(6);
  MF(cv_00, b202_00, b202_01, qa10_0);
  RDT(b203_00, bb0, 49152);
  RDT(b203_01, bb0, 51200);
  FMA(ck_t11[1], ck_01[3], rsq[0], ck_t11[1]);
  RDF(ck_c2, tb, 128);
  MF(cv_10, b202_00, b202_01, qa10_1);
  MF(cv_01, b202_10, b202_11, qa10_0);
  RDT(b203_10, bb1, 49152);
  RDT(b203_11, bb1, 51200);
  FMA(ck_t10[0], ck_r1[0], rsq[0], ck_t10[0]);
  FMA(ck_t10[1], ck_r1[1], rsq[0], ck_t10[1]);
  MF(cv_11, b202_10, b202_11, qa10_1);
  PRIO(1);
  FMA(ck_t11[0], ck_r1[2], rsq[0], ck_t11[0]);
  FMA(ck_t11[1], ck_r1[3], rsq[0], ck_t11[1]);
  WLG(5);
  MF(cv_02, b202_20, b202_21, qa10_0);
  RDT(b203_20, bb2, 49152);
  RDT(b203_21, bb2, 51200);
  unsigned ck_p10; CVT(ck_p10, ck_t10[0], ck_t10[1]);
  unsigned ck_p11; CVT(ck_p11, ck_t11[0], ck_t11[1]);
  MF(cv_12, b202_20, b202_21, qa10_1);
  const u32x4 ck_o0 = {ck_p00, ck_p01, ck_p10, ck_p11};
  unsigned ck_ia = kis + v16; PIN(ck_ia);
  WR(ck_o0, ck_ia, 0);
  if (wid0 == 7) GST(clamp4(ck_o0), v16, hso, sL + L_HALO + 0);
  f32x2 ck_t20, ck_t21;
  WLG(5);
  MUL(ck_t20[0], ck_c2[0], nmq[0]);
  MF(cv_03, b202_30, b202_31, qa10_0);
  RDT(b203_30, bb3, 49152);
  RDT(b203_31, bb3, 51200);
  MUL(ck_t20[1], ck_c2[1], nmq[0]);
  MUL(ck_t21[0], ck_c2[2], nmq[0]);
  MF(cv_13, b202_30, b202_31, qa10_1);
  MUL(ck_t21[1], ck_c2[3], nmq[0]);
  FMA(ck_t20[0], ck_02[0], rsq[0], ck_t20[0]);
  MF(cv_00, b203_00, b203_01, qa11_0);
  PRIO(0);
  WVM(5);
  WLG(2);
  BAR(); STB(6);
  PRIO(3);
  RDT(b204_00, bb0, 0);
  RDT(b204_01, bb0, 2048);
  DMA(rQ, vwq, s_q, 983040, 36864);
  FMA(ck_t20[1], ck_02[1], rsq[0], ck_t20[1]);
  FMA(ck_t21[0], ck_02[2], rsq[0], ck_t21[0]);
  MF(cv_10, b203_00, b203_01, qa11_1);
  FMA(ck_t21[1], ck_02[3], rsq[0], ck_t21[1]);
  RDF(ck_c3, tb, 192);
  MF(cv_01, b203_10, b203_11, qa11_0);
  RDT(b204_10, bb1, 0);
  RDT(b204_11, bb1, 2048);
  MF(cv_11, b203_10, b203_11, qa11_1);
  FMA(ck_t20[0], ck_r2[0], rsq[0], ck_t20[0]);
  FMA(ck_t20[1], ck_r2[1], rsq[0], ck_t20[1]);
  MF(cv_02, b203_20, b203_21, qa11_0);
  RDT(b204_20, bb2, 0);
  RDT(b204_21, bb2, 2048);
  DMA(rQ, vwq, s_q, 1179648, 40960);
  FMA(ck_t21[0], ck_r2[2], rsq[0], ck_t21[0]);
  FMA(ck_t21[1], ck_r2[3], rsq[0], ck_t21[1]);
  MF(cv_12, b203_20, b203_21, qa11_1);
  unsigned ck_p20; CVT(ck_p20, ck_t20[0], ck_t20[1]);
  unsigned ck_p21; CVT(ck_p21, ck_t21[0], ck_t21[1]);
  WLG(5);
  MF(cv_03, b203_30, b203_31, qa11_0);
  RDT(b204_30, bb3, 0);
  RDT(b204_31, bb3, 2048);
  f32x2 ck_t30, ck_t31;
  WLG(6);
  MUL(ck_t30[0], ck_c3[0], nmq[0]);
  MUL(ck_t30[1], ck_c3[1], nmq[0]);
  MF(cv_13, b203_30, b203_31, qa11_1);
  MUL(ck_t31[0], ck_c3[2], nmq[0]);
  MUL(ck_t31[1], ck_c3[3], nmq[0]);
  MF(cv_00, b204_00, b204_01, qa12_0);
  RDT(b205_00, bb0, 4096);
  RDT(b205_01, bb0, 6144);
  FMA(ck_t30[0], ck_03[0], rsq[0], ck_t30[0]);
  FMA(ck_t30[1], ck_03[1], rsq[0], ck_t30[1]);
  MF(cv_10, b204_00, b204_01, qa12_1);
  DMA(rQ, vwq, s_q, 1376256, 45056);
  FMA(ck_t31[0], ck_03[2], rsq[0], ck_t31[0]);
  FMA(ck_t31[1], ck_03[3], rsq[0], ck_t31[1]);
  WLG(4);
  MF(cv_01, b204_10, b204_11, qa12_0);
  RDT(b205_10, bb1, 4096);
  RDT(b205_11, bb1, 6144);
  MF(cv_11, b204_10, b204_11, qa12_1);
  FMA(ck_t30[0], ck_r3[0], rsq[0], ck_t30[0]);
  FMA(ck_t30[1], ck_r3[1], rsq[0], ck_t30[1]);
  MF(cv_02, b204_20, b204_21, qa12_0);
  RDT(b205_20, bb2, 4096);
  RDT(b205_21, bb2, 6144);
  FMA(ck_t31[0], ck_r3[2], rsq[0], ck_t31[0]);
  FMA(ck_t31[1], ck_r3[3], rsq[0], ck_t31[1]);
  MF(cv_12, b204_20, b204_21, qa12_1);
  unsigned ck_p30; CVT(ck_p30, ck_t30[0], ck_t30[1]);
  unsigned ck_p31; CVT(ck_p31, ck_t31[0], ck_t31[1]);
  WLG(4);
  MF(cv_03, b204_30, b204_31, qa12_0);
  RDT(b205_30, bb3, 4096);
  RDT(b205_31, bb3, 6144);
  const u32x4 ck_o1 = {ck_p20, ck_p21, ck_p30, ck_p31};
  WR(ck_o1, ck_ia, 1024);
  if (wid0 == 7) GST(clamp4(ck_o1), v16, hso, sL + L_HALO + 1024);
  MF(cv_13, b204_30, b204_31, qa12_1);
  MF(cv_00, b205_00, b205_01, qa13_0);
  PRIO(2);
  RDT(b206_00, bb0, 8192);
  RDT(b206_01, bb0, 10240);
  MF(cv_10, b205_00, b205_01, qa13_1);
  RDT(b207_00, bb0, 12288);
  RDT(b207_01, bb0, 14336);
  WLG(7);
  MF(cv_01, b205_10, b205_11, qa13_0);
  RDT(b206_10, bb1, 8192);
  RDT(b206_11, bb1, 10240);
  MF(cv_11, b205_10, b205_11, qa13_1);
  RDT(b207_10, bb1, 12288);
  RDT(b207_11, bb1, 14336);
  MF(cv_02, b205_20, b205_21, qa13_0);
  RDT(b206_20, bb2, 8192);
  RDT(b206_21, bb2, 10240);
  MF(cv_12, b205_20, b205_21, qa13_1);
  RDT(b207_20, bb2, 12288);
  RDT(b207_21, bb2, 14336);
  WLG(8);
  MF(cv_03, b205_30, b205_31, qa13_0);
  RDT(b206_30, bb3, 8192);
  RDT(b206_31, bb3, 10240);
  MF(cv_13, b205_30, b205_31, qa13_1);
  RDT(b207_30, bb3, 12288);
  RDT(b207_31, bb3, 14336);
  MF(cv_10, b206_00, b206_01, qa14_1);
  PRIO(1);
  MF(cv_10, b207_00, b207_01, qa15_1);
  WLG(4);
  MF(cv_11, b206_10, b206_11, qa14_1);
  MF(cv_11, b207_10, b207_11, qa15_1);
  MF(cv_12, b206_20, b206_21, qa14_1);
  NOP(3);
  WR(cv_10, xw, 0);
  MF(cv_12, b207_20, b207_21, qa15_1);
  PRIO(0);
  WLG(1);
  MF(cv_13, b206_30, b206_31, qa14_1);
  NOP(1);
  WR(cv_11, xw, 1024);
  MF(cv_13, b207_30, b207_31, qa15_1);
  MF(cv_00, b206_00, b206_01, qa14_0);
  NOP(1);
  WR(cv_12, xw, 2048);
  MF(cv_01, b206_10, b206_11, qa14_0);
  MF(cv_02, b206_20, b206_21, qa14_0);
  VW(vwo, 2048);
  NOP(3);
  WR(cv_13, xw, 3072);
  MF(cv_03, b206_30, b206_31, qa14_0);
  WVM(4);
  WLG(0);
  BAR(); STB(7);
  PRIO(3);
  RDT(b208_00, bb0, 16384);
  RDT(b208_01, bb0, 18432);
  DMA(rQ, vwq, s_q, 1572864, 49152);
  MF(cv_00, b207_00, b207_01, qa15_0);
  RDT(b208_10, bb1, 16384);
  RDT(b208_11, bb1, 18432);
  MF(cv_01, b207_10, b207_11, qa15_0);
  RDT(b208_20, bb2, 16384);
  RDT(b208_21, bb2, 18432);
  MF(cv_02, b207_20, b207_21, qa15_0);
  RDT(b208_30, bb3, 16384);
  RDT(b208_31, bb3, 18432);
  MF(cv_03, b207_30, b207_31, qa15_0);
  DMA(rQ, vwq, s_q, 1769472, 0);
  STP(13);
  WLG(4);
  MF0(cq_00, b208_00, b208_01, qa0_0);
  RDT(b209_00, bb0, 20480);
  RDT(b209_01, bb0, 22528);
  RDF(cv_c0, tb, 256);
  MF0(cq_10, b208_00, b208_01, qa0_1);
  MF0(cq_01, b208_10, b208_11, qa0_0);
  RDT(b209_10, bb1, 20480);
  RDT(b209_11, bb1, 22528);
  MF0(cq_11, b208_10, b208_11, qa0_1);
  f32x2 cv_t00, cv_t01;
  WLG(2);
  MUL(cv_t00[0], cv_c0[0], nmq[0]);
  MUL(cv_t00[1], cv_c0[1], nmq[0]);
  MF0(cq_02, b208_20, b208_21, qa0_0);
  RDT(b209_20, bb2, 20480);
  RDT(b209_21, bb2, 22528);
  DMA(rQ, vwq, s_q, 1966080, 4096);
  MUL(cv_t01[0], cv_c0[2], nmq[0]);
  MUL(cv_t01[1], cv_c0[3], nmq[0]);
  MF0(cq_12, b208_20, b208_21, qa0_1);
  RDF(cv_r0, xr, 0);
  RDF(cv_r1, xr, 1024);
  FMA(cv_t00[0], cv_00[0], rsq[0], cv_t00[0]);
  MF0(cq_03, b208_30, b208_31, qa0_0);
  RDT(b209_30, bb3, 20480);
  RDT(b209_31, bb3, 22528);
  FMA(cv_t00[1], cv_00[1], rsq[0], cv_t00[1]);
  FMA(cv_t01[0], cv_00[2], rsq[0], cv_t01[0]);
  MF0(cq_13, b208_30, b208_31, qa0_1);
  FMA(cv_t01[1], cv_00[3], rsq[0], cv_t01[1]);
  RDF(cv_c1, tb, 320);
  MF(cq_00, b209_00, b209_01, qa1_0);
  RDT(b210_00, bb0, 24576);
  RDT(b210_01, bb0, 26624);
  DMA(rQ, vwq, s_q, 2162688, 8192);
  MF(cq_10, b209_00, b209_01, qa1_1);
  WLG(6);
  FMA(cv_t00[0], cv_r0[0], rsq[0], cv_t00[0]);
  FMA(cv_t00[1], cv_r0[1], rsq[0], cv_t00[1]);
  MF(cq_01, b209_10, b209_11, qa1_0);
  RDT(b210_10, bb1, 24576);
  RDT(b210_11, bb1, 26624);
  FMA(cv_t01[0], cv_r0[2], rsq[0], cv_t01[0]);
  FMA(cv_t01[1], cv_r0[3], rsq[0], cv_t01[1]);
  MF(cq_11, b209_10, b209_11, qa1_1);
  PRIO(2);
  RDF(cv_r2, xr, 2048);
  RDF(cv_r3, xr, 3072);
  unsigned cv_p00; CVT(cv_p00, cv_t00[0], cv_t00[1]);
  MF(cq_02, b209_20, b209_21, qa1_0);
  RDT(b210_20, bb2, 24576);
  RDT(b210_21, bb2, 26624);
  unsigned cv_p01; CVT(cv_p01, cv_t01[0], cv_t01[1]);
  f32x2 cv_t10, cv_t11;
  WLG(8);
  MUL(cv_t10[0], cv_c1[0], nmq[0]);
  MF(cq_12, b209_20, b209_21, qa1_1);
  DMA(rQ, vwq, s_q, 2359296, 12288);
  MUL(cv_t10[1], cv_c1[1], nmq[0]);
  MUL(cv_t11[0], cv_c1[2], nmq[0]);
  MF(cq_03, b209_30, b209_31, qa1_0);
  RDT(b210_30, bb3, 24576);
  RDT(b210_31, bb3, 26624);
  MUL(cv_t11[1], cv_c1[3], nmq[0]);
  FMA(cv_t10[0], cv_01[0], rsq[0], cv_t10[0]);
  MF(cq_13, b209_30, b209_31, qa1_1);
  FMA(cv_t10[1], cv_01[1], rsq[0], cv_t10[1]);
  FMA(cv_t11[0], cv_01[2], rsq[0], cv_t11[0]);
  WLG(6);
  MF(cq_00, b210_00, b210_01, qa2_0);
  RDT(b211_00, bb0, 28672);
  RDT(b211_01, bb0, 30720);
  FMA(cv_t11[1], cv_01[3], rsq[0], cv_t11[1]);
  RDF(cv_c2, tb, 384);
  MF(cq_10, b210_00, b210_01, qa2_1);
  MF(cq_01, b210_10, b210_11, qa2_0);
  RDT(b211_10, bb1, 28672);
  RDT(b211_11, bb1, 30720);
  FMA(cv_t10[0], cv_r1[0], rsq[0], cv_t10[0]);
  FMA(cv_t10[1], cv_r1[1], rsq[0], cv_t10[1]);
  MF(cq_11, b210_10, b210_11, qa2_1);
  PRIO(1);
  FMA(cv_t11[0], cv_r1[2], rsq[0], cv_t11[0]);
  FMA(cv_t11[1], cv_r1[3], rsq[0], cv_t11[1]);
  WLG(5);
  MF(cq_02, b210_20, b210_21, qa2_0);
  RDT(b211_20, bb2, 28672);
  RDT(b211_21, bb2, 30720);
  unsigned cv_p10; CVT(cv_p10, cv_t10[0], cv_t10[1]);
  unsigned cv_p11; CVT(cv_p11, cv_t11[0], cv_t11[1]);
  MF(cq_12, b210_20, b210_21, qa2_1);
  const u32x4 cv_o0 = {cv_p00, cv_p01, cv_p10, cv_p11};
  const int cv_ia = xr; int vao; { LANE(l_); vao = xr - l_ * 16 + ((l_ & 3) * 16 + 4 * (l_ >> 4) + ((l_ & 15) >> 2)) * 16; PIN(vao); }
  WR(cv_o0, cv_ia, 0);
  RDT(vo00, vao, 0);
  RDT(vo01, vao, 8);
  f32x2 cv_t20, cv_t21;
  WLG(7);
  MUL(cv_t20[0], cv_c2[0], nmq[0]);
  MF(cq_03, b210_30, b210_31, qa2_0);
  RDT(b211_30, bb3, 28672);
  RDT(b211_31, bb3, 30720);
  MUL(cv_t20[1], cv_c2[1], nmq[0]);
  MUL(cv_t21[0], cv_c2[2], nmq[0]);
  MF(cq_13, b210_30, b210_31, qa2_1);
  MUL(cv_t21[1], cv_c2[3], nmq[0]);
  FMA(cv_t20[0], cv_02[0], rsq[0], cv_t20[0]);
  MF(cq_00, b211_00, b211_01, qa3_0);
  PRIO(0);
  WVM(5);
  WLG(4);
  BAR(); STB(8);
  PRIO(3);
  RDT(b212_00, bb0, 32768);
  RDT(b212_01, bb0, 34816);
  DMA(rQ, vwq, s_q, 2555904, 16384);
  FMA(cv_t20[1], cv_02[1], rsq[0], cv_t20[1]);
  FMA(cv_t21[0], cv_02[2], rsq[0], cv_t21[0]);
  MF(cq_10, b211_00, b211_01, qa3_1);
  FMA(cv_t21[1], cv_02[3], rsq[0], cv_t21[1]);
  RDF(cv_c3, tb, 448);
  MF(cq_01, b211_10, b211_11, qa3_0);
  RDT(b212_10, bb1, 32768);
  RDT(b212_11, bb1, 34816);
  WLG(7);
  PIN(vo00); PIN(vo01); if (wid0 == 7) GST(clamp4(__builtin_bit_cast(u32x4, CAT8(vo00, vo01))), v16, hso, sL + L_HALO + 2048);
  MF(cq_11, b211_10, b211_11, qa3_1);
  FMA(cv_t20[0], cv_r2[0], rsq[0], cv_t20[0]);
  FMA(cv_t20[1], cv_r2[1], rsq[0], cv_t20[1]);
  MF(cq_02, b211_20, b211_21, qa3_0);
  RDT(b212_20, bb2, 32768);
  RDT(b212_21, bb2, 34816);
  DMA(rQ, vwq, s_q, 2752512, 20480);
  FMA(cv_t21[0], cv_r2[2], rsq[0], cv_t21[0]);
  FMA(cv_t21[1], cv_r2[3], rsq[0], cv_t21[1]);
  MF(cq_12, b211_20, b211_21, qa3_1);
  unsigned cv_p20; CVT(cv_p20, cv_t20[0], cv_t20[1]);
  unsigned cv_p21; CVT(cv_p21, cv_t21[0], cv_t21[1]);
  WLG(5);
  MF(cq_03, b211_30, b211_31, qa3_0);
  RDT(b212_30, bb3, 32768);
  RDT(b212_31, bb3, 34816);
  f32x2 cv_t30, cv_t31;
  WLG(6);
  MUL(cv_t30[0], cv_c3[0], nmq[0]);
  MUL(cv_t30[1], cv_c3[1], nmq[0]);
  MF(cq_13, b211_30, b211_31, qa3_1);
  MUL(cv_t31[0], cv_c3[2], nmq[0]);
  MUL(cv_t31[1], cv_c3[3], nmq[0]);
  MF(cq_00, b212_00, b212_01, qa4_0);
  RDT(b213_00, bb0, 36864);
  RDT(b213_01, bb0, 38912);
  FMA(cv_t30[0], cv_03[0], rsq[0], cv_t30[0]);
  FMA(cv_t30[1], cv_03[1], rsq[0], cv_t30[1]);
  MF(cq_10, b212_00, b212_01, qa4_1);
  DMA(rQ, vwq, s_q, 2949120, 24576);
  FMA(cv_t31[0], cv_03[2], rsq[0], cv_t31[0]);
  FMA(cv_t31[1], cv_03[3], rsq[0], cv_t31[1]);
  WLG(4);
  MF(cq_01, b212_10, b212_11, qa4_0);
  RDT(b213_10, bb1, 36864);
  RDT(b213_11, bb1, 38912);
  MF(cq_11, b212_10, b212_11, qa4_1);
  FMA(cv_t30[0], cv_r3[0], rsq[0], cv_t30[0]);
  FMA(cv_t30[1], cv_r3[1], rsq[0], cv_t30[1]);
  MF(cq_02, b212_20, b212_21, qa4_0);
  RDT(b213_20, bb2, 36864);
  RDT(b213_21, bb2, 38912);
  FMA(cv_t31[0], cv_r3[2], rsq[0], cv_t31[0]);
  FMA(cv_t31[1], cv_r3[3], rsq[0], cv_t31[1]);
  MF(cq_12, b212_20, b212_21, qa4_1);
  unsigned cv_p30; CVT(cv_p30, cv_t30[0], cv_t30[1]);
  unsigned cv_p31; CVT(cv_p31, cv_t31[0], cv_t31[1]);
  WLG(4);
  MF(cq_03, b212_30, b212_31, qa4_0);
  RDT(b213_30, bb3, 36864);
  RDT(b213_31, bb3, 38912);
  const u32x4 cv_o1 = {cv_p20, cv_p21, cv_p30, cv_p31};
  WR(cv_o1, cv_ia, 1024);
  RDT(vo10, vao, 1024);
  RDT(vo11, vao, 1032);
  MF(cq_13, b212_30, b212_31, qa4_1);
  MF(cq_00, b213_00, b213_01, qa5_0);
  PRIO(2);
  RDT(b214_00, bb0, 40960);
  RDT(b214_01, bb0, 43008);
  MF(cq_10, b213_00, b213_01, qa5_1);
  WLG(7);
  MF(cq_01, b213_10, b213_11, qa5_0);
  RDT(b214_10, bb1, 40960);
  RDT(b214_11, bb1, 43008);
  MF(cq_11, b213_10, b213_11, qa5_1);
  WLG(4);
  PIN(vo10); PIN(vo11); if (wid0 == 7) GST(clamp4(__builtin_bit_cast(u32x4, CAT8(vo10, vo11))), v16, hso, sL + L_HALO + 3072);
  MF(cq_02, b213_20, b213_21, qa5_0);
  RDT(b214_20, bb2, 40960);
  RDT(b214_21, bb2, 43008);
  MF(cq_12, b213_20, b213_21, qa5_1);
  MF(cq_03, b213_30, b213_31, qa5_0);
  RDT(b214_30, bb3, 40960);
  RDT(b214_31, bb3, 43008);
  MF(cq_13, b213_30, b213_31, qa5_1);
  WLG(4);
  MF(cq_00, b214_00, b214_01, qa6_0);
  PRIO(1);
  RDT(b215_00, bb0, 45056);
  RDT(b215_01, bb0, 47104);
  MF(cq_10, b214_00, b214_01, qa6_1);
  MF(cq_01, b214_10, b214_11, qa6_0);
  RDT(b215_10, bb1, 45056);
  RDT(b215_11, bb1, 47104);
  int kao, kap, vap;
      { IDS(); const int mb = max(rg * 2 + kh - 1, 0);
        kao = kis + v16; kap = (int)lds0 + (mb >> 2) * HALF + (mb & 3) * 2048 + KIM_OFF + v16;
        vap = vao + (2 * kh - 1) * HALF + ((mb >> 1) - rg) * 4096;
        PIN(kao); PIN(kap); PIN(vap); }
  MF(cq_11, b214_10, b214_11, qa6_1);
  WLG(4);
  MF(cq_02, b214_20, b214_21, qa6_0);
  RDT(b215_20, bb2, 45056);
  RDT(b215_21, bb2, 47104);
  MF(cq_12, b214_20, b214_21, qa6_1);
  PRIO(0);
  MF(cq_03, b214_30, b214_31, qa6_0);
  RDT(b215_30, bb3, 45056);
  RDT(b215_31, bb3, 47104);
  MF(cq_13, b214_30, b214_31, qa6_1);
  WLG(4);
  MF(cq_00, b215_00, b215_01, qa7_0);
  WVM(4);
  BAR(); STB(9);
  PRIO(3);
  RDT(b216_00, bb0, 49152);
  RDT(b216_01, bb0, 51200);
  DMA(rO, vwo, s_o, 0, 28672);
  MF(cq_10, b215_00, b215_01, qa7_1);
  MF(cq_01, b215_10, b215_11, qa7_0);
  RDT(b216_10, bb1, 49152);
  RDT(b216_11, bb1, 51200);
  RDB(ko0, kao, 0);
  MF(cq_11, b215_10, b215_11, qa7_1);
  RDB(kp0, kap, 0);
  WLG(6);
  MF(cq_02, b215_20, b215_21, qa7_0);
  RDT(b216_20, bb2, 49152);
  RDT(b216_21, bb2, 51200);
  DMA(rO, vwo, s_o, 65536, 32768);
  RDT(vp00, vap, 0);
  MF(cq_12, b215_20, b215_21, qa7_1);
  RDT(vp01, vap, 8);
  MF(cq_03, b215_30, b215_31, qa7_0);
  RDT(b216_30, bb3, 49152);
  RDT(b216_31, bb3, 51200);
  RDB(ko1, kao, 1024);
  MF(cq_13, b215_30, b215_31, qa7_1);
  RDB(kp1, kap, 1024);
  WLG(10);
  MF(cq_00, b216_00, b216_01, qa8_0);
  RDT(b217_00, bb0, 0);
  RDT(b217_01, bb0, 2048);
  RDT(vp10, vap, 1024);
  MF(cq_10, b216_00, b216_01, qa8_1);
  DMA(rO, vwo, s_o, 131072, 36864);
  RDT(vp11, vap, 1032);
  MF(cq_01, b216_10, b216_11, qa8_0);
  RDT(b217_10, bb1, 0);
  RDT(b217_11, bb1, 2048);
  MF(cq_11, b216_10, b216_11, qa8_1);
  WLG(8);
  MF(cq_02, b216_20, b216_21, qa8_0);
  RDT(b217_20, bb2, 0);
  RDT(b217_21, bb2, 2048);
  MF(cq_12, b216_20, b216_21, qa8_1);
  DMA(rO, vwo, s_o, 196608, 40960);
  MF(cq_03, b216_30, b216_31, qa8_0);
  RDT(b217_30, bb3, 0);
  RDT(b217_31, bb3, 2048);
  MF(cq_13, b216_30, b216_31, qa8_1);
  WLG(4);
  MF(cq_00, b217_00, b217_01, qa9_0);
  PRIO(2);
  RDT(b218_00, bb0, 4096);
  RDT(b218_01, bb0, 6144);
  MF(cq_10, b217_00, b217_01, qa9_1);
  MF(cq_01, b217_10, b217_11, qa9_0);
  RDT(b218_10, bb1, 4096);
  RDT(b218_11, bb1, 6144);
  MF(cq_11, b217_10, b217_11, qa9_1);
  WLG(4);
  MF(cq_02, b217_20, b217_21, qa9_0);
  RDT(b218_20, bb2, 4096);
  RDT(b218_21, bb2, 6144);
  MF(cq_12, b217_20, b217_21, qa9_1);
  MF(cq_03, b217_30, b217_31, qa9_0);
  RDT(b218_30, bb3, 4096);
  RDT(b218_31, bb3, 6144);
  MF(cq_13, b217_30, b217_31, qa9_1);
  WLG(4);
  MF(cq_00, b218_00, b218_01, qa10_0);
  PRIO(1);
  RDT(b219_00, bb0, 8192);
  RDT(b219_01, bb0, 10240);
  MF(cq_10, b218_00, b218_01, qa10_1);
  MF(cq_01, b218_10, b218_11, qa10_0);
  RDT(b219_10, bb1, 8192);
  RDT(b219_11, bb1, 10240);
  MF(cq_11, b218_10, b218_11, qa10_1);
  WLG(4);
  MF(cq_02, b218_20, b218_21, qa10_0);
  RDT(b219_20, bb2, 8192);
  RDT(b219_21, bb2, 10240);
  MF(cq_12, b218_20, b218_21, qa10_1);
  PRIO(0);
  MF(cq_03, b218_30, b218_31, qa10_0);
  RDT(b219_30, bb3, 8192);
  RDT(b219_31, bb3, 10240);
  MF(cq_13, b218_30, b218_31, qa10_1);
  WLG(4);
  MF(cq_00, b219_00, b219_01, qa11_0);
  WVM(4);
  BAR(); STB(10);
  PRIO(3);
  RDT(b220_00, bb0, 12288);
  RDT(b220_01, bb0, 14336);
  DMA(rO, vwo, s_o, 262144, 53248);
  GLD(hl0, v16, hsi, sL + L_HALO + 0);
  RDF(cq_c0, tb, 512);
  RDF(cq_c1, tb, 576);
  RDF(cq_c2, tb, 640);
  RDF(cq_c3, tb, 704);
  MF(cq_10, b219_00, b219_01, qa11_1);
  GLD(hl1, v16, hsi, sL + L_HALO + 1024);
  bool par, own0, own1, nop;
      { IDS(); LANE(l_); nop = mk == 0; const int li = l_ & 15, lg = l_ >> 4;
        par = li & 1; own0 = li - 4 * lg - (li & 1) >= 0; own1 = li - 4 * lg - (li & 1) - 2 >= 0; }
  MF(cq_01, b219_10, b219_11, qa11_0);
  RDT(b220_10, bb1, 12288);
  RDT(b220_11, bb1, 14336);
  GLD(hl2, v16, hsi, sL + L_HALO + 2048);
  MF(cq_11, b219_10, b219_11, qa11_1);
  GLD(hl3, v16, hsi, sL + L_HALO + 3072);
  WLG(8);
  MF(cq_02, b219_20, b219_21, qa11_0);
  RDT(b220_20, bb2, 12288);
  RDT(b220_21, bb2, 14336);
  DMA(rO, vwo, s_o, 327680, 57344);
  WLG(7);
  PIN(cq_c0); f32x4 cq_u0 = cq_c0 * nmq[0]; PIN(cq_u0);
  MF(cq_12, b219_20, b219_21, qa11_1);
  WLG(6);
  PIN(cq_c1); f32x4 cq_u1 = cq_c1 * nmq[0]; PIN(cq_u1);
  MF(cq_03, b219_30, b219_31, qa11_0);
  RDT(b220_30, bb3, 12288);
  RDT(b220_31, bb3, 14336);
  WLG(7);
  PIN(cq_c2); f32x4 cq_u2 = cq_c2 * nmq[0]; PIN(cq_u2);
  MF(cq_13, b219_30, b219_31, qa11_1);
  WLG(6);
  PIN(cq_c3); f32x4 cq_u3 = cq_c3 * nmq[0]; PIN(cq_u3);
  MF(cq_00, b220_00, b220_01, qa12_0);
  RDT(b221_00, bb0, 16384);
  RDT(b221_01, bb0, 18432);
  MF(cq_10, b220_00, b220_01, qa12_1);
  DMA(rO, vwo, s_o, 393216, 45056);
  WLG(4);
  MF(cq_01, b220_10, b220_11, qa12_0);
  RDT(b221_10, bb1, 16384);
  RDT(b221_11, bb1, 18432);
  MF(cq_11, b220_10, b220_11, qa12_1);
  MF(cq_02, b220_20, b220_21, qa12_0);
  RDT(b221_20, bb2, 16384);
  RDT(b221_21, bb2, 18432);
  MF(cq_12, b220_20, b220_21, qa12_1);
  DMA(rO, vwo, s_o, 458752, 49152);
  WLG(4);
  MF(cq_03, b220_30, b220_31, qa12_0);
  RDT(b221_30, bb3, 16384);
  RDT(b221_31, bb3, 18432);
  MF(cq_13, b220_30, b220_31, qa12_1);
  MF(cq_00, b221_00, b221_01, qa13_0);
  PRIO(2);
  RDT(b222_00, bb0, 20480);
  RDT(b222_01, bb0, 22528);
  MF(cq_10, b221_00, b221_01, qa13_1);
  RDT(b223_00, bb0, 24576);
  RDT(b223_01, bb0, 26624);
  WLG(6);
  MF(cq_01, b221_10, b221_11, qa13_0);
  RDT(b222_10, bb1, 20480);
  RDT(b222_11, bb1, 22528);
  DMA(rO, vwo, s_o, 524288, 0);
  MF(cq_11, b221_10, b221_11, qa13_1);
  RDT(b223_10, bb1, 24576);
  RDT(b223_11, bb1, 26624);
  MF(cq_02, b221_20, b221_21, qa13_0);
  RDT(b222_20, bb2, 20480);
  RDT(b222_21, bb2, 22528);
  MF(cq_12, b221_20, b221_21, qa13_1);
  RDT(b223_20, bb2, 24576);
  RDT(b223_21, bb2, 26624);
  WLG(8);
  MF(cq_03, b221_30, b221_31, qa13_0);
  RDT(b222_30, bb3, 20480);
  RDT(b222_31, bb3, 22528);
  MF(cq_13, b221_30, b221_31, qa13_1);
  RDT(b223_30, bb3, 24576);
  RDT(b223_31, bb3, 26624);
  DMA(rO, vwo, s_o, 589824, 4096);
  MF(cq_10, b222_00, b222_01, qa14_1);
  PRIO(1);
  MF(cq_10, b223_00, b223_01, qa15_1);
  WLG(4);
  MF(cq_11, b222_10, b222_11, qa14_1);
  MF(cq_11, b223_10, b223_11, qa15_1);
  MF(cq_12, b222_20, b222_21, qa14_1);
  NOP(3);
  WR(cq_10, xw, 0);
  MF(cq_12, b223_20, b223_21, qa15_1);
  PRIO(0);
  WLG(1);
  MF(cq_13, b222_30, b222_31, qa14_1);
  NOP(1);
  WR(cq_11, xw, 1024);
  MF(cq_13, b223_30, b223_31, qa15_1);
  MF(cq_00, b222_00, b222_01, qa14_0);
  NOP(1);
  WR(cq_12, xw, 2048);
  MF(cq_01, b222_10, b222_11, qa14_0);
  MF(cq_02, b222_20, b222_21, qa14_0);
  NOP(3);
  WR(cq_13, xw, 3072);
  MF(cq_03, b222_30, b222_31, qa14_0);
  MF(cq_00, b223_00, b223_01, qa15_0);
  MF(cq_01, b223_10, b223_11, qa15_0);
  MF(cq_02, b223_20, b223_21, qa15_0);
  MF(cq_03, b223_30, b223_31, qa15_0);
  PRIO(0);
  STP(2);
  WVM(11);
  WLG(0);
  BAR(); STB(11);
  RDF(cq_r0, xr, 0);
  RDF(cq_r1, xr, 1024);
  RDF(cq_r2, xr, 2048);
  RDF(cq_r3, xr, 3072);
  PIN(kp0); PIN(kp1); PIN(vp00); PIN(vp01); PIN(vp10); PIN(vp11);
  WVM(5);
  PIN(hl0); PIN(hl1); PIN(hl2); PIN(hl3);
  if (wid0 == 0) {
    { unsigned t_ = mx8(hl0, hl1); CHK2(t_, hl0, hl1, v16, hsi, sL + L_HALO, hsi, sL + L_HALO + 1024, 2); }
    { unsigned t_ = mx8(hl2, hl3); CHK2(t_, hl2, hl3, v16, hsi, sL + L_HALO + 2048, hsi, sL + L_HALO + 3072, 2); }
    kp0 = __builtin_bit_cast(bf16x8, hl0); kp1 = __builtin_bit_cast(bf16x8, hl1);
    vp00 = __builtin_bit_cast(s16x4, (u32x2){hl2[0], hl2[1]}); vp01 = __builtin_bit_cast(s16x4, (u32x2){hl2[2], hl2[3]});
    vp10 = __builtin_bit_cast(s16x4, (u32x2){hl3[0], hl3[1]}); vp11 = __builtin_bit_cast(s16x4, (u32x2){hl3[2], hl3[3]});
  }
  WLG(0);
  PIN(cq_r0); PIN(cq_00);
  PIN(cq_r1); PIN(cq_01);
  PIN(cq_r2); PIN(cq_02);
  PIN(cq_r3); PIN(cq_03);
  const f32x4 cq_t0 = (cq_00 + cq_r0) * rsq[0] + cq_u0;
  const f32x4 cq_t1 = (cq_01 + cq_r1) * rsq[0] + cq_u1;
  const f32x4 cq_t2 = (cq_02 + cq_r2) * rsq[0] + cq_u2;
  const f32x4 cq_t3 = (cq_03 + cq_r3) * rsq[0] + cq_u3;
  u32x4 qf0 = {pk2(cq_t0[0], cq_t0[1]), pk2(cq_t0[2], cq_t0[3]), pk2(cq_t1[0], cq_t1[1]), pk2(cq_t1[2], cq_t1[3])}; PIN(qf0);
  u32x4 qf1 = {pk2(cq_t2[0], cq_t2[1]), pk2(cq_t2[2], cq_t2[3]), pk2(cq_t3[0], cq_t3[1]), pk2(cq_t3[2], cq_t3[3])}; PIN(qf1);
  PIN(kp0); PIN(kp1); PIN(ko0); PIN(ko1); PIN(vp00); PIN(vp01); PIN(vp10); PIN(vp11); PIN(vo00); PIN(vo01); PIN(vo10); PIN(vo11);
  u32x4 ao[2]; attn2(ao, {qf0, qf1}, {kp0, kp1}, {ko0, ko1}, {{CAT8(vp00, vo00), CAT8(vp01, vo01)}, {CAT8(vp10, vo10), CAT8(vp11, vo11)}}, par, own0, own1, nop);
  GST(ao[0], v16, so, sL + L_OB + 0);
  GST(ao[1], v16, so, sL + L_OB + 1024);
  STP(3);
  VW(vw1, 7168);
  RDT(b224_00, bb0, 28672);
  RDT(b224_01, bb0, 30720);
  RDT(b224_10, bb1, 28672);
  RDT(b224_11, bb1, 30720);
  RDT(b224_20, bb2, 28672);
  RDT(b224_21, bb2, 30720);
  RDT(b224_30, bb3, 28672);
  RDT(b224_31, bb3, 30720);
  RDT(b225_00, bb0, 32768);
  RDT(b225_01, bb0, 34816);
  RDT(b225_10, bb1, 32768);
  RDT(b225_11, bb1, 34816);
  RDT(b225_20, bb2, 32768);
  RDT(b225_21, bb2, 34816);
  RDT(b225_30, bb3, 32768);
  RDT(b225_31, bb3, 34816);
  RDT(b226_00, bb0, 36864);
  RDT(b226_01, bb0, 38912);
  RDT(b226_10, bb1, 36864);
  RDT(b226_11, bb1, 38912);
  RDT(b226_20, bb2, 36864);
  RDT(b226_21, bb2, 38912);
  RDT(b226_30, bb3, 36864);
  RDT(b226_31, bb3, 38912);
  WLG(0);
  WVM(0);
  BAR(); STB(12);
  ARRIVE(L_CN + CNB);
  DMA(rO, vwo, s_o, 655360, 8192);
  DMA(rO, vwo, s_o, 720896, 12288);
  DMA(rO, vwo, s_o, 786432, 16384);
  DMA(rO, vwo, s_o, 851968, 20480);
  DMA(rO, vwo, s_o, 917504, 24576);
  DMA(rO, vwo, s_o, 983040, 28672);
  DMA(r1, vw1, s_1w, 0, 32768);
  DMA(r1, vw1, s_1w, 128, 36864);
  RDT(b227_00, bb0, 40960);
  RDT(b227_01, bb0, 43008);
  RDT(b227_10, bb1, 40960);
  RDT(b227_11, bb1, 43008);
  RDT(b227_20, bb2, 40960);
  RDT(b227_21, bb2, 43008);
  RDT(b227_30, bb3, 40960);
  RDT(b227_31, bb3, 43008);
  RDT(b228_00, bb0, 53248);
  RDT(b228_01, bb0, 55296);
  RDT(b228_10, bb1, 53248);
  RDT(b228_11, bb1, 55296);
  RDT(b228_20, bb2, 53248);
  RDT(b228_21, bb2, 55296);
  RDT(b228_30, bb3, 53248);
  RDT(b228_31, bb3, 55296);
  RDT(b229_00, bb0, 57344);
  RDT(b229_01, bb0, 59392);
  RDT(b229_10, bb1, 57344);
  RDT(b229_11, bb1, 59392);
  RDT(b229_20, bb2, 57344);
  RDT(b229_21, bb2, 59392);
  RDT(b229_30, bb3, 57344);
  RDT(b229_31, bb3, 59392);
  WLG(0);
  MEET(L_CN + CNB);
  STP(21);
  BAR(); STB(13);
  STP(4);
  PRIO(3);
  GLD(oa0_0, v16, sa0, sL + L_OB + 0);
  GLD(oa0_1, v16, sa1, sL + L_OB + 0);
  PRIO(2);
  GLD(oa1_0, v16, sa0, sL + L_OB + 1024);
  GLD(oa1_1, v16, sa1, sL + L_OB + 1024);
  PRIO(1);
  GLD(oa2_0, v16, sa0, sL + L_OB + 2048);
  GLD(oa2_1, v16, sa1, sL + L_OB + 2048);
  PRIO(0);
  GLD(oa3_0, v16, sa0, sL + L_OB + 3072);
  GLD(oa3_1, v16, sa1, sL + L_OB + 3072);
  PRIO(3);
  WVM(6);
  MF0(co_00, b224_00, b224_01, oa0_0);
  GLD(oa4_0, v16, sa0, sL + L_OB + 4096);
  MF0(co_10, b224_00, b224_01, oa0_1);
  MF0(co_01, b224_10, b224_11, oa0_0);
  MF0(co_11, b224_10, b224_11, oa0_1);
  MF0(co_02, b224_20, b224_21, oa0_0);
  GLD(oa4_1, v16, sa1, sL + L_OB + 4096);
  MF0(co_12, b224_20, b224_21, oa0_1);
  MF0(co_03, b224_30, b224_31, oa0_0);
  WVM(6);
  MF0(co_13, b224_30, b224_31, oa0_1);
  STP(24);
  MF(co_00, b225_00, b225_01, oa1_0);
  GLD(oa5_0, v16, sa0, sL + L_OB + 5120);
  MF(co_10, b225_00, b225_01, oa1_1);
  MF(co_01, b225_10, b225_11, oa1_0);
  MF(co_11, b225_10, b225_11, oa1_1);
  MF(co_02, b225_20, b225_21, oa1_0);
  GLD(oa5_1, v16, sa1, sL + L_OB + 5120);
  MF(co_12, b225_20, b225_21, oa1_1);
  MF(co_03, b225_30, b225_31, oa1_0);
  WVM(6);
  MF(co_13, b225_30, b225_31, oa1_1);
  PRIO(2);
  MF(co_00, b226_00, b226_01, oa2_0);
  GLD(oa6_0, v16, sa0, sL + L_OB + 6144);
  MF(co_10, b226_00, b226_01, oa2_1);
  MF(co_01, b226_10, b226_11, oa2_0);
  MF(co_11, b226_10, b226_11, oa2_1);
  MF(co_02, b226_20, b226_21, oa2_0);
  GLD(oa6_1, v16, sa1, sL + L_OB + 6144);
  MF(co_12, b226_20, b226_21, oa2_1);
  MF(co_03, b226_30, b226_31, oa2_0);
  WVM(6);
  MF(co_13, b226_30, b226_31, oa2_1);
  PRIO(1);
  MF(co_00, b227_00, b227_01, oa3_0);
  GLD(oa7_0, v16, sa0, sL + L_OB + 7168);
  MF(co_10, b227_00, b227_01, oa3_1);
  MF(co_01, b227_10, b227_11, oa3_0);
  MF(co_11, b227_10, b227_11, oa3_1);
  MF(co_02, b227_20, b227_21, oa3_0);
  PRIO(0);
  GLD(oa7_1, v16, sa1, sL + L_OB + 7168);
  MF(co_12, b227_20, b227_21, oa3_1);
  MF(co_03, b227_30, b227_31, oa3_0);
  WVM(6);
  MF(co_13, b227_30, b227_31, oa3_1);
  MF(co_00, b228_00, b228_01, oa4_0);
  GLD(oa8_0, v16, sa0, sL + L_OB + 8192);
  MF(co_10, b228_00, b228_01, oa4_1);
  MF(co_01, b228_10, b228_11, oa4_0);
  MF(co_11, b228_10, b228_11, oa4_1);
  MF(co_02, b228_20, b228_21, oa4_0);
  GLD(oa8_1, v16, sa1, sL + L_OB + 8192);
  MF(co_12, b228_20, b228_21, oa4_1);
  MF(co_03, b228_30, b228_31, oa4_0);
  WVM(6);
  MF(co_13, b228_30, b228_31, oa4_1);
  MF(co_00, b229_00, b229_01, oa5_0);
  RDT(b230_00, bb0, 45056);
  RDT(b230_01, bb0, 47104);
  GLD(oa9_0, v16, sa0, sL + L_OB + 9216);
  MF(co_10, b229_00, b229_01, oa5_1);
  MF(co_01, b229_10, b229_11, oa5_0);
  RDT(b230_10, bb1, 45056);
  RDT(b230_11, bb1, 47104);
  MF(co_11, b229_10, b229_11, oa5_1);
  MF(co_02, b229_20, b229_21, oa5_0);
  RDT(b230_20, bb2, 45056);
  RDT(b230_21, bb2, 47104);
  GLD(oa9_1, v16, sa1, sL + L_OB + 9216);
  MF(co_12, b229_20, b229_21, oa5_1);
  MF(co_03, b229_30, b229_31, oa5_0);
  RDT(b230_30, bb3, 45056);
  RDT(b230_31, bb3, 47104);
  WVM(6);
  MF(co_13, b229_30, b229_31, oa5_1);
  WLG(4);
  MF(co_00, b230_00, b230_01, oa6_0);
  RDT(b231_00, bb0, 49152);
  RDT(b231_01, bb0, 51200);
  GLD(oa10_0, v16, sa0, sL + L_OB + 10240);
  MF(co_10, b230_00, b230_01, oa6_1);
  MF(co_01, b230_10, b230_11, oa6_0);
  RDT(b231_10, bb1, 49152);
  RDT(b231_11, bb1, 51200);
  MF(co_11, b230_10, b230_11, oa6_1);
  WLG(4);
  MF(co_02, b230_20, b230_21, oa6_0);
  RDT(b231_20, bb2, 49152);
  RDT(b231_21, bb2, 51200);
  GLD(oa10_1, v16, sa1, sL + L_OB + 10240);
  MF(co_12, b230_20, b230_21, oa6_1);
  MF(co_03, b230_30, b230_31, oa6_0);
  RDT(b231_30, bb3, 49152);
  RDT(b231_31, bb3, 51200);
  WVM(6);
  MF(co_13, b230_30, b230_31, oa6_1);
  WLG(4);
  MF(co_00, b231_00, b231_01, oa7_0);
  BAR(); STB(14);
  PRIO(3);
  RDT(b232_00, bb0, 0);
  RDT(b232_01, bb0, 2048);
  GLD(oa11_0, v16, sa0, sL + L_OB + 11264);
  MF(co_10, b231_00, b231_01, oa7_1);
  MF(co_01, b231_10, b231_11, oa7_0);
  RDT(b232_10, bb1, 0);
  RDT(b232_11, bb1, 2048);
  MF(co_11, b231_10, b231_11, oa7_1);
  WLG(4);
  MF(co_02, b231_20, b231_21, oa7_0);
  RDT(b232_20, bb2, 0);
  RDT(b232_21, bb2, 2048);
  GLD(oa11_1, v16, sa1, sL + L_OB + 11264);
  MF(co_12, b231_20, b231_21, oa7_1);
  MF(co_03, b231_30, b231_31, oa7_0);
  RDT(b232_30, bb3, 0);
  RDT(b232_31, bb3, 2048);
  WVM(6);
  MF(co_13, b231_30, b231_31, oa7_1);
  WLG(4);
  MF(co_00, b232_00, b232_01, oa8_0);
  RDT(b233_00, bb0, 4096);
  RDT(b233_01, bb0, 6144);
  GLD(oa12_0, v16, sa0, sL + L_OB + 12288);
  MF(co_10, b232_00, b232_01, oa8_1);
  MF(co_01, b232_10, b232_11, oa8_0);
  RDT(b233_10, bb1, 4096);
  RDT(b233_11, bb1, 6144);
  MF(co_11, b232_10, b232_11, oa8_1);
  WLG(4);
  MF(co_02, b232_20, b232_21, oa8_0);
  RDT(b233_20, bb2, 4096);
  RDT(b233_21, bb2, 6144);
  GLD(oa12_1, v16, sa1, sL + L_OB + 12288);
  MF(co_12, b232_20, b232_21, oa8_1);
  MF(co_03, b232_30, b232_31, oa8_0);
  RDT(b233_30, bb3, 4096);
  RDT(b233_31, bb3, 6144);
  WVM(6);
  MF(co_13, b232_30, b232_31, oa8_1);
  WLG(4);
  MF(co_00, b233_00, b233_01, oa9_0);
  PRIO(2);
  RDT(b234_00, bb0, 8192);
  RDT(b234_01, bb0, 10240);
  GLD(oa13_0, v16, sa0, sL + L_OB + 13312);
  MF(co_10, b233_00, b233_01, oa9_1);
  MF(co_01, b233_10, b233_11, oa9_0);
  RDT(b234_10, bb1, 8192);
  RDT(b234_11, bb1, 10240);
  MF(co_11, b233_10, b233_11, oa9_1);
  WLG(4);
  MF(co_02, b233_20, b233_21, oa9_0);
  RDT(b234_20, bb2, 8192);
  RDT(b234_21, bb2, 10240);
  GLD(oa13_1, v16, sa1, sL + L_OB + 13312);
  MF(co_12, b233_20, b233_21, oa9_1);
  MF(co_03, b233_30, b233_31, oa9_0);
  RDT(b234_30, bb3, 8192);
  RDT(b234_31, bb3, 10240);
  WVM(6);
  MF(co_13, b233_30, b233_31, oa9_1);
  WLG(4);
  MF(co_00, b234_00, b234_01, oa10_0);
  PRIO(1);
  RDT(b235_00, bb0, 12288);
  RDT(b235_01, bb0, 14336);
  GLD(oa14_0, v16, sa0, sL + L_OB + 14336);
  MF(co_10, b234_00, b234_01, oa10_1);
  MF(co_01, b234_10, b234_11, oa10_0);
  RDT(b235_10, bb1, 12288);
  RDT(b235_11, bb1, 14336);
  MF(co_11, b234_10, b234_11, oa10_1);
  WLG(4);
  MF(co_02, b234_20, b234_21, oa10_0);
  RDT(b235_20, bb2, 12288);
  RDT(b235_21, bb2, 14336);
  GLD(oa14_1, v16, sa1, sL + L_OB + 14336);
  MF(co_12, b234_20, b234_21, oa10_1);
  PRIO(0);
  MF(co_03, b234_30, b234_31, oa10_0);
  RDT(b235_30, bb3, 12288);
  RDT(b235_31, bb3, 14336);
  WVM(6);
  MF(co_13, b234_30, b234_31, oa10_1);
  WLG(4);
  MF(co_00, b235_00, b235_01, oa11_0);
  BAR(); STB(15);
  PRIO(3);
  RDT(b236_00, bb0, 16384);
  RDT(b236_01, bb0, 18432);
  GLD(oa15_0, v16, sa0, sL + L_OB + 15360);
  MF(co_10, b235_00, b235_01, oa11_1);
  MF(co_01, b235_10, b235_11, oa11_0);
  RDT(b236_10, bb1, 16384);
  RDT(b236_11, bb1, 18432);
  MF(co_11, b235_10, b235_11, oa11_1);
  WLG(4);
  MF(co_02, b235_20, b235_21, oa11_0);
  RDT(b236_20, bb2, 16384);
  RDT(b236_21, bb2, 18432);
  GLD(oa15_1, v16, sa1, sL + L_OB + 15360);
  MF(co_12, b235_20, b235_21, oa11_1);
  MF(co_03, b235_30, b235_31, oa11_0);
  RDT(b236_30, bb3, 16384);
  RDT(b236_31, bb3, 18432);
  WVM(6);
  MF(co_13, b235_30, b235_31, oa11_1);
  WLG(4);
  MF(co_00, b236_00, b236_01, oa12_0);
  RDT(b237_00, bb0, 20480);
  RDT(b237_01, bb0, 22528);
  GLD(rx0, v16, so, sL + L_XIN + 0);
  MF(co_10, b236_00, b236_01, oa12_1);
  MF(co_01, b236_10, b236_11, oa12_0);
  RDT(b237_10, bb1, 20480);
  RDT(b237_11, bb1, 22528);
  MF(co_11, b236_10, b236_11, oa12_1);
  WLG(4);
  MF(co_02, b236_20, b236_21, oa12_0);
  RDT(b237_20, bb2, 20480);
  RDT(b237_21, bb2, 22528);
  GLD(rx1, v16, so, sL + L_XIN + 1024);
  MF(co_12, b236_20, b236_21, oa12_1);
  MF(co_03, b236_30, b236_31, oa12_0);
  RDT(b237_30, bb3, 20480);
  RDT(b237_31, bb3, 22528);
  WVM(6);
  MF(co_13, b236_30, b236_31, oa12_1);
  WLG(4);
  MF(co_00, b237_00, b237_01, oa13_0);
  PRIO(2);
  RDT(b238_00, bb0, 24576);
  RDT(b238_01, bb0, 26624);
  DMA(r1, vw1, s_1w, 229376, 40960);
  MF(co_10, b237_00, b237_01, oa13_1);
  RDT(b239_00, bb0, 28672);
  RDT(b239_01, bb0, 30720);
  MF(co_01, b237_10, b237_11, oa13_0);
  RDT(b238_10, bb1, 24576);
  RDT(b238_11, bb1, 26624);
  MF(co_11, b237_10, b237_11, oa13_1);
  RDT(b239_10, bb1, 28672);
  RDT(b239_11, bb1, 30720);
  WLG(8);
  MF(co_02, b237_20, b237_21, oa13_0);
  RDT(b238_20, bb2, 24576);
  RDT(b238_21, bb2, 26624);
  DMA(r1, vw1, s_1w, 229504, 53248);
  MF(co_12, b237_20, b237_21, oa13_1);
  RDT(b239_20, bb2, 28672);
  RDT(b239_21, bb2, 30720);
  MF(co_03, b237_30, b237_31, oa13_0);
  RDT(b238_30, bb3, 24576);
  RDT(b238_31, bb3, 26624);
  WVM(6);
  MF(co_13, b237_30, b237_31, oa13_1);
  RDT(b239_30, bb3, 28672);
  RDT(b239_31, bb3, 30720);
  WVM(4);
  WLG(8);
  MF(co_10, b238_00, b238_01, oa14_1);
  PRIO(1);
  DMA(r1, vw1, s_1w, 458752, 57344);
  MF(co_10, b239_00, b239_01, oa15_1);
  MF(co_11, b238_10, b238_11, oa14_1);
  MF(co_11, b239_10, b239_11, oa15_1);
  WLG(0);
  MF(co_12, b238_20, b238_21, oa14_1);
  DMA(r1, vw1, s_1w, 458880, 45056);
  NOP(2);
  WR(co_10, xw, 0);
  MF(co_12, b239_20, b239_21, oa15_1);
  PRIO(0);
  MF(co_13, b238_30, b238_31, oa14_1);
  NOP(0);
  WR(co_11, xw, 1024);
  MF(co_13, b239_30, b239_31, oa15_1);
  MF(co_00, b238_00, b238_01, oa14_0);
  DMA(r1, vw1, s_1w, 688128, 49152);
  NOP(1);
  WR(co_12, xw, 2048);
  MF(co_01, b238_10, b238_11, oa14_0);
  MF(co_02, b238_20, b238_21, oa14_0);
  NOP(2);
  WR(co_13, xw, 3072);
  MF(co_03, b238_30, b238_31, oa14_0);
  MF(co_00, b239_00, b239_01, oa15_0);
  DMA(r1, vw1, s_1w, 688256, 0);
  MF(co_01, b239_10, b239_11, oa15_0);
  MF(co_02, b239_20, b239_21, oa15_0);
  MF(co_03, b239_30, b239_31, oa15_0);
  PRIO(0);
  STP(5);
  WLG(0);
  BAR(); STB(16);
  RDF(co_r0, xr, 0);
  RDF(co_r1, xr, 1024);
  RDF(co_r2, xr, 2048);
  RDF(co_r3, xr, 3072);
  WVM(6);
  WLG(0);
  PIN(rx0); PIN(rx1);
  PIN(co_r0); PIN(co_00);
  PIN(co_r1); PIN(co_01);
  PIN(co_r2); PIN(co_02);
  PIN(co_r3); PIN(co_03);
  const u32x4 y0 = resid_add(co_00 + co_r0, co_01 + co_r1, rx0), y1 = resid_add(co_02 + co_r2, co_03 + co_r3, rx1);
  GST(y0, v16, so, sL + L_X1);
  GST(y1, v16, so, sL + L_X1 + 1024);
  { float s_ = 0.f, ss_ = 0.f; row_stats(y0, s_, ss_); row_stats(y1, s_, ss_);
  const u32x2 sv_ = {__float_as_uint(rows_sum(s_)), __float_as_uint(rows_sum(ss_))};
  V8(v8_); GST2(sv_, v8_, st1, sL + L_ST1); }
  STP(6);
  RDT(b240_00, bb0, 32768);
  RDT(b240_01, bb0, 34816);
  RDT(b240_10, bb1, 32768);
  RDT(b240_11, bb1, 34816);
  RDT(b240_20, bb2, 32768);
  RDT(b240_21, bb2, 34816);
  RDT(b240_30, bb3, 32768);
  RDT(b240_31, bb3, 34816);
  RDT(b241_00, bb0, 36864);
  RDT(b241_01, bb0, 38912);
  RDT(b241_10, bb1, 36864);
  RDT(b241_11, bb1, 38912);
  RDT(b241_20, bb2, 36864);
  RDT(b241_21, bb2, 38912);
  RDT(b241_30, bb3, 36864);
  RDT(b241_31, bb3, 38912);
  WLG(0);
  WVM(0);
  BAR(); STB(17);
  ARRIVE(L_CN + 2 * CNB);
  DMA(r1, vw1, s_1w, 917504, 4096);
  DMA(r1, vw1, s_1w, 917632, 8192);
  DMA(r1, vw1, s_1w, 1146880, 12288);
  DMA(r1, vw1, s_1w, 1147008, 16384);
  DMA(r1, vw1, s_1w, 1376256, 20480);
  DMA(r1, vw1, s_1w, 1376384, 24576);
  DMA(r1, vw1, s_1w, 1605632, 28672);
  DMA(r1, vw1, s_1w, 1605760, 32768);
  DMA(r1, vw1, s_1w, 1835008, 36864);
  RDT(b242_00, bb0, 40960);
  RDT(b242_01, bb0, 43008);
  RDT(b242_10, bb1, 40960);
  RDT(b242_11, bb1, 43008);
  RDT(b242_20, bb2, 40960);
  RDT(b242_21, bb2, 43008);
  RDT(b242_30, bb3, 40960);
  RDT(b242_31, bb3, 43008);
  RDT(b243_00, bb0, 53248);
  RDT(b243_01, bb0, 55296);
  RDT(b243_10, bb1, 53248);
  RDT(b243_11, bb1, 55296);
  RDT(b243_20, bb2, 53248);
  RDT(b243_21, bb2, 55296);
  RDT(b243_30, bb3, 53248);
  RDT(b243_31, bb3, 55296);
  RDT(b244_00, bb0, 57344);
  RDT(b244_01, bb0, 59392);
  RDT(b244_10, bb1, 57344);
  RDT(b244_11, bb1, 59392);
  RDT(b244_20, bb2, 57344);
  RDT(b244_21, bb2, 59392);
  RDT(b244_30, bb3, 57344);
  RDT(b244_31, bb3, 59392);
  RDT(b245_00, bb0, 45056);
  RDT(b245_01, bb0, 47104);
  RDT(b245_10, bb1, 45056);
  RDT(b245_11, bb1, 47104);
  RDT(b245_20, bb2, 45056);
  RDT(b245_21, bb2, 47104);
  RDT(b245_30, bb3, 45056);
  RDT(b245_31, bb3, 47104);
  WLG(0);
  MEET(L_CN + 2 * CNB);
  STP(22);
  BAR(); STB(18);
  STP(7);
  PRIO(3);
  GLD(fa0_0, v16, sa0, sL + L_X1 + 0);
  GLD(fa0_1, v16, sa1, sL + L_X1 + 0);
  PRIO(2);
  GLD(fa1_0, v16, sa0, sL + L_X1 + 1024);
  GLD(fa1_1, v16, sa1, sL + L_X1 + 1024);
  PRIO(1);
  GLD(fa2_0, v16, sa0, sL + L_X1 + 2048);
  GLD(fa2_1, v16, sa1, sL + L_X1 + 2048);
  PRIO(0);
  GLD(fa3_0, v16, sa0, sL + L_X1 + 3072);
  GLD(fa3_1, v16, sa1, sL + L_X1 + 3072);
  PRIO(3);
  WVM(6);
  MF0(f0_00, b240_00, b240_01, fa0_0);
  GLD(fa4_0, v16, sa0, sL + L_X1 + 4096);
  MF0(f0_10, b240_00, b240_01, fa0_1);
  MF0(f0_01, b240_10, b240_11, fa0_0);
  MF0(f0_11, b240_10, b240_11, fa0_1);
  MF0(f0_02, b240_20, b240_21, fa0_0);
  MF0(f0_12, b240_20, b240_21, fa0_1);
  GLD(fa4_1, v16, sa1, sL + L_X1 + 4096);
  MF0(f0_03, b240_30, b240_31, fa0_0);
  MF0(f0_13, b240_30, b240_31, fa0_1);
  STP(25);
  MF0(f1_00, b241_00, b241_01, fa0_0);
  MF0(f1_10, b241_00, b241_01, fa0_1);
  MF0(f1_01, b241_10, b241_11, fa0_0);
  GLD(fa5_0, v16, sa0, sL + L_X1 + 5120);
  MF0(f1_11, b241_10, b241_11, fa0_1);
  MF0(f1_02, b241_20, b241_21, fa0_0);
  MF0(f1_12, b241_20, b241_21, fa0_1);
  MF0(f1_03, b241_30, b241_31, fa0_0);
  GLD(fa5_1, v16, sa1, sL + L_X1 + 5120);
  WVM(8);
  MF0(f1_13, b241_30, b241_31, fa0_1);
  PRIO(2);
  MF(f0_00, b242_00, b242_01, fa1_0);
  MF(f0_10, b242_00, b242_01, fa1_1);
  MF(f0_01, b242_10, b242_11, fa1_0);
  MF(f0_11, b242_10, b242_11, fa1_1);
  GLD(fa6_0, v16, sa0, sL + L_X1 + 6144);
  MF(f0_02, b242_20, b242_21, fa1_0);
  MF(f0_12, b242_20, b242_21, fa1_1);
  MF(f0_03, b242_30, b242_31, fa1_0);
  MF(f0_13, b242_30, b242_31, fa1_1);
  PRIO(1);
  GLD(fa6_1, v16, sa1, sL + L_X1 + 6144);
  MF(f1_00, b243_00, b243_01, fa1_0);
  MF(f1_10, b243_00, b243_01, fa1_1);
  MF(f1_01, b243_10, b243_11, fa1_0);
  MF(f1_11, b243_10, b243_11, fa1_1);
  MF(f1_02, b243_20, b243_21, fa1_0);
  PRIO(0);
  GLD(fa7_0, v16, sa0, sL + L_X1 + 7168);
  MF(f1_12, b243_20, b243_21, fa1_1);
  MF(f1_03, b243_30, b243_31, fa1_0);
  WVM(9);
  MF(f1_13, b243_30, b243_31, fa1_1);
  MF(f0_00, b244_00, b244_01, fa2_0);
  MF(f0_10, b244_00, b244_01, fa2_1);
  GLD(fa7_1, v16, sa1, sL + L_X1 + 7168);
  MF(f0_01, b244_10, b244_11, fa2_0);
  MF(f0_11, b244_10, b244_11, fa2_1);
  MF(f0_02, b244_20, b244_21, fa2_0);
  MF(f0_12, b244_20, b244_21, fa2_1);
  DMA(r1, vw1, s_1w, 2064384, 40960);
  MF(f0_03, b244_30, b244_31, fa2_0);
  MF(f0_13, b244_30, b244_31, fa2_1);
  MF(f1_00, b245_00, b245_01, fa2_0);
  RDT(b246_00, bb0, 49152);
  RDT(b246_01, bb0, 51200);
  MF(f1_10, b245_00, b245_01, fa2_1);
  MF(f1_01, b245_10, b245_11, fa2_0);
  RDT(b246_10, bb1, 49152);
  RDT(b246_11, bb1, 51200);
  DMA(r1, vw1, s_1w, 2293760, 53248);
  MF(f1_11, b245_10, b245_11, fa2_1);
  MF(f1_02, b245_20, b245_21, fa2_0);
  RDT(b246_20, bb2, 49152);
  RDT(b246_21, bb2, 51200);
  MF(f1_12, b245_20, b245_21, fa2_1);
  MF(f1_03, b245_30, b245_31, fa2_0);
  RDT(b246_30, bb3, 49152);
  RDT(b246_31, bb3, 51200);
  WVM(10);
  MF(f1_13, b245_30, b245_31, fa2_1);
  DMA(r1, vw1, s_1w, 2523136, 57344);
  WLG(4);
  MF(f0_00, b246_00, b246_01, fa3_0);
  RDT(b247_00, bb0, 0);
  RDT(b247_01, bb0, 2048);
  MF(f0_10, b246_00, b246_01, fa3_1);
  MF(f0_01, b246_10, b246_11, fa3_0);
  RDT(b247_10, bb1, 0);
  RDT(b247_11, bb1, 2048);
  MF(f0_11, b246_10, b246_11, fa3_1);
  GLD(fa8_0, v16, sa0, sL + L_X1 + 8192);
  WLG(4);
  MF(f0_02, b246_20, b246_21, fa3_0);
  RDT(b247_20, bb2, 0);
  RDT(b247_21, bb2, 2048);
  MF(f0_12, b246_20, b246_21, fa3_1);
  MF(f0_03, b246_30, b246_31, fa3_0);
  RDT(b247_30, bb3, 0);
  RDT(b247_31, bb3, 2048);
  MF(f0_13, b246_30, b246_31, fa3_1);
  WLG(4);
  MF(f1_00, b247_00, b247_01, fa3_0);
  BAR(); STB(19);
  PRIO(3);
  RDT(b248_00, bb0, 4096);
  RDT(b248_01, bb0, 6144);
  GLD(fa8_1, v16, sa1, sL + L_X1 + 8192);
  MF(f1_10, b247_00, b247_01, fa3_1);
  MF(f1_01, b247_10, b247_11, fa3_0);
  RDT(b248_10, bb1, 4096);
  RDT(b248_11, bb1, 6144);
  MF(f1_11, b247_10, b247_11, fa3_1);
  WLG(4);
  MF(f1_02, b247_20, b247_21, fa3_0);
  RDT(b248_20, bb2, 4096);
  RDT(b248_21, bb2, 6144);
  GLD(fa9_0, v16, sa0, sL + L_X1 + 9216);
  MF(f1_12, b247_20, b247_21, fa3_1);
  MF(f1_03, b247_30, b247_31, fa3_0);
  RDT(b248_30, bb3, 4096);
  RDT(b248_31, bb3, 6144);
  WVM(12);
  MF(f1_13, b247_30, b247_31, fa3_1);
  WLG(4);
  MF(f0_00, b248_00, b248_01, fa4_0);
  RDT(b249_00, bb0, 8192);
  RDT(b249_01, bb0, 10240);
  MF(f0_10, b248_00, b248_01, fa4_1);
  GLD(fa9_1, v16, sa1, sL + L_X1 + 9216);
  MF(f0_01, b248_10, b248_11, fa4_0);
  RDT(b249_10, bb1, 8192);
  RDT(b249_11, bb1, 10240);
  MF(f0_11, b248_10, b248_11, fa4_1);
  WLG(4);
  MF(f0_02, b248_20, b248_21, fa4_0);
  RDT(b249_20, bb2, 8192);
  RDT(b249_21, bb2, 10240);
  MF(f0_12, b248_20, b248_21, fa4_1);
  MF(f0_03, b248_30, b248_31, fa4_0);
  RDT(b249_30, bb3, 8192);
  RDT(b249_31, bb3, 10240);
  GLD(fa10_0, v16, sa0, sL + L_X1 + 10240);
  MF(f0_13, b248_30, b248_31, fa4_1);
  WLG(4);
  MF(f1_00, b249_00, b249_01, fa4_0);
  PRIO(2);
  RDT(b250_00, bb0, 12288);
  RDT(b250_01, bb0, 14336);
  MF(f1_10, b249_00, b249_01, fa4_1);
  MF(f1_01, b249_10, b249_11, fa4_0);
  RDT(b250_10, bb1, 12288);
  RDT(b250_11, bb1, 14336);
  GLD(fa10_1, v16, sa1, sL + L_X1 + 10240);
  MF(f1_11, b249_10, b249_11, fa4_1);
  WLG(4);
  MF(f1_02, b249_20, b249_21, fa4_0);
  RDT(b250_20, bb2, 12288);
  RDT(b250_21, bb2, 14336);
  MF(f1_12, b249_20, b249_21, fa4_1);
  MF(f1_03, b249_30, b249_31, fa4_0);
  RDT(b250_30, bb3, 12288);
  RDT(b250_31, bb3, 14336);
  WVM(13);
  MF(f1_13, b249_30, b249_31, fa4_1);
  GLD(fa11_0, v16, sa0, sL + L_X1 + 11264);
  WLG(4);
  MF(f0_00, b250_00, b250_01, fa5_0);
  PRIO(1);
  RDT(b251_00, bb0, 16384);
  RDT(b251_01, bb0, 18432);
  MF(f0_10, b250_00, b250_01, fa5_1);
  MF(f0_01, b250_10, b250_11, fa5_0);
  RDT(b251_10, bb1, 16384);
  RDT(b251_11, bb1, 18432);
  MF(f0_11, b250_10, b250_11, fa5_1);
  GLD(fa11_1, v16, sa1, sL + L_X1 + 11264);
  WLG(4);
  MF(f0_02, b250_20, b250_21, fa5_0);
  RDT(b251_20, bb2, 16384);
  RDT(b251_21, bb2, 18432);
  MF(f0_12, b250_20, b250_21, fa5_1);
  PRIO(0);
  MF(f0_03, b250_30, b250_31, fa5_0);
  RDT(b251_30, bb3, 16384);
  RDT(b251_31, bb3, 18432);
  MF(f0_13, b250_30, b250_31, fa5_1);
  WLG(4);
  MF(f1_00, b251_00, b251_01, fa5_0);
  BAR(); STB(20);
  PRIO(3);
  RDT(b252_00, bb0, 20480);
  RDT(b252_01, bb0, 22528);
  DMA(r1, vw1, s_1w, 2752512, 45056);
  MF(f1_10, b251_00, b251_01, fa5_1);
  MF(f1_01, b251_10, b251_11, fa5_0);
  RDT(b252_10, bb1, 20480);
  RDT(b252_11, bb1, 22528);
  MF(f1_11, b251_10, b251_11, fa5_1);
  WLG(4);
  MF(f1_02, b251_20, b251_21, fa5_0);
  RDT(b252_20, bb2, 20480);
  RDT(b252_21, bb2, 22528);
  MF(f1_12, b251_20, b251_21, fa5_1);
  DMA(r1, vw1, s_1w, 2981888, 49152);
  MF(f1_03, b251_30, b251_31, fa5_0);
  RDT(b252_30, bb3, 20480);
  RDT(b252_31, bb3, 22528);
  WVM(15);
  MF(f1_13, b251_30, b251_31, fa5_1);
  WLG(4);
  MF(f0_00, b252_00, b252_01, fa6_0);
  RDT(b253_00, bb0, 24576);
  RDT(b253_01, bb0, 26624);
  MF(f0_10, b252_00, b252_01, fa6_1);
  DMA(r1, vw1, s_1w, 3211264, 0);
  MF(f0_01, b252_10, b252_11, fa6_0);
  RDT(b253_10, bb1, 24576);
  RDT(b253_11, bb1, 26624);
  MF(f0_11, b252_10, b252_11, fa6_1);
  WLG(4);
  MF(f0_02, b252_20, b252_21, fa6_0);
  RDT(b253_20, bb2, 24576);
  RDT(b253_21, bb2, 26624);
  MF(f0_12, b252_20, b252_21, fa6_1);
  MF(f0_03, b252_30, b252_31, fa6_0);
  RDT(b253_30, bb3, 24576);
  RDT(b253_31, bb3, 26624);
  DMA(r1, vw1, s_1w, 3440640, 4096);
  MF(f0_13, b252_30, b252_31, fa6_1);
  WLG(4);
  MF(f1_00, b253_00, b253_01, fa6_0);
  PRIO(2);
  RDT(b254_00, bb0, 28672);
  RDT(b254_01, bb0, 30720);
  MF(f1_10, b253_00, b253_01, fa6_1);
  MF(f1_01, b253_10, b253_11, fa6_0);
  RDT(b254_10, bb1, 28672);
  RDT(b254_11, bb1, 30720);
  GLD(fa12_0, v16, sa0, sL + L_X1 + 12288);
  MF(f1_11, b253_10, b253_11, fa6_1);
  WLG(4);
  MF(f1_02, b253_20, b253_21, fa6_0);
  RDT(b254_20, bb2, 28672);
  RDT(b254_21, bb2, 30720);
  MF(f1_12, b253_20, b253_21, fa6_1);
  MF(f1_03, b253_30, b253_31, fa6_0);
  RDT(b254_30, bb3, 28672);
  RDT(b254_31, bb3, 30720);
  WVM(16);
  MF(f1_13, b253_30, b253_31, fa6_1);
  GLD(fa12_1, v16, sa1, sL + L_X1 + 12288);
  WLG(4);
  MF(f0_00, b254_00, b254_01, fa7_0);
  PRIO(1);
  RDT(b255_00, bb0, 32768);
  RDT(b255_01, bb0, 34816);
  MF(f0_10, b254_00, b254_01, fa7_1);
  MF(f0_01, b254_10, b254_11, fa7_0);
  RDT(b255_10, bb1, 32768);
  RDT(b255_11, bb1, 34816);
  MF(f0_11, b254_10, b254_11, fa7_1);
  WLG(4);
  MF(f0_02, b254_20, b254_21, fa7_0);
  RDT(b255_20, bb2, 32768);
  RDT(b255_21, bb2, 34816);
  GLD(fa13_0, v16, sa0, sL + L_X1 + 13312);
  MF(f0_12, b254_20, b254_21, fa7_1);
  PRIO(0);
  MF(f0_03, b254_30, b254_31, fa7_0);
  RDT(b255_30, bb3, 32768);
  RDT(b255_31, bb3, 34816);
  MF(f0_13, b254_30, b254_31, fa7_1);
  WLG(4);
  MF(f1_00, b255_00, b255_01, fa7_0);
  WVM(15);
  BAR(); STB(21);
  PRIO(3);
  RDT(b256_00, bb0, 36864);
  RDT(b256_01, bb0, 38912);
  GLD(fa13_1, v16, sa1, sL + L_X1 + 13312);
  MF(f1_10, b255_00, b255_01, fa7_1);
  MF(f1_01, b255_10, b255_11, fa7_0);
  RDT(b256_10, bb1, 36864);
  RDT(b256_11, bb1, 38912);
  MF(f1_11, b255_10, b255_11, fa7_1);
  WLG(4);
  MF(f1_02, b255_20, b255_21, fa7_0);
  RDT(b256_20, bb2, 36864);
  RDT(b256_21, bb2, 38912);
  MF(f1_12, b255_20, b255_21, fa7_1);
  GLD(fa14_0, v16, sa0, sL + L_X1 + 14336);
  V8(v8f);
  MF(f1_03, b255_30, b255_31, fa7_0);
  RDT(b256_30, bb3, 36864);
  RDT(b256_31, bb3, 38912);
  WVM(15);
  MF(f1_13, b255_30, b255_31, fa7_1);
  STP(14);
  WLG(4);
  MF(f0_00, b256_00, b256_01, fa8_0);
  RDT(b257_00, bb0, 40960);
  RDT(b257_01, bb0, 43008);
  MF(f0_10, b256_00, b256_01, fa8_1);
  GLD(fa14_1, v16, sa1, sL + L_X1 + 14336);
  MF(f0_01, b256_10, b256_11, fa8_0);
  RDT(b257_10, bb1, 40960);
  RDT(b257_11, bb1, 43008);
  MF(f0_11, b256_10, b256_11, fa8_1);
  WLG(4);
  MF(f0_02, b256_20, b256_21, fa8_0);
  RDT(b257_20, bb2, 40960);
  RDT(b257_21, bb2, 43008);
  MF(f0_12, b256_20, b256_21, fa8_1);
  MF(f0_03, b256_30, b256_31, fa8_0);
  RDT(b257_30, bb3, 40960);
  RDT(b257_31, bb3, 43008);
  GL8(stf0, v8f, sst, sL + L_ST1 + 0);
  GL8(stf1, v8f, sst, sL + L_ST1 + 512);
  WVM(16);
  MF(f0_13, b256_30, b256_31, fa8_1);
  WLG(4);
  MF(f0_00, b257_00, b257_01, fa9_0);
  PRIO(2);
  RDT(b258_00, bb0, 53248);
  RDT(b258_01, bb0, 55296);
  MF(f0_10, b257_00, b257_01, fa9_1);
  MF(f0_01, b257_10, b257_11, fa9_0);
  RDT(b258_10, bb1, 53248);
  RDT(b258_11, bb1, 55296);
  MF(f0_11, b257_10, b257_11, fa9_1);
  GL8(stf2, v8f, sst, sL + L_ST1 + 1024);
  GL8(stf3, v8f, sst, sL + L_ST1 + 1536);
  WLG(4);
  MF(f0_02, b257_20, b257_21, fa9_0);
  RDT(b258_20, bb2, 53248);
  RDT(b258_21, bb2, 55296);
  MF(f0_12, b257_20, b257_21, fa9_1);
  MF(f0_03, b257_30, b257_31, fa9_0);
  RDT(b258_30, bb3, 53248);
  RDT(b258_31, bb3, 55296);
  WVM(16);
  MF(f0_13, b257_30, b257_31, fa9_1);
  GLD(fa15_0, v16, sa0, sL + L_X1 + 15360);
  WLG(4);
  MF(f0_00, b258_00, b258_01, fa10_0);
  PRIO(1);
  RDT(b259_00, bb0, 57344);
  RDT(b259_01, bb0, 59392);
  MF(f0_10, b258_00, b258_01, fa10_1);
  MF(f0_01, b258_10, b258_11, fa10_0);
  RDT(b259_10, bb1, 57344);
  RDT(b259_11, bb1, 59392);
  MF(f0_11, b258_10, b258_11, fa10_1);
  WLG(4);
  MF(f0_02, b258_20, b258_21, fa10_0);
  RDT(b259_20, bb2, 57344);
  RDT(b259_21, bb2, 59392);
  GLD(fa15_1, v16, sa1, sL + L_X1 + 15360);
  MF(f0_12, b258_20, b258_21, fa10_1);
  PRIO(0);
  MF(f0_03, b258_30, b258_31, fa10_0);
  RDT(b259_30, bb3, 57344);
  RDT(b259_31, bb3, 59392);
  GMK(v16, shn, sL + L_HB);
  WVM(17);
  MF(f0_13, b258_30, b258_31, fa10_1);
  WLG(4);
  MF(f0_00, b259_00, b259_01, fa11_0);
  WVM(13);
  BAR(); STB(22);
  PRIO(3);
  RDT(b260_00, bb0, 45056);
  RDT(b260_01, bb0, 47104);
  MF(f0_10, b259_00, b259_01, fa11_1);
  MF(f0_01, b259_10, b259_11, fa11_0);
  RDT(b260_10, bb1, 45056);
  RDT(b260_11, bb1, 47104);
  MF(f0_11, b259_10, b259_11, fa11_1);
  DMA(r1, vw1, s_1w, 1835136, 8192);
  WLG(4);
  MF(f0_02, b259_20, b259_21, fa11_0);
  RDT(b260_20, bb2, 45056);
  RDT(b260_21, bb2, 47104);
  MF(f0_12, b259_20, b259_21, fa11_1);
  MF(f0_03, b259_30, b259_31, fa11_0);
  RDT(b260_30, bb3, 45056);
  RDT(b260_31, bb3, 47104);
  WVM(12);
  MF(f0_13, b259_30, b259_31, fa11_1);
  WLG(4);
  MF(f0_00, b260_00, b260_01, fa12_0);
  RDT(b261_00, bb0, 49152);
  RDT(b261_01, bb0, 51200);
  DMA(r1, vw1, s_1w, 2064512, 12288);
  MF(f0_10, b260_00, b260_01, fa12_1);
  MF(f0_01, b260_10, b260_11, fa12_0);
  RDT(b261_10, bb1, 49152);
  RDT(b261_11, bb1, 51200);
  MF(f0_11, b260_10, b260_11, fa12_1);
  WLG(4);
  MF(f0_02, b260_20, b260_21, fa12_0);
  RDT(b261_20, bb2, 49152);
  RDT(b261_21, bb2, 51200);
  MF(f0_12, b260_20, b260_21, fa12_1);
  DMA(r1, vw1, s_1w, 2293888, 16384);
  MF(f0_03, b260_30, b260_31, fa12_0);
  RDT(b261_30, bb3, 49152);
  RDT(b261_31, bb3, 51200);
  WVM(12);
  MF(f0_13, b260_30, b260_31, fa12_1);
  WLG(4);
  MF(f0_00, b261_00, b261_01, fa13_0);
  PRIO(2);
  RDT(b262_00, bb0, 0);
  RDT(b262_01, bb0, 2048);
  MF(f0_10, b261_00, b261_01, fa13_1);
  RDT(b263_00, bb0, 4096);
  RDT(b263_01, bb0, 6144);
  DMA(r1, vw1, s_1w, 2523264, 20480);
  MF(f0_01, b261_10, b261_11, fa13_0);
  RDT(b262_10, bb1, 0);
  RDT(b262_11, bb1, 2048);
  MF(f0_11, b261_10, b261_11, fa13_1);
  RDT(b263_10, bb1, 4096);
  RDT(b263_11, bb1, 6144);
  WLG(8);
  MF(f0_02, b261_20, b261_21, fa13_0);
  RDT(b262_20, bb2, 0);
  RDT(b262_21, bb2, 2048);
  MF(f0_12, b261_20, b261_21, fa13_1);
  RDT(b263_20, bb2, 4096);
  RDT(b263_21, bb2, 6144);
  MF(f0_03, b261_30, b261_31, fa13_0);
  RDT(b262_30, bb3, 0);
  RDT(b262_31, bb3, 2048);
  DMA(r1, vw1, s_1w, 2752640, 24576);
  WVM(12);
  MF(f0_13, b261_30, b261_31, fa13_1);
  RDT(b263_30, bb3, 4096);
  RDT(b263_31, bb3, 6144);
  WVM(6);
  WLG(8);
  MF(f0_10, b262_00, b262_01, fa14_1);
  PRIO(1);
  MF(f0_10, b263_00, b263_01, fa15_1);
  MF(f0_11, b262_10, b262_11, fa14_1);
  DMA(r1, vw1, s_1w, 2982016, 28672);
  MF(f0_11, b263_10, b263_11, fa15_1);
  WLG(0);
  MF(f0_12, b262_20, b262_21, fa14_1);
  PIN(stf0); PIN(stf1); PIN(stf2); PIN(stf3); float sf = (stf0[0] + stf1[0]) + (stf2[0] + stf3[0]), tf = (stf0[1] + stf1[1]) + (stf2[1] + stf3[1]); PIN(sf); PIN(tf);
  NOP(2);
  WR(f0_10, xw, 0);
  MF(f0_12, b263_20, b263_21, fa15_1);
  PRIO(0);
  sf = half_sum<16>(sf); tf = half_sum<16>(tf); PIN(sf); PIN(tf);
  MF(f0_13, b262_30, b262_31, fa14_1);
  sf = half_sum<32>(sf) * (1.0f / D_MODEL); tf = half_sum<32>(tf); PIN(sf); PIN(tf);
  NOP(1);
  WR(f0_11, xw, 1024);
  MF(f0_13, b263_30, b263_31, fa15_1);
  DMA(r1, vw1, s_1w, 3211392, 32768);
  tf = __builtin_amdgcn_rsqf(fmaxf(tf * (1.0f / D_MODEL) - sf * sf, 0.f) + 1e-5f); PIN(tf);
  MF(f0_00, b262_00, b262_01, fa14_0);
  sf = -sf * tf; PIN(sf); const f32x2 rsf = {tf, tf}, nmf = {sf, sf};
  NOP(1);
  WR(f0_12, xw, 2048);
  MF(f0_01, b262_10, b262_11, fa14_0);
  MF(f0_02, b262_20, b262_21, fa14_0);
  NOP(2);
  WR(f0_13, xw, 3072);
  MF(f0_03, b262_30, b262_31, fa14_0);
  WVM(3);
  WLG(0);
  BAR(); STB(23);
  PRIO(3);
  RDT(b264_00, bb0, 8192);
  RDT(b264_01, bb0, 10240);
  MF(f0_00, b263_00, b263_01, fa15_0);
  RDT(b264_10, bb1, 8192);
  RDT(b264_11, bb1, 10240);
  DMA(r1, vw1, s_1w, 3440768, 36864);
  MF(f0_01, b263_10, b263_11, fa15_0);
  RDT(b264_20, bb2, 8192);
  RDT(b264_21, bb2, 10240);
  MF(f0_02, b263_20, b263_21, fa15_0);
  RDT(b264_30, bb3, 8192);
  RDT(b264_31, bb3, 10240);
  MF(f0_03, b263_30, b263_31, fa15_0);
  WLG(4);
  MF(f1_00, b264_00, b264_01, fa8_0);
  RDT(b265_00, bb0, 12288);
  RDT(b265_01, bb0, 14336);
  DMA(r1, vw1, s_1w, 256, 40960);
  RDF(f0_c0, tb, 768);
  RDF(f0_b0, tb, 2816);
  MF(f1_10, b264_00, b264_01, fa8_1);
  MF(f1_01, b264_10, b264_11, fa8_0);
  RDT(b265_10, bb1, 12288);
  RDT(b265_11, bb1, 14336);
  MF(f1_11, b264_10, b264_11, fa8_1);
  f32x2 f0_t00, f0_t01;
  WLG(2);
  FMA(f0_t00[0], f0_c0[0], nmf[0], f0_b0[0]);
  FMA(f0_t00[1], f0_c0[1], nmf[0], f0_b0[1]);
  MF(f1_02, b264_20, b264_21, fa8_0);
  RDT(b265_20, bb2, 12288);
  RDT(b265_21, bb2, 14336);
  FMA(f0_t01[0], f0_c0[2], nmf[0], f0_b0[2]);
  FMA(f0_t01[1], f0_c0[3], nmf[0], f0_b0[3]);
  MF(f1_12, b264_20, b264_21, fa8_1);
  DMA(r1, vw1, s_1w, 229632, 53248);
  RDF(f0_r0, xr, 0);
  RDF(f0_r1, xr, 1024);
  FMA(f0_t00[0], f0_00[0], rsf[0], f0_t00[0]);
  MF(f1_03, b264_30, b264_31, fa8_0);
  RDT(b265_30, bb3, 12288);
  RDT(b265_31, bb3, 14336);
  FMA(f0_t00[1], f0_00[1], rsf[0], f0_t00[1]);
  FMA(f0_t01[0], f0_00[2], rsf[0], f0_t01[0]);
  MF(f1_13, b264_30, b264_31, fa8_1);
  FMA(f0_t01[1], f0_00[3], rsf[0], f0_t01[1]);
  RDF(f0_c1, tb, 832);
  RDF(f0_b1, tb, 2880);
  MF(f1_00, b265_00, b265_01, fa9_0);
  RDT(b266_00, bb0, 16384);
  RDT(b266_01, bb0, 18432);
  MF(f1_10, b265_00, b265_01, fa9_1);
  DMA(r1, vw1, s_1w, 459008, 57344);
  WLG(7);
  FMA(f0_t00[0], f0_r0[0], rsf[0], f0_t00[0]);
  FMA(f0_t00[1], f0_r0[1], rsf[0], f0_t00[1]);
  MF(f1_01, b265_10, b265_11, fa9_0);
  RDT(b266_10, bb1, 16384);
  RDT(b266_11, bb1, 18432);
  FMA(f0_t01[0], f0_r0[2], rsf[0], f0_t01[0]);
  FMA(f0_t01[1], f0_r0[3], rsf[0], f0_t01[1]);
  MF(f1_11, b265_10, b265_11, fa9_1);
  PRIO(2);
  RDF(f0_r2, xr, 2048);
  RDF(f0_r3, xr, 3072);
  SQA(f0_t00[0], f0_t00[0]);
  MF(f1_02, b265_20, b265_21, fa9_0);
  RDT(b266_20, bb2, 16384);
  RDT(b266_21, bb2, 18432);
  SQA(f0_t00[1], f0_t00[1]);
  SQA(f0_t01[0], f0_t01[0]);
  MF(f1_12, b265_20, b265_21, fa9_1);
  SQA(f0_t01[1], f0_t01[1]);
  unsigned f0_p00; CVT(f0_p00, f0_t00[0], f0_t00[1]);
  WLG(6);
  MF(f1_03, b265_30, b265_31, fa9_0);
  RDT(b266_30, bb3, 16384);
  RDT(b266_31, bb3, 18432);
  DMA(r1, vw1, s_1w, 688384, 45056);
  unsigned f0_p01; CVT(f0_p01, f0_t01[0], f0_t01[1]);
  RLU(f0_p00);
  MF(f1_13, b265_30, b265_31, fa9_1);
  RLU(f0_p01);
  f32x2 f0_t10, f0_t11;
  FMA(f0_t10[0], f0_c1[0], nmf[0], f0_b1[0]);
  MF(f1_00, b266_00, b266_01, fa10_0);
  RDT(b267_00, bb0, 20480);
  RDT(b267_01, bb0, 22528);
  FMA(f0_t10[1], f0_c1[1], nmf[0], f0_b1[1]);
  FMA(f0_t11[0], f0_c1[2], nmf[0], f0_b1[2]);
  MF(f1_10, b266_00, b266_01, fa10_1);
  FMA(f0_t11[1], f0_c1[3], nmf[0], f0_b1[3]);
  FMA(f0_t10[0], f0_01[0], rsf[0], f0_t10[0]);
  WLG(4);
  MF(f1_01, b266_10, b266_11, fa10_0);
  RDT(b267_10, bb1, 20480);
  RDT(b267_11, bb1, 22528);
  FMA(f0_t10[1], f0_01[1], rsf[0], f0_t10[1]);
  FMA(f0_t11[0], f0_01[2], rsf[0], f0_t11[0]);
  MF(f1_11, b266_10, b266_11, fa10_1);
  PRIO(1);
  DMA(r1, vw1, s_1w, 917760, 49152);
  FMA(f0_t11[1], f0_01[3], rsf[0], f0_t11[1]);
  RDF(f0_c2, tb, 896);
  RDF(f0_b2, tb, 2944);
  MF(f1_02, b266_20, b266_21, fa10_0);
  RDT(b267_20, bb2, 20480);
  RDT(b267_21, bb2, 22528);
  MF(f1_12, b266_20, b266_21, fa10_1);
  FMA(f0_t10[0], f0_r1[0], rsf[0], f0_t10[0]);
  FMA(f0_t10[1], f0_r1[1], rsf[0], f0_t10[1]);
  WLG(6);
  MF(f1_03, b266_30, b266_31, fa10_0);
  RDT(b267_30, bb3, 20480);
  RDT(b267_31, bb3, 22528);
  FMA(f0_t11[0], f0_r1[2], rsf[0], f0_t11[0]);
  FMA(f0_t11[1], f0_r1[3], rsf[0], f0_t11[1]);
  MF(f1_13, b266_30, b266_31, fa10_1);
  DMA(r1, vw1, s_1w, 1147136, 0);
  SQA(f0_t10[0], f0_t10[0]);
  SQA(f0_t10[1], f0_t10[1]);
  MF(f1_00, b267_00, b267_01, fa11_0);
  PRIO(0);
  WVM(6);
  BAR(); STB(24);
  PRIO(3);
  RDT(b268_00, bb0, 24576);
  RDT(b268_01, bb0, 26624);
  SQA(f0_t11[0], f0_t11[0]);
  SQA(f0_t11[1], f0_t11[1]);
  MF(f1_10, b267_00, b267_01, fa11_1);
  unsigned f0_p10; CVT(f0_p10, f0_t10[0], f0_t10[1]);
  unsigned f0_p11; CVT(f0_p11, f0_t11[0], f0_t11[1]);
  WLG(4);
  MF(f1_01, b267_10, b267_11, fa11_0);
  RDT(b268_10, bb1, 24576);
  RDT(b268_11, bb1, 26624);
  RLU(f0_p10);
  RLU(f0_p11);
  MF(f1_11, b267_10, b267_11, fa11_1);
  const u32x4 f0_o0 = {f0_p00, f0_p01, f0_p10, f0_p11};
  GST(f0_o0, v16, shw, sL + L_HB + 0);
  f32x2 f0_t20, f0_t21;
  FMA(f0_t20[0], f0_c2[0], nmf[0], f0_b2[0]);
  MF(f1_02, b267_20, b267_21, fa11_0);
  RDT(b268_20, bb2, 24576);
  RDT(b268_21, bb2, 26624);
  FMA(f0_t20[1], f0_c2[1], nmf[0], f0_b2[1]);
  FMA(f0_t21[0], f0_c2[2], nmf[0], f0_b2[2]);
  MF(f1_12, b267_20, b267_21, fa11_1);
  FMA(f0_t21[1], f0_c2[3], nmf[0], f0_b2[3]);
  FMA(f0_t20[0], f0_02[0], rsf[0], f0_t20[0]);
  WLG(4);
  MF(f1_03, b267_30, b267_31, fa11_0);
  RDT(b268_30, bb3, 24576);
  RDT(b268_31, bb3, 26624);
  FMA(f0_t20[1], f0_02[1], rsf[0], f0_t20[1]);
  FMA(f0_t21[0], f0_02[2], rsf[0], f0_t21[0]);
  MF(f1_13, b267_30, b267_31, fa11_1);
  FMA(f0_t21[1], f0_02[3], rsf[0], f0_t21[1]);
  RDF(f0_c3, tb, 960);
  RDF(f0_b3, tb, 3008);
  MF(f1_00, b268_00, b268_01, fa12_0);
  RDT(b269_00, bb0, 28672);
  RDT(b269_01, bb0, 30720);
  DMA(r1, vw1, s_1w, 1376512, 4096);
  MF(f1_10, b268_00, b268_01, fa12_1);
  FMA(f0_t20[0], f0_r2[0], rsf[0], f0_t20[0]);
  FMA(f0_t20[1], f0_r2[1], rsf[0], f0_t20[1]);
  WLG(6);
  MF(f1_01, b268_10, b268_11, fa12_0);
  RDT(b269_10, bb1, 28672);
  RDT(b269_11, bb1, 30720);
  FMA(f0_t21[0], f0_r2[2], rsf[0], f0_t21[0]);
  FMA(f0_t21[1], f0_r2[3], rsf[0], f0_t21[1]);
  MF(f1_11, b268_10, b268_11, fa12_1);
  SQA(f0_t20[0], f0_t20[0]);
  SQA(f0_t20[1], f0_t20[1]);
  MF(f1_02, b268_20, b268_21, fa12_0);
  RDT(b269_20, bb2, 28672);
  RDT(b269_21, bb2, 30720);
  SQA(f0_t21[0], f0_t21[0]);
  SQA(f0_t21[1], f0_t21[1]);
  MF(f1_12, b268_20, b268_21, fa12_1);
  DMA(r1, vw1, s_1w, 1605888, 8192);
  unsigned f0_p20; CVT(f0_p20, f0_t20[0], f0_t20[1]);
  unsigned f0_p21; CVT(f0_p21, f0_t21[0], f0_t21[1]);
  WLG(4);
  MF(f1_03, b268_30, b268_31, fa12_0);
  RDT(b269_30, bb3, 28672);
  RDT(b269_31, bb3, 30720);
  RLU(f0_p20);
  RLU(f0_p21);
  MF(f1_13, b268_30, b268_31, fa12_1);
  f32x2 f0_t30, f0_t31;
  FMA(f0_t30[0], f0_c3[0], nmf[0], f0_b3[0]);
  FMA(f0_t30[1], f0_c3[1], nmf[0], f0_b3[1]);
  MF(f1_00, b269_00, b269_01, fa13_0);
  PRIO(2);
  RDT(b270_00, bb0, 32768);
  RDT(b270_01, bb0, 34816);
  FMA(f0_t31[0], f0_c3[2], nmf[0], f0_b3[2]);
  FMA(f0_t31[1], f0_c3[3], nmf[0], f0_b3[3]);
  MF(f1_10, b269_00, b269_01, fa13_1);
  RDT(b271_00, bb0, 36864);
  RDT(b271_01, bb0, 38912);
  FMA(f0_t30[0], f0_03[0], rsf[0], f0_t30[0]);
  FMA(f0_t30[1], f0_03[1], rsf[0], f0_t30[1]);
  WLG(6);
  MF(f1_01, b269_10, b269_11, fa13_0);
  RDT(b270_10, bb1, 32768);
  RDT(b270_11, bb1, 34816);
  DMA(r1, vw1, s_1w, 1835264, 12288);
  FMA(f0_t31[0], f0_03[2], rsf[0], f0_t31[0]);
  FMA(f0_t31[1], f0_03[3], rsf[0], f0_t31[1]);
  MF(f1_11, b269_10, b269_11, fa13_1);
  RDT(b271_10, bb1, 36864);
  RDT(b271_11, bb1, 38912);
  MF(f1_02, b269_20, b269_21, fa13_0);
  RDT(b270_20, bb2, 32768);
  RDT(b270_21, bb2, 34816);
  FMA(f0_t30[0], f0_r3[0], rsf[0], f0_t30[0]);
  FMA(f0_t30[1], f0_r3[1], rsf[0], f0_t30[1]);
  VWN(vw1n, 7168);
  MF(f1_12, b269_20, b269_21, fa13_1);
  RDT(b271_20, bb2, 36864);
  RDT(b271_21, bb2, 38912);
  FMA(f0_t31[0], f0_r3[2], rsf[0], f0_t31[0]);
  FMA(f0_t31[1], f0_r3[3], rsf[0], f0_t31[1]);
  WLG(8);
  MF(f1_03, b269_30, b269_31, fa13_0);
  RDT(b270_30, bb3, 32768);
  RDT(b270_31, bb3, 34816);
  DMA(r1, vw1, s_1w, 2064640, 16384);
  SQA(f0_t30[0], f0_t30[0]);
  SQA(f0_t30[1], f0_t30[1]);
  MF(f1_13, b269_30, b269_31, fa13_1);
  RDT(b271_30, bb3, 36864);
  RDT(b271_31, bb3, 38912);
  SQA(f0_t31[0], f0_t31[0]);
  SQA(f0_t31[1], f0_t31[1]);
  MF(f1_10, b270_00, b270_01, fa14_1);
  PRIO(1);
  unsigned f0_p30; CVT(f0_p30, f0_t30[0], f0_t30[1]);
  unsigned f0_p31; CVT(f0_p31, f0_t31[0], f0_t31[1]);
  MF(f1_10, b271_00, b271_01, fa15_1);
  RLU(f0_p30);
  RLU(f0_p31);
  WLG(4);
  MF(f1_11, b270_10, b270_11, fa14_1);
  const u32x4 f0_o1 = {f0_p20, f0_p21, f0_p30, f0_p31};
  GST(f0_o1, v16, shw, sL + L_HB + 1024);
  MF(f1_11, b271_10, b271_11, fa15_1);
  MF(f1_12, b270_20, b270_21, fa14_1);
  NOP(1);
  WR(f1_10, xw, 0);
  MF(f1_12, b271_20, b271_21, fa15_1);
  PRIO(0);
  WLG(1);
  MF(f1_13, b270_30, b270_31, fa14_1);
  NOP(1);
  WR(f1_11, xw, 1024);
  MF(f1_13, b271_30, b271_31, fa15_1);
  MF(f1_00, b270_00, b270_01, fa14_0);
  NOP(1);
  WR(f1_12, xw, 2048);
  MF(f1_01, b270_10, b270_11, fa14_0);
  MF(f1_02, b270_20, b270_21, fa14_0);
  NOP(3);
  WR(f1_13, xw, 3072);
  MF(f1_03, b270_30, b270_31, fa14_0);
  WVM(8);
  WLG(0);
  BAR(); STB(25);
  PRIO(3);
  RDT(b272_00, bb0, 40960);
  RDT(b272_01, bb0, 43008);
  DMA(r1, vw1, s_1w, 2294016, 20480);
  MF(f1_00, b271_00, b271_01, fa15_0);
  RDT(b272_10, bb1, 40960);
  RDT(b272_11, bb1, 43008);
  MF(f1_01, b271_10, b271_11, fa15_0);
  RDT(b272_20, bb2, 40960);
  RDT(b272_21, bb2, 43008);
  MF(f1_02, b271_20, b271_21, fa15_0);
  RDT(b272_30, bb3, 40960);
  RDT(b272_31, bb3, 43008);
  MF(f1_03, b271_30, b271_31, fa15_0);
  DMA(r1, vw1, s_1w, 2523392, 24576);
  STP(15);
  WLG(4);
  MF0(f2_00, b272_00, b272_01, fa0_0);
  RDT(b273_00, bb0, 53248);
  RDT(b273_01, bb0, 55296);
  RDF(f1_c0, tb, 1024);
  RDF(f1_b0, tb, 3072);
  MF0(f2_10, b272_00, b272_01, fa0_1);
  MF0(f2_01, b272_10, b272_11, fa0_0);
  RDT(b273_10, bb1, 53248);
  RDT(b273_11, bb1, 55296);
  MF0(f2_11, b272_10, b272_11, fa0_1);
  f32x2 f1_t00, f1_t01;
  WLG(2);
  FMA(f1_t00[0], f1_c0[0], nmf[0], f1_b0[0]);
  FMA(f1_t00[1], f1_c0[1], nmf[0], f1_b0[1]);
  MF0(f2_02, b272_20, b272_21, fa0_0);
  RDT(b273_20, bb2, 53248);
  RDT(b273_21, bb2, 55296);
  DMA(r1, vw1, s_1w, 2752768, 28672);
  FMA(f1_t01[0], f1_c0[2], nmf[0], f1_b0[2]);
  FMA(f1_t01[1], f1_c0[3], nmf[0], f1_b0[3]);
  MF0(f2_12, b272_20, b272_21, fa0_1);
  RDF(f1_r0, xr, 0);
  RDF(f1_r1, xr, 1024);
  FMA(f1_t00[0], f1_00[0], rsf[0], f1_t00[0]);
  MF0(f2_03, b272_30, b272_31, fa0_0);
  RDT(b273_30, bb3, 53248);
  RDT(b273_31, bb3, 55296);
  FMA(f1_t00[1], f1_00[1], rsf[0], f1_t00[1]);
  FMA(f1_t01[0], f1_00[2], rsf[0], f1_t01[0]);
  MF0(f2_13, b272_30, b272_31, fa0_1);
  FMA(f1_t01[1], f1_00[3], rsf[0], f1_t01[1]);
  RDF(f1_c1, tb, 1088);
  RDF(f1_b1, tb, 3136);
  MF(f2_00, b273_00, b273_01, fa1_0);
  RDT(b274_00, bb0, 57344);
  RDT(b274_01, bb0, 59392);
  DMA(r1, vw1, s_1w, 2982144, 32768);
  MF(f2_10, b273_00, b273_01, fa1_1);
  WLG(7);
  FMA(f1_t00[0], f1_r0[0], rsf[0], f1_t00[0]);
  FMA(f1_t00[1], f1_r0[1], rsf[0], f1_t00[1]);
  MF(f2_01, b273_10, b273_11, fa1_0);
  RDT(b274_10, bb1, 57344);
  RDT(b274_11, bb1, 59392);
  FMA(f1_t01[0], f1_r0[2], rsf[0], f1_t01[0]);
  FMA(f1_t01[1], f1_r0[3], rsf[0], f1_t01[1]);
  MF(f2_11, b273_10, b273_11, fa1_1);
  PRIO(2);
  RDF(f1_r2, xr, 2048);
  RDF(f1_r3, xr, 3072);
  SQA(f1_t00[0], f1_t00[0]);
  MF(f2_02, b273_20, b273_21, fa1_0);
  RDT(b274_20, bb2, 57344);
  RDT(b274_21, bb2, 59392);
  SQA(f1_t00[1], f1_t00[1]);
  SQA(f1_t01[0], f1_t01[0]);
  MF(f2_12, b273_20, b273_21, fa1_1);
  DMA(r1, vw1, s_1w, 3211520, 36864);
  SQA(f1_t01[1], f1_t01[1]);
  unsigned f1_p00; CVT(f1_p00, f1_t00[0], f1_t00[1]);
  WLG(6);
  MF(f2_03, b273_30, b273_31, fa1_0);
  RDT(b274_30, bb3, 57344);
  RDT(b274_31, bb3, 59392);
  unsigned f1_p01; CVT(f1_p01, f1_t01[0], f1_t01[1]);
  RLU(f1_p00);
  MF(f2_13, b273_30, b273_31, fa1_1);
  RLU(f1_p01);
  f32x2 f1_t10, f1_t11;
  FMA(f1_t10[0], f1_c1[0], nmf[0], f1_b1[0]);
  MF(f2_00, b274_00, b274_01, fa2_0);
  RDT(b275_00, bb0, 45056);
  RDT(b275_01, bb0, 47104);
  FMA(f1_t10[1], f1_c1[1], nmf[0], f1_b1[1]);
  FMA(f1_t11[0], f1_c1[2], nmf[0], f1_b1[2]);
  MF(f2_10, b274_00, b274_01, fa2_1);
  FMA(f1_t11[1], f1_c1[3], nmf[0], f1_b1[3]);
  FMA(f1_t10[0], f1_01[0], rsf[0], f1_t10[0]);
  WLG(4);
  MF(f2_01, b274_10, b274_11, fa2_0);
  RDT(b275_10, bb1, 45056);
  RDT(b275_11, bb1, 47104);
  FMA(f1_t10[1], f1_01[1], rsf[0], f1_t10[1]);
  FMA(f1_t11[0], f1_01[2], rsf[0], f1_t11[0]);
  MF(f2_11, b274_10, b274_11, fa2_1);
  PRIO(1);
  FMA(f1_t11[1], f1_01[3], rsf[0], f1_t11[1]);
  RDF(f1_c2, tb, 1152);
  RDF(f1_b2, tb, 3200);
  MF(f2_02, b274_20, b274_21, fa2_0);
  RDT(b275_20, bb2, 45056);
  RDT(b275_21, bb2, 47104);
  MF(f2_12, b274_20, b274_21, fa2_1);
  FMA(f1_t10[0], f1_r1[0], rsf[0], f1_t10[0]);
  FMA(f1_t10[1], f1_r1[1], rsf[0], f1_t10[1]);
  WLG(6);
  MF(f2_03, b274_30, b274_31, fa2_0);
  RDT(b275_30, bb3, 45056);
  RDT(b275_31, bb3, 47104);
  FMA(f1_t11[0], f1_r1[2], rsf[0], f1_t11[0]);
  FMA(f1_t11[1], f1_r1[3], rsf[0], f1_t11[1]);
  MF(f2_13, b274_30, b274_31, fa2_1);
  SQA(f1_t10[0], f1_t10[0]);
  SQA(f1_t10[1], f1_t10[1]);
  MF(f2_00, b275_00, b275_01, fa3_0);
  PRIO(0);
  WVM(8);
  BAR(); STB(26);
  PRIO(3);
  RDT(b276_00, bb0, 49152);
  RDT(b276_01, bb0, 51200);
  DMA(r1, vw1, s_1w, 3440896, 40960);
  SQA(f1_t11[0], f1_t11[0]);
  SQA(f1_t11[1], f1_t11[1]);
  MF(f2_10, b275_00, b275_01, fa3_1);
  unsigned f1_p10; CVT(f1_p10, f1_t10[0], f1_t10[1]);
  unsigned f1_p11; CVT(f1_p11, f1_t11[0], f1_t11[1]);
  WLG(4);
  MF(f2_01, b275_10, b275_11, fa3_0);
  RDT(b276_10, bb1, 49152);
  RDT(b276_11, bb1, 51200);
  RLU(f1_p10);
  RLU(f1_p11);
  MF(f2_11, b275_10, b275_11, fa3_1);
  const u32x4 f1_o0 = {f1_p00, f1_p01, f1_p10, f1_p11};
  GST(f1_o0, v16, shw, sL + L_HB + 2048);
  f32x2 f1_t20, f1_t21;
  FMA(f1_t20[0], f1_c2[0], nmf[0], f1_b2[0]);
  MF(f2_02, b275_20, b275_21, fa3_0);
  RDT(b276_20, bb2, 49152);
  RDT(b276_21, bb2, 51200);
  FMA(f1_t20[1], f1_c2[1], nmf[0], f1_b2[1]);
  FMA(f1_t21[0], f1_c2[2], nmf[0], f1_b2[2]);
  MF(f2_12, b275_20, b275_21, fa3_1);
  FMA(f1_t21[1], f1_c2[3], nmf[0], f1_b2[3]);
  FMA(f1_t20[0], f1_02[0], rsf[0], f1_t20[0]);
  WLG(4);
  MF(f2_03, b275_30, b275_31, fa3_0);
  RDT(b276_30, bb3, 49152);
  RDT(b276_31, bb3, 51200);
  FMA(f1_t20[1], f1_02[1], rsf[0], f1_t20[1]);
  FMA(f1_t21[0], f1_02[2], rsf[0], f1_t21[0]);
  MF(f2_13, b275_30, b275_31, fa3_1);
  FMA(f1_t21[1], f1_02[3], rsf[0], f1_t21[1]);
  RDF(f1_c3, tb, 1216);
  RDF(f1_b3, tb, 3264);
  MF(f2_00, b276_00, b276_01, fa4_0);
  RDT(b277_00, bb0, 0);
  RDT(b277_01, bb0, 2048);
  DMA(r1, vw1n, s_1n, 0, 53248);
  MF(f2_10, b276_00, b276_01, fa4_1);
  FMA(f1_t20[0], f1_r2[0], rsf[0], f1_t20[0]);
  FMA(f1_t20[1], f1_r2[1], rsf[0], f1_t20[1]);
  WLG(6);
  MF(f2_01, b276_10, b276_11, fa4_0);
  RDT(b277_10, bb1, 0);
  RDT(b277_11, bb1, 2048);
  FMA(f1_t21[0], f1_r2[2], rsf[0], f1_t21[0]);
  FMA(f1_t21[1], f1_r2[3], rsf[0], f1_t21[1]);
  MF(f2_11, b276_10, b276_11, fa4_1);
  SQA(f1_t20[0], f1_t20[0]);
  SQA(f1_t20[1], f1_t20[1]);
  MF(f2_02, b276_20, b276_21, fa4_0);
  RDT(b277_20, bb2, 0);
  RDT(b277_21, bb2, 2048);
  SQA(f1_t21[0], f1_t21[0]);
  SQA(f1_t21[1], f1_t21[1]);
  MF(f2_12, b276_20, b276_21, fa4_1);
  DMA(r1, vw1n, s_1n, 458752, 57344);
  unsigned f1_p20; CVT(f1_p20, f1_t20[0], f1_t20[1]);
  unsigned f1_p21; CVT(f1_p21, f1_t21[0], f1_t21[1]);
  WLG(4);
  MF(f2_03, b276_30, b276_31, fa4_0);
  RDT(b277_30, bb3, 0);
  RDT(b277_31, bb3, 2048);
  RLU(f1_p20);
  RLU(f1_p21);
  MF(f2_13, b276_30, b276_31, fa4_1);
  f32x2 f1_t30, f1_t31;
  FMA(f1_t30[0], f1_c3[0], nmf[0], f1_b3[0]);
  FMA(f1_t30[1], f1_c3[1], nmf[0], f1_b3[1]);
  MF(f2_00, b277_00, b277_01, fa5_0);
  PRIO(2);
  RDT(b278_00, bb0, 4096);
  RDT(b278_01, bb0, 6144);
  FMA(f1_t31[0], f1_c3[2], nmf[0], f1_b3[2]);
  FMA(f1_t31[1], f1_c3[3], nmf[0], f1_b3[3]);
  MF(f2_10, b277_00, b277_01, fa5_1);
  FMA(f1_t30[0], f1_03[0], rsf[0], f1_t30[0]);
  FMA(f1_t30[1], f1_03[1], rsf[0], f1_t30[1]);
  WLG(4);
  MF(f2_01, b277_10, b277_11, fa5_0);
  RDT(b278_10, bb1, 4096);
  RDT(b278_11, bb1, 6144);
  FMA(f1_t31[0], f1_03[2], rsf[0], f1_t31[0]);
  FMA(f1_t31[1], f1_03[3], rsf[0], f1_t31[1]);
  MF(f2_11, b277_10, b277_11, fa5_1);
  MF(f2_02, b277_20, b277_21, fa5_0);
  RDT(b278_20, bb2, 4096);
  RDT(b278_21, bb2, 6144);
  FMA(f1_t30[0], f1_r3[0], rsf[0], f1_t30[0]);
  FMA(f1_t30[1], f1_r3[1], rsf[0], f1_t30[1]);
  VW(vw2, 2048);
  MF(f2_12, b277_20, b277_21, fa5_1);
  FMA(f1_t31[0], f1_r3[2], rsf[0], f1_t31[0]);
  FMA(f1_t31[1], f1_r3[3], rsf[0], f1_t31[1]);
  WLG(4);
  MF(f2_03, b277_30, b277_31, fa5_0);
  RDT(b278_30, bb3, 4096);
  RDT(b278_31, bb3, 6144);
  SQA(f1_t30[0], f1_t30[0]);
  SQA(f1_t30[1], f1_t30[1]);
  MF(f2_13, b277_30, b277_31, fa5_1);
  SQA(f1_t31[0], f1_t31[0]);
  SQA(f1_t31[1], f1_t31[1]);
  MF(f2_00, b278_00, b278_01, fa6_0);
  PRIO(1);
  RDT(b279_00, bb0, 8192);
  RDT(b279_01, bb0, 10240);
  unsigned f1_p30; CVT(f1_p30, f1_t30[0], f1_t30[1]);
  unsigned f1_p31; CVT(f1_p31, f1_t31[0], f1_t31[1]);
  MF(f2_10, b278_00, b278_01, fa6_1);
  RLU(f1_p30);
  RLU(f1_p31);
  WLG(4);
  MF(f2_01, b278_10, b278_11, fa6_0);
  RDT(b279_10, bb1, 8192);
  RDT(b279_11, bb1, 10240);
  const u32x4 f1_o1 = {f1_p20, f1_p21, f1_p30, f1_p31};
  GST(f1_o1, v16, shw, sL + L_HB + 3072);
  MF(f2_11, b278_10, b278_11, fa6_1);
  MF(f2_02, b278_20, b278_21, fa6_0);
  RDT(b279_20, bb2, 8192);
  RDT(b279_21, bb2, 10240);
  MF(f2_12, b278_20, b278_21, fa6_1);
  PRIO(0);
  WLG(4);
  MF(f2_03, b278_30, b278_31, fa6_0);
  RDT(b279_30, bb3, 8192);
  RDT(b279_31, bb3, 10240);
  MF(f2_13, b278_30, b278_31, fa6_1);
  MF(f2_00, b279_00, b279_01, fa7_0);
  WVM(8);
  BAR(); STB(27);
  PRIO(3);
  RDT(b280_00, bb0, 12288);
  RDT(b280_01, bb0, 14336);
  DMA(r1, vw1n, s_1n, 917504, 45056);
  GSIG(1, sfo, sL + L_FH);
  MF(f2_10, b279_00, b279_01, fa7_1);
  WLG(4);
  MF(f2_01, b279_10, b279_11, fa7_0);
  RDT(b280_10, bb1, 12288);
  RDT(b280_11, bb1, 14336);
  MF(f2_11, b279_10, b279_11, fa7_1);
  MF(f2_02, b279_20, b279_21, fa7_0);
  RDT(b280_20, bb2, 12288);
  RDT(b280_21, bb2, 14336);
  DMA(r1, vw1n, s_1n, 1376256, 49152);
  MF(f2_12, b279_20, b279_21, fa7_1);
  WLG(4);
  MF(f2_03, b279_30, b279_31, fa7_0);
  RDT(b280_30, bb3, 12288);
  RDT(b280_31, bb3, 14336);
  MF(f2_13, b279_30, b279_31, fa7_1);
  MF(f2_00, b280_00, b280_01, fa8_0);
  RDT(b281_00, bb0, 16384);
  RDT(b281_01, bb0, 18432);
  MF(f2_10, b280_00, b280_01, fa8_1);
  DMA(r1, vw1n, s_1n, 1835008, 0);
  WLG(4);
  MF(f2_01, b280_10, b280_11, fa8_0);
  RDT(b281_10, bb1, 16384);
  RDT(b281_11, bb1, 18432);
  MF(f2_11, b280_10, b280_11, fa8_1);
  MF(f2_02, b280_20, b280_21, fa8_0);
  RDT(b281_20, bb2, 16384);
  RDT(b281_21, bb2, 18432);
  MF(f2_12, b280_20, b280_21, fa8_1);
  DMA(r1, vw1n, s_1n, 2293760, 4096);
  WLG(4);
  MF(f2_03, b280_30, b280_31, fa8_0);
  RDT(b281_30, bb3, 16384);
  RDT(b281_31, bb3, 18432);
  MF(f2_13, b280_30, b280_31, fa8_1);
  MF(f2_00, b281_00, b281_01, fa9_0);
  PRIO(2);
  RDT(b282_00, bb0, 20480);
  RDT(b282_01, bb0, 22528);
  MF(f2_10, b281_00, b281_01, fa9_1);
  WLG(4);
  MF(f2_01, b281_10, b281_11, fa9_0);
  RDT(b282_10, bb1, 20480);
  RDT(b282_11, bb1, 22528);
  GMK(v16, sp2, sL + L_P);
  MF(f2_11, b281_10, b281_11, fa9_1);
  MF(f2_02, b281_20, b281_21, fa9_0);
  RDT(b282_20, bb2, 20480);
  RDT(b282_21, bb2, 22528);
  MF(f2_12, b281_20, b281_21, fa9_1);
  WLG(4);
  MF(f2_03, b281_30, b281_31, fa9_0);
  RDT(b282_30, bb3, 20480);
  RDT(b282_31, bb3, 22528);
  MF(f2_13, b281_30, b281_31, fa9_1);
  MF(f2_00, b282_00, b282_01, fa10_0);
  PRIO(1);
  RDT(b283_00, bb0, 24576);
  RDT(b283_01, bb0, 26624);
  MF(f2_10, b282_00, b282_01, fa10_1);
  WLG(4);
  MF(f2_01, b282_10, b282_11, fa10_0);
  RDT(b283_10, bb1, 24576);
  RDT(b283_11, bb1, 26624);
  MF(f2_11, b282_10, b282_11, fa10_1);
  MF(f2_02, b282_20, b282_21, fa10_0);
  RDT(b283_20, bb2, 24576);
  RDT(b283_21, bb2, 26624);
  int bn0, bn1; { LANE(l_); const int lg = l_ >> 4, b_ = (int)lds0 + (wid0 >> 2) * HALF + (4 * lg + ((l_ & 15) >> 2)) * 64 + (l_ & 3) * 8; bn0 = b_ + ((lg & 1) << 5); bn1 = b_ + (((lg & 1) ^ 1) << 5); PIN(bn0); PIN(bn1); }
  MF(f2_12, b282_20, b282_21, fa10_1);
  PRIO(0);
  WLG(4);
  MF(f2_03, b282_30, b282_31, fa10_0);
  RDT(b283_30, bb3, 24576);
  RDT(b283_31, bb3, 26624);
  MF(f2_13, b282_30, b282_31, fa10_1);
  MF(f2_00, b283_00, b283_01, fa11_0);
  WVM(10);
  BAR(); STB(28);
  PRIO(3);
  RDT(b284_00, bb0, 28672);
  RDT(b284_01, bb0, 30720);
  DMA(r1, vw1n, s_1n, 2752512, 8192);
  MF(f2_10, b283_00, b283_01, fa11_1);
  WLG(4);
  MF(f2_01, b283_10, b283_11, fa11_0);
  RDT(b284_10, bb1, 28672);
  RDT(b284_11, bb1, 30720);
  unsigned vfl; { LANE(l_); vfl = (l_ & 3) * 512; PIN(vfl); }
  GLD1(fl0, vfl, sfi, sL + L_FH);
  MF(f2_11, b283_10, b283_11, fa11_1);
  MF(f2_02, b283_20, b283_21, fa11_0);
  RDT(b284_20, bb2, 28672);
  RDT(b284_21, bb2, 30720);
  DMA(r1, vw1n, s_1n, 3211264, 12288);
  MF(f2_12, b283_20, b283_21, fa11_1);
  WLG(4);
  MF(f2_03, b283_30, b283_31, fa11_0);
  RDT(b284_30, bb3, 28672);
  RDT(b284_31, bb3, 30720);
  MF(f2_13, b283_30, b283_31, fa11_1);
  MF(f2_00, b284_00, b284_01, fa12_0);
  RDT(b285_00, bb0, 32768);
  RDT(b285_01, bb0, 34816);
  MF(f2_10, b284_00, b284_01, fa12_1);
  DMA(r2, vw2, s_20, 0, 16384);
  WLG(4);
  MF(f2_01, b284_10, b284_11, fa12_0);
  RDT(b285_10, bb1, 32768);
  RDT(b285_11, bb1, 34816);
  MF(f2_11, b284_10, b284_11, fa12_1);
  MF(f2_02, b284_20, b284_21, fa12_0);
  RDT(b285_20, bb2, 32768);
  RDT(b285_21, bb2, 34816);
  MF(f2_12, b284_20, b284_21, fa12_1);
  DMA(r2, vw2, s_21, 0, 20480);
  WLG(4);
  MF(f2_03, b284_30, b284_31, fa12_0);
  RDT(b285_30, bb3, 32768);
  RDT(b285_31, bb3, 34816);
  MF(f2_13, b284_30, b284_31, fa12_1);
  MF(f2_00, b285_00, b285_01, fa13_0);
  PRIO(2);
  RDT(b286_00, bb0, 36864);
  RDT(b286_01, bb0, 38912);
  MF(f2_10, b285_00, b285_01, fa13_1);
  RDT(b287_00, bb0, 40960);
  RDT(b287_01, bb0, 43008);
  WLG(6);
  MF(f2_01, b285_10, b285_11, fa13_0);
  RDT(b286_10, bb1, 36864);
  RDT(b286_11, bb1, 38912);
  MF(f2_11, b285_10, b285_11, fa13_1);
  RDT(b287_10, bb1, 40960);
  RDT(b287_11, bb1, 43008);
  MF(f2_02, b285_20, b285_21, fa13_0);
  RDT(b286_20, bb2, 36864);
  RDT(b286_21, bb2, 38912);
  MF(f2_12, b285_20, b285_21, fa13_1);
  RDT(b287_20, bb2, 40960);
  RDT(b287_21, bb2, 43008);
  WLG(8);
  MF(f2_03, b285_30, b285_31, fa13_0);
  RDT(b286_30, bb3, 36864);
  RDT(b286_31, bb3, 38912);
  GMK(v16, sp2, sL + L_P + 1024);
  MF(f2_13, b285_30, b285_31, fa13_1);
  RDT(b287_30, bb3, 40960);
  RDT(b287_31, bb3, 43008);
  MF(f2_10, b286_00, b286_01, fa14_1);
  PRIO(1);
  MF(f2_10, b287_00, b287_01, fa15_1);
  WLG(4);
  MF(f2_11, b286_10, b286_11, fa14_1);
  MF(f2_11, b287_10, b287_11, fa15_1);
  MF(f2_12, b286_20, b286_21, fa14_1);
  NOP(3);
  WR(f2_10, xw, 0);
  MF(f2_12, b287_20, b287_21, fa15_1);
  PRIO(0);
  WLG(1);
  MF(f2_13, b286_30, b286_31, fa14_1);
  NOP(1);
  WR(f2_11, xw, 1024);
  MF(f2_13, b287_30, b287_31, fa15_1);
  MF(f2_00, b286_00, b286_01, fa14_0);
  WVM(12);
  GSIG(2, sfo, sL + L_FH);
  NOP(0);
  WR(f2_12, xw, 2048);
  MF(f2_01, b286_10, b286_11, fa14_0);
  MF(f2_02, b286_20, b286_21, fa14_0);
  NOP(2);
  WR(f2_13, xw, 3072);
  MF(f2_03, b286_30, b286_31, fa14_0);
  WVM(10);
  WLG(0);
  BAR(); STB(29);
  PRIO(3);
  RDT(b288_00, bn0, 53248);
  RDT(b288_01, bn0, 54272);
  DMA(r2, vw2, s_20, 65536, 24576);
  MF(f2_00, b287_00, b287_01, fa15_0);
  RDT(b288_10, bn1, 53248);
  RDT(b288_11, bn1, 54272);
  MF(f2_01, b287_10, b287_11, fa15_0);
  RDT(b288_20, bn0, 55296);
  RDT(b288_21, bn0, 56320);
  MF(f2_02, b287_20, b287_21, fa15_0);
  RDT(b288_30, bn1, 55296);
  RDT(b288_31, bn1, 56320);
  MF(f2_03, b287_30, b287_31, fa15_0);
  DMA(r2, vw2, s_21, 65536, 28672);
  STP(16);
  WLG(4);
  MF0(f3_00, b288_00, b288_01, fa0_0);
  RDT(b289_00, bn0, 57344);
  RDT(b289_01, bn0, 58368);
  RDF(f2_c0, tb, 1280);
  RDF(f2_b0, tb, 3328);
  MF0(f3_10, b288_00, b288_01, fa0_1);
  MF0(f3_01, b288_10, b288_11, fa0_0);
  RDT(b289_10, bn1, 57344);
  RDT(b289_11, bn1, 58368);
  MF0(f3_11, b288_10, b288_11, fa0_1);
  f32x2 f2_t00, f2_t01;
  WLG(2);
  FMA(f2_t00[0], f2_c0[0], nmf[0], f2_b0[0]);
  FMA(f2_t00[1], f2_c0[1], nmf[0], f2_b0[1]);
  MF(f3_00, b288_20, b288_21, fa1_0);
  RDT(b289_20, bn0, 59392);
  RDT(b289_21, bn0, 60416);
  DMA(r2, vw2, s_20, 524288, 32768);
  FMA(f2_t01[0], f2_c0[2], nmf[0], f2_b0[2]);
  FMA(f2_t01[1], f2_c0[3], nmf[0], f2_b0[3]);
  MF(f3_10, b288_20, b288_21, fa1_1);
  RDF(f2_r0, xr, 0);
  RDF(f2_r1, xr, 1024);
  FMA(f2_t00[0], f2_00[0], rsf[0], f2_t00[0]);
  MF(f3_01, b288_30, b288_31, fa1_0);
  RDT(b289_30, bn1, 59392);
  RDT(b289_31, bn1, 60416);
  FMA(f2_t00[1], f2_00[1], rsf[0], f2_t00[1]);
  FMA(f2_t01[0], f2_00[2], rsf[0], f2_t01[0]);
  MF(f3_11, b288_30, b288_31, fa1_1);
  FMA(f2_t01[1], f2_00[3], rsf[0], f2_t01[1]);
  RDF(f2_c1, tb, 1344);
  RDF(f2_b1, tb, 3392);
  MF(f3_00, b289_00, b289_01, fa2_0);
  RDT(b290_00, bn0, 45056);
  RDT(b290_01, bn0, 46080);
  DMA(r2, vw2, s_21, 524288, 36864);
  WVM(9);
  CHKF(fl0, 1u, vfl, sfi, sL + L_FH);
  MF(f3_10, b289_00, b289_01, fa2_1);
  WLG(7);
  FMA(f2_t00[0], f2_r0[0], rsf[0], f2_t00[0]);
  FMA(f2_t00[1], f2_r0[1], rsf[0], f2_t00[1]);
  MF(f3_01, b289_10, b289_11, fa2_0);
  RDT(b290_10, bn1, 45056);
  RDT(b290_11, bn1, 46080);
  FMA(f2_t01[0], f2_r0[2], rsf[0], f2_t01[0]);
  FMA(f2_t01[1], f2_r0[3], rsf[0], f2_t01[1]);
  MF(f3_11, b289_10, b289_11, fa2_1);
  PRIO(2);
  RDF(f2_r2, xr, 2048);
  RDF(f2_r3, xr, 3072);
  SQA(f2_t00[0], f2_t00[0]);
  MF(f3_00, b289_20, b289_21, fa3_0);
  RDT(b290_20, bn0, 47104);
  RDT(b290_21, bn0, 48128);
  SQA(f2_t00[1], f2_t00[1]);
  SQA(f2_t01[0], f2_t01[0]);
  MF(f3_10, b289_20, b289_21, fa3_1);
  GLD(ha0_0, v16, sh0, sL + L_HB + 0);
  SQA(f2_t01[1], f2_t01[1]);
  unsigned f2_p00; CVT(f2_p00, f2_t00[0], f2_t00[1]);
  WLG(6);
  MF(f3_01, b289_30, b289_31, fa3_0);
  RDT(b290_30, bn1, 47104);
  RDT(b290_31, bn1, 48128);
  unsigned f2_p01; CVT(f2_p01, f2_t01[0], f2_t01[1]);
  RLU(f2_p00);
  MF(f3_11, b289_30, b289_31, fa3_1);
  RLU(f2_p01);
  f32x2 f2_t10, f2_t11;
  FMA(f2_t10[0], f2_c1[0], nmf[0], f2_b1[0]);
  MF(f3_00, b290_00, b290_01, fa4_0);
  RDT(b291_00, bn0, 49152);
  RDT(b291_01, bn0, 50176);
  FMA(f2_t10[1], f2_c1[1], nmf[0], f2_b1[1]);
  FMA(f2_t11[0], f2_c1[2], nmf[0], f2_b1[2]);
  MF(f3_10, b290_00, b290_01, fa4_1);
  GLD(ha0_1, v16, sh1, sL + L_HB + 0);
  FMA(f2_t11[1], f2_c1[3], nmf[0], f2_b1[3]);
  FMA(f2_t10[0], f2_01[0], rsf[0], f2_t10[0]);
  WLG(4);
  MF(f3_01, b290_10, b290_11, fa4_0);
  RDT(b291_10, bn1, 49152);
  RDT(b291_11, bn1, 50176);
  FMA(f2_t10[1], f2_01[1], rsf[0], f2_t10[1]);
  FMA(f2_t11[0], f2_01[2], rsf[0], f2_t11[0]);
  MF(f3_11, b290_10, b290_11, fa4_1);
  PRIO(1);
  FMA(f2_t11[1], f2_01[3], rsf[0], f2_t11[1]);
  RDF(f2_c2, tb, 1408);
  RDF(f2_b2, tb, 3456);
  MF(f3_00, b290_20, b290_21, fa5_0);
  RDT(b291_20, bn0, 51200);
  RDT(b291_21, bn0, 52224);
  MF(f3_10, b290_20, b290_21, fa5_1);
  FMA(f2_t10[0], f2_r1[0], rsf[0], f2_t10[0]);
  FMA(f2_t10[1], f2_r1[1], rsf[0], f2_t10[1]);
  WLG(6);
  MF(f3_01, b290_30, b290_31, fa5_0);
  RDT(b291_30, bn1, 51200);
  RDT(b291_31, bn1, 52224);
  GLD(ha1_0, v16, sh0, sL + L_HB + 1024);
  FMA(f2_t11[0], f2_r1[2], rsf[0], f2_t11[0]);
  FMA(f2_t11[1], f2_r1[3], rsf[0], f2_t11[1]);
  MF(f3_11, b290_30, b290_31, fa5_1);
  SQA(f2_t10[0], f2_t10[0]);
  SQA(f2_t10[1], f2_t10[1]);
  MF(f3_00, b291_00, b291_01, fa6_0);
  PRIO(0);
  WVM(11);
  BAR(); STB(30);
  PRIO(3);
  RDT(b292_00, bn0, 0);
  RDT(b292_01, bn0, 1024);
  SQA(f2_t11[0], f2_t11[0]);
  SQA(f2_t11[1], f2_t11[1]);
  MF(f3_10, b291_00, b291_01, fa6_1);
  unsigned f2_p10; CVT(f2_p10, f2_t10[0], f2_t10[1]);
  unsigned f2_p11; CVT(f2_p11, f2_t11[0], f2_t11[1]);
  WLG(4);
  MF(f3_01, b291_10, b291_11, fa6_0);
  RDT(b292_10, bn1, 0);
  RDT(b292_11, bn1, 1024);
  GLD(ha1_1, v16, sh1, sL + L_HB + 1024);
  RLU(f2_p10);
  RLU(f2_p11);
  MF(f3_11, b291_10, b291_11, fa6_1);
  const u32x4 f2_o0 = {f2_p00, f2_p01, f2_p10, f2_p11};
  GST(f2_o0, v16, shw, sL + L_HB + 4096);
  f32x2 f2_t20, f2_t21;
  FMA(f2_t20[0], f2_c2[0], nmf[0], f2_b2[0]);
  MF(f3_00, b291_20, b291_21, fa7_0);
  RDT(b292_20, bn0, 2048);
  RDT(b292_21, bn0, 3072);
  FMA(f2_t20[1], f2_c2[1], nmf[0], f2_b2[1]);
  FMA(f2_t21[0], f2_c2[2], nmf[0], f2_b2[2]);
  MF(f3_10, b291_20, b291_21, fa7_1);
  FMA(f2_t21[1], f2_c2[3], nmf[0], f2_b2[3]);
  FMA(f2_t20[0], f2_02[0], rsf[0], f2_t20[0]);
  WLG(4);
  MF(f3_01, b291_30, b291_31, fa7_0);
  RDT(b292_30, bn1, 2048);
  RDT(b292_31, bn1, 3072);
  FMA(f2_t20[1], f2_02[1], rsf[0], f2_t20[1]);
  FMA(f2_t21[0], f2_02[2], rsf[0], f2_t21[0]);
  MF(f3_11, b291_30, b291_31, fa7_1);
  FMA(f2_t21[1], f2_02[3], rsf[0], f2_t21[1]);
  RDF(f2_c3, tb, 1472);
  RDF(f2_b3, tb, 3520);
  MF(f3_00, b292_00, b292_01, fa8_0);
  RDT(b293_00, bn0, 4096);
  RDT(b293_01, bn0, 5120);
  DMA(r2, vw2, s_20, 589824, 40960);
  MF(f3_10, b292_00, b292_01, fa8_1);
  FMA(f2_t20[0], f2_r2[0], rsf[0], f2_t20[0]);
  FMA(f2_t20[1], f2_r2[1], rsf[0], f2_t20[1]);
  WLG(6);
  MF(f3_01, b292_10, b292_11, fa8_0);
  RDT(b293_10, bn1, 4096);
  RDT(b293_11, bn1, 5120);
  FMA(f2_t21[0], f2_r2[2], rsf[0], f2_t21[0]);
  FMA(f2_t21[1], f2_r2[3], rsf[0], f2_t21[1]);
  MF(f3_11, b292_10, b292_11, fa8_1);
  SQA(f2_t20[0], f2_t20[0]);
  SQA(f2_t20[1], f2_t20[1]);
  MF(f3_00, b292_20, b292_21, fa9_0);
  RDT(b293_20, bn0, 6144);
  RDT(b293_21, bn0, 7168);
  SQA(f2_t21[0], f2_t21[0]);
  SQA(f2_t21[1], f2_t21[1]);
  GLD1(fl1, vfl, sfi, sL + L_FH);
  MF(f3_10, b292_20, b292_21, fa9_1);
  DMA(r2, vw2, s_21, 589824, 53248);
  unsigned f2_p20; CVT(f2_p20, f2_t20[0], f2_t20[1]);
  unsigned f2_p21; CVT(f2_p21, f2_t21[0], f2_t21[1]);
  WLG(4);
  MF(f3_01, b292_30, b292_31, fa9_0);
  RDT(b293_30, bn1, 6144);
  RDT(b293_31, bn1, 7168);
  RLU(f2_p20);
  RLU(f2_p21);
  MF(f3_11, b292_30, b292_31, fa9_1);
  f32x2 f2_t30, f2_t31;
  FMA(f2_t30[0], f2_c3[0], nmf[0], f2_b3[0]);
  FMA(f2_t30[1], f2_c3[1], nmf[0], f2_b3[1]);
  MF(f3_00, b293_00, b293_01, fa10_0);
  PRIO(2);
  RDT(b294_00, bn0, 8192);
  RDT(b294_01, bn0, 9216);
  FMA(f2_t31[0], f2_c3[2], nmf[0], f2_b3[2]);
  FMA(f2_t31[1], f2_c3[3], nmf[0], f2_b3[3]);
  MF(f3_10, b293_00, b293_01, fa10_1);
  RDT(b294_20, bn0, 10240);
  RDT(b294_21, bn0, 11264);
  GLD(ha2_0, v16, sh0, sL + L_HB + 8192);
  FMA(f2_t30[0], f2_03[0], rsf[0], f2_t30[0]);
  FMA(f2_t30[1], f2_03[1], rsf[0], f2_t30[1]);
  WLG(6);
  MF(f3_01, b293_10, b293_11, fa10_0);
  RDT(b295_00, bn0, 12288);
  RDT(b295_01, bn0, 13312);
  FMA(f2_t31[0], f2_03[2], rsf[0], f2_t31[0]);
  FMA(f2_t31[1], f2_03[3], rsf[0], f2_t31[1]);
  MF(f3_11, b293_10, b293_11, fa10_1);
  RDT(b295_20, bn0, 14336);
  RDT(b295_21, bn0, 15360);
  MF(f3_00, b293_20, b293_21, fa11_0);
  RDT(b294_10, bn1, 8192);
  RDT(b294_11, bn1, 9216);
  FMA(f2_t30[0], f2_r3[0], rsf[0], f2_t30[0]);
  FMA(f2_t30[1], f2_r3[1], rsf[0], f2_t30[1]);
  MF(f3_10, b293_20, b293_21, fa11_1);
  RDT(b294_30, bn1, 10240);
  RDT(b294_31, bn1, 11264);
  FMA(f2_t31[0], f2_r3[2], rsf[0], f2_t31[0]);
  FMA(f2_t31[1], f2_r3[3], rsf[0], f2_t31[1]);
  WLG(8);
  MF(f3_01, b293_30, b293_31, fa11_0);
  RDT(b295_10, bn1, 12288);
  RDT(b295_11, bn1, 13312);
  GLD(ha2_1, v16, sh1, sL + L_HB + 8192);
  SQA(f2_t30[0], f2_t30[0]);
  SQA(f2_t30[1], f2_t30[1]);
  MF(f3_11, b293_30, b293_31, fa11_1);
  RDT(b295_30, bn1, 14336);
  RDT(b295_31, bn1, 15360);
  SQA(f2_t31[0], f2_t31[0]);
  SQA(f2_t31[1], f2_t31[1]);
  MF(f3_10, b294_00, b294_01, fa12_1);
  PRIO(1);
  unsigned f2_p30; CVT(f2_p30, f2_t30[0], f2_t30[1]);
  unsigned f2_p31; CVT(f2_p31, f2_t31[0], f2_t31[1]);
  MF(f3_10, b294_20, b294_21, fa13_1);
  RLU(f2_p30);
  RLU(f2_p31);
  WLG(4);
  MF(f3_10, b295_00, b295_01, fa14_1);
  GLD(ha3_0, v16, sh0, sL + L_HB + 9216);
  const u32x4 f2_o1 = {f2_p20, f2_p21, f2_p30, f2_p31};
  GST(f2_o1, v16, shw, sL + L_HB + 5120);
  MF(f3_10, b295_20, b295_21, fa15_1);
  MF(f3_11, b294_10, b294_11, fa12_1);
  MF(f3_11, b294_30, b294_31, fa13_1);
  PRIO(0);
  WLG(0);
  MF(f3_11, b295_10, b295_11, fa14_1);
  NOP(2);
  WR(f3_10, xw, 0);
  MF(f3_11, b295_30, b295_31, fa15_1);
  GLD(ha3_1, v16, sh1, sL + L_HB + 9216);
  MF(f3_00, b294_00, b294_01, fa12_0);
  MF(f3_01, b294_10, b294_11, fa12_0);
  MF(f3_00, b294_20, b294_21, fa13_0);
  NOP(3);
  WR(f3_11, xw, 1024);
  MF(f3_01, b294_30, b294_31, fa13_0);
  WVM(15);
  WLG(0);
  BAR(); STB(31);
  PRIO(3);
  RDT(b296_00, bb0, 16384);
  RDT(b296_01, bb0, 18432);
  MF(f3_00, b295_00, b295_01, fa14_0);
  RDT(b296_10, bb1, 16384);
  RDT(b296_11, bb1, 18432);
  DMA(r2, vw2, s_20, 131072, 57344);
  MF(f3_01, b295_10, b295_11, fa14_0);
  RDT(b296_20, bb2, 16384);
  RDT(b296_21, bb2, 18432);
  MF(f3_00, b295_20, b295_21, fa15_0);
  RDT(b296_30, bb3, 16384);
  RDT(b296_31, bb3, 18432);
  STP(8);
  MF(f3_01, b295_30, b295_31, fa15_0);
  STP(9);
  WVM(12);
  WLG(4);
  MF0(g0_00, b296_00, b296_01, ha0_0);
  RDT(b297_00, bb0, 20480);
  RDT(b297_01, bb0, 22528);
  DMA(r2, vw2, s_21, 131072, 45056);
  RDF(f3_c0, tb, 1536);
  RDF(f3_b0, tb, 3584);
  MF0(g0_10, b296_00, b296_01, ha0_1);
  MF0(g0_01, b296_10, b296_11, ha0_0);
  RDT(b297_10, bb1, 20480);
  RDT(b297_11, bb1, 22528);
  MF0(g0_11, b296_10, b296_11, ha0_1);
  f32x2 f3_t00, f3_t01;
  WLG(2);
  FMA(f3_t00[0], f3_c0[0], nmf[0], f3_b0[0]);
  FMA(f3_t00[1], f3_c0[1], nmf[0], f3_b0[1]);
  MF0(g0_02, b296_20, b296_21, ha0_0);
  RDT(b297_20, bb2, 20480);
  RDT(b297_21, bb2, 22528);
  FMA(f3_t01[0], f3_c0[2], nmf[0], f3_b0[2]);
  FMA(f3_t01[1], f3_c0[3], nmf[0], f3_b0[3]);
  MF0(g0_12, b296_20, b296_21, ha0_1);
  DMA(r2, vw2, s_20, 196608, 49152);
  RDF(f3_r0, xr, 0);
  RDF(f3_r1, xr, 1024);
  FMA(f3_t00[0], f3_00[0], rsf[0], f3_t00[0]);
  MF0(g0_03, b296_30, b296_31, ha0_0);
  RDT(b297_30, bb3, 20480);
  RDT(b297_31, bb3, 22528);
  FMA(f3_t00[1], f3_00[1], rsf[0], f3_t00[1]);
  FMA(f3_t01[0], f3_00[2], rsf[0], f3_t01[0]);
  MF0(g0_13, b296_30, b296_31, ha0_1);
  FMA(f3_t01[1], f3_00[3], rsf[0], f3_t01[1]);
  RDF(f3_c1, tb, 1600);
  RDF(f3_b1, tb, 3648);
  MF0(g1_00, b297_00, b297_01, ha0_0);
  RDT(b298_00, bb0, 24576);
  RDT(b298_01, bb0, 26624);
  MF0(g1_10, b297_00, b297_01, ha0_1);
  DMA(r2, vw2, s_21, 196608, 0);
  WLG(7);
  FMA(f3_t00[0], f3_r0[0], rsf[0], f3_t00[0]);
  FMA(f3_t00[1], f3_r0[1], rsf[0], f3_t00[1]);
  MF0(g1_01, b297_10, b297_11, ha0_0);
  RDT(b298_10, bb1, 24576);
  RDT(b298_11, bb1, 26624);
  FMA(f3_t01[0], f3_r0[2], rsf[0], f3_t01[0]);
  FMA(f3_t01[1], f3_r0[3], rsf[0], f3_t01[1]);
  MF0(g1_11, b297_10, b297_11, ha0_1);
  PRIO(2);
  SQA(f3_t00[0], f3_t00[0]);
  SQA(f3_t00[1], f3_t00[1]);
  MF0(g1_02, b297_20, b297_21, ha0_0);
  RDT(b298_20, bb2, 24576);
  RDT(b298_21, bb2, 26624);
  SQA(f3_t01[0], f3_t01[0]);
  SQA(f3_t01[1], f3_t01[1]);
  WVM(10);
  CHKF(fl1, 2u, vfl, sfi, sL + L_FH);
  MF0(g1_12, b297_20, b297_21, ha0_1);
  GLD(ha4_0, v16, sh0, sL + L_HB + 2048);
  unsigned f3_p00; CVT(f3_p00, f3_t00[0], f3_t00[1]);
  unsigned f3_p01; CVT(f3_p01, f3_t01[0], f3_t01[1]);
  WLG(4);
  MF0(g1_03, b297_30, b297_31, ha0_0);
  RDT(b298_30, bb3, 24576);
  RDT(b298_31, bb3, 26624);
  RLU(f3_p00);
  RLU(f3_p01);
  MF0(g1_13, b297_30, b297_31, ha0_1);
  f32x2 f3_t10, f3_t11;
  FMA(f3_t10[0], f3_c1[0], nmf[0], f3_b1[0]);
  FMA(f3_t10[1], f3_c1[1], nmf[0], f3_b1[1]);
  MF(g0_00, b298_00, b298_01, ha1_0);
  RDT(b299_00, bb0, 28672);
  RDT(b299_01, bb0, 30720);
  FMA(f3_t11[0], f3_c1[2], nmf[0], f3_b1[2]);
  FMA(f3_t11[1], f3_c1[3], nmf[0], f3_b1[3]);
  MF(g0_10, b298_00, b298_01, ha1_1);
  FMA(f3_t10[0], f3_01[0], rsf[0], f3_t10[0]);
  FMA(f3_t10[1], f3_01[1], rsf[0], f3_t10[1]);
  WLG(4);
  MF(g0_01, b298_10, b298_11, ha1_0);
  RDT(b299_10, bb1, 28672);
  RDT(b299_11, bb1, 30720);
  GLD(ha4_1, v16, sh1, sL + L_HB + 2048);
  FMA(f3_t11[0], f3_01[2], rsf[0], f3_t11[0]);
  FMA(f3_t11[1], f3_01[3], rsf[0], f3_t11[1]);
  MF(g0_11, b298_10, b298_11, ha1_1);
  PRIO(1);
  MF(g0_02, b298_20, b298_21, ha1_0);
  RDT(b299_20, bb2, 28672);
  RDT(b299_21, bb2, 30720);
  FMA(f3_t10[0], f3_r1[0], rsf[0], f3_t10[0]);
  FMA(f3_t10[1], f3_r1[1], rsf[0], f3_t10[1]);
  MF(g0_12, b298_20, b298_21, ha1_1);
  FMA(f3_t11[0], f3_r1[2], rsf[0], f3_t11[0]);
  FMA(f3_t11[1], f3_r1[3], rsf[0], f3_t11[1]);
  WLG(4);
  MF(g0_03, b298_30, b298_31, ha1_0);
  RDT(b299_30, bb3, 28672);
  RDT(b299_31, bb3, 30720);
  GLD(ha5_0, v16, sh0, sL + L_HB + 3072);
  SQA(f3_t10[0], f3_t10[0]);
  SQA(f3_t10[1], f3_t10[1]);
  MF(g0_13, b298_30, b298_31, ha1_1);
  SQA(f3_t11[0], f3_t11[0]);
  SQA(f3_t11[1], f3_t11[1]);
  MF(g1_00, b299_00, b299_01, ha1_0);
  PRIO(0);
  WVM(12);
  BAR(); STB(32);
  PRIO(3);
  RDT(b300_00, bb0, 32768);
  RDT(b300_01, bb0, 34816);
  unsigned f3_p10; CVT(f3_p10, f3_t10[0], f3_t10[1]);
  unsigned f3_p11; CVT(f3_p11, f3_t11[0], f3_t11[1]);
  MF(g1_10, b299_00, b299_01, ha1_1);
  RLU(f3_p10);
  RLU(f3_p11);
  WLG(4);
  MF(g1_01, b299_10, b299_11, ha1_0);
  RDT(b300_10, bb1, 32768);
  RDT(b300_11, bb1, 34816);
  const u32x4 f3_o0 = {f3_p00, f3_p01, f3_p10, f3_p11};
  GST(f3_o0, v16, shn, sL + L_HB);
  MF(g1_11, b299_10, b299_11, ha1_1);
  MF(g1_02, b299_20, b299_21, ha1_0);
  RDT(b300_20, bb2, 32768);
  RDT(b300_21, bb2, 34816);
  MF(g1_12, b299_20, b299_21, ha1_1);
  WLG(4);
  MF(g1_03, b299_30, b299_31, ha1_0);
  RDT(b300_30, bb3, 32768);
  RDT(b300_31, bb3, 34816);
  WVM(9);
  GSIG(3, sfo, sL + L_FH);
  MF(g1_13, b299_30, b299_31, ha1_1);
  GLD(ha5_1, v16, sh1, sL + L_HB + 3072);
  MF(g0_00, b300_00, b300_01, ha2_0);
  RDT(b301_00, bb0, 36864);
  RDT(b301_01, bb0, 38912);
  MF(g0_10, b300_00, b300_01, ha2_1);
  WLG(4);
  MF(g0_01, b300_10, b300_11, ha2_0);
  RDT(b301_10, bb1, 36864);
  RDT(b301_11, bb1, 38912);
  MF(g0_11, b300_10, b300_11, ha2_1);
  MF(g0_02, b300_20, b300_21, ha2_0);
  RDT(b301_20, bb2, 36864);
  RDT(b301_21, bb2, 38912);
  DMA(r2, vw2, s_20, 655360, 4096);
  MF(g0_12, b300_20, b300_21, ha2_1);
  WLG(4);
  MF(g0_03, b300_30, b300_31, ha2_0);
  RDT(b301_30, bb3, 36864);
  RDT(b301_31, bb3, 38912);
  MF(g0_13, b300_30, b300_31, ha2_1);
  MF(g1_00, b301_00, b301_01, ha2_0);
  PRIO(2);
  RDT(b302_00, bb0, 40960);
  RDT(b302_01, bb0, 43008);
  DMA(r2, vw2, s_21, 655360, 8192);
  MF(g1_10, b301_00, b301_01, ha2_1);
  WLG(4);
  MF(g1_01, b301_10, b301_11, ha2_0);
  RDT(b302_10, bb1, 40960);
  RDT(b302_11, bb1, 43008);
  MF(g1_11, b301_10, b301_11, ha2_1);
  MF(g1_02, b301_20, b301_21, ha2_0);
  RDT(b302_20, bb2, 40960);
  RDT(b302_21, bb2, 43008);
  MF(g1_12, b301_20, b301_21, ha2_1);
  DMA(r2, vw2, s_20, 720896, 12288);
  WLG(4);
  MF(g1_03, b301_30, b301_31, ha2_0);
  RDT(b302_30, bb3, 40960);
  RDT(b302_31, bb3, 43008);
  MF(g1_13, b301_30, b301_31, ha2_1);
  WVM(13);
  MF(g0_00, b302_00, b302_01, ha3_0);
  PRIO(1);
  RDT(b303_00, bb0, 53248);
  RDT(b303_01, bb0, 55296);
  MF(g0_10, b302_00, b302_01, ha3_1);
  DMA(r2, vw2, s_21, 720896, 16384);
  WLG(4);
  MF(g0_01, b302_10, b302_11, ha3_0);
  RDT(b303_10, bb1, 53248);
  RDT(b303_11, bb1, 55296);
  MF(g0_11, b302_10, b302_11, ha3_1);
  MF(g0_02, b302_20, b302_21, ha3_0);
  RDT(b303_20, bb2, 53248);
  RDT(b303_21, bb2, 55296);
  MF(g0_12, b302_20, b302_21, ha3_1);
  PRIO(0);
  GLD(ha6_0, v16, sh0, sL + L_HB + 10240);
  WLG(4);
  MF(g0_03, b302_30, b302_31, ha3_0);
  RDT(b303_30, bb3, 53248);
  RDT(b303_31, bb3, 55296);
  MF(g0_13, b302_30, b302_31, ha3_1);
  MF(g1_00, b303_00, b303_01, ha3_0);
  WVM(11);
  BAR(); STB(33);
  PRIO(3);
  RDT(b304_00, bb0, 57344);
  RDT(b304_01, bb0, 59392);
  MF(g1_10, b303_00, b303_01, ha3_1);
  WLG(4);
  MF(g1_01, b303_10, b303_11, ha3_0);
  RDT(b304_10, bb1, 57344);
  RDT(b304_11, bb1, 59392);
  GLD(ha6_1, v16, sh1, sL + L_HB + 10240);
  MF(g1_11, b303_10, b303_11, ha3_1);
  MF(g1_02, b303_20, b303_21, ha3_0);
  RDT(b304_20, bb2, 57344);
  RDT(b304_21, bb2, 59392);
  MF(g1_12, b303_20, b303_21, ha3_1);
  WLG(4);
  MF(g1_03, b303_30, b303_31, ha3_0);
  RDT(b304_30, bb3, 57344);
  RDT(b304_31, bb3, 59392);
  GLD(ha7_0, v16, sh0, sL + L_HB + 11264);
  MF(g1_13, b303_30, b303_31, ha3_1);
  WVM(11);
  MF(g0_00, b304_00, b304_01, ha4_0);
  RDT(b305_00, bb0, 45056);
  RDT(b305_01, bb0, 47104);
  MF(g0_10, b304_00, b304_01, ha4_1);
  WLG(4);
  MF(g0_01, b304_10, b304_11, ha4_0);
  RDT(b305_10, bb1, 45056);
  RDT(b305_11, bb1, 47104);
  MF(g0_11, b304_10, b304_11, ha4_1);
  GLD(ha7_1, v16, sh1, sL + L_HB + 11264);
  MF(g0_02, b304_20, b304_21, ha4_0);
  RDT(b305_20, bb2, 45056);
  RDT(b305_21, bb2, 47104);
  MF(g0_12, b304_20, b304_21, ha4_1);
  WLG(4);
  MF(g0_03, b304_30, b304_31, ha4_0);
  RDT(b305_30, bb3, 45056);
  RDT(b305_31, bb3, 47104);
  MF(g0_13, b304_30, b304_31, ha4_1);
  DMA(r2, vw2, s_20, 393216, 20480);
  MF(g1_00, b305_00, b305_01, ha4_0);
  PRIO(2);
  RDT(b306_00, bb0, 49152);
  RDT(b306_01, bb0, 51200);
  MF(g1_10, b305_00, b305_01, ha4_1);
  GLD1(fl2, vfl, sfi, sL + L_FH);
  WLG(4);
  MF(g1_01, b305_10, b305_11, ha4_0);
  RDT(b306_10, bb1, 49152);
  RDT(b306_11, bb1, 51200);
  MF(g1_11, b305_10, b305_11, ha4_1);
  DMA(r2, vw2, s_21, 393216, 24576);
  MF(g1_02, b305_20, b305_21, ha4_0);
  RDT(b306_20, bb2, 49152);
  RDT(b306_21, bb2, 51200);
  MF(g1_12, b305_20, b305_21, ha4_1);
  WLG(4);
  MF(g1_03, b305_30, b305_31, ha4_0);
  RDT(b306_30, bb3, 49152);
  RDT(b306_31, bb3, 51200);
  MF(g1_13, b305_30, b305_31, ha4_1);
  WVM(11);
  MF(g0_00, b306_00, b306_01, ha5_0);
  PRIO(1);
  RDT(b307_00, bb0, 0);
  RDT(b307_01, bb0, 2048);
  DMA(r2, vw2, s_20, 458752, 28672);
  MF(g0_10, b306_00, b306_01, ha5_1);
  WLG(4);
  MF(g0_01, b306_10, b306_11, ha5_0);
  RDT(b307_10, bb1, 0);
  RDT(b307_11, bb1, 2048);
  MF(g0_11, b306_10, b306_11, ha5_1);
  MF(g0_02, b306_20, b306_21, ha5_0);
  RDT(b307_20, bb2, 0);
  RDT(b307_21, bb2, 2048);
  DMA(r2, vw2, s_21, 458752, 32768);
  MF(g0_12, b306_20, b306_21, ha5_1);
  PRIO(0);
  WLG(4);
  MF(g0_03, b306_30, b306_31, ha5_0);
  RDT(b307_30, bb3, 0);
  RDT(b307_31, bb3, 2048);
  MF(g0_13, b306_30, b306_31, ha5_1);
  MF(g1_00, b307_00, b307_01, ha5_0);
  WVM(9);
  BAR(); STB(34);
  PRIO(3);
  RDT(b308_00, bb0, 4096);
  RDT(b308_01, bb0, 6144);
  MF(g1_10, b307_00, b307_01, ha5_1);
  GLD(ha8_0, v16, sh0, sL + L_HB + 6144);
  WLG(4);
  MF(g1_01, b307_10, b307_11, ha5_0);
  RDT(b308_10, bb1, 4096);
  RDT(b308_11, bb1, 6144);
  MF(g1_11, b307_10, b307_11, ha5_1);
  MF(g1_02, b307_20, b307_21, ha5_0);
  RDT(b308_20, bb2, 4096);
  RDT(b308_21, bb2, 6144);
  MF(g1_12, b307_20, b307_21, ha5_1);
  GLD(ha8_1, v16, sh1, sL + L_HB + 6144);
  WLG(4);
  MF(g1_03, b307_30, b307_31, ha5_0);
  RDT(b308_30, bb3, 4096);
  RDT(b308_31, bb3, 6144);
  MF(g1_13, b307_30, b307_31, ha5_1);
  WVM(9);
  MF(g0_00, b308_00, b308_01, ha6_0);
  RDT(b309_00, bb0, 8192);
  RDT(b309_01, bb0, 10240);
  MF(g0_10, b308_00, b308_01, ha6_1);
  GLD(ha9_0, v16, sh0, sL + L_HB + 7168);
  WLG(4);
  MF(g0_01, b308_10, b308_11, ha6_0);
  RDT(b309_10, bb1, 8192);
  RDT(b309_11, bb1, 10240);
  MF(g0_11, b308_10, b308_11, ha6_1);
  MF(g0_02, b308_20, b308_21, ha6_0);
  RDT(b309_20, bb2, 8192);
  RDT(b309_21, bb2, 10240);
  MF(g0_12, b308_20, b308_21, ha6_1);
  WLG(4);
  MF(g0_03, b308_30, b308_31, ha6_0);
  RDT(b309_30, bb3, 8192);
  RDT(b309_31, bb3, 10240);
  GLD(ha9_1, v16, sh1, sL + L_HB + 7168);
  MF(g0_13, b308_30, b308_31, ha6_1);
  MF(g1_00, b309_00, b309_01, ha6_0);
  PRIO(2);
  RDT(b310_00, bb0, 12288);
  RDT(b310_01, bb0, 14336);
  MF(g1_10, b309_00, b309_01, ha6_1);
  WLG(4);
  MF(g1_01, b309_10, b309_11, ha6_0);
  RDT(b310_10, bb1, 12288);
  RDT(b310_11, bb1, 14336);
  DMA(r2, vw2, s_20, 262144, 36864);
  MF(g1_11, b309_10, b309_11, ha6_1);
  MF(g1_02, b309_20, b309_21, ha6_0);
  RDT(b310_20, bb2, 12288);
  RDT(b310_21, bb2, 14336);
  WVM(8);
  CHKF(fl2, 3u, vfl, sfi, sL + L_FH);
  MF(g1_12, b309_20, b309_21, ha6_1);
  WLG(4);
  MF(g1_03, b309_30, b309_31, ha6_0);
  RDT(b310_30, bb3, 12288);
  RDT(b310_31, bb3, 14336);
  MF(g1_13, b309_30, b309_31, ha6_1);
  DMA(r2, vw2, s_20, 327680, 40960);
  MF(g0_00, b310_00, b310_01, ha7_0);
  PRIO(1);
  RDT(b311_00, bb0, 16384);
  RDT(b311_01, bb0, 18432);
  MF(g0_10, b310_00, b310_01, ha7_1);
  WLG(4);
  MF(g0_01, b310_10, b310_11, ha7_0);
  RDT(b311_10, bb1, 16384);
  RDT(b311_11, bb1, 18432);
  MF(g0_11, b310_10, b310_11, ha7_1);
  DMA(r2, vw2, s_20, 786432, 53248);
  MF(g0_02, b310_20, b310_21, ha7_0);
  RDT(b311_20, bb2, 16384);
  RDT(b311_21, bb2, 18432);
  MF(g0_12, b310_20, b310_21, ha7_1);
  PRIO(0);
  WLG(4);
  MF(g0_03, b310_30, b310_31, ha7_0);
  RDT(b311_30, bb3, 16384);
  RDT(b311_31, bb3, 18432);
  MF(g0_13, b310_30, b310_31, ha7_1);
  DMA(r2, vw2, s_20, 851968, 57344);
  MF(g1_00, b311_00, b311_01, ha7_0);
  WVM(8);
  BAR(); STB(35);
  PRIO(3);
  RDT(b312_00, bb0, 20480);
  RDT(b312_01, bb0, 22528);
  MF(g1_10, b311_00, b311_01, ha7_1);
  WLG(4);
  MF(g1_01, b311_10, b311_11, ha7_0);
  RDT(b312_10, bb1, 20480);
  RDT(b312_11, bb1, 22528);
  WVM(6);
  unsigned hat8, hat8b; MX3(hat8, ha8_0[0], ha8_0[1], ha8_0[2]);
  MF(g1_11, b311_10, b311_11, ha7_1);
  MX3(hat8b, ha8_1[0], ha8_1[1], ha8_1[2]);
  MF(g1_02, b311_20, b311_21, ha7_0);
  RDT(b312_20, bb2, 20480);
  RDT(b312_21, bb2, 22528);
  GLD(ha10_0, v16, sh0, sL + L_HB + 4096);
  MX3(hat8, hat8, ha8_0[3], ha8_1[3]);
  MF(g1_12, b311_20, b311_21, ha7_1);
  MX2(hat8, hat8, hat8b);
  WLG(4);
  MF(g1_03, b311_30, b311_31, ha7_0);
  RDT(b312_30, bb3, 20480);
  RDT(b312_31, bb3, 22528);
  CHK2(hat8, ha8_0, ha8_1, v16, sh0, sL + L_HB + 6144, sh1, sL + L_HB + 6144, 4);
  MF(g1_13, b311_30, b311_31, ha7_1);
  MF(g0_00, b312_00, b312_01, ha8_0);
  RDT(b313_00, bb0, 24576);
  RDT(b313_01, bb0, 26624);
  GLD(ha10_1, v16, sh1, sL + L_HB + 4096);
  MF(g0_10, b312_00, b312_01, ha8_1);
  WLG(4);
  MF(g0_01, b312_10, b312_11, ha8_0);
  RDT(b313_10, bb1, 24576);
  RDT(b313_11, bb1, 26624);
  MF(g0_11, b312_10, b312_11, ha8_1);
  MF(g0_02, b312_20, b312_21, ha8_0);
  RDT(b313_20, bb2, 24576);
  RDT(b313_21, bb2, 26624);
  MF(g0_12, b312_20, b312_21, ha8_1);
  GLD(ha11_0, v16, sh0, sL + L_HB + 5120);
  WLG(4);
  MF(g0_03, b312_30, b312_31, ha8_0);
  RDT(b313_30, bb3, 24576);
  RDT(b313_31, bb3, 26624);
  MF(g0_13, b312_30, b312_31, ha8_1);
  MF(g1_00, b313_00, b313_01, ha8_0);
  PRIO(2);
  RDT(b314_00, bb0, 28672);
  RDT(b314_01, bb0, 30720);
  MF(g1_10, b313_00, b313_01, ha8_1);
  GLD(ha11_1, v16, sh1, sL + L_HB + 5120);
  WLG(4);
  MF(g1_01, b313_10, b313_11, ha8_0);
  RDT(b314_10, bb1, 28672);
  RDT(b314_11, bb1, 30720);
  WVM(8);
  unsigned hat9, hat9b; MX3(hat9, ha9_0[0], ha9_0[1], ha9_0[2]);
  MF(g1_11, b313_10, b313_11, ha8_1);
  MX3(hat9b, ha9_1[0], ha9_1[1], ha9_1[2]);
  MF(g1_02, b313_20, b313_21, ha8_0);
  RDT(b314_20, bb2, 28672);
  RDT(b314_21, bb2, 30720);
  MX3(hat9, hat9, ha9_0[3], ha9_1[3]);
  MF(g1_12, b313_20, b313_21, ha8_1);
  GLD(ha12_0, v16, sh0, sL + L_HB + 12288);
  MX2(hat9, hat9, hat9b);
  WLG(4);
  MF(g1_03, b313_30, b313_31, ha8_0);
  RDT(b314_30, bb3, 28672);
  RDT(b314_31, bb3, 30720);
  CHK2(hat9, ha9_0, ha9_1, v16, sh0, sL + L_HB + 7168, sh1, sL + L_HB + 7168, 4);
  MF(g1_13, b313_30, b313_31, ha8_1);
  MF(g0_00, b314_00, b314_01, ha9_0);
  PRIO(1);
  RDT(b315_00, bb0, 32768);
  RDT(b315_01, bb0, 34816);
  MF(g0_10, b314_00, b314_01, ha9_1);
  WLG(4);
  MF(g0_01, b314_10, b314_11, ha9_0);
  RDT(b315_10, bb1, 32768);
  RDT(b315_11, bb1, 34816);
  GLD(ha12_1, v16, sh1, sL + L_HB + 12288);
  MF(g0_11, b314_10, b314_11, ha9_1);
  MF(g0_02, b314_20, b314_21, ha9_0);
  RDT(b315_20, bb2, 32768);
  RDT(b315_21, bb2, 34816);
  MF(g0_12, b314_20, b314_21, ha9_1);
  PRIO(0);
  WLG(4);
  MF(g0_03, b314_30, b314_31, ha9_0);
  RDT(b315_30, bb3, 32768);
  RDT(b315_31, bb3, 34816);
  GLD(ha13_0, v16, sh0, sL + L_HB + 13312);
  MF(g0_13, b314_30, b314_31, ha9_1);
  MF(g1_00, b315_00, b315_01, ha9_0);
  WVM(7);
  BAR(); STB(36);
  PRIO(3);
  RDT(b316_00, bb0, 36864);
  RDT(b316_01, bb0, 38912);
  MF(g1_10, b315_00, b315_01, ha9_1);
  WLG(4);
  MF(g1_01, b315_10, b315_11, ha9_0);
  RDT(b316_10, bb1, 36864);
  RDT(b316_11, bb1, 38912);
  MF(g1_11, b315_10, b315_11, ha9_1);
  GLD(ha13_1, v16, sh1, sL + L_HB + 13312);
  MF(g1_02, b315_20, b315_21, ha9_0);
  RDT(b316_20, bb2, 36864);
  RDT(b316_21, bb2, 38912);
  MF(g1_12, b315_20, b315_21, ha9_1);
  WLG(4);
  MF(g1_03, b315_30, b315_31, ha9_0);
  RDT(b316_30, bb3, 36864);
  RDT(b316_31, bb3, 38912);
  MF(g1_13, b315_30, b315_31, ha9_1);
  DMA(r2, vw2, s_21, 262144, 45056);
  WVM(7);
  MF(g0_00, b316_00, b316_01, ha10_0);
  RDT(b317_00, bb0, 40960);
  RDT(b317_01, bb0, 43008);
  MF(g0_10, b316_00, b316_01, ha10_1);
  WLG(4);
  MF(g0_01, b316_10, b316_11, ha10_0);
  RDT(b317_10, bb1, 40960);
  RDT(b317_11, bb1, 43008);
  MF(g0_11, b316_10, b316_11, ha10_1);
  DMA(r2, vw2, s_21, 327680, 49152);
  MF(g0_02, b316_20, b316_21, ha10_0);
  RDT(b317_20, bb2, 40960);
  RDT(b317_21, bb2, 43008);
  MF(g0_12, b316_20, b316_21, ha10_1);
  WLG(4);
  MF(g0_03, b316_30, b316_31, ha10_0);
  RDT(b317_30, bb3, 40960);
  RDT(b317_31, bb3, 43008);
  MF(g0_13, b316_30, b316_31, ha10_1);
  WVM(6);
  MF(g0_00, b317_00, b317_01, ha11_0);
  PRIO(2);
  RDT(b318_00, bb0, 53248);
  RDT(b318_01, bb0, 55296);
  DMA(r2, vw2, s_21, 786432, 0);
  MF(g0_10, b317_00, b317_01, ha11_1);
  RDT(b319_00, bb0, 57344);
  RDT(b319_01, bb0, 59392);
  WLG(6);
  MF(g0_01, b317_10, b317_11, ha11_0);
  RDT(b318_10, bb1, 53248);
  RDT(b318_11, bb1, 55296);
  MF(g0_11, b317_10, b317_11, ha11_1);
  RDT(b319_10, bb1, 57344);
  RDT(b319_11, bb1, 59392);
  MF(g0_02, b317_20, b317_21, ha11_0);
  RDT(b318_20, bb2, 53248);
  RDT(b318_21, bb2, 55296);
  DMA(r2, vw2, s_21, 851968, 4096);
  MF(g0_12, b317_20, b317_21, ha11_1);
  RDT(b319_20, bb2, 57344);
  RDT(b319_21, bb2, 59392);
  WLG(8);
  MF(g0_03, b317_30, b317_31, ha11_0);
  RDT(b318_30, bb3, 53248);
  RDT(b318_31, bb3, 55296);
  MF(g0_13, b317_30, b317_31, ha11_1);
  RDT(b319_30, bb3, 57344);
  RDT(b319_31, bb3, 59392);
  WVM(6);
  MF(g0_10, b318_00, b318_01, ha12_1);
  PRIO(1);
  WVM(4);
  MF(g0_10, b319_00, b319_01, ha13_1);
  DMA(r2, vw2, s_22, 0, 8192);
  WLG(4);
  MF(g0_11, b318_10, b318_11, ha12_1);
  MF(g0_11, b319_10, b319_11, ha13_1);
  MF(g0_12, b318_20, b318_21, ha12_1);
  NOP(2);
  unsigned g0_s00, g0_s01; CVT(g0_s00, g0_10[0], g0_10[1]); CVT(g0_s01, g0_10[2], g0_10[3]);
  MF(g0_12, b319_20, b319_21, ha13_1);
  PRIO(0);
  DMA(r2, vw2, s_22, 65536, 12288);
  WLG(0);
  MF(g0_13, b318_30, b318_31, ha12_1);
  NOP(1);
  unsigned g0_s10, g0_s11; CVT(g0_s10, g0_11[0], g0_11[1]); CVT(g0_s11, g0_11[2], g0_11[3]);
  WR(((u32x4){g0_s00, g0_s01, g0_s10, g0_s11}), xw, 1024);
  MF(g0_13, b319_30, b319_31, ha13_1);
  MF(g0_00, b318_00, b318_01, ha12_0);
  NOP(0);
  unsigned g0_s20, g0_s21; CVT(g0_s20, g0_12[0], g0_12[1]); CVT(g0_s21, g0_12[2], g0_12[3]);
  MF(g0_01, b318_10, b318_11, ha12_0);
  DMA(r2, vw2, s_22, 524288, 16384);
  MF(g0_02, b318_20, b318_21, ha12_0);
  NOP(3);
  unsigned g0_s30, g0_s31; CVT(g0_s30, g0_13[0], g0_13[1]); CVT(g0_s31, g0_13[2], g0_13[3]);
  WR(((u32x4){g0_s20, g0_s21, g0_s30, g0_s31}), xw, 3072);
  MF(g0_03, b318_30, b318_31, ha12_0);
  WVM(3);
  WLG(0);
  BAR(); STB(37);
  PRIO(3);
  RDT(b320_00, bb0, 45056);
  RDT(b320_01, bb0, 47104);
  MF(g0_00, b319_00, b319_01, ha13_0);
  RDT(b320_10, bb1, 45056);
  RDT(b320_11, bb1, 47104);
  MF(g0_01, b319_10, b319_11, ha13_0);
  RDT(b320_20, bb2, 45056);
  RDT(b320_21, bb2, 47104);
  MF(g0_02, b319_20, b319_21, ha13_0);
  RDT(b320_30, bb3, 45056);
  RDT(b320_31, bb3, 47104);
  DMA(r2, vw2, s_22, 589824, 20480);
  MF(g0_03, b319_30, b319_31, ha13_0);
  STP(17);
  WLG(4);
  MF(g1_00, b320_00, b320_01, ha10_0);
  RDT(b321_00, bb0, 49152);
  RDT(b321_01, bb0, 51200);
  RDF(g0_r1, xr, 1024);
  RDF(g0_r3, xr, 3072);
  MF(g1_10, b320_00, b320_01, ha10_1);
  MF(g1_01, b320_10, b320_11, ha10_0);
  RDT(b321_10, bb1, 49152);
  RDT(b321_11, bb1, 51200);
  DMA(r2, vw2, s_22, 131072, 24576);
  MF(g1_11, b320_10, b320_11, ha10_1);
  f32x2 g0_t00, g0_t01;
  WLG(3);
  { float u_; ULO(u_, g0_r1[0]); g0_t00[0] = u_; }
  { float u_; UHI(u_, g0_r1[0]); g0_t00[1] = u_; }
  MF(g1_02, b320_20, b320_21, ha10_0);
  RDT(b321_20, bb2, 49152);
  RDT(b321_21, bb2, 51200);
  { float u_; ULO(u_, g0_r1[1]); g0_t01[0] = u_; }
  { float u_; UHI(u_, g0_r1[1]); g0_t01[1] = u_; }
  MF(g1_12, b320_20, b320_21, ha10_1);
  ADD(g0_t00[0], g0_t00[0], g0_00[0]);
  ADD(g0_t00[1], g0_t00[1], g0_00[1]);
  MF(g1_03, b320_30, b320_31, ha10_0);
  RDT(b321_30, bb3, 49152);
  RDT(b321_31, bb3, 51200);
  ADD(g0_t01[0], g0_t01[0], g0_00[2]);
  ADD(g0_t01[1], g0_t01[1], g0_00[3]);
  MF(g1_13, b320_30, b320_31, ha10_1);
  DMA(r2, vw2, s_22, 196608, 28672);
  unsigned g0_p00; CVT(g0_p00, g0_t00[0], g0_t00[1]);
  unsigned g0_p01; CVT(g0_p01, g0_t01[0], g0_t01[1]);
  MF(g1_00, b321_00, b321_01, ha11_0);
  RDT(b322_00, bb0, 0);
  RDT(b322_01, bb0, 2048);
  f32x2 g0_t10, g0_t11;
  { float u_; ULO(u_, g0_r1[2]); g0_t10[0] = u_; }
  { float u_; UHI(u_, g0_r1[2]); g0_t10[1] = u_; }
  MF(g1_10, b321_00, b321_01, ha11_1);
  RDT(b323_00, bb0, 4096);
  RDT(b323_01, bb0, 6144);
  { float u_; ULO(u_, g0_r1[3]); g0_t11[0] = u_; }
  { float u_; UHI(u_, g0_r1[3]); g0_t11[1] = u_; }
  WLG(6);
  MF(g1_01, b321_10, b321_11, ha11_0);
  RDT(b322_10, bb1, 0);
  RDT(b322_11, bb1, 2048);
  ADD(g0_t10[0], g0_t10[0], g0_01[0]);
  ADD(g0_t10[1], g0_t10[1], g0_01[1]);
  MF(g1_11, b321_10, b321_11, ha11_1);
  PRIO(2);
  RDT(b323_10, bb1, 4096);
  RDT(b323_11, bb1, 6144);
  DMA(r2, vw2, s_22, 655360, 32768);
  ADD(g0_t11[0], g0_t11[0], g0_01[2]);
  ADD(g0_t11[1], g0_t11[1], g0_01[3]);
  MF(g1_02, b321_20, b321_21, ha11_0);
  RDT(b322_20, bb2, 0);
  RDT(b322_21, bb2, 2048);
  unsigned g0_p10; CVT(g0_p10, g0_t10[0], g0_t10[1]);
  unsigned g0_p11; CVT(g0_p11, g0_t11[0], g0_t11[1]);
  MF(g1_12, b321_20, b321_21, ha11_1);
  RDT(b323_20, bb2, 4096);
  RDT(b323_21, bb2, 6144);
  const u32x4 g0_o0 = {g0_p00, g0_p01, g0_p10, g0_p11};
  GST(g0_o0, v16, sp0, sL + L_P + 0);
  f32x2 g0_t20, g0_t21;
  { float u_; ULO(u_, g0_r3[0]); g0_t20[0] = u_; }
  WLG(8);
  MF(g1_03, b321_30, b321_31, ha11_0);
  RDT(b322_30, bb3, 0);
  RDT(b322_31, bb3, 2048);
  { float u_; UHI(u_, g0_r3[0]); g0_t20[1] = u_; }
  { float u_; ULO(u_, g0_r3[1]); g0_t21[0] = u_; }
  MF(g1_13, b321_30, b321_31, ha11_1);
  RDT(b323_30, bb3, 4096);
  RDT(b323_31, bb3, 6144);
  { float u_; UHI(u_, g0_r3[1]); g0_t21[1] = u_; }
  ADD(g0_t20[0], g0_t20[0], g0_02[0]);
  MF(g1_10, b322_00, b322_01, ha12_1);
  ADD(g0_t20[1], g0_t20[1], g0_02[1]);
  ADD(g0_t21[0], g0_t21[0], g0_02[2]);
  MF(g1_10, b323_00, b323_01, ha13_1);
  ADD(g0_t21[1], g0_t21[1], g0_02[3]);
  unsigned g0_p20; CVT(g0_p20, g0_t20[0], g0_t20[1]);
  WLG(4);
  MF(g1_11, b322_10, b322_11, ha12_1);
  DMA(r2, vw2, s_22, 720896, 36864);
  unsigned g0_p21; CVT(g0_p21, g0_t21[0], g0_t21[1]);
  f32x2 g0_t30, g0_t31;
  { float u_; ULO(u_, g0_r3[2]); g0_t30[0] = u_; }
  MF(g1_11, b323_10, b323_11, ha13_1);
  PRIO(1);
  { float u_; UHI(u_, g0_r3[2]); g0_t30[1] = u_; }
  { float u_; ULO(u_, g0_r3[3]); g0_t31[0] = u_; }
  MF(g1_12, b322_20, b322_21, ha12_1);
  { float u_; UHI(u_, g0_r3[3]); g0_t31[1] = u_; }
  ADD(g0_t30[0], g0_t30[0], g0_03[0]);
  unsigned g1_s00, g1_s01; CVT(g1_s00, g1_10[0], g1_10[1]); CVT(g1_s01, g1_10[2], g1_10[3]);
  MF(g1_12, b323_20, b323_21, ha13_1);
  ADD(g0_t30[1], g0_t30[1], g0_03[1]);
  ADD(g0_t31[0], g0_t31[0], g0_03[2]);
  WLG(0);
  MF(g1_13, b322_30, b322_31, ha12_1);
  ADD(g0_t31[1], g0_t31[1], g0_03[3]);
  unsigned g0_p30; CVT(g0_p30, g0_t30[0], g0_t30[1]);
  unsigned g1_s10, g1_s11; CVT(g1_s10, g1_11[0], g1_11[1]); CVT(g1_s11, g1_11[2], g1_11[3]);
  WR(((u32x4){g1_s00, g1_s01, g1_s10, g1_s11}), xw, 0);
  MF(g1_13, b323_30, b323_31, ha13_1);
  DMA(r2, vw2, s_22, 393216, 40960);
  unsigned g0_p31; CVT(g0_p31, g0_t31[0], g0_t31[1]);
  const u32x4 g0_o1 = {g0_p20, g0_p21, g0_p30, g0_p31};
  GST(g0_o1, v16, sp0, sL + L_P + 1024);
  MF(g1_00, b322_00, b322_01, ha12_0);
  PRIO(0);
  unsigned g1_s20, g1_s21; CVT(g1_s20, g1_12[0], g1_12[1]); CVT(g1_s21, g1_12[2], g1_12[3]);
  MF(g1_01, b322_10, b322_11, ha12_0);
  MF(g1_02, b322_20, b322_21, ha12_0);
  NOP(2);
  unsigned g1_s30, g1_s31; CVT(g1_s30, g1_13[0], g1_13[1]); CVT(g1_s31, g1_13[2], g1_13[3]);
  WR(((u32x4){g1_s20, g1_s21, g1_s30, g1_s31}), xw, 2048);
  MF(g1_03, b322_30, b322_31, ha12_0);
  WVM(7);
  WLG(0);
  BAR(); STB(38);
  PRIO(3);
  RDT(b324_00, bb0, 8192);
  RDT(b324_01, bb0, 10240);
  MF(g1_00, b323_00, b323_01, ha13_0);
  RDT(b324_10, bb1, 8192);
  RDT(b324_11, bb1, 10240);
  DMA(r2, vw2, s_22, 458752, 53248);
  MF(g1_01, b323_10, b323_11, ha13_0);
  RDT(b324_20, bb2, 8192);
  RDT(b324_21, bb2, 10240);
  MF(g1_02, b323_20, b323_21, ha13_0);
  RDT(b324_30, bb3, 8192);
  RDT(b324_31, bb3, 10240);
  MF(g1_03, b323_30, b323_31, ha13_0);
  STP(18);
  WLG(4);
  MF0(g2_00, b324_00, b324_01, ha0_0);
  RDT(b325_00, bb0, 12288);
  RDT(b325_01, bb0, 14336);
  RDF(g1_r1, xr, 0);
  RDF(g1_r3, xr, 2048);
  MF0(g2_10, b324_00, b324_01, ha0_1);
  DMA(r2, vw2, s_22, 262144, 57344);
  MF0(g2_01, b324_10, b324_11, ha0_0);
  RDT(b325_10, bb1, 12288);
  RDT(b325_11, bb1, 14336);
  MF0(g2_11, b324_10, b324_11, ha0_1);
  f32x2 g1_t00, g1_t01;
  WLG(3);
  { float u_; ULO(u_, g1_r1[0]); g1_t00[0] = u_; }
  { float u_; UHI(u_, g1_r1[0]); g1_t00[1] = u_; }
  MF0(g2_02, b324_20, b324_21, ha0_0);
  RDT(b325_20, bb2, 12288);
  RDT(b325_21, bb2, 14336);
  { float u_; ULO(u_, g1_r1[1]); g1_t01[0] = u_; }
  { float u_; UHI(u_, g1_r1[1]); g1_t01[1] = u_; }
  MF0(g2_12, b324_20, b324_21, ha0_1);
  DMA(r2, vw2, s_22, 327680, 45056);
  ADD(g1_t00[0], g1_t00[0], g1_00[0]);
  ADD(g1_t00[1], g1_t00[1], g1_00[1]);
  MF0(g2_03, b324_30, b324_31, ha0_0);
  RDT(b325_30, bb3, 12288);
  RDT(b325_31, bb3, 14336);
  ADD(g1_t01[0], g1_t01[0], g1_00[2]);
  ADD(g1_t01[1], g1_t01[1], g1_00[3]);
  MF0(g2_13, b324_30, b324_31, ha0_1);
  unsigned g1_p00; CVT(g1_p00, g1_t00[0], g1_t00[1]);
  unsigned g1_p01; CVT(g1_p01, g1_t01[0], g1_t01[1]);
  MF(g2_00, b325_00, b325_01, ha1_0);
  RDT(b326_00, bb0, 16384);
  RDT(b326_01, bb0, 18432);
  f32x2 g1_t10, g1_t11;
  { float u_; ULO(u_, g1_r1[2]); g1_t10[0] = u_; }
  { float u_; UHI(u_, g1_r1[2]); g1_t10[1] = u_; }
  MF(g2_10, b325_00, b325_01, ha1_1);
  { float u_; ULO(u_, g1_r1[3]); g1_t11[0] = u_; }
  { float u_; UHI(u_, g1_r1[3]); g1_t11[1] = u_; }
  WLG(4);
  MF(g2_01, b325_10, b325_11, ha1_0);
  RDT(b326_10, bb1, 16384);
  RDT(b326_11, bb1, 18432);
  DMA(r2, vw2, s_22, 786432, 49152);
  ADD(g1_t10[0], g1_t10[0], g1_01[0]);
  ADD(g1_t10[1], g1_t10[1], g1_01[1]);
  MF(g2_11, b325_10, b325_11, ha1_1);
  PRIO(2);
  ADD(g1_t11[0], g1_t11[0], g1_01[2]);
  ADD(g1_t11[1], g1_t11[1], g1_01[3]);
  MF(g2_02, b325_20, b325_21, ha1_0);
  RDT(b326_20, bb2, 16384);
  RDT(b326_21, bb2, 18432);
  unsigned g1_p10; CVT(g1_p10, g1_t10[0], g1_t10[1]);
  unsigned g1_p11; CVT(g1_p11, g1_t11[0], g1_t11[1]);
  MF(g2_12, b325_20, b325_21, ha1_1);
  const u32x4 g1_o0 = {g1_p00, g1_p01, g1_p10, g1_p11};
  GST(g1_o0, v16, sp1, sL + L_P + 0);
  f32x2 g1_t20, g1_t21;
  { float u_; ULO(u_, g1_r3[0]); g1_t20[0] = u_; }
  WLG(4);
  MF(g2_03, b325_30, b325_31, ha1_0);
  RDT(b326_30, bb3, 16384);
  RDT(b326_31, bb3, 18432);
  { float u_; UHI(u_, g1_r3[0]); g1_t20[1] = u_; }
  { float u_; ULO(u_, g1_r3[1]); g1_t21[0] = u_; }
  MF(g2_13, b325_30, b325_31, ha1_1);
  { float u_; UHI(u_, g1_r3[1]); g1_t21[1] = u_; }
  ADD(g1_t20[0], g1_t20[0], g1_02[0]);
  MF(g2_00, b326_00, b326_01, ha2_0);
  RDT(b327_00, bb0, 20480);
  RDT(b327_01, bb0, 22528);
  ADD(g1_t20[1], g1_t20[1], g1_02[1]);
  ADD(g1_t21[0], g1_t21[0], g1_02[2]);
  MF(g2_10, b326_00, b326_01, ha2_1);
  ADD(g1_t21[1], g1_t21[1], g1_02[3]);
  unsigned g1_p20; CVT(g1_p20, g1_t20[0], g1_t20[1]);
  WLG(4);
  MF(g2_01, b326_10, b326_11, ha2_0);
  RDT(b327_10, bb1, 20480);
  RDT(b327_11, bb1, 22528);
  DMA(r2, vw2, s_22, 851968, 0);
  unsigned g1_p21; CVT(g1_p21, g1_t21[0], g1_t21[1]);
  f32x2 g1_t30, g1_t31;
  { float u_; ULO(u_, g1_r3[2]); g1_t30[0] = u_; }
  MF(g2_11, b326_10, b326_11, ha2_1);
  PRIO(1);
  { float u_; UHI(u_, g1_r3[2]); g1_t30[1] = u_; }
  { float u_; ULO(u_, g1_r3[3]); g1_t31[0] = u_; }
  MF(g2_02, b326_20, b326_21, ha2_0);
  RDT(b327_20, bb2, 20480);
  RDT(b327_21, bb2, 22528);
  { float u_; UHI(u_, g1_r3[3]); g1_t31[1] = u_; }
  ADD(g1_t30[0], g1_t30[0], g1_03[0]);
  MF(g2_12, b326_20, b326_21, ha2_1);
  ADD(g1_t30[1], g1_t30[1], g1_03[1]);
  ADD(g1_t31[0], g1_t31[0], g1_03[2]);
  WLG(4);
  MF(g2_03, b326_30, b326_31, ha2_0);
  RDT(b327_30, bb3, 20480);
  RDT(b327_31, bb3, 22528);
  ADD(g1_t31[1], g1_t31[1], g1_03[3]);
  unsigned g1_p30; CVT(g1_p30, g1_t30[0], g1_t30[1]);
  MF(g2_13, b326_30, b326_31, ha2_1);
  DMA(r2, vw2, s_23, 0, 4096);
  unsigned g1_p31; CVT(g1_p31, g1_t31[0], g1_t31[1]);
  const u32x4 g1_o1 = {g1_p20, g1_p21, g1_p30, g1_p31};
  GST(g1_o1, v16, sp1, sL + L_P + 1024);
  MF(g2_00, b327_00, b327_01, ha3_0);
  PRIO(0);
  WVM(10);
  BAR(); STB(39);
  PRIO(3);
  RDT(b328_00, bb0, 24576);
  RDT(b328_01, bb0, 26624);
  MF(g2_10, b327_00, b327_01, ha3_1);
  WLG(4);
  MF(g2_01, b327_10, b327_11, ha3_0);
  RDT(b328_10, bb1, 24576);
  RDT(b328_11, bb1, 26624);
  MF(g2_11, b327_10, b327_11, ha3_1);
  MF(g2_02, b327_20, b327_21, ha3_0);
  RDT(b328_20, bb2, 24576);
  RDT(b328_21, bb2, 26624);
  DMA(r2, vw2, s_23, 65536, 8192);
  MF(g2_12, b327_20, b327_21, ha3_1);
  WLG(4);
  MF(g2_03, b327_30, b327_31, ha3_0);
  RDT(b328_30, bb3, 24576);
  RDT(b328_31, bb3, 26624);
  MF(g2_13, b327_30, b327_31, ha3_1);
  MF(g2_00, b328_00, b328_01, ha4_0);
  RDT(b329_00, bb0, 28672);
  RDT(b329_01, bb0, 30720);
  MF(g2_10, b328_00, b328_01, ha4_1);
  DMA(r2, vw2, s_23, 524288, 12288);
  WLG(4);
  MF(g2_01, b328_10, b328_11, ha4_0);
  RDT(b329_10, bb1, 28672);
  RDT(b329_11, bb1, 30720);
  MF(g2_11, b328_10, b328_11, ha4_1);
  MF(g2_02, b328_20, b328_21, ha4_0);
  RDT(b329_20, bb2, 28672);
  RDT(b329_21, bb2, 30720);
  MF(g2_12, b328_20, b328_21, ha4_1);
  DMA(r2, vw2, s_23, 589824, 16384);
  WLG(4);
  MF(g2_03, b328_30, b328_31, ha4_0);
  RDT(b329_30, bb3, 28672);
  RDT(b329_31, bb3, 30720);
  MF(g2_13, b328_30, b328_31, ha4_1);
  MF(g2_00, b329_00, b329_01, ha5_0);
  PRIO(2);
  RDT(b330_00, bb0, 32768);
  RDT(b330_01, bb0, 34816);
  MF(g2_10, b329_00, b329_01, ha5_1);
  WLG(4);
  MF(g2_01, b329_10, b329_11, ha5_0);
  RDT(b330_10, bb1, 32768);
  RDT(b330_11, bb1, 34816);
  MF(g2_11, b329_10, b329_11, ha5_1);
  MF(g2_02, b329_20, b329_21, ha5_0);
  RDT(b330_20, bb2, 32768);
  RDT(b330_21, bb2, 34816);
  MF(g2_12, b329_20, b329_21, ha5_1);
  WLG(4);
  MF(g2_03, b329_30, b329_31, ha5_0);
  RDT(b330_30, bb3, 32768);
  RDT(b330_31, bb3, 34816);
  MF(g2_13, b329_30, b329_31, ha5_1);
  MF(g2_00, b330_00, b330_01, ha6_0);
  PRIO(1);
  RDT(b331_00, bb0, 36864);
  RDT(b331_01, bb0, 38912);
  MF(g2_10, b330_00, b330_01, ha6_1);
  WLG(4);
  MF(g2_01, b330_10, b330_11, ha6_0);
  RDT(b331_10, bb1, 36864);
  RDT(b331_11, bb1, 38912);
  MF(g2_11, b330_10, b330_11, ha6_1);
  MF(g2_02, b330_20, b330_21, ha6_0);
  RDT(b331_20, bb2, 36864);
  RDT(b331_21, bb2, 38912);
  MF(g2_12, b330_20, b330_21, ha6_1);
  PRIO(0);
  WLG(4);
  MF(g2_03, b330_30, b330_31, ha6_0);
  RDT(b331_30, bb3, 36864);
  RDT(b331_31, bb3, 38912);
  MF(g2_13, b330_30, b330_31, ha6_1);
  MF(g2_00, b331_00, b331_01, ha7_0);
  WVM(9);
  BAR(); STB(40);
  PRIO(3);
  RDT(b332_00, bb0, 40960);
  RDT(b332_01, bb0, 43008);
  DMA(r2, vw2, s_23, 131072, 20480);
  MF(g2_10, b331_00, b331_01, ha7_1);
  WLG(4);
  MF(g2_01, b331_10, b331_11, ha7_0);
  RDT(b332_10, bb1, 40960);
  RDT(b332_11, bb1, 43008);
  MF(g2_11, b331_10, b331_11, ha7_1);
  MF(g2_02, b331_20, b331_21, ha7_0);
  RDT(b332_20, bb2, 40960);
  RDT(b332_21, bb2, 43008);
  DMA(r2, vw2, s_23, 196608, 24576);
  MF(g2_12, b331_20, b331_21, ha7_1);
  WLG(4);
  MF(g2_03, b331_30, b331_31, ha7_0);
  RDT(b332_30, bb3, 40960);
  RDT(b332_31, bb3, 43008);
  MF(g2_13, b331_30, b331_31, ha7_1);
  MF(g2_00, b332_00, b332_01, ha8_0);
  RDT(b333_00, bb0, 53248);
  RDT(b333_01, bb0, 55296);
  DMA(r2, vw2, s_23, 655360, 28672);
  MF(g2_10, b332_00, b332_01, ha8_1);
  WLG(4);
  MF(g2_01, b332_10, b332_11, ha8_0);
  RDT(b333_10, bb1, 53248);
  RDT(b333_11, bb1, 55296);
  MF(g2_11, b332_10, b332_11, ha8_1);
  MF(g2_02, b332_20, b332_21, ha8_0);
  RDT(b333_20, bb2, 53248);
  RDT(b333_21, bb2, 55296);
  MF(g2_12, b332_20, b332_21, ha8_1);
  DMA(r2, vw2, s_23, 720896, 32768);
  WLG(4);
  MF(g2_03, b332_30, b332_31, ha8_0);
  RDT(b333_30, bb3, 53248);
  RDT(b333_31, bb3, 55296);
  MF(g2_13, b332_30, b332_31, ha8_1);
  MF(g2_00, b333_00, b333_01, ha9_0);
  PRIO(2);
  RDT(b334_00, bb0, 57344);
  RDT(b334_01, bb0, 59392);
  MF(g2_10, b333_00, b333_01, ha9_1);
  WLG(4);
  MF(g2_01, b333_10, b333_11, ha9_0);
  RDT(b334_10, bb1, 57344);
  RDT(b334_11, bb1, 59392);
  WVM(7);
  GSIG(4, sfo, sL + L_FH);
  MF(g2_11, b333_10, b333_11, ha9_1);
  MF(g2_02, b333_20, b333_21, ha9_0);
  RDT(b334_20, bb2, 57344);
  RDT(b334_21, bb2, 59392);
  MF(g2_12, b333_20, b333_21, ha9_1);
  WLG(4);
  MF(g2_03, b333_30, b333_31, ha9_0);
  RDT(b334_30, bb3, 57344);
  RDT(b334_31, bb3, 59392);
  MF(g2_13, b333_30, b333_31, ha9_1);
  MF(g2_00, b334_00, b334_01, ha10_0);
  PRIO(1);
  BAR(); STB(41);
  PRIO(3);
  RDT(b335_00, bb0, 45056);
  RDT(b335_01, bb0, 47104);
  DMA(r2, vw2, s_23, 393216, 36864);
  MF(g2_10, b334_00, b334_01, ha10_1);
  WLG(4);
  MF(g2_01, b334_10, b334_11, ha10_0);
  RDT(b335_10, bb1, 45056);
  RDT(b335_11, bb1, 47104);
  MF(g2_11, b334_10, b334_11, ha10_1);
  MF(g2_02, b334_20, b334_21, ha10_0);
  RDT(b335_20, bb2, 45056);
  RDT(b335_21, bb2, 47104);
  DMA(r2, vw2, s_23, 458752, 40960);
  MF(g2_12, b334_20, b334_21, ha10_1);
  WLG(4);
  MF(g2_03, b334_30, b334_31, ha10_0);
  RDT(b335_30, bb3, 45056);
  RDT(b335_31, bb3, 47104);
  MF(g2_13, b334_30, b334_31, ha10_1);
  MF(g2_00, b335_00, b335_01, ha11_0);
  RDT(b336_00, bb0, 49152);
  RDT(b336_01, bb0, 51200);
  DMA(r2, vw2, s_23, 262144, 53248);
  MF(g2_10, b335_00, b335_01, ha11_1);
  RDT(b337_00, bb0, 0);
  RDT(b337_01, bb0, 2048);
  WLG(6);
  MF(g2_01, b335_10, b335_11, ha11_0);
  RDT(b336_10, bb1, 49152);
  RDT(b336_11, bb1, 51200);
  MF(g2_11, b335_10, b335_11, ha11_1);
  RDT(b337_10, bb1, 0);
  RDT(b337_11, bb1, 2048);
  MF(g2_02, b335_20, b335_21, ha11_0);
  RDT(b336_20, bb2, 49152);
  RDT(b336_21, bb2, 51200);
  MF(g2_12, b335_20, b335_21, ha11_1);
  RDT(b337_20, bb2, 0);
  RDT(b337_21, bb2, 2048);
  WLG(8);
  MF(g2_03, b335_30, b335_31, ha11_0);
  RDT(b336_30, bb3, 49152);
  RDT(b336_31, bb3, 51200);
  MF(g2_13, b335_30, b335_31, ha11_1);
  RDT(b337_30, bb3, 0);
  RDT(b337_31, bb3, 2048);
  MF(g2_10, b336_00, b336_01, ha12_1);
  PRIO(2);
  MF(g2_10, b337_00, b337_01, ha13_1);
  WLG(4);
  MF(g2_11, b336_10, b336_11, ha12_1);
  MF(g2_11, b337_10, b337_11, ha13_1);
  MF(g2_12, b336_20, b336_21, ha12_1);
  NOP(3);
  unsigned g2_s00, g2_s01; CVT(g2_s00, g2_10[0], g2_10[1]); CVT(g2_s01, g2_10[2], g2_10[3]);
  MF(g2_12, b337_20, b337_21, ha13_1);
  WLG(0);
  MF(g2_13, b336_30, b336_31, ha12_1);
  NOP(3);
  unsigned g2_s10, g2_s11; CVT(g2_s10, g2_11[0], g2_11[1]); CVT(g2_s11, g2_11[2], g2_11[3]);
  WR(((u32x4){g2_s00, g2_s01, g2_s10, g2_s11}), xw, 1024);
  MF(g2_13, b337_30, b337_31, ha13_1);
  MF(g2_00, b336_00, b336_01, ha12_0);
  PRIO(1);
  NOP(1);
  unsigned g2_s20, g2_s21; CVT(g2_s20, g2_12[0], g2_12[1]); CVT(g2_s21, g2_12[2], g2_12[3]);
  MF(g2_01, b336_10, b336_11, ha12_0);
  MF(g2_02, b336_20, b336_21, ha12_0);
  NOP(3);
  unsigned g2_s30, g2_s31; CVT(g2_s30, g2_13[0], g2_13[1]); CVT(g2_s31, g2_13[2], g2_13[3]);
  WR(((u32x4){g2_s20, g2_s21, g2_s30, g2_s31}), xw, 3072);
  MF(g2_03, b336_30, b336_31, ha12_0);
  WVM(8);
  WLG(0);
  BAR(); STB(42);
  PRIO(3);
  RDT(b338_00, bb0, 4096);
  RDT(b338_01, bb0, 6144);
  DMA(r2, vw2, s_23, 327680, 57344);
  MF(g2_00, b337_00, b337_01, ha13_0);
  RDT(b338_10, bb1, 4096);
  RDT(b338_11, bb1, 6144);
  MF(g2_01, b337_10, b337_11, ha13_0);
  RDT(b338_20, bb2, 4096);
  RDT(b338_21, bb2, 6144);
  MF(g2_02, b337_20, b337_21, ha13_0);
  RDT(b338_30, bb3, 4096);
  RDT(b338_31, bb3, 6144);
  MF(g2_03, b337_30, b337_31, ha13_0);
  DMA(r2, vw2, s_23, 786432, 45056);
  WLG(4);
  MF0(g3_00, b338_00, b338_01, ha0_0);
  RDT(b339_00, bb0, 8192);
  RDT(b339_01, bb0, 10240);
  RDF(g2_r1, xr, 1024);
  RDF(g2_r3, xr, 3072);
  unsigned vfl2 = vfl * 8; PIN(vfl2);
  GLD1(fl3, vfl2, sfp, sL + L_FH);
  MF0(g3_10, b338_00, b338_01, ha0_1);
  MF0(g3_01, b338_10, b338_11, ha0_0);
  RDT(b339_10, bb1, 8192);
  RDT(b339_11, bb1, 10240);
  MF0(g3_11, b338_10, b338_11, ha0_1);
  DMA(r2, vw2, s_23, 851968, 49152);
  f32x2 g2_t00, g2_t01;
  WLG(3);
  { float u_; ULO(u_, g2_r1[0]); g2_t00[0] = u_; }
  { float u_; UHI(u_, g2_r1[0]); g2_t00[1] = u_; }
  MF0(g3_02, b338_20, b338_21, ha0_0);
  RDT(b339_20, bb2, 8192);
  RDT(b339_21, bb2, 10240);
  { float u_; ULO(u_, g2_r1[1]); g2_t01[0] = u_; }
  { float u_; UHI(u_, g2_r1[1]); g2_t01[1] = u_; }
  MF0(g3_12, b338_20, b338_21, ha0_1);
  ADD(g2_t00[0], g2_t00[0], g2_00[0]);
  ADD(g2_t00[1], g2_t00[1], g2_00[1]);
  MF0(g3_03, b338_30, b338_31, ha0_0);
  RDT(b339_30, bb3, 8192);
  RDT(b339_31, bb3, 10240);
  ADD(g2_t01[0], g2_t01[0], g2_00[2]);
  ADD(g2_t01[1], g2_t01[1], g2_00[3]);
  MF0(g3_13, b338_30, b338_31, ha0_1);
  unsigned g2_p00; CVT(g2_p00, g2_t00[0], g2_t00[1]);
  unsigned g2_p01; CVT(g2_p01, g2_t01[0], g2_t01[1]);
  MF(g3_00, b339_00, b339_01, ha1_0);
  RDT(b340_00, bb0, 12288);
  RDT(b340_01, bb0, 14336);
  CLP(g2_p00);
  CLP(g2_p01);
  MF(g3_10, b339_00, b339_01, ha1_1);
  f32x2 g2_t10, g2_t11;
  { float u_; ULO(u_, g2_r1[2]); g2_t10[0] = u_; }
  { float u_; UHI(u_, g2_r1[2]); g2_t10[1] = u_; }
  WLG(4);
  MF(g3_01, b339_10, b339_11, ha1_0);
  RDT(b340_10, bb1, 12288);
  RDT(b340_11, bb1, 14336);
  { float u_; ULO(u_, g2_r1[3]); g2_t11[0] = u_; }
  { float u_; UHI(u_, g2_r1[3]); g2_t11[1] = u_; }
  MF(g3_11, b339_10, b339_11, ha1_1);
  PRIO(2);
  ADD(g2_t10[0], g2_t10[0], g2_01[0]);
  ADD(g2_t10[1], g2_t10[1], g2_01[1]);
  MF(g3_02, b339_20, b339_21, ha1_0);
  RDT(b340_20, bb2, 12288);
  RDT(b340_21, bb2, 14336);
  ADD(g2_t11[0], g2_t11[0], g2_01[2]);
  ADD(g2_t11[1], g2_t11[1], g2_01[3]);
  MF(g3_12, b339_20, b339_21, ha1_1);
  unsigned g2_p10; CVT(g2_p10, g2_t10[0], g2_t10[1]);
  unsigned g2_p11; CVT(g2_p11, g2_t11[0], g2_t11[1]);
  WLG(4);
  MF(g3_03, b339_30, b339_31, ha1_0);
  RDT(b340_30, bb3, 12288);
  RDT(b340_31, bb3, 14336);
  CLP(g2_p10);
  CLP(g2_p11);
  MF(g3_13, b339_30, b339_31, ha1_1);
  const u32x4 g2_o0 = {g2_p00, g2_p01, g2_p10, g2_p11};
  GST(g2_o0, v16, sp2, sL + L_P + 0);
  f32x2 g2_t20, g2_t21;
  { float u_; ULO(u_, g2_r3[0]); g2_t20[0] = u_; }
  MF(g3_00, b340_00, b340_01, ha2_0);
  RDT(b341_00, bb0, 16384);
  RDT(b341_01, bb0, 18432);
  { float u_; UHI(u_, g2_r3[0]); g2_t20[1] = u_; }
  { float u_; ULO(u_, g2_r3[1]); g2_t21[0] = u_; }
  MF(g3_10, b340_00, b340_01, ha2_1);
  { float u_; UHI(u_, g2_r3[1]); g2_t21[1] = u_; }
  ADD(g2_t20[0], g2_t20[0], g2_02[0]);
  WLG(4);
  MF(g3_01, b340_10, b340_11, ha2_0);
  RDT(b341_10, bb1, 16384);
  RDT(b341_11, bb1, 18432);
  ADD(g2_t20[1], g2_t20[1], g2_02[1]);
  ADD(g2_t21[0], g2_t21[0], g2_02[2]);
  MF(g3_11, b340_10, b340_11, ha2_1);
  PRIO(1);
  ADD(g2_t21[1], g2_t21[1], g2_02[3]);
  unsigned g2_p20; CVT(g2_p20, g2_t20[0], g2_t20[1]);
  MF(g3_02, b340_20, b340_21, ha2_0);
  RDT(b341_20, bb2, 16384);
  RDT(b341_21, bb2, 18432);
  unsigned g2_p21; CVT(g2_p21, g2_t21[0], g2_t21[1]);
  CLP(g2_p20);
  MF(g3_12, b340_20, b340_21, ha2_1);
  GLD(ry0, v16, sx2, sL + L_X1 + 0);
  CLP(g2_p21);
  f32x2 g2_t30, g2_t31;
  { float u_; ULO(u_, g2_r3[2]); g2_t30[0] = u_; }
  WLG(4);
  MF(g3_03, b340_30, b340_31, ha2_0);
  RDT(b341_30, bb3, 16384);
  RDT(b341_31, bb3, 18432);
  { float u_; UHI(u_, g2_r3[2]); g2_t30[1] = u_; }
  { float u_; ULO(u_, g2_r3[3]); g2_t31[0] = u_; }
  MF(g3_13, b340_30, b340_31, ha2_1);
  { float u_; UHI(u_, g2_r3[3]); g2_t31[1] = u_; }
  ADD(g2_t30[0], g2_t30[0], g2_03[0]);
  MF(g3_00, b341_00, b341_01, ha3_0);
  PRIO(0);
  WVM(10);
  BAR(); STB(43);
  PRIO(3);
  RDT(b342_00, bb0, 20480);
  RDT(b342_01, bb0, 22528);
  ADD(g2_t30[1], g2_t30[1], g2_03[1]);
  ADD(g2_t31[0], g2_t31[0], g2_03[2]);
  MF(g3_10, b341_00, b341_01, ha3_1);
  GLD(ry1, v16, sx2, sL + L_X1 + 1024);
  ADD(g2_t31[1], g2_t31[1], g2_03[3]);
  unsigned g2_p30; CVT(g2_p30, g2_t30[0], g2_t30[1]);
  WLG(4);
  MF(g3_01, b341_10, b341_11, ha3_0);
  RDT(b342_10, bb1, 20480);
  RDT(b342_11, bb1, 22528);
  unsigned g2_p31; CVT(g2_p31, g2_t31[0], g2_t31[1]);
  CLP(g2_p30);
  MF(g3_11, b341_10, b341_11, ha3_1);
  CLP(g2_p31);
  const u32x4 g2_o1 = {g2_p20, g2_p21, g2_p30, g2_p31};
  GST(g2_o1, v16, sp2, sL + L_P + 1024);
  MF(g3_02, b341_20, b341_21, ha3_0);
  RDT(b342_20, bb2, 20480);
  RDT(b342_21, bb2, 22528);
  MF(g3_12, b341_20, b341_21, ha3_1);
  WLG(4);
  MF(g3_03, b341_30, b341_31, ha3_0);
  RDT(b342_30, bb3, 20480);
  RDT(b342_31, bb3, 22528);
  MF(g3_13, b341_30, b341_31, ha3_1);
  MF(g3_00, b342_00, b342_01, ha4_0);
  RDT(b343_00, bb0, 24576);
  RDT(b343_01, bb0, 26624);
  MF(g3_10, b342_00, b342_01, ha4_1);
  WLG(4);
  MF(g3_01, b342_10, b342_11, ha4_0);
  RDT(b343_10, bb1, 24576);
  RDT(b343_11, bb1, 26624);
  MF(g3_11, b342_10, b342_11, ha4_1);
  MF(g3_02, b342_20, b342_21, ha4_0);
  RDT(b343_20, bb2, 24576);
  RDT(b343_21, bb2, 26624);
  MF(g3_12, b342_20, b342_21, ha4_1);
  WLG(4);
  MF(g3_03, b342_30, b342_31, ha4_0);
  RDT(b343_30, bb3, 24576);
  RDT(b343_31, bb3, 26624);
  WVM(5);
  CHKF(fl3, 4u, vfl2, sfp, sL + L_FH);
  MF(g3_13, b342_30, b342_31, ha4_1);
  GLD(pi00, v16, spi, sL + L_P + 0);
  MF(g3_00, b343_00, b343_01, ha5_0);
  PRIO(2);
  RDT(b344_00, bb0, 28672);
  RDT(b344_01, bb0, 30720);
  MF(g3_10, b343_00, b343_01, ha5_1);
  WLG(4);
  MF(g3_01, b343_10, b343_11, ha5_0);
  RDT(b344_10, bb1, 28672);
  RDT(b344_11, bb1, 30720);
  MF(g3_11, b343_10, b343_11, ha5_1);
  GLD(pi01, v16, spi, sL + L_P + 1024);
  MF(g3_02, b343_20, b343_21, ha5_0);
  RDT(b344_20, bb2, 28672);
  RDT(b344_21, bb2, 30720);
  MF(g3_12, b343_20, b343_21, ha5_1);
  WLG(4);
  MF(g3_03, b343_30, b343_31, ha5_0);
  RDT(b344_30, bb3, 28672);
  RDT(b344_31, bb3, 30720);
  MF(g3_13, b343_30, b343_31, ha5_1);
  GLD(pi10, v16, spi, sL + L_P + 16384);
  MF(g3_00, b344_00, b344_01, ha6_0);
  PRIO(1);
  RDT(b345_00, bb0, 32768);
  RDT(b345_01, bb0, 34816);
  MF(g3_10, b344_00, b344_01, ha6_1);
  WLG(4);
  MF(g3_01, b344_10, b344_11, ha6_0);
  RDT(b345_10, bb1, 32768);
  RDT(b345_11, bb1, 34816);
  MF(g3_11, b344_10, b344_11, ha6_1);
  MF(g3_02, b344_20, b344_21, ha6_0);
  RDT(b345_20, bb2, 32768);
  RDT(b345_21, bb2, 34816);
  GLD(pi11, v16, spi, sL + L_P + 17408);
  MF(g3_12, b344_20, b344_21, ha6_1);
  PRIO(0);
  WLG(4);
  MF(g3_03, b344_30, b344_31, ha6_0);
  RDT(b345_30, bb3, 32768);
  RDT(b345_31, bb3, 34816);
  MF(g3_13, b344_30, b344_31, ha6_1);
  MF(g3_00, b345_00, b345_01, ha7_0);
  BAR(); STB(44);
  PRIO(3);
  RDT(b346_00, bb0, 36864);
  RDT(b346_01, bb0, 38912);
  MF(g3_10, b345_00, b345_01, ha7_1);
  WLG(4);
  MF(g3_01, b345_10, b345_11, ha7_0);
  RDT(b346_10, bb1, 36864);
  RDT(b346_11, bb1, 38912);
  MF(g3_11, b345_10, b345_11, ha7_1);
  MF(g3_02, b345_20, b345_21, ha7_0);
  RDT(b346_20, bb2, 36864);
  RDT(b346_21, bb2, 38912);
  MF(g3_12, b345_20, b345_21, ha7_1);
  WLG(4);
  MF(g3_03, b345_30, b345_31, ha7_0);
  RDT(b346_30, bb3, 36864);
  RDT(b346_31, bb3, 38912);
  MF(g3_13, b345_30, b345_31, ha7_1);
  MF(g3_00, b346_00, b346_01, ha8_0);
  RDT(b347_00, bb0, 40960);
  RDT(b347_01, bb0, 43008);
  MF(g3_10, b346_00, b346_01, ha8_1);
  WLG(4);
  MF(g3_01, b346_10, b346_11, ha8_0);
  RDT(b347_10, bb1, 40960);
  RDT(b347_11, bb1, 43008);
  MF(g3_11, b346_10, b346_11, ha8_1);
  MF(g3_02, b346_20, b346_21, ha8_0);
  RDT(b347_20, bb2, 40960);
  RDT(b347_21, bb2, 43008);
  MF(g3_12, b346_20, b346_21, ha8_1);
  WLG(4);
  MF(g3_03, b346_30, b346_31, ha8_0);
  RDT(b347_30, bb3, 40960);
  RDT(b347_31, bb3, 43008);
  MF(g3_13, b346_30, b346_31, ha8_1);
  MF(g3_00, b347_00, b347_01, ha9_0);
  PRIO(2);
  RDT(b348_00, bb0, 53248);
  RDT(b348_01, bb0, 55296);
  RDF(g3_b0, tb, 3712);
  RDF(g3_b1, tb, 3776);
  RDF(g3_b2, tb, 3840);
  RDF(g3_b3, tb, 3904);
  MF(g3_10, b347_00, b347_01, ha9_1);
  WLG(8);
  MF(g3_01, b347_10, b347_11, ha9_0);
  RDT(b348_10, bb1, 53248);
  RDT(b348_11, bb1, 55296);
  MF(g3_11, b347_10, b347_11, ha9_1);
  MF(g3_02, b347_20, b347_21, ha9_0);
  RDT(b348_20, bb2, 53248);
  RDT(b348_21, bb2, 55296);
  MF(g3_12, b347_20, b347_21, ha9_1);
  WLG(8);
  MF(g3_03, b347_30, b347_31, ha9_0);
  RDT(b348_30, bb3, 53248);
  RDT(b348_31, bb3, 55296);
  MF(g3_13, b347_30, b347_31, ha9_1);
  MF(g3_00, b348_00, b348_01, ha10_0);
  PRIO(1);
  WVM(8);
  BAR(); STB(45);
  PRIO(3);
  RDT(b349_00, bb0, 57344);
  RDT(b349_01, bb0, 59392);
  MF(g3_10, b348_00, b348_01, ha10_1);
  WLG(4);
  MF(g3_01, b348_10, b348_11, ha10_0);
  RDT(b349_10, bb1, 57344);
  RDT(b349_11, bb1, 59392);
  MF(g3_11, b348_10, b348_11, ha10_1);
  MF(g3_02, b348_20, b348_21, ha10_0);
  RDT(b349_20, bb2, 57344);
  RDT(b349_21, bb2, 59392);
  MF(g3_12, b348_20, b348_21, ha10_1);
  GLD(pi20, v16, spi, sL + L_P + 32768);
  WVM(1);
  PIN(pi00); PIN(pi01); PIN(pi10); PIN(pi11);
  WLG(4);
  MF(g3_03, b348_30, b348_31, ha10_0);
  RDT(b349_30, bb3, 57344);
  RDT(b349_31, bb3, 59392);
  PIN(g3_b0); f32x4 g3_u0 = ((f32x4){lo2f(pi00[0]), hi2f(pi00[0]), lo2f(pi00[1]), hi2f(pi00[1])} + (f32x4){lo2f(pi10[0]), hi2f(pi10[0]), lo2f(pi10[1]), hi2f(pi10[1])}) + g3_b0; PIN(g3_u0);
  MF(g3_13, b348_30, b348_31, ha10_1);
  MF(g3_00, b349_00, b349_01, ha11_0);
  RDT(b350_00, bb0, 45056);
  RDT(b350_01, bb0, 47104);
  MF(g3_10, b349_00, b349_01, ha11_1);
  RDT(b351_00, bb0, 49152);
  RDT(b351_01, bb0, 51200);
  GLD(pi21, v16, spi, sL + L_P + 33792);
  PIN(g3_b1); f32x4 g3_u1 = ((f32x4){lo2f(pi00[2]), hi2f(pi00[2]), lo2f(pi00[3]), hi2f(pi00[3])} + (f32x4){lo2f(pi10[2]), hi2f(pi10[2]), lo2f(pi10[3]), hi2f(pi10[3])}) + g3_b1; PIN(g3_u1);
  WLG(6);
  MF(g3_01, b349_10, b349_11, ha11_0);
  RDT(b350_10, bb1, 45056);
  RDT(b350_11, bb1, 47104);
  MF(g3_11, b349_10, b349_11, ha11_1);
  RDT(b351_10, bb1, 49152);
  RDT(b351_11, bb1, 51200);
  MF(g3_02, b349_20, b349_21, ha11_0);
  RDT(b350_20, bb2, 45056);
  RDT(b350_21, bb2, 47104);
  PIN(g3_b2); f32x4 g3_u2 = ((f32x4){lo2f(pi01[0]), hi2f(pi01[0]), lo2f(pi01[1]), hi2f(pi01[1])} + (f32x4){lo2f(pi11[0]), hi2f(pi11[0]), lo2f(pi11[1]), hi2f(pi11[1])}) + g3_b2; PIN(g3_u2);
  MF(g3_12, b349_20, b349_21, ha11_1);
  RDT(b351_20, bb2, 49152);
  RDT(b351_21, bb2, 51200);
  WLG(8);
  MF(g3_03, b349_30, b349_31, ha11_0);
  RDT(b350_30, bb3, 45056);
  RDT(b350_31, bb3, 47104);
  MF(g3_13, b349_30, b349_31, ha11_1);
  RDT(b351_30, bb3, 49152);
  RDT(b351_31, bb3, 51200);
  PIN(g3_b3); f32x4 g3_u3 = ((f32x4){lo2f(pi01[2]), hi2f(pi01[2]), lo2f(pi01[3]), hi2f(pi01[3])} + (f32x4){lo2f(pi11[2]), hi2f(pi11[2]), lo2f(pi11[3]), hi2f(pi11[3])}) + g3_b3; PIN(g3_u3);
  MF(g3_10, b350_00, b350_01, ha12_1);
  PRIO(2);
  MF(g3_10, b351_00, b351_01, ha13_1);
  WLG(4);
  MF(g3_11, b350_10, b350_11, ha12_1);
  MF(g3_11, b351_10, b351_11, ha13_1);
  MF(g3_12, b350_20, b350_21, ha12_1);
  NOP(3);
  WR(g3_10, xw, 0);
  MF(g3_12, b351_20, b351_21, ha13_1);
  WLG(1);
  MF(g3_13, b350_30, b350_31, ha12_1);
  NOP(2);
  WR(g3_11, xw, 1024);
  MF(g3_13, b351_30, b351_31, ha13_1);
  MF(g3_00, b350_00, b350_01, ha12_0);
  PRIO(1);
  NOP(1);
  WR(g3_12, xw, 2048);
  MF(g3_01, b350_10, b350_11, ha12_0);
  MF(g3_02, b350_20, b350_21, ha12_0);
  NOP(2);
  WR(g3_13, xw, 3072);
  MF(g3_03, b350_30, b350_31, ha12_0);
  MF(g3_00, b351_00, b351_01, ha13_0);
  MF(g3_01, b351_10, b351_11, ha13_0);
  PRIO(0);
  MF(g3_02, b351_20, b351_21, ha13_0);
  MF(g3_03, b351_30, b351_31, ha13_0);
  PRIO(0);
  STP(10);
  WLG(0);
  BAR(); STB(46);
  RDF(g3_r0, xr, 0);
  RDF(g3_r1, xr, 1024);
  RDF(g3_r2, xr, 2048);
  RDF(g3_r3, xr, 3072);
  WVM(0);
  PIN(ry0); PIN(ry1); PIN(pi20); PIN(pi21);
  { unsigned t_ = mx8(pi20, pi21); CHK2(t_, pi20, pi21, v16, spi, sL + L_P + 32768, spi, sL + L_P + 33792, 3); }
  WLG(0);
  PIN(g3_r0); PIN(g3_00);
  PIN(g3_r1); PIN(g3_01);
  PIN(g3_r2); PIN(g3_02);
  PIN(g3_r3); PIN(g3_03);
  const f32x4 g3_t0 = (g3_00 + g3_r0) + ((f32x4){lo2f(pi20[0]), hi2f(pi20[0]), lo2f(pi20[1]), hi2f(pi20[1])} + g3_u0);
  const f32x4 g3_t1 = (g3_01 + g3_r1) + ((f32x4){lo2f(pi20[2]), hi2f(pi20[2]), lo2f(pi20[3]), hi2f(pi20[3])} + g3_u1);
  const f32x4 g3_t2 = (g3_02 + g3_r2) + ((f32x4){lo2f(pi21[0]), hi2f(pi21[0]), lo2f(pi21[1]), hi2f(pi21[1])} + g3_u2);
  const f32x4 g3_t3 = (g3_03 + g3_r3) + ((f32x4){lo2f(pi21[2]), hi2f(pi21[2]), lo2f(pi21[3]), hi2f(pi21[3])} + g3_u3);
  const u32x4 z0 = resid_add(g3_t0, g3_t1, ry0), z1 = resid_add(g3_t2, g3_t3, ry1);
  {
    const rsrc_t rC = __builtin_amdgcn_make_buffer_rsrc((void*)m.out, 0, S_LEN * D_MODEL * 2, 0x00020000);
    IDS(); LANE(l_); const int li = l_ & 15, lg = l_ >> 4;
    const int off = ((mk * 16 + li) * D_MODEL + nq * 256 + kq * 64 + (lg & 1) * 16 + (lg >> 1) * 8) * 2;
    const auto a0 = __builtin_amdgcn_permlane16_swap(z0[0], z0[2], false, false), a1 = __builtin_amdgcn_permlane16_swap(z0[1], z0[3], false, false);
    const auto b0 = __builtin_amdgcn_permlane16_swap(z1[0], z1[2], false, false), b1 = __builtin_amdgcn_permlane16_swap(z1[1], z1[3], false, false);
    __builtin_amdgcn_raw_buffer_store_b128((u32x4){a0[0], a1[0], a0[1], a1[1]}, rC, off, 0, 16);
    __builtin_amdgcn_raw_buffer_store_b128((u32x4){b0[0], b1[0], b0[1], b1[1]}, rC, off + 64, 0, 16);
    STP(11);
  }
  STR(31);
  }
  asm volatile(".Lmega_end:");
}

extern "C" void kernel_launch(void* const* d_in, const int* in_sizes, int n_in, void* d_out, int out_size,
                              void* d_ws, size_t ws_size, hipStream_t stream) {
  MegaArgs m{};
  m.x = (const u16*)d_in[0]; m.wqkv = (const u16*)d_in[1]; m.wo = (const u16*)d_in[2]; m.w1 = (const u16*)d_in[4];
  m.b1 = (const u16*)d_in[5]; m.w2 = (const u16*)d_in[6]; m.b2 = (const u16*)d_in[7];
  m.out = (u16*)d_out; m.ws = (char*)d_ws;
  (void)hipFuncSetAttribute((const void*)mega_kernel, hipFuncAttributeMaxDynamicSharedMemorySize, LDS_BYTES);
  hipLaunchKernelGGL(mega_kernel, dim3(256), dim3(512), LDS_BYTES, stream, m);
}
```

```cpp
#include <hip/hip_runtime.h>
#include <stdint.h>
typedef unsigned short u16;
typedef unsigned long long u64;
typedef __attribute__((ext_vector_type(4))) unsigned u32x4;
constexpr unsigned F_X = 0;
constexpr unsigned F_ATT = 32768;
constexpr unsigned F_X1 = 65536;
constexpr unsigned F_H = 98304;
constexpr unsigned F_P = 212992;
constexpr unsigned F_ST1 = 344064;
constexpr unsigned F_ST2 = 348160;
constexpr unsigned F_G = 352256;
constexpr unsigned F_FA = 352512;
constexpr unsigned F_FX1 = 352768;
constexpr unsigned F_FH = 353024;
constexpr unsigned F_FP = 354048;
constexpr unsigned F_FX = 355072;
constexpr unsigned RS = 524288;
constexpr unsigned PADF = 256;
constexpr unsigned LOWB = 64;
constexpr unsigned L_TAB = 98304;
constexpr unsigned LDS_BYTES = 163840;
constexpr int Q_GL = 2;
constexpr int Q_EX = 6;
constexpr int O_GL = 2;
constexpr int O_EX = 3;
constexpr int F_GL = 2;
constexpr int F_EX = 4;
constexpr int G_GL = 1;
constexpr int G_EX = 4;
constexpr int H_GL = 1;
constexpr int H_EX = 4;
constexpr int J_GL = 2;
constexpr int J_EX = 7;
#define MAPV 1
#define FDLY 0
#define PF 7
#define KR 1

constexpr unsigned DROP = 0x40000000u, WS0 = 1u << 20;
static_assert(WS0 + (PADF + 256 - LOWB) * RS <= (256u << 20), "workspace");
struct Args { const u16 *x, *wqkv, *wo, *w1, *b1, *w2, *b2; u16* out; char* ws;
};
__device__ __forceinline__ u32x4 desc(const void* p, unsigned n) { const u64 a = (u64)p; return (u32x4){(unsigned)a, (unsigned)(a >> 32) & 0xffffu, n, 0x00020000u}; }
__device__ __forceinline__ float lo16(unsigned v) { return __builtin_bit_cast(float, v << 16); }
__device__ __forceinline__ float hi16(unsigned v) { return __builtin_bit_cast(float, v & 0xffff0000u); }

__host__ __device__ inline void decode(int bid, int& role, int& set, int& idx) {
  const int xg = bid & 7, xj = bid >> 3;
  if (xg < 4) {
    if (xj < 16) { role = 0; set = xj & 1; idx = MAPV == 1 ? 8 * xg + (xj >> 1) : MAPV == 2 ? 4 * (xj >> 1) + xg : 4 * (xj >> 1) + ((xg + 2 * set) & 3); }
    else { role = 2; set = xj < 23 ? 1 : xj < 30 ? 2 : 0; idx = 7 * xg + (xj - 16) % 7; }
  } else if (xg == 4) {
    if (xj >= 20) { role = 1; set = xj < 28 ? 2 : 1; idx = xj < 28 ? xj - 20 : xj - 24; }
    else { role = 2; set = 0; idx = 7 * (xj / 5) + 2 + xj % 5; }
  } else if (xj >= 28) { role = 1; set = xg == 7; idx = (xg == 6) * 4 + xj - 28; }
  else { role = 3; set = xg - 5; idx = xj; }
}

__global__ __launch_bounds__(256, 1) void stream_kernel(Args a) {
  extern __shared__ __attribute__((aligned(16))) char smem[];
  const int tid = threadIdx.x, lane = tid & 63, wid = __builtin_amdgcn_readfirstlane(tid >> 6), li = lane & 15, lg = lane >> 4;
  const unsigned lds0 = (unsigned)(size_t)smem;
  const int bid = blockIdx.x;
  int role, set, idx;
  decode(bid, role, set, idx);
  const u64 wsp = (u64)a.ws;
  const u32x4 rS = desc(a.ws + WS0 - LOWB * RS, (PADF + 256) * RS);
  const int pi = (li >> 2) + 4 * (li & 3);
  const int xj = bid >> 3, xo = (bid & 7) - 4;
  if (role == 2 && xo < 0 && (PF & 1)) {
    const int g = (xj - 16) * 4 + wid;
    const char* p = (const char*)a.wqkv + (((g >> 2) * 2 + 1) * 32 + (g & 3) * 8 + (lane >> 5)) * 6144 + (bid & 7) * 512 + (lane & 31) * 16;
    u32x4 t[12];
    unsigned fl = 0;
    if (PF & 4) fl = __hip_atomic_load((const unsigned*)(a.ws + WS0 + (size_t)(PADF - LOWB + g * 4 + (bid & 7)) * RS + F_ST2 + (lane < 56 ? lane : 0) * 128), __ATOMIC_RELAXED, __HIP_MEMORY_SCOPE_AGENT);
#pragma unroll
    for (int i = 0; i < 12; ++i) t[i] = *(const u32x4*)(p + (i & 3) * 12288 + (i >> 2) * 2048);
#pragma unroll
    for (int i = 0; i < 12; ++i) asm volatile("" ::"v"(t[i]));
    asm volatile("" ::"v"(fl));
  }
  if (role == 2 && xo < 0) for (int i = 0; i < FDLY; ++i) __builtin_amdgcn_s_sleep(16);
  for (int pr = role < 2 || xo < 0 ? 128 : xj < 20 ? xj * 4 + xo : xj * 3 + 19 + xo; pr < 128; pr += pr < 80 ? 128 : 24) {
    const __amdgpu_buffer_rsrc_t rb = __builtin_amdgcn_make_buffer_rsrc(a.ws + WS0 - LOWB * RS, 0, (PADF + 256) * RS, 0x00020000);
    const u32x4* src = (const u32x4*)(a.x + ((pr >> 1) * 32 + 2 * pi + (pr & 1)) * 1024 + wid * 256 + lg * 8);
    const u64 ev = __hip_atomic_load((const u64*)a.ws, __ATOMIC_RELAXED, __HIP_MEMORY_SCOPE_AGENT);
    u32x4 v[8];
#pragma unroll
    for (int k = 0; k < 8; ++k) v[k] = src[k * 4];
    u32x4 pf = {};
    if (pr < 104 && (PF & 2)) {
      const int g = xj * 4 + wid, n = xo ? 7 : 20;
      for (int i = 0; i < n; ++i) {
        const int u = g * n + i, r0 = xo ? u : u >> 1, row = r0 < 768 ? (r0 / 192) * 256 + 64 + r0 % 192 : 0;
        asm volatile("global_load_dwordx4 %0, %1, off" : "+v"(pf) : "v"((const char*)a.wo + row * 2048 + (xo ? xo == 2 : u & 1) * 1024 + lane * 16));
      }
    }
    float s = 0.f, q = 0.f;
#pragma unroll
    for (int k = 0; k < 8; ++k) {
      __builtin_amdgcn_raw_buffer_store_b128(v[k], rb, lane * 16 + k * 1024, (PADF + pr) * RS + F_X + wid * 8192, 16);
#pragma unroll
      for (int d = 0; d < 4; ++d) { const float l = lo16(v[k][d]), h = hi16(v[k][d]); s += l + h; q = fmaf(l, l, fmaf(h, h, q)); }
    }
    s += __shfl_xor(s, 16); q += __shfl_xor(q, 16); s += __shfl_xor(s, 32); q += __shfl_xor(q, 32);
    float2* t = (float2*)smem;
    if (lg == 0) t[wid * 16 + li] = make_float2(s, q);
    asm volatile("s_waitcnt vmcnt(0)" : "+v"(pf)::"memory");
    __syncthreads();
    if (tid < 16) {
      const float2 t0 = t[li], t1 = t[16 + li], t2 = t[32 + li], t3 = t[48 + li];
      const float nm = ((t0.x + t1.x) + (t2.x + t3.x)) * -0x1p-10f;
      const float var = fmaxf(fmaf(-nm, nm, fmaf((t0.y + t1.y) + (t2.y + t3.y), 0x1p-10f, 1e-5f)), 1e-5f);
      const u32x4 g = {__builtin_bit_cast(unsigned, __builtin_amdgcn_rsqf(var)), __builtin_bit_cast(unsigned, nm), ((unsigned)ev + 2u) | 1u, (unsigned)(ev >> 32) + 0x9E3779B9u};
      __builtin_amdgcn_raw_buffer_store_b128(g, rb, li * 16, (PADF + pr) * RS + F_G, 16);
    }
    __syncthreads();
  }
  const unsigned sLo = (PADF + set) * RS, sLd = lds0 + wid * 40960, vT = sLd + (8 * lg + (li >> 2)) * 64 + (li & 3) * 16;
  const unsigned trp = (li & 3) * 4608 + (li & 1) * 32 + (li & 2) * 64 + (li >> 2) * 64 + lg * 16;
  if (role == 0) {
    const unsigned sA = sLo - 2 * RS, sHi = sLo + 127 * 2 * RS, sEx = sHi + Q_EX * 2 * RS, sEnt = -wid & 3, tsw = 0, rot = KR * (set + 2 * (idx & 1));
    const unsigned sW0 = wid * 256 * 6144 + idx * 64, vD = (lane >> 2) * 6144 + (lane & 3) * 16;
    const u32x4 rW = desc(a.wqkv, 1024 * 6144);
    auto ok = [&](int r, int prev) { const int k = lg + 4 * r; return prev ? k >= pi + 9 : (k <= pi && k >= pi - 7); };
    auto qmask = [&](int r, int prev) { return ok(r, prev) ? 0u : 0xff800000u; };
    auto pmask = [&](int d) { return (ok(2 * (d & 1), d >> 1) ? 0xffffu : 0u) | (ok(2 * (d & 1) + 1, d >> 1) ? 0xffff0000u : 0u); };
    asm volatile(
"s_nop 4\n"
"v_mov_b32 v186, 0\n"
"global_load_dwordx2 v[0:1], v186, s[48:49] sc1\n"
"v_mov_b32 v188, 0x3f803f80\n"
"v_mov_b32 v189, 0x3f803f80\n"
"v_mov_b32 v190, 0x3f803f80\n"
"v_mov_b32 v191, 0x3f803f80\n"
"s_add_u32 m0, s55, 0\n"
"s_add_u32 s58, s61, 0\n"
"buffer_load_dwordx4 v50, s[44:47], s58 offen lds\n"
"s_add_u32 m0, s55, 1024\n"
"s_add_u32 s58, s61, 98304\n"
"buffer_load_dwordx4 v50, s[44:47], s58 offen lds\n"
"s_add_u32 m0, s55, 2048\n"
"s_add_u32 s58, s62, 0\n"
"buffer_load_dwordx4 v50, s[44:47], s58 offen lds\n"
"s_add_u32 m0, s55, 3072\n"
"s_add_u32 s58, s62, 98304\n"
"buffer_load_dwordx4 v50, s[44:47], s58 offen lds\n"
"s_add_u32 m0, s55, 4096\n"
"s_add_u32 s58, s63, 0\n"
"buffer_load_dwordx4 v50, s[44:47], s58 offen lds\n"
"s_add_u32 m0, s55, 5120\n"
"s_add_u32 s58, s63, 98304\n"
"buffer_load_dwordx4 v50, s[44:47], s58 offen lds\n"
"s_add_u32 m0, s55, 6144\n"
"s_add_u32 s58, s61, 196608\n"
"buffer_load_dwordx4 v50, s[44:47], s58 offen lds\n"
"s_add_u32 m0, s55, 7168\n"
"s_add_u32 s58, s61, 294912\n"
"buffer_load_dwordx4 v50, s[44:47], s58 offen lds\n"
"s_add_u32 m0, s55, 8192\n"
"s_add_u32 s58, s62, 196608\n"
"buffer_load_dwordx4 v50, s[44:47], s58 offen lds\n"
"s_add_u32 m0, s55, 9216\n"
"s_add_u32 s58, s62, 294912\n"
"buffer_load_dwordx4 v50, s[44:47], s58 offen lds\n"
"s_add_u32 m0, s55, 10240\n"
"s_add_u32 s58, s63, 196608\n"
"buffer_load_dwordx4 v50, s[44:47], s58 offen lds\n"
"s_add_u32 m0, s55, 11264\n"
"s_add_u32 s58, s63, 294912\n"
"buffer_load_dwordx4 v50, s[44:47], s58 offen lds\n"
"s_add_u32 m0, s55, 12288\n"
"s_add_u32 s58, s61, 0\n"
"buffer_load_dwordx4 v51, s[44:47], s58 offen lds\n"
"s_add_u32 m0, s55, 13312\n"
"s_add_u32 s58, s61, 98304\n"
"buffer_load_dwordx4 v51, s[44:47], s58 offen lds\n"
"s_add_u32 m0, s55, 14336\n"
"s_add_u32 s58, s62, 0\n"
"buffer_load_dwordx4 v51, s[44:47], s58 offen lds\n"
"s_add_u32 m0, s55, 15360\n"
"s_add_u32 s58, s62, 98304\n"
"buffer_load_dwordx4 v51, s[44:47], s58 offen lds\n"
"s_add_u32 m0, s55, 16384\n"
"s_add_u32 s58, s63, 0\n"
"buffer_load_dwordx4 v51, s[44:47], s58 offen lds\n"
"s_add_u32 m0, s55, 17408\n"
"s_add_u32 s58, s63, 98304\n"
"buffer_load_dwordx4 v51, s[44:47], s58 offen lds\n"
"s_add_u32 m0, s55, 18432\n"
"s_add_u32 s58, s61, 196608\n"
"buffer_load_dwordx4 v51, s[44:47], s58 offen lds\n"
"s_add_u32 m0, s55, 19456\n"
"s_add_u32 s58, s61, 294912\n"
"buffer_load_dwordx4 v51, s[44:47], s58 offen lds\n"
"s_add_u32 m0, s55, 20480\n"
"s_add_u32 s58, s62, 196608\n"
"buffer_load_dwordx4 v51, s[44:47], s58 offen lds\n"
"s_add_u32 m0, s55, 21504\n"
"s_add_u32 s58, s62, 294912\n"
"buffer_load_dwordx4 v51, s[44:47], s58 offen lds\n"
"s_add_u32 m0, s55, 22528\n"
"s_add_u32 s58, s63, 196608\n"
"buffer_load_dwordx4 v51, s[44:47], s58 offen lds\n"
"s_add_u32 m0, s55, 23552\n"
"s_add_u32 s58, s63, 294912\n"
"buffer_load_dwordx4 v51, s[44:47], s58 offen lds\n"
"s_add_u32 m0, s55, 24576\n"
"s_add_u32 s58, s61, 0\n"
"buffer_load_dwordx4 v52, s[44:47], s58 offen lds\n"
"s_add_u32 m0, s55, 25600\n"
"s_add_u32 s58, s61, 98304\n"
"buffer_load_dwordx4 v52, s[44:47], s58 offen lds\n"
"s_add_u32 m0, s55, 26624\n"
"s_add_u32 s58, s62, 0\n"
"buffer_load_dwordx4 v52, s[44:47], s58 offen lds\n"
"s_add_u32 m0, s55, 27648\n"
"s_add_u32 s58, s62, 98304\n"
"buffer_load_dwordx4 v52, s[44:47], s58 offen lds\n"
"s_add_u32 m0, s55, 28672\n"
"s_add_u32 s58, s63, 0\n"
"buffer_load_dwordx4 v52, s[44:47], s58 offen lds\n"
"s_add_u32 m0, s55, 29696\n"
"s_add_u32 s58, s63, 98304\n"
"buffer_load_dwordx4 v52, s[44:47], s58 offen lds\n"
"s_add_u32 m0, s55, 30720\n"
"s_add_u32 s58, s61, 196608\n"
"buffer_load_dwordx4 v52, s[44:47], s58 offen lds\n"
"s_add_u32 m0, s55, 31744\n"
"s_add_u32 s58, s61, 294912\n"
"buffer_load_dwordx4 v52, s[44:47], s58 offen lds\n"
"s_add_u32 m0, s55, 32768\n"
"s_add_u32 s58, s62, 196608\n"
"buffer_load_dwordx4 v52, s[44:47], s58 offen lds\n"
"s_add_u32 m0, s55, 33792\n"
"s_add_u32 s58, s62, 294912\n"
"buffer_load_dwordx4 v52, s[44:47], s58 offen lds\n"
"s_add_u32 m0, s55, 34816\n"
"s_add_u32 s58, s63, 196608\n"
"buffer_load_dwordx4 v52, s[44:47], s58 offen lds\n"
"s_add_u32 m0, s55, 35840\n"
"s_add_u32 s58, s63, 294912\n"
"buffer_load_dwordx4 v52, s[44:47], s58 offen lds\n"
"s_add_u32 m0, s55, 36864\n"
"s_add_u32 s58, s61, 0\n"
"buffer_load_dwordx4 v53, s[44:47], s58 offen lds\n"
"s_add_u32 m0, s55, 37888\n"
"s_add_u32 s58, s61, 98304\n"
"buffer_load_dwordx4 v53, s[44:47], s58 offen lds\n"
"s_add_u32 m0, s55, 38912\n"
"s_add_u32 s58, s62, 0\n"
"buffer_load_dwordx4 v53, s[44:47], s58 offen lds\n"
"s_add_u32 m0, s55, 39936\n"
"s_add_u32 s58, s62, 98304\n"
"buffer_load_dwordx4 v53, s[44:47], s58 offen lds\n"
"s_waitcnt vmcnt(38)\n"
"ds_read_b64_tr_b16 a[0:1], v48 offset:0\n"
"ds_read_b64_tr_b16 a[2:3], v48 offset:256\n"
"ds_read_b64_tr_b16 a[32:33], v49 offset:0\n"
"ds_read_b64_tr_b16 a[34:35], v49 offset:256\n"
"v_add_u32 v0, 2, v0\n"
"v_add_u32 v1, 0x9e3779b9, v1\n"
"v_or_b32 v0, 1, v0\n"
"v_readfirstlane_b32 s49, v1\n"
"v_readfirstlane_b32 s48, v0\n"
"s_waitcnt vmcnt(36)\n"
"ds_read_b64_tr_b16 a[64:65], v48 offset:2048\n"
"ds_read_b64_tr_b16 a[66:67], v48 offset:2304\n"
"ds_read_b64_tr_b16 a[96:97], v49 offset:2048\n"
"ds_read_b64_tr_b16 a[98:99], v49 offset:2304\n"
"s_waitcnt lgkmcnt(4)\n"
"s_add_u32 m0, s55, 0\n"
"s_add_u32 s58, s63, 0\n"
"buffer_load_dwordx4 v53, s[44:47], s58 offen lds\n"
"s_add_u32 m0, s55, 1024\n"
"s_add_u32 s58, s63, 98304\n"
"buffer_load_dwordx4 v53, s[44:47], s58 offen lds\n"
"v_mfma_f32_16x16x32_bf16 v[192:195], v[188:191], a[0:3], 0\n"
"v_mfma_f32_16x16x32_bf16 v[196:199], v[188:191], a[32:35], 0\n"
"s_waitcnt vmcnt(36)\n"
"ds_read_b64_tr_b16 a[128:129], v48 offset:4096\n"
"ds_read_b64_tr_b16 a[130:131], v48 offset:4352\n"
"ds_read_b64_tr_b16 a[160:161], v49 offset:4096\n"
"ds_read_b64_tr_b16 a[162:163], v49 offset:4352\n"
"s_waitcnt lgkmcnt(4)\n"
"s_add_u32 m0, s55, 2048\n"
"s_add_u32 s58, s61, 196608\n"
"buffer_load_dwordx4 v53, s[44:47], s58 offen lds\n"
"s_add_u32 m0, s55, 3072\n"
"s_add_u32 s58, s61, 294912\n"
"buffer_load_dwordx4 v53, s[44:47], s58 offen lds\n"
"v_mfma_f32_16x16x32_bf16 v[200:203], v[188:191], a[64:67], 0\n"
"v_mfma_f32_16x16x32_bf16 v[204:207], v[188:191], a[96:99], 0\n"
"s_waitcnt vmcnt(36)\n"
"ds_read_b64_tr_b16 a[4:5], v48 offset:6144\n"
"ds_read_b64_tr_b16 a[6:7], v48 offset:6400\n"
"ds_read_b64_tr_b16 a[36:37], v49 offset:6144\n"
"ds_read_b64_tr_b16 a[38:39], v49 offset:6400\n"
"s_waitcnt lgkmcnt(4)\n"
"s_add_u32 m0, s55, 4096\n"
"s_add_u32 s58, s62, 196608\n"
"buffer_load_dwordx4 v53, s[44:47], s58 offen lds\n"
"s_add_u32 m0, s55, 5120\n"
"s_add_u32 s58, s62, 294912\n"
"buffer_load_dwordx4 v53, s[44:47], s58 offen lds\n"
"v_mfma_f32_16x16x32_bf16 v[208:211], a[128:131], v[188:191], 0\n"
"v_mfma_f32_16x16x32_bf16 v[212:215], a[160:163], v[188:191], 0\n"
"s_waitcnt vmcnt(36)\n"
"ds_read_b64_tr_b16 a[68:69], v48 offset:8192\n"
"ds_read_b64_tr_b16 a[70:71], v48 offset:8448\n"
"ds_read_b64_tr_b16 a[100:101], v49 offset:8192\n"
"ds_read_b64_tr_b16 a[102:103], v49 offset:8448\n"
"s_waitcnt lgkmcnt(4)\n"
"s_add_u32 m0, s55, 6144\n"
"s_add_u32 s58, s63, 196608\n"
"buffer_load_dwordx4 v53, s[44:47], s58 offen lds\n"
"s_add_u32 m0, s55, 7168\n"
"s_add_u32 s58, s63, 294912\n"
"buffer_load_dwordx4 v53, s[44:47], s58 offen lds\n"
"v_mfma_f32_16x16x32_bf16 v[192:195], v[188:191], a[4:7], v[192:195]\n"
"v_mfma_f32_16x16x32_bf16 v[196:199], v[188:191], a[36:39], v[196:199]\n"
"s_waitcnt vmcnt(36)\n"
"ds_read_b64_tr_b16 a[132:133], v48 offset:10240\n"
"ds_read_b64_tr_b16 a[134:135], v48 offset:10496\n"
"ds_read_b64_tr_b16 a[164:165], v49 offset:10240\n"
"ds_read_b64_tr_b16 a[166:167], v49 offset:10496\n"
"s_waitcnt lgkmcnt(4)\n"
"v_mfma_f32_16x16x32_bf16 v[200:203], v[188:191], a[68:71], v[200:203]\n"
"v_mfma_f32_16x16x32_bf16 v[204:207], v[188:191], a[100:103], v[204:207]\n"
"s_waitcnt vmcnt(34)\n"
"ds_read_b64_tr_b16 a[8:9], v48 offset:12288\n"
"ds_read_b64_tr_b16 a[10:11], v48 offset:12544\n"
"ds_read_b64_tr_b16 a[40:41], v49 offset:12288\n"
"ds_read_b64_tr_b16 a[42:43], v49 offset:12544\n"
"s_waitcnt lgkmcnt(4)\n"
"v_mfma_f32_16x16x32_bf16 v[208:211], a[132:135], v[188:191], v[208:211]\n"
"v_mfma_f32_16x16x32_bf16 v[212:215], a[164:167], v[188:191], v[212:215]\n"
"s_waitcnt vmcnt(32)\n"
"ds_read_b64_tr_b16 a[72:73], v48 offset:14336\n"
"ds_read_b64_tr_b16 a[74:75], v48 offset:14592\n"
"ds_read_b64_tr_b16 a[104:105], v49 offset:14336\n"
"ds_read_b64_tr_b16 a[106:107], v49 offset:14592\n"
"s_waitcnt lgkmcnt(4)\n"
"v_mfma_f32_16x16x32_bf16 v[192:195], v[188:191], a[8:11], v[192:195]\n"
"v_mfma_f32_16x16x32_bf16 v[196:199], v[188:191], a[40:43], v[196:199]\n"
"s_waitcnt vmcnt(30)\n"
"ds_read_b64_tr_b16 a[136:137], v48 offset:16384\n"
"ds_read_b64_tr_b16 a[138:139], v48 offset:16640\n"
"ds_read_b64_tr_b16 a[168:169], v49 offset:16384\n"
"ds_read_b64_tr_b16 a[170:171], v49 offset:16640\n"
"s_waitcnt lgkmcnt(4)\n"
"v_mfma_f32_16x16x32_bf16 v[200:203], v[188:191], a[72:75], v[200:203]\n"
"v_mfma_f32_16x16x32_bf16 v[204:207], v[188:191], a[104:107], v[204:207]\n"
"s_waitcnt vmcnt(28)\n"
"ds_read_b64_tr_b16 a[12:13], v48 offset:18432\n"
"ds_read_b64_tr_b16 a[14:15], v48 offset:18688\n"
"ds_read_b64_tr_b16 a[44:45], v49 offset:18432\n"
"ds_read_b64_tr_b16 a[46:47], v49 offset:18688\n"
"s_waitcnt lgkmcnt(4)\n"
"v_mfma_f32_16x16x32_bf16 v[208:211], a[136:139], v[188:191], v[208:211]\n"
"v_mfma_f32_16x16x32_bf16 v[212:215], a[168:171], v[188:191], v[212:215]\n"
"s_waitcnt vmcnt(26)\n"
"ds_read_b64_tr_b16 a[76:77], v48 offset:20480\n"
"ds_read_b64_tr_b16 a[78:79], v48 offset:20736\n"
"ds_read_b64_tr_b16 a[108:109], v49 offset:20480\n"
"ds_read_b64_tr_b16 a[110:111], v49 offset:20736\n"
"s_waitcnt lgkmcnt(4)\n"
"v_mfma_f32_16x16x32_bf16 v[192:195], v[188:191], a[12:15], v[192:195]\n"
"v_mfma_f32_16x16x32_bf16 v[196:199], v[188:191], a[44:47], v[196:199]\n"
"s_waitcnt vmcnt(24)\n"
"ds_read_b64_tr_b16 a[140:141], v48 offset:22528\n"
"ds_read_b64_tr_b16 a[142:143], v48 offset:22784\n"
"ds_read_b64_tr_b16 a[172:173], v49 offset:22528\n"
"ds_read_b64_tr_b16 a[174:175], v49 offset:22784\n"
"s_waitcnt lgkmcnt(4)\n"
"v_mfma_f32_16x16x32_bf16 v[200:203], v[188:191], a[76:79], v[200:203]\n"
"v_mfma_f32_16x16x32_bf16 v[204:207], v[188:191], a[108:111], v[204:207]\n"
"s_waitcnt vmcnt(22)\n"
"ds_read_b64_tr_b16 a[16:17], v48 offset:24576\n"
"ds_read_b64_tr_b16 a[18:19], v48 offset:24832\n"
"ds_read_b64_tr_b16 a[48:49], v49 offset:24576\n"
"ds_read_b64_tr_b16 a[50:51], v49 offset:24832\n"
"s_waitcnt lgkmcnt(4)\n"
"v_mfma_f32_16x16x32_bf16 v[208:211], a[140:143], v[188:191], v[208:211]\n"
"v_mfma_f32_16x16x32_bf16 v[212:215], a[172:175], v[188:191], v[212:215]\n"
"s_waitcnt vmcnt(20)\n"
"ds_read_b64_tr_b16 a[80:81], v48 offset:26624\n"
"ds_read_b64_tr_b16 a[82:83], v48 offset:26880\n"
"ds_read_b64_tr_b16 a[112:113], v49 offset:26624\n"
"ds_read_b64_tr_b16 a[114:115], v49 offset:26880\n"
"s_waitcnt lgkmcnt(4)\n"
"v_mfma_f32_16x16x32_bf16 v[192:195], v[188:191], a[16:19], v[192:195]\n"
"v_mfma_f32_16x16x32_bf16 v[196:199], v[188:191], a[48:51], v[196:199]\n"
"s_waitcnt vmcnt(18)\n"
"ds_read_b64_tr_b16 a[144:145], v48 offset:28672\n"
"ds_read_b64_tr_b16 a[146:147], v48 offset:28928\n"
"ds_read_b64_tr_b16 a[176:177], v49 offset:28672\n"
"ds_read_b64_tr_b16 a[178:179], v49 offset:28928\n"
"s_waitcnt lgkmcnt(4)\n"
"v_mfma_f32_16x16x32_bf16 v[200:203], v[188:191], a[80:83], v[200:203]\n"
"v_mfma_f32_16x16x32_bf16 v[204:207], v[188:191], a[112:115], v[204:207]\n"
"s_waitcnt vmcnt(16)\n"
"ds_read_b64_tr_b16 a[20:21], v48 offset:30720\n"
"ds_read_b64_tr_b16 a[22:23], v48 offset:30976\n"
"ds_read_b64_tr_b16 a[52:53], v49 offset:30720\n"
"ds_read_b64_tr_b16 a[54:55], v49 offset:30976\n"
"s_waitcnt lgkmcnt(4)\n"
"v_mfma_f32_16x16x32_bf16 v[208:211], a[144:147], v[188:191], v[208:211]\n"
"v_mfma_f32_16x16x32_bf16 v[212:215], a[176:179], v[188:191], v[212:215]\n"
"s_waitcnt vmcnt(14)\n"
"ds_read_b64_tr_b16 a[84:85], v48 offset:32768\n"
"ds_read_b64_tr_b16 a[86:87], v48 offset:33024\n"
"ds_read_b64_tr_b16 a[116:117], v49 offset:32768\n"
"ds_read_b64_tr_b16 a[118:119], v49 offset:33024\n"
"s_waitcnt lgkmcnt(4)\n"
"v_mfma_f32_16x16x32_bf16 v[192:195], v[188:191], a[20:23], v[192:195]\n"
"v_mfma_f32_16x16x32_bf16 v[196:199], v[188:191], a[52:55], v[196:199]\n"
"s_waitcnt vmcnt(12)\n"
"ds_read_b64_tr_b16 a[148:149], v48 offset:34816\n"
"ds_read_b64_tr_b16 a[150:151], v48 offset:35072\n"
"ds_read_b64_tr_b16 a[180:181], v49 offset:34816\n"
"ds_read_b64_tr_b16 a[182:183], v49 offset:35072\n"
"s_waitcnt lgkmcnt(4)\n"
"v_mfma_f32_16x16x32_bf16 v[200:203], v[188:191], a[84:87], v[200:203]\n"
"v_mfma_f32_16x16x32_bf16 v[204:207], v[188:191], a[116:119], v[204:207]\n"
"s_waitcnt vmcnt(10)\n"
"ds_read_b64_tr_b16 a[24:25], v48 offset:36864\n"
"ds_read_b64_tr_b16 a[26:27], v48 offset:37120\n"
"ds_read_b64_tr_b16 a[56:57], v49 offset:36864\n"
"ds_read_b64_tr_b16 a[58:59], v49 offset:37120\n"
"s_waitcnt lgkmcnt(4)\n"
"v_mfma_f32_16x16x32_bf16 v[208:211], a[148:151], v[188:191], v[208:211]\n"
"v_mfma_f32_16x16x32_bf16 v[212:215], a[180:183], v[188:191], v[212:215]\n"
"s_waitcnt vmcnt(8)\n"
"ds_read_b64_tr_b16 a[88:89], v48 offset:38912\n"
"ds_read_b64_tr_b16 a[90:91], v48 offset:39168\n"
"ds_read_b64_tr_b16 a[120:121], v49 offset:38912\n"
"ds_read_b64_tr_b16 a[122:123], v49 offset:39168\n"
"s_waitcnt lgkmcnt(4)\n"
"v_mfma_f32_16x16x32_bf16 v[192:195], v[188:191], a[24:27], v[192:195]\n"
"v_mfma_f32_16x16x32_bf16 v[196:199], v[188:191], a[56:59], v[196:199]\n"
"s_waitcnt vmcnt(6)\n"
"ds_read_b64_tr_b16 a[152:153], v48 offset:0\n"
"ds_read_b64_tr_b16 a[154:155], v48 offset:256\n"
"ds_read_b64_tr_b16 a[184:185], v49 offset:0\n"
"ds_read_b64_tr_b16 a[186:187], v49 offset:256\n"
"s_waitcnt lgkmcnt(4)\n"
"v_mfma_f32_16x16x32_bf16 v[200:203], v[188:191], a[88:91], v[200:203]\n"
"v_mfma_f32_16x16x32_bf16 v[204:207], v[188:191], a[120:123], v[204:207]\n"
"s_waitcnt vmcnt(4)\n"
"ds_read_b64_tr_b16 a[28:29], v48 offset:2048\n"
"ds_read_b64_tr_b16 a[30:31], v48 offset:2304\n"
"ds_read_b64_tr_b16 a[60:61], v49 offset:2048\n"
"ds_read_b64_tr_b16 a[62:63], v49 offset:2304\n"
"s_waitcnt lgkmcnt(4)\n"
"v_mfma_f32_16x16x32_bf16 v[208:211], a[152:155], v[188:191], v[208:211]\n"
"v_mfma_f32_16x16x32_bf16 v[212:215], a[184:187], v[188:191], v[212:215]\n"
"s_waitcnt vmcnt(2)\n"
"ds_read_b64_tr_b16 a[92:93], v48 offset:4096\n"
"ds_read_b64_tr_b16 a[94:95], v48 offset:4352\n"
"ds_read_b64_tr_b16 a[124:125], v49 offset:4096\n"
"ds_read_b64_tr_b16 a[126:127], v49 offset:4352\n"
"s_waitcnt lgkmcnt(4)\n"
"v_mfma_f32_16x16x32_bf16 v[192:195], v[188:191], a[28:31], v[192:195]\n"
"v_mfma_f32_16x16x32_bf16 v[196:199], v[188:191], a[60:63], v[196:199]\n"
"s_waitcnt vmcnt(0)\n"
"ds_read_b64_tr_b16 a[156:157], v48 offset:6144\n"
"ds_read_b64_tr_b16 a[158:159], v48 offset:6400\n"
"ds_read_b64_tr_b16 a[188:189], v49 offset:6144\n"
"ds_read_b64_tr_b16 a[190:191], v49 offset:6400\n"
"s_waitcnt lgkmcnt(4)\n"
"v_mfma_f32_16x16x32_bf16 v[200:203], v[188:191], a[92:95], v[200:203]\n"
"v_mfma_f32_16x16x32_bf16 v[204:207], v[188:191], a[124:127], v[204:207]\n"
"s_waitcnt lgkmcnt(0)\n"
"v_mfma_f32_16x16x32_bf16 v[208:211], a[156:159], v[188:191], v[208:211]\n"
"v_mfma_f32_16x16x32_bf16 v[212:215], a[188:191], v[188:191], v[212:215]\n"
"s_mov_b32 m0, s57\n"
"s_barrier\n"
"ds_write_addtid_b32 v192 offset:0\n"
"ds_write_addtid_b32 v193 offset:4640\n"
"ds_write_addtid_b32 v194 offset:9344\n"
"ds_write_addtid_b32 v195 offset:13984\n"
"ds_write_addtid_b32 v196 offset:256\n"
"ds_write_addtid_b32 v197 offset:4896\n"
"ds_write_addtid_b32 v198 offset:9600\n"
"ds_write_addtid_b32 v199 offset:14240\n"
"ds_write_addtid_b32 v200 offset:512\n"
"ds_write_addtid_b32 v201 offset:5152\n"
"ds_write_addtid_b32 v202 offset:9856\n"
"ds_write_addtid_b32 v203 offset:14496\n"
"ds_write_addtid_b32 v204 offset:768\n"
"ds_write_addtid_b32 v205 offset:5408\n"
"ds_write_addtid_b32 v206 offset:10112\n"
"ds_write_addtid_b32 v207 offset:14752\n"
"ds_write_addtid_b32 v208 offset:1024\n"
"ds_write_addtid_b32 v209 offset:5664\n"
"ds_write_addtid_b32 v210 offset:10368\n"
"ds_write_addtid_b32 v211 offset:15008\n"
"ds_write_addtid_b32 v212 offset:1280\n"
"ds_write_addtid_b32 v213 offset:5920\n"
"ds_write_addtid_b32 v214 offset:10624\n"
"ds_write_addtid_b32 v215 offset:15264\n"
"s_waitcnt lgkmcnt(0)\n"
"s_barrier\n"
"ds_read_b128 v[96:99], v2 offset:0\n"
"ds_read_b128 v[100:103], v3 offset:0\n"
"ds_read_b128 v[104:107], v4 offset:0\n"
"ds_read_b128 v[108:111], v54 offset:0\n"
"ds_read_b128 v[112:115], v2 offset:256\n"
"ds_read_b128 v[116:119], v3 offset:256\n"
"ds_read_b128 v[120:123], v4 offset:256\n"
"ds_read_b128 v[124:127], v54 offset:256\n"
"ds_read_b128 v[128:131], v2 offset:512\n"
"ds_read_b128 v[132:135], v3 offset:512\n"
"ds_read_b128 v[136:139], v4 offset:512\n"
"ds_read_b128 v[140:143], v54 offset:512\n"
"ds_read_b128 v[144:147], v2 offset:768\n"
"ds_read_b128 v[148:151], v3 offset:768\n"
"ds_read_b128 v[152:155], v4 offset:768\n"
"ds_read_b128 v[156:159], v54 offset:768\n"
"ds_read_b128 v[160:163], v2 offset:1024\n"
"ds_read_b128 v[164:167], v3 offset:1024\n"
"ds_read_b128 v[168:171], v4 offset:1024\n"
"ds_read_b128 v[172:175], v54 offset:1024\n"
"ds_read_b128 v[176:179], v2 offset:1280\n"
"ds_read_b128 v[180:183], v3 offset:1280\n"
"ds_read_b128 v[184:187], v4 offset:1280\n"
"ds_read_b128 v[188:191], v54 offset:1280\n"
"s_waitcnt lgkmcnt(0)\n"
"s_barrier\n"
"v_add_f32 v96, v96, v100\n"
"v_add_f32 v104, v104, v108\n"
"v_add_f32 v28, v96, v104\n"
"v_add_f32 v97, v97, v101\n"
"v_add_f32 v105, v105, v109\n"
"v_add_f32 v29, v97, v105\n"
"v_add_f32 v98, v98, v102\n"
"v_add_f32 v106, v106, v110\n"
"v_add_f32 v30, v98, v106\n"
"v_add_f32 v99, v99, v103\n"
"v_add_f32 v107, v107, v111\n"
"v_add_f32 v31, v99, v107\n"
"v_add_f32 v112, v112, v116\n"
"v_add_f32 v120, v120, v124\n"
"v_add_f32 v32, v112, v120\n"
"v_add_f32 v113, v113, v117\n"
"v_add_f32 v121, v121, v125\n"
"v_add_f32 v33, v113, v121\n"
"v_add_f32 v114, v114, v118\n"
"v_add_f32 v122, v122, v126\n"
"v_add_f32 v34, v114, v122\n"
"v_add_f32 v115, v115, v119\n"
"v_add_f32 v123, v123, v127\n"
"v_add_f32 v35, v115, v123\n"
"v_add_f32 v128, v128, v132\n"
"v_add_f32 v136, v136, v140\n"
"v_add_f32 v36, v128, v136\n"
"v_add_f32 v129, v129, v133\n"
"v_add_f32 v137, v137, v141\n"
"v_add_f32 v37, v129, v137\n"
"v_add_f32 v130, v130, v134\n"
"v_add_f32 v138, v138, v142\n"
"v_add_f32 v38, v130, v138\n"
"v_add_f32 v131, v131, v135\n"
"v_add_f32 v139, v139, v143\n"
"v_add_f32 v39, v131, v139\n"
"v_add_f32 v144, v144, v148\n"
"v_add_f32 v152, v152, v156\n"
"v_add_f32 v40, v144, v152\n"
"v_add_f32 v145, v145, v149\n"
"v_add_f32 v153, v153, v157\n"
"v_add_f32 v41, v145, v153\n"
"v_add_f32 v146, v146, v150\n"
"v_add_f32 v154, v154, v158\n"
"v_add_f32 v42, v146, v154\n"
"v_add_f32 v147, v147, v151\n"
"v_add_f32 v155, v155, v159\n"
"v_add_f32 v43, v147, v155\n"
"v_add_f32 v160, v160, v164\n"
"v_add_f32 v168, v168, v172\n"
"v_add_f32 v44, v160, v168\n"
"v_add_f32 v176, v176, v180\n"
"v_add_f32 v184, v184, v188\n"
"v_add_f32 v45, v176, v184\n"
"v_mov_b32 v128, 0\n"
"v_mov_b32 v129, 0\n"
"v_mov_b32 v130, 0\n"
"v_mov_b32 v131, 0\n"
"v_mov_b32 v136, 0\n"
"v_mov_b32 v137, 0\n"
"v_mov_b32 v138, 0\n"
"v_mov_b32 v139, 0\n"
"v_mov_b32 v140, 0\n"
"v_mov_b32 v141, 0\n"
"v_mov_b32 v142, 0\n"
"v_mov_b32 v143, 0\n"
"v_mov_b32 v168, 0\n"
"v_mov_b32 v169, 0\n"
"v_mov_b32 v170, 0\n"
"v_mov_b32 v171, 0\n"
"s_mov_b32 s94, 0x80000000\n"
"s_mov_b32 s95, 0x80000000\n"
"s_cmp_eq_u32 s54, 0\n"
"s_cbranch_scc1 .LQ_1s\n"
"s_cmp_eq_u32 s54, 1\n"
"s_cbranch_scc1 .LQ_2s\n"
"s_cmp_eq_u32 s54, 2\n"
"s_cbranch_scc1 .LQ_3s\n"
"s_cmp_eq_u32 s54, 3\n"
"s_cbranch_scc1 .LQ_4s\n"
".p2align 6\n"
".LQ_top:\n"
"v_mfma_f32_16x16x32_bf16 v[112:115], a[128:131], v[48:51], 0\n"
"buffer_load_dwordx4 a[192:195], v5, s[40:43], s56 offen offset:0 sc1\n"
"v_mfma_f32_16x16x32_bf16 v[112:115], a[132:135], v[52:55], v[112:115]\n"
"ds_write_addtid_b32 v152 offset:4352\n"
"v_mfma_f32_16x16x32_bf16 v[112:115], a[136:139], v[56:59], v[112:115]\n"
"ds_write_addtid_b32 v153 offset:8992\n"
"v_mfma_f32_16x16x32_bf16 v[112:115], a[140:143], v[60:63], v[112:115]\n"
"ds_write_addtid_b32 v154 offset:13696\n"
"v_mfma_f32_16x16x32_bf16 v[112:115], a[144:147], v[64:67], v[112:115]\n"
"ds_write_addtid_b32 v155 offset:18336\n"
"buffer_load_dwordx4 a[196:199], v5, s[40:43], s56 offen offset:1024 sc1\n"
"v_mfma_f32_16x16x32_bf16 v[112:115], a[148:151], v[68:71], v[112:115]\n"
"s_waitcnt vmcnt(11)\n"
"v_mul_f32 v119, v129, v39\n"
"v_mov_b32 v255, v128\n"
"v_mfma_f32_16x16x32_bf16 v[112:115], a[152:155], v[72:75], v[112:115]\n"
"v_mul_f32 v117, v129, v37\n"
"v_mul_f32 v118, v129, v38\n"
"v_mfma_f32_16x16x32_bf16 v[112:115], a[156:159], v[76:79], v[112:115]\n"
"v_mul_f32 v123, v129, v43\n"
"v_mul_f32 v116, v129, v36\n"
"v_mfma_f32_16x16x32_bf16 v[124:127], a[160:163], v[48:51], 0\n"
"buffer_load_dwordx4 a[200:203], v6, s[40:43], s56 offen offset:0 sc1\n"
"v_mul_f32 v122, v129, v42\n"
"v_mfma_f32_16x16x32_bf16 v[124:127], a[164:167], v[52:55], v[124:127]\n"
"ds_write_addtid_b32 v112 offset:0\n"
"v_mul_f32 v121, v129, v41\n"
"v_mfma_f32_16x16x32_bf16 v[124:127], a[168:171], v[56:59], v[124:127]\n"
"s_waitcnt lgkmcnt(1)\n"
"ds_write_addtid_b32 v113 offset:4640\n"
"v_mul_f32 v120, v129, v40\n"
"v_mfma_f32_16x16x32_bf16 v[124:127], a[172:175], v[60:63], v[124:127]\n"
"s_barrier\n"
"v_mfma_f32_16x16x32_bf16 v[124:127], a[176:179], v[64:67], v[124:127]\n"
"ds_write_addtid_b32 v114 offset:9344\n"
"buffer_load_dwordx4 a[204:207], v6, s[40:43], s56 offen offset:1024 sc1\n"
"v_mfma_f32_16x16x32_bf16 v[124:127], a[180:183], v[68:71], v[124:127]\n"
"ds_write_addtid_b32 v115 offset:13984\n"
"ds_read2st64_b32 v[128:129], v12 offset0:0 offset1:1\n"
"v_mfma_f32_16x16x32_bf16 v[124:127], a[184:187], v[72:75], v[124:127]\n"
"ds_read2st64_b32 v[130:131], v12 offset0:2 offset1:3\n"
"ds_read2st64_b32 v[144:145], v11 offset0:4 offset1:5\n"
"v_mfma_f32_16x16x32_bf16 v[124:127], a[188:191], v[76:79], v[124:127]\n"
"ds_read2st64_b32 v[148:149], v11 offset0:6 offset1:7\n"
"v_mfma_f32_16x16x32_bf16 v[112:115], v[48:51], a[0:3], 0\n"
"s_add_u32 s94, s50, 0xffc00000\n"
"s_and_b32 s95, s94, 0xfbffffff\n"
"buffer_load_dwordx4 a[208:211], v7, s[40:43], s56 offen offset:0 sc1\n"
"v_mfma_f32_16x16x32_bf16 v[112:115], v[52:55], a[4:7], v[112:115]\n"
"ds_write_addtid_b32 v124 offset:256\n"
"v_mfma_f32_16x16x32_bf16 v[112:115], v[56:59], a[8:11], v[112:115]\n"
"ds_write_addtid_b32 v125 offset:4896\n"
"v_mfma_f32_16x16x32_bf16 v[112:115], v[60:63], a[12:15], v[112:115]\n"
"s_waitcnt lgkmcnt(4)\n"
"ds_write_addtid_b32 v126 offset:9600\n"
"v_mfma_f32_16x16x32_bf16 v[132:135], v[128:131], v[156:159], v[16:19]\n"
"s_cmp_eq_u32 s94, s60\n"
"s_cbranch_scc1 .LQ_fs\n"
".LQ_fr:\n"
"v_mfma_f32_16x16x32_bf16 v[112:115], v[64:67], a[16:19], v[112:115]\n"
"ds_write_addtid_b32 v127 offset:14240\n"
"buffer_load_dwordx4 a[212:215], v7, s[40:43], s56 offen offset:1024 sc1\n"
"v_mfma_f32_16x16x32_bf16 v[112:115], v[68:71], a[20:23], v[112:115]\n"
"v_max_f32 v254, v160, v162\n"
"v_max_f32 v253, v161, v163\n"
"v_mfma_f32_16x16x32_bf16 v[112:115], v[72:75], a[24:27], v[112:115]\n"
"v_max3_f32 v254, v132, v134, v254\n"
"v_max3_f32 v253, v133, v135, v253\n"
"v_mfma_f32_16x16x32_bf16 v[112:115], v[76:79], a[28:31], v[112:115]\n"
"v_max_f32 v252, v254, v253\n"
"v_mov_b32 v251, v252\n"
"v_mfma_f32_16x16x32_bf16 v[124:127], v[48:51], a[32:35], 0\n"
"buffer_load_dwordx4 a[216:219], v8, s[40:43], s56 offen offset:0 sc1\n"
"v_mfma_f32_16x16x32_bf16 v[124:127], v[52:55], a[36:39], v[124:127]\n"
"ds_write_addtid_b32 v112 offset:512\n"
"v_mfma_f32_16x16x32_bf16 v[124:127], v[56:59], a[40:43], v[124:127]\n"
"ds_write_addtid_b32 v113 offset:5152\n"
"v_mfma_f32_16x16x32_bf16 v[124:127], v[60:63], a[44:47], v[124:127]\n"
"ds_write_addtid_b32 v114 offset:9856\n"
"buffer_load_dwordx4 v[128:131], v9, s[40:43], s50 offen sc1\n"
"v_mfma_f32_16x16x32_bf16 v[124:127], v[64:67], a[48:51], v[124:127]\n"
"ds_write_addtid_b32 v115 offset:14496\n"
"buffer_load_dwordx4 a[220:223], v8, s[40:43], s56 offen offset:1024 sc1\n"
"v_mfma_f32_16x16x32_bf16 v[124:127], v[68:71], a[52:55], v[124:127]\n"
"v_permlane32_swap_b32 v251, v252\n"
"v_mfma_f32_16x16x32_bf16 v[124:127], v[72:75], a[56:59], v[124:127]\n"
"v_max_f32 v252, v251, v252\n"
"v_mov_b32 v251, v252\n"
"v_mfma_f32_16x16x32_bf16 v[124:127], v[76:79], a[60:63], v[124:127]\n"
"v_permlane16_swap_b32 v251, v252\n"
"v_mfma_f32_16x16x32_bf16 v[116:119], a[64:67], v[48:51], v[116:119]\n"
"v_max_f32 v252, v251, v252\n"
"v_mul_f32 v252, v252, v250\n"
"v_mfma_f32_16x16x32_bf16 v[116:119], a[68:71], v[52:55], v[116:119]\n"
"ds_write_addtid_b32 v124 offset:768\n"
"v_fma_f32 v254, v254, v250, -v252\n"
"v_mfma_f32_16x16x32_bf16 v[116:119], a[72:75], v[56:59], v[116:119]\n"
"ds_write_addtid_b32 v125 offset:5408\n"
"v_fma_f32 v253, v253, v250, -v252\n"
"v_mfma_f32_16x16x32_bf16 v[116:119], a[76:79], v[60:63], v[116:119]\n"
"ds_write_addtid_b32 v126 offset:10112\n"
"v_mfma_f32_16x16x32_bf16 v[116:119], a[80:83], v[64:67], v[116:119]\n"
"ds_write_addtid_b32 v127 offset:14752\n"
"v_mfma_f32_16x16x32_bf16 v[116:119], a[84:87], v[68:71], v[116:119]\n"
"v_exp_f32 v254, v254\n"
"v_mfma_f32_16x16x32_bf16 v[116:119], a[88:91], v[72:75], v[116:119]\n"
"v_exp_f32 v253, v253\n"
"v_mfma_f32_16x16x32_bf16 v[116:119], a[92:95], v[76:79], v[116:119]\n"
"v_add_f32 v252, v254, v253\n"
"v_mov_b32 v251, v252\n"
"v_mfma_f32_16x16x32_bf16 v[120:123], a[96:99], v[48:51], v[120:123]\n"
"v_permlane32_swap_b32 v251, v252\n"
"v_mfma_f32_16x16x32_bf16 v[120:123], a[100:103], v[52:55], v[120:123]\n"
"v_add_f32 v252, v251, v252\n"
"v_mov_b32 v251, v252\n"
"v_mfma_f32_16x16x32_bf16 v[120:123], a[104:107], v[56:59], v[120:123]\n"
"v_permlane16_swap_b32 v251, v252\n"
"v_mfma_f32_16x16x32_bf16 v[120:123], a[108:111], v[60:63], v[120:123]\n"
"s_waitcnt vmcnt(10)\n"
"v_cmp_ne_u64 vcc, s[48:49], v[170:171]\n"
"v_add_f32 v252, v251, v252\n"
"v_mfma_f32_16x16x32_bf16 v[120:123], a[112:115], v[64:67], v[120:123]\n"
"v_rcp_f32 v252, v252\n"
"v_mfma_f32_16x16x32_bf16 v[120:123], a[116:119], v[68:71], v[120:123]\n"
"s_cbranch_vccnz .LQ_4s\n"
".LQ_4r:\n"
"v_mul_f32 v254, v254, v252\n"
"v_mfma_f32_16x16x32_bf16 v[120:123], a[120:123], v[72:75], v[120:123]\n"
"s_add_u32 s50, s50, 0x100000\n"
"s_and_b32 s56, s50, 0xfbffffff\n"
"v_mul_f32 v253, v253, v252\n"
"v_mfma_f32_16x16x32_bf16 v[120:123], a[124:127], v[76:79], v[120:123]\n"
"v_cvt_pk_bf16_f32 v251, v254, v253\n"
"v_mfma_f32_16x16x32_bf16 v[48:51], v[80:83], a[64:67], 0\n"
"s_waitcnt lgkmcnt(12)\n"
"buffer_load_dwordx4 a[224:227], v5, s[40:43], s56 offen offset:0 sc1\n"
"v_and_b32 v52, v251, v24\n"
"v_mfma_f32_16x16x32_bf16 v[48:51], v[84:87], a[68:71], v[48:51]\n"
"v_and_b32 v53, v251, v25\n"
"v_and_b32 v54, v251, v26\n"
"v_mfma_f32_16x16x32_bf16 v[48:51], v[88:91], a[72:75], v[48:51]\n"
"v_and_b32 v55, v251, v27\n"
"v_mfma_f32_16x16x32_bf16 v[48:51], v[92:95], a[76:79], v[48:51]\n"
"v_mfma_f32_16x16x32_bf16 v[56:59], v[144:147], v[52:55], 0\n"
"v_mov_b32 v254, v136\n"
"v_mfma_f32_16x16x32_bf16 v[60:63], v[148:151], v[52:55], 0\n"
"v_mul_f32_dpp v114, v137, v44 row_newbcast:2 row_mask:0xf bank_mask:0xf\n"
"v_mul_f32_dpp v115, v137, v44 row_newbcast:3 row_mask:0xf bank_mask:0xf\n"
"v_mfma_f32_16x16x32_bf16 v[48:51], v[96:99], a[80:83], v[48:51]\n"
"buffer_load_dwordx4 a[228:231], v5, s[40:43], s56 offen offset:1024 sc1\n"
"v_cvt_pk_bf16_f32 v52, v56, v57\n"
"v_mfma_f32_16x16x32_bf16 v[48:51], v[100:103], a[84:87], v[48:51]\n"
"s_waitcnt vmcnt(11)\n"
"v_cvt_pk_bf16_f32 v53, v58, v59\n"
"v_cvt_pk_bf16_f32 v54, v60, v61\n"
"v_mfma_f32_16x16x32_bf16 v[48:51], v[104:107], a[88:91], v[48:51]\n"
"v_cvt_pk_bf16_f32 v55, v62, v63\n"
"v_mul_f32_dpp v113, v137, v44 row_newbcast:1 row_mask:0xf bank_mask:0xf\n"
"v_mfma_f32_16x16x32_bf16 v[48:51], v[108:111], a[92:95], v[48:51]\n"
"buffer_store_dwordx4 v[52:55], v13, s[40:43], s95 offen sc1\n"
"v_mul_f32_dpp v125, v137, v45 row_newbcast:1 row_mask:0xf bank_mask:0xf\n"
"v_mul_f32_dpp v126, v137, v45 row_newbcast:2 row_mask:0xf bank_mask:0xf\n"
"v_mul_f32_dpp v127, v137, v45 row_newbcast:3 row_mask:0xf bank_mask:0xf\n"
"v_mul_f32_dpp v112, v137, v44 row_newbcast:0 row_mask:0xf bank_mask:0xf\n"
"v_mfma_f32_16x16x32_bf16 v[52:55], v[80:83], a[96:99], 0\n"
"s_waitcnt lgkmcnt(0)\n"
"buffer_load_dwordx4 a[232:235], v6, s[40:43], s56 offen offset:0 sc1\n"
"v_mul_f32_dpp v124, v137, v45 row_newbcast:0 row_mask:0xf bank_mask:0xf\n"
"v_mfma_f32_16x16x32_bf16 v[52:55], v[84:87], a[100:103], v[52:55]\n"
"s_barrier\n"
"v_mfma_f32_16x16x32_bf16 v[52:55], v[88:91], a[104:107], v[52:55]\n"
"ds_write_addtid_b32 v48 offset:1024\n"
"ds_read_b128 v[56:59], v2 offset:1024\n"
"v_mfma_f32_16x16x32_bf16 v[52:55], v[92:95], a[108:111], v[52:55]\n"
"ds_write_addtid_b32 v49 offset:5664\n"
"ds_read_b128 v[60:63], v3 offset:2048\n"
"v_mfma_f32_16x16x32_bf16 v[52:55], v[96:99], a[112:115], v[52:55]\n"
"ds_write_addtid_b32 v50 offset:10368\n"
"buffer_load_dwordx4 a[236:239], v6, s[40:43], s56 offen offset:1024 sc1\n"
"v_mfma_f32_16x16x32_bf16 v[52:55], v[100:103], a[116:119], v[52:55]\n"
"ds_write_addtid_b32 v51 offset:15008\n"
"ds_read_b128 v[64:67], v4 offset:3072\n"
"v_mfma_f32_16x16x32_bf16 v[52:55], v[104:107], a[120:123], v[52:55]\n"
"ds_read_b128 v[68:71], v2 offset:1280\n"
"ds_read_b128 v[48:51], v3 offset:2304\n"
"v_mfma_f32_16x16x32_bf16 v[52:55], v[108:111], a[124:127], v[52:55]\n"
"ds_read_b128 v[72:75], v4 offset:3328\n"
"v_mfma_f32_16x16x32_bf16 v[76:79], v[80:83], a[0:3], 0\n"
"buffer_load_dwordx4 a[240:243], v7, s[40:43], s56 offen offset:0 sc1\n"
"v_mfma_f32_16x16x32_bf16 v[76:79], v[84:87], a[4:7], v[76:79]\n"
"ds_write_addtid_b32 v52 offset:1280\n"
"v_mfma_f32_16x16x32_bf16 v[76:79], v[88:91], a[8:11], v[76:79]\n"
"ds_write_addtid_b32 v53 offset:5920\n"
"v_mfma_f32_16x16x32_bf16 v[76:79], v[92:95], a[12:15], v[76:79]\n"
"s_waitcnt lgkmcnt(5)\n"
"ds_write_addtid_b32 v54 offset:10624\n"
"v_add_f32 v56, v56, v60\n"
"v_mfma_f32_16x16x32_bf16 v[76:79], v[96:99], a[16:19], v[76:79]\n"
"ds_write_addtid_b32 v55 offset:15264\n"
"buffer_load_dwordx4 a[244:247], v7, s[40:43], s56 offen offset:1024 sc1\n"
"v_mfma_f32_16x16x32_bf16 v[76:79], v[100:103], a[20:23], v[76:79]\n"
"v_add_f32 v57, v57, v61\n"
"v_add_f32 v58, v58, v62\n"
"v_mfma_f32_16x16x32_bf16 v[76:79], v[104:107], a[24:27], v[76:79]\n"
"v_add_f32 v59, v59, v63\n"
"v_add_f32 v56, v56, v64\n"
"v_mfma_f32_16x16x32_bf16 v[76:79], v[108:111], a[28:31], v[76:79]\n"
"v_add_f32 v57, v57, v65\n"
"v_add_f32 v58, v58, v66\n"
"v_mfma_f32_16x16x32_bf16 v[52:55], v[80:83], a[32:35], 0\n"
"buffer_load_dwordx4 a[248:251], v8, s[40:43], s56 offen offset:0 sc1\n"
"v_add_f32 v59, v59, v67\n"
"v_mfma_f32_16x16x32_bf16 v[52:55], v[84:87], a[36:39], v[52:55]\n"
"ds_write_addtid_b32 v76 offset:1536\n"
"v_add_f32 v56, v56, v116\n"
"v_mfma_f32_16x16x32_bf16 v[52:55], v[88:91], a[40:43], v[52:55]\n"
"ds_write_addtid_b32 v77 offset:6176\n"
"v_add_f32 v57, v57, v117\n"
"v_mfma_f32_16x16x32_bf16 v[52:55], v[92:95], a[44:47], v[52:55]\n"
"ds_write_addtid_b32 v78 offset:10880\n"
"buffer_load_dwordx4 v[136:139], v10, s[40:43], s50 offen sc1\n"
"v_mfma_f32_16x16x32_bf16 v[52:55], v[96:99], a[48:51], v[52:55]\n"
"ds_write_addtid_b32 v79 offset:15520\n"
"buffer_load_dwordx4 a[252:255], v8, s[40:43], s56 offen offset:1024 sc1\n"
"v_mfma_f32_16x16x32_bf16 v[52:55], v[100:103], a[52:55], v[52:55]\n"
"v_add_f32 v58, v58, v118\n"
"v_add_f32 v59, v59, v119\n"
"v_mfma_f32_16x16x32_bf16 v[52:55], v[104:107], a[56:59], v[52:55]\n"
"v_mul_f32 v56, v255, v56\n"
"v_mul_f32 v57, v255, v57\n"
"v_mfma_f32_16x16x32_bf16 v[52:55], v[108:111], a[60:63], v[52:55]\n"
"v_mul_f32 v58, v255, v58\n"
"v_mul_f32 v59, v255, v59\n"
"v_mfma_f32_16x16x32_bf16 v[112:115], v[80:83], a[128:131], v[112:115]\n"
"v_cvt_pk_bf16_f32 v60, v56, v57\n"
"v_cvt_pk_bf16_f32 v61, v58, v59\n"
"v_mfma_f32_16x16x32_bf16 v[112:115], v[84:87], a[132:135], v[112:115]\n"
"ds_write_addtid_b32 v52 offset:1792\n"
"ds_write_addtid_b32 v60 offset:18592\n"
"v_mfma_f32_16x16x32_bf16 v[112:115], v[88:91], a[136:139], v[112:115]\n"
"ds_write_addtid_b32 v53 offset:6432\n"
"ds_write_addtid_b32 v61 offset:18848\n"
"v_mfma_f32_16x16x32_bf16 v[112:115], v[92:95], a[140:143], v[112:115]\n"
"s_waitcnt lgkmcnt(12)\n"
"ds_write_addtid_b32 v54 offset:11136\n"
"v_add_f32 v68, v68, v48\n"
"v_mfma_f32_16x16x32_bf16 v[112:115], v[96:99], a[144:147], v[112:115]\n"
"ds_write_addtid_b32 v55 offset:15776\n"
"v_add_f32 v69, v69, v49\n"
"v_mfma_f32_16x16x32_bf16 v[112:115], v[100:103], a[148:151], v[112:115]\n"
"v_add_f32 v70, v70, v50\n"
"v_add_f32 v71, v71, v51\n"
"v_mfma_f32_16x16x32_bf16 v[112:115], v[104:107], a[152:155], v[112:115]\n"
"v_add_f32 v68, v68, v72\n"
"v_add_f32 v69, v69, v73\n"
"v_mfma_f32_16x16x32_bf16 v[112:115], v[108:111], a[156:159], v[112:115]\n"
"v_add_f32 v70, v70, v74\n"
"v_add_f32 v71, v71, v75\n"
"v_mfma_f32_16x16x32_bf16 v[124:127], v[80:83], a[160:163], v[124:127]\n"
"v_add_f32 v68, v68, v120\n"
"v_add_f32 v69, v69, v121\n"
"v_mfma_f32_16x16x32_bf16 v[124:127], v[84:87], a[164:167], v[124:127]\n"
"v_add_f32 v70, v70, v122\n"
"v_add_f32 v71, v71, v123\n"
"v_mfma_f32_16x16x32_bf16 v[124:127], v[88:91], a[168:171], v[124:127]\n"
"v_mul_f32 v68, v255, v68\n"
"v_mul_f32 v69, v255, v69\n"
"v_mfma_f32_16x16x32_bf16 v[124:127], v[92:95], a[172:175], v[124:127]\n"
"s_waitcnt vmcnt(11)\n"
"v_cmp_ne_u64 vcc, s[48:49], v[130:131]\n"
"v_mul_f32 v70, v255, v70\n"
"v_mfma_f32_16x16x32_bf16 v[124:127], v[96:99], a[176:179], v[124:127]\n"
"v_mul_f32 v71, v255, v71\n"
"v_cvt_pk_bf16_f32 v62, v68, v69\n"
"v_mfma_f32_16x16x32_bf16 v[124:127], v[100:103], a[180:183], v[124:127]\n"
"s_cbranch_vccnz .LQ_1s\n"
".LQ_1r:\n"
"v_cvt_pk_bf16_f32 v63, v70, v71\n"
"v_mfma_f32_16x16x32_bf16 v[124:127], v[104:107], a[184:187], v[124:127]\n"
"s_add_u32 s50, s50, 0x100000\n"
"s_and_b32 s56, s50, 0xfbffffff\n"
"ds_write_addtid_b32 v62 offset:19104\n"
"v_mfma_f32_16x16x32_bf16 v[124:127], v[108:111], a[188:191], v[124:127]\n"
"ds_write_addtid_b32 v63 offset:19360\n"
"v_mfma_f32_16x16x32_bf16 v[64:67], a[192:195], a[64:67], 0\n"
"buffer_load_dwordx4 v[48:51], v5, s[40:43], s56 offen offset:0 sc1\n"
"v_mfma_f32_16x16x32_bf16 v[64:67], a[196:199], a[68:71], v[64:67]\n"
"v_mfma_f32_16x16x32_bf16 v[64:67], a[200:203], a[72:75], v[64:67]\n"
"v_mfma_f32_16x16x32_bf16 v[64:67], a[204:207], a[76:79], v[64:67]\n"
"v_mfma_f32_16x16x32_bf16 v[64:67], a[208:211], a[80:83], v[64:67]\n"
"buffer_load_dwordx4 v[52:55], v5, s[40:43], s56 offen offset:1024 sc1\n"
"v_mul_f32 v250, 0x3e8293ee, v140\n"
"v_mfma_f32_16x16x32_bf16 v[64:67], a[212:215], a[84:87], v[64:67]\n"
"s_waitcnt vmcnt(12)\n"
"v_mul_f32 v118, v141, v30\n"
"v_mul_f32 v119, v141, v31\n"
"v_mfma_f32_16x16x32_bf16 v[64:67], a[216:219], a[88:91], v[64:67]\n"
"v_mul_f32 v116, v141, v28\n"
"v_mul_f32 v117, v141, v29\n"
"v_mfma_f32_16x16x32_bf16 v[64:67], a[220:223], a[92:95], v[64:67]\n"
"v_mul_f32 v122, v141, v34\n"
"v_mul_f32 v123, v141, v35\n"
"v_mfma_f32_16x16x32_bf16 v[72:75], a[192:195], a[96:99], 0\n"
"buffer_load_dwordx4 v[56:59], v6, s[40:43], s56 offen offset:0 sc1\n"
"v_mul_f32 v121, v141, v33\n"
"v_mfma_f32_16x16x32_bf16 v[72:75], a[196:199], a[100:103], v[72:75]\n"
"s_waitcnt lgkmcnt(0)\n"
"ds_write_addtid_b32 v64 offset:2048\n"
"v_mul_f32 v120, v141, v32\n"
"v_mfma_f32_16x16x32_bf16 v[72:75], a[200:203], a[104:107], v[72:75]\n"
"s_barrier\n"
"v_mfma_f32_16x16x32_bf16 v[72:75], a[204:207], a[108:111], v[72:75]\n"
"ds_write_addtid_b32 v65 offset:6688\n"
"ds_read_b128 v[80:83], v2 offset:2560\n"
"v_mfma_f32_16x16x32_bf16 v[72:75], a[208:211], a[112:115], v[72:75]\n"
"ds_write_addtid_b32 v66 offset:11392\n"
"buffer_load_dwordx4 v[60:63], v6, s[40:43], s56 offen offset:1024 sc1\n"
"v_mfma_f32_16x16x32_bf16 v[72:75], a[212:215], a[116:119], v[72:75]\n"
"ds_write_addtid_b32 v67 offset:16032\n"
"ds_read_b128 v[76:79], v3 offset:3584\n"
"v_mfma_f32_16x16x32_bf16 v[72:75], a[216:219], a[120:123], v[72:75]\n"
"ds_read_b128 v[84:87], v4 offset:0\n"
"ds_read_b128 v[88:91], v2 offset:2816\n"
"v_mfma_f32_16x16x32_bf16 v[72:75], a[220:223], a[124:127], v[72:75]\n"
"ds_read_b128 v[92:95], v3 offset:3840\n"
"ds_read_b128 v[96:99], v4 offset:256\n"
"v_mfma_f32_16x16x32_bf16 v[100:103], a[128:131], a[192:195], 0\n"
"buffer_load_dwordx4 v[64:67], v7, s[40:43], s56 offen offset:0 sc1\n"
"v_mfma_f32_16x16x32_bf16 v[100:103], a[132:135], a[196:199], v[100:103]\n"
"ds_write_addtid_b32 v72 offset:2304\n"
"v_mfma_f32_16x16x32_bf16 v[100:103], a[136:139], a[200:203], v[100:103]\n"
"ds_write_addtid_b32 v73 offset:6944\n"
"v_mfma_f32_16x16x32_bf16 v[100:103], a[140:143], a[204:207], v[100:103]\n"
"ds_write_addtid_b32 v74 offset:11648\n"
"v_mfma_f32_16x16x32_bf16 v[100:103], a[144:147], a[208:211], v[100:103]\n"
"s_waitcnt lgkmcnt(6)\n"
"ds_write_addtid_b32 v75 offset:16288\n"
"buffer_load_dwordx4 v[68:71], v7, s[40:43], s56 offen offset:1024 sc1\n"
"v_mfma_f32_16x16x32_bf16 v[100:103], a[148:151], a[212:215], v[100:103]\n"
"v_add_f32 v80, v80, v76\n"
"v_add_f32 v81, v81, v77\n"
"v_mfma_f32_16x16x32_bf16 v[100:103], a[152:155], a[216:219], v[100:103]\n"
"v_add_f32 v82, v82, v78\n"
"v_add_f32 v83, v83, v79\n"
"v_mfma_f32_16x16x32_bf16 v[100:103], a[156:159], a[220:223], v[100:103]\n"
"s_waitcnt vmcnt(13)\n"
"buffer_store_dwordx2 v[0:1], v14, s[40:43], s94 offen offset:0 sc1\n"
"v_add_f32 v80, v80, v84\n"
"v_add_f32 v81, v81, v85\n"
"v_mfma_f32_16x16x32_bf16 v[104:107], a[160:163], a[192:195], 0\n"
"buffer_load_dwordx4 v[72:75], v8, s[40:43], s56 offen offset:0 sc1\n"
"v_add_f32 v82, v82, v86\n"
"v_mfma_f32_16x16x32_bf16 v[104:107], a[164:167], a[196:199], v[104:107]\n"
"ds_write_addtid_b32 v100 offset:2560\n"
"v_add_f32 v83, v83, v87\n"
"v_mfma_f32_16x16x32_bf16 v[104:107], a[168:171], a[200:203], v[104:107]\n"
"ds_write_addtid_b32 v101 offset:7200\n"
"v_add_f32 v80, v80, v112\n"
"v_mfma_f32_16x16x32_bf16 v[104:107], a[172:175], a[204:207], v[104:107]\n"
"ds_write_addtid_b32 v102 offset:11904\n"
"buffer_load_dwordx4 v[140:143], v9, s[40:43], s50 offen sc1\n"
"v_mfma_f32_16x16x32_bf16 v[104:107], a[176:179], a[208:211], v[104:107]\n"
"ds_write_addtid_b32 v103 offset:16544\n"
"buffer_load_dwordx4 v[76:79], v8, s[40:43], s56 offen offset:1024 sc1\n"
"v_mfma_f32_16x16x32_bf16 v[104:107], a[180:183], a[212:215], v[104:107]\n"
"v_add_f32 v81, v81, v113\n"
"v_add_f32 v82, v82, v114\n"
"v_mfma_f32_16x16x32_bf16 v[104:107], a[184:187], a[216:219], v[104:107]\n"
"v_add_f32 v83, v83, v115\n"
"v_mul_f32_dpp v80, v254, v80 row_newbcast:0 row_mask:0xf bank_mask:0xf\n"
"v_mfma_f32_16x16x32_bf16 v[104:107], a[188:191], a[220:223], v[104:107]\n"
"v_mul_f32_dpp v81, v254, v81 row_newbcast:1 row_mask:0xf bank_mask:0xf\n"
"v_mul_f32_dpp v82, v254, v82 row_newbcast:2 row_mask:0xf bank_mask:0xf\n"
"v_mfma_f32_16x16x32_bf16 v[116:119], a[0:3], a[192:195], v[116:119]\n"
"v_mul_f32_dpp v83, v254, v83 row_newbcast:3 row_mask:0xf bank_mask:0xf\n"
"v_cvt_pk_bf16_f32 v146, v80, v81\n"
"v_mfma_f32_16x16x32_bf16 v[116:119], a[4:7], a[196:199], v[116:119]\n"
"ds_write_addtid_b32 v104 offset:2816\n"
"v_cvt_pk_bf16_f32 v147, v82, v83\n"
"v_mfma_f32_16x16x32_bf16 v[116:119], a[8:11], a[200:203], v[116:119]\n"
"ds_write_addtid_b32 v105 offset:7456\n"
"ds_write_addtid_b32 v146 offset:19616\n"
"v_mfma_f32_16x16x32_bf16 v[116:119], a[12:15], a[204:207], v[116:119]\n"
"ds_write_addtid_b32 v106 offset:12160\n"
"ds_write_addtid_b32 v147 offset:19872\n"
"v_mfma_f32_16x16x32_bf16 v[116:119], a[16:19], a[208:211], v[116:119]\n"
"s_waitcnt lgkmcnt(13)\n"
"ds_write_addtid_b32 v107 offset:16800\n"
"v_add_f32 v88, v88, v92\n"
"v_mfma_f32_16x16x32_bf16 v[116:119], a[20:23], a[212:215], v[116:119]\n"
"v_add_f32 v89, v89, v93\n"
"v_add_f32 v90, v90, v94\n"
"v_mfma_f32_16x16x32_bf16 v[116:119], a[24:27], a[216:219], v[116:119]\n"
"v_add_f32 v91, v91, v95\n"
"v_add_f32 v88, v88, v96\n"
"v_mfma_f32_16x16x32_bf16 v[116:119], a[28:31], a[220:223], v[116:119]\n"
"v_add_f32 v89, v89, v97\n"
"v_add_f32 v90, v90, v98\n"
"v_mfma_f32_16x16x32_bf16 v[120:123], a[32:35], a[192:195], v[120:123]\n"
"v_add_f32 v91, v91, v99\n"
"v_add_f32 v88, v88, v124\n"
"v_mfma_f32_16x16x32_bf16 v[120:123], a[36:39], a[196:199], v[120:123]\n"
"v_add_f32 v89, v89, v125\n"
"v_add_f32 v90, v90, v126\n"
"v_mfma_f32_16x16x32_bf16 v[120:123], a[40:43], a[200:203], v[120:123]\n"
"v_add_f32 v91, v91, v127\n"
"v_mul_f32_dpp v88, v254, v88 row_newbcast:0 row_mask:0xf bank_mask:0xf\n"
"v_mfma_f32_16x16x32_bf16 v[120:123], a[44:47], a[204:207], v[120:123]\n"
"s_waitcnt vmcnt(11)\n"
"v_cmp_ne_u64 vcc, s[48:49], v[138:139]\n"
"v_mul_f32_dpp v89, v254, v89 row_newbcast:1 row_mask:0xf bank_mask:0xf\n"
"v_mfma_f32_16x16x32_bf16 v[120:123], a[48:51], a[208:211], v[120:123]\n"
"v_mul_f32_dpp v90, v254, v90 row_newbcast:2 row_mask:0xf bank_mask:0xf\n"
"v_mul_f32_dpp v91, v254, v91 row_newbcast:3 row_mask:0xf bank_mask:0xf\n"
"v_mfma_f32_16x16x32_bf16 v[120:123], a[52:55], a[212:215], v[120:123]\n"
"s_cbranch_vccnz .LQ_2s\n"
".LQ_2r:\n"
"v_cvt_pk_bf16_f32 v150, v88, v89\n"
"v_mfma_f32_16x16x32_bf16 v[120:123], a[56:59], a[216:219], v[120:123]\n"
"s_add_u32 s50, s50, 0x100000\n"
"s_and_b32 s56, s50, 0xfbffffff\n"
"v_cvt_pk_bf16_f32 v151, v90, v91\n"
"v_mfma_f32_16x16x32_bf16 v[120:123], a[60:63], a[220:223], v[120:123]\n"
"ds_write_addtid_b32 v150 offset:20128\n"
"ds_write_addtid_b32 v151 offset:20384\n"
"v_mfma_f32_16x16x32_bf16 v[96:99], a[224:227], a[64:67], 0\n"
"buffer_load_dwordx4 v[80:83], v5, s[40:43], s56 offen offset:0 sc1\n"
"v_mfma_f32_16x16x32_bf16 v[96:99], a[228:231], a[68:71], v[96:99]\n"
"v_mfma_f32_16x16x32_bf16 v[96:99], a[232:235], a[72:75], v[96:99]\n"
"v_mfma_f32_16x16x32_bf16 v[96:99], a[236:239], a[76:79], v[96:99]\n"
"v_mfma_f32_16x16x32_bf16 v[96:99], a[240:243], a[80:83], v[96:99]\n"
"buffer_load_dwordx4 v[84:87], v5, s[40:43], s56 offen offset:1024 sc1\n"
"v_mfma_f32_16x16x32_bf16 v[96:99], a[244:247], a[84:87], v[96:99]\n"
"s_waitcnt vmcnt(12)\n"
"v_mfma_f32_16x16x32_bf16 v[96:99], a[248:251], a[88:91], v[96:99]\n"
"v_mfma_f32_16x16x32_bf16 v[96:99], a[252:255], a[92:95], v[96:99]\n"
"v_mfma_f32_16x16x32_bf16 v[104:107], a[224:227], a[96:99], 0\n"
"buffer_load_dwordx4 v[88:91], v6, s[40:43], s56 offen offset:0 sc1\n"
"v_mfma_f32_16x16x32_bf16 v[104:107], a[228:231], a[100:103], v[104:107]\n"
"ds_write_addtid_b32 v96 offset:3072\n"
"v_mfma_f32_16x16x32_bf16 v[104:107], a[232:235], a[104:107], v[104:107]\n"
"s_waitcnt lgkmcnt(1)\n"
"ds_write_addtid_b32 v97 offset:7712\n"
"v_mfma_f32_16x16x32_bf16 v[104:107], a[236:239], a[108:111], v[104:107]\n"
"s_barrier\n"
"v_mfma_f32_16x16x32_bf16 v[104:107], a[240:243], a[112:115], v[104:107]\n"
"ds_write_addtid_b32 v98 offset:12416\n"
"buffer_load_dwordx4 v[92:95], v6, s[40:43], s56 offen offset:1024 sc1\n"
"v_mfma_f32_16x16x32_bf16 v[104:107], a[244:247], a[116:119], v[104:107]\n"
"ds_write_addtid_b32 v99 offset:17056\n"
"ds_read_b128 v[112:115], v2 offset:4096\n"
"v_mfma_f32_16x16x32_bf16 v[104:107], a[248:251], a[120:123], v[104:107]\n"
"ds_read_b128 v[108:111], v3 offset:512\n"
"ds_read_b128 v[124:127], v4 offset:1536\n"
"v_mfma_f32_16x16x32_bf16 v[104:107], a[252:255], a[124:127], v[104:107]\n"
"ds_read_b128 v[132:135], v2 offset:4352\n"
"ds_read_b128 v[152:155], v3 offset:768\n"
"v_mfma_f32_16x16x32_bf16 v[156:159], a[128:131], a[224:227], 0\n"
"buffer_load_dwordx4 v[96:99], v7, s[40:43], s56 offen offset:0 sc1\n"
"ds_read_b128 v[160:163], v4 offset:1792\n"
"v_mfma_f32_16x16x32_bf16 v[156:159], a[132:135], a[228:231], v[156:159]\n"
"ds_write_addtid_b32 v104 offset:3328\n"
"v_mfma_f32_16x16x32_bf16 v[156:159], a[136:139], a[232:235], v[156:159]\n"
"ds_write_addtid_b32 v105 offset:7968\n"
"v_mfma_f32_16x16x32_bf16 v[156:159], a[140:143], a[236:239], v[156:159]\n"
"ds_write_addtid_b32 v106 offset:12672\n"
"v_mfma_f32_16x16x32_bf16 v[156:159], a[144:147], a[240:243], v[156:159]\n"
"s_waitcnt lgkmcnt(6)\n"
"ds_write_addtid_b32 v107 offset:17312\n"
"buffer_load_dwordx4 v[100:103], v7, s[40:43], s56 offen offset:1024 sc1\n"
"v_mfma_f32_16x16x32_bf16 v[156:159], a[148:151], a[244:247], v[156:159]\n"
"v_add_f32 v112, v112, v108\n"
"v_add_f32 v113, v113, v109\n"
"v_mfma_f32_16x16x32_bf16 v[156:159], a[152:155], a[248:251], v[156:159]\n"
"v_add_f32 v114, v114, v110\n"
"v_add_f32 v115, v115, v111\n"
"v_mfma_f32_16x16x32_bf16 v[156:159], a[156:159], a[252:255], v[156:159]\n"
"v_add_f32 v112, v112, v124\n"
"v_add_f32 v113, v113, v125\n"
"v_mfma_f32_16x16x32_bf16 v[164:167], a[160:163], a[224:227], 0\n"
"buffer_load_dwordx4 v[104:107], v8, s[40:43], s56 offen offset:0 sc1\n"
"v_add_f32 v114, v114, v126\n"
"v_mfma_f32_16x16x32_bf16 v[164:167], a[164:167], a[228:231], v[164:167]\n"
"ds_write_addtid_b32 v156 offset:3584\n"
"v_add_f32 v115, v115, v127\n"
"v_mfma_f32_16x16x32_bf16 v[164:167], a[168:171], a[232:235], v[164:167]\n"
"ds_write_addtid_b32 v157 offset:8224\n"
"v_add_f32 v112, v112, v116\n"
"v_mfma_f32_16x16x32_bf16 v[164:167], a[172:175], a[236:239], v[164:167]\n"
"ds_write_addtid_b32 v158 offset:12928\n"
"buffer_load_dwordx4 v[168:171], v9, s[40:43], s50 offen sc1\n"
"v_mfma_f32_16x16x32_bf16 v[164:167], a[176:179], a[240:243], v[164:167]\n"
"ds_write_addtid_b32 v159 offset:17568\n"
"buffer_load_dwordx4 v[108:111], v8, s[40:43], s56 offen offset:1024 sc1\n"
"v_mfma_f32_16x16x32_bf16 v[164:167], a[180:183], a[244:247], v[164:167]\n"
"v_add_f32 v113, v113, v117\n"
"v_add_f32 v114, v114, v118\n"
"v_mfma_f32_16x16x32_bf16 v[164:167], a[184:187], a[248:251], v[164:167]\n"
"v_add_f32 v115, v115, v119\n"
"v_cvt_pk_bf16_f32 v156, v112, v113\n"
"v_mfma_f32_16x16x32_bf16 v[164:167], a[188:191], a[252:255], v[164:167]\n"
"v_cvt_pk_bf16_f32 v157, v114, v115\n"
"v_mfma_f32_16x16x32_bf16 v[116:119], a[224:227], a[0:3], 0\n"
"ds_read2st64_b32 v[124:125], v11 offset0:0 offset1:1\n"
"ds_read2st64_b32 v[126:127], v11 offset0:2 offset1:3\n"
"v_mfma_f32_16x16x32_bf16 v[116:119], a[228:231], a[4:7], v[116:119]\n"
"s_waitcnt lgkmcnt(10)\n"
"ds_write_addtid_b32 v164 offset:3840\n"
"v_add_f32 v132, v132, v152\n"
"v_mfma_f32_16x16x32_bf16 v[116:119], a[232:235], a[8:11], v[116:119]\n"
"ds_write_addtid_b32 v165 offset:8480\n"
"v_add_f32 v133, v133, v153\n"
"v_mfma_f32_16x16x32_bf16 v[116:119], a[236:239], a[12:15], v[116:119]\n"
"ds_write_addtid_b32 v166 offset:13184\n"
"v_add_f32 v134, v134, v154\n"
"v_mfma_f32_16x16x32_bf16 v[116:119], a[240:243], a[16:19], v[116:119]\n"
"ds_write_addtid_b32 v167 offset:17824\n"
"v_add_f32 v135, v135, v155\n"
"v_mfma_f32_16x16x32_bf16 v[116:119], a[244:247], a[20:23], v[116:119]\n"
"v_add_f32 v132, v132, v160\n"
"v_add_f32 v133, v133, v161\n"
"v_mfma_f32_16x16x32_bf16 v[116:119], a[248:251], a[24:27], v[116:119]\n"
"v_add_f32 v134, v134, v162\n"
"v_add_f32 v135, v135, v163\n"
"v_mfma_f32_16x16x32_bf16 v[116:119], a[252:255], a[28:31], v[116:119]\n"
"v_add_f32 v132, v132, v120\n"
"v_add_f32 v133, v133, v121\n"
"v_mfma_f32_16x16x32_bf16 v[152:155], a[224:227], a[32:35], 0\n"
"v_add_f32 v134, v134, v122\n"
"v_add_f32 v135, v135, v123\n"
"v_mfma_f32_16x16x32_bf16 v[152:155], a[228:231], a[36:39], v[152:155]\n"
"ds_write_addtid_b32 v116 offset:4096\n"
"v_cvt_pk_bf16_f32 v158, v132, v133\n"
"v_mfma_f32_16x16x32_bf16 v[152:155], a[232:235], a[40:43], v[152:155]\n"
"ds_write_addtid_b32 v117 offset:8736\n"
"v_cvt_pk_bf16_f32 v159, v134, v135\n"
"v_mfma_f32_16x16x32_bf16 v[152:155], a[236:239], a[44:47], v[152:155]\n"
"s_waitcnt vmcnt(10)\n"
"ds_write_addtid_b32 v118 offset:13440\n"
"v_cmp_ne_u64 vcc, s[48:49], v[142:143]\n"
"v_mfma_f32_16x16x32_bf16 v[152:155], a[240:243], a[48:51], v[152:155]\n"
"s_waitcnt lgkmcnt(7)\n"
"ds_write_addtid_b32 v119 offset:18080\n"
"v_mfma_f32_16x16x32_bf16 v[160:163], v[124:127], v[156:159], v[20:23]\n"
"v_mfma_f32_16x16x32_bf16 v[152:155], a[244:247], a[52:55], v[152:155]\n"
"s_cbranch_vccnz .LQ_3s\n"
".LQ_3r:\n"
"v_mfma_f32_16x16x32_bf16 v[152:155], a[248:251], a[56:59], v[152:155]\n"
"s_add_u32 s50, s50, 0x100000\n"
"s_and_b32 s56, s50, 0xfbffffff\n"
"v_mfma_f32_16x16x32_bf16 v[152:155], a[252:255], a[60:63], v[152:155]\n"
"s_branch .LQ_top\n"
".LQ_1s:\n"
"s_nop 7\n"
"s_cmp_ge_u32 s50, s53\n"
"s_cbranch_scc1 .LQ_end\n"
"s_cmp_ge_u32 s50, s52\n"
"s_cbranch_scc1 .LQ_1r\n"
"s_sub_u32 s58, s50, 0x100000\n"
"s_mov_b32 s59, 200000\n"
".LQ_1p:\n"
"buffer_load_dwordx4 v[128:131], v9, s[40:43], s58 offen sc1\n"
"s_waitcnt vmcnt(0)\n"
"v_cmp_ne_u64 vcc, s[48:49], v[130:131]\n"
"s_cbranch_vccz .LQ_1px\n"
"s_sleep 1\n"
"s_sub_u32 s59, s59, 1\n"
"s_cbranch_scc0 .LQ_1p\n"
"s_branch .LQ_end\n"
".LQ_1px:\n"
"s_branch .LQ_1r\n"
".LQ_2s:\n"
"s_nop 7\n"
"s_cmp_ge_u32 s50, s53\n"
"s_cbranch_scc1 .LQ_end\n"
"s_cmp_ge_u32 s50, s52\n"
"s_cbranch_scc1 .LQ_2r\n"
"s_sub_u32 s58, s50, 0x100000\n"
"s_mov_b32 s59, 200000\n"
".LQ_2p:\n"
"buffer_load_dwordx4 v[136:139], v10, s[40:43], s58 offen sc1\n"
"s_waitcnt vmcnt(0)\n"
"v_cmp_ne_u64 vcc, s[48:49], v[138:139]\n"
"s_cbranch_vccz .LQ_2px\n"
"s_sleep 1\n"
"s_sub_u32 s59, s59, 1\n"
"s_cbranch_scc0 .LQ_2p\n"
"s_branch .LQ_end\n"
".LQ_2px:\n"
"s_branch .LQ_2r\n"
".LQ_3s:\n"
"s_nop 7\n"
"s_cmp_ge_u32 s50, s53\n"
"s_cbranch_scc1 .LQ_end\n"
"s_cmp_ge_u32 s50, s52\n"
"s_cbranch_scc1 .LQ_3r\n"
"s_sub_u32 s58, s50, 0x100000\n"
"s_mov_b32 s59, 200000\n"
".LQ_3p:\n"
"buffer_load_dwordx4 v[140:143], v9, s[40:43], s58 offen sc1\n"
"s_waitcnt vmcnt(0)\n"
"v_cmp_ne_u64 vcc, s[48:49], v[142:143]\n"
"s_cbranch_vccz .LQ_3px\n"
"s_sleep 1\n"
"s_sub_u32 s59, s59, 1\n"
"s_cbranch_scc0 .LQ_3p\n"
"s_branch .LQ_end\n"
".LQ_3px:\n"
"s_branch .LQ_3r\n"
".LQ_4s:\n"
"s_nop 7\n"
"s_cmp_ge_u32 s50, s53\n"
"s_cbranch_scc1 .LQ_end\n"
"s_cmp_ge_u32 s50, s52\n"
"s_cbranch_scc1 .LQ_4r\n"
"s_sub_u32 s58, s50, 0x100000\n"
"s_mov_b32 s59, 200000\n"
".LQ_4p:\n"
"buffer_load_dwordx4 v[168:171], v9, s[40:43], s58 offen sc1\n"
"s_waitcnt vmcnt(0)\n"
"v_cmp_ne_u64 vcc, s[48:49], v[170:171]\n"
"s_cbranch_vccz .LQ_4px\n"
"s_sleep 1\n"
"s_sub_u32 s59, s59, 1\n"
"s_cbranch_scc0 .LQ_4p\n"
"s_branch .LQ_end\n"
".LQ_4px:\n"
"s_branch .LQ_4r\n"
".LQ_fs:\n"
"s_nop 7\n"
"v_mov_b32 v160, 0xff800000\n"
"v_mov_b32 v161, 0xff800000\n"
"v_mov_b32 v162, 0xff800000\n"
"v_mov_b32 v163, 0xff800000\n"
"v_mov_b32 v146, 0\n"
"v_mov_b32 v147, 0\n"
"v_mov_b32 v150, 0\n"
"v_mov_b32 v151, 0\n"
"s_add_u32 s60, s60, 0x4000000\n"
"s_branch .LQ_fr\n"
".LQ_end:\n"
"s_waitcnt vmcnt(0) lgkmcnt(0)\n"
"s_endpgm\n"
      :: "{v2}"((unsigned)(lds0 + ((wid + 3) & 3) * 20640 + trp)), "{v3}"((unsigned)(lds0 + ((wid + 2) & 3) * 20640 + trp)), "{v4}"((unsigned)(lds0 + ((wid + 1) & 3) * 20640 + trp)), "{v5}"((unsigned)(lane * 16 + wid * 8192 + ((0 + rot) & 3) * 2048 + 0)), "{v6}"((unsigned)(lane * 16 + wid * 8192 + ((1 + rot) & 3) * 2048 + 0)), "{v7}"((unsigned)(lane * 16 + wid * 8192 + ((2 + rot) & 3) * 2048 + 0)), "{v8}"((unsigned)(lane * 16 + wid * 8192 + ((3 + rot) & 3) * 2048 + 0)), "{v9}"((unsigned)(li * 16 + 352256 + Q_GL * 1048576)), "{v10}"((unsigned)(((li + 4 * lg) & 15) * 16 + 352256 + Q_GL * 1048576)), "{v11}"((unsigned)(lds0 + ((wid + 1) & 3) * 20640 + 18592 + lane * 4)), "{v12}"((unsigned)(lds0 + ((wid + 2) & 3) * 20640 + 18592 + lane * 4)), "{v13}"((unsigned)(lane * 16 + idx * 1024 + 32768)), "{v14}"((unsigned)(lane ? DROP : idx * 8 + 352512)), "{v16}"((unsigned)(qmask(0, 0))), "{v17}"((unsigned)(qmask(1, 0))), "{v18}"((unsigned)(qmask(2, 0))), "{v19}"((unsigned)(qmask(3, 0))), "{v20}"((unsigned)(qmask(0, 1))), "{v21}"((unsigned)(qmask(1, 1))), "{v22}"((unsigned)(qmask(2, 1))), "{v23}"((unsigned)(qmask(3, 1))), "{v24}"((unsigned)(pmask(0))), "{v25}"((unsigned)(pmask(1))), "{v26}"((unsigned)(pmask(2))), "{v27}"((unsigned)(pmask(3))), "{v48}"((unsigned)(vT + tsw)), "{v49}"((unsigned)(vT + (tsw ^ 8))), "{v50}"((unsigned)(vD + ((0 + rot) & 3) * 393216)), "{v51}"((unsigned)(vD + ((1 + rot) & 3) * 393216)), "{v52}"((unsigned)(vD + ((2 + rot) & 3) * 393216)), "{v53}"((unsigned)(vD + ((3 + rot) & 3) * 393216)), "{v54}"((unsigned)(lds0 + wid * 20640 + trp)), "{s[40:43]}"(rS), "{s[44:47]}"(rW), "{s[48:49]}"(wsp), "{s50}"((unsigned)(sA)), "{s51}"((unsigned)(sLo)), "{s52}"((unsigned)(sHi)), "{s53}"((unsigned)(sEx)), "{s54}"((unsigned)(sEnt)), "{s55}"((unsigned)(sLd)), "{s56}"((unsigned)(sA & 0xfbffffff)), "{s57}"((unsigned)(lds0 + wid * 20640)), "{s60}"((unsigned)(sLo)), "{s61}"((unsigned)(sW0 + 0)), "{s62}"((unsigned)(sW0 + 2048)), "{s63}"((unsigned)(sW0 + 4096))
      : "v255", "a255", "s95", "vcc", "scc", "memory");

  }
  const unsigned sA = sLo - 3 * RS, sHi = sLo + (set ? 84 : 85) * 3 * RS;
  if (role == 1) {
    const unsigned sEx = sHi + O_EX * 3 * RS, sEnt = 0, tsw = 0, pair = idx * 4 + wid, rot = 0;
    const unsigned sW0 = wid * 256 * 2048 + idx * 256, vD = (lane >> 2) * 2048 + (lane & 3) * 16;
    const u32x4 rW = desc(a.wo, 1024 * 2048);
    asm volatile(
"s_nop 4\n"
"v_mov_b32 v186, 0\n"
"global_load_dwordx2 v[0:1], v186, s[48:49] sc1\n"
"v_mov_b32 v188, 0x3f803f80\n"
"v_mov_b32 v189, 0x3f803f80\n"
"v_mov_b32 v190, 0x3f803f80\n"
"v_mov_b32 v191, 0x3f803f80\n"
"s_add_u32 m0, s55, 0\n"
"s_add_u32 s58, s64, 0\n"
"buffer_load_dwordx4 v22, s[44:47], s58 offen lds\n"
"s_add_u32 m0, s55, 1024\n"
"s_add_u32 s58, s64, 32768\n"
"buffer_load_dwordx4 v22, s[44:47], s58 offen lds\n"
"s_add_u32 m0, s55, 2048\n"
"s_add_u32 s58, s65, 0\n"
"buffer_load_dwordx4 v22, s[44:47], s58 offen lds\n"
"s_add_u32 m0, s55, 3072\n"
"s_add_u32 s58, s65, 32768\n"
"buffer_load_dwordx4 v22, s[44:47], s58 offen lds\n"
"s_add_u32 m0, s55, 4096\n"
"s_add_u32 s58, s66, 0\n"
"buffer_load_dwordx4 v22, s[44:47], s58 offen lds\n"
"s_add_u32 m0, s55, 5120\n"
"s_add_u32 s58, s66, 32768\n"
"buffer_load_dwordx4 v22, s[44:47], s58 offen lds\n"
"s_add_u32 m0, s55, 6144\n"
"s_add_u32 s58, s67, 0\n"
"buffer_load_dwordx4 v22, s[44:47], s58 offen lds\n"
"s_add_u32 m0, s55, 7168\n"
"s_add_u32 s58, s67, 32768\n"
"buffer_load_dwordx4 v22, s[44:47], s58 offen lds\n"
"s_add_u32 m0, s55, 8192\n"
"s_add_u32 s58, s64, 65536\n"
"buffer_load_dwordx4 v22, s[44:47], s58 offen lds\n"
"s_add_u32 m0, s55, 9216\n"
"s_add_u32 s58, s64, 98304\n"
"buffer_load_dwordx4 v22, s[44:47], s58 offen lds\n"
"s_add_u32 m0, s55, 10240\n"
"s_add_u32 s58, s65, 65536\n"
"buffer_load_dwordx4 v22, s[44:47], s58 offen lds\n"
"s_add_u32 m0, s55, 11264\n"
"s_add_u32 s58, s65, 98304\n"
"buffer_load_dwordx4 v22, s[44:47], s58 offen lds\n"
"s_add_u32 m0, s55, 12288\n"
"s_add_u32 s58, s66, 65536\n"
"buffer_load_dwordx4 v22, s[44:47], s58 offen lds\n"
"s_add_u32 m0, s55, 13312\n"
"s_add_u32 s58, s66, 98304\n"
"buffer_load_dwordx4 v22, s[44:47], s58 offen lds\n"
"s_add_u32 m0, s55, 14336\n"
"s_add_u32 s58, s67, 65536\n"
"buffer_load_dwordx4 v22, s[44:47], s58 offen lds\n"
"s_add_u32 m0, s55, 15360\n"
"s_add_u32 s58, s67, 98304\n"
"buffer_load_dwordx4 v22, s[44:47], s58 offen lds\n"
"s_add_u32 m0, s55, 16384\n"
"s_add_u32 s58, s64, 0\n"
"buffer_load_dwordx4 v23, s[44:47], s58 offen lds\n"
"s_add_u32 m0, s55, 17408\n"
"s_add_u32 s58, s64, 32768\n"
"buffer_load_dwordx4 v23, s[44:47], s58 offen lds\n"
"s_add_u32 m0, s55, 18432\n"
"s_add_u32 s58, s65, 0\n"
"buffer_load_dwordx4 v23, s[44:47], s58 offen lds\n"
"s_add_u32 m0, s55, 19456\n"
"s_add_u32 s58, s65, 32768\n"
"buffer_load_dwordx4 v23, s[44:47], s58 offen lds\n"
"s_add_u32 m0, s55, 20480\n"
"s_add_u32 s58, s66, 0\n"
"buffer_load_dwordx4 v23, s[44:47], s58 offen lds\n"
"s_add_u32 m0, s55, 21504\n"
"s_add_u32 s58, s66, 32768\n"
"buffer_load_dwordx4 v23, s[44:47], s58 offen lds\n"
"s_add_u32 m0, s55, 22528\n"
"s_add_u32 s58, s67, 0\n"
"buffer_load_dwordx4 v23, s[44:47], s58 offen lds\n"
"s_add_u32 m0, s55, 23552\n"
"s_add_u32 s58, s67, 32768\n"
"buffer_load_dwordx4 v23, s[44:47], s58 offen lds\n"
"s_add_u32 m0, s55, 24576\n"
"s_add_u32 s58, s64, 65536\n"
"buffer_load_dwordx4 v23, s[44:47], s58 offen lds\n"
"s_add_u32 m0, s55, 25600\n"
"s_add_u32 s58, s64, 98304\n"
"buffer_load_dwordx4 v23, s[44:47], s58 offen lds\n"
"s_add_u32 m0, s55, 26624\n"
"s_add_u32 s58, s65, 65536\n"
"buffer_load_dwordx4 v23, s[44:47], s58 offen lds\n"
"s_add_u32 m0, s55, 27648\n"
"s_add_u32 s58, s65, 98304\n"
"buffer_load_dwordx4 v23, s[44:47], s58 offen lds\n"
"s_add_u32 m0, s55, 28672\n"
"s_add_u32 s58, s66, 65536\n"
"buffer_load_dwordx4 v23, s[44:47], s58 offen lds\n"
"s_add_u32 m0, s55, 29696\n"
"s_add_u32 s58, s66, 98304\n"
"buffer_load_dwordx4 v23, s[44:47], s58 offen lds\n"
"s_add_u32 m0, s55, 30720\n"
"s_add_u32 s58, s67, 65536\n"
"buffer_load_dwordx4 v23, s[44:47], s58 offen lds\n"
"s_add_u32 m0, s55, 31744\n"
"s_add_u32 s58, s67, 98304\n"
"buffer_load_dwordx4 v23, s[44:47], s58 offen lds\n"
"s_add_u32 m0, s55, 32768\n"
"s_add_u32 s58, s64, 0\n"
"buffer_load_dwordx4 v24, s[44:47], s58 offen lds\n"
"s_add_u32 m0, s55, 33792\n"
"s_add_u32 s58, s64, 32768\n"
"buffer_load_dwordx4 v24, s[44:47], s58 offen lds\n"
"s_add_u32 m0, s55, 34816\n"
"s_add_u32 s58, s65, 0\n"
"buffer_load_dwordx4 v24, s[44:47], s58 offen lds\n"
"s_add_u32 m0, s55, 35840\n"
"s_add_u32 s58, s65, 32768\n"
"buffer_load_dwordx4 v24, s[44:47], s58 offen lds\n"
"s_add_u32 m0, s55, 36864\n"
"s_add_u32 s58, s66, 0\n"
"buffer_load_dwordx4 v24, s[44:47], s58 offen lds\n"
"s_add_u32 m0, s55, 37888\n"
"s_add_u32 s58, s66, 32768\n"
"buffer_load_dwordx4 v24, s[44:47], s58 offen lds\n"
"s_add_u32 m0, s55, 38912\n"
"s_add_u32 s58, s67, 0\n"
"buffer_load_dwordx4 v24, s[44:47], s58 offen lds\n"
"s_add_u32 m0, s55, 39936\n"
"s_add_u32 s58, s67, 32768\n"
"buffer_load_dwordx4 v24, s[44:47], s58 offen lds\n"
"s_waitcnt vmcnt(38)\n"
"ds_read_b64_tr_b16 a[0:1], v20 offset:0\n"
"ds_read_b64_tr_b16 a[2:3], v20 offset:256\n"
"ds_read_b64_tr_b16 a[32:33], v21 offset:0\n"
"ds_read_b64_tr_b16 a[34:35], v21 offset:256\n"
"v_add_u32 v0, 2, v0\n"
"v_add_u32 v1, 0x9e3779b9, v1\n"
"v_or_b32 v0, 1, v0\n"
"v_readfirstlane_b32 s49, v1\n"
"v_readfirstlane_b32 s48, v0\n"
"s_waitcnt vmcnt(36)\n"
"ds_read_b64_tr_b16 a[64:65], v20 offset:2048\n"
"ds_read_b64_tr_b16 a[66:67], v20 offset:2304\n"
"ds_read_b64_tr_b16 a[96:97], v21 offset:2048\n"
"ds_read_b64_tr_b16 a[98:99], v21 offset:2304\n"
"s_waitcnt lgkmcnt(4)\n"
"s_add_u32 m0, s55, 0\n"
"s_add_u32 s58, s64, 65536\n"
"buffer_load_dwordx4 v24, s[44:47], s58 offen lds\n"
"s_add_u32 m0, s55, 1024\n"
"s_add_u32 s58, s64, 98304\n"
"buffer_load_dwordx4 v24, s[44:47], s58 offen lds\n"
"s_waitcnt vmcnt(36)\n"
"ds_read_b64_tr_b16 a[128:129], v20 offset:4096\n"
"ds_read_b64_tr_b16 a[130:131], v20 offset:4352\n"
"ds_read_b64_tr_b16 a[160:161], v21 offset:4096\n"
"ds_read_b64_tr_b16 a[162:163], v21 offset:4352\n"
"s_waitcnt lgkmcnt(4)\n"
"s_add_u32 m0, s55, 2048\n"
"s_add_u32 s58, s65, 65536\n"
"buffer_load_dwordx4 v24, s[44:47], s58 offen lds\n"
"s_add_u32 m0, s55, 3072\n"
"s_add_u32 s58, s65, 98304\n"
"buffer_load_dwordx4 v24, s[44:47], s58 offen lds\n"
"s_waitcnt vmcnt(36)\n"
"ds_read_b64_tr_b16 a[192:193], v20 offset:6144\n"
"ds_read_b64_tr_b16 a[194:195], v20 offset:6400\n"
"ds_read_b64_tr_b16 a[224:225], v21 offset:6144\n"
"ds_read_b64_tr_b16 a[226:227], v21 offset:6400\n"
"s_waitcnt lgkmcnt(4)\n"
"s_add_u32 m0, s55, 4096\n"
"s_add_u32 s58, s66, 65536\n"
"buffer_load_dwordx4 v24, s[44:47], s58 offen lds\n"
"s_add_u32 m0, s55, 5120\n"
"s_add_u32 s58, s66, 98304\n"
"buffer_load_dwordx4 v24, s[44:47], s58 offen lds\n"
"s_waitcnt vmcnt(36)\n"
"ds_read_b64_tr_b16 a[4:5], v20 offset:8192\n"
"ds_read_b64_tr_b16 a[6:7], v20 offset:8448\n"
"ds_read_b64_tr_b16 a[36:37], v21 offset:8192\n"
"ds_read_b64_tr_b16 a[38:39], v21 offset:8448\n"
"s_waitcnt lgkmcnt(4)\n"
"s_add_u32 m0, s55, 6144\n"
"s_add_u32 s58, s67, 65536\n"
"buffer_load_dwordx4 v24, s[44:47], s58 offen lds\n"
"s_add_u32 m0, s55, 7168\n"
"s_add_u32 s58, s67, 98304\n"
"buffer_load_dwordx4 v24, s[44:47], s58 offen lds\n"
"s_waitcnt vmcnt(36)\n"
"ds_read_b64_tr_b16 a[68:69], v20 offset:10240\n"
"ds_read_b64_tr_b16 a[70:71], v20 offset:10496\n"
"ds_read_b64_tr_b16 a[100:101], v21 offset:10240\n"
"ds_read_b64_tr_b16 a[102:103], v21 offset:10496\n"
"s_waitcnt lgkmcnt(4)\n"
"s_add_u32 m0, s55, 8192\n"
"s_add_u32 s58, s64, 0\n"
"buffer_load_dwordx4 v25, s[44:47], s58 offen lds\n"
"s_add_u32 m0, s55, 9216\n"
"s_add_u32 s58, s64, 32768\n"
"buffer_load_dwordx4 v25, s[44:47], s58 offen lds\n"
"s_waitcnt vmcnt(36)\n"
"ds_read_b64_tr_b16 a[132:133], v20 offset:12288\n"
"ds_read_b64_tr_b16 a[134:135], v20 offset:12544\n"
"ds_read_b64_tr_b16 a[164:165], v21 offset:12288\n"
"ds_read_b64_tr_b16 a[166:167], v21 offset:12544\n"
"s_waitcnt lgkmcnt(4)\n"
"s_add_u32 m0, s55, 10240\n"
"s_add_u32 s58, s65, 0\n"
"buffer_load_dwordx4 v25, s[44:47], s58 offen lds\n"
"s_add_u32 m0, s55, 11264\n"
"s_add_u32 s58, s65, 32768\n"
"buffer_load_dwordx4 v25, s[44:47], s58 offen lds\n"
"s_waitcnt vmcnt(36)\n"
"ds_read_b64_tr_b16 a[196:197], v20 offset:14336\n"
"ds_read_b64_tr_b16 a[198:199], v20 offset:14592\n"
"ds_read_b64_tr_b16 a[228:229], v21 offset:14336\n"
"ds_read_b64_tr_b16 a[230:231], v21 offset:14592\n"
"s_waitcnt lgkmcnt(4)\n"
"s_add_u32 m0, s55, 12288\n"
"s_add_u32 s58, s66, 0\n"
"buffer_load_dwordx4 v25, s[44:47], s58 offen lds\n"
"s_add_u32 m0, s55, 13312\n"
"s_add_u32 s58, s66, 32768\n"
"buffer_load_dwordx4 v25, s[44:47], s58 offen lds\n"
"s_waitcnt vmcnt(36)\n"
"ds_read_b64_tr_b16 a[8:9], v20 offset:16384\n"
"ds_read_b64_tr_b16 a[10:11], v20 offset:16640\n"
"ds_read_b64_tr_b16 a[40:41], v21 offset:16384\n"
"ds_read_b64_tr_b16 a[42:43], v21 offset:16640\n"
"s_waitcnt lgkmcnt(4)\n"
"s_add_u32 m0, s55, 14336\n"
"s_add_u32 s58, s67, 0\n"
"buffer_load_dwordx4 v25, s[44:47], s58 offen lds\n"
"s_add_u32 m0, s55, 15360\n"
"s_add_u32 s58, s67, 32768\n"
"buffer_load_dwordx4 v25, s[44:47], s58 offen lds\n"
"s_waitcnt vmcnt(36)\n"
"ds_read_b64_tr_b16 a[72:73], v20 offset:18432\n"
"ds_read_b64_tr_b16 a[74:75], v20 offset:18688\n"
"ds_read_b64_tr_b16 a[104:105], v21 offset:18432\n"
"ds_read_b64_tr_b16 a[106:107], v21 offset:18688\n"
"s_waitcnt lgkmcnt(4)\n"
"s_add_u32 m0, s55, 16384\n"
"s_add_u32 s58, s64, 65536\n"
"buffer_load_dwordx4 v25, s[44:47], s58 offen lds\n"
"s_add_u32 m0, s55, 17408\n"
"s_add_u32 s58, s64, 98304\n"
"buffer_load_dwordx4 v25, s[44:47], s58 offen lds\n"
"s_waitcnt vmcnt(36)\n"
"ds_read_b64_tr_b16 a[136:137], v20 offset:20480\n"
"ds_read_b64_tr_b16 a[138:139], v20 offset:20736\n"
"ds_read_b64_tr_b16 a[168:169], v21 offset:20480\n"
"ds_read_b64_tr_b16 a[170:171], v21 offset:20736\n"
"s_waitcnt lgkmcnt(4)\n"
"s_add_u32 m0, s55, 18432\n"
"s_add_u32 s58, s65, 65536\n"
"buffer_load_dwordx4 v25, s[44:47], s58 offen lds\n"
"s_add_u32 m0, s55, 19456\n"
"s_add_u32 s58, s65, 98304\n"
"buffer_load_dwordx4 v25, s[44:47], s58 offen lds\n"
"s_waitcnt vmcnt(36)\n"
"ds_read_b64_tr_b16 a[200:201], v20 offset:22528\n"
"ds_read_b64_tr_b16 a[202:203], v20 offset:22784\n"
"ds_read_b64_tr_b16 a[232:233], v21 offset:22528\n"
"ds_read_b64_tr_b16 a[234:235], v21 offset:22784\n"
"s_waitcnt lgkmcnt(4)\n"
"s_add_u32 m0, s55, 20480\n"
"s_add_u32 s58, s66, 65536\n"
"buffer_load_dwordx4 v25, s[44:47], s58 offen lds\n"
"s_add_u32 m0, s55, 21504\n"
"s_add_u32 s58, s66, 98304\n"
"buffer_load_dwordx4 v25, s[44:47], s58 offen lds\n"
"s_waitcnt vmcnt(36)\n"
"ds_read_b64_tr_b16 a[12:13], v20 offset:24576\n"
"ds_read_b64_tr_b16 a[14:15], v20 offset:24832\n"
"ds_read_b64_tr_b16 a[44:45], v21 offset:24576\n"
"ds_read_b64_tr_b16 a[46:47], v21 offset:24832\n"
"s_waitcnt lgkmcnt(4)\n"
"s_add_u32 m0, s55, 22528\n"
"s_add_u32 s58, s67, 65536\n"
"buffer_load_dwordx4 v25, s[44:47], s58 offen lds\n"
"s_add_u32 m0, s55, 23552\n"
"s_add_u32 s58, s67, 98304\n"
"buffer_load_dwordx4 v25, s[44:47], s58 offen lds\n"
"s_waitcnt vmcnt(36)\n"
"ds_read_b64_tr_b16 a[76:77], v20 offset:26624\n"
"ds_read_b64_tr_b16 a[78:79], v20 offset:26880\n"
"ds_read_b64_tr_b16 a[108:109], v21 offset:26624\n"
"ds_read_b64_tr_b16 a[110:111], v21 offset:26880\n"
"s_waitcnt lgkmcnt(4)\n"
"s_waitcnt vmcnt(34)\n"
"ds_read_b64_tr_b16 a[140:141], v20 offset:28672\n"
"ds_read_b64_tr_b16 a[142:143], v20 offset:28928\n"
"ds_read_b64_tr_b16 a[172:173], v21 offset:28672\n"
"ds_read_b64_tr_b16 a[174:175], v21 offset:28928\n"
"s_waitcnt lgkmcnt(4)\n"
"s_waitcnt vmcnt(32)\n"
"ds_read_b64_tr_b16 a[204:205], v20 offset:30720\n"
"ds_read_b64_tr_b16 a[206:207], v20 offset:30976\n"
"ds_read_b64_tr_b16 a[236:237], v21 offset:30720\n"
"ds_read_b64_tr_b16 a[238:239], v21 offset:30976\n"
"s_waitcnt lgkmcnt(4)\n"
"s_waitcnt vmcnt(30)\n"
"ds_read_b64_tr_b16 a[16:17], v20 offset:32768\n"
"ds_read_b64_tr_b16 a[18:19], v20 offset:33024\n"
"ds_read_b64_tr_b16 a[48:49], v21 offset:32768\n"
"ds_read_b64_tr_b16 a[50:51], v21 offset:33024\n"
"s_waitcnt lgkmcnt(4)\n"
"s_waitcnt vmcnt(28)\n"
"ds_read_b64_tr_b16 a[80:81], v20 offset:34816\n"
"ds_read_b64_tr_b16 a[82:83], v20 offset:35072\n"
"ds_read_b64_tr_b16 a[112:113], v21 offset:34816\n"
"ds_read_b64_tr_b16 a[114:115], v21 offset:35072\n"
"s_waitcnt lgkmcnt(4)\n"
"s_waitcnt vmcnt(26)\n"
"ds_read_b64_tr_b16 a[144:145], v20 offset:36864\n"
"ds_read_b64_tr_b16 a[146:147], v20 offset:37120\n"
"ds_read_b64_tr_b16 a[176:177], v21 offset:36864\n"
"ds_read_b64_tr_b16 a[178:179], v21 offset:37120\n"
"s_waitcnt lgkmcnt(4)\n"
"s_waitcnt vmcnt(24)\n"
"ds_read_b64_tr_b16 a[208:209], v20 offset:38912\n"
"ds_read_b64_tr_b16 a[210:211], v20 offset:39168\n"
"ds_read_b64_tr_b16 a[240:241], v21 offset:38912\n"
"ds_read_b64_tr_b16 a[242:243], v21 offset:39168\n"
"s_waitcnt lgkmcnt(4)\n"
"s_waitcnt vmcnt(22)\n"
"ds_read_b64_tr_b16 a[20:21], v20 offset:0\n"
"ds_read_b64_tr_b16 a[22:23], v20 offset:256\n"
"ds_read_b64_tr_b16 a[52:53], v21 offset:0\n"
"ds_read_b64_tr_b16 a[54:55], v21 offset:256\n"
"s_waitcnt lgkmcnt(4)\n"
"s_waitcnt vmcnt(20)\n"
"ds_read_b64_tr_b16 a[84:85], v20 offset:2048\n"
"ds_read_b64_tr_b16 a[86:87], v20 offset:2304\n"
"ds_read_b64_tr_b16 a[116:117], v21 offset:2048\n"
"ds_read_b64_tr_b16 a[118:119], v21 offset:2304\n"
"s_waitcnt lgkmcnt(4)\n"
"s_waitcnt vmcnt(18)\n"
"ds_read_b64_tr_b16 a[148:149], v20 offset:4096\n"
"ds_read_b64_tr_b16 a[150:151], v20 offset:4352\n"
"ds_read_b64_tr_b16 a[180:181], v21 offset:4096\n"
"ds_read_b64_tr_b16 a[182:183], v21 offset:4352\n"
"s_waitcnt lgkmcnt(4)\n"
"s_waitcnt vmcnt(16)\n"
"ds_read_b64_tr_b16 a[212:213], v20 offset:6144\n"
"ds_read_b64_tr_b16 a[214:215], v20 offset:6400\n"
"ds_read_b64_tr_b16 a[244:245], v21 offset:6144\n"
"ds_read_b64_tr_b16 a[246:247], v21 offset:6400\n"
"s_waitcnt lgkmcnt(4)\n"
"s_waitcnt vmcnt(14)\n"
"ds_read_b64_tr_b16 a[24:25], v20 offset:8192\n"
"ds_read_b64_tr_b16 a[26:27], v20 offset:8448\n"
"ds_read_b64_tr_b16 a[56:57], v21 offset:8192\n"
"ds_read_b64_tr_b16 a[58:59], v21 offset:8448\n"
"s_waitcnt lgkmcnt(4)\n"
"s_waitcnt vmcnt(12)\n"
"ds_read_b64_tr_b16 a[88:89], v20 offset:10240\n"
"ds_read_b64_tr_b16 a[90:91], v20 offset:10496\n"
"ds_read_b64_tr_b16 a[120:121], v21 offset:10240\n"
"ds_read_b64_tr_b16 a[122:123], v21 offset:10496\n"
"s_waitcnt lgkmcnt(4)\n"
"s_waitcnt vmcnt(10)\n"
"ds_read_b64_tr_b16 a[152:153], v20 offset:12288\n"
"ds_read_b64_tr_b16 a[154:155], v20 offset:12544\n"
"ds_read_b64_tr_b16 a[184:185], v21 offset:12288\n"
"ds_read_b64_tr_b16 a[186:187], v21 offset:12544\n"
"s_waitcnt lgkmcnt(4)\n"
"s_waitcnt vmcnt(8)\n"
"ds_read_b64_tr_b16 a[216:217], v20 offset:14336\n"
"ds_read_b64_tr_b16 a[218:219], v20 offset:14592\n"
"ds_read_b64_tr_b16 a[248:249], v21 offset:14336\n"
"ds_read_b64_tr_b16 a[250:251], v21 offset:14592\n"
"s_waitcnt lgkmcnt(4)\n"
"s_waitcnt vmcnt(6)\n"
"ds_read_b64_tr_b16 a[28:29], v20 offset:16384\n"
"ds_read_b64_tr_b16 a[30:31], v20 offset:16640\n"
"ds_read_b64_tr_b16 a[60:61], v21 offset:16384\n"
"ds_read_b64_tr_b16 a[62:63], v21 offset:16640\n"
"s_waitcnt lgkmcnt(4)\n"
"s_waitcnt vmcnt(4)\n"
"ds_read_b64_tr_b16 a[92:93], v20 offset:18432\n"
"ds_read_b64_tr_b16 a[94:95], v20 offset:18688\n"
"ds_read_b64_tr_b16 a[124:125], v21 offset:18432\n"
"ds_read_b64_tr_b16 a[126:127], v21 offset:18688\n"
"s_waitcnt lgkmcnt(4)\n"
"s_waitcnt vmcnt(2)\n"
"ds_read_b64_tr_b16 a[156:157], v20 offset:20480\n"
"ds_read_b64_tr_b16 a[158:159], v20 offset:20736\n"
"ds_read_b64_tr_b16 a[188:189], v21 offset:20480\n"
"ds_read_b64_tr_b16 a[190:191], v21 offset:20736\n"
"s_waitcnt lgkmcnt(4)\n"
"s_waitcnt vmcnt(0)\n"
"ds_read_b64_tr_b16 a[220:221], v20 offset:22528\n"
"ds_read_b64_tr_b16 a[222:223], v20 offset:22784\n"
"ds_read_b64_tr_b16 a[252:253], v21 offset:22528\n"
"ds_read_b64_tr_b16 a[254:255], v21 offset:22784\n"
"s_waitcnt lgkmcnt(4)\n"
"s_waitcnt lgkmcnt(0)\n"
"s_mov_b32 m0, s57\n"
"s_barrier\n"
"v_mov_b32 v150, 0\n"
"v_mov_b32 v151, 0\n"
"v_mov_b32 v132, 0\n"
"v_mov_b32 v133, 0\n"
"v_mov_b32 v150, 0\n"
"v_mov_b32 v151, 0\n"
"v_mov_b32 v148, 0\n"
"v_mov_b32 v149, 0\n"
"s_mov_b32 s93, 0x80000000\n"
"s_mov_b32 s94, 0x80000000\n"
"s_mov_b32 s94, 0x80000000\n"
"s_mov_b32 s95, 0x80000000\n"
"s_mov_b32 s93, 0x80000000\n"
"s_mov_b32 s95, 0x80000000\n"
"s_mov_b32 s95, 0x80000000\n"
"s_mov_b32 s94, 0x80000000\n"
"s_mov_b32 s92, 0x80000000\n"
"s_mov_b32 s94, 0x80000000\n"
"s_mov_b32 s92, 0x80000000\n"
"s_mov_b32 s92, 0x80000000\n"
"s_cmp_eq_u32 s54, 0\n"
"s_cbranch_scc1 .LO_1s\n"
"s_cmp_eq_u32 s54, 1\n"
"s_cbranch_scc1 .LO_2s\n"
"s_cmp_eq_u32 s54, 2\n"
"s_cbranch_scc1 .LO_3s\n"
"s_cmp_eq_u32 s54, 3\n"
"s_cbranch_scc1 .LO_4s\n"
".p2align 6\n"
".LO_top:\n"
"v_mfma_f32_16x16x32_bf16 v[20:23], v[84:87], a[0:3], 0\n"
"v_mfma_f32_16x16x32_bf16 v[20:23], v[88:91], a[4:7], v[20:23]\n"
"s_waitcnt vmcnt(17)\n"
"v_mfma_f32_16x16x32_bf16 v[20:23], v[92:95], a[8:11], v[20:23]\n"
"s_waitcnt lgkmcnt(3)\n"
"v_add_f32 v44, v44, v24\n"
"v_add_f32 v45, v45, v25\n"
"v_mfma_f32_16x16x32_bf16 v[20:23], v[96:99], a[12:15], v[20:23]\n"
"s_waitcnt vmcnt(15)\n"
"v_add_f32 v46, v46, v26\n"
"v_add_f32 v47, v47, v27\n"
"v_mfma_f32_16x16x32_bf16 v[20:23], v[100:103], a[16:19], v[20:23]\n"
"v_add_f32 v44, v44, v28\n"
"v_add_f32 v45, v45, v29\n"
"v_mfma_f32_16x16x32_bf16 v[20:23], v[104:107], a[20:23], v[20:23]\n"
"s_waitcnt vmcnt(13)\n"
"v_add_f32 v46, v46, v30\n"
"v_add_f32 v47, v47, v31\n"
"v_mfma_f32_16x16x32_bf16 v[20:23], v[108:111], a[24:27], v[20:23]\n"
"v_add_f32 v44, v44, v32\n"
"v_add_f32 v45, v45, v33\n"
"v_mfma_f32_16x16x32_bf16 v[20:23], v[112:115], a[28:31], v[20:23]\n"
"v_add_f32 v46, v46, v34\n"
"v_add_f32 v47, v47, v35\n"
"v_mfma_f32_16x16x32_bf16 v[24:27], v[84:87], a[32:35], 0\n"
"s_add_u32 s95, s50, 0xffd00000\n"
"s_and_b32 s94, s95, 0xfbffffff\n"
"v_cvt_pk_bf16_f32 v28, v44, v45\n"
"v_mfma_f32_16x16x32_bf16 v[24:27], v[88:91], a[36:39], v[24:27]\n"
"ds_write_addtid_b32 v20 offset:0\n"
"v_cvt_pk_bf16_f32 v29, v46, v47\n"
"v_mfma_f32_16x16x32_bf16 v[24:27], v[92:95], a[40:43], v[24:27]\n"
"s_waitcnt lgkmcnt(1)\n"
"ds_write_addtid_b32 v21 offset:4640\n"
"v_add_f32 v48, v48, v52\n"
"v_mfma_f32_16x16x32_bf16 v[24:27], v[96:99], a[44:47], v[24:27]\n"
"ds_write_addtid_b32 v22 offset:9344\n"
"v_add_f32 v49, v49, v53\n"
"v_mfma_f32_16x16x32_bf16 v[24:27], v[100:103], a[48:51], v[24:27]\n"
"ds_write_addtid_b32 v23 offset:13984\n"
"v_add_f32 v50, v50, v54\n"
"v_mfma_f32_16x16x32_bf16 v[24:27], v[104:107], a[52:55], v[24:27]\n"
"v_add_f32 v51, v51, v55\n"
"v_add_f32 v48, v48, v56\n"
"v_mfma_f32_16x16x32_bf16 v[24:27], v[108:111], a[56:59], v[24:27]\n"
"v_add_f32 v49, v49, v57\n"
"v_add_f32 v50, v50, v58\n"
"v_mfma_f32_16x16x32_bf16 v[24:27], v[112:115], a[60:63], v[24:27]\n"
"v_add_f32 v51, v51, v59\n"
"v_add_f32 v48, v48, v36\n"
"v_mfma_f32_16x16x32_bf16 v[20:23], v[84:87], a[64:67], 0\n"
"v_add_f32 v49, v49, v37\n"
"v_add_f32 v50, v50, v38\n"
"v_mfma_f32_16x16x32_bf16 v[20:23], v[88:91], a[68:71], v[20:23]\n"
"ds_write_addtid_b32 v24 offset:256\n"
"v_add_f32 v51, v51, v39\n"
"v_mfma_f32_16x16x32_bf16 v[20:23], v[92:95], a[72:75], v[20:23]\n"
"ds_write_addtid_b32 v25 offset:4896\n"
"v_cvt_pk_bf16_f32 v30, v48, v49\n"
"v_mfma_f32_16x16x32_bf16 v[20:23], v[96:99], a[76:79], v[20:23]\n"
"ds_write_addtid_b32 v26 offset:9600\n"
"v_cvt_pk_bf16_f32 v31, v50, v51\n"
"v_mfma_f32_16x16x32_bf16 v[20:23], v[100:103], a[80:83], v[20:23]\n"
"buffer_store_dwordx4 v[28:31], v11, s[40:43], s94 offen sc1\n"
"ds_write_addtid_b32 v27 offset:14240\n"
"v_mfma_f32_16x16x32_bf16 v[32:35], v[28:31], v[28:31], 0\n"
"v_mfma_f32_16x16x32_bf16 v[36:39], v[14:17], v[28:31], 0\n"
"s_waitcnt vmcnt(12)\n"
"v_lshlrev_b32 v52, 16, v40\n"
"v_and_b32 v53, 0xffff0000, v40\n"
"v_mfma_f32_16x16x32_bf16 v[20:23], v[104:107], a[84:87], v[20:23]\n"
"v_cndmask_b32_e64 v32, v32, v33, s[60:61]\n"
"v_cndmask_b32_e64 v34, v34, v35, s[60:61]\n"
"v_mfma_f32_16x16x32_bf16 v[20:23], v[108:111], a[88:91], v[20:23]\n"
"v_cndmask_b32_e64 v37, v32, v34, s[62:63]\n"
"v_lshlrev_b32 v54, 16, v41\n"
"v_mfma_f32_16x16x32_bf16 v[20:23], v[112:115], a[92:95], v[20:23]\n"
"s_waitcnt vmcnt(11)\n"
"buffer_store_dwordx2 v[0:1], v12, s[40:43], s93 offen offset:0 sc1\n"
"v_and_b32 v55, 0xffff0000, v41\n"
"v_lshlrev_b32 v56, 16, v42\n"
"v_mfma_f32_16x16x32_bf16 v[28:31], v[84:87], a[96:99], 0\n"
"buffer_store_dwordx2 v[36:37], v13, s[40:43], s94 offen offset:0 sc1\n"
"v_and_b32 v57, 0xffff0000, v42\n"
"v_lshlrev_b32 v58, 16, v43\n"
"v_mfma_f32_16x16x32_bf16 v[28:31], v[88:91], a[100:103], v[28:31]\n"
"ds_write_addtid_b32 v20 offset:512\n"
"v_and_b32 v59, 0xffff0000, v43\n"
"v_mfma_f32_16x16x32_bf16 v[28:31], v[92:95], a[104:107], v[28:31]\n"
"s_add_u32 s94, s50, 0x0\n"
"s_and_b32 s94, s94, 0xfbffffff\n"
"ds_write_addtid_b32 v21 offset:5152\n"
"v_mfma_f32_16x16x32_bf16 v[28:31], v[96:99], a[108:111], v[28:31]\n"
"ds_write_addtid_b32 v22 offset:9856\n"
"buffer_load_dwordx2 v[148:149], v9, s[40:43], s50 offen sc1\n"
"v_mfma_f32_16x16x32_bf16 v[28:31], v[100:103], a[112:115], v[28:31]\n"
"s_waitcnt vmcnt(12)\n"
"ds_write_addtid_b32 v23 offset:14496\n"
"v_cmp_ne_u64 vcc, s[48:49], v[150:151]\n"
"v_mfma_f32_16x16x32_bf16 v[28:31], v[104:107], a[116:119], v[28:31]\n"
"buffer_load_dwordx4 v[60:63], v10, s[40:43], s94 offen sc1\n"
"v_mfma_f32_16x16x32_bf16 v[28:31], v[108:111], a[120:123], v[28:31]\n"
"s_cbranch_vccnz .LO_3s\n"
".LO_3r:\n"
"v_mfma_f32_16x16x32_bf16 v[28:31], v[112:115], a[124:127], v[28:31]\n"
"s_add_u32 s50, s50, 0x180000\n"
"s_and_b32 s56, s50, 0xfbffffff\n"
"v_mfma_f32_16x16x32_bf16 v[36:39], v[84:87], a[128:131], 0\n"
"buffer_load_dwordx4 v[20:23], v5, s[40:43], s56 offen offset:0 sc1\n"
"v_mfma_f32_16x16x32_bf16 v[36:39], v[88:91], a[132:135], v[36:39]\n"
"ds_write_addtid_b32 v28 offset:768\n"
"v_mfma_f32_16x16x32_bf16 v[36:39], v[92:95], a[136:139], v[36:39]\n"
"ds_write_addtid_b32 v29 offset:5408\n"
"v_mfma_f32_16x16x32_bf16 v[36:39], v[96:99], a[140:143], v[36:39]\n"
"ds_write_addtid_b32 v30 offset:10112\n"
"v_mfma_f32_16x16x32_bf16 v[36:39], v[100:103], a[144:147], v[36:39]\n"
"ds_write_addtid_b32 v31 offset:14752\n"
"buffer_load_dwordx4 v[24:27], v5, s[40:43], s56 offen offset:1024 sc1\n"
"v_mfma_f32_16x16x32_bf16 v[36:39], v[104:107], a[148:151], v[36:39]\n"
"v_mfma_f32_16x16x32_bf16 v[36:39], v[108:111], a[152:155], v[36:39]\n"
"v_mfma_f32_16x16x32_bf16 v[36:39], v[112:115], a[156:159], v[36:39]\n"
"v_mfma_f32_16x16x32_bf16 v[44:47], v[84:87], a[160:163], 0\n"
"buffer_load_dwordx4 v[28:31], v6, s[40:43], s56 offen offset:0 sc1\n"
"v_mfma_f32_16x16x32_bf16 v[44:47], v[88:91], a[164:167], v[44:47]\n"
"ds_write_addtid_b32 v36 offset:1024\n"
"v_mfma_f32_16x16x32_bf16 v[44:47], v[92:95], a[168:171], v[44:47]\n"
"ds_write_addtid_b32 v37 offset:5664\n"
"v_mfma_f32_16x16x32_bf16 v[44:47], v[96:99], a[172:175], v[44:47]\n"
"ds_write_addtid_b32 v38 offset:10368\n"
"v_mfma_f32_16x16x32_bf16 v[44:47], v[100:103], a[176:179], v[44:47]\n"
"ds_write_addtid_b32 v39 offset:15008\n"
"buffer_load_dwordx4 v[32:35], v6, s[40:43], s56 offen offset:1024 sc1\n"
"v_mfma_f32_16x16x32_bf16 v[44:47], v[104:107], a[180:183], v[44:47]\n"
"v_mfma_f32_16x16x32_bf16 v[44:47], v[108:111], a[184:187], v[44:47]\n"
"v_mfma_f32_16x16x32_bf16 v[44:47], v[112:115], a[188:191], v[44:47]\n"
"v_mfma_f32_16x16x32_bf16 v[52:55], a[192:195], v[84:87], v[52:55]\n"
"buffer_load_dwordx4 v[36:39], v7, s[40:43], s56 offen offset:0 sc1\n"
"v_mfma_f32_16x16x32_bf16 v[52:55], a[196:199], v[88:91], v[52:55]\n"
"ds_write_addtid_b32 v44 offset:1280\n"
"v_mfma_f32_16x16x32_bf16 v[52:55], a[200:203], v[92:95], v[52:55]\n"
"ds_write_addtid_b32 v45 offset:5920\n"
"v_mfma_f32_16x16x32_bf16 v[52:55], a[204:207], v[96:99], v[52:55]\n"
"ds_write_addtid_b32 v46 offset:10624\n"
"v_mfma_f32_16x16x32_bf16 v[52:55], a[208:211], v[100:103], v[52:55]\n"
"ds_write_addtid_b32 v47 offset:15264\n"
"buffer_load_dwordx4 v[40:43], v7, s[40:43], s56 offen offset:1024 sc1\n"
"v_mfma_f32_16x16x32_bf16 v[52:55], a[212:215], v[104:107], v[52:55]\n"
"v_mfma_f32_16x16x32_bf16 v[52:55], a[216:219], v[108:111], v[52:55]\n"
"v_mfma_f32_16x16x32_bf16 v[52:55], a[220:223], v[112:115], v[52:55]\n"
"v_mfma_f32_16x16x32_bf16 v[56:59], a[224:227], v[84:87], v[56:59]\n"
"s_waitcnt lgkmcnt(0)\n"
"buffer_load_dwordx4 v[44:47], v8, s[40:43], s56 offen offset:0 sc1\n"
"v_mfma_f32_16x16x32_bf16 v[56:59], a[228:231], v[88:91], v[56:59]\n"
"s_barrier\n"
"v_mfma_f32_16x16x32_bf16 v[56:59], a[232:235], v[92:95], v[56:59]\n"
"ds_read_b128 v[64:67], v2 offset:0\n"
"ds_read_b128 v[68:71], v3 offset:512\n"
"v_mfma_f32_16x16x32_bf16 v[56:59], a[236:239], v[96:99], v[56:59]\n"
"ds_read_b128 v[72:75], v4 offset:1024\n"
"ds_read_b128 v[76:79], v2 offset:256\n"
"v_mfma_f32_16x16x32_bf16 v[56:59], a[240:243], v[100:103], v[56:59]\n"
"buffer_load_dwordx4 v[48:51], v8, s[40:43], s56 offen offset:1024 sc1\n"
"ds_read_b128 v[80:83], v3 offset:768\n"
"v_mfma_f32_16x16x32_bf16 v[56:59], a[244:247], v[104:107], v[56:59]\n"
"ds_read_b128 v[84:87], v4 offset:1280\n"
"v_mfma_f32_16x16x32_bf16 v[56:59], a[248:251], v[108:111], v[56:59]\n"
"v_mfma_f32_16x16x32_bf16 v[56:59], a[252:255], v[112:115], v[56:59]\n"
"s_waitcnt vmcnt(19)\n"
"v_mfma_f32_16x16x32_bf16 v[88:91], v[116:119], a[0:3], 0\n"
"v_mfma_f32_16x16x32_bf16 v[88:91], v[120:123], a[4:7], v[88:91]\n"
"s_waitcnt vmcnt(17)\n"
"v_mfma_f32_16x16x32_bf16 v[88:91], v[124:127], a[8:11], v[88:91]\n"
"s_waitcnt lgkmcnt(3)\n"
"v_add_f32 v64, v64, v68\n"
"v_add_f32 v65, v65, v69\n"
"v_mfma_f32_16x16x32_bf16 v[88:91], v[128:131], a[12:15], v[88:91]\n"
"s_waitcnt vmcnt(15)\n"
"v_add_f32 v66, v66, v70\n"
"v_add_f32 v67, v67, v71\n"
"v_mfma_f32_16x16x32_bf16 v[88:91], v[132:135], a[16:19], v[88:91]\n"
"v_add_f32 v64, v64, v72\n"
"v_add_f32 v65, v65, v73\n"
"v_mfma_f32_16x16x32_bf16 v[88:91], v[136:139], a[20:23], v[88:91]\n"
"s_waitcnt vmcnt(13)\n"
"v_add_f32 v66, v66, v74\n"
"v_add_f32 v67, v67, v75\n"
"v_mfma_f32_16x16x32_bf16 v[88:91], v[140:143], a[24:27], v[88:91]\n"
"v_add_f32 v64, v64, v52\n"
"v_add_f32 v65, v65, v53\n"
"v_mfma_f32_16x16x32_bf16 v[88:91], v[144:147], a[28:31], v[88:91]\n"
"v_add_f32 v66, v66, v54\n"
"v_add_f32 v67, v67, v55\n"
"v_mfma_f32_16x16x32_bf16 v[68:71], v[116:119], a[32:35], 0\n"
"s_add_u32 s93, s50, 0xffd00000\n"
"s_and_b32 s94, s93, 0xfbffffff\n"
"v_cvt_pk_bf16_f32 v72, v64, v65\n"
"v_mfma_f32_16x16x32_bf16 v[68:71], v[120:123], a[36:39], v[68:71]\n"
"ds_write_addtid_b32 v88 offset:1536\n"
"v_cvt_pk_bf16_f32 v73, v66, v67\n"
"v_mfma_f32_16x16x32_bf16 v[68:71], v[124:127], a[40:43], v[68:71]\n"
"s_waitcnt lgkmcnt(1)\n"
"ds_write_addtid_b32 v89 offset:6176\n"
"v_add_f32 v76, v76, v80\n"
"v_mfma_f32_16x16x32_bf16 v[68:71], v[128:131], a[44:47], v[68:71]\n"
"ds_write_addtid_b32 v90 offset:10880\n"
"v_add_f32 v77, v77, v81\n"
"v_mfma_f32_16x16x32_bf16 v[68:71], v[132:135], a[48:51], v[68:71]\n"
"ds_write_addtid_b32 v91 offset:15520\n"
"v_add_f32 v78, v78, v82\n"
"v_mfma_f32_16x16x32_bf16 v[68:71], v[136:139], a[52:55], v[68:71]\n"
"v_add_f32 v79, v79, v83\n"
"v_add_f32 v76, v76, v84\n"
"v_mfma_f32_16x16x32_bf16 v[68:71], v[140:143], a[56:59], v[68:71]\n"
"v_add_f32 v77, v77, v85\n"
"v_add_f32 v78, v78, v86\n"
"v_mfma_f32_16x16x32_bf16 v[68:71], v[144:147], a[60:63], v[68:71]\n"
"v_add_f32 v79, v79, v87\n"
"v_add_f32 v76, v76, v56\n"
"v_mfma_f32_16x16x32_bf16 v[52:55], v[116:119], a[64:67], 0\n"
"v_add_f32 v77, v77, v57\n"
"v_add_f32 v78, v78, v58\n"
"v_mfma_f32_16x16x32_bf16 v[52:55], v[120:123], a[68:71], v[52:55]\n"
"ds_write_addtid_b32 v68 offset:1792\n"
"v_add_f32 v79, v79, v59\n"
"v_mfma_f32_16x16x32_bf16 v[52:55], v[124:127], a[72:75], v[52:55]\n"
"ds_write_addtid_b32 v69 offset:6432\n"
"v_cvt_pk_bf16_f32 v74, v76, v77\n"
"v_mfma_f32_16x16x32_bf16 v[52:55], v[128:131], a[76:79], v[52:55]\n"
"ds_write_addtid_b32 v70 offset:11136\n"
"v_cvt_pk_bf16_f32 v75, v78, v79\n"
"v_mfma_f32_16x16x32_bf16 v[52:55], v[132:135], a[80:83], v[52:55]\n"
"buffer_store_dwordx4 v[72:75], v11, s[40:43], s94 offen sc1\n"
"ds_write_addtid_b32 v71 offset:15776\n"
"v_mfma_f32_16x16x32_bf16 v[56:59], v[72:75], v[72:75], 0\n"
"v_mfma_f32_16x16x32_bf16 v[64:67], v[14:17], v[72:75], 0\n"
"s_add_u32 s92, s50, 0x0\n"
"s_and_b32 s92, s92, 0xfbffffff\n"
"v_mfma_f32_16x16x32_bf16 v[52:55], v[136:139], a[84:87], v[52:55]\n"
"v_cndmask_b32_e64 v56, v56, v57, s[60:61]\n"
"v_cndmask_b32_e64 v58, v58, v59, s[60:61]\n"
"v_mfma_f32_16x16x32_bf16 v[52:55], v[140:143], a[88:91], v[52:55]\n"
"s_waitcnt vmcnt(11)\n"
"buffer_store_dwordx2 v[0:1], v12, s[40:43], s95 offen offset:0 sc1\n"
"v_cndmask_b32_e64 v65, v56, v58, s[62:63]\n"
"v_mfma_f32_16x16x32_bf16 v[52:55], v[144:147], a[92:95], v[52:55]\n"
"buffer_store_dwordx2 v[64:65], v13, s[40:43], s94 offen offset:0 sc1\n"
"v_mfma_f32_16x16x32_bf16 v[68:71], v[116:119], a[96:99], 0\n"
"buffer_load_dwordx4 v[84:87], v10, s[40:43], s92 offen sc1\n"
"v_mfma_f32_16x16x32_bf16 v[68:71], v[120:123], a[100:103], v[68:71]\n"
"ds_write_addtid_b32 v52 offset:2048\n"
"v_mfma_f32_16x16x32_bf16 v[68:71], v[124:127], a[104:107], v[68:71]\n"
"ds_write_addtid_b32 v53 offset:6688\n"
"v_mfma_f32_16x16x32_bf16 v[68:71], v[128:131], a[108:111], v[68:71]\n"
"s_waitcnt vmcnt(13)\n"
"ds_write_addtid_b32 v54 offset:11392\n"
"v_cmp_ne_u64 vcc, s[48:49], v[148:149]\n"
"v_mfma_f32_16x16x32_bf16 v[68:71], v[132:135], a[112:115], v[68:71]\n"
"s_waitcnt vmcnt(12)\n"
"ds_write_addtid_b32 v55 offset:16032\n"
"buffer_load_dwordx2 v[150:151], v9, s[40:43], s50 offen sc1\n"
"v_mfma_f32_16x16x32_bf16 v[68:71], v[136:139], a[116:119], v[68:71]\n"
"s_cbranch_vccnz .LO_4s\n"
".LO_4r:\n"
"v_lshlrev_b32 v88, 16, v60\n"
"v_mfma_f32_16x16x32_bf16 v[68:71], v[140:143], a[120:123], v[68:71]\n"
"v_and_b32 v89, 0xffff0000, v60\n"
"v_lshlrev_b32 v90, 16, v61\n"
"v_mfma_f32_16x16x32_bf16 v[68:71], v[144:147], a[124:127], v[68:71]\n"
"s_add_u32 s50, s50, 0x180000\n"
"s_and_b32 s56, s50, 0xfbffffff\n"
"v_and_b32 v91, 0xffff0000, v61\n"
"v_mfma_f32_16x16x32_bf16 v[72:75], v[116:119], a[128:131], 0\n"
"buffer_load_dwordx4 v[52:55], v5, s[40:43], s56 offen offset:0 sc1\n"
"v_lshlrev_b32 v92, 16, v62\n"
"v_mfma_f32_16x16x32_bf16 v[72:75], v[120:123], a[132:135], v[72:75]\n"
"ds_write_addtid_b32 v68 offset:2304\n"
"v_and_b32 v93, 0xffff0000, v62\n"
"v_mfma_f32_16x16x32_bf16 v[72:75], v[124:127], a[136:139], v[72:75]\n"
"ds_write_addtid_b32 v69 offset:6944\n"
"v_lshlrev_b32 v94, 16, v63\n"
"v_mfma_f32_16x16x32_bf16 v[72:75], v[128:131], a[140:143], v[72:75]\n"
"ds_write_addtid_b32 v70 offset:11648\n"
"v_and_b32 v95, 0xffff0000, v63\n"
"v_mfma_f32_16x16x32_bf16 v[72:75], v[132:135], a[144:147], v[72:75]\n"
"ds_write_addtid_b32 v71 offset:16288\n"
"buffer_load_dwordx4 v[56:59], v5, s[40:43], s56 offen offset:1024 sc1\n"
"v_mfma_f32_16x16x32_bf16 v[72:75], v[136:139], a[148:151], v[72:75]\n"
"v_mfma_f32_16x16x32_bf16 v[72:75], v[140:143], a[152:155], v[72:75]\n"
"v_mfma_f32_16x16x32_bf16 v[72:75], v[144:147], a[156:159], v[72:75]\n"
"v_mfma_f32_16x16x32_bf16 v[76:79], v[116:119], a[160:163], 0\n"
"buffer_load_dwordx4 v[60:63], v6, s[40:43], s56 offen offset:0 sc1\n"
"v_mfma_f32_16x16x32_bf16 v[76:79], v[120:123], a[164:167], v[76:79]\n"
"ds_write_addtid_b32 v72 offset:2560\n"
"v_mfma_f32_16x16x32_bf16 v[76:79], v[124:127], a[168:171], v[76:79]\n"
"ds_write_addtid_b32 v73 offset:7200\n"
"v_mfma_f32_16x16x32_bf16 v[76:79], v[128:131], a[172:175], v[76:79]\n"
"ds_write_addtid_b32 v74 offset:11904\n"
"v_mfma_f32_16x16x32_bf16 v[76:79], v[132:135], a[176:179], v[76:79]\n"
"ds_write_addtid_b32 v75 offset:16544\n"
"buffer_load_dwordx4 v[64:67], v6, s[40:43], s56 offen offset:1024 sc1\n"
"v_mfma_f32_16x16x32_bf16 v[76:79], v[136:139], a[180:183], v[76:79]\n"
"v_mfma_f32_16x16x32_bf16 v[76:79], v[140:143], a[184:187], v[76:79]\n"
"v_mfma_f32_16x16x32_bf16 v[76:79], v[144:147], a[188:191], v[76:79]\n"
"v_mfma_f32_16x16x32_bf16 v[88:91], a[192:195], v[116:119], v[88:91]\n"
"buffer_load_dwordx4 v[68:71], v7, s[40:43], s56 offen offset:0 sc1\n"
"v_mfma_f32_16x16x32_bf16 v[88:91], a[196:199], v[120:123], v[88:91]\n"
"ds_write_addtid_b32 v76 offset:2816\n"
"v_mfma_f32_16x16x32_bf16 v[88:91], a[200:203], v[124:127], v[88:91]\n"
"ds_write_addtid_b32 v77 offset:7456\n"
"v_mfma_f32_16x16x32_bf16 v[88:91], a[204:207], v[128:131], v[88:91]\n"
"ds_write_addtid_b32 v78 offset:12160\n"
"v_mfma_f32_16x16x32_bf16 v[88:91], a[208:211], v[132:135], v[88:91]\n"
"ds_write_addtid_b32 v79 offset:16800\n"
"buffer_load_dwordx4 v[72:75], v7, s[40:43], s56 offen offset:1024 sc1\n"
"v_mfma_f32_16x16x32_bf16 v[88:91], a[212:215], v[136:139], v[88:91]\n"
"v_mfma_f32_16x16x32_bf16 v[88:91], a[216:219], v[140:143], v[88:91]\n"
"v_mfma_f32_16x16x32_bf16 v[88:91], a[220:223], v[144:147], v[88:91]\n"
"v_mfma_f32_16x16x32_bf16 v[92:95], a[224:227], v[116:119], v[92:95]\n"
"s_waitcnt lgkmcnt(0)\n"
"buffer_load_dwordx4 v[76:79], v8, s[40:43], s56 offen offset:0 sc1\n"
"v_mfma_f32_16x16x32_bf16 v[92:95], a[228:231], v[120:123], v[92:95]\n"
"s_barrier\n"
"v_mfma_f32_16x16x32_bf16 v[92:95], a[232:235], v[124:127], v[92:95]\n"
"ds_read_b128 v[96:99], v2 offset:1536\n"
"ds_read_b128 v[100:103], v3 offset:2048\n"
"v_mfma_f32_16x16x32_bf16 v[92:95], a[236:239], v[128:131], v[92:95]\n"
"ds_read_b128 v[104:107], v4 offset:2560\n"
"ds_read_b128 v[108:111], v2 offset:1792\n"
"v_mfma_f32_16x16x32_bf16 v[92:95], a[240:243], v[132:135], v[92:95]\n"
"buffer_load_dwordx4 v[80:83], v8, s[40:43], s56 offen offset:1024 sc1\n"
"ds_read_b128 v[112:115], v3 offset:2304\n"
"v_mfma_f32_16x16x32_bf16 v[92:95], a[244:247], v[136:139], v[92:95]\n"
"ds_read_b128 v[116:119], v4 offset:2816\n"
"v_mfma_f32_16x16x32_bf16 v[92:95], a[248:251], v[140:143], v[92:95]\n"
"v_mfma_f32_16x16x32_bf16 v[92:95], a[252:255], v[144:147], v[92:95]\n"
"s_waitcnt vmcnt(19)\n"
"v_mfma_f32_16x16x32_bf16 v[120:123], v[20:23], a[0:3], 0\n"
"v_mfma_f32_16x16x32_bf16 v[120:123], v[24:27], a[4:7], v[120:123]\n"
"s_waitcnt vmcnt(17)\n"
"v_mfma_f32_16x16x32_bf16 v[120:123], v[28:31], a[8:11], v[120:123]\n"
"s_waitcnt lgkmcnt(3)\n"
"v_add_f32 v96, v96, v100\n"
"v_add_f32 v97, v97, v101\n"
"v_mfma_f32_16x16x32_bf16 v[120:123], v[32:35], a[12:15], v[120:123]\n"
"s_waitcnt vmcnt(15)\n"
"v_add_f32 v98, v98, v102\n"
"v_add_f32 v99, v99, v103\n"
"v_mfma_f32_16x16x32_bf16 v[120:123], v[36:39], a[16:19], v[120:123]\n"
"v_add_f32 v96, v96, v104\n"
"v_add_f32 v97, v97, v105\n"
"v_mfma_f32_16x16x32_bf16 v[120:123], v[40:43], a[20:23], v[120:123]\n"
"s_waitcnt vmcnt(13)\n"
"v_add_f32 v98, v98, v106\n"
"v_add_f32 v99, v99, v107\n"
"v_mfma_f32_16x16x32_bf16 v[120:123], v[44:47], a[24:27], v[120:123]\n"
"v_add_f32 v96, v96, v88\n"
"v_add_f32 v97, v97, v89\n"
"v_mfma_f32_16x16x32_bf16 v[120:123], v[48:51], a[28:31], v[120:123]\n"
"v_add_f32 v98, v98, v90\n"
"v_add_f32 v99, v99, v91\n"
"v_mfma_f32_16x16x32_bf16 v[100:103], v[20:23], a[32:35], 0\n"
"s_add_u32 s94, s50, 0xffd00000\n"
"s_and_b32 s95, s94, 0xfbffffff\n"
"v_cvt_pk_bf16_f32 v104, v96, v97\n"
"v_mfma_f32_16x16x32_bf16 v[100:103], v[24:27], a[36:39], v[100:103]\n"
"ds_write_addtid_b32 v120 offset:0\n"
"v_cvt_pk_bf16_f32 v105, v98, v99\n"
"v_mfma_f32_16x16x32_bf16 v[100:103], v[28:31], a[40:43], v[100:103]\n"
"s_waitcnt lgkmcnt(1)\n"
"ds_write_addtid_b32 v121 offset:4640\n"
"v_add_f32 v108, v108, v112\n"
"v_mfma_f32_16x16x32_bf16 v[100:103], v[32:35], a[44:47], v[100:103]\n"
"ds_write_addtid_b32 v122 offset:9344\n"
"v_add_f32 v109, v109, v113\n"
"v_mfma_f32_16x16x32_bf16 v[100:103], v[36:39], a[48:51], v[100:103]\n"
"ds_write_addtid_b32 v123 offset:13984\n"
"v_add_f32 v110, v110, v114\n"
"v_mfma_f32_16x16x32_bf16 v[100:103], v[40:43], a[52:55], v[100:103]\n"
"v_add_f32 v111, v111, v115\n"
"v_add_f32 v108, v108, v116\n"
"v_mfma_f32_16x16x32_bf16 v[100:103], v[44:47], a[56:59], v[100:103]\n"
"v_add_f32 v109, v109, v117\n"
"v_add_f32 v110, v110, v118\n"
"v_mfma_f32_16x16x32_bf16 v[100:103], v[48:51], a[60:63], v[100:103]\n"
"v_add_f32 v111, v111, v119\n"
"v_add_f32 v108, v108, v92\n"
"v_mfma_f32_16x16x32_bf16 v[88:91], v[20:23], a[64:67], 0\n"
"v_add_f32 v109, v109, v93\n"
"v_add_f32 v110, v110, v94\n"
"v_mfma_f32_16x16x32_bf16 v[88:91], v[24:27], a[68:71], v[88:91]\n"
"ds_write_addtid_b32 v100 offset:256\n"
"v_add_f32 v111, v111, v95\n"
"v_mfma_f32_16x16x32_bf16 v[88:91], v[28:31], a[72:75], v[88:91]\n"
"ds_write_addtid_b32 v101 offset:4896\n"
"v_cvt_pk_bf16_f32 v106, v108, v109\n"
"v_mfma_f32_16x16x32_bf16 v[88:91], v[32:35], a[76:79], v[88:91]\n"
"ds_write_addtid_b32 v102 offset:9600\n"
"v_cvt_pk_bf16_f32 v107, v110, v111\n"
"v_mfma_f32_16x16x32_bf16 v[88:91], v[36:39], a[80:83], v[88:91]\n"
"buffer_store_dwordx4 v[104:107], v11, s[40:43], s95 offen sc1\n"
"ds_write_addtid_b32 v103 offset:14240\n"
"v_mfma_f32_16x16x32_bf16 v[92:95], v[104:107], v[104:107], 0\n"
"v_mfma_f32_16x16x32_bf16 v[96:99], v[14:17], v[104:107], 0\n"
"s_add_u32 s92, s50, 0x0\n"
"s_and_b32 s92, s92, 0xfbffffff\n"
"v_mfma_f32_16x16x32_bf16 v[88:91], v[40:43], a[84:87], v[88:91]\n"
"v_cndmask_b32_e64 v92, v92, v93, s[60:61]\n"
"v_cndmask_b32_e64 v94, v94, v95, s[60:61]\n"
"v_mfma_f32_16x16x32_bf16 v[88:91], v[44:47], a[88:91], v[88:91]\n"
"s_waitcnt vmcnt(10)\n"
"v_cndmask_b32_e64 v97, v92, v94, s[62:63]\n"
"v_lshlrev_b32 v116, 16, v84\n"
"v_mfma_f32_16x16x32_bf16 v[88:91], v[48:51], a[92:95], v[88:91]\n"
"buffer_store_dwordx2 v[0:1], v12, s[40:43], s93 offen offset:0 sc1\n"
"v_and_b32 v117, 0xffff0000, v84\n"
"v_lshlrev_b32 v118, 16, v85\n"
"v_mfma_f32_16x16x32_bf16 v[92:95], v[20:23], a[96:99], 0\n"
"buffer_store_dwordx2 v[96:97], v13, s[40:43], s95 offen offset:0 sc1\n"
"v_and_b32 v119, 0xffff0000, v85\n"
"v_lshlrev_b32 v120, 16, v86\n"
"v_mfma_f32_16x16x32_bf16 v[92:95], v[24:27], a[100:103], v[92:95]\n"
"ds_write_addtid_b32 v88 offset:512\n"
"v_and_b32 v121, 0xffff0000, v86\n"
"v_mfma_f32_16x16x32_bf16 v[92:95], v[28:31], a[104:107], v[92:95]\n"
"ds_write_addtid_b32 v89 offset:5152\n"
"v_lshlrev_b32 v122, 16, v87\n"
"v_mfma_f32_16x16x32_bf16 v[92:95], v[32:35], a[108:111], v[92:95]\n"
"ds_write_addtid_b32 v90 offset:9856\n"
"buffer_load_dwordx2 v[132:133], v9, s[40:43], s50 offen sc1\n"
"v_mfma_f32_16x16x32_bf16 v[92:95], v[36:39], a[112:115], v[92:95]\n"
"s_waitcnt vmcnt(12)\n"
"ds_write_addtid_b32 v91 offset:14496\n"
"v_cmp_ne_u64 vcc, s[48:49], v[150:151]\n"
"v_mfma_f32_16x16x32_bf16 v[92:95], v[40:43], a[116:119], v[92:95]\n"
"buffer_load_dwordx4 v[124:127], v10, s[40:43], s92 offen sc1\n"
"v_and_b32 v123, 0xffff0000, v87\n"
"v_mfma_f32_16x16x32_bf16 v[92:95], v[44:47], a[120:123], v[92:95]\n"
"s_cbranch_vccnz .LO_1s\n"
".LO_1r:\n"
"v_mfma_f32_16x16x32_bf16 v[92:95], v[48:51], a[124:127], v[92:95]\n"
"s_add_u32 s50, s50, 0x180000\n"
"s_and_b32 s56, s50, 0xfbffffff\n"
"v_mfma_f32_16x16x32_bf16 v[100:103], v[20:23], a[128:131], 0\n"
"buffer_load_dwordx4 v[84:87], v5, s[40:43], s56 offen offset:0 sc1\n"
"v_mfma_f32_16x16x32_bf16 v[100:103], v[24:27], a[132:135], v[100:103]\n"
"ds_write_addtid_b32 v92 offset:768\n"
"v_mfma_f32_16x16x32_bf16 v[100:103], v[28:31], a[136:139], v[100:103]\n"
"ds_write_addtid_b32 v93 offset:5408\n"
"v_mfma_f32_16x16x32_bf16 v[100:103], v[32:35], a[140:143], v[100:103]\n"
"ds_write_addtid_b32 v94 offset:10112\n"
"v_mfma_f32_16x16x32_bf16 v[100:103], v[36:39], a[144:147], v[100:103]\n"
"ds_write_addtid_b32 v95 offset:14752\n"
"buffer_load_dwordx4 v[88:91], v5, s[40:43], s56 offen offset:1024 sc1\n"
"v_mfma_f32_16x16x32_bf16 v[100:103], v[40:43], a[148:151], v[100:103]\n"
"v_mfma_f32_16x16x32_bf16 v[100:103], v[44:47], a[152:155], v[100:103]\n"
"v_mfma_f32_16x16x32_bf16 v[100:103], v[48:51], a[156:159], v[100:103]\n"
"v_mfma_f32_16x16x32_bf16 v[108:111], v[20:23], a[160:163], 0\n"
"buffer_load_dwordx4 v[92:95], v6, s[40:43], s56 offen offset:0 sc1\n"
"v_mfma_f32_16x16x32_bf16 v[108:111], v[24:27], a[164:167], v[108:111]\n"
"ds_write_addtid_b32 v100 offset:1024\n"
"v_mfma_f32_16x16x32_bf16 v[108:111], v[28:31], a[168:171], v[108:111]\n"
"ds_write_addtid_b32 v101 offset:5664\n"
"v_mfma_f32_16x16x32_bf16 v[108:111], v[32:35], a[172:175], v[108:111]\n"
"ds_write_addtid_b32 v102 offset:10368\n"
"v_mfma_f32_16x16x32_bf16 v[108:111], v[36:39], a[176:179], v[108:111]\n"
"ds_write_addtid_b32 v103 offset:15008\n"
"buffer_load_dwordx4 v[96:99], v6, s[40:43], s56 offen offset:1024 sc1\n"
"v_mfma_f32_16x16x32_bf16 v[108:111], v[40:43], a[180:183], v[108:111]\n"
"v_mfma_f32_16x16x32_bf16 v[108:111], v[44:47], a[184:187], v[108:111]\n"
"v_mfma_f32_16x16x32_bf16 v[108:111], v[48:51], a[188:191], v[108:111]\n"
"v_mfma_f32_16x16x32_bf16 v[116:119], a[192:195], v[20:23], v[116:119]\n"
"buffer_load_dwordx4 v[100:103], v7, s[40:43], s56 offen offset:0 sc1\n"
"v_mfma_f32_16x16x32_bf16 v[116:119], a[196:199], v[24:27], v[116:119]\n"
"ds_write_addtid_b32 v108 offset:1280\n"
"v_mfma_f32_16x16x32_bf16 v[116:119], a[200:203], v[28:31], v[116:119]\n"
"ds_write_addtid_b32 v109 offset:5920\n"
"v_mfma_f32_16x16x32_bf16 v[116:119], a[204:207], v[32:35], v[116:119]\n"
"ds_write_addtid_b32 v110 offset:10624\n"
"v_mfma_f32_16x16x32_bf16 v[116:119], a[208:211], v[36:39], v[116:119]\n"
"ds_write_addtid_b32 v111 offset:15264\n"
"buffer_load_dwordx4 v[104:107], v7, s[40:43], s56 offen offset:1024 sc1\n"
"v_mfma_f32_16x16x32_bf16 v[116:119], a[212:215], v[40:43], v[116:119]\n"
"v_mfma_f32_16x16x32_bf16 v[116:119], a[216:219], v[44:47], v[116:119]\n"
"v_mfma_f32_16x16x32_bf16 v[116:119], a[220:223], v[48:51], v[116:119]\n"
"v_mfma_f32_16x16x32_bf16 v[120:123], a[224:227], v[20:23], v[120:123]\n"
"s_waitcnt lgkmcnt(0)\n"
"buffer_load_dwordx4 v[108:111], v8, s[40:43], s56 offen offset:0 sc1\n"
"v_mfma_f32_16x16x32_bf16 v[120:123], a[228:231], v[24:27], v[120:123]\n"
"s_barrier\n"
"v_mfma_f32_16x16x32_bf16 v[120:123], a[232:235], v[28:31], v[120:123]\n"
"ds_read_b128 v[20:23], v2 offset:0\n"
"ds_read_b128 v[24:27], v3 offset:512\n"
"v_mfma_f32_16x16x32_bf16 v[120:123], a[236:239], v[32:35], v[120:123]\n"
"ds_read_b128 v[28:31], v4 offset:1024\n"
"ds_read_b128 v[128:131], v2 offset:256\n"
"v_mfma_f32_16x16x32_bf16 v[120:123], a[240:243], v[36:39], v[120:123]\n"
"buffer_load_dwordx4 v[112:115], v8, s[40:43], s56 offen offset:1024 sc1\n"
"ds_read_b128 v[32:35], v3 offset:768\n"
"v_mfma_f32_16x16x32_bf16 v[120:123], a[244:247], v[40:43], v[120:123]\n"
"ds_read_b128 v[36:39], v4 offset:1280\n"
"v_mfma_f32_16x16x32_bf16 v[120:123], a[248:251], v[44:47], v[120:123]\n"
"v_mfma_f32_16x16x32_bf16 v[120:123], a[252:255], v[48:51], v[120:123]\n"
"s_waitcnt vmcnt(19)\n"
"v_mfma_f32_16x16x32_bf16 v[40:43], v[52:55], a[0:3], 0\n"
"v_mfma_f32_16x16x32_bf16 v[40:43], v[56:59], a[4:7], v[40:43]\n"
"s_waitcnt vmcnt(17)\n"
"v_mfma_f32_16x16x32_bf16 v[40:43], v[60:63], a[8:11], v[40:43]\n"
"s_waitcnt lgkmcnt(3)\n"
"v_add_f32 v20, v20, v24\n"
"v_add_f32 v21, v21, v25\n"
"v_mfma_f32_16x16x32_bf16 v[40:43], v[64:67], a[12:15], v[40:43]\n"
"s_waitcnt vmcnt(15)\n"
"v_add_f32 v22, v22, v26\n"
"v_add_f32 v23, v23, v27\n"
"v_mfma_f32_16x16x32_bf16 v[40:43], v[68:71], a[16:19], v[40:43]\n"
"v_add_f32 v20, v20, v28\n"
"v_add_f32 v21, v21, v29\n"
"v_mfma_f32_16x16x32_bf16 v[40:43], v[72:75], a[20:23], v[40:43]\n"
"s_waitcnt vmcnt(13)\n"
"v_add_f32 v22, v22, v30\n"
"v_add_f32 v23, v23, v31\n"
"v_mfma_f32_16x16x32_bf16 v[40:43], v[76:79], a[24:27], v[40:43]\n"
"v_add_f32 v20, v20, v116\n"
"v_add_f32 v21, v21, v117\n"
"v_mfma_f32_16x16x32_bf16 v[40:43], v[80:83], a[28:31], v[40:43]\n"
"v_add_f32 v22, v22, v118\n"
"v_add_f32 v23, v23, v119\n"
"v_mfma_f32_16x16x32_bf16 v[24:27], v[52:55], a[32:35], 0\n"
"s_add_u32 s93, s50, 0xffd00000\n"
"s_and_b32 s95, s93, 0xfbffffff\n"
"v_cvt_pk_bf16_f32 v28, v20, v21\n"
"v_mfma_f32_16x16x32_bf16 v[24:27], v[56:59], a[36:39], v[24:27]\n"
"ds_write_addtid_b32 v40 offset:1536\n"
"v_cvt_pk_bf16_f32 v29, v22, v23\n"
"v_mfma_f32_16x16x32_bf16 v[24:27], v[60:63], a[40:43], v[24:27]\n"
"s_waitcnt lgkmcnt(1)\n"
"ds_write_addtid_b32 v41 offset:6176\n"
"v_add_f32 v128, v128, v32\n"
"v_mfma_f32_16x16x32_bf16 v[24:27], v[64:67], a[44:47], v[24:27]\n"
"ds_write_addtid_b32 v42 offset:10880\n"
"v_add_f32 v129, v129, v33\n"
"v_mfma_f32_16x16x32_bf16 v[24:27], v[68:71], a[48:51], v[24:27]\n"
"ds_write_addtid_b32 v43 offset:15520\n"
"v_add_f32 v130, v130, v34\n"
"v_mfma_f32_16x16x32_bf16 v[24:27], v[72:75], a[52:55], v[24:27]\n"
"v_add_f32 v131, v131, v35\n"
"v_add_f32 v128, v128, v36\n"
"v_mfma_f32_16x16x32_bf16 v[24:27], v[76:79], a[56:59], v[24:27]\n"
"v_add_f32 v129, v129, v37\n"
"v_add_f32 v130, v130, v38\n"
"v_mfma_f32_16x16x32_bf16 v[24:27], v[80:83], a[60:63], v[24:27]\n"
"v_add_f32 v131, v131, v39\n"
"v_add_f32 v128, v128, v120\n"
"v_mfma_f32_16x16x32_bf16 v[20:23], v[52:55], a[64:67], 0\n"
"v_add_f32 v129, v129, v121\n"
"v_add_f32 v130, v130, v122\n"
"v_mfma_f32_16x16x32_bf16 v[20:23], v[56:59], a[68:71], v[20:23]\n"
"ds_write_addtid_b32 v24 offset:1792\n"
"v_add_f32 v131, v131, v123\n"
"v_mfma_f32_16x16x32_bf16 v[20:23], v[60:63], a[72:75], v[20:23]\n"
"ds_write_addtid_b32 v25 offset:6432\n"
"v_cvt_pk_bf16_f32 v30, v128, v129\n"
"v_mfma_f32_16x16x32_bf16 v[20:23], v[64:67], a[76:79], v[20:23]\n"
"ds_write_addtid_b32 v26 offset:11136\n"
"v_cvt_pk_bf16_f32 v31, v130, v131\n"
"v_mfma_f32_16x16x32_bf16 v[20:23], v[68:71], a[80:83], v[20:23]\n"
"buffer_store_dwordx4 v[28:31], v11, s[40:43], s95 offen sc1\n"
"ds_write_addtid_b32 v27 offset:15776\n"
"v_mfma_f32_16x16x32_bf16 v[32:35], v[28:31], v[28:31], 0\n"
"v_mfma_f32_16x16x32_bf16 v[36:39], v[14:17], v[28:31], 0\n"
"s_add_u32 s92, s50, 0x0\n"
"s_and_b32 s92, s92, 0xfbffffff\n"
"v_mfma_f32_16x16x32_bf16 v[20:23], v[72:75], a[84:87], v[20:23]\n"
"v_cndmask_b32_e64 v32, v32, v33, s[60:61]\n"
"v_cndmask_b32_e64 v34, v34, v35, s[60:61]\n"
"v_mfma_f32_16x16x32_bf16 v[20:23], v[76:79], a[88:91], v[20:23]\n"
"buffer_load_dwordx4 v[40:43], v10, s[40:43], s92 offen sc1\n"
"v_cndmask_b32_e64 v37, v32, v34, s[62:63]\n"
"v_mfma_f32_16x16x32_bf16 v[20:23], v[80:83], a[92:95], v[20:23]\n"
"buffer_store_dwordx2 v[36:37], v13, s[40:43], s95 offen offset:0 sc1\n"
"v_mfma_f32_16x16x32_bf16 v[24:27], v[52:55], a[96:99], 0\n"
"s_waitcnt vmcnt(13)\n"
"buffer_store_dwordx2 v[0:1], v12, s[40:43], s94 offen offset:0 sc1\n"
"v_mfma_f32_16x16x32_bf16 v[24:27], v[56:59], a[100:103], v[24:27]\n"
"ds_write_addtid_b32 v20 offset:2048\n"
"v_mfma_f32_16x16x32_bf16 v[24:27], v[60:63], a[104:107], v[24:27]\n"
"ds_write_addtid_b32 v21 offset:6688\n"
"v_mfma_f32_16x16x32_bf16 v[24:27], v[64:67], a[108:111], v[24:27]\n"
"s_waitcnt vmcnt(12)\n"
"ds_write_addtid_b32 v22 offset:11392\n"
"buffer_load_dwordx2 v[150:151], v9, s[40:43], s50 offen sc1\n"
"v_mfma_f32_16x16x32_bf16 v[24:27], v[68:71], a[112:115], v[24:27]\n"
"ds_write_addtid_b32 v23 offset:16032\n"
"v_cmp_ne_u64 vcc, s[48:49], v[132:133]\n"
"v_mfma_f32_16x16x32_bf16 v[24:27], v[72:75], a[116:119], v[24:27]\n"
"v_lshlrev_b32 v32, 16, v124\n"
"v_and_b32 v33, 0xffff0000, v124\n"
"v_mfma_f32_16x16x32_bf16 v[24:27], v[76:79], a[120:123], v[24:27]\n"
"s_cbranch_vccnz .LO_2s\n"
".LO_2r:\n"
"v_lshlrev_b32 v34, 16, v125\n"
"v_mfma_f32_16x16x32_bf16 v[24:27], v[80:83], a[124:127], v[24:27]\n"
"s_add_u32 s50, s50, 0x180000\n"
"s_and_b32 s56, s50, 0xfbffffff\n"
"v_and_b32 v35, 0xffff0000, v125\n"
"v_mfma_f32_16x16x32_bf16 v[20:23], v[52:55], a[128:131], 0\n"
"buffer_load_dwordx4 v[116:119], v5, s[40:43], s56 offen offset:0 sc1\n"
"v_lshlrev_b32 v36, 16, v126\n"
"v_mfma_f32_16x16x32_bf16 v[20:23], v[56:59], a[132:135], v[20:23]\n"
"ds_write_addtid_b32 v24 offset:2304\n"
"v_and_b32 v37, 0xffff0000, v126\n"
"v_mfma_f32_16x16x32_bf16 v[20:23], v[60:63], a[136:139], v[20:23]\n"
"ds_write_addtid_b32 v25 offset:6944\n"
"v_lshlrev_b32 v38, 16, v127\n"
"v_mfma_f32_16x16x32_bf16 v[20:23], v[64:67], a[140:143], v[20:23]\n"
"ds_write_addtid_b32 v26 offset:11648\n"
"v_and_b32 v39, 0xffff0000, v127\n"
"v_mfma_f32_16x16x32_bf16 v[20:23], v[68:71], a[144:147], v[20:23]\n"
"ds_write_addtid_b32 v27 offset:16288\n"
"buffer_load_dwordx4 v[120:123], v5, s[40:43], s56 offen offset:1024 sc1\n"
"v_mfma_f32_16x16x32_bf16 v[20:23], v[72:75], a[148:151], v[20:23]\n"
"v_mfma_f32_16x16x32_bf16 v[20:23], v[76:79], a[152:155], v[20:23]\n"
"v_mfma_f32_16x16x32_bf16 v[20:23], v[80:83], a[156:159], v[20:23]\n"
"v_mfma_f32_16x16x32_bf16 v[24:27], v[52:55], a[160:163], 0\n"
"buffer_load_dwordx4 v[124:127], v6, s[40:43], s56 offen offset:0 sc1\n"
"v_mfma_f32_16x16x32_bf16 v[24:27], v[56:59], a[164:167], v[24:27]\n"
"ds_write_addtid_b32 v20 offset:2560\n"
"v_mfma_f32_16x16x32_bf16 v[24:27], v[60:63], a[168:171], v[24:27]\n"
"ds_write_addtid_b32 v21 offset:7200\n"
"v_mfma_f32_16x16x32_bf16 v[24:27], v[64:67], a[172:175], v[24:27]\n"
"ds_write_addtid_b32 v22 offset:11904\n"
"v_mfma_f32_16x16x32_bf16 v[24:27], v[68:71], a[176:179], v[24:27]\n"
"ds_write_addtid_b32 v23 offset:16544\n"
"buffer_load_dwordx4 v[128:131], v6, s[40:43], s56 offen offset:1024 sc1\n"
"v_mfma_f32_16x16x32_bf16 v[24:27], v[72:75], a[180:183], v[24:27]\n"
"v_mfma_f32_16x16x32_bf16 v[24:27], v[76:79], a[184:187], v[24:27]\n"
"v_mfma_f32_16x16x32_bf16 v[24:27], v[80:83], a[188:191], v[24:27]\n"
"v_mfma_f32_16x16x32_bf16 v[32:35], a[192:195], v[52:55], v[32:35]\n"
"buffer_load_dwordx4 v[132:135], v7, s[40:43], s56 offen offset:0 sc1\n"
"v_mfma_f32_16x16x32_bf16 v[32:35], a[196:199], v[56:59], v[32:35]\n"
"ds_write_addtid_b32 v24 offset:2816\n"
"v_mfma_f32_16x16x32_bf16 v[32:35], a[200:203], v[60:63], v[32:35]\n"
"ds_write_addtid_b32 v25 offset:7456\n"
"v_mfma_f32_16x16x32_bf16 v[32:35], a[204:207], v[64:67], v[32:35]\n"
"ds_write_addtid_b32 v26 offset:12160\n"
"v_mfma_f32_16x16x32_bf16 v[32:35], a[208:211], v[68:71], v[32:35]\n"
"ds_write_addtid_b32 v27 offset:16800\n"
"buffer_load_dwordx4 v[136:139], v7, s[40:43], s56 offen offset:1024 sc1\n"
"v_mfma_f32_16x16x32_bf16 v[32:35], a[212:215], v[72:75], v[32:35]\n"
"v_mfma_f32_16x16x32_bf16 v[32:35], a[216:219], v[76:79], v[32:35]\n"
"v_mfma_f32_16x16x32_bf16 v[32:35], a[220:223], v[80:83], v[32:35]\n"
"v_mfma_f32_16x16x32_bf16 v[36:39], a[224:227], v[52:55], v[36:39]\n"
"s_waitcnt lgkmcnt(0)\n"
"buffer_load_dwordx4 v[140:143], v8, s[40:43], s56 offen offset:0 sc1\n"
"v_mfma_f32_16x16x32_bf16 v[36:39], a[228:231], v[56:59], v[36:39]\n"
"s_barrier\n"
"v_mfma_f32_16x16x32_bf16 v[36:39], a[232:235], v[60:63], v[36:39]\n"
"ds_read_b128 v[44:47], v2 offset:1536\n"
"ds_read_b128 v[24:27], v3 offset:2048\n"
"v_mfma_f32_16x16x32_bf16 v[36:39], a[236:239], v[64:67], v[36:39]\n"
"ds_read_b128 v[28:31], v4 offset:2560\n"
"ds_read_b128 v[48:51], v2 offset:1792\n"
"v_mfma_f32_16x16x32_bf16 v[36:39], a[240:243], v[68:71], v[36:39]\n"
"buffer_load_dwordx4 v[144:147], v8, s[40:43], s56 offen offset:1024 sc1\n"
"ds_read_b128 v[52:55], v3 offset:2304\n"
"v_mfma_f32_16x16x32_bf16 v[36:39], a[244:247], v[72:75], v[36:39]\n"
"ds_read_b128 v[56:59], v4 offset:2816\n"
"v_mfma_f32_16x16x32_bf16 v[36:39], a[248:251], v[76:79], v[36:39]\n"
"v_mfma_f32_16x16x32_bf16 v[36:39], a[252:255], v[80:83], v[36:39]\n"
"s_waitcnt vmcnt(19)\n"
"s_branch .LO_top\n"
".LO_1s:\n"
"s_nop 7\n"
"s_cmp_ge_u32 s50, s53\n"
"s_cbranch_scc1 .LO_end\n"
"s_cmp_ge_u32 s50, s52\n"
"s_cbranch_scc1 .LO_1r\n"
"s_sub_u32 s58, s50, 0x180000\n"
"s_mov_b32 s59, 200000\n"
".LO_1p:\n"
"buffer_load_dwordx2 v[150:151], v9, s[40:43], s58 offen sc1\n"
"s_waitcnt vmcnt(0)\n"
"v_cmp_ne_u64 vcc, s[48:49], v[150:151]\n"
"s_cbranch_vccz .LO_1px\n"
"s_sleep 1\n"
"s_sub_u32 s59, s59, 1\n"
"s_cbranch_scc0 .LO_1p\n"
"s_branch .LO_end\n"
".LO_1px:\n"
"s_branch .LO_1r\n"
".LO_2s:\n"
"s_nop 7\n"
"s_cmp_ge_u32 s50, s53\n"
"s_cbranch_scc1 .LO_end\n"
"s_cmp_ge_u32 s50, s52\n"
"s_cbranch_scc1 .LO_2r\n"
"s_sub_u32 s58, s50, 0x180000\n"
"s_mov_b32 s59, 200000\n"
".LO_2p:\n"
"buffer_load_dwordx2 v[132:133], v9, s[40:43], s58 offen sc1\n"
"s_waitcnt vmcnt(0)\n"
"v_cmp_ne_u64 vcc, s[48:49], v[132:133]\n"
"s_cbranch_vccz .LO_2px\n"
"s_sleep 1\n"
"s_sub_u32 s59, s59, 1\n"
"s_cbranch_scc0 .LO_2p\n"
"s_branch .LO_end\n"
".LO_2px:\n"
"s_branch .LO_2r\n"
".LO_3s:\n"
"s_nop 7\n"
"s_cmp_ge_u32 s50, s53\n"
"s_cbranch_scc1 .LO_end\n"
"s_cmp_ge_u32 s50, s52\n"
"s_cbranch_scc1 .LO_3r\n"
"s_sub_u32 s58, s50, 0x180000\n"
"s_mov_b32 s59, 200000\n"
".LO_3p:\n"
"buffer_load_dwordx2 v[150:151], v9, s[40:43], s58 offen sc1\n"
"s_waitcnt vmcnt(0)\n"
"v_cmp_ne_u64 vcc, s[48:49], v[150:151]\n"
"s_cbranch_vccz .LO_3px\n"
"s_sleep 1\n"
"s_sub_u32 s59, s59, 1\n"
"s_cbranch_scc0 .LO_3p\n"
"s_branch .LO_end\n"
".LO_3px:\n"
"s_branch .LO_3r\n"
".LO_4s:\n"
"s_nop 7\n"
"s_cmp_ge_u32 s50, s53\n"
"s_cbranch_scc1 .LO_end\n"
"s_cmp_ge_u32 s50, s52\n"
"s_cbranch_scc1 .LO_4r\n"
"s_sub_u32 s58, s50, 0x180000\n"
"s_mov_b32 s59, 200000\n"
".LO_4p:\n"
"buffer_load_dwordx2 v[148:149], v9, s[40:43], s58 offen sc1\n"
"s_waitcnt vmcnt(0)\n"
"v_cmp_ne_u64 vcc, s[48:49], v[148:149]\n"
"s_cbranch_vccz .LO_4px\n"
"s_sleep 1\n"
"s_sub_u32 s59, s59, 1\n"
"s_cbranch_scc0 .LO_4p\n"
"s_branch .LO_end\n"
".LO_4px:\n"
"s_branch .LO_4r\n"
".LO_end:\n"
"s_waitcnt vmcnt(0) lgkmcnt(0)\n"
"s_endpgm\n"
      :: "{v2}"((unsigned)(lds0 + ((wid + 3) & 3) * 18592 + trp)), "{v3}"((unsigned)(lds0 + ((wid + 2) & 3) * 18592 + trp)), "{v4}"((unsigned)(lds0 + ((wid + 1) & 3) * 18592 + trp)), "{v5}"((unsigned)(lane * 16 + wid * 8192 + ((0 + rot) & 3) * 2048 + 32768)), "{v6}"((unsigned)(lane * 16 + wid * 8192 + ((1 + rot) & 3) * 2048 + 32768)), "{v7}"((unsigned)(lane * 16 + wid * 8192 + ((2 + rot) & 3) * 2048 + 32768)), "{v8}"((unsigned)(lane * 16 + wid * 8192 + ((3 + rot) & 3) * 2048 + 32768)), "{v9}"((unsigned)((wid * 8 + (lane & 7)) * 8 + 352512 + O_GL * 1572864)), "{v10}"((unsigned)(lane * 16 + pair * 1024 + 0)), "{v11}"((unsigned)(lane * 16 + pair * 1024 + 65536)), "{v12}"((unsigned)(lane ? DROP : pair * 8 + 352768)), "{v13}"((unsigned)(lg == (li >> 2) ? pair * 128 + li * 8 + 344064 : DROP)), "{v14}"((unsigned)(0x3f803f80)), "{v15}"((unsigned)(0x3f803f80)), "{v16}"((unsigned)(0x3f803f80)), "{v17}"((unsigned)(0x3f803f80)), "{v20}"((unsigned)(vT + tsw)), "{v21}"((unsigned)(vT + (tsw ^ 8))), "{v22}"((unsigned)(vD + ((0 + rot) & 3) * 131072)), "{v23}"((unsigned)(vD + ((1 + rot) & 3) * 131072)), "{v24}"((unsigned)(vD + ((2 + rot) & 3) * 131072)), "{v25}"((unsigned)(vD + ((3 + rot) & 3) * 131072)), "{s[40:43]}"(rS), "{s[44:47]}"(rW), "{s[48:49]}"(wsp), "{s50}"((unsigned)(sA)), "{s51}"((unsigned)(sLo)), "{s52}"((unsigned)(sHi)), "{s53}"((unsigned)(sEx)), "{s54}"((unsigned)(sEnt)), "{s55}"((unsigned)(sLd)), "{s56}"((unsigned)(sA & 0xfbffffff)), "{s57}"((unsigned)(lds0 + wid * 18592)), "{s[60:61]}"(0xAAAAAAAAAAAAAAAAull), "{s[62:63]}"(0xCCCCCCCCCCCCCCCCull), "{s64}"((unsigned)(sW0 + 64 * ((wid + 1) & 3))), "{s65}"((unsigned)(sW0 + 64 * ((wid + 2) & 3))), "{s66}"((unsigned)(sW0 + 64 * ((wid + 3) & 3))), "{s67}"((unsigned)(sW0 + 64 * ((wid + 4) & 3)))
      : "v255", "a255", "s95", "vcc", "scc", "memory");

  } else if (role == 2) {
    const unsigned sEx = sHi + F_EX * 3 * RS, sEnt = -wid & 3, tsw = 0, pair = idx * 4 + wid, rot = KR * (set ? set & 2 : 1);
    const unsigned sW0 = wid * 256 * 7168 + idx * 256, vD = (lane >> 2) * 7168 + (lane & 3) * 16;
    const u32x4 rW = desc(a.w1, 1024 * 7168), bp = *(const u32x4*)(a.b1 + pair * 32 + lg * 8);
    const unsigned bia[8] = {bp[0] << 16, bp[0] & 0xffff0000u, bp[1] << 16, bp[1] & 0xffff0000u, bp[2] << 16, bp[2] & 0xffff0000u, bp[3] << 16, bp[3] & 0xffff0000u};
    asm volatile(
"s_nop 4\n"
"v_mov_b32 v186, 0\n"
"global_load_dwordx2 v[0:1], v186, s[48:49] sc1\n"
"v_mov_b32 v188, 0x3f803f80\n"
"v_mov_b32 v189, 0x3f803f80\n"
"v_mov_b32 v190, 0x3f803f80\n"
"v_mov_b32 v191, 0x3f803f80\n"
"s_add_u32 m0, s55, 0\n"
"s_add_u32 s58, s61, 0\n"
"buffer_load_dwordx4 v38, s[44:47], s58 offen lds\n"
"s_add_u32 m0, s55, 1024\n"
"s_add_u32 s58, s61, 114688\n"
"buffer_load_dwordx4 v38, s[44:47], s58 offen lds\n"
"s_add_u32 m0, s55, 2048\n"
"s_add_u32 s58, s62, 0\n"
"buffer_load_dwordx4 v38, s[44:47], s58 offen lds\n"
"s_add_u32 m0, s55, 3072\n"
"s_add_u32 s58, s62, 114688\n"
"buffer_load_dwordx4 v38, s[44:47], s58 offen lds\n"
"s_add_u32 m0, s55, 4096\n"
"s_add_u32 s58, s63, 0\n"
"buffer_load_dwordx4 v38, s[44:47], s58 offen lds\n"
"s_add_u32 m0, s55, 5120\n"
"s_add_u32 s58, s63, 114688\n"
"buffer_load_dwordx4 v38, s[44:47], s58 offen lds\n"
"s_add_u32 m0, s55, 6144\n"
"s_add_u32 s58, s64, 0\n"
"buffer_load_dwordx4 v38, s[44:47], s58 offen lds\n"
"s_add_u32 m0, s55, 7168\n"
"s_add_u32 s58, s64, 114688\n"
"buffer_load_dwordx4 v38, s[44:47], s58 offen lds\n"
"s_add_u32 m0, s55, 8192\n"
"s_add_u32 s58, s61, 229376\n"
"buffer_load_dwordx4 v38, s[44:47], s58 offen lds\n"
"s_add_u32 m0, s55, 9216\n"
"s_add_u32 s58, s61, 344064\n"
"buffer_load_dwordx4 v38, s[44:47], s58 offen lds\n"
"s_add_u32 m0, s55, 10240\n"
"s_add_u32 s58, s62, 229376\n"
"buffer_load_dwordx4 v38, s[44:47], s58 offen lds\n"
"s_add_u32 m0, s55, 11264\n"
"s_add_u32 s58, s62, 344064\n"
"buffer_load_dwordx4 v38, s[44:47], s58 offen lds\n"
"s_add_u32 m0, s55, 12288\n"
"s_add_u32 s58, s63, 229376\n"
"buffer_load_dwordx4 v38, s[44:47], s58 offen lds\n"
"s_add_u32 m0, s55, 13312\n"
"s_add_u32 s58, s63, 344064\n"
"buffer_load_dwordx4 v38, s[44:47], s58 offen lds\n"
"s_add_u32 m0, s55, 14336\n"
"s_add_u32 s58, s64, 229376\n"
"buffer_load_dwordx4 v38, s[44:47], s58 offen lds\n"
"s_add_u32 m0, s55, 15360\n"
"s_add_u32 s58, s64, 344064\n"
"buffer_load_dwordx4 v38, s[44:47], s58 offen lds\n"
"s_add_u32 m0, s55, 16384\n"
"s_add_u32 s58, s61, 0\n"
"buffer_load_dwordx4 v39, s[44:47], s58 offen lds\n"
"s_add_u32 m0, s55, 17408\n"
"s_add_u32 s58, s61, 114688\n"
"buffer_load_dwordx4 v39, s[44:47], s58 offen lds\n"
"s_add_u32 m0, s55, 18432\n"
"s_add_u32 s58, s62, 0\n"
"buffer_load_dwordx4 v39, s[44:47], s58 offen lds\n"
"s_add_u32 m0, s55, 19456\n"
"s_add_u32 s58, s62, 114688\n"
"buffer_load_dwordx4 v39, s[44:47], s58 offen lds\n"
"s_add_u32 m0, s55, 20480\n"
"s_add_u32 s58, s63, 0\n"
"buffer_load_dwordx4 v39, s[44:47], s58 offen lds\n"
"s_add_u32 m0, s55, 21504\n"
"s_add_u32 s58, s63, 114688\n"
"buffer_load_dwordx4 v39, s[44:47], s58 offen lds\n"
"s_add_u32 m0, s55, 22528\n"
"s_add_u32 s58, s64, 0\n"
"buffer_load_dwordx4 v39, s[44:47], s58 offen lds\n"
"s_add_u32 m0, s55, 23552\n"
"s_add_u32 s58, s64, 114688\n"
"buffer_load_dwordx4 v39, s[44:47], s58 offen lds\n"
"s_add_u32 m0, s55, 24576\n"
"s_add_u32 s58, s61, 229376\n"
"buffer_load_dwordx4 v39, s[44:47], s58 offen lds\n"
"s_add_u32 m0, s55, 25600\n"
"s_add_u32 s58, s61, 344064\n"
"buffer_load_dwordx4 v39, s[44:47], s58 offen lds\n"
"s_add_u32 m0, s55, 26624\n"
"s_add_u32 s58, s62, 229376\n"
"buffer_load_dwordx4 v39, s[44:47], s58 offen lds\n"
"s_add_u32 m0, s55, 27648\n"
"s_add_u32 s58, s62, 344064\n"
"buffer_load_dwordx4 v39, s[44:47], s58 offen lds\n"
"s_add_u32 m0, s55, 28672\n"
"s_add_u32 s58, s63, 229376\n"
"buffer_load_dwordx4 v39, s[44:47], s58 offen lds\n"
"s_add_u32 m0, s55, 29696\n"
"s_add_u32 s58, s63, 344064\n"
"buffer_load_dwordx4 v39, s[44:47], s58 offen lds\n"
"s_add_u32 m0, s55, 30720\n"
"s_add_u32 s58, s64, 229376\n"
"buffer_load_dwordx4 v39, s[44:47], s58 offen lds\n"
"s_add_u32 m0, s55, 31744\n"
"s_add_u32 s58, s64, 344064\n"
"buffer_load_dwordx4 v39, s[44:47], s58 offen lds\n"
"s_add_u32 m0, s55, 32768\n"
"s_add_u32 s58, s61, 0\n"
"buffer_load_dwordx4 v40, s[44:47], s58 offen lds\n"
"s_add_u32 m0, s55, 33792\n"
"s_add_u32 s58, s61, 114688\n"
"buffer_load_dwordx4 v40, s[44:47], s58 offen lds\n"
"s_add_u32 m0, s55, 34816\n"
"s_add_u32 s58, s62, 0\n"
"buffer_load_dwordx4 v40, s[44:47], s58 offen lds\n"
"s_add_u32 m0, s55, 35840\n"
"s_add_u32 s58, s62, 114688\n"
"buffer_load_dwordx4 v40, s[44:47], s58 offen lds\n"
"s_add_u32 m0, s55, 36864\n"
"s_add_u32 s58, s63, 0\n"
"buffer_load_dwordx4 v40, s[44:47], s58 offen lds\n"
"s_add_u32 m0, s55, 37888\n"
"s_add_u32 s58, s63, 114688\n"
"buffer_load_dwordx4 v40, s[44:47], s58 offen lds\n"
"s_add_u32 m0, s55, 38912\n"
"s_add_u32 s58, s64, 0\n"
"buffer_load_dwordx4 v40, s[44:47], s58 offen lds\n"
"s_add_u32 m0, s55, 39936\n"
"s_add_u32 s58, s64, 114688\n"
"buffer_load_dwordx4 v40, s[44:47], s58 offen lds\n"
"s_waitcnt vmcnt(38)\n"
"ds_read_b64_tr_b16 a[0:1], v36 offset:0\n"
"ds_read_b64_tr_b16 a[2:3], v36 offset:256\n"
"ds_read_b64_tr_b16 a[32:33], v37 offset:0\n"
"ds_read_b64_tr_b16 a[34:35], v37 offset:256\n"
"v_add_u32 v0, 2, v0\n"
"v_add_u32 v1, 0x9e3779b9, v1\n"
"v_or_b32 v0, 1, v0\n"
"v_readfirstlane_b32 s49, v1\n"
"v_readfirstlane_b32 s48, v0\n"
"s_waitcnt vmcnt(36)\n"
"ds_read_b64_tr_b16 a[64:65], v36 offset:2048\n"
"ds_read_b64_tr_b16 a[66:67], v36 offset:2304\n"
"ds_read_b64_tr_b16 a[96:97], v37 offset:2048\n"
"ds_read_b64_tr_b16 a[98:99], v37 offset:2304\n"
"s_waitcnt lgkmcnt(4)\n"
"s_add_u32 m0, s55, 0\n"
"s_add_u32 s58, s61, 229376\n"
"buffer_load_dwordx4 v40, s[44:47], s58 offen lds\n"
"s_add_u32 m0, s55, 1024\n"
"s_add_u32 s58, s61, 344064\n"
"buffer_load_dwordx4 v40, s[44:47], s58 offen lds\n"
"v_mfma_f32_16x16x32_bf16 v[192:195], v[188:191], a[0:3], 0\n"
"v_mfma_f32_16x16x32_bf16 v[196:199], v[188:191], a[32:35], 0\n"
"s_waitcnt vmcnt(36)\n"
"ds_read_b64_tr_b16 a[128:129], v36 offset:4096\n"
"ds_read_b64_tr_b16 a[130:131], v36 offset:4352\n"
"ds_read_b64_tr_b16 a[160:161], v37 offset:4096\n"
"ds_read_b64_tr_b16 a[162:163], v37 offset:4352\n"
"s_waitcnt lgkmcnt(4)\n"
"s_add_u32 m0, s55, 2048\n"
"s_add_u32 s58, s62, 229376\n"
"buffer_load_dwordx4 v40, s[44:47], s58 offen lds\n"
"s_add_u32 m0, s55, 3072\n"
"s_add_u32 s58, s62, 344064\n"
"buffer_load_dwordx4 v40, s[44:47], s58 offen lds\n"
"v_mfma_f32_16x16x32_bf16 v[200:203], v[188:191], a[64:67], 0\n"
"v_mfma_f32_16x16x32_bf16 v[204:207], v[188:191], a[96:99], 0\n"
"s_waitcnt vmcnt(36)\n"
"ds_read_b64_tr_b16 a[192:193], v36 offset:6144\n"
"ds_read_b64_tr_b16 a[194:195], v36 offset:6400\n"
"ds_read_b64_tr_b16 a[224:225], v37 offset:6144\n"
"ds_read_b64_tr_b16 a[226:227], v37 offset:6400\n"
"s_waitcnt lgkmcnt(4)\n"
"s_add_u32 m0, s55, 4096\n"
"s_add_u32 s58, s63, 229376\n"
"buffer_load_dwordx4 v40, s[44:47], s58 offen lds\n"
"s_add_u32 m0, s55, 5120\n"
"s_add_u32 s58, s63, 344064\n"
"buffer_load_dwordx4 v40, s[44:47], s58 offen lds\n"
"v_mfma_f32_16x16x32_bf16 v[208:211], v[188:191], a[128:131], 0\n"
"v_mfma_f32_16x16x32_bf16 v[212:215], v[188:191], a[160:163], 0\n"
"s_waitcnt vmcnt(36)\n"
"ds_read_b64_tr_b16 a[4:5], v36 offset:8192\n"
"ds_read_b64_tr_b16 a[6:7], v36 offset:8448\n"
"ds_read_b64_tr_b16 a[36:37], v37 offset:8192\n"
"ds_read_b64_tr_b16 a[38:39], v37 offset:8448\n"
"s_waitcnt lgkmcnt(4)\n"
"s_add_u32 m0, s55, 6144\n"
"s_add_u32 s58, s64, 229376\n"
"buffer_load_dwordx4 v40, s[44:47], s58 offen lds\n"
"s_add_u32 m0, s55, 7168\n"
"s_add_u32 s58, s64, 344064\n"
"buffer_load_dwordx4 v40, s[44:47], s58 offen lds\n"
"v_mfma_f32_16x16x32_bf16 v[216:219], v[188:191], a[192:195], 0\n"
"v_mfma_f32_16x16x32_bf16 v[220:223], v[188:191], a[224:227], 0\n"
"s_waitcnt vmcnt(36)\n"
"ds_read_b64_tr_b16 a[68:69], v36 offset:10240\n"
"ds_read_b64_tr_b16 a[70:71], v36 offset:10496\n"
"ds_read_b64_tr_b16 a[100:101], v37 offset:10240\n"
"ds_read_b64_tr_b16 a[102:103], v37 offset:10496\n"
"s_waitcnt lgkmcnt(4)\n"
"s_add_u32 m0, s55, 8192\n"
"s_add_u32 s58, s61, 0\n"
"buffer_load_dwordx4 v41, s[44:47], s58 offen lds\n"
"s_add_u32 m0, s55, 9216\n"
"s_add_u32 s58, s61, 114688\n"
"buffer_load_dwordx4 v41, s[44:47], s58 offen lds\n"
"v_mfma_f32_16x16x32_bf16 v[192:195], v[188:191], a[4:7], v[192:195]\n"
"v_mfma_f32_16x16x32_bf16 v[196:199], v[188:191], a[36:39], v[196:199]\n"
"s_waitcnt vmcnt(36)\n"
"ds_read_b64_tr_b16 a[132:133], v36 offset:12288\n"
"ds_read_b64_tr_b16 a[134:135], v36 offset:12544\n"
"ds_read_b64_tr_b16 a[164:165], v37 offset:12288\n"
"ds_read_b64_tr_b16 a[166:167], v37 offset:12544\n"
"s_waitcnt lgkmcnt(4)\n"
"s_add_u32 m0, s55, 10240\n"
"s_add_u32 s58, s62, 0\n"
"buffer_load_dwordx4 v41, s[44:47], s58 offen lds\n"
"s_add_u32 m0, s55, 11264\n"
"s_add_u32 s58, s62, 114688\n"
"buffer_load_dwordx4 v41, s[44:47], s58 offen lds\n"
"v_mfma_f32_16x16x32_bf16 v[200:203], v[188:191], a[68:71], v[200:203]\n"
"v_mfma_f32_16x16x32_bf16 v[204:207], v[188:191], a[100:103], v[204:207]\n"
"s_waitcnt vmcnt(36)\n"
"ds_read_b64_tr_b16 a[196:197], v36 offset:14336\n"
"ds_read_b64_tr_b16 a[198:199], v36 offset:14592\n"
"ds_read_b64_tr_b16 a[228:229], v37 offset:14336\n"
"ds_read_b64_tr_b16 a[230:231], v37 offset:14592\n"
"s_waitcnt lgkmcnt(4)\n"
"s_add_u32 m0, s55, 12288\n"
"s_add_u32 s58, s63, 0\n"
"buffer_load_dwordx4 v41, s[44:47], s58 offen lds\n"
"s_add_u32 m0, s55, 13312\n"
"s_add_u32 s58, s63, 114688\n"
"buffer_load_dwordx4 v41, s[44:47], s58 offen lds\n"
"v_mfma_f32_16x16x32_bf16 v[208:211], v[188:191], a[132:135], v[208:211]\n"
"v_mfma_f32_16x16x32_bf16 v[212:215], v[188:191], a[164:167], v[212:215]\n"
"s_waitcnt vmcnt(36)\n"
"ds_read_b64_tr_b16 a[8:9], v36 offset:16384\n"
"ds_read_b64_tr_b16 a[10:11], v36 offset:16640\n"
"ds_read_b64_tr_b16 a[40:41], v37 offset:16384\n"
"ds_read_b64_tr_b16 a[42:43], v37 offset:16640\n"
"s_waitcnt lgkmcnt(4)\n"
"s_add_u32 m0, s55, 14336\n"
"s_add_u32 s58, s64, 0\n"
"buffer_load_dwordx4 v41, s[44:47], s58 offen lds\n"
"s_add_u32 m0, s55, 15360\n"
"s_add_u32 s58, s64, 114688\n"
"buffer_load_dwordx4 v41, s[44:47], s58 offen lds\n"
"v_mfma_f32_16x16x32_bf16 v[216:219], v[188:191], a[196:199], v[216:219]\n"
"v_mfma_f32_16x16x32_bf16 v[220:223], v[188:191], a[228:231], v[220:223]\n"
"s_waitcnt vmcnt(36)\n"
"ds_read_b64_tr_b16 a[72:73], v36 offset:18432\n"
"ds_read_b64_tr_b16 a[74:75], v36 offset:18688\n"
"ds_read_b64_tr_b16 a[104:105], v37 offset:18432\n"
"ds_read_b64_tr_b16 a[106:107], v37 offset:18688\n"
"s_waitcnt lgkmcnt(4)\n"
"s_add_u32 m0, s55, 16384\n"
"s_add_u32 s58, s61, 229376\n"
"buffer_load_dwordx4 v41, s[44:47], s58 offen lds\n"
"s_add_u32 m0, s55, 17408\n"
"s_add_u32 s58, s61, 344064\n"
"buffer_load_dwordx4 v41, s[44:47], s58 offen lds\n"
"v_mfma_f32_16x16x32_bf16 v[192:195], v[188:191], a[8:11], v[192:195]\n"
"v_mfma_f32_16x16x32_bf16 v[196:199], v[188:191], a[40:43], v[196:199]\n"
"s_waitcnt vmcnt(36)\n"
"ds_read_b64_tr_b16 a[136:137], v36 offset:20480\n"
"ds_read_b64_tr_b16 a[138:139], v36 offset:20736\n"
"ds_read_b64_tr_b16 a[168:169], v37 offset:20480\n"
"ds_read_b64_tr_b16 a[170:171], v37 offset:20736\n"
"s_waitcnt lgkmcnt(4)\n"
"s_add_u32 m0, s55, 18432\n"
"s_add_u32 s58, s62, 229376\n"
"buffer_load_dwordx4 v41, s[44:47], s58 offen lds\n"
"s_add_u32 m0, s55, 19456\n"
"s_add_u32 s58, s62, 344064\n"
"buffer_load_dwordx4 v41, s[44:47], s58 offen lds\n"
"v_mfma_f32_16x16x32_bf16 v[200:203], v[188:191], a[72:75], v[200:203]\n"
"v_mfma_f32_16x16x32_bf16 v[204:207], v[188:191], a[104:107], v[204:207]\n"
"s_waitcnt vmcnt(36)\n"
"ds_read_b64_tr_b16 a[200:201], v36 offset:22528\n"
"ds_read_b64_tr_b16 a[202:203], v36 offset:22784\n"
"ds_read_b64_tr_b16 a[232:233], v37 offset:22528\n"
"ds_read_b64_tr_b16 a[234:235], v37 offset:22784\n"
"s_waitcnt lgkmcnt(4)\n"
"s_add_u32 m0, s55, 20480\n"
"s_add_u32 s58, s63, 229376\n"
"buffer_load_dwordx4 v41, s[44:47], s58 offen lds\n"
"s_add_u32 m0, s55, 21504\n"
"s_add_u32 s58, s63, 344064\n"
"buffer_load_dwordx4 v41, s[44:47], s58 offen lds\n"
"v_mfma_f32_16x16x32_bf16 v[208:211], v[188:191], a[136:139], v[208:211]\n"
"v_mfma_f32_16x16x32_bf16 v[212:215], v[188:191], a[168:171], v[212:215]\n"
"s_waitcnt vmcnt(36)\n"
"ds_read_b64_tr_b16 a[12:13], v36 offset:24576\n"
"ds_read_b64_tr_b16 a[14:15], v36 offset:24832\n"
"ds_read_b64_tr_b16 a[44:45], v37 offset:24576\n"
"ds_read_b64_tr_b16 a[46:47], v37 offset:24832\n"
"s_waitcnt lgkmcnt(4)\n"
"s_add_u32 m0, s55, 22528\n"
"s_add_u32 s58, s64, 229376\n"
"buffer_load_dwordx4 v41, s[44:47], s58 offen lds\n"
"s_add_u32 m0, s55, 23552\n"
"s_add_u32 s58, s64, 344064\n"
"buffer_load_dwordx4 v41, s[44:47], s58 offen lds\n"
"v_mfma_f32_16x16x32_bf16 v[216:219], v[188:191], a[200:203], v[216:219]\n"
"v_mfma_f32_16x16x32_bf16 v[220:223], v[188:191], a[232:235], v[220:223]\n"
"s_waitcnt vmcnt(36)\n"
"ds_read_b64_tr_b16 a[76:77], v36 offset:26624\n"
"ds_read_b64_tr_b16 a[78:79], v36 offset:26880\n"
"ds_read_b64_tr_b16 a[108:109], v37 offset:26624\n"
"ds_read_b64_tr_b16 a[110:111], v37 offset:26880\n"
"s_waitcnt lgkmcnt(4)\n"
"v_mfma_f32_16x16x32_bf16 v[192:195], v[188:191], a[12:15], v[192:195]\n"
"v_mfma_f32_16x16x32_bf16 v[196:199], v[188:191], a[44:47], v[196:199]\n"
"s_waitcnt vmcnt(34)\n"
"ds_read_b64_tr_b16 a[140:141], v36 offset:28672\n"
"ds_read_b64_tr_b16 a[142:143], v36 offset:28928\n"
"ds_read_b64_tr_b16 a[172:173], v37 offset:28672\n"
"ds_read_b64_tr_b16 a[174:175], v37 offset:28928\n"
"s_waitcnt lgkmcnt(4)\n"
"v_mfma_f32_16x16x32_bf16 v[200:203], v[188:191], a[76:79], v[200:203]\n"
"v_mfma_f32_16x16x32_bf16 v[204:207], v[188:191], a[108:111], v[204:207]\n"
"s_waitcnt vmcnt(32)\n"
"ds_read_b64_tr_b16 a[204:205], v36 offset:30720\n"
"ds_read_b64_tr_b16 a[206:207], v36 offset:30976\n"
"ds_read_b64_tr_b16 a[236:237], v37 offset:30720\n"
"ds_read_b64_tr_b16 a[238:239], v37 offset:30976\n"
"s_waitcnt lgkmcnt(4)\n"
"v_mfma_f32_16x16x32_bf16 v[208:211], v[188:191], a[140:143], v[208:211]\n"
"v_mfma_f32_16x16x32_bf16 v[212:215], v[188:191], a[172:175], v[212:215]\n"
"s_waitcnt vmcnt(30)\n"
"ds_read_b64_tr_b16 a[16:17], v36 offset:32768\n"
"ds_read_b64_tr_b16 a[18:19], v36 offset:33024\n"
"ds_read_b64_tr_b16 a[48:49], v37 offset:32768\n"
"ds_read_b64_tr_b16 a[50:51], v37 offset:33024\n"
"s_waitcnt lgkmcnt(4)\n"
"v_mfma_f32_16x16x32_bf16 v[216:219], v[188:191], a[204:207], v[216:219]\n"
"v_mfma_f32_16x16x32_bf16 v[220:223], v[188:191], a[236:239], v[220:223]\n"
"s_waitcnt vmcnt(28)\n"
"ds_read_b64_tr_b16 a[80:81], v36 offset:34816\n"
"ds_read_b64_tr_b16 a[82:83], v36 offset:35072\n"
"ds_read_b64_tr_b16 a[112:113], v37 offset:34816\n"
"ds_read_b64_tr_b16 a[114:115], v37 offset:35072\n"
"s_waitcnt lgkmcnt(4)\n"
"v_mfma_f32_16x16x32_bf16 v[192:195], v[188:191], a[16:19], v[192:195]\n"
"v_mfma_f32_16x16x32_bf16 v[196:199], v[188:191], a[48:51], v[196:199]\n"
"s_waitcnt vmcnt(26)\n"
"ds_read_b64_tr_b16 a[144:145], v36 offset:36864\n"
"ds_read_b64_tr_b16 a[146:147], v36 offset:37120\n"
"ds_read_b64_tr_b16 a[176:177], v37 offset:36864\n"
"ds_read_b64_tr_b16 a[178:179], v37 offset:37120\n"
"s_waitcnt lgkmcnt(4)\n"
"v_mfma_f32_16x16x32_bf16 v[200:203], v[188:191], a[80:83], v[200:203]\n"
"v_mfma_f32_16x16x32_bf16 v[204:207], v[188:191], a[112:115], v[204:207]\n"
"s_waitcnt vmcnt(24)\n"
"ds_read_b64_tr_b16 a[208:209], v36 offset:38912\n"
"ds_read_b64_tr_b16 a[210:211], v36 offset:39168\n"
"ds_read_b64_tr_b16 a[240:241], v37 offset:38912\n"
"ds_read_b64_tr_b16 a[242:243], v37 offset:39168\n"
"s_waitcnt lgkmcnt(4)\n"
"v_mfma_f32_16x16x32_bf16 v[208:211], v[188:191], a[144:147], v[208:211]\n"
"v_mfma_f32_16x16x32_bf16 v[212:215], v[188:191], a[176:179], v[212:215]\n"
"s_waitcnt vmcnt(22)\n"
"ds_read_b64_tr_b16 a[20:21], v36 offset:0\n"
"ds_read_b64_tr_b16 a[22:23], v36 offset:256\n"
"ds_read_b64_tr_b16 a[52:53], v37 offset:0\n"
"ds_read_b64_tr_b16 a[54:55], v37 offset:256\n"
"s_waitcnt lgkmcnt(4)\n"
"v_mfma_f32_16x16x32_bf16 v[216:219], v[188:191], a[208:211], v[216:219]\n"
"v_mfma_f32_16x16x32_bf16 v[220:223], v[188:191], a[240:243], v[220:223]\n"
"s_waitcnt vmcnt(20)\n"
"ds_read_b64_tr_b16 a[84:85], v36 offset:2048\n"
"ds_read_b64_tr_b16 a[86:87], v36 offset:2304\n"
"ds_read_b64_tr_b16 a[116:117], v37 offset:2048\n"
"ds_read_b64_tr_b16 a[118:119], v37 offset:2304\n"
"s_waitcnt lgkmcnt(4)\n"
"v_mfma_f32_16x16x32_bf16 v[192:195], v[188:191], a[20:23], v[192:195]\n"
"v_mfma_f32_16x16x32_bf16 v[196:199], v[188:191], a[52:55], v[196:199]\n"
"s_waitcnt vmcnt(18)\n"
"ds_read_b64_tr_b16 a[148:149], v36 offset:4096\n"
"ds_read_b64_tr_b16 a[150:151], v36 offset:4352\n"
"ds_read_b64_tr_b16 a[180:181], v37 offset:4096\n"
"ds_read_b64_tr_b16 a[182:183], v37 offset:4352\n"
"s_waitcnt lgkmcnt(4)\n"
"v_mfma_f32_16x16x32_bf16 v[200:203], v[188:191], a[84:87], v[200:203]\n"
"v_mfma_f32_16x16x32_bf16 v[204:207], v[188:191], a[116:119], v[204:207]\n"
"s_waitcnt vmcnt(16)\n"
"ds_read_b64_tr_b16 a[212:213], v36 offset:6144\n"
"ds_read_b64_tr_b16 a[214:215], v36 offset:6400\n"
"ds_read_b64_tr_b16 a[244:245], v37 offset:6144\n"
"ds_read_b64_tr_b16 a[246:247], v37 offset:6400\n"
"s_waitcnt lgkmcnt(4)\n"
"v_mfma_f32_16x16x32_bf16 v[208:211], v[188:191], a[148:151], v[208:211]\n"
"v_mfma_f32_16x16x32_bf16 v[212:215], v[188:191], a[180:183], v[212:215]\n"
"s_waitcnt vmcnt(14)\n"
"ds_read_b64_tr_b16 a[24:25], v36 offset:8192\n"
"ds_read_b64_tr_b16 a[26:27], v36 offset:8448\n"
"ds_read_b64_tr_b16 a[56:57], v37 offset:8192\n"
"ds_read_b64_tr_b16 a[58:59], v37 offset:8448\n"
"s_waitcnt lgkmcnt(4)\n"
"v_mfma_f32_16x16x32_bf16 v[216:219], v[188:191], a[212:215], v[216:219]\n"
"v_mfma_f32_16x16x32_bf16 v[220:223], v[188:191], a[244:247], v[220:223]\n"
"s_waitcnt vmcnt(12)\n"
"ds_read_b64_tr_b16 a[88:89], v36 offset:10240\n"
"ds_read_b64_tr_b16 a[90:91], v36 offset:10496\n"
"ds_read_b64_tr_b16 a[120:121], v37 offset:10240\n"
"ds_read_b64_tr_b16 a[122:123], v37 offset:10496\n"
"s_waitcnt lgkmcnt(4)\n"
"v_mfma_f32_16x16x32_bf16 v[192:195], v[188:191], a[24:27], v[192:195]\n"
"v_mfma_f32_16x16x32_bf16 v[196:199], v[188:191], a[56:59], v[196:199]\n"
"s_waitcnt vmcnt(10)\n"
"ds_read_b64_tr_b16 a[152:153], v36 offset:12288\n"
"ds_read_b64_tr_b16 a[154:155], v36 offset:12544\n"
"ds_read_b64_tr_b16 a[184:185], v37 offset:12288\n"
"ds_read_b64_tr_b16 a[186:187], v37 offset:12544\n"
"s_waitcnt lgkmcnt(4)\n"
"v_mfma_f32_16x16x32_bf16 v[200:203], v[188:191], a[88:91], v[200:203]\n"
"v_mfma_f32_16x16x32_bf16 v[204:207], v[188:191], a[120:123], v[204:207]\n"
"s_waitcnt vmcnt(8)\n"
"ds_read_b64_tr_b16 a[216:217], v36 offset:14336\n"
"ds_read_b64_tr_b16 a[218:219], v36 offset:14592\n"
"ds_read_b64_tr_b16 a[248:249], v37 offset:14336\n"
"ds_read_b64_tr_b16 a[250:251], v37 offset:14592\n"
"s_waitcnt lgkmcnt(4)\n"
"v_mfma_f32_16x16x32_bf16 v[208:211], v[188:191], a[152:155], v[208:211]\n"
"v_mfma_f32_16x16x32_bf16 v[212:215], v[188:191], a[184:187], v[212:215]\n"
"s_waitcnt vmcnt(6)\n"
"ds_read_b64_tr_b16 a[28:29], v36 offset:16384\n"
"ds_read_b64_tr_b16 a[30:31], v36 offset:16640\n"
"ds_read_b64_tr_b16 a[60:61], v37 offset:16384\n"
"ds_read_b64_tr_b16 a[62:63], v37 offset:16640\n"
"s_waitcnt lgkmcnt(4)\n"
"v_mfma_f32_16x16x32_bf16 v[216:219], v[188:191], a[216:219], v[216:219]\n"
"v_mfma_f32_16x16x32_bf16 v[220:223], v[188:191], a[248:251], v[220:223]\n"
"s_waitcnt vmcnt(4)\n"
"ds_read_b64_tr_b16 a[92:93], v36 offset:18432\n"
"ds_read_b64_tr_b16 a[94:95], v36 offset:18688\n"
"ds_read_b64_tr_b16 a[124:125], v37 offset:18432\n"
"ds_read_b64_tr_b16 a[126:127], v37 offset:18688\n"
"s_waitcnt lgkmcnt(4)\n"
"v_mfma_f32_16x16x32_bf16 v[192:195], v[188:191], a[28:31], v[192:195]\n"
"v_mfma_f32_16x16x32_bf16 v[196:199], v[188:191], a[60:63], v[196:199]\n"
"s_waitcnt vmcnt(2)\n"
"ds_read_b64_tr_b16 a[156:157], v36 offset:20480\n"
"ds_read_b64_tr_b16 a[158:159], v36 offset:20736\n"
"ds_read_b64_tr_b16 a[188:189], v37 offset:20480\n"
"ds_read_b64_tr_b16 a[190:191], v37 offset:20736\n"
"s_waitcnt lgkmcnt(4)\n"
"v_mfma_f32_16x16x32_bf16 v[200:203], v[188:191], a[92:95], v[200:203]\n"
"v_mfma_f32_16x16x32_bf16 v[204:207], v[188:191], a[124:127], v[204:207]\n"
"s_waitcnt vmcnt(0)\n"
"ds_read_b64_tr_b16 a[220:221], v36 offset:22528\n"
"ds_read_b64_tr_b16 a[222:223], v36 offset:22784\n"
"ds_read_b64_tr_b16 a[252:253], v37 offset:22528\n"
"ds_read_b64_tr_b16 a[254:255], v37 offset:22784\n"
"s_waitcnt lgkmcnt(4)\n"
"v_mfma_f32_16x16x32_bf16 v[208:211], v[188:191], a[156:159], v[208:211]\n"
"v_mfma_f32_16x16x32_bf16 v[212:215], v[188:191], a[188:191], v[212:215]\n"
"s_waitcnt lgkmcnt(0)\n"
"v_mfma_f32_16x16x32_bf16 v[216:219], v[188:191], a[220:223], v[216:219]\n"
"v_mfma_f32_16x16x32_bf16 v[220:223], v[188:191], a[252:255], v[220:223]\n"
"s_mov_b32 m0, s57\n"
"s_barrier\n"
"ds_write_addtid_b32 v192 offset:0\n"
"ds_write_addtid_b32 v193 offset:4640\n"
"ds_write_addtid_b32 v194 offset:9344\n"
"ds_write_addtid_b32 v195 offset:13984\n"
"ds_write_addtid_b32 v196 offset:256\n"
"ds_write_addtid_b32 v197 offset:4896\n"
"ds_write_addtid_b32 v198 offset:9600\n"
"ds_write_addtid_b32 v199 offset:14240\n"
"ds_write_addtid_b32 v200 offset:512\n"
"ds_write_addtid_b32 v201 offset:5152\n"
"ds_write_addtid_b32 v202 offset:9856\n"
"ds_write_addtid_b32 v203 offset:14496\n"
"ds_write_addtid_b32 v204 offset:768\n"
"ds_write_addtid_b32 v205 offset:5408\n"
"ds_write_addtid_b32 v206 offset:10112\n"
"ds_write_addtid_b32 v207 offset:14752\n"
"ds_write_addtid_b32 v208 offset:1024\n"
"ds_write_addtid_b32 v209 offset:5664\n"
"ds_write_addtid_b32 v210 offset:10368\n"
"ds_write_addtid_b32 v211 offset:15008\n"
"ds_write_addtid_b32 v212 offset:1280\n"
"ds_write_addtid_b32 v213 offset:5920\n"
"ds_write_addtid_b32 v214 offset:10624\n"
"ds_write_addtid_b32 v215 offset:15264\n"
"ds_write_addtid_b32 v216 offset:1536\n"
"ds_write_addtid_b32 v217 offset:6176\n"
"ds_write_addtid_b32 v218 offset:10880\n"
"ds_write_addtid_b32 v219 offset:15520\n"
"ds_write_addtid_b32 v220 offset:1792\n"
"ds_write_addtid_b32 v221 offset:6432\n"
"ds_write_addtid_b32 v222 offset:11136\n"
"ds_write_addtid_b32 v223 offset:15776\n"
"s_waitcnt lgkmcnt(0)\n"
"s_barrier\n"
"ds_read_b128 v[96:99], v2 offset:0\n"
"ds_read_b128 v[100:103], v3 offset:512\n"
"ds_read_b128 v[104:107], v4 offset:1024\n"
"ds_read_b128 v[108:111], v42 offset:1536\n"
"ds_read_b128 v[112:115], v2 offset:256\n"
"ds_read_b128 v[116:119], v3 offset:768\n"
"ds_read_b128 v[120:123], v4 offset:1280\n"
"ds_read_b128 v[124:127], v42 offset:1792\n"
"s_waitcnt lgkmcnt(0)\n"
"s_barrier\n"
"v_add_f32 v96, v96, v100\n"
"v_add_f32 v104, v104, v108\n"
"v_add_f32 v18, v96, v104\n"
"v_add_f32 v97, v97, v101\n"
"v_add_f32 v105, v105, v109\n"
"v_add_f32 v19, v97, v105\n"
"v_add_f32 v98, v98, v102\n"
"v_add_f32 v106, v106, v110\n"
"v_add_f32 v20, v98, v106\n"
"v_add_f32 v99, v99, v103\n"
"v_add_f32 v107, v107, v111\n"
"v_add_f32 v21, v99, v107\n"
"v_add_f32 v112, v112, v116\n"
"v_add_f32 v120, v120, v124\n"
"v_add_f32 v22, v112, v120\n"
"v_add_f32 v113, v113, v117\n"
"v_add_f32 v121, v121, v125\n"
"v_add_f32 v23, v113, v121\n"
"v_add_f32 v114, v114, v118\n"
"v_add_f32 v122, v122, v126\n"
"v_add_f32 v24, v114, v122\n"
"v_add_f32 v115, v115, v119\n"
"v_add_f32 v123, v123, v127\n"
"v_add_f32 v25, v115, v123\n"
"v_mov_b32 v168, 0\n"
"v_mov_b32 v169, 0\n"
"v_mov_b32 v156, 0\n"
"v_mov_b32 v157, 0\n"
"v_mov_b32 v176, 0\n"
"v_mov_b32 v177, 0\n"
"v_mov_b32 v164, 0\n"
"v_mov_b32 v165, 0\n"
"s_mov_b32 s92, 0x80000000\n"
"s_mov_b32 s93, 0x80000000\n"
"s_mov_b32 s94, 0x80000000\n"
"s_mov_b32 s94, 0x80000000\n"
"s_mov_b32 s95, 0x80000000\n"
"s_mov_b32 s93, 0x80000000\n"
"s_mov_b32 s95, 0x80000000\n"
"s_mov_b32 s95, 0x80000000\n"
"s_mov_b32 s94, 0x80000000\n"
"s_cmp_eq_u32 s54, 0\n"
"s_cbranch_scc1 .LF_1s\n"
"s_cmp_eq_u32 s54, 1\n"
"s_cbranch_scc1 .LF_2s\n"
"s_cmp_eq_u32 s54, 2\n"
"s_cbranch_scc1 .LF_3s\n"
"s_cmp_eq_u32 s54, 3\n"
"s_cbranch_scc1 .LF_4s\n"
".p2align 6\n"
".LF_top:\n"
"v_mfma_f32_16x16x32_bf16 v[36:39], v[100:103], a[0:3], 0\n"
"ds_write_addtid_b32 v164 offset:18592\n"
"ds_write_addtid_b32 v165 offset:18848\n"
"v_mfma_f32_16x16x32_bf16 v[36:39], v[104:107], a[4:7], v[36:39]\n"
"v_cvt_pk_bf16_f32 v59, v50, v51\n"
"v_pk_max_i16 v58, v58, 0\n"
"v_mfma_f32_16x16x32_bf16 v[36:39], v[108:111], a[8:11], v[36:39]\n"
"ds_read2st64_b32 v[76:77], v14 offset0:0 offset1:1\n"
"v_pk_max_i16 v59, v59, 0\n"
"v_mfma_f32_16x16x32_bf16 v[36:39], v[112:115], a[12:15], v[36:39]\n"
"s_waitcnt vmcnt(12)\n"
"buffer_store_dwordx4 v[56:59], v15, s[40:43], s95 offen sc1\n"
"ds_read_b128 v[40:43], v2 offset:0\n"
"ds_read_b128 v[44:47], v3 offset:2048\n"
"ds_read_b128 v[48:51], v4 offset:1024\n"
"ds_read_b128 v[60:63], v2 offset:256\n"
"v_mfma_f32_16x16x32_bf16 v[36:39], v[116:119], a[16:19], v[36:39]\n"
"ds_read_b128 v[52:55], v3 offset:2304\n"
"ds_read_b128 v[56:59], v4 offset:1280\n"
"v_mfma_f32_16x16x32_bf16 v[36:39], v[120:123], a[20:23], v[36:39]\n"
"s_waitcnt vmcnt(11)\n"
"v_mfma_f32_16x16x32_bf16 v[36:39], v[124:127], a[24:27], v[36:39]\n"
"v_mfma_f32_16x16x32_bf16 v[36:39], v[128:131], a[28:31], v[36:39]\n"
"v_mfma_f32_16x16x32_bf16 v[64:67], v[100:103], a[32:35], 0\n"
"v_mfma_f32_16x16x32_bf16 v[64:67], v[104:107], a[36:39], v[64:67]\n"
"s_waitcnt lgkmcnt(3)\n"
"ds_write_addtid_b32 v36 offset:0\n"
"v_add_f32 v40, v40, v44\n"
"v_mfma_f32_16x16x32_bf16 v[64:67], v[108:111], a[40:43], v[64:67]\n"
"ds_write_addtid_b32 v37 offset:4640\n"
"v_add_f32 v41, v41, v45\n"
"v_mfma_f32_16x16x32_bf16 v[64:67], v[112:115], a[44:47], v[64:67]\n"
"ds_write_addtid_b32 v38 offset:9344\n"
"v_add_f32 v42, v42, v46\n"
"v_mfma_f32_16x16x32_bf16 v[64:67], v[116:119], a[48:51], v[64:67]\n"
"ds_write_addtid_b32 v39 offset:13984\n"
"v_add_f32 v43, v43, v47\n"
"v_mfma_f32_16x16x32_bf16 v[64:67], v[120:123], a[52:55], v[64:67]\n"
"v_add_f32 v40, v40, v48\n"
"v_add_f32 v41, v41, v49\n"
"v_mfma_f32_16x16x32_bf16 v[64:67], v[124:127], a[56:59], v[64:67]\n"
"v_add_f32 v42, v42, v50\n"
"v_add_f32 v43, v43, v51\n"
"v_mfma_f32_16x16x32_bf16 v[64:67], v[128:131], a[60:63], v[64:67]\n"
"v_add_f32 v40, v40, v168\n"
"v_add_f32 v41, v41, v169\n"
"v_mfma_f32_16x16x32_bf16 v[36:39], v[100:103], a[64:67], 0\n"
"v_add_f32 v42, v42, v170\n"
"v_add_f32 v43, v43, v171\n"
"v_mfma_f32_16x16x32_bf16 v[36:39], v[104:107], a[68:71], v[36:39]\n"
"s_add_u32 s95, s50, 0xffd00000\n"
"s_and_b32 s94, s95, 0xfbffffff\n"
"ds_write_addtid_b32 v64 offset:256\n"
"v_mfma_f32_16x16x32_bf16 v[36:39], v[108:111], a[72:75], v[36:39]\n"
"ds_write_addtid_b32 v65 offset:4896\n"
"v_fma_f32 v44, v18, v77, v26\n"
"v_mfma_f32_16x16x32_bf16 v[36:39], v[112:115], a[76:79], v[36:39]\n"
"ds_write_addtid_b32 v66 offset:9600\n"
"v_fma_f32 v45, v19, v77, v27\n"
"v_mfma_f32_16x16x32_bf16 v[36:39], v[116:119], a[80:83], v[36:39]\n"
"ds_write_addtid_b32 v67 offset:14240\n"
"v_fma_f32 v46, v20, v77, v28\n"
"v_mfma_f32_16x16x32_bf16 v[36:39], v[120:123], a[84:87], v[36:39]\n"
"v_fma_f32 v47, v21, v77, v29\n"
"v_fma_f32 v40, v40, v76, v44\n"
"v_mfma_f32_16x16x32_bf16 v[36:39], v[124:127], a[88:91], v[36:39]\n"
"v_fma_f32 v41, v41, v76, v45\n"
"v_fma_f32 v42, v42, v76, v46\n"
"v_mfma_f32_16x16x32_bf16 v[36:39], v[128:131], a[92:95], v[36:39]\n"
"v_fma_f32 v43, v43, v76, v47\n"
"v_mul_f32_e64 v40, v40, |v40|\n"
"v_mfma_f32_16x16x32_bf16 v[48:51], v[100:103], a[96:99], 0\n"
"v_mul_f32_e64 v41, v41, |v41|\n"
"v_mul_f32_e64 v42, v42, |v42|\n"
"v_mfma_f32_16x16x32_bf16 v[48:51], v[104:107], a[100:103], v[48:51]\n"
"ds_write_addtid_b32 v36 offset:512\n"
"v_mul_f32_e64 v43, v43, |v43|\n"
"v_mfma_f32_16x16x32_bf16 v[48:51], v[108:111], a[104:107], v[48:51]\n"
"ds_write_addtid_b32 v37 offset:5152\n"
"v_cvt_pk_bf16_f32 v64, v40, v41\n"
"v_mfma_f32_16x16x32_bf16 v[48:51], v[112:115], a[108:111], v[48:51]\n"
"ds_write_addtid_b32 v38 offset:9856\n"
"buffer_load_dwordx2 v[164:165], v9, s[40:43], s50 offen sc1\n"
"v_mfma_f32_16x16x32_bf16 v[48:51], v[116:119], a[112:115], v[48:51]\n"
"s_waitcnt vmcnt(11)\n"
"ds_write_addtid_b32 v39 offset:14496\n"
"v_cmp_ne_u64 vcc, s[48:49], v[176:177]\n"
"v_mfma_f32_16x16x32_bf16 v[48:51], v[120:123], a[116:119], v[48:51]\n"
"v_cvt_pk_bf16_f32 v65, v42, v43\n"
"v_pk_max_i16 v64, v64, 0\n"
"v_mfma_f32_16x16x32_bf16 v[48:51], v[124:127], a[120:123], v[48:51]\n"
"s_cbranch_vccnz .LF_3s\n"
".LF_3r:\n"
"v_pk_max_i16 v65, v65, 0\n"
"v_mfma_f32_16x16x32_bf16 v[48:51], v[128:131], a[124:127], v[48:51]\n"
"s_add_u32 s50, s50, 0x180000\n"
"s_and_b32 s56, s50, 0xfbffffff\n"
"v_mfma_f32_16x16x32_bf16 v[68:71], v[100:103], a[128:131], 0\n"
"s_waitcnt lgkmcnt(12)\n"
"buffer_load_dwordx4 v[36:39], v5, s[40:43], s56 offen offset:0 sc1\n"
"v_add_f32 v60, v60, v52\n"
"v_mfma_f32_16x16x32_bf16 v[68:71], v[104:107], a[132:135], v[68:71]\n"
"ds_write_addtid_b32 v48 offset:768\n"
"v_add_f32 v61, v61, v53\n"
"v_mfma_f32_16x16x32_bf16 v[68:71], v[108:111], a[136:139], v[68:71]\n"
"ds_write_addtid_b32 v49 offset:5408\n"
"v_add_f32 v62, v62, v54\n"
"v_mfma_f32_16x16x32_bf16 v[68:71], v[112:115], a[140:143], v[68:71]\n"
"ds_write_addtid_b32 v50 offset:10112\n"
"v_add_f32 v63, v63, v55\n"
"v_mfma_f32_16x16x32_bf16 v[68:71], v[116:119], a[144:147], v[68:71]\n"
"ds_write_addtid_b32 v51 offset:14752\n"
"buffer_load_dwordx4 v[40:43], v5, s[40:43], s56 offen offset:1024 sc1\n"
"v_mfma_f32_16x16x32_bf16 v[68:71], v[120:123], a[148:151], v[68:71]\n"
"v_add_f32 v60, v60, v56\n"
"v_add_f32 v61, v61, v57\n"
"v_mfma_f32_16x16x32_bf16 v[68:71], v[124:127], a[152:155], v[68:71]\n"
"v_add_f32 v62, v62, v58\n"
"v_add_f32 v63, v63, v59\n"
"v_mfma_f32_16x16x32_bf16 v[68:71], v[128:131], a[156:159], v[68:71]\n"
"v_add_f32 v60, v60, v172\n"
"v_add_f32 v61, v61, v173\n"
"v_mfma_f32_16x16x32_bf16 v[72:75], v[100:103], a[160:163], 0\n"
"buffer_load_dwordx4 v[44:47], v6, s[40:43], s56 offen offset:0 sc1\n"
"v_add_f32 v62, v62, v174\n"
"v_mfma_f32_16x16x32_bf16 v[72:75], v[104:107], a[164:167], v[72:75]\n"
"ds_write_addtid_b32 v68 offset:1024\n"
"v_add_f32 v63, v63, v175\n"
"v_mfma_f32_16x16x32_bf16 v[72:75], v[108:111], a[168:171], v[72:75]\n"
"ds_write_addtid_b32 v69 offset:5664\n"
"v_fma_f32 v56, v22, v77, v30\n"
"v_mfma_f32_16x16x32_bf16 v[72:75], v[112:115], a[172:175], v[72:75]\n"
"ds_write_addtid_b32 v70 offset:10368\n"
"v_fma_f32 v57, v23, v77, v31\n"
"v_mfma_f32_16x16x32_bf16 v[72:75], v[116:119], a[176:179], v[72:75]\n"
"ds_write_addtid_b32 v71 offset:15008\n"
"buffer_load_dwordx4 v[48:51], v6, s[40:43], s56 offen offset:1024 sc1\n"
"v_mfma_f32_16x16x32_bf16 v[72:75], v[120:123], a[180:183], v[72:75]\n"
"v_fma_f32 v58, v24, v77, v32\n"
"v_fma_f32 v59, v25, v77, v33\n"
"v_mfma_f32_16x16x32_bf16 v[72:75], v[124:127], a[184:187], v[72:75]\n"
"v_fma_f32 v60, v60, v76, v56\n"
"v_fma_f32 v61, v61, v76, v57\n"
"v_mfma_f32_16x16x32_bf16 v[72:75], v[128:131], a[188:191], v[72:75]\n"
"v_fma_f32 v62, v62, v76, v58\n"
"v_fma_f32 v63, v63, v76, v59\n"
"v_mfma_f32_16x16x32_bf16 v[68:71], a[192:195], v[100:103], 0\n"
"buffer_load_dwordx4 v[52:55], v7, s[40:43], s56 offen offset:0 sc1\n"
"v_mul_f32_e64 v60, v60, |v60|\n"
"v_mfma_f32_16x16x32_bf16 v[68:71], a[196:199], v[104:107], v[68:71]\n"
"ds_write_addtid_b32 v72 offset:1280\n"
"v_mul_f32_e64 v61, v61, |v61|\n"
"v_mfma_f32_16x16x32_bf16 v[68:71], a[200:203], v[108:111], v[68:71]\n"
"ds_write_addtid_b32 v73 offset:5920\n"
"v_mul_f32_e64 v62, v62, |v62|\n"
"v_mfma_f32_16x16x32_bf16 v[68:71], a[204:207], v[112:115], v[68:71]\n"
"ds_write_addtid_b32 v74 offset:10624\n"
"v_mul_f32_e64 v63, v63, |v63|\n"
"v_mfma_f32_16x16x32_bf16 v[68:71], a[208:211], v[116:119], v[68:71]\n"
"ds_write_addtid_b32 v75 offset:15264\n"
"buffer_load_dwordx4 v[56:59], v7, s[40:43], s56 offen offset:1024 sc1\n"
"v_mfma_f32_16x16x32_bf16 v[68:71], a[212:215], v[120:123], v[68:71]\n"
"v_cvt_pk_bf16_f32 v66, v60, v61\n"
"v_cvt_pk_bf16_f32 v67, v62, v63\n"
"v_mfma_f32_16x16x32_bf16 v[68:71], a[216:219], v[124:127], v[68:71]\n"
"v_pk_max_i16 v66, v66, 0\n"
"v_pk_max_i16 v67, v67, 0\n"
"v_mfma_f32_16x16x32_bf16 v[68:71], a[220:223], v[128:131], v[68:71]\n"
"buffer_store_dwordx4 v[64:67], v15, s[40:43], s94 offen sc1\n"
"v_mfma_f32_16x16x32_bf16 v[72:75], a[224:227], v[100:103], 0\n"
"s_waitcnt lgkmcnt(0)\n"
"buffer_load_dwordx4 v[60:63], v8, s[40:43], s56 offen offset:0 sc1\n"
"v_mfma_f32_16x16x32_bf16 v[72:75], a[228:231], v[104:107], v[72:75]\n"
"s_barrier\n"
"v_mfma_f32_16x16x32_bf16 v[72:75], a[232:235], v[108:111], v[72:75]\n"
"ds_read2st64_b32 v[166:167], v11 offset0:0 offset1:1\n"
"ds_read_b128 v[76:79], v2 offset:1536\n"
"v_mfma_f32_16x16x32_bf16 v[72:75], a[236:239], v[112:115], v[72:75]\n"
"ds_read_b128 v[80:83], v3 offset:512\n"
"ds_read_b128 v[84:87], v4 offset:2560\n"
"v_mfma_f32_16x16x32_bf16 v[72:75], a[240:243], v[116:119], v[72:75]\n"
"buffer_load_dwordx4 v[64:67], v8, s[40:43], s56 offen offset:1024 sc1\n"
"ds_read_b128 v[88:91], v2 offset:1792\n"
"v_mfma_f32_16x16x32_bf16 v[72:75], a[244:247], v[120:123], v[72:75]\n"
"ds_read_b128 v[92:95], v3 offset:768\n"
"ds_read_b128 v[96:99], v4 offset:2816\n"
"v_mfma_f32_16x16x32_bf16 v[72:75], a[248:251], v[124:127], v[72:75]\n"
"v_mfma_f32_16x16x32_bf16 v[72:75], a[252:255], v[128:131], v[72:75]\n"
"s_waitcnt vmcnt(18)\n"
"v_mfma_f32_16x16x32_bf16 v[100:103], v[132:135], a[0:3], 0\n"
"v_mfma_f32_16x16x32_bf16 v[100:103], v[136:139], a[4:7], v[100:103]\n"
"s_waitcnt vmcnt(16)\n"
"v_mfma_f32_16x16x32_bf16 v[100:103], v[140:143], a[8:11], v[100:103]\n"
"s_waitcnt lgkmcnt(3)\n"
"v_add_f32 v76, v76, v80\n"
"v_add_f32 v77, v77, v81\n"
"v_mfma_f32_16x16x32_bf16 v[100:103], v[144:147], a[12:15], v[100:103]\n"
"s_waitcnt vmcnt(13)\n"
"v_add_f32 v78, v78, v82\n"
"v_add_f32 v79, v79, v83\n"
"v_mfma_f32_16x16x32_bf16 v[100:103], v[148:151], a[16:19], v[100:103]\n"
"s_waitcnt vmcnt(10)\n"
"buffer_store_dwordx2 v[0:1], v16, s[40:43], s93 offen offset:0 sc1\n"
"v_add_f32 v76, v76, v84\n"
"v_add_f32 v77, v77, v85\n"
"v_mfma_f32_16x16x32_bf16 v[100:103], v[152:155], a[20:23], v[100:103]\n"
"v_add_f32 v78, v78, v86\n"
"v_add_f32 v79, v79, v87\n"
"v_mfma_f32_16x16x32_bf16 v[100:103], v[156:159], a[24:27], v[100:103]\n"
"v_add_f32 v76, v76, v68\n"
"v_add_f32 v77, v77, v69\n"
"v_mfma_f32_16x16x32_bf16 v[100:103], v[160:163], a[28:31], v[100:103]\n"
"v_add_f32 v78, v78, v70\n"
"v_add_f32 v79, v79, v71\n"
"v_mfma_f32_16x16x32_bf16 v[80:83], v[132:135], a[32:35], 0\n"
"s_add_u32 s93, s50, 0xffd00000\n"
"s_and_b32 s94, s93, 0xfbffffff\n"
"v_fma_f32 v84, v18, v167, v26\n"
"v_mfma_f32_16x16x32_bf16 v[80:83], v[136:139], a[36:39], v[80:83]\n"
"ds_write_addtid_b32 v100 offset:1536\n"
"v_fma_f32 v85, v19, v167, v27\n"
"v_mfma_f32_16x16x32_bf16 v[80:83], v[140:143], a[40:43], v[80:83]\n"
"ds_write_addtid_b32 v101 offset:6176\n"
"v_fma_f32 v86, v20, v167, v28\n"
"v_mfma_f32_16x16x32_bf16 v[80:83], v[144:147], a[44:47], v[80:83]\n"
"ds_write_addtid_b32 v102 offset:10880\n"
"v_fma_f32 v87, v21, v167, v29\n"
"v_mfma_f32_16x16x32_bf16 v[80:83], v[148:151], a[48:51], v[80:83]\n"
"ds_write_addtid_b32 v103 offset:15520\n"
"v_fma_f32 v76, v76, v166, v84\n"
"v_mfma_f32_16x16x32_bf16 v[80:83], v[152:155], a[52:55], v[80:83]\n"
"v_fma_f32 v77, v77, v166, v85\n"
"v_fma_f32 v78, v78, v166, v86\n"
"v_mfma_f32_16x16x32_bf16 v[80:83], v[156:159], a[56:59], v[80:83]\n"
"v_fma_f32 v79, v79, v166, v87\n"
"v_mul_f32_e64 v76, v76, |v76|\n"
"v_mfma_f32_16x16x32_bf16 v[80:83], v[160:163], a[60:63], v[80:83]\n"
"v_mul_f32_e64 v77, v77, |v77|\n"
"v_mul_f32_e64 v78, v78, |v78|\n"
"v_mfma_f32_16x16x32_bf16 v[68:71], v[132:135], a[64:67], 0\n"
"v_mul_f32_e64 v79, v79, |v79|\n"
"v_cvt_pk_bf16_f32 v84, v76, v77\n"
"v_mfma_f32_16x16x32_bf16 v[68:71], v[136:139], a[68:71], v[68:71]\n"
"ds_write_addtid_b32 v80 offset:1792\n"
"v_cvt_pk_bf16_f32 v85, v78, v79\n"
"v_mfma_f32_16x16x32_bf16 v[68:71], v[140:143], a[72:75], v[68:71]\n"
"ds_write_addtid_b32 v81 offset:6432\n"
"v_pk_max_i16 v84, v84, 0\n"
"v_mfma_f32_16x16x32_bf16 v[68:71], v[144:147], a[76:79], v[68:71]\n"
"ds_write_addtid_b32 v82 offset:11136\n"
"v_pk_max_i16 v85, v85, 0\n"
"v_mfma_f32_16x16x32_bf16 v[68:71], v[148:151], a[80:83], v[68:71]\n"
"s_waitcnt lgkmcnt(7)\n"
"ds_write_addtid_b32 v83 offset:15776\n"
"v_add_f32 v88, v88, v92\n"
"v_mfma_f32_16x16x32_bf16 v[68:71], v[152:155], a[84:87], v[68:71]\n"
"v_add_f32 v89, v89, v93\n"
"v_add_f32 v90, v90, v94\n"
"v_mfma_f32_16x16x32_bf16 v[68:71], v[156:159], a[88:91], v[68:71]\n"
"v_add_f32 v91, v91, v95\n"
"v_add_f32 v88, v88, v96\n"
"v_mfma_f32_16x16x32_bf16 v[68:71], v[160:163], a[92:95], v[68:71]\n"
"v_add_f32 v89, v89, v97\n"
"v_add_f32 v90, v90, v98\n"
"v_mfma_f32_16x16x32_bf16 v[76:79], v[132:135], a[96:99], 0\n"
"v_add_f32 v91, v91, v99\n"
"v_add_f32 v88, v88, v72\n"
"v_mfma_f32_16x16x32_bf16 v[76:79], v[136:139], a[100:103], v[76:79]\n"
"ds_write_addtid_b32 v68 offset:2048\n"
"v_add_f32 v89, v89, v73\n"
"v_mfma_f32_16x16x32_bf16 v[76:79], v[140:143], a[104:107], v[76:79]\n"
"ds_write_addtid_b32 v69 offset:6688\n"
"v_add_f32 v90, v90, v74\n"
"v_mfma_f32_16x16x32_bf16 v[76:79], v[144:147], a[108:111], v[76:79]\n"
"s_waitcnt vmcnt(10)\n"
"ds_write_addtid_b32 v70 offset:11392\n"
"v_cmp_ne_u64 vcc, s[48:49], v[164:165]\n"
"v_mfma_f32_16x16x32_bf16 v[76:79], v[148:151], a[112:115], v[76:79]\n"
"ds_write_addtid_b32 v71 offset:16032\n"
"buffer_load_dwordx2 v[168:169], v9, s[40:43], s50 offen sc1\n"
"v_mfma_f32_16x16x32_bf16 v[76:79], v[152:155], a[116:119], v[76:79]\n"
"s_cbranch_vccnz .LF_4s\n"
".LF_4r:\n"
"v_add_f32 v91, v91, v75\n"
"v_mfma_f32_16x16x32_bf16 v[76:79], v[156:159], a[120:123], v[76:79]\n"
"v_fma_f32 v80, v22, v167, v30\n"
"v_fma_f32 v81, v23, v167, v31\n"
"v_mfma_f32_16x16x32_bf16 v[76:79], v[160:163], a[124:127], v[76:79]\n"
"s_add_u32 s50, s50, 0x180000\n"
"s_and_b32 s56, s50, 0xfbffffff\n"
"v_fma_f32 v82, v24, v167, v32\n"
"v_mfma_f32_16x16x32_bf16 v[92:95], v[132:135], a[128:131], 0\n"
"buffer_load_dwordx4 v[68:71], v5, s[40:43], s56 offen offset:0 sc1\n"
"v_fma_f32 v83, v25, v167, v33\n"
"v_mfma_f32_16x16x32_bf16 v[92:95], v[136:139], a[132:135], v[92:95]\n"
"ds_write_addtid_b32 v76 offset:2304\n"
"v_fma_f32 v88, v88, v166, v80\n"
"v_mfma_f32_16x16x32_bf16 v[92:95], v[140:143], a[136:139], v[92:95]\n"
"ds_write_addtid_b32 v77 offset:6944\n"
"v_fma_f32 v89, v89, v166, v81\n"
"v_mfma_f32_16x16x32_bf16 v[92:95], v[144:147], a[140:143], v[92:95]\n"
"ds_write_addtid_b32 v78 offset:11648\n"
"v_fma_f32 v90, v90, v166, v82\n"
"v_mfma_f32_16x16x32_bf16 v[92:95], v[148:151], a[144:147], v[92:95]\n"
"ds_write_addtid_b32 v79 offset:16288\n"
"buffer_load_dwordx4 v[72:75], v5, s[40:43], s56 offen offset:1024 sc1\n"
"v_mfma_f32_16x16x32_bf16 v[92:95], v[152:155], a[148:151], v[92:95]\n"
"v_fma_f32 v91, v91, v166, v83\n"
"v_mul_f32_e64 v88, v88, |v88|\n"
"v_mfma_f32_16x16x32_bf16 v[92:95], v[156:159], a[152:155], v[92:95]\n"
"v_mul_f32_e64 v89, v89, |v89|\n"
"v_mul_f32_e64 v90, v90, |v90|\n"
"v_mfma_f32_16x16x32_bf16 v[92:95], v[160:163], a[156:159], v[92:95]\n"
"v_mul_f32_e64 v91, v91, |v91|\n"
"v_cvt_pk_bf16_f32 v86, v88, v89\n"
"v_mfma_f32_16x16x32_bf16 v[96:99], v[132:135], a[160:163], 0\n"
"buffer_load_dwordx4 v[76:79], v6, s[40:43], s56 offen offset:0 sc1\n"
"v_cvt_pk_bf16_f32 v87, v90, v91\n"
"v_mfma_f32_16x16x32_bf16 v[96:99], v[136:139], a[164:167], v[96:99]\n"
"ds_write_addtid_b32 v92 offset:2560\n"
"v_pk_max_i16 v86, v86, 0\n"
"v_mfma_f32_16x16x32_bf16 v[96:99], v[140:143], a[168:171], v[96:99]\n"
"ds_write_addtid_b32 v93 offset:7200\n"
"v_pk_max_i16 v87, v87, 0\n"
"v_mfma_f32_16x16x32_bf16 v[96:99], v[144:147], a[172:175], v[96:99]\n"
"buffer_store_dwordx4 v[84:87], v15, s[40:43], s94 offen sc1\n"
"ds_write_addtid_b32 v94 offset:11904\n"
"v_mfma_f32_16x16x32_bf16 v[96:99], v[148:151], a[176:179], v[96:99]\n"
"ds_write_addtid_b32 v95 offset:16544\n"
"buffer_load_dwordx4 v[80:83], v6, s[40:43], s56 offen offset:1024 sc1\n"
"v_mfma_f32_16x16x32_bf16 v[96:99], v[152:155], a[180:183], v[96:99]\n"
"v_mfma_f32_16x16x32_bf16 v[96:99], v[156:159], a[184:187], v[96:99]\n"
"v_mfma_f32_16x16x32_bf16 v[96:99], v[160:163], a[188:191], v[96:99]\n"
"v_mfma_f32_16x16x32_bf16 v[100:103], a[192:195], v[132:135], 0\n"
"buffer_load_dwordx4 v[84:87], v7, s[40:43], s56 offen offset:0 sc1\n"
"v_mfma_f32_16x16x32_bf16 v[100:103], a[196:199], v[136:139], v[100:103]\n"
"ds_write_addtid_b32 v96 offset:2816\n"
"v_mfma_f32_16x16x32_bf16 v[100:103], a[200:203], v[140:143], v[100:103]\n"
"ds_write_addtid_b32 v97 offset:7456\n"
"v_mfma_f32_16x16x32_bf16 v[100:103], a[204:207], v[144:147], v[100:103]\n"
"ds_write_addtid_b32 v98 offset:12160\n"
"v_mfma_f32_16x16x32_bf16 v[100:103], a[208:211], v[148:151], v[100:103]\n"
"ds_write_addtid_b32 v99 offset:16800\n"
"buffer_load_dwordx4 v[88:91], v7, s[40:43], s56 offen offset:1024 sc1\n"
"v_mfma_f32_16x16x32_bf16 v[100:103], a[212:215], v[152:155], v[100:103]\n"
"v_mfma_f32_16x16x32_bf16 v[100:103], a[216:219], v[156:159], v[100:103]\n"
"v_mfma_f32_16x16x32_bf16 v[100:103], a[220:223], v[160:163], v[100:103]\n"
"s_waitcnt vmcnt(11)\n"
"buffer_store_dwordx2 v[0:1], v16, s[40:43], s95 offen offset:0 sc1\n"
"v_mfma_f32_16x16x32_bf16 v[104:107], a[224:227], v[132:135], 0\n"
"s_waitcnt lgkmcnt(0)\n"
"buffer_load_dwordx4 v[92:95], v8, s[40:43], s56 offen offset:0 sc1\n"
"v_mfma_f32_16x16x32_bf16 v[104:107], a[228:231], v[136:139], v[104:107]\n"
"s_barrier\n"
"v_mfma_f32_16x16x32_bf16 v[104:107], a[232:235], v[140:143], v[104:107]\n"
"ds_read2st64_b32 v[170:171], v12 offset0:0 offset1:1\n"
"ds_read_b128 v[108:111], v2 offset:0\n"
"v_mfma_f32_16x16x32_bf16 v[104:107], a[236:239], v[144:147], v[104:107]\n"
"ds_read_b128 v[112:115], v3 offset:2048\n"
"ds_read_b128 v[116:119], v4 offset:1024\n"
"v_mfma_f32_16x16x32_bf16 v[104:107], a[240:243], v[148:151], v[104:107]\n"
"buffer_load_dwordx4 v[96:99], v8, s[40:43], s56 offen offset:1024 sc1\n"
"ds_read_b128 v[120:123], v2 offset:256\n"
"v_mfma_f32_16x16x32_bf16 v[104:107], a[244:247], v[152:155], v[104:107]\n"
"ds_read_b128 v[124:127], v3 offset:2304\n"
"ds_read_b128 v[128:131], v4 offset:1280\n"
"v_mfma_f32_16x16x32_bf16 v[104:107], a[248:251], v[156:159], v[104:107]\n"
"v_mfma_f32_16x16x32_bf16 v[104:107], a[252:255], v[160:163], v[104:107]\n"
"v_mfma_f32_16x16x32_bf16 v[132:135], v[36:39], a[0:3], 0\n"
"v_mfma_f32_16x16x32_bf16 v[132:135], v[40:43], a[4:7], v[132:135]\n"
"v_mfma_f32_16x16x32_bf16 v[132:135], v[44:47], a[8:11], v[132:135]\n"
"s_waitcnt lgkmcnt(3)\n"
"v_add_f32 v108, v108, v112\n"
"v_add_f32 v109, v109, v113\n"
"v_mfma_f32_16x16x32_bf16 v[132:135], v[48:51], a[12:15], v[132:135]\n"
"v_add_f32 v110, v110, v114\n"
"v_add_f32 v111, v111, v115\n"
"v_mfma_f32_16x16x32_bf16 v[132:135], v[52:55], a[16:19], v[132:135]\n"
"v_add_f32 v108, v108, v116\n"
"v_add_f32 v109, v109, v117\n"
"v_mfma_f32_16x16x32_bf16 v[132:135], v[56:59], a[20:23], v[132:135]\n"
"s_waitcnt vmcnt(12)\n"
"v_add_f32 v110, v110, v118\n"
"v_add_f32 v111, v111, v119\n"
"v_mfma_f32_16x16x32_bf16 v[132:135], v[60:63], a[24:27], v[132:135]\n"
"v_add_f32 v108, v108, v100\n"
"v_add_f32 v109, v109, v101\n"
"v_mfma_f32_16x16x32_bf16 v[132:135], v[64:67], a[28:31], v[132:135]\n"
"v_add_f32 v110, v110, v102\n"
"v_add_f32 v111, v111, v103\n"
"v_mfma_f32_16x16x32_bf16 v[112:115], v[36:39], a[32:35], 0\n"
"s_add_u32 s94, s50, 0xffd00000\n"
"s_and_b32 s95, s94, 0xfbffffff\n"
"v_fma_f32 v116, v18, v171, v26\n"
"v_mfma_f32_16x16x32_bf16 v[112:115], v[40:43], a[36:39], v[112:115]\n"
"ds_write_addtid_b32 v132 offset:0\n"
"v_fma_f32 v117, v19, v171, v27\n"
"v_mfma_f32_16x16x32_bf16 v[112:115], v[44:47], a[40:43], v[112:115]\n"
"ds_write_addtid_b32 v133 offset:4640\n"
"v_fma_f32 v118, v20, v171, v28\n"
"v_mfma_f32_16x16x32_bf16 v[112:115], v[48:51], a[44:47], v[112:115]\n"
"ds_write_addtid_b32 v134 offset:9344\n"
"v_fma_f32 v119, v21, v171, v29\n"
"v_mfma_f32_16x16x32_bf16 v[112:115], v[52:55], a[48:51], v[112:115]\n"
"ds_write_addtid_b32 v135 offset:13984\n"
"v_fma_f32 v108, v108, v170, v116\n"
"v_mfma_f32_16x16x32_bf16 v[112:115], v[56:59], a[52:55], v[112:115]\n"
"v_fma_f32 v109, v109, v170, v117\n"
"v_fma_f32 v110, v110, v170, v118\n"
"v_mfma_f32_16x16x32_bf16 v[112:115], v[60:63], a[56:59], v[112:115]\n"
"v_fma_f32 v111, v111, v170, v119\n"
"v_mul_f32_e64 v108, v108, |v108|\n"
"v_mfma_f32_16x16x32_bf16 v[112:115], v[64:67], a[60:63], v[112:115]\n"
"v_mul_f32_e64 v109, v109, |v109|\n"
"v_mul_f32_e64 v110, v110, |v110|\n"
"v_mfma_f32_16x16x32_bf16 v[100:103], v[36:39], a[64:67], 0\n"
"v_mul_f32_e64 v111, v111, |v111|\n"
"v_cvt_pk_bf16_f32 v132, v108, v109\n"
"v_mfma_f32_16x16x32_bf16 v[100:103], v[40:43], a[68:71], v[100:103]\n"
"ds_write_addtid_b32 v112 offset:256\n"
"v_cvt_pk_bf16_f32 v133, v110, v111\n"
"v_mfma_f32_16x16x32_bf16 v[100:103], v[44:47], a[72:75], v[100:103]\n"
"ds_write_addtid_b32 v113 offset:4896\n"
"v_pk_max_i16 v132, v132, 0\n"
"v_mfma_f32_16x16x32_bf16 v[100:103], v[48:51], a[76:79], v[100:103]\n"
"ds_write_addtid_b32 v114 offset:9600\n"
"v_pk_max_i16 v133, v133, 0\n"
"v_mfma_f32_16x16x32_bf16 v[100:103], v[52:55], a[80:83], v[100:103]\n"
"s_waitcnt lgkmcnt(7)\n"
"ds_write_addtid_b32 v115 offset:14240\n"
"v_add_f32 v120, v120, v124\n"
"v_mfma_f32_16x16x32_bf16 v[100:103], v[56:59], a[84:87], v[100:103]\n"
"v_add_f32 v121, v121, v125\n"
"v_add_f32 v122, v122, v126\n"
"v_mfma_f32_16x16x32_bf16 v[100:103], v[60:63], a[88:91], v[100:103]\n"
"v_add_f32 v123, v123, v127\n"
"v_add_f32 v120, v120, v128\n"
"v_mfma_f32_16x16x32_bf16 v[100:103], v[64:67], a[92:95], v[100:103]\n"
"v_add_f32 v121, v121, v129\n"
"v_add_f32 v122, v122, v130\n"
"v_mfma_f32_16x16x32_bf16 v[108:111], v[36:39], a[96:99], 0\n"
"v_add_f32 v123, v123, v131\n"
"v_add_f32 v120, v120, v104\n"
"v_mfma_f32_16x16x32_bf16 v[108:111], v[40:43], a[100:103], v[108:111]\n"
"ds_write_addtid_b32 v100 offset:512\n"
"v_add_f32 v121, v121, v105\n"
"v_mfma_f32_16x16x32_bf16 v[108:111], v[44:47], a[104:107], v[108:111]\n"
"ds_write_addtid_b32 v101 offset:5152\n"
"v_add_f32 v122, v122, v106\n"
"v_mfma_f32_16x16x32_bf16 v[108:111], v[48:51], a[108:111], v[108:111]\n"
"ds_write_addtid_b32 v102 offset:9856\n"
"buffer_load_dwordx2 v[156:157], v9, s[40:43], s50 offen sc1\n"
"v_mfma_f32_16x16x32_bf16 v[108:111], v[52:55], a[112:115], v[108:111]\n"
"s_waitcnt vmcnt(11)\n"
"ds_write_addtid_b32 v103 offset:14496\n"
"v_cmp_ne_u64 vcc, s[48:49], v[168:169]\n"
"v_mfma_f32_16x16x32_bf16 v[108:111], v[56:59], a[116:119], v[108:111]\n"
"v_add_f32 v123, v123, v107\n"
"v_fma_f32 v112, v22, v171, v30\n"
"v_mfma_f32_16x16x32_bf16 v[108:111], v[60:63], a[120:123], v[108:111]\n"
"s_cbranch_vccnz .LF_1s\n"
".LF_1r:\n"
"v_fma_f32 v113, v23, v171, v31\n"
"v_mfma_f32_16x16x32_bf16 v[108:111], v[64:67], a[124:127], v[108:111]\n"
"s_add_u32 s50, s50, 0x180000\n"
"s_and_b32 s56, s50, 0xfbffffff\n"
"v_fma_f32 v114, v24, v171, v32\n"
"v_mfma_f32_16x16x32_bf16 v[116:119], v[36:39], a[128:131], 0\n"
"s_add_u32 s92, s50, 0x0\n"
"s_and_b32 s92, s92, 0xfbffffff\n"
"buffer_load_dwordx4 v[100:103], v5, s[40:43], s56 offen offset:0 sc1\n"
"v_mfma_f32_16x16x32_bf16 v[116:119], v[40:43], a[132:135], v[116:119]\n"
"ds_write_addtid_b32 v108 offset:768\n"
"buffer_load_dwordx4 v[164:167], v10, s[40:43], s92 offen offset:0 sc1\n"
"v_mfma_f32_16x16x32_bf16 v[116:119], v[44:47], a[136:139], v[116:119]\n"
"ds_write_addtid_b32 v109 offset:5408\n"
"buffer_load_dwordx4 v[140:143], v10, s[40:43], s92 offen offset:1024 sc1\n"
"v_mfma_f32_16x16x32_bf16 v[116:119], v[48:51], a[140:143], v[116:119]\n"
"ds_write_addtid_b32 v110 offset:10112\n"
"buffer_load_dwordx4 v[148:151], v10, s[40:43], s92 offen offset:2048 sc1\n"
"v_mfma_f32_16x16x32_bf16 v[116:119], v[52:55], a[144:147], v[116:119]\n"
"ds_write_addtid_b32 v111 offset:14752\n"
"buffer_load_dwordx4 v[104:107], v5, s[40:43], s56 offen offset:1024 sc1\n"
"v_mfma_f32_16x16x32_bf16 v[116:119], v[56:59], a[148:151], v[116:119]\n"
"buffer_load_dwordx4 v[144:147], v10, s[40:43], s92 offen offset:3072 sc1\n"
"v_fma_f32 v115, v25, v171, v33\n"
"v_mfma_f32_16x16x32_bf16 v[116:119], v[60:63], a[152:155], v[116:119]\n"
"v_fma_f32 v120, v120, v170, v112\n"
"v_fma_f32 v121, v121, v170, v113\n"
"v_mfma_f32_16x16x32_bf16 v[116:119], v[64:67], a[156:159], v[116:119]\n"
"v_fma_f32 v122, v122, v170, v114\n"
"v_fma_f32 v123, v123, v170, v115\n"
"v_mfma_f32_16x16x32_bf16 v[124:127], v[36:39], a[160:163], 0\n"
"buffer_load_dwordx4 v[108:111], v6, s[40:43], s56 offen offset:0 sc1\n"
"v_mul_f32_e64 v120, v120, |v120|\n"
"v_mfma_f32_16x16x32_bf16 v[124:127], v[40:43], a[164:167], v[124:127]\n"
"ds_write_addtid_b32 v116 offset:1024\n"
"v_mul_f32_e64 v121, v121, |v121|\n"
"v_mfma_f32_16x16x32_bf16 v[124:127], v[44:47], a[168:171], v[124:127]\n"
"ds_write_addtid_b32 v117 offset:5664\n"
"v_mul_f32_e64 v122, v122, |v122|\n"
"v_mfma_f32_16x16x32_bf16 v[124:127], v[48:51], a[172:175], v[124:127]\n"
"s_waitcnt vmcnt(14)\n"
"buffer_store_dwordx2 v[0:1], v16, s[40:43], s93 offen offset:0 sc1\n"
"ds_write_addtid_b32 v118 offset:10368\n"
"v_mul_f32_e64 v123, v123, |v123|\n"
"v_mfma_f32_16x16x32_bf16 v[124:127], v[52:55], a[176:179], v[124:127]\n"
"ds_write_addtid_b32 v119 offset:15008\n"
"buffer_load_dwordx4 v[112:115], v6, s[40:43], s56 offen offset:1024 sc1\n"
"v_mfma_f32_16x16x32_bf16 v[124:127], v[56:59], a[180:183], v[124:127]\n"
"v_cvt_pk_bf16_f32 v134, v120, v121\n"
"v_cvt_pk_bf16_f32 v135, v122, v123\n"
"v_mfma_f32_16x16x32_bf16 v[124:127], v[60:63], a[184:187], v[124:127]\n"
"v_pk_max_i16 v134, v134, 0\n"
"v_pk_max_i16 v135, v135, 0\n"
"v_mfma_f32_16x16x32_bf16 v[124:127], v[64:67], a[188:191], v[124:127]\n"
"buffer_store_dwordx4 v[132:135], v15, s[40:43], s95 offen sc1\n"
"v_mfma_f32_16x16x32_bf16 v[136:139], a[192:195], v[36:39], 0\n"
"buffer_load_dwordx4 v[116:119], v7, s[40:43], s56 offen offset:0 sc1\n"
"v_mfma_f32_16x16x32_bf16 v[136:139], a[196:199], v[40:43], v[136:139]\n"
"ds_write_addtid_b32 v124 offset:1280\n"
"v_mfma_f32_16x16x32_bf16 v[136:139], a[200:203], v[44:47], v[136:139]\n"
"ds_write_addtid_b32 v125 offset:5920\n"
"v_mfma_f32_16x16x32_bf16 v[136:139], a[204:207], v[48:51], v[136:139]\n"
"ds_write_addtid_b32 v126 offset:10624\n"
"v_mfma_f32_16x16x32_bf16 v[136:139], a[208:211], v[52:55], v[136:139]\n"
"ds_write_addtid_b32 v127 offset:15264\n"
"buffer_load_dwordx4 v[120:123], v7, s[40:43], s56 offen offset:1024 sc1\n"
"v_mfma_f32_16x16x32_bf16 v[136:139], a[212:215], v[56:59], v[136:139]\n"
"v_mfma_f32_16x16x32_bf16 v[136:139], a[216:219], v[60:63], v[136:139]\n"
"v_mfma_f32_16x16x32_bf16 v[136:139], a[220:223], v[64:67], v[136:139]\n"
"v_mfma_f32_16x16x32_bf16 v[132:135], a[224:227], v[36:39], 0\n"
"s_waitcnt lgkmcnt(0)\n"
"buffer_load_dwordx4 v[124:127], v8, s[40:43], s56 offen offset:0 sc1\n"
"v_mfma_f32_16x16x32_bf16 v[132:135], a[228:231], v[40:43], v[132:135]\n"
"s_barrier\n"
"v_mfma_f32_16x16x32_bf16 v[132:135], a[232:235], v[44:47], v[132:135]\n"
"ds_read2st64_b32 v[158:159], v13 offset0:0 offset1:1\n"
"ds_read_b128 v[36:39], v2 offset:1536\n"
"v_mfma_f32_16x16x32_bf16 v[132:135], a[236:239], v[48:51], v[132:135]\n"
"ds_read_b128 v[40:43], v3 offset:512\n"
"ds_read_b128 v[44:47], v4 offset:2560\n"
"v_mfma_f32_16x16x32_bf16 v[132:135], a[240:243], v[52:55], v[132:135]\n"
"buffer_load_dwordx4 v[128:131], v8, s[40:43], s56 offen offset:1024 sc1\n"
"ds_read_b128 v[48:51], v2 offset:1792\n"
"v_mfma_f32_16x16x32_bf16 v[132:135], a[244:247], v[56:59], v[132:135]\n"
"ds_read_b128 v[52:55], v3 offset:768\n"
"ds_read_b128 v[152:155], v4 offset:2816\n"
"v_mfma_f32_16x16x32_bf16 v[132:135], a[248:251], v[60:63], v[132:135]\n"
"v_mfma_f32_16x16x32_bf16 v[132:135], a[252:255], v[64:67], v[132:135]\n"
"v_mfma_f32_16x16x32_bf16 v[56:59], v[68:71], a[0:3], 0\n"
"v_mfma_f32_16x16x32_bf16 v[56:59], v[72:75], a[4:7], v[56:59]\n"
"s_waitcnt vmcnt(20)\n"
"v_mfma_f32_16x16x32_bf16 v[56:59], v[76:79], a[8:11], v[56:59]\n"
"s_waitcnt lgkmcnt(3)\n"
"v_add_f32 v36, v36, v40\n"
"v_add_f32 v37, v37, v41\n"
"v_mfma_f32_16x16x32_bf16 v[56:59], v[80:83], a[12:15], v[56:59]\n"
"s_waitcnt vmcnt(18)\n"
"v_add_f32 v38, v38, v42\n"
"v_add_f32 v39, v39, v43\n"
"v_mfma_f32_16x16x32_bf16 v[56:59], v[84:87], a[16:19], v[56:59]\n"
"v_add_f32 v36, v36, v44\n"
"v_add_f32 v37, v37, v45\n"
"v_mfma_f32_16x16x32_bf16 v[56:59], v[88:91], a[20:23], v[56:59]\n"
"s_waitcnt vmcnt(15)\n"
"v_add_f32 v38, v38, v46\n"
"v_add_f32 v39, v39, v47\n"
"v_mfma_f32_16x16x32_bf16 v[56:59], v[92:95], a[24:27], v[56:59]\n"
"v_add_f32 v36, v36, v136\n"
"v_add_f32 v37, v37, v137\n"
"v_mfma_f32_16x16x32_bf16 v[56:59], v[96:99], a[28:31], v[56:59]\n"
"v_add_f32 v38, v38, v138\n"
"v_add_f32 v39, v39, v139\n"
"v_mfma_f32_16x16x32_bf16 v[40:43], v[68:71], a[32:35], 0\n"
"s_add_u32 s93, s50, 0xffd00000\n"
"s_and_b32 s95, s93, 0xfbffffff\n"
"v_fma_f32 v44, v18, v159, v26\n"
"v_mfma_f32_16x16x32_bf16 v[40:43], v[72:75], a[36:39], v[40:43]\n"
"ds_write_addtid_b32 v56 offset:1536\n"
"v_fma_f32 v45, v19, v159, v27\n"
"v_mfma_f32_16x16x32_bf16 v[40:43], v[76:79], a[40:43], v[40:43]\n"
"ds_write_addtid_b32 v57 offset:6176\n"
"v_fma_f32 v46, v20, v159, v28\n"
"v_mfma_f32_16x16x32_bf16 v[40:43], v[80:83], a[44:47], v[40:43]\n"
"ds_write_addtid_b32 v58 offset:10880\n"
"v_fma_f32 v47, v21, v159, v29\n"
"v_mfma_f32_16x16x32_bf16 v[40:43], v[84:87], a[48:51], v[40:43]\n"
"ds_write_addtid_b32 v59 offset:15520\n"
"v_fma_f32 v36, v36, v158, v44\n"
"v_mfma_f32_16x16x32_bf16 v[40:43], v[88:91], a[52:55], v[40:43]\n"
"v_fma_f32 v37, v37, v158, v45\n"
"v_fma_f32 v38, v38, v158, v46\n"
"v_mfma_f32_16x16x32_bf16 v[40:43], v[92:95], a[56:59], v[40:43]\n"
"v_fma_f32 v39, v39, v158, v47\n"
"v_mul_f32_e64 v36, v36, |v36|\n"
"v_mfma_f32_16x16x32_bf16 v[40:43], v[96:99], a[60:63], v[40:43]\n"
"v_mul_f32_e64 v37, v37, |v37|\n"
"v_mul_f32_e64 v38, v38, |v38|\n"
"v_mfma_f32_16x16x32_bf16 v[44:47], v[68:71], a[64:67], 0\n"
"v_mul_f32_e64 v39, v39, |v39|\n"
"v_cvt_pk_bf16_f32 v56, v36, v37\n"
"v_mfma_f32_16x16x32_bf16 v[44:47], v[72:75], a[68:71], v[44:47]\n"
"ds_write_addtid_b32 v40 offset:1792\n"
"v_cvt_pk_bf16_f32 v57, v38, v39\n"
"v_mfma_f32_16x16x32_bf16 v[44:47], v[76:79], a[72:75], v[44:47]\n"
"ds_write_addtid_b32 v41 offset:6432\n"
"v_pk_max_i16 v56, v56, 0\n"
"v_mfma_f32_16x16x32_bf16 v[44:47], v[80:83], a[76:79], v[44:47]\n"
"ds_write_addtid_b32 v42 offset:11136\n"
"v_pk_max_i16 v57, v57, 0\n"
"v_mfma_f32_16x16x32_bf16 v[44:47], v[84:87], a[80:83], v[44:47]\n"
"s_waitcnt lgkmcnt(7)\n"
"ds_write_addtid_b32 v43 offset:15776\n"
"v_add_f32 v48, v48, v52\n"
"v_mfma_f32_16x16x32_bf16 v[44:47], v[88:91], a[84:87], v[44:47]\n"
"v_add_f32 v49, v49, v53\n"
"v_add_f32 v50, v50, v54\n"
"v_mfma_f32_16x16x32_bf16 v[44:47], v[92:95], a[88:91], v[44:47]\n"
"v_add_f32 v51, v51, v55\n"
"v_add_f32 v48, v48, v152\n"
"v_mfma_f32_16x16x32_bf16 v[44:47], v[96:99], a[92:95], v[44:47]\n"
"v_add_f32 v49, v49, v153\n"
"v_add_f32 v50, v50, v154\n"
"v_mfma_f32_16x16x32_bf16 v[36:39], v[68:71], a[96:99], 0\n"
"v_add_f32 v51, v51, v155\n"
"v_add_f32 v48, v48, v132\n"
"v_mfma_f32_16x16x32_bf16 v[36:39], v[72:75], a[100:103], v[36:39]\n"
"ds_write_addtid_b32 v44 offset:2048\n"
"v_add_f32 v49, v49, v133\n"
"v_mfma_f32_16x16x32_bf16 v[36:39], v[76:79], a[104:107], v[36:39]\n"
"ds_write_addtid_b32 v45 offset:6688\n"
"v_add_f32 v50, v50, v134\n"
"v_mfma_f32_16x16x32_bf16 v[36:39], v[80:83], a[108:111], v[36:39]\n"
"ds_write_addtid_b32 v46 offset:11392\n"
"buffer_load_dwordx2 v[176:177], v9, s[40:43], s50 offen sc1\n"
"v_mfma_f32_16x16x32_bf16 v[36:39], v[84:87], a[112:115], v[36:39]\n"
"s_waitcnt vmcnt(15)\n"
"ds_write_addtid_b32 v47 offset:16032\n"
"v_cmp_ne_u64 vcc, s[48:49], v[156:157]\n"
"v_mfma_f32_16x16x32_bf16 v[36:39], v[88:91], a[116:119], v[36:39]\n"
"v_add_f32 v51, v51, v135\n"
"v_fma_f32 v40, v22, v159, v30\n"
"v_mfma_f32_16x16x32_bf16 v[36:39], v[92:95], a[120:123], v[36:39]\n"
"s_cbranch_vccnz .LF_2s\n"
".LF_2r:\n"
"v_fma_f32 v41, v23, v159, v31\n"
"v_mfma_f32_16x16x32_bf16 v[36:39], v[96:99], a[124:127], v[36:39]\n"
"s_add_u32 s50, s50, 0x180000\n"
"s_and_b32 s56, s50, 0xfbffffff\n"
"v_fma_f32 v42, v24, v159, v32\n"
"v_mfma_f32_16x16x32_bf16 v[44:47], v[68:71], a[128:131], 0\n"
"buffer_load_dwordx4 v[132:135], v5, s[40:43], s56 offen offset:0 sc1\n"
"v_fma_f32 v43, v25, v159, v33\n"
"v_mfma_f32_16x16x32_bf16 v[44:47], v[72:75], a[132:135], v[44:47]\n"
"ds_write_addtid_b32 v36 offset:2304\n"
"v_fma_f32 v48, v48, v158, v40\n"
"v_mfma_f32_16x16x32_bf16 v[44:47], v[76:79], a[136:139], v[44:47]\n"
"ds_write_addtid_b32 v37 offset:6944\n"
"v_fma_f32 v49, v49, v158, v41\n"
"v_mfma_f32_16x16x32_bf16 v[44:47], v[80:83], a[140:143], v[44:47]\n"
"ds_write_addtid_b32 v38 offset:11648\n"
"v_fma_f32 v50, v50, v158, v42\n"
"v_mfma_f32_16x16x32_bf16 v[44:47], v[84:87], a[144:147], v[44:47]\n"
"ds_write_addtid_b32 v39 offset:16288\n"
"buffer_load_dwordx4 v[136:139], v5, s[40:43], s56 offen offset:1024 sc1\n"
"v_mfma_f32_16x16x32_bf16 v[44:47], v[88:91], a[148:151], v[44:47]\n"
"s_waitcnt vmcnt(11)\n"
"v_add_f32 v164, v164, v140\n"
"v_add_f32 v165, v165, v141\n"
"v_mfma_f32_16x16x32_bf16 v[44:47], v[92:95], a[152:155], v[44:47]\n"
"v_add_f32 v166, v166, v142\n"
"v_add_f32 v167, v167, v143\n"
"v_mfma_f32_16x16x32_bf16 v[44:47], v[96:99], a[156:159], v[44:47]\n"
"v_add_f32 v148, v148, v144\n"
"v_add_f32 v149, v149, v145\n"
"v_mfma_f32_16x16x32_bf16 v[36:39], v[68:71], a[160:163], 0\n"
"buffer_load_dwordx4 v[140:143], v6, s[40:43], s56 offen offset:0 sc1\n"
"v_add_f32 v150, v150, v146\n"
"v_mfma_f32_16x16x32_bf16 v[36:39], v[72:75], a[164:167], v[36:39]\n"
"ds_write_addtid_b32 v44 offset:2560\n"
"v_add_f32 v151, v151, v147\n"
"v_mfma_f32_16x16x32_bf16 v[36:39], v[76:79], a[168:171], v[36:39]\n"
"ds_write_addtid_b32 v45 offset:7200\n"
"v_add_f32 v164, v164, v148\n"
"v_mfma_f32_16x16x32_bf16 v[36:39], v[80:83], a[172:175], v[36:39]\n"
"ds_write_addtid_b32 v46 offset:11904\n"
"v_add_f32 v165, v165, v149\n"
"v_mfma_f32_16x16x32_bf16 v[36:39], v[84:87], a[176:179], v[36:39]\n"
"ds_write_addtid_b32 v47 offset:16544\n"
"buffer_load_dwordx4 v[144:147], v6, s[40:43], s56 offen offset:1024 sc1\n"
"v_mfma_f32_16x16x32_bf16 v[36:39], v[88:91], a[180:183], v[36:39]\n"
"v_add_f32 v166, v166, v150\n"
"v_add_f32 v167, v167, v151\n"
"v_mfma_f32_16x16x32_bf16 v[36:39], v[92:95], a[184:187], v[36:39]\n"
"v_permlane32_swap_b32 v164, v166\n"
"v_mfma_f32_16x16x32_bf16 v[36:39], v[96:99], a[188:191], v[36:39]\n"
"s_waitcnt vmcnt(9)\n"
"buffer_store_dwordx2 v[0:1], v16, s[40:43], s94 offen offset:0 sc1\n"
"v_permlane32_swap_b32 v165, v167\n"
"v_mfma_f32_16x16x32_bf16 v[168:171], a[192:195], v[68:71], 0\n"
"buffer_load_dwordx4 v[148:151], v7, s[40:43], s56 offen offset:0 sc1\n"
"v_add_f32 v164, v164, v166\n"
"v_mfma_f32_16x16x32_bf16 v[168:171], a[196:199], v[72:75], v[168:171]\n"
"ds_write_addtid_b32 v36 offset:2816\n"
"v_add_f32 v165, v165, v167\n"
"v_mfma_f32_16x16x32_bf16 v[168:171], a[200:203], v[76:79], v[168:171]\n"
"ds_write_addtid_b32 v37 offset:7456\n"
"v_fma_f32 v51, v51, v158, v43\n"
"v_mfma_f32_16x16x32_bf16 v[168:171], a[204:207], v[80:83], v[168:171]\n"
"ds_write_addtid_b32 v38 offset:12160\n"
"v_mul_f32_e64 v48, v48, |v48|\n"
"v_mfma_f32_16x16x32_bf16 v[168:171], a[208:211], v[84:87], v[168:171]\n"
"ds_write_addtid_b32 v39 offset:16800\n"
"buffer_load_dwordx4 v[152:155], v7, s[40:43], s56 offen offset:1024 sc1\n"
"v_mfma_f32_16x16x32_bf16 v[168:171], a[212:215], v[88:91], v[168:171]\n"
"v_permlane16_swap_b32 v164, v165\n"
"v_mfma_f32_16x16x32_bf16 v[168:171], a[216:219], v[92:95], v[168:171]\n"
"v_add_f32 v164, v164, v165\n"
"v_mul_f32_e64 v49, v49, |v49|\n"
"v_mfma_f32_16x16x32_bf16 v[168:171], a[220:223], v[96:99], v[168:171]\n"
"v_add_f32_dpp v165, v164, v164 row_ror:8 row_mask:0xf bank_mask:0xf\n"
"v_mov_b32 v164, v165\n"
"v_mfma_f32_16x16x32_bf16 v[172:175], a[224:227], v[68:71], 0\n"
"s_waitcnt lgkmcnt(0)\n"
"buffer_load_dwordx4 v[156:159], v8, s[40:43], s56 offen offset:0 sc1\n"
"v_mul_f32_e64 v50, v50, |v50|\n"
"v_mfma_f32_16x16x32_bf16 v[172:175], a[228:231], v[72:75], v[172:175]\n"
"s_barrier\n"
"v_mfma_f32_16x16x32_bf16 v[172:175], a[232:235], v[76:79], v[172:175]\n"
"v_permlane16_swap_b32 v165, v164\n"
"v_mfma_f32_16x16x32_bf16 v[172:175], a[236:239], v[80:83], v[172:175]\n"
"v_mul_f32 v165, 0xba800000, v165\n"
"v_fma_f32 v164, v164, s60, v17\n"
"v_mfma_f32_16x16x32_bf16 v[172:175], a[240:243], v[84:87], v[172:175]\n"
"buffer_load_dwordx4 v[160:163], v8, s[40:43], s56 offen offset:1024 sc1\n"
"v_fma_f32 v164, -v165, v165, v164\n"
"v_mfma_f32_16x16x32_bf16 v[172:175], a[244:247], v[88:91], v[172:175]\n"
"v_max_f32 v164, v17, v164\n"
"v_mul_f32_e64 v51, v51, |v51|\n"
"v_mfma_f32_16x16x32_bf16 v[172:175], a[248:251], v[92:95], v[172:175]\n"
"v_rsq_f32 v164, v164\n"
"v_mfma_f32_16x16x32_bf16 v[172:175], a[252:255], v[96:99], v[172:175]\n"
"v_mul_f32 v165, v165, v164\n"
"v_cvt_pk_bf16_f32 v58, v48, v49\n"
"s_branch .LF_top\n"
".LF_1s:\n"
"s_nop 7\n"
"s_cmp_ge_u32 s50, s53\n"
"s_cbranch_scc1 .LF_end\n"
"s_cmp_ge_u32 s50, s52\n"
"s_cbranch_scc1 .LF_1r\n"
"s_sub_u32 s58, s50, 0x180000\n"
"s_mov_b32 s59, 200000\n"
".LF_1p:\n"
"buffer_load_dwordx2 v[168:169], v9, s[40:43], s58 offen sc1\n"
"s_waitcnt vmcnt(0)\n"
"v_cmp_ne_u64 vcc, s[48:49], v[168:169]\n"
"s_cbranch_vccz .LF_1px\n"
"s_sleep 1\n"
"s_sub_u32 s59, s59, 1\n"
"s_cbranch_scc0 .LF_1p\n"
"s_branch .LF_end\n"
".LF_1px:\n"
"s_branch .LF_1r\n"
".LF_2s:\n"
"s_nop 7\n"
"s_cmp_ge_u32 s50, s53\n"
"s_cbranch_scc1 .LF_end\n"
"s_cmp_ge_u32 s50, s52\n"
"s_cbranch_scc1 .LF_2r\n"
"s_sub_u32 s58, s50, 0x180000\n"
"s_mov_b32 s59, 200000\n"
".LF_2p:\n"
"buffer_load_dwordx2 v[156:157], v9, s[40:43], s58 offen sc1\n"
"s_waitcnt vmcnt(0)\n"
"v_cmp_ne_u64 vcc, s[48:49], v[156:157]\n"
"s_cbranch_vccz .LF_2px\n"
"s_sleep 1\n"
"s_sub_u32 s59, s59, 1\n"
"s_cbranch_scc0 .LF_2p\n"
"s_branch .LF_end\n"
".LF_2px:\n"
"s_branch .LF_2r\n"
".LF_3s:\n"
"s_nop 7\n"
"s_cmp_ge_u32 s50, s53\n"
"s_cbranch_scc1 .LF_end\n"
"s_cmp_ge_u32 s50, s52\n"
"s_cbranch_scc1 .LF_3r\n"
"s_sub_u32 s58, s50, 0x180000\n"
"s_mov_b32 s59, 200000\n"
".LF_3p:\n"
"buffer_load_dwordx2 v[176:177], v9, s[40:43], s58 offen sc1\n"
"s_waitcnt vmcnt(0)\n"
"v_cmp_ne_u64 vcc, s[48:49], v[176:177]\n"
"s_cbranch_vccz .LF_3px\n"
"s_sleep 1\n"
"s_sub_u32 s59, s59, 1\n"
"s_cbranch_scc0 .LF_3p\n"
"s_branch .LF_end\n"
".LF_3px:\n"
"s_branch .LF_3r\n"
".LF_4s:\n"
"s_nop 7\n"
"s_cmp_ge_u32 s50, s53\n"
"s_cbranch_scc1 .LF_end\n"
"s_cmp_ge_u32 s50, s52\n"
"s_cbranch_scc1 .LF_4r\n"
"s_sub_u32 s58, s50, 0x180000\n"
"s_mov_b32 s59, 200000\n"
".LF_4p:\n"
"buffer_load_dwordx2 v[164:165], v9, s[40:43], s58 offen sc1\n"
"s_waitcnt vmcnt(0)\n"
"v_cmp_ne_u64 vcc, s[48:49], v[164:165]\n"
"s_cbranch_vccz .LF_4px\n"
"s_sleep 1\n"
"s_sub_u32 s59, s59, 1\n"
"s_cbranch_scc0 .LF_4p\n"
"s_branch .LF_end\n"
".LF_4px:\n"
"s_branch .LF_4r\n"
".LF_end:\n"
"s_waitcnt vmcnt(0) lgkmcnt(0)\n"
"s_endpgm\n"
      :: "{v2}"((unsigned)(lds0 + ((wid + 3) & 3) * 19104 + trp)), "{v3}"((unsigned)(lds0 + ((wid + 2) & 3) * 19104 + trp)), "{v4}"((unsigned)(lds0 + ((wid + 1) & 3) * 19104 + trp)), "{v5}"((unsigned)(lane * 16 + wid * 8192 + ((0 + rot) & 3) * 2048 + 65536)), "{v6}"((unsigned)(lane * 16 + wid * 8192 + ((1 + rot) & 3) * 2048 + 65536)), "{v7}"((unsigned)(lane * 16 + wid * 8192 + ((2 + rot) & 3) * 2048 + 65536)), "{v8}"((unsigned)(lane * 16 + wid * 8192 + ((3 + rot) & 3) * 2048 + 65536)), "{v9}"((unsigned)((lane & 31) * 8 + 352768 + F_GL * 1572864)), "{v10}"((unsigned)(lane * 16 + 344064)), "{v11}"((unsigned)(lds0 + ((wid + 0) & 3) * 19104 + 18592 + ((li >> 1) + 32 * (li & 1)) * 4)), "{v12}"((unsigned)(lds0 + ((wid + 1) & 3) * 19104 + 18592 + ((li >> 1) + 32 * (li & 1)) * 4)), "{v13}"((unsigned)(lds0 + ((wid + 2) & 3) * 19104 + 18592 + ((li >> 1) + 32 * (li & 1)) * 4)), "{v14}"((unsigned)(lds0 + ((wid + 3) & 3) * 19104 + 18592 + ((li >> 1) + 32 * (li & 1)) * 4)), "{v15}"((unsigned)(lane * 16 + pair * 1024 + 98304)), "{v16}"((unsigned)(lane ? DROP : pair * 8 + 353024)), "{v17}"((unsigned)(0x3727c5ac)), "{v26}"((unsigned)(bia[0])), "{v27}"((unsigned)(bia[1])), "{v28}"((unsigned)(bia[2])), "{v29}"((unsigned)(bia[3])), "{v30}"((unsigned)(bia[4])), "{v31}"((unsigned)(bia[5])), "{v32}"((unsigned)(bia[6])), "{v33}"((unsigned)(bia[7])), "{v36}"((unsigned)(vT + tsw)), "{v37}"((unsigned)(vT + (tsw ^ 8))), "{v38}"((unsigned)(vD + ((0 + rot) & 3) * 458752)), "{v39}"((unsigned)(vD + ((1 + rot) & 3) * 458752)), "{v40}"((unsigned)(vD + ((2 + rot) & 3) * 458752)), "{v41}"((unsigned)(vD + ((3 + rot) & 3) * 458752)), "{v42}"((unsigned)(lds0 + wid * 19104 + trp)), "{s[40:43]}"(rS), "{s[44:47]}"(rW), "{s[48:49]}"(wsp), "{s50}"((unsigned)(sA)), "{s51}"((unsigned)(sLo)), "{s52}"((unsigned)(sHi)), "{s53}"((unsigned)(sEx)), "{s54}"((unsigned)(sEnt)), "{s55}"((unsigned)(sLd)), "{s56}"((unsigned)(sA & 0xfbffffff)), "{s57}"((unsigned)(lds0 + wid * 19104)), "{s60}"((unsigned)(0x3a800000)), "{s61}"((unsigned)(sW0 + 64 * ((wid + 1) & 3))), "{s62}"((unsigned)(sW0 + 64 * ((wid + 2) & 3))), "{s63}"((unsigned)(sW0 + 64 * ((wid + 3) & 3))), "{s64}"((unsigned)(sW0 + 64 * ((wid + 4) & 3)))
      : "v255", "a255", "s95", "vcc", "scc", "memory");

  } else {
    const int kq = idx / 7, grp = idx % 7, f0 = kq * 28 + wid * 7, pg0 = grp < 4 ? grp * 5 : 4 + grp * 4, pair = pg0 + wid;
    const unsigned tsw = (kq & 1) * 8, sW0 = f0 * 32 * 2048 + pg0 * 64, vD = (lane >> 2) * 2048 + (lane & 3) * 16;
    const u32x4 rW = desc(a.w2, 3584 * 2048);
    if (idx == 0) {
      const unsigned sEx = sHi + H_EX * 3 * RS, sEnt = -wid & 3;
    asm volatile(
"s_nop 4\n"
"v_mov_b32 v186, 0\n"
"global_load_dwordx2 v[0:1], v186, s[48:49] sc1\n"
"v_mov_b32 v188, 0x3f803f80\n"
"v_mov_b32 v189, 0x3f803f80\n"
"v_mov_b32 v190, 0x3f803f80\n"
"v_mov_b32 v191, 0x3f803f80\n"
"s_add_u32 m0, s55, 0\n"
"s_add_u32 s58, s61, 0\n"
"buffer_load_dwordx4 v42, s[44:47], s58 offen lds\n"
"s_add_u32 m0, s55, 1024\n"
"s_add_u32 s58, s61, 32768\n"
"buffer_load_dwordx4 v42, s[44:47], s58 offen lds\n"
"s_add_u32 m0, s55, 2048\n"
"s_add_u32 s58, s62, 0\n"
"buffer_load_dwordx4 v42, s[44:47], s58 offen lds\n"
"s_add_u32 m0, s55, 3072\n"
"s_add_u32 s58, s62, 32768\n"
"buffer_load_dwordx4 v42, s[44:47], s58 offen lds\n"
"s_add_u32 m0, s55, 4096\n"
"s_add_u32 s58, s63, 0\n"
"buffer_load_dwordx4 v42, s[44:47], s58 offen lds\n"
"s_add_u32 m0, s55, 5120\n"
"s_add_u32 s58, s63, 32768\n"
"buffer_load_dwordx4 v42, s[44:47], s58 offen lds\n"
"s_add_u32 m0, s55, 6144\n"
"s_add_u32 s58, s64, 0\n"
"buffer_load_dwordx4 v42, s[44:47], s58 offen lds\n"
"s_add_u32 m0, s55, 7168\n"
"s_add_u32 s58, s64, 32768\n"
"buffer_load_dwordx4 v42, s[44:47], s58 offen lds\n"
"s_add_u32 m0, s55, 8192\n"
"s_add_u32 s58, s65, 0\n"
"buffer_load_dwordx4 v42, s[44:47], s58 offen lds\n"
"s_add_u32 m0, s55, 9216\n"
"s_add_u32 s58, s65, 32768\n"
"buffer_load_dwordx4 v42, s[44:47], s58 offen lds\n"
"s_add_u32 m0, s55, 10240\n"
"s_add_u32 s58, s61, 65536\n"
"buffer_load_dwordx4 v42, s[44:47], s58 offen lds\n"
"s_add_u32 m0, s55, 11264\n"
"s_add_u32 s58, s61, 98304\n"
"buffer_load_dwordx4 v42, s[44:47], s58 offen lds\n"
"s_add_u32 m0, s55, 12288\n"
"s_add_u32 s58, s62, 65536\n"
"buffer_load_dwordx4 v42, s[44:47], s58 offen lds\n"
"s_add_u32 m0, s55, 13312\n"
"s_add_u32 s58, s62, 98304\n"
"buffer_load_dwordx4 v42, s[44:47], s58 offen lds\n"
"s_add_u32 m0, s55, 14336\n"
"s_add_u32 s58, s63, 65536\n"
"buffer_load_dwordx4 v42, s[44:47], s58 offen lds\n"
"s_add_u32 m0, s55, 15360\n"
"s_add_u32 s58, s63, 98304\n"
"buffer_load_dwordx4 v42, s[44:47], s58 offen lds\n"
"s_add_u32 m0, s55, 16384\n"
"s_add_u32 s58, s64, 65536\n"
"buffer_load_dwordx4 v42, s[44:47], s58 offen lds\n"
"s_add_u32 m0, s55, 17408\n"
"s_add_u32 s58, s64, 98304\n"
"buffer_load_dwordx4 v42, s[44:47], s58 offen lds\n"
"s_add_u32 m0, s55, 18432\n"
"s_add_u32 s58, s65, 65536\n"
"buffer_load_dwordx4 v42, s[44:47], s58 offen lds\n"
"s_add_u32 m0, s55, 19456\n"
"s_add_u32 s58, s65, 98304\n"
"buffer_load_dwordx4 v42, s[44:47], s58 offen lds\n"
"s_add_u32 m0, s55, 20480\n"
"s_add_u32 s58, s61, 131072\n"
"buffer_load_dwordx4 v42, s[44:47], s58 offen lds\n"
"s_add_u32 m0, s55, 21504\n"
"s_add_u32 s58, s61, 163840\n"
"buffer_load_dwordx4 v42, s[44:47], s58 offen lds\n"
"s_add_u32 m0, s55, 22528\n"
"s_add_u32 s58, s62, 131072\n"
"buffer_load_dwordx4 v42, s[44:47], s58 offen lds\n"
"s_add_u32 m0, s55, 23552\n"
"s_add_u32 s58, s62, 163840\n"
"buffer_load_dwordx4 v42, s[44:47], s58 offen lds\n"
"s_add_u32 m0, s55, 24576\n"
"s_add_u32 s58, s63, 131072\n"
"buffer_load_dwordx4 v42, s[44:47], s58 offen lds\n"
"s_add_u32 m0, s55, 25600\n"
"s_add_u32 s58, s63, 163840\n"
"buffer_load_dwordx4 v42, s[44:47], s58 offen lds\n"
"s_add_u32 m0, s55, 26624\n"
"s_add_u32 s58, s64, 131072\n"
"buffer_load_dwordx4 v42, s[44:47], s58 offen lds\n"
"s_add_u32 m0, s55, 27648\n"
"s_add_u32 s58, s64, 163840\n"
"buffer_load_dwordx4 v42, s[44:47], s58 offen lds\n"
"s_add_u32 m0, s55, 28672\n"
"s_add_u32 s58, s65, 131072\n"
"buffer_load_dwordx4 v42, s[44:47], s58 offen lds\n"
"s_add_u32 m0, s55, 29696\n"
"s_add_u32 s58, s65, 163840\n"
"buffer_load_dwordx4 v42, s[44:47], s58 offen lds\n"
"s_add_u32 m0, s55, 30720\n"
"s_add_u32 s58, s61, 196608\n"
"buffer_load_dwordx4 v42, s[44:47], s58 offen lds\n"
"s_add_u32 m0, s55, 31744\n"
"s_add_u32 s58, s61, 229376\n"
"buffer_load_dwordx4 v42, s[44:47], s58 offen lds\n"
"s_add_u32 m0, s55, 32768\n"
"s_add_u32 s58, s62, 196608\n"
"buffer_load_dwordx4 v42, s[44:47], s58 offen lds\n"
"s_add_u32 m0, s55, 33792\n"
"s_add_u32 s58, s62, 229376\n"
"buffer_load_dwordx4 v42, s[44:47], s58 offen lds\n"
"s_add_u32 m0, s55, 34816\n"
"s_add_u32 s58, s63, 196608\n"
"buffer_load_dwordx4 v42, s[44:47], s58 offen lds\n"
"s_add_u32 m0, s55, 35840\n"
"s_add_u32 s58, s63, 229376\n"
"buffer_load_dwordx4 v42, s[44:47], s58 offen lds\n"
"s_add_u32 m0, s55, 36864\n"
"s_add_u32 s58, s64, 196608\n"
"buffer_load_dwordx4 v42, s[44:47], s58 offen lds\n"
"s_add_u32 m0, s55, 37888\n"
"s_add_u32 s58, s64, 229376\n"
"buffer_load_dwordx4 v42, s[44:47], s58 offen lds\n"
"s_add_u32 m0, s55, 38912\n"
"s_add_u32 s58, s65, 196608\n"
"buffer_load_dwordx4 v42, s[44:47], s58 offen lds\n"
"s_add_u32 m0, s55, 39936\n"
"s_add_u32 s58, s65, 229376\n"
"buffer_load_dwordx4 v42, s[44:47], s58 offen lds\n"
"s_waitcnt vmcnt(38)\n"
"ds_read_b64_tr_b16 a[0:1], v40 offset:0\n"
"ds_read_b64_tr_b16 a[2:3], v40 offset:256\n"
"ds_read_b64_tr_b16 a[28:29], v41 offset:0\n"
"ds_read_b64_tr_b16 a[30:31], v41 offset:256\n"
"v_add_u32 v0, 2, v0\n"
"v_add_u32 v1, 0x9e3779b9, v1\n"
"v_or_b32 v0, 1, v0\n"
"v_readfirstlane_b32 s49, v1\n"
"v_readfirstlane_b32 s48, v0\n"
"s_waitcnt vmcnt(36)\n"
"ds_read_b64_tr_b16 a[56:57], v40 offset:2048\n"
"ds_read_b64_tr_b16 a[58:59], v40 offset:2304\n"
"ds_read_b64_tr_b16 a[84:85], v41 offset:2048\n"
"ds_read_b64_tr_b16 a[86:87], v41 offset:2304\n"
"s_waitcnt lgkmcnt(4)\n"
"s_add_u32 m0, s55, 0\n"
"s_add_u32 s58, s61, 262144\n"
"buffer_load_dwordx4 v42, s[44:47], s58 offen lds\n"
"s_add_u32 m0, s55, 1024\n"
"s_add_u32 s58, s61, 294912\n"
"buffer_load_dwordx4 v42, s[44:47], s58 offen lds\n"
"s_waitcnt vmcnt(36)\n"
"ds_read_b64_tr_b16 a[112:113], v40 offset:4096\n"
"ds_read_b64_tr_b16 a[114:115], v40 offset:4352\n"
"ds_read_b64_tr_b16 a[140:141], v41 offset:4096\n"
"ds_read_b64_tr_b16 a[142:143], v41 offset:4352\n"
"s_waitcnt lgkmcnt(4)\n"
"s_add_u32 m0, s55, 2048\n"
"s_add_u32 s58, s62, 262144\n"
"buffer_load_dwordx4 v42, s[44:47], s58 offen lds\n"
"s_add_u32 m0, s55, 3072\n"
"s_add_u32 s58, s62, 294912\n"
"buffer_load_dwordx4 v42, s[44:47], s58 offen lds\n"
"s_waitcnt vmcnt(36)\n"
"ds_read_b64_tr_b16 a[168:169], v40 offset:6144\n"
"ds_read_b64_tr_b16 a[170:171], v40 offset:6400\n"
"ds_read_b64_tr_b16 a[196:197], v41 offset:6144\n"
"ds_read_b64_tr_b16 a[198:199], v41 offset:6400\n"
"s_waitcnt lgkmcnt(4)\n"
"s_add_u32 m0, s55, 4096\n"
"s_add_u32 s58, s63, 262144\n"
"buffer_load_dwordx4 v42, s[44:47], s58 offen lds\n"
"s_add_u32 m0, s55, 5120\n"
"s_add_u32 s58, s63, 294912\n"
"buffer_load_dwordx4 v42, s[44:47], s58 offen lds\n"
"s_waitcnt vmcnt(36)\n"
"ds_read_b64_tr_b16 a[224:225], v40 offset:8192\n"
"ds_read_b64_tr_b16 a[226:227], v40 offset:8448\n"
"ds_read_b64_tr_b16 a[252:253], v41 offset:8192\n"
"ds_read_b64_tr_b16 a[254:255], v41 offset:8448\n"
"s_waitcnt lgkmcnt(4)\n"
"s_add_u32 m0, s55, 6144\n"
"s_add_u32 s58, s64, 262144\n"
"buffer_load_dwordx4 v42, s[44:47], s58 offen lds\n"
"s_add_u32 m0, s55, 7168\n"
"s_add_u32 s58, s64, 294912\n"
"buffer_load_dwordx4 v42, s[44:47], s58 offen lds\n"
"s_waitcnt vmcnt(36)\n"
"ds_read_b64_tr_b16 a[4:5], v40 offset:10240\n"
"ds_read_b64_tr_b16 a[6:7], v40 offset:10496\n"
"ds_read_b64_tr_b16 a[32:33], v41 offset:10240\n"
"ds_read_b64_tr_b16 a[34:35], v41 offset:10496\n"
"s_waitcnt lgkmcnt(4)\n"
"s_add_u32 m0, s55, 8192\n"
"s_add_u32 s58, s65, 262144\n"
"buffer_load_dwordx4 v42, s[44:47], s58 offen lds\n"
"s_add_u32 m0, s55, 9216\n"
"s_add_u32 s58, s65, 294912\n"
"buffer_load_dwordx4 v42, s[44:47], s58 offen lds\n"
"s_waitcnt vmcnt(36)\n"
"ds_read_b64_tr_b16 a[60:61], v40 offset:12288\n"
"ds_read_b64_tr_b16 a[62:63], v40 offset:12544\n"
"ds_read_b64_tr_b16 a[88:89], v41 offset:12288\n"
"ds_read_b64_tr_b16 a[90:91], v41 offset:12544\n"
"s_waitcnt lgkmcnt(4)\n"
"s_add_u32 m0, s55, 10240\n"
"s_add_u32 s58, s61, 327680\n"
"buffer_load_dwordx4 v42, s[44:47], s58 offen lds\n"
"s_add_u32 m0, s55, 11264\n"
"s_add_u32 s58, s61, 360448\n"
"buffer_load_dwordx4 v42, s[44:47], s58 offen lds\n"
"s_waitcnt vmcnt(36)\n"
"ds_read_b64_tr_b16 a[116:117], v40 offset:14336\n"
"ds_read_b64_tr_b16 a[118:119], v40 offset:14592\n"
"ds_read_b64_tr_b16 a[144:145], v41 offset:14336\n"
"ds_read_b64_tr_b16 a[146:147], v41 offset:14592\n"
"s_waitcnt lgkmcnt(4)\n"
"s_add_u32 m0, s55, 12288\n"
"s_add_u32 s58, s62, 327680\n"
"buffer_load_dwordx4 v42, s[44:47], s58 offen lds\n"
"s_add_u32 m0, s55, 13312\n"
"s_add_u32 s58, s62, 360448\n"
"buffer_load_dwordx4 v42, s[44:47], s58 offen lds\n"
"s_waitcnt vmcnt(36)\n"
"ds_read_b64_tr_b16 a[172:173], v40 offset:16384\n"
"ds_read_b64_tr_b16 a[174:175], v40 offset:16640\n"
"ds_read_b64_tr_b16 a[200:201], v41 offset:16384\n"
"ds_read_b64_tr_b16 a[202:203], v41 offset:16640\n"
"s_waitcnt lgkmcnt(4)\n"
"s_add_u32 m0, s55, 14336\n"
"s_add_u32 s58, s63, 327680\n"
"buffer_load_dwordx4 v42, s[44:47], s58 offen lds\n"
"s_add_u32 m0, s55, 15360\n"
"s_add_u32 s58, s63, 360448\n"
"buffer_load_dwordx4 v42, s[44:47], s58 offen lds\n"
"s_waitcnt vmcnt(36)\n"
"ds_read_b64_tr_b16 a[228:229], v40 offset:18432\n"
"ds_read_b64_tr_b16 a[230:231], v40 offset:18688\n"
"ds_read_b64_tr_b16 v[16:17], v41 offset:18432\n"
"ds_read_b64_tr_b16 v[18:19], v41 offset:18688\n"
"s_waitcnt lgkmcnt(4)\n"
"s_add_u32 m0, s55, 16384\n"
"s_add_u32 s58, s64, 327680\n"
"buffer_load_dwordx4 v42, s[44:47], s58 offen lds\n"
"s_add_u32 m0, s55, 17408\n"
"s_add_u32 s58, s64, 360448\n"
"buffer_load_dwordx4 v42, s[44:47], s58 offen lds\n"
"s_waitcnt vmcnt(36)\n"
"ds_read_b64_tr_b16 a[8:9], v40 offset:20480\n"
"ds_read_b64_tr_b16 a[10:11], v40 offset:20736\n"
"ds_read_b64_tr_b16 a[36:37], v41 offset:20480\n"
"ds_read_b64_tr_b16 a[38:39], v41 offset:20736\n"
"s_waitcnt lgkmcnt(4)\n"
"s_add_u32 m0, s55, 18432\n"
"s_add_u32 s58, s65, 327680\n"
"buffer_load_dwordx4 v42, s[44:47], s58 offen lds\n"
"s_add_u32 m0, s55, 19456\n"
"s_add_u32 s58, s65, 360448\n"
"buffer_load_dwordx4 v42, s[44:47], s58 offen lds\n"
"s_waitcnt vmcnt(36)\n"
"ds_read_b64_tr_b16 a[64:65], v40 offset:22528\n"
"ds_read_b64_tr_b16 a[66:67], v40 offset:22784\n"
"ds_read_b64_tr_b16 a[92:93], v41 offset:22528\n"
"ds_read_b64_tr_b16 a[94:95], v41 offset:22784\n"
"s_waitcnt lgkmcnt(4)\n"
"s_add_u32 m0, s55, 20480\n"
"s_add_u32 s58, s61, 393216\n"
"buffer_load_dwordx4 v42, s[44:47], s58 offen lds\n"
"s_add_u32 m0, s55, 21504\n"
"s_add_u32 s58, s61, 425984\n"
"buffer_load_dwordx4 v42, s[44:47], s58 offen lds\n"
"s_waitcnt vmcnt(36)\n"
"ds_read_b64_tr_b16 a[120:121], v40 offset:24576\n"
"ds_read_b64_tr_b16 a[122:123], v40 offset:24832\n"
"ds_read_b64_tr_b16 a[148:149], v41 offset:24576\n"
"ds_read_b64_tr_b16 a[150:151], v41 offset:24832\n"
"s_waitcnt lgkmcnt(4)\n"
"s_add_u32 m0, s55, 22528\n"
"s_add_u32 s58, s62, 393216\n"
"buffer_load_dwordx4 v42, s[44:47], s58 offen lds\n"
"s_add_u32 m0, s55, 23552\n"
"s_add_u32 s58, s62, 425984\n"
"buffer_load_dwordx4 v42, s[44:47], s58 offen lds\n"
"s_waitcnt vmcnt(36)\n"
"ds_read_b64_tr_b16 a[176:177], v40 offset:26624\n"
"ds_read_b64_tr_b16 a[178:179], v40 offset:26880\n"
"ds_read_b64_tr_b16 a[204:205], v41 offset:26624\n"
"ds_read_b64_tr_b16 a[206:207], v41 offset:26880\n"
"s_waitcnt lgkmcnt(4)\n"
"s_add_u32 m0, s55, 24576\n"
"s_add_u32 s58, s63, 393216\n"
"buffer_load_dwordx4 v42, s[44:47], s58 offen lds\n"
"s_add_u32 m0, s55, 25600\n"
"s_add_u32 s58, s63, 425984\n"
"buffer_load_dwordx4 v42, s[44:47], s58 offen lds\n"
"s_waitcnt vmcnt(36)\n"
"ds_read_b64_tr_b16 a[232:233], v40 offset:28672\n"
"ds_read_b64_tr_b16 a[234:235], v40 offset:28928\n"
"ds_read_b64_tr_b16 v[20:21], v41 offset:28672\n"
"ds_read_b64_tr_b16 v[22:23], v41 offset:28928\n"
"s_waitcnt lgkmcnt(4)\n"
"s_add_u32 m0, s55, 26624\n"
"s_add_u32 s58, s64, 393216\n"
"buffer_load_dwordx4 v42, s[44:47], s58 offen lds\n"
"s_add_u32 m0, s55, 27648\n"
"s_add_u32 s58, s64, 425984\n"
"buffer_load_dwordx4 v42, s[44:47], s58 offen lds\n"
"s_waitcnt vmcnt(36)\n"
"ds_read_b64_tr_b16 a[12:13], v40 offset:30720\n"
"ds_read_b64_tr_b16 a[14:15], v40 offset:30976\n"
"ds_read_b64_tr_b16 a[40:41], v41 offset:30720\n"
"ds_read_b64_tr_b16 a[42:43], v41 offset:30976\n"
"s_waitcnt lgkmcnt(4)\n"
"s_add_u32 m0, s55, 28672\n"
"s_add_u32 s58, s65, 393216\n"
"buffer_load_dwordx4 v42, s[44:47], s58 offen lds\n"
"s_add_u32 m0, s55, 29696\n"
"s_add_u32 s58, s65, 425984\n"
"buffer_load_dwordx4 v42, s[44:47], s58 offen lds\n"
"s_waitcnt vmcnt(36)\n"
"ds_read_b64_tr_b16 a[68:69], v40 offset:32768\n"
"ds_read_b64_tr_b16 a[70:71], v40 offset:33024\n"
"ds_read_b64_tr_b16 a[96:97], v41 offset:32768\n"
"ds_read_b64_tr_b16 a[98:99], v41 offset:33024\n"
"s_waitcnt lgkmcnt(4)\n"
"s_waitcnt vmcnt(34)\n"
"ds_read_b64_tr_b16 a[124:125], v40 offset:34816\n"
"ds_read_b64_tr_b16 a[126:127], v40 offset:35072\n"
"ds_read_b64_tr_b16 a[152:153], v41 offset:34816\n"
"ds_read_b64_tr_b16 a[154:155], v41 offset:35072\n"
"s_waitcnt lgkmcnt(4)\n"
"s_waitcnt vmcnt(32)\n"
"ds_read_b64_tr_b16 a[180:181], v40 offset:36864\n"
"ds_read_b64_tr_b16 a[182:183], v40 offset:37120\n"
"ds_read_b64_tr_b16 a[208:209], v41 offset:36864\n"
"ds_read_b64_tr_b16 a[210:211], v41 offset:37120\n"
"s_waitcnt lgkmcnt(4)\n"
"s_waitcnt vmcnt(30)\n"
"ds_read_b64_tr_b16 a[236:237], v40 offset:38912\n"
"ds_read_b64_tr_b16 a[238:239], v40 offset:39168\n"
"ds_read_b64_tr_b16 v[24:25], v41 offset:38912\n"
"ds_read_b64_tr_b16 v[26:27], v41 offset:39168\n"
"s_waitcnt lgkmcnt(4)\n"
"s_waitcnt vmcnt(28)\n"
"ds_read_b64_tr_b16 a[16:17], v40 offset:0\n"
"ds_read_b64_tr_b16 a[18:19], v40 offset:256\n"
"ds_read_b64_tr_b16 a[44:45], v41 offset:0\n"
"ds_read_b64_tr_b16 a[46:47], v41 offset:256\n"
"s_waitcnt lgkmcnt(4)\n"
"s_waitcnt vmcnt(26)\n"
"ds_read_b64_tr_b16 a[72:73], v40 offset:2048\n"
"ds_read_b64_tr_b16 a[74:75], v40 offset:2304\n"
"ds_read_b64_tr_b16 a[100:101], v41 offset:2048\n"
"ds_read_b64_tr_b16 a[102:103], v41 offset:2304\n"
"s_waitcnt lgkmcnt(4)\n"
"s_waitcnt vmcnt(24)\n"
"ds_read_b64_tr_b16 a[128:129], v40 offset:4096\n"
"ds_read_b64_tr_b16 a[130:131], v40 offset:4352\n"
"ds_read_b64_tr_b16 a[156:157], v41 offset:4096\n"
"ds_read_b64_tr_b16 a[158:159], v41 offset:4352\n"
"s_waitcnt lgkmcnt(4)\n"
"s_waitcnt vmcnt(22)\n"
"ds_read_b64_tr_b16 a[184:185], v40 offset:6144\n"
"ds_read_b64_tr_b16 a[186:187], v40 offset:6400\n"
"ds_read_b64_tr_b16 a[212:213], v41 offset:6144\n"
"ds_read_b64_tr_b16 a[214:215], v41 offset:6400\n"
"s_waitcnt lgkmcnt(4)\n"
"s_waitcnt vmcnt(20)\n"
"ds_read_b64_tr_b16 a[240:241], v40 offset:8192\n"
"ds_read_b64_tr_b16 a[242:243], v40 offset:8448\n"
"ds_read_b64_tr_b16 v[28:29], v41 offset:8192\n"
"ds_read_b64_tr_b16 v[30:31], v41 offset:8448\n"
"s_waitcnt lgkmcnt(4)\n"
"s_waitcnt vmcnt(18)\n"
"ds_read_b64_tr_b16 a[20:21], v40 offset:10240\n"
"ds_read_b64_tr_b16 a[22:23], v40 offset:10496\n"
"ds_read_b64_tr_b16 a[48:49], v41 offset:10240\n"
"ds_read_b64_tr_b16 a[50:51], v41 offset:10496\n"
"s_waitcnt lgkmcnt(4)\n"
"s_waitcnt vmcnt(16)\n"
"ds_read_b64_tr_b16 a[76:77], v40 offset:12288\n"
"ds_read_b64_tr_b16 a[78:79], v40 offset:12544\n"
"ds_read_b64_tr_b16 a[104:105], v41 offset:12288\n"
"ds_read_b64_tr_b16 a[106:107], v41 offset:12544\n"
"s_waitcnt lgkmcnt(4)\n"
"s_waitcnt vmcnt(14)\n"
"ds_read_b64_tr_b16 a[132:133], v40 offset:14336\n"
"ds_read_b64_tr_b16 a[134:135], v40 offset:14592\n"
"ds_read_b64_tr_b16 a[160:161], v41 offset:14336\n"
"ds_read_b64_tr_b16 a[162:163], v41 offset:14592\n"
"s_waitcnt lgkmcnt(4)\n"
"s_waitcnt vmcnt(12)\n"
"ds_read_b64_tr_b16 a[188:189], v40 offset:16384\n"
"ds_read_b64_tr_b16 a[190:191], v40 offset:16640\n"
"ds_read_b64_tr_b16 a[216:217], v41 offset:16384\n"
"ds_read_b64_tr_b16 a[218:219], v41 offset:16640\n"
"s_waitcnt lgkmcnt(4)\n"
"s_waitcnt vmcnt(10)\n"
"ds_read_b64_tr_b16 a[244:245], v40 offset:18432\n"
"ds_read_b64_tr_b16 a[246:247], v40 offset:18688\n"
"ds_read_b64_tr_b16 v[32:33], v41 offset:18432\n"
"ds_read_b64_tr_b16 v[34:35], v41 offset:18688\n"
"s_waitcnt lgkmcnt(4)\n"
"s_waitcnt vmcnt(8)\n"
"ds_read_b64_tr_b16 a[24:25], v40 offset:20480\n"
"ds_read_b64_tr_b16 a[26:27], v40 offset:20736\n"
"ds_read_b64_tr_b16 a[52:53], v41 offset:20480\n"
"ds_read_b64_tr_b16 a[54:55], v41 offset:20736\n"
"s_waitcnt lgkmcnt(4)\n"
"s_waitcnt vmcnt(6)\n"
"ds_read_b64_tr_b16 a[80:81], v40 offset:22528\n"
"ds_read_b64_tr_b16 a[82:83], v40 offset:22784\n"
"ds_read_b64_tr_b16 a[108:109], v41 offset:22528\n"
"ds_read_b64_tr_b16 a[110:111], v41 offset:22784\n"
"s_waitcnt lgkmcnt(4)\n"
"s_waitcnt vmcnt(4)\n"
"ds_read_b64_tr_b16 a[136:137], v40 offset:24576\n"
"ds_read_b64_tr_b16 a[138:139], v40 offset:24832\n"
"ds_read_b64_tr_b16 a[164:165], v41 offset:24576\n"
"ds_read_b64_tr_b16 a[166:167], v41 offset:24832\n"
"s_waitcnt lgkmcnt(4)\n"
"s_waitcnt vmcnt(2)\n"
"ds_read_b64_tr_b16 a[192:193], v40 offset:26624\n"
"ds_read_b64_tr_b16 a[194:195], v40 offset:26880\n"
"ds_read_b64_tr_b16 a[220:221], v41 offset:26624\n"
"ds_read_b64_tr_b16 a[222:223], v41 offset:26880\n"
"s_waitcnt lgkmcnt(4)\n"
"s_waitcnt vmcnt(0)\n"
"ds_read_b64_tr_b16 a[248:249], v40 offset:28672\n"
"ds_read_b64_tr_b16 a[250:251], v40 offset:28928\n"
"ds_read_b64_tr_b16 v[36:37], v41 offset:28672\n"
"ds_read_b64_tr_b16 v[38:39], v41 offset:28928\n"
"s_waitcnt lgkmcnt(4)\n"
"s_waitcnt vmcnt(0)\n"
"s_waitcnt lgkmcnt(0)\n"
"s_mov_b32 m0, s57\n"
"s_barrier\n"
"v_mov_b32 v156, 0\n"
"v_mov_b32 v157, 0\n"
"v_mov_b32 v152, 0\n"
"v_mov_b32 v153, 0\n"
"v_mov_b32 v152, 0\n"
"v_mov_b32 v153, 0\n"
"v_mov_b32 v164, 0\n"
"v_mov_b32 v165, 0\n"
"s_mov_b32 s92, 0x80000000\n"
"s_mov_b32 s93, 0x80000000\n"
"s_mov_b32 s93, 0x80000000\n"
"s_mov_b32 s95, 0x80000000\n"
"s_mov_b32 s92, 0x80000000\n"
"s_mov_b32 s95, 0x80000000\n"
"s_mov_b32 s95, 0x80000000\n"
"s_mov_b32 s94, 0x80000000\n"
"s_mov_b32 s94, 0x80000000\n"
"v_mov_b32 v166, 0\n"
"v_mov_b32 v167, 0\n"
"s_cmp_eq_u32 s54, 0\n"
"s_cbranch_scc1 .LH_1s\n"
"s_cmp_eq_u32 s54, 1\n"
"s_cbranch_scc1 .LH_2s\n"
"s_cmp_eq_u32 s54, 2\n"
"s_cbranch_scc1 .LH_3s\n"
"s_cmp_eq_u32 s54, 3\n"
"s_cbranch_scc1 .LH_4s\n"
".p2align 6\n"
".LH_top:\n"
"v_mfma_f32_16x16x32_bf16 v[40:43], v[96:99], a[0:3], 0\n"
"v_mfma_f32_16x16x32_bf16 v[40:43], v[100:103], a[4:7], v[40:43]\n"
"s_waitcnt vmcnt(14)\n"
"v_mfma_f32_16x16x32_bf16 v[40:43], v[104:107], a[8:11], v[40:43]\n"
"v_mfma_f32_16x16x32_bf16 v[40:43], v[108:111], a[12:15], v[40:43]\n"
"s_waitcnt vmcnt(12)\n"
"v_mfma_f32_16x16x32_bf16 v[40:43], v[112:115], a[16:19], v[40:43]\n"
"s_waitcnt lgkmcnt(3)\n"
"v_add_f32 v56, v56, v44\n"
"v_add_f32 v57, v57, v45\n"
"v_mfma_f32_16x16x32_bf16 v[40:43], v[116:119], a[20:23], v[40:43]\n"
"s_waitcnt vmcnt(11)\n"
"v_add_f32 v58, v58, v46\n"
"v_add_f32 v59, v59, v47\n"
"v_mfma_f32_16x16x32_bf16 v[40:43], v[120:123], a[24:27], v[40:43]\n"
"v_add_f32 v56, v56, v60\n"
"v_add_f32 v57, v57, v61\n"
"v_mfma_f32_16x16x32_bf16 v[44:47], v[96:99], a[28:31], 0\n"
"v_add_f32 v58, v58, v62\n"
"v_add_f32 v59, v59, v63\n"
"v_mfma_f32_16x16x32_bf16 v[44:47], v[100:103], a[32:35], v[44:47]\n"
"ds_write_addtid_b32 v40 offset:0\n"
"v_add_f32 v56, v56, v48\n"
"v_mfma_f32_16x16x32_bf16 v[44:47], v[104:107], a[36:39], v[44:47]\n"
"ds_write_addtid_b32 v41 offset:4640\n"
"v_add_f32 v57, v57, v49\n"
"v_mfma_f32_16x16x32_bf16 v[44:47], v[108:111], a[40:43], v[44:47]\n"
"ds_write_addtid_b32 v42 offset:9344\n"
"v_add_f32 v58, v58, v50\n"
"v_mfma_f32_16x16x32_bf16 v[44:47], v[112:115], a[44:47], v[44:47]\n"
"ds_write_addtid_b32 v43 offset:13984\n"
"v_add_f32 v59, v59, v51\n"
"v_mfma_f32_16x16x32_bf16 v[44:47], v[116:119], a[48:51], v[44:47]\n"
"s_add_u32 s95, s50, 0xffd00000\n"
"s_and_b32 s94, s95, 0xfbffffff\n"
"v_cvt_pk_bf16_f32 v40, v56, v57\n"
"v_mfma_f32_16x16x32_bf16 v[44:47], v[120:123], a[52:55], v[44:47]\n"
"v_cvt_pk_bf16_f32 v41, v58, v59\n"
"v_mfma_f32_16x16x32_bf16 v[48:51], v[96:99], a[56:59], 0\n"
"s_waitcnt lgkmcnt(4)\n"
"v_add_f32 v64, v64, v68\n"
"v_add_f32 v65, v65, v69\n"
"v_mfma_f32_16x16x32_bf16 v[48:51], v[100:103], a[60:63], v[48:51]\n"
"ds_write_addtid_b32 v44 offset:256\n"
"v_add_f32 v66, v66, v70\n"
"v_mfma_f32_16x16x32_bf16 v[48:51], v[104:107], a[64:67], v[48:51]\n"
"ds_write_addtid_b32 v45 offset:4896\n"
"v_add_f32 v67, v67, v71\n"
"v_mfma_f32_16x16x32_bf16 v[48:51], v[108:111], a[68:71], v[48:51]\n"
"ds_write_addtid_b32 v46 offset:9600\n"
"v_add_f32 v64, v64, v72\n"
"v_mfma_f32_16x16x32_bf16 v[48:51], v[112:115], a[72:75], v[48:51]\n"
"s_waitcnt vmcnt(9)\n"
"buffer_store_dwordx2 v[0:1], v9, s[40:43], s92 offen offset:0 sc1\n"
"ds_write_addtid_b32 v47 offset:14240\n"
"v_add_f32 v65, v65, v73\n"
"v_mfma_f32_16x16x32_bf16 v[48:51], v[116:119], a[76:79], v[48:51]\n"
"v_add_f32 v66, v66, v74\n"
"v_add_f32 v67, v67, v75\n"
"v_mfma_f32_16x16x32_bf16 v[48:51], v[120:123], a[80:83], v[48:51]\n"
"v_add_f32 v64, v64, v52\n"
"v_add_f32 v65, v65, v53\n"
"v_mfma_f32_16x16x32_bf16 v[44:47], v[96:99], a[84:87], 0\n"
"v_add_f32 v66, v66, v54\n"
"v_add_f32 v67, v67, v55\n"
"v_mfma_f32_16x16x32_bf16 v[44:47], v[100:103], a[88:91], v[44:47]\n"
"ds_write_addtid_b32 v48 offset:512\n"
"v_cvt_pk_bf16_f32 v42, v64, v65\n"
"v_mfma_f32_16x16x32_bf16 v[44:47], v[104:107], a[92:95], v[44:47]\n"
"ds_write_addtid_b32 v49 offset:5152\n"
"v_cvt_pk_bf16_f32 v43, v66, v67\n"
"v_mfma_f32_16x16x32_bf16 v[44:47], v[108:111], a[96:99], v[44:47]\n"
"buffer_store_dwordx4 v[40:43], v8, s[40:43], s94 offen sc1\n"
"ds_write_addtid_b32 v50 offset:9856\n"
"v_mfma_f32_16x16x32_bf16 v[44:47], v[112:115], a[100:103], v[44:47]\n"
"ds_write_addtid_b32 v51 offset:14496\n"
"v_mfma_f32_16x16x32_bf16 v[44:47], v[116:119], a[104:107], v[44:47]\n"
"v_mfma_f32_16x16x32_bf16 v[44:47], v[120:123], a[108:111], v[44:47]\n"
"v_mfma_f32_16x16x32_bf16 v[40:43], v[96:99], a[112:115], 0\n"
"v_mfma_f32_16x16x32_bf16 v[40:43], v[100:103], a[116:119], v[40:43]\n"
"ds_write_addtid_b32 v44 offset:768\n"
"v_mfma_f32_16x16x32_bf16 v[40:43], v[104:107], a[120:123], v[40:43]\n"
"ds_write_addtid_b32 v45 offset:5408\n"
"v_mfma_f32_16x16x32_bf16 v[40:43], v[108:111], a[124:127], v[40:43]\n"
"ds_write_addtid_b32 v46 offset:10112\n"
"v_mfma_f32_16x16x32_bf16 v[40:43], v[112:115], a[128:131], v[40:43]\n"
"ds_write_addtid_b32 v47 offset:14752\n"
"v_mfma_f32_16x16x32_bf16 v[40:43], v[116:119], a[132:135], v[40:43]\n"
"v_mfma_f32_16x16x32_bf16 v[40:43], v[120:123], a[136:139], v[40:43]\n"
"v_mfma_f32_16x16x32_bf16 v[48:51], v[96:99], a[140:143], 0\n"
"v_mfma_f32_16x16x32_bf16 v[48:51], v[100:103], a[144:147], v[48:51]\n"
"ds_write_addtid_b32 v40 offset:1024\n"
"v_mfma_f32_16x16x32_bf16 v[48:51], v[104:107], a[148:151], v[48:51]\n"
"ds_write_addtid_b32 v41 offset:5664\n"
"v_mfma_f32_16x16x32_bf16 v[48:51], v[108:111], a[152:155], v[48:51]\n"
"ds_write_addtid_b32 v42 offset:10368\n"
"v_mfma_f32_16x16x32_bf16 v[48:51], v[112:115], a[156:159], v[48:51]\n"
"ds_write_addtid_b32 v43 offset:15008\n"
"v_mfma_f32_16x16x32_bf16 v[48:51], v[116:119], a[160:163], v[48:51]\n"
"s_waitcnt vmcnt(9)\n"
"v_cmp_ne_u64 vcc, s[48:49], v[152:153]\n"
"v_mfma_f32_16x16x32_bf16 v[48:51], v[120:123], a[164:167], v[48:51]\n"
"v_mfma_f32_16x16x32_bf16 v[68:71], a[168:171], v[96:99], 0\n"
"s_cbranch_vccnz .LH_3s\n"
".LH_3r:\n"
"v_mfma_f32_16x16x32_bf16 v[68:71], a[172:175], v[100:103], v[68:71]\n"
"s_add_u32 s50, s50, 0x180000\n"
"s_and_b32 s56, s50, 0xfbffffff\n"
"ds_write_addtid_b32 v48 offset:1280\n"
"v_mfma_f32_16x16x32_bf16 v[68:71], a[176:179], v[104:107], v[68:71]\n"
"ds_write_addtid_b32 v49 offset:5920\n"
"buffer_load_dwordx4 v[40:43], v5, s[40:43], s56 offen offset:0 sc1\n"
"v_mfma_f32_16x16x32_bf16 v[68:71], a[180:183], v[108:111], v[68:71]\n"
"ds_write_addtid_b32 v50 offset:10624\n"
"v_mfma_f32_16x16x32_bf16 v[68:71], a[184:187], v[112:115], v[68:71]\n"
"ds_write_addtid_b32 v51 offset:15264\n"
"buffer_load_dwordx2 v[164:165], v7, s[40:43], s50 offen sc1\n"
"v_mfma_f32_16x16x32_bf16 v[68:71], a[188:191], v[116:119], v[68:71]\n"
"v_mfma_f32_16x16x32_bf16 v[68:71], a[192:195], v[120:123], v[68:71]\n"
"buffer_load_dwordx4 v[44:47], v5, s[40:43], s56 offen offset:1024 sc1\n"
"v_mfma_f32_16x16x32_bf16 v[72:75], a[196:199], v[96:99], 0\n"
"v_mfma_f32_16x16x32_bf16 v[72:75], a[200:203], v[100:103], v[72:75]\n"
"v_mfma_f32_16x16x32_bf16 v[72:75], a[204:207], v[104:107], v[72:75]\n"
"v_mfma_f32_16x16x32_bf16 v[72:75], a[208:211], v[108:111], v[72:75]\n"
"buffer_load_dwordx4 v[48:51], v5, s[40:43], s56 offen offset:2048 sc1\n"
"v_mfma_f32_16x16x32_bf16 v[72:75], a[212:215], v[112:115], v[72:75]\n"
"v_mfma_f32_16x16x32_bf16 v[72:75], a[216:219], v[116:119], v[72:75]\n"
"v_mfma_f32_16x16x32_bf16 v[72:75], a[220:223], v[120:123], v[72:75]\n"
"v_mfma_f32_16x16x32_bf16 v[76:79], a[224:227], v[96:99], 0\n"
"buffer_load_dwordx4 v[52:55], v5, s[40:43], s56 offen offset:3072 sc1\n"
"v_mfma_f32_16x16x32_bf16 v[76:79], a[228:231], v[100:103], v[76:79]\n"
"v_mfma_f32_16x16x32_bf16 v[76:79], a[232:235], v[104:107], v[76:79]\n"
"v_mfma_f32_16x16x32_bf16 v[76:79], a[236:239], v[108:111], v[76:79]\n"
"v_mfma_f32_16x16x32_bf16 v[76:79], a[240:243], v[112:115], v[76:79]\n"
"buffer_load_dwordx4 v[56:59], v6, s[40:43], s56 offen offset:0 sc1\n"
"v_mfma_f32_16x16x32_bf16 v[76:79], a[244:247], v[116:119], v[76:79]\n"
"v_mfma_f32_16x16x32_bf16 v[76:79], a[248:251], v[120:123], v[76:79]\n"
"s_add_u32 s94, s50, 0xff000000\n"
"v_mfma_f32_16x16x32_bf16 v[80:83], a[252:255], v[96:99], 0\n"
"s_cmp_lt_u32 s94, 0x8000000\n"
"s_cselect_b32 s94, 0x80000000, s94\n"
"v_mfma_f32_16x16x32_bf16 v[80:83], v[16:19], v[100:103], v[80:83]\n"
"s_waitcnt lgkmcnt(0)\n"
"buffer_load_dwordx4 v[60:63], v6, s[40:43], s56 offen offset:1024 sc1\n"
"v_mfma_f32_16x16x32_bf16 v[80:83], v[20:23], v[104:107], v[80:83]\n"
"s_barrier\n"
"v_mfma_f32_16x16x32_bf16 v[80:83], v[24:27], v[108:111], v[80:83]\n"
"ds_read_b128 v[84:87], v2 offset:2048\n"
"ds_read_b128 v[88:91], v3 offset:512\n"
"v_mfma_f32_16x16x32_bf16 v[80:83], v[28:31], v[112:115], v[80:83]\n"
"ds_read_b128 v[92:95], v4 offset:3072\n"
"ds_read_b128 v[96:99], v2 offset:2304\n"
"v_mfma_f32_16x16x32_bf16 v[80:83], v[32:35], v[116:119], v[80:83]\n"
"buffer_load_dwordx4 v[64:67], v6, s[40:43], s56 offen offset:2048 sc1\n"
"ds_read_b128 v[100:103], v3 offset:768\n"
"v_mfma_f32_16x16x32_bf16 v[80:83], v[36:39], v[120:123], v[80:83]\n"
"s_waitcnt vmcnt(16)\n"
"ds_read_b128 v[104:107], v4 offset:3328\n"
"ds_read_b128 v[108:111], v2 offset:3584\n"
"v_mfma_f32_16x16x32_bf16 v[112:115], v[124:127], a[0:3], 0\n"
"ds_read_b128 v[116:119], v3 offset:1536\n"
"ds_read_b128 v[120:123], v4 offset:3584\n"
"v_mfma_f32_16x16x32_bf16 v[112:115], v[128:131], a[4:7], v[112:115]\n"
"s_waitcnt vmcnt(14)\n"
"ds_read_b128 v[152:155], v2 offset:3840\n"
"ds_read_b128 v[156:159], v3 offset:1792\n"
"v_mfma_f32_16x16x32_bf16 v[112:115], v[132:135], a[8:11], v[112:115]\n"
"ds_read_b128 v[160:163], v4 offset:3840\n"
"buffer_load_dwordx2 v[166:167], v12, s[40:43], s94 offen offset:0 sc1\n"
"v_mfma_f32_16x16x32_bf16 v[112:115], v[136:139], a[12:15], v[112:115]\n"
"s_waitcnt vmcnt(12)\n"
"v_mfma_f32_16x16x32_bf16 v[112:115], v[140:143], a[16:19], v[112:115]\n"
"s_waitcnt lgkmcnt(9)\n"
"v_add_f32 v84, v84, v88\n"
"v_add_f32 v85, v85, v89\n"
"v_mfma_f32_16x16x32_bf16 v[112:115], v[144:147], a[20:23], v[112:115]\n"
"s_waitcnt vmcnt(11)\n"
"v_add_f32 v86, v86, v90\n"
"v_add_f32 v87, v87, v91\n"
"v_mfma_f32_16x16x32_bf16 v[112:115], v[148:151], a[24:27], v[112:115]\n"
"v_add_f32 v84, v84, v92\n"
"v_add_f32 v85, v85, v93\n"
"v_mfma_f32_16x16x32_bf16 v[88:91], v[124:127], a[28:31], 0\n"
"v_add_f32 v86, v86, v94\n"
"v_add_f32 v87, v87, v95\n"
"v_mfma_f32_16x16x32_bf16 v[88:91], v[128:131], a[32:35], v[88:91]\n"
"ds_write_addtid_b32 v112 offset:2048\n"
"v_add_f32 v84, v84, v68\n"
"v_mfma_f32_16x16x32_bf16 v[88:91], v[132:135], a[36:39], v[88:91]\n"
"ds_write_addtid_b32 v113 offset:6688\n"
"v_add_f32 v85, v85, v69\n"
"v_mfma_f32_16x16x32_bf16 v[88:91], v[136:139], a[40:43], v[88:91]\n"
"ds_write_addtid_b32 v114 offset:11392\n"
"v_add_f32 v86, v86, v70\n"
"v_mfma_f32_16x16x32_bf16 v[88:91], v[140:143], a[44:47], v[88:91]\n"
"ds_write_addtid_b32 v115 offset:16032\n"
"v_add_f32 v87, v87, v71\n"
"v_mfma_f32_16x16x32_bf16 v[88:91], v[144:147], a[48:51], v[88:91]\n"
"s_add_u32 s92, s50, 0xffd00000\n"
"s_and_b32 s93, s92, 0xfbffffff\n"
"v_cvt_pk_bf16_f32 v92, v84, v85\n"
"v_mfma_f32_16x16x32_bf16 v[88:91], v[148:151], a[52:55], v[88:91]\n"
"v_cvt_pk_bf16_f32 v93, v86, v87\n"
"v_mfma_f32_16x16x32_bf16 v[68:71], v[124:127], a[56:59], 0\n"
"s_waitcnt lgkmcnt(10)\n"
"v_add_f32 v96, v96, v100\n"
"v_add_f32 v97, v97, v101\n"
"v_mfma_f32_16x16x32_bf16 v[68:71], v[128:131], a[60:63], v[68:71]\n"
"ds_write_addtid_b32 v88 offset:2304\n"
"v_add_f32 v98, v98, v102\n"
"v_mfma_f32_16x16x32_bf16 v[68:71], v[132:135], a[64:67], v[68:71]\n"
"ds_write_addtid_b32 v89 offset:6944\n"
"v_add_f32 v99, v99, v103\n"
"v_mfma_f32_16x16x32_bf16 v[68:71], v[136:139], a[68:71], v[68:71]\n"
"ds_write_addtid_b32 v90 offset:11648\n"
"v_add_f32 v96, v96, v104\n"
"v_mfma_f32_16x16x32_bf16 v[68:71], v[140:143], a[72:75], v[68:71]\n"
"s_waitcnt vmcnt(9)\n"
"buffer_store_dwordx2 v[0:1], v9, s[40:43], s95 offen offset:0 sc1\n"
"ds_write_addtid_b32 v91 offset:16288\n"
"v_add_f32 v97, v97, v105\n"
"v_mfma_f32_16x16x32_bf16 v[68:71], v[144:147], a[76:79], v[68:71]\n"
"v_add_f32 v98, v98, v106\n"
"v_add_f32 v99, v99, v107\n"
"v_mfma_f32_16x16x32_bf16 v[68:71], v[148:151], a[80:83], v[68:71]\n"
"v_add_f32 v96, v96, v72\n"
"v_add_f32 v97, v97, v73\n"
"v_mfma_f32_16x16x32_bf16 v[84:87], v[124:127], a[84:87], 0\n"
"v_add_f32 v98, v98, v74\n"
"v_add_f32 v99, v99, v75\n"
"v_mfma_f32_16x16x32_bf16 v[84:87], v[128:131], a[88:91], v[84:87]\n"
"ds_write_addtid_b32 v68 offset:2560\n"
"v_cvt_pk_bf16_f32 v94, v96, v97\n"
"v_mfma_f32_16x16x32_bf16 v[84:87], v[132:135], a[92:95], v[84:87]\n"
"ds_write_addtid_b32 v69 offset:7200\n"
"v_cvt_pk_bf16_f32 v95, v98, v99\n"
"v_mfma_f32_16x16x32_bf16 v[84:87], v[136:139], a[96:99], v[84:87]\n"
"buffer_store_dwordx4 v[92:95], v8, s[40:43], s93 offen sc1\n"
"ds_write_addtid_b32 v70 offset:11904\n"
"v_mfma_f32_16x16x32_bf16 v[84:87], v[140:143], a[100:103], v[84:87]\n"
"s_waitcnt lgkmcnt(14)\n"
"ds_write_addtid_b32 v71 offset:16544\n"
"v_add_f32 v108, v108, v116\n"
"v_mfma_f32_16x16x32_bf16 v[84:87], v[144:147], a[104:107], v[84:87]\n"
"v_add_f32 v109, v109, v117\n"
"v_add_f32 v110, v110, v118\n"
"v_mfma_f32_16x16x32_bf16 v[84:87], v[148:151], a[108:111], v[84:87]\n"
"v_add_f32 v111, v111, v119\n"
"v_add_f32 v108, v108, v120\n"
"v_mfma_f32_16x16x32_bf16 v[68:71], v[124:127], a[112:115], 0\n"
"v_add_f32 v109, v109, v121\n"
"v_add_f32 v110, v110, v122\n"
"v_mfma_f32_16x16x32_bf16 v[68:71], v[128:131], a[116:119], v[68:71]\n"
"ds_write_addtid_b32 v84 offset:2816\n"
"v_add_f32 v111, v111, v123\n"
"v_mfma_f32_16x16x32_bf16 v[68:71], v[132:135], a[120:123], v[68:71]\n"
"ds_write_addtid_b32 v85 offset:7456\n"
"v_add_f32 v108, v108, v76\n"
"v_mfma_f32_16x16x32_bf16 v[68:71], v[136:139], a[124:127], v[68:71]\n"
"ds_write_addtid_b32 v86 offset:12160\n"
"v_add_f32 v109, v109, v77\n"
"v_mfma_f32_16x16x32_bf16 v[68:71], v[140:143], a[128:131], v[68:71]\n"
"ds_write_addtid_b32 v87 offset:16800\n"
"v_add_f32 v110, v110, v78\n"
"v_mfma_f32_16x16x32_bf16 v[68:71], v[144:147], a[132:135], v[68:71]\n"
"v_add_f32 v111, v111, v79\n"
"v_cvt_pk_bf16_f32 v84, v108, v109\n"
"v_mfma_f32_16x16x32_bf16 v[68:71], v[148:151], a[136:139], v[68:71]\n"
"v_cvt_pk_bf16_f32 v85, v110, v111\n"
"v_mfma_f32_16x16x32_bf16 v[72:75], v[124:127], a[140:143], 0\n"
"s_waitcnt lgkmcnt(15)\n"
"v_add_f32 v152, v152, v156\n"
"v_add_f32 v153, v153, v157\n"
"v_mfma_f32_16x16x32_bf16 v[72:75], v[128:131], a[144:147], v[72:75]\n"
"ds_write_addtid_b32 v68 offset:3072\n"
"v_add_f32 v154, v154, v158\n"
"v_mfma_f32_16x16x32_bf16 v[72:75], v[132:135], a[148:151], v[72:75]\n"
"ds_write_addtid_b32 v69 offset:7712\n"
"v_add_f32 v155, v155, v159\n"
"v_mfma_f32_16x16x32_bf16 v[72:75], v[136:139], a[152:155], v[72:75]\n"
"ds_write_addtid_b32 v70 offset:12416\n"
"v_add_f32 v152, v152, v160\n"
"v_mfma_f32_16x16x32_bf16 v[72:75], v[140:143], a[156:159], v[72:75]\n"
"ds_write_addtid_b32 v71 offset:17056\n"
"v_add_f32 v153, v153, v161\n"
"v_mfma_f32_16x16x32_bf16 v[72:75], v[144:147], a[160:163], v[72:75]\n"
"s_waitcnt vmcnt(9)\n"
"v_cmp_ne_u64 vcc, s[48:49], v[164:165]\n"
"v_add_f32 v154, v154, v162\n"
"v_mfma_f32_16x16x32_bf16 v[72:75], v[148:151], a[164:167], v[72:75]\n"
"v_add_f32 v155, v155, v163\n"
"v_add_f32 v152, v152, v80\n"
"v_mfma_f32_16x16x32_bf16 v[88:91], v[124:127], a[224:227], 0\n"
"s_cbranch_vccnz .LH_4s\n"
".LH_4r:\n"
"v_add_f32 v153, v153, v81\n"
"v_mfma_f32_16x16x32_bf16 v[88:91], v[128:131], a[228:231], v[88:91]\n"
"s_add_u32 s50, s50, 0x180000\n"
"s_and_b32 s56, s50, 0xfbffffff\n"
"ds_write_addtid_b32 v72 offset:3328\n"
"v_mfma_f32_16x16x32_bf16 v[88:91], v[132:135], a[232:235], v[88:91]\n"
"ds_write_addtid_b32 v73 offset:7968\n"
"buffer_load_dwordx4 v[68:71], v5, s[40:43], s56 offen offset:0 sc1\n"
"v_mfma_f32_16x16x32_bf16 v[88:91], v[136:139], a[236:239], v[88:91]\n"
"ds_write_addtid_b32 v74 offset:12672\n"
"v_add_f32 v154, v154, v82\n"
"v_mfma_f32_16x16x32_bf16 v[88:91], v[140:143], a[240:243], v[88:91]\n"
"ds_write_addtid_b32 v75 offset:17312\n"
"buffer_load_dwordx2 v[156:157], v7, s[40:43], s50 offen sc1\n"
"v_mfma_f32_16x16x32_bf16 v[88:91], v[144:147], a[244:247], v[88:91]\n"
"v_add_f32 v155, v155, v83\n"
"v_cvt_pk_bf16_f32 v86, v152, v153\n"
"v_mfma_f32_16x16x32_bf16 v[88:91], v[148:151], a[248:251], v[88:91]\n"
"buffer_load_dwordx4 v[72:75], v5, s[40:43], s56 offen offset:1024 sc1\n"
"v_cvt_pk_bf16_f32 v87, v154, v155\n"
"v_mfma_f32_16x16x32_bf16 v[92:95], v[124:127], a[252:255], 0\n"
"buffer_store_dwordx4 v[84:87], v10, s[40:43], s93 offen sc1\n"
"v_mfma_f32_16x16x32_bf16 v[92:95], v[128:131], v[16:19], v[92:95]\n"
"ds_write_addtid_b32 v88 offset:3584\n"
"v_mfma_f32_16x16x32_bf16 v[92:95], v[132:135], v[20:23], v[92:95]\n"
"ds_write_addtid_b32 v89 offset:8224\n"
"v_mfma_f32_16x16x32_bf16 v[92:95], v[136:139], v[24:27], v[92:95]\n"
"ds_write_addtid_b32 v90 offset:12928\n"
"buffer_load_dwordx4 v[76:79], v5, s[40:43], s56 offen offset:2048 sc1\n"
"v_mfma_f32_16x16x32_bf16 v[92:95], v[140:143], v[28:31], v[92:95]\n"
"ds_write_addtid_b32 v91 offset:17568\n"
"v_mfma_f32_16x16x32_bf16 v[92:95], v[144:147], v[32:35], v[92:95]\n"
"v_mfma_f32_16x16x32_bf16 v[92:95], v[148:151], v[36:39], v[92:95]\n"
"v_mfma_f32_16x16x32_bf16 v[96:99], a[168:171], v[124:127], 0\n"
"buffer_load_dwordx4 v[80:83], v5, s[40:43], s56 offen offset:3072 sc1\n"
"v_mfma_f32_16x16x32_bf16 v[96:99], a[172:175], v[128:131], v[96:99]\n"
"ds_write_addtid_b32 v92 offset:3840\n"
"v_mfma_f32_16x16x32_bf16 v[96:99], a[176:179], v[132:135], v[96:99]\n"
"ds_write_addtid_b32 v93 offset:8480\n"
"v_mfma_f32_16x16x32_bf16 v[96:99], a[180:183], v[136:139], v[96:99]\n"
"ds_write_addtid_b32 v94 offset:13184\n"
"v_mfma_f32_16x16x32_bf16 v[96:99], a[184:187], v[140:143], v[96:99]\n"
"ds_write_addtid_b32 v95 offset:17824\n"
"buffer_load_dwordx4 v[84:87], v6, s[40:43], s56 offen offset:0 sc1\n"
"v_mfma_f32_16x16x32_bf16 v[96:99], a[188:191], v[144:147], v[96:99]\n"
"v_mfma_f32_16x16x32_bf16 v[96:99], a[192:195], v[148:151], v[96:99]\n"
"v_mfma_f32_16x16x32_bf16 v[100:103], a[196:199], v[124:127], 0\n"
"v_mfma_f32_16x16x32_bf16 v[100:103], a[200:203], v[128:131], v[100:103]\n"
"s_waitcnt lgkmcnt(0)\n"
"buffer_load_dwordx4 v[88:91], v6, s[40:43], s56 offen offset:1024 sc1\n"
"v_mfma_f32_16x16x32_bf16 v[100:103], a[204:207], v[132:135], v[100:103]\n"
"s_barrier\n"
"v_mfma_f32_16x16x32_bf16 v[100:103], a[208:211], v[136:139], v[100:103]\n"
"s_waitcnt vmcnt(10)\n"
"ds_read_b128 v[104:107], v2 offset:0\n"
"ds_read_b128 v[108:111], v3 offset:2560\n"
"v_mfma_f32_16x16x32_bf16 v[100:103], a[212:215], v[140:143], v[100:103]\n"
"ds_read_b128 v[112:115], v4 offset:1024\n"
"ds_read_b128 v[116:119], v2 offset:256\n"
"v_mfma_f32_16x16x32_bf16 v[100:103], a[216:219], v[144:147], v[100:103]\n"
"buffer_load_dwordx4 v[92:95], v6, s[40:43], s56 offen offset:2048 sc1\n"
"ds_read_b128 v[120:123], v3 offset:2816\n"
"v_mfma_f32_16x16x32_bf16 v[100:103], a[220:223], v[148:151], v[100:103]\n"
"ds_read_b128 v[124:127], v4 offset:1280\n"
"v_mfma_f32_16x16x32_bf16 v[128:131], v[40:43], a[0:3], 0\n"
"v_cmp_ne_u64 vcc, s[48:49], v[166:167]\n"
"v_mfma_f32_16x16x32_bf16 v[128:131], v[44:47], a[4:7], v[128:131]\n"
"v_mfma_f32_16x16x32_bf16 v[128:131], v[48:51], a[8:11], v[128:131]\n"
"s_cbranch_vccnz .LH_5s\n"
".LH_5r:\n"
"v_mfma_f32_16x16x32_bf16 v[128:131], v[52:55], a[12:15], v[128:131]\n"
"buffer_load_dwordx4 v[140:143], v13, s[40:43], s94 offen offset:0 sc1\n"
"v_mfma_f32_16x16x32_bf16 v[128:131], v[56:59], a[16:19], v[128:131]\n"
"s_waitcnt lgkmcnt(3)\n"
"v_add_f32 v104, v104, v108\n"
"v_add_f32 v105, v105, v109\n"
"v_mfma_f32_16x16x32_bf16 v[128:131], v[60:63], a[20:23], v[128:131]\n"
"v_add_f32 v106, v106, v110\n"
"v_add_f32 v107, v107, v111\n"
"v_mfma_f32_16x16x32_bf16 v[128:131], v[64:67], a[24:27], v[128:131]\n"
"v_add_f32 v104, v104, v112\n"
"v_add_f32 v105, v105, v113\n"
"v_mfma_f32_16x16x32_bf16 v[108:111], v[40:43], a[28:31], 0\n"
"v_add_f32 v106, v106, v114\n"
"v_add_f32 v107, v107, v115\n"
"v_mfma_f32_16x16x32_bf16 v[108:111], v[44:47], a[32:35], v[108:111]\n"
"ds_write_addtid_b32 v128 offset:0\n"
"v_add_f32 v104, v104, v96\n"
"v_mfma_f32_16x16x32_bf16 v[108:111], v[48:51], a[36:39], v[108:111]\n"
"ds_write_addtid_b32 v129 offset:4640\n"
"v_add_f32 v105, v105, v97\n"
"v_mfma_f32_16x16x32_bf16 v[108:111], v[52:55], a[40:43], v[108:111]\n"
"ds_write_addtid_b32 v130 offset:9344\n"
"v_add_f32 v106, v106, v98\n"
"v_mfma_f32_16x16x32_bf16 v[108:111], v[56:59], a[44:47], v[108:111]\n"
"ds_write_addtid_b32 v131 offset:13984\n"
"v_add_f32 v107, v107, v99\n"
"v_mfma_f32_16x16x32_bf16 v[108:111], v[60:63], a[48:51], v[108:111]\n"
"s_add_u32 s93, s50, 0xffd00000\n"
"s_and_b32 s95, s93, 0xfbffffff\n"
"v_cvt_pk_bf16_f32 v112, v104, v105\n"
"v_mfma_f32_16x16x32_bf16 v[108:111], v[64:67], a[52:55], v[108:111]\n"
"buffer_load_dwordx4 v[128:131], v13, s[40:43], s94 offen offset:1024 sc1\n"
"v_cvt_pk_bf16_f32 v113, v106, v107\n"
"v_mfma_f32_16x16x32_bf16 v[96:99], v[40:43], a[56:59], 0\n"
"s_waitcnt lgkmcnt(4)\n"
"v_add_f32 v116, v116, v120\n"
"v_add_f32 v117, v117, v121\n"
"v_mfma_f32_16x16x32_bf16 v[96:99], v[44:47], a[60:63], v[96:99]\n"
"ds_write_addtid_b32 v108 offset:256\n"
"v_add_f32 v118, v118, v122\n"
"v_mfma_f32_16x16x32_bf16 v[96:99], v[48:51], a[64:67], v[96:99]\n"
"ds_write_addtid_b32 v109 offset:4896\n"
"v_add_f32 v119, v119, v123\n"
"v_mfma_f32_16x16x32_bf16 v[96:99], v[52:55], a[68:71], v[96:99]\n"
"ds_write_addtid_b32 v110 offset:9600\n"
"v_add_f32 v116, v116, v124\n"
"v_mfma_f32_16x16x32_bf16 v[96:99], v[56:59], a[72:75], v[96:99]\n"
"ds_write_addtid_b32 v111 offset:14240\n"
"v_add_f32 v117, v117, v125\n"
"v_mfma_f32_16x16x32_bf16 v[96:99], v[60:63], a[76:79], v[96:99]\n"
"v_add_f32 v118, v118, v126\n"
"v_add_f32 v119, v119, v127\n"
"v_mfma_f32_16x16x32_bf16 v[96:99], v[64:67], a[80:83], v[96:99]\n"
"v_add_f32 v116, v116, v100\n"
"v_add_f32 v117, v117, v101\n"
"v_mfma_f32_16x16x32_bf16 v[104:107], v[40:43], a[84:87], 0\n"
"v_add_f32 v118, v118, v102\n"
"v_add_f32 v119, v119, v103\n"
"v_mfma_f32_16x16x32_bf16 v[104:107], v[44:47], a[88:91], v[104:107]\n"
"ds_write_addtid_b32 v96 offset:512\n"
"v_cvt_pk_bf16_f32 v114, v116, v117\n"
"v_mfma_f32_16x16x32_bf16 v[104:107], v[48:51], a[92:95], v[104:107]\n"
"ds_write_addtid_b32 v97 offset:5152\n"
"v_cvt_pk_bf16_f32 v115, v118, v119\n"
"v_mfma_f32_16x16x32_bf16 v[104:107], v[52:55], a[96:99], v[104:107]\n"
"buffer_store_dwordx4 v[112:115], v8, s[40:43], s95 offen sc1\n"
"ds_write_addtid_b32 v98 offset:9856\n"
"buffer_load_dwordx4 v[124:127], v13, s[40:43], s94 offen offset:2048 sc1\n"
"v_mfma_f32_16x16x32_bf16 v[104:107], v[56:59], a[100:103], v[104:107]\n"
"ds_write_addtid_b32 v99 offset:14496\n"
"buffer_load_dwordx4 v[132:135], v13, s[40:43], s94 offen offset:3072 sc1\n"
"v_mfma_f32_16x16x32_bf16 v[104:107], v[60:63], a[104:107], v[104:107]\n"
"v_mfma_f32_16x16x32_bf16 v[104:107], v[64:67], a[108:111], v[104:107]\n"
"v_mfma_f32_16x16x32_bf16 v[96:99], v[40:43], a[112:115], 0\n"
"v_mfma_f32_16x16x32_bf16 v[96:99], v[44:47], a[116:119], v[96:99]\n"
"ds_write_addtid_b32 v104 offset:768\n"
"v_mfma_f32_16x16x32_bf16 v[96:99], v[48:51], a[120:123], v[96:99]\n"
"ds_write_addtid_b32 v105 offset:5408\n"
"v_mfma_f32_16x16x32_bf16 v[96:99], v[52:55], a[124:127], v[96:99]\n"
"ds_write_addtid_b32 v106 offset:10112\n"
"v_mfma_f32_16x16x32_bf16 v[96:99], v[56:59], a[128:131], v[96:99]\n"
"ds_write_addtid_b32 v107 offset:14752\n"
"v_mfma_f32_16x16x32_bf16 v[96:99], v[60:63], a[132:135], v[96:99]\n"
"v_mfma_f32_16x16x32_bf16 v[96:99], v[64:67], a[136:139], v[96:99]\n"
"v_mfma_f32_16x16x32_bf16 v[100:103], v[40:43], a[140:143], 0\n"
"v_mfma_f32_16x16x32_bf16 v[100:103], v[44:47], a[144:147], v[100:103]\n"
"ds_write_addtid_b32 v96 offset:1024\n"
"v_mfma_f32_16x16x32_bf16 v[100:103], v[48:51], a[148:151], v[100:103]\n"
"ds_write_addtid_b32 v97 offset:5664\n"
"v_mfma_f32_16x16x32_bf16 v[100:103], v[52:55], a[152:155], v[100:103]\n"
"ds_write_addtid_b32 v98 offset:10368\n"
"v_mfma_f32_16x16x32_bf16 v[100:103], v[56:59], a[156:159], v[100:103]\n"
"ds_write_addtid_b32 v99 offset:15008\n"
"v_mfma_f32_16x16x32_bf16 v[100:103], v[60:63], a[160:163], v[100:103]\n"
"s_waitcnt vmcnt(12)\n"
"v_cmp_ne_u64 vcc, s[48:49], v[156:157]\n"
"v_mfma_f32_16x16x32_bf16 v[100:103], v[64:67], a[164:167], v[100:103]\n"
"v_mfma_f32_16x16x32_bf16 v[108:111], v[40:43], a[224:227], 0\n"
"s_cbranch_vccnz .LH_1s\n"
".LH_1r:\n"
"v_mfma_f32_16x16x32_bf16 v[108:111], v[44:47], a[228:231], v[108:111]\n"
"s_add_u32 s50, s50, 0x180000\n"
"s_and_b32 s56, s50, 0xfbffffff\n"
"ds_write_addtid_b32 v100 offset:1280\n"
"v_mfma_f32_16x16x32_bf16 v[108:111], v[48:51], a[232:235], v[108:111]\n"
"s_waitcnt vmcnt(10)\n"
"ds_write_addtid_b32 v101 offset:5920\n"
"buffer_load_dwordx4 v[96:99], v5, s[40:43], s56 offen offset:0 sc1\n"
"v_mfma_f32_16x16x32_bf16 v[108:111], v[52:55], a[236:239], v[108:111]\n"
"buffer_store_dwordx2 v[0:1], v11, s[40:43], s92 offen offset:0 sc1\n"
"ds_write_addtid_b32 v102 offset:10624\n"
"v_mfma_f32_16x16x32_bf16 v[108:111], v[56:59], a[240:243], v[108:111]\n"
"ds_write_addtid_b32 v103 offset:15264\n"
"buffer_load_dwordx2 v[152:153], v7, s[40:43], s50 offen sc1\n"
"v_mfma_f32_16x16x32_bf16 v[108:111], v[60:63], a[244:247], v[108:111]\n"
"v_mfma_f32_16x16x32_bf16 v[108:111], v[64:67], a[248:251], v[108:111]\n"
"buffer_load_dwordx4 v[100:103], v5, s[40:43], s56 offen offset:1024 sc1\n"
"v_mfma_f32_16x16x32_bf16 v[116:119], v[40:43], a[252:255], 0\n"
"v_mfma_f32_16x16x32_bf16 v[116:119], v[44:47], v[16:19], v[116:119]\n"
"ds_write_addtid_b32 v108 offset:1536\n"
"v_mfma_f32_16x16x32_bf16 v[116:119], v[48:51], v[20:23], v[116:119]\n"
"ds_write_addtid_b32 v109 offset:6176\n"
"v_mfma_f32_16x16x32_bf16 v[116:119], v[52:55], v[24:27], v[116:119]\n"
"ds_write_addtid_b32 v110 offset:10880\n"
"buffer_load_dwordx4 v[104:107], v5, s[40:43], s56 offen offset:2048 sc1\n"
"v_mfma_f32_16x16x32_bf16 v[116:119], v[56:59], v[28:31], v[116:119]\n"
"ds_write_addtid_b32 v111 offset:15520\n"
"v_mfma_f32_16x16x32_bf16 v[116:119], v[60:63], v[32:35], v[116:119]\n"
"v_mfma_f32_16x16x32_bf16 v[116:119], v[64:67], v[36:39], v[116:119]\n"
"v_mfma_f32_16x16x32_bf16 v[136:139], a[168:171], v[40:43], 0\n"
"buffer_load_dwordx4 v[108:111], v5, s[40:43], s56 offen offset:3072 sc1\n"
"v_mfma_f32_16x16x32_bf16 v[136:139], a[172:175], v[44:47], v[136:139]\n"
"ds_write_addtid_b32 v116 offset:1792\n"
"v_mfma_f32_16x16x32_bf16 v[136:139], a[176:179], v[48:51], v[136:139]\n"
"ds_write_addtid_b32 v117 offset:6432\n"
"v_mfma_f32_16x16x32_bf16 v[136:139], a[180:183], v[52:55], v[136:139]\n"
"ds_write_addtid_b32 v118 offset:11136\n"
"v_mfma_f32_16x16x32_bf16 v[136:139], a[184:187], v[56:59], v[136:139]\n"
"ds_write_addtid_b32 v119 offset:15776\n"
"buffer_load_dwordx4 v[112:115], v6, s[40:43], s56 offen offset:0 sc1\n"
"v_mfma_f32_16x16x32_bf16 v[136:139], a[188:191], v[60:63], v[136:139]\n"
"v_mfma_f32_16x16x32_bf16 v[136:139], a[192:195], v[64:67], v[136:139]\n"
"v_mfma_f32_16x16x32_bf16 v[144:147], a[196:199], v[40:43], 0\n"
"v_mfma_f32_16x16x32_bf16 v[144:147], a[200:203], v[44:47], v[144:147]\n"
"s_waitcnt lgkmcnt(0)\n"
"buffer_load_dwordx4 v[116:119], v6, s[40:43], s56 offen offset:1024 sc1\n"
"v_mfma_f32_16x16x32_bf16 v[144:147], a[204:207], v[48:51], v[144:147]\n"
"s_barrier\n"
"v_mfma_f32_16x16x32_bf16 v[144:147], a[208:211], v[52:55], v[144:147]\n"
"ds_read_b128 v[40:43], v2 offset:2048\n"
"ds_read_b128 v[44:47], v3 offset:512\n"
"v_mfma_f32_16x16x32_bf16 v[144:147], a[212:215], v[56:59], v[144:147]\n"
"ds_read_b128 v[48:51], v4 offset:3072\n"
"ds_read_b128 v[52:55], v2 offset:2304\n"
"v_mfma_f32_16x16x32_bf16 v[144:147], a[216:219], v[60:63], v[144:147]\n"
"buffer_load_dwordx4 v[120:123], v6, s[40:43], s56 offen offset:2048 sc1\n"
"ds_read_b128 v[56:59], v3 offset:768\n"
"v_mfma_f32_16x16x32_bf16 v[144:147], a[220:223], v[64:67], v[144:147]\n"
"ds_read_b128 v[60:63], v4 offset:3328\n"
"v_mfma_f32_16x16x32_bf16 v[148:151], v[68:71], a[0:3], 0\n"
"v_mfma_f32_16x16x32_bf16 v[148:151], v[72:75], a[4:7], v[148:151]\n"
"s_waitcnt vmcnt(17)\n"
"v_mfma_f32_16x16x32_bf16 v[148:151], v[76:79], a[8:11], v[148:151]\n"
"v_mfma_f32_16x16x32_bf16 v[148:151], v[80:83], a[12:15], v[148:151]\n"
"s_waitcnt vmcnt(15)\n"
"v_mfma_f32_16x16x32_bf16 v[148:151], v[84:87], a[16:19], v[148:151]\n"
"s_waitcnt lgkmcnt(3)\n"
"v_add_f32 v40, v40, v44\n"
"v_add_f32 v41, v41, v45\n"
"v_mfma_f32_16x16x32_bf16 v[148:151], v[88:91], a[20:23], v[148:151]\n"
"s_waitcnt vmcnt(14)\n"
"v_add_f32 v42, v42, v46\n"
"v_add_f32 v43, v43, v47\n"
"v_mfma_f32_16x16x32_bf16 v[148:151], v[92:95], a[24:27], v[148:151]\n"
"v_add_f32 v40, v40, v48\n"
"v_add_f32 v41, v41, v49\n"
"v_mfma_f32_16x16x32_bf16 v[44:47], v[68:71], a[28:31], 0\n"
"v_add_f32 v42, v42, v50\n"
"v_add_f32 v43, v43, v51\n"
"v_mfma_f32_16x16x32_bf16 v[44:47], v[72:75], a[32:35], v[44:47]\n"
"ds_write_addtid_b32 v148 offset:2048\n"
"v_add_f32 v40, v40, v136\n"
"v_mfma_f32_16x16x32_bf16 v[44:47], v[76:79], a[36:39], v[44:47]\n"
"ds_write_addtid_b32 v149 offset:6688\n"
"v_add_f32 v41, v41, v137\n"
"v_mfma_f32_16x16x32_bf16 v[44:47], v[80:83], a[40:43], v[44:47]\n"
"ds_write_addtid_b32 v150 offset:11392\n"
"v_add_f32 v42, v42, v138\n"
"v_mfma_f32_16x16x32_bf16 v[44:47], v[84:87], a[44:47], v[44:47]\n"
"ds_write_addtid_b32 v151 offset:16032\n"
"v_add_f32 v43, v43, v139\n"
"v_mfma_f32_16x16x32_bf16 v[44:47], v[88:91], a[48:51], v[44:47]\n"
"s_add_u32 s92, s50, 0xffd00000\n"
"s_and_b32 s95, s92, 0xfbffffff\n"
"v_cvt_pk_bf16_f32 v48, v40, v41\n"
"v_mfma_f32_16x16x32_bf16 v[44:47], v[92:95], a[52:55], v[44:47]\n"
"v_cvt_pk_bf16_f32 v49, v42, v43\n"
"v_mfma_f32_16x16x32_bf16 v[64:67], v[68:71], a[56:59], 0\n"
"s_waitcnt lgkmcnt(4)\n"
"v_add_f32 v52, v52, v56\n"
"v_add_f32 v53, v53, v57\n"
"v_mfma_f32_16x16x32_bf16 v[64:67], v[72:75], a[60:63], v[64:67]\n"
"ds_write_addtid_b32 v44 offset:2304\n"
"v_add_f32 v54, v54, v58\n"
"v_mfma_f32_16x16x32_bf16 v[64:67], v[76:79], a[64:67], v[64:67]\n"
"ds_write_addtid_b32 v45 offset:6944\n"
"v_add_f32 v55, v55, v59\n"
"v_mfma_f32_16x16x32_bf16 v[64:67], v[80:83], a[68:71], v[64:67]\n"
"ds_write_addtid_b32 v46 offset:11648\n"
"v_add_f32 v52, v52, v60\n"
"v_mfma_f32_16x16x32_bf16 v[64:67], v[84:87], a[72:75], v[64:67]\n"
"s_waitcnt vmcnt(11)\n"
"buffer_store_dwordx2 v[0:1], v9, s[40:43], s93 offen offset:0 sc1\n"
"ds_write_addtid_b32 v47 offset:16288\n"
"v_add_f32 v53, v53, v61\n"
"v_mfma_f32_16x16x32_bf16 v[64:67], v[88:91], a[76:79], v[64:67]\n"
"s_waitcnt vmcnt(10)\n"
"v_add_f32 v54, v54, v62\n"
"v_add_f32 v55, v55, v63\n"
"v_mfma_f32_16x16x32_bf16 v[64:67], v[92:95], a[80:83], v[64:67]\n"
"v_add_f32 v52, v52, v144\n"
"v_add_f32 v53, v53, v145\n"
"v_mfma_f32_16x16x32_bf16 v[40:43], v[68:71], a[84:87], 0\n"
"v_add_f32 v54, v54, v146\n"
"v_add_f32 v55, v55, v147\n"
"v_mfma_f32_16x16x32_bf16 v[40:43], v[72:75], a[88:91], v[40:43]\n"
"ds_write_addtid_b32 v64 offset:2560\n"
"v_cvt_pk_bf16_f32 v50, v52, v53\n"
"v_mfma_f32_16x16x32_bf16 v[40:43], v[76:79], a[92:95], v[40:43]\n"
"ds_write_addtid_b32 v65 offset:7200\n"
"v_cvt_pk_bf16_f32 v51, v54, v55\n"
"v_mfma_f32_16x16x32_bf16 v[40:43], v[80:83], a[96:99], v[40:43]\n"
"buffer_store_dwordx4 v[48:51], v8, s[40:43], s95 offen sc1\n"
"ds_write_addtid_b32 v66 offset:11904\n"
"v_add_f32 v140, v140, v128\n"
"v_add_f32 v141, v141, v129\n"
"v_add_f32 v142, v142, v130\n"
"v_mfma_f32_16x16x32_bf16 v[40:43], v[84:87], a[100:103], v[40:43]\n"
"ds_write_addtid_b32 v67 offset:16544\n"
"v_add_f32 v143, v143, v131\n"
"v_mfma_f32_16x16x32_bf16 v[40:43], v[88:91], a[104:107], v[40:43]\n"
"v_add_f32 v124, v124, v132\n"
"v_add_f32 v125, v125, v133\n"
"v_mfma_f32_16x16x32_bf16 v[40:43], v[92:95], a[108:111], v[40:43]\n"
"v_add_f32 v126, v126, v134\n"
"v_add_f32 v127, v127, v135\n"
"v_mfma_f32_16x16x32_bf16 v[44:47], v[68:71], a[112:115], 0\n"
"v_add_f32 v140, v140, v124\n"
"v_add_f32 v141, v141, v125\n"
"v_mfma_f32_16x16x32_bf16 v[44:47], v[72:75], a[116:119], v[44:47]\n"
"ds_write_addtid_b32 v40 offset:2816\n"
"v_add_f32 v142, v142, v126\n"
"v_mfma_f32_16x16x32_bf16 v[44:47], v[76:79], a[120:123], v[44:47]\n"
"ds_write_addtid_b32 v41 offset:7456\n"
"v_add_f32 v143, v143, v127\n"
"v_mfma_f32_16x16x32_bf16 v[44:47], v[80:83], a[124:127], v[44:47]\n"
"ds_write_addtid_b32 v42 offset:12160\n"
"v_mfma_f32_16x16x32_bf16 v[44:47], v[84:87], a[128:131], v[44:47]\n"
"ds_write_addtid_b32 v43 offset:16800\n"
"v_mfma_f32_16x16x32_bf16 v[44:47], v[88:91], a[132:135], v[44:47]\n"
"v_permlane32_swap_b32 v140, v142\n"
"v_mfma_f32_16x16x32_bf16 v[44:47], v[92:95], a[136:139], v[44:47]\n"
"v_permlane32_swap_b32 v141, v143\n"
"v_mfma_f32_16x16x32_bf16 v[40:43], v[68:71], a[140:143], 0\n"
"v_add_f32 v140, v140, v142\n"
"v_add_f32 v141, v141, v143\n"
"v_mfma_f32_16x16x32_bf16 v[40:43], v[72:75], a[144:147], v[40:43]\n"
"ds_write_addtid_b32 v44 offset:3072\n"
"v_mfma_f32_16x16x32_bf16 v[40:43], v[76:79], a[148:151], v[40:43]\n"
"ds_write_addtid_b32 v45 offset:7712\n"
"v_mfma_f32_16x16x32_bf16 v[40:43], v[80:83], a[152:155], v[40:43]\n"
"ds_write_addtid_b32 v46 offset:12416\n"
"v_mfma_f32_16x16x32_bf16 v[40:43], v[84:87], a[156:159], v[40:43]\n"
"ds_write_addtid_b32 v47 offset:17056\n"
"v_mfma_f32_16x16x32_bf16 v[40:43], v[88:91], a[160:163], v[40:43]\n"
"s_waitcnt vmcnt(8)\n"
"v_cmp_ne_u64 vcc, s[48:49], v[152:153]\n"
"v_mfma_f32_16x16x32_bf16 v[40:43], v[92:95], a[164:167], v[40:43]\n"
"v_permlane16_swap_b32 v140, v141\n"
"v_mfma_f32_16x16x32_bf16 v[44:47], v[68:71], a[224:227], 0\n"
"s_cbranch_vccnz .LH_2s\n"
".LH_2r:\n"
"v_add_f32 v140, v140, v141\n"
"v_mfma_f32_16x16x32_bf16 v[44:47], v[72:75], a[228:231], v[44:47]\n"
"s_add_u32 s50, s50, 0x180000\n"
"s_and_b32 s56, s50, 0xfbffffff\n"
"ds_write_addtid_b32 v40 offset:3328\n"
"v_mfma_f32_16x16x32_bf16 v[44:47], v[76:79], a[232:235], v[44:47]\n"
"ds_write_addtid_b32 v41 offset:7968\n"
"buffer_load_dwordx4 v[124:127], v5, s[40:43], s56 offen offset:0 sc1\n"
"v_mfma_f32_16x16x32_bf16 v[44:47], v[80:83], a[236:239], v[44:47]\n"
"ds_write_addtid_b32 v42 offset:12672\n"
"v_add_f32_dpp v141, v140, v140 row_ror:8 row_mask:0xf bank_mask:0xf\n"
"v_mfma_f32_16x16x32_bf16 v[44:47], v[84:87], a[240:243], v[44:47]\n"
"ds_write_addtid_b32 v43 offset:17312\n"
"buffer_load_dwordx2 v[152:153], v7, s[40:43], s50 offen sc1\n"
"v_mfma_f32_16x16x32_bf16 v[44:47], v[88:91], a[244:247], v[44:47]\n"
"v_mov_b32 v140, v141\n"
"v_mfma_f32_16x16x32_bf16 v[44:47], v[92:95], a[248:251], v[44:47]\n"
"buffer_load_dwordx4 v[128:131], v5, s[40:43], s56 offen offset:1024 sc1\n"
"v_mfma_f32_16x16x32_bf16 v[40:43], v[68:71], a[252:255], 0\n"
"v_permlane16_swap_b32 v141, v140\n"
"v_mfma_f32_16x16x32_bf16 v[40:43], v[72:75], v[16:19], v[40:43]\n"
"ds_write_addtid_b32 v44 offset:3584\n"
"v_mul_f32 v141, 0xba800000, v141\n"
"v_mfma_f32_16x16x32_bf16 v[40:43], v[76:79], v[20:23], v[40:43]\n"
"ds_write_addtid_b32 v45 offset:8224\n"
"v_fma_f32 v140, v140, s60, v15\n"
"v_mfma_f32_16x16x32_bf16 v[40:43], v[80:83], v[24:27], v[40:43]\n"
"ds_write_addtid_b32 v46 offset:12928\n"
"buffer_load_dwordx4 v[132:135], v5, s[40:43], s56 offen offset:2048 sc1\n"
"v_mfma_f32_16x16x32_bf16 v[40:43], v[84:87], v[28:31], v[40:43]\n"
"ds_write_addtid_b32 v47 offset:17568\n"
"v_fma_f32 v140, -v141, v141, v140\n"
"v_mfma_f32_16x16x32_bf16 v[40:43], v[88:91], v[32:35], v[40:43]\n"
"v_max_f32 v140, v15, v140\n"
"v_mfma_f32_16x16x32_bf16 v[40:43], v[92:95], v[36:39], v[40:43]\n"
"v_rsq_f32 v140, v140\n"
"v_mfma_f32_16x16x32_bf16 v[48:51], a[168:171], v[68:71], 0\n"
"buffer_load_dwordx4 v[136:139], v5, s[40:43], s56 offen offset:3072 sc1\n"
"v_mov_b32 v142, v0\n"
"v_mfma_f32_16x16x32_bf16 v[48:51], a[172:175], v[72:75], v[48:51]\n"
"ds_write_addtid_b32 v40 offset:3840\n"
"v_mov_b32 v143, v1\n"
"v_mfma_f32_16x16x32_bf16 v[48:51], a[176:179], v[76:79], v[48:51]\n"
"buffer_store_dwordx4 v[140:143], v14, s[40:43], s94 offen sc1\n"
"ds_write_addtid_b32 v41 offset:8480\n"
"v_mfma_f32_16x16x32_bf16 v[48:51], a[180:183], v[80:83], v[48:51]\n"
"ds_write_addtid_b32 v42 offset:13184\n"
"v_mfma_f32_16x16x32_bf16 v[48:51], a[184:187], v[84:87], v[48:51]\n"
"ds_write_addtid_b32 v43 offset:17824\n"
"buffer_load_dwordx4 v[140:143], v6, s[40:43], s56 offen offset:0 sc1\n"
"v_mfma_f32_16x16x32_bf16 v[48:51], a[188:191], v[88:91], v[48:51]\n"
"v_mfma_f32_16x16x32_bf16 v[48:51], a[192:195], v[92:95], v[48:51]\n"
"v_mfma_f32_16x16x32_bf16 v[52:55], a[196:199], v[68:71], 0\n"
"v_mfma_f32_16x16x32_bf16 v[52:55], a[200:203], v[72:75], v[52:55]\n"
"s_waitcnt lgkmcnt(0)\n"
"buffer_load_dwordx4 v[144:147], v6, s[40:43], s56 offen offset:1024 sc1\n"
"v_mfma_f32_16x16x32_bf16 v[52:55], a[204:207], v[76:79], v[52:55]\n"
"s_barrier\n"
"v_mfma_f32_16x16x32_bf16 v[52:55], a[208:211], v[80:83], v[52:55]\n"
"ds_read_b128 v[56:59], v2 offset:0\n"
"ds_read_b128 v[44:47], v3 offset:2560\n"
"v_mfma_f32_16x16x32_bf16 v[52:55], a[212:215], v[84:87], v[52:55]\n"
"ds_read_b128 v[60:63], v4 offset:1024\n"
"ds_read_b128 v[64:67], v2 offset:256\n"
"v_mfma_f32_16x16x32_bf16 v[52:55], a[216:219], v[88:91], v[52:55]\n"
"buffer_load_dwordx4 v[148:151], v6, s[40:43], s56 offen offset:2048 sc1\n"
"ds_read_b128 v[68:71], v3 offset:2816\n"
"v_mfma_f32_16x16x32_bf16 v[52:55], a[220:223], v[92:95], v[52:55]\n"
"s_waitcnt vmcnt(16)\n"
"ds_read_b128 v[72:75], v4 offset:1280\n"
"s_branch .LH_top\n"
".LH_1s:\n"
"s_nop 7\n"
"s_cmp_ge_u32 s50, s53\n"
"s_cbranch_scc1 .LH_end\n"
"s_cmp_ge_u32 s50, s52\n"
"s_cbranch_scc1 .LH_1r\n"
"s_sub_u32 s58, s50, 0x0\n"
"s_mov_b32 s59, 200000\n"
".LH_1p:\n"
"buffer_load_dwordx2 v[156:157], v7, s[40:43], s58 offen sc1\n"
"s_waitcnt vmcnt(0)\n"
"v_cmp_ne_u64 vcc, s[48:49], v[156:157]\n"
"s_cbranch_vccz .LH_1px\n"
"s_sleep 1\n"
"s_sub_u32 s59, s59, 1\n"
"s_cbranch_scc0 .LH_1p\n"
"s_branch .LH_end\n"
".LH_1px:\n"
"s_branch .LH_1r\n"
".LH_2s:\n"
"s_nop 7\n"
"s_cmp_ge_u32 s50, s53\n"
"s_cbranch_scc1 .LH_end\n"
"s_cmp_ge_u32 s50, s52\n"
"s_cbranch_scc1 .LH_2r\n"
"s_sub_u32 s58, s50, 0x0\n"
"s_mov_b32 s59, 200000\n"
".LH_2p:\n"
"buffer_load_dwordx2 v[152:153], v7, s[40:43], s58 offen sc1\n"
"s_waitcnt vmcnt(0)\n"
"v_cmp_ne_u64 vcc, s[48:49], v[152:153]\n"
"s_cbranch_vccz .LH_2px\n"
"s_sleep 1\n"
"s_sub_u32 s59, s59, 1\n"
"s_cbranch_scc0 .LH_2p\n"
"s_branch .LH_end\n"
".LH_2px:\n"
"s_branch .LH_2r\n"
".LH_3s:\n"
"s_nop 7\n"
"s_cmp_ge_u32 s50, s53\n"
"s_cbranch_scc1 .LH_end\n"
"s_cmp_ge_u32 s50, s52\n"
"s_cbranch_scc1 .LH_3r\n"
"s_sub_u32 s58, s50, 0x0\n"
"s_mov_b32 s59, 200000\n"
".LH_3p:\n"
"buffer_load_dwordx2 v[152:153], v7, s[40:43], s58 offen sc1\n"
"s_waitcnt vmcnt(0)\n"
"v_cmp_ne_u64 vcc, s[48:49], v[152:153]\n"
"s_cbranch_vccz .LH_3px\n"
"s_sleep 1\n"
"s_sub_u32 s59, s59, 1\n"
"s_cbranch_scc0 .LH_3p\n"
"s_branch .LH_end\n"
".LH_3px:\n"
"s_branch .LH_3r\n"
".LH_4s:\n"
"s_nop 7\n"
"s_cmp_ge_u32 s50, s53\n"
"s_cbranch_scc1 .LH_end\n"
"s_cmp_ge_u32 s50, s52\n"
"s_cbranch_scc1 .LH_4r\n"
"s_sub_u32 s58, s50, 0x0\n"
"s_mov_b32 s59, 200000\n"
".LH_4p:\n"
"buffer_load_dwordx2 v[164:165], v7, s[40:43], s58 offen sc1\n"
"s_waitcnt vmcnt(0)\n"
"v_cmp_ne_u64 vcc, s[48:49], v[164:165]\n"
"s_cbranch_vccz .LH_4px\n"
"s_sleep 1\n"
"s_sub_u32 s59, s59, 1\n"
"s_cbranch_scc0 .LH_4p\n"
"s_branch .LH_end\n"
".LH_4px:\n"
"s_branch .LH_4r\n"
".LH_5s:\n"
"s_nop 7\n"
"s_cmp_ge_u32 s94, 0xc000000\n"
"s_cbranch_scc1 .LH_5r\n"
"s_mov_b32 s59, 200000\n"
".LH_5p:\n"
"buffer_load_dwordx2 v[166:167], v12, s[40:43], s94 offen offset:0 sc1\n"
"s_waitcnt vmcnt(0)\n"
"v_cmp_ne_u64 vcc, s[48:49], v[166:167]\n"
"s_cbranch_vccz .LH_5px\n"
"s_sleep 1\n"
"s_sub_u32 s59, s59, 1\n"
"s_cbranch_scc0 .LH_5p\n"
"s_branch .LH_end\n"
".LH_5px:\n"
"s_branch .LH_5r\n"
".LH_end:\n"
"s_waitcnt vmcnt(0) lgkmcnt(0)\n"
"s_endpgm\n"
      :: "{v2}"((unsigned)(lds0 + ((wid + 3) & 3) * 18592 + trp)), "{v3}"((unsigned)(lds0 + ((wid + 2) & 3) * 18592 + trp)), "{v4}"((unsigned)(lds0 + ((wid + 1) & 3) * 18592 + trp)), "{v5}"((unsigned)(lane * 16 + f0 * 1024 + 98304)), "{v6}"((unsigned)(lane * 16 + f0 * 1024 + 102400)), "{v7}"((unsigned)((f0 + lane % 7) * 8 + 353024 + H_GL * 1572864)), "{v8}"((unsigned)(lane * 16 + pair * 4096 + kq * 1024 + 212992)), "{v9}"((unsigned)(lane ? DROP : pair * 32 + kq * 8 + 354048)), "{v10}"((unsigned)(lane * 16 + (pg0 + 4) * 4096 + kq * 1024 + 212992)), "{v11}"((unsigned)(lane > 1 ? DROP : (lane ? pg0 + 4 : pair) * 32 + kq * 8 + 354048)), "{v12}"((unsigned)((lane & 31) * 8 + 67463936)), "{v13}"((unsigned)(lane * 16 + 67457024)), "{v14}"((unsigned)((lane & 24) ? DROP : (2 * (lane & 7) + (lane >> 5)) * 16 + 67461120)), "{v15}"((unsigned)(0x3727c5ac)), "{v40}"((unsigned)(vT + tsw)), "{v41}"((unsigned)(vT + (tsw ^ 8))), "{v42}"((unsigned)(vD)), "{s[40:43]}"(rS), "{s[44:47]}"(rW), "{s[48:49]}"(wsp), "{s50}"((unsigned)(sA)), "{s51}"((unsigned)(sLo)), "{s52}"((unsigned)(sHi)), "{s53}"((unsigned)(sEx)), "{s54}"((unsigned)(sEnt)), "{s55}"((unsigned)(sLd)), "{s56}"((unsigned)(sA & 0xfbffffff)), "{s57}"((unsigned)(lds0 + wid * 18592)), "{s60}"((unsigned)(0x3a800000)), "{s61}"((unsigned)(sW0 + 64 * ((wid + 1) & 3))), "{s62}"((unsigned)(sW0 + 64 * ((wid + 2) & 3))), "{s63}"((unsigned)(sW0 + 64 * ((wid + 3) & 3))), "{s64}"((unsigned)(sW0 + 64 * ((wid + 4) & 3))), "{s65}"((unsigned)(sW0 + 256))
      : "v255", "a255", "s95", "vcc", "scc", "memory");

    } else if (grp < 4) {
      const unsigned sEx = sHi + G_EX * 3 * RS, sEnt = -wid & 3;
    asm volatile(
"s_nop 4\n"
"v_mov_b32 v186, 0\n"
"global_load_dwordx2 v[0:1], v186, s[48:49] sc1\n"
"v_mov_b32 v188, 0x3f803f80\n"
"v_mov_b32 v189, 0x3f803f80\n"
"v_mov_b32 v190, 0x3f803f80\n"
"v_mov_b32 v191, 0x3f803f80\n"
"s_add_u32 m0, s55, 0\n"
"s_add_u32 s58, s60, 0\n"
"buffer_load_dwordx4 v38, s[44:47], s58 offen lds\n"
"s_add_u32 m0, s55, 1024\n"
"s_add_u32 s58, s60, 32768\n"
"buffer_load_dwordx4 v38, s[44:47], s58 offen lds\n"
"s_add_u32 m0, s55, 2048\n"
"s_add_u32 s58, s61, 0\n"
"buffer_load_dwordx4 v38, s[44:47], s58 offen lds\n"
"s_add_u32 m0, s55, 3072\n"
"s_add_u32 s58, s61, 32768\n"
"buffer_load_dwordx4 v38, s[44:47], s58 offen lds\n"
"s_add_u32 m0, s55, 4096\n"
"s_add_u32 s58, s62, 0\n"
"buffer_load_dwordx4 v38, s[44:47], s58 offen lds\n"
"s_add_u32 m0, s55, 5120\n"
"s_add_u32 s58, s62, 32768\n"
"buffer_load_dwordx4 v38, s[44:47], s58 offen lds\n"
"s_add_u32 m0, s55, 6144\n"
"s_add_u32 s58, s63, 0\n"
"buffer_load_dwordx4 v38, s[44:47], s58 offen lds\n"
"s_add_u32 m0, s55, 7168\n"
"s_add_u32 s58, s63, 32768\n"
"buffer_load_dwordx4 v38, s[44:47], s58 offen lds\n"
"s_add_u32 m0, s55, 8192\n"
"s_add_u32 s58, s64, 0\n"
"buffer_load_dwordx4 v38, s[44:47], s58 offen lds\n"
"s_add_u32 m0, s55, 9216\n"
"s_add_u32 s58, s64, 32768\n"
"buffer_load_dwordx4 v38, s[44:47], s58 offen lds\n"
"s_add_u32 m0, s55, 10240\n"
"s_add_u32 s58, s60, 65536\n"
"buffer_load_dwordx4 v38, s[44:47], s58 offen lds\n"
"s_add_u32 m0, s55, 11264\n"
"s_add_u32 s58, s60, 98304\n"
"buffer_load_dwordx4 v38, s[44:47], s58 offen lds\n"
"s_add_u32 m0, s55, 12288\n"
"s_add_u32 s58, s61, 65536\n"
"buffer_load_dwordx4 v38, s[44:47], s58 offen lds\n"
"s_add_u32 m0, s55, 13312\n"
"s_add_u32 s58, s61, 98304\n"
"buffer_load_dwordx4 v38, s[44:47], s58 offen lds\n"
"s_add_u32 m0, s55, 14336\n"
"s_add_u32 s58, s62, 65536\n"
"buffer_load_dwordx4 v38, s[44:47], s58 offen lds\n"
"s_add_u32 m0, s55, 15360\n"
"s_add_u32 s58, s62, 98304\n"
"buffer_load_dwordx4 v38, s[44:47], s58 offen lds\n"
"s_add_u32 m0, s55, 16384\n"
"s_add_u32 s58, s63, 65536\n"
"buffer_load_dwordx4 v38, s[44:47], s58 offen lds\n"
"s_add_u32 m0, s55, 17408\n"
"s_add_u32 s58, s63, 98304\n"
"buffer_load_dwordx4 v38, s[44:47], s58 offen lds\n"
"s_add_u32 m0, s55, 18432\n"
"s_add_u32 s58, s64, 65536\n"
"buffer_load_dwordx4 v38, s[44:47], s58 offen lds\n"
"s_add_u32 m0, s55, 19456\n"
"s_add_u32 s58, s64, 98304\n"
"buffer_load_dwordx4 v38, s[44:47], s58 offen lds\n"
"s_add_u32 m0, s55, 20480\n"
"s_add_u32 s58, s60, 131072\n"
"buffer_load_dwordx4 v38, s[44:47], s58 offen lds\n"
"s_add_u32 m0, s55, 21504\n"
"s_add_u32 s58, s60, 163840\n"
"buffer_load_dwordx4 v38, s[44:47], s58 offen lds\n"
"s_add_u32 m0, s55, 22528\n"
"s_add_u32 s58, s61, 131072\n"
"buffer_load_dwordx4 v38, s[44:47], s58 offen lds\n"
"s_add_u32 m0, s55, 23552\n"
"s_add_u32 s58, s61, 163840\n"
"buffer_load_dwordx4 v38, s[44:47], s58 offen lds\n"
"s_add_u32 m0, s55, 24576\n"
"s_add_u32 s58, s62, 131072\n"
"buffer_load_dwordx4 v38, s[44:47], s58 offen lds\n"
"s_add_u32 m0, s55, 25600\n"
"s_add_u32 s58, s62, 163840\n"
"buffer_load_dwordx4 v38, s[44:47], s58 offen lds\n"
"s_add_u32 m0, s55, 26624\n"
"s_add_u32 s58, s63, 131072\n"
"buffer_load_dwordx4 v38, s[44:47], s58 offen lds\n"
"s_add_u32 m0, s55, 27648\n"
"s_add_u32 s58, s63, 163840\n"
"buffer_load_dwordx4 v38, s[44:47], s58 offen lds\n"
"s_add_u32 m0, s55, 28672\n"
"s_add_u32 s58, s64, 131072\n"
"buffer_load_dwordx4 v38, s[44:47], s58 offen lds\n"
"s_add_u32 m0, s55, 29696\n"
"s_add_u32 s58, s64, 163840\n"
"buffer_load_dwordx4 v38, s[44:47], s58 offen lds\n"
"s_add_u32 m0, s55, 30720\n"
"s_add_u32 s58, s60, 196608\n"
"buffer_load_dwordx4 v38, s[44:47], s58 offen lds\n"
"s_add_u32 m0, s55, 31744\n"
"s_add_u32 s58, s60, 229376\n"
"buffer_load_dwordx4 v38, s[44:47], s58 offen lds\n"
"s_add_u32 m0, s55, 32768\n"
"s_add_u32 s58, s61, 196608\n"
"buffer_load_dwordx4 v38, s[44:47], s58 offen lds\n"
"s_add_u32 m0, s55, 33792\n"
"s_add_u32 s58, s61, 229376\n"
"buffer_load_dwordx4 v38, s[44:47], s58 offen lds\n"
"s_add_u32 m0, s55, 34816\n"
"s_add_u32 s58, s62, 196608\n"
"buffer_load_dwordx4 v38, s[44:47], s58 offen lds\n"
"s_add_u32 m0, s55, 35840\n"
"s_add_u32 s58, s62, 229376\n"
"buffer_load_dwordx4 v38, s[44:47], s58 offen lds\n"
"s_add_u32 m0, s55, 36864\n"
"s_add_u32 s58, s63, 196608\n"
"buffer_load_dwordx4 v38, s[44:47], s58 offen lds\n"
"s_add_u32 m0, s55, 37888\n"
"s_add_u32 s58, s63, 229376\n"
"buffer_load_dwordx4 v38, s[44:47], s58 offen lds\n"
"s_add_u32 m0, s55, 38912\n"
"s_add_u32 s58, s64, 196608\n"
"buffer_load_dwordx4 v38, s[44:47], s58 offen lds\n"
"s_add_u32 m0, s55, 39936\n"
"s_add_u32 s58, s64, 229376\n"
"buffer_load_dwordx4 v38, s[44:47], s58 offen lds\n"
"s_waitcnt vmcnt(38)\n"
"ds_read_b64_tr_b16 a[0:1], v36 offset:0\n"
"ds_read_b64_tr_b16 a[2:3], v36 offset:256\n"
"ds_read_b64_tr_b16 a[28:29], v37 offset:0\n"
"ds_read_b64_tr_b16 a[30:31], v37 offset:256\n"
"v_add_u32 v0, 2, v0\n"
"v_add_u32 v1, 0x9e3779b9, v1\n"
"v_or_b32 v0, 1, v0\n"
"v_readfirstlane_b32 s49, v1\n"
"v_readfirstlane_b32 s48, v0\n"
"s_waitcnt vmcnt(36)\n"
"ds_read_b64_tr_b16 a[56:57], v36 offset:2048\n"
"ds_read_b64_tr_b16 a[58:59], v36 offset:2304\n"
"ds_read_b64_tr_b16 a[84:85], v37 offset:2048\n"
"ds_read_b64_tr_b16 a[86:87], v37 offset:2304\n"
"s_waitcnt lgkmcnt(4)\n"
"s_add_u32 m0, s55, 0\n"
"s_add_u32 s58, s60, 262144\n"
"buffer_load_dwordx4 v38, s[44:47], s58 offen lds\n"
"s_add_u32 m0, s55, 1024\n"
"s_add_u32 s58, s60, 294912\n"
"buffer_load_dwordx4 v38, s[44:47], s58 offen lds\n"
"s_waitcnt vmcnt(36)\n"
"ds_read_b64_tr_b16 a[112:113], v36 offset:4096\n"
"ds_read_b64_tr_b16 a[114:115], v36 offset:4352\n"
"ds_read_b64_tr_b16 a[140:141], v37 offset:4096\n"
"ds_read_b64_tr_b16 a[142:143], v37 offset:4352\n"
"s_waitcnt lgkmcnt(4)\n"
"s_add_u32 m0, s55, 2048\n"
"s_add_u32 s58, s61, 262144\n"
"buffer_load_dwordx4 v38, s[44:47], s58 offen lds\n"
"s_add_u32 m0, s55, 3072\n"
"s_add_u32 s58, s61, 294912\n"
"buffer_load_dwordx4 v38, s[44:47], s58 offen lds\n"
"s_waitcnt vmcnt(36)\n"
"ds_read_b64_tr_b16 a[168:169], v36 offset:6144\n"
"ds_read_b64_tr_b16 a[170:171], v36 offset:6400\n"
"ds_read_b64_tr_b16 a[196:197], v37 offset:6144\n"
"ds_read_b64_tr_b16 a[198:199], v37 offset:6400\n"
"s_waitcnt lgkmcnt(4)\n"
"s_add_u32 m0, s55, 4096\n"
"s_add_u32 s58, s62, 262144\n"
"buffer_load_dwordx4 v38, s[44:47], s58 offen lds\n"
"s_add_u32 m0, s55, 5120\n"
"s_add_u32 s58, s62, 294912\n"
"buffer_load_dwordx4 v38, s[44:47], s58 offen lds\n"
"s_waitcnt vmcnt(36)\n"
"ds_read_b64_tr_b16 a[224:225], v36 offset:8192\n"
"ds_read_b64_tr_b16 a[226:227], v36 offset:8448\n"
"ds_read_b64_tr_b16 a[252:253], v37 offset:8192\n"
"ds_read_b64_tr_b16 a[254:255], v37 offset:8448\n"
"s_waitcnt lgkmcnt(4)\n"
"s_add_u32 m0, s55, 6144\n"
"s_add_u32 s58, s63, 262144\n"
"buffer_load_dwordx4 v38, s[44:47], s58 offen lds\n"
"s_add_u32 m0, s55, 7168\n"
"s_add_u32 s58, s63, 294912\n"
"buffer_load_dwordx4 v38, s[44:47], s58 offen lds\n"
"s_waitcnt vmcnt(36)\n"
"ds_read_b64_tr_b16 a[4:5], v36 offset:10240\n"
"ds_read_b64_tr_b16 a[6:7], v36 offset:10496\n"
"ds_read_b64_tr_b16 a[32:33], v37 offset:10240\n"
"ds_read_b64_tr_b16 a[34:35], v37 offset:10496\n"
"s_waitcnt lgkmcnt(4)\n"
"s_add_u32 m0, s55, 8192\n"
"s_add_u32 s58, s64, 262144\n"
"buffer_load_dwordx4 v38, s[44:47], s58 offen lds\n"
"s_add_u32 m0, s55, 9216\n"
"s_add_u32 s58, s64, 294912\n"
"buffer_load_dwordx4 v38, s[44:47], s58 offen lds\n"
"s_waitcnt vmcnt(36)\n"
"ds_read_b64_tr_b16 a[60:61], v36 offset:12288\n"
"ds_read_b64_tr_b16 a[62:63], v36 offset:12544\n"
"ds_read_b64_tr_b16 a[88:89], v37 offset:12288\n"
"ds_read_b64_tr_b16 a[90:91], v37 offset:12544\n"
"s_waitcnt lgkmcnt(4)\n"
"s_add_u32 m0, s55, 10240\n"
"s_add_u32 s58, s60, 327680\n"
"buffer_load_dwordx4 v38, s[44:47], s58 offen lds\n"
"s_add_u32 m0, s55, 11264\n"
"s_add_u32 s58, s60, 360448\n"
"buffer_load_dwordx4 v38, s[44:47], s58 offen lds\n"
"s_waitcnt vmcnt(36)\n"
"ds_read_b64_tr_b16 a[116:117], v36 offset:14336\n"
"ds_read_b64_tr_b16 a[118:119], v36 offset:14592\n"
"ds_read_b64_tr_b16 a[144:145], v37 offset:14336\n"
"ds_read_b64_tr_b16 a[146:147], v37 offset:14592\n"
"s_waitcnt lgkmcnt(4)\n"
"s_add_u32 m0, s55, 12288\n"
"s_add_u32 s58, s61, 327680\n"
"buffer_load_dwordx4 v38, s[44:47], s58 offen lds\n"
"s_add_u32 m0, s55, 13312\n"
"s_add_u32 s58, s61, 360448\n"
"buffer_load_dwordx4 v38, s[44:47], s58 offen lds\n"
"s_waitcnt vmcnt(36)\n"
"ds_read_b64_tr_b16 a[172:173], v36 offset:16384\n"
"ds_read_b64_tr_b16 a[174:175], v36 offset:16640\n"
"ds_read_b64_tr_b16 a[200:201], v37 offset:16384\n"
"ds_read_b64_tr_b16 a[202:203], v37 offset:16640\n"
"s_waitcnt lgkmcnt(4)\n"
"s_add_u32 m0, s55, 14336\n"
"s_add_u32 s58, s62, 327680\n"
"buffer_load_dwordx4 v38, s[44:47], s58 offen lds\n"
"s_add_u32 m0, s55, 15360\n"
"s_add_u32 s58, s62, 360448\n"
"buffer_load_dwordx4 v38, s[44:47], s58 offen lds\n"
"s_waitcnt vmcnt(36)\n"
"ds_read_b64_tr_b16 a[228:229], v36 offset:18432\n"
"ds_read_b64_tr_b16 a[230:231], v36 offset:18688\n"
"ds_read_b64_tr_b16 v[12:13], v37 offset:18432\n"
"ds_read_b64_tr_b16 v[14:15], v37 offset:18688\n"
"s_waitcnt lgkmcnt(4)\n"
"s_add_u32 m0, s55, 16384\n"
"s_add_u32 s58, s63, 327680\n"
"buffer_load_dwordx4 v38, s[44:47], s58 offen lds\n"
"s_add_u32 m0, s55, 17408\n"
"s_add_u32 s58, s63, 360448\n"
"buffer_load_dwordx4 v38, s[44:47], s58 offen lds\n"
"s_waitcnt vmcnt(36)\n"
"ds_read_b64_tr_b16 a[8:9], v36 offset:20480\n"
"ds_read_b64_tr_b16 a[10:11], v36 offset:20736\n"
"ds_read_b64_tr_b16 a[36:37], v37 offset:20480\n"
"ds_read_b64_tr_b16 a[38:39], v37 offset:20736\n"
"s_waitcnt lgkmcnt(4)\n"
"s_add_u32 m0, s55, 18432\n"
"s_add_u32 s58, s64, 327680\n"
"buffer_load_dwordx4 v38, s[44:47], s58 offen lds\n"
"s_add_u32 m0, s55, 19456\n"
"s_add_u32 s58, s64, 360448\n"
"buffer_load_dwordx4 v38, s[44:47], s58 offen lds\n"
"s_waitcnt vmcnt(36)\n"
"ds_read_b64_tr_b16 a[64:65], v36 offset:22528\n"
"ds_read_b64_tr_b16 a[66:67], v36 offset:22784\n"
"ds_read_b64_tr_b16 a[92:93], v37 offset:22528\n"
"ds_read_b64_tr_b16 a[94:95], v37 offset:22784\n"
"s_waitcnt lgkmcnt(4)\n"
"s_add_u32 m0, s55, 20480\n"
"s_add_u32 s58, s60, 393216\n"
"buffer_load_dwordx4 v38, s[44:47], s58 offen lds\n"
"s_add_u32 m0, s55, 21504\n"
"s_add_u32 s58, s60, 425984\n"
"buffer_load_dwordx4 v38, s[44:47], s58 offen lds\n"
"s_waitcnt vmcnt(36)\n"
"ds_read_b64_tr_b16 a[120:121], v36 offset:24576\n"
"ds_read_b64_tr_b16 a[122:123], v36 offset:24832\n"
"ds_read_b64_tr_b16 a[148:149], v37 offset:24576\n"
"ds_read_b64_tr_b16 a[150:151], v37 offset:24832\n"
"s_waitcnt lgkmcnt(4)\n"
"s_add_u32 m0, s55, 22528\n"
"s_add_u32 s58, s61, 393216\n"
"buffer_load_dwordx4 v38, s[44:47], s58 offen lds\n"
"s_add_u32 m0, s55, 23552\n"
"s_add_u32 s58, s61, 425984\n"
"buffer_load_dwordx4 v38, s[44:47], s58 offen lds\n"
"s_waitcnt vmcnt(36)\n"
"ds_read_b64_tr_b16 a[176:177], v36 offset:26624\n"
"ds_read_b64_tr_b16 a[178:179], v36 offset:26880\n"
"ds_read_b64_tr_b16 a[204:205], v37 offset:26624\n"
"ds_read_b64_tr_b16 a[206:207], v37 offset:26880\n"
"s_waitcnt lgkmcnt(4)\n"
"s_add_u32 m0, s55, 24576\n"
"s_add_u32 s58, s62, 393216\n"
"buffer_load_dwordx4 v38, s[44:47], s58 offen lds\n"
"s_add_u32 m0, s55, 25600\n"
"s_add_u32 s58, s62, 425984\n"
"buffer_load_dwordx4 v38, s[44:47], s58 offen lds\n"
"s_waitcnt vmcnt(36)\n"
"ds_read_b64_tr_b16 a[232:233], v36 offset:28672\n"
"ds_read_b64_tr_b16 a[234:235], v36 offset:28928\n"
"ds_read_b64_tr_b16 v[16:17], v37 offset:28672\n"
"ds_read_b64_tr_b16 v[18:19], v37 offset:28928\n"
"s_waitcnt lgkmcnt(4)\n"
"s_add_u32 m0, s55, 26624\n"
"s_add_u32 s58, s63, 393216\n"
"buffer_load_dwordx4 v38, s[44:47], s58 offen lds\n"
"s_add_u32 m0, s55, 27648\n"
"s_add_u32 s58, s63, 425984\n"
"buffer_load_dwordx4 v38, s[44:47], s58 offen lds\n"
"s_waitcnt vmcnt(36)\n"
"ds_read_b64_tr_b16 a[12:13], v36 offset:30720\n"
"ds_read_b64_tr_b16 a[14:15], v36 offset:30976\n"
"ds_read_b64_tr_b16 a[40:41], v37 offset:30720\n"
"ds_read_b64_tr_b16 a[42:43], v37 offset:30976\n"
"s_waitcnt lgkmcnt(4)\n"
"s_add_u32 m0, s55, 28672\n"
"s_add_u32 s58, s64, 393216\n"
"buffer_load_dwordx4 v38, s[44:47], s58 offen lds\n"
"s_add_u32 m0, s55, 29696\n"
"s_add_u32 s58, s64, 425984\n"
"buffer_load_dwordx4 v38, s[44:47], s58 offen lds\n"
"s_waitcnt vmcnt(36)\n"
"ds_read_b64_tr_b16 a[68:69], v36 offset:32768\n"
"ds_read_b64_tr_b16 a[70:71], v36 offset:33024\n"
"ds_read_b64_tr_b16 a[96:97], v37 offset:32768\n"
"ds_read_b64_tr_b16 a[98:99], v37 offset:33024\n"
"s_waitcnt lgkmcnt(4)\n"
"s_waitcnt vmcnt(34)\n"
"ds_read_b64_tr_b16 a[124:125], v36 offset:34816\n"
"ds_read_b64_tr_b16 a[126:127], v36 offset:35072\n"
"ds_read_b64_tr_b16 a[152:153], v37 offset:34816\n"
"ds_read_b64_tr_b16 a[154:155], v37 offset:35072\n"
"s_waitcnt lgkmcnt(4)\n"
"s_waitcnt vmcnt(32)\n"
"ds_read_b64_tr_b16 a[180:181], v36 offset:36864\n"
"ds_read_b64_tr_b16 a[182:183], v36 offset:37120\n"
"ds_read_b64_tr_b16 a[208:209], v37 offset:36864\n"
"ds_read_b64_tr_b16 a[210:211], v37 offset:37120\n"
"s_waitcnt lgkmcnt(4)\n"
"s_waitcnt vmcnt(30)\n"
"ds_read_b64_tr_b16 a[236:237], v36 offset:38912\n"
"ds_read_b64_tr_b16 a[238:239], v36 offset:39168\n"
"ds_read_b64_tr_b16 v[20:21], v37 offset:38912\n"
"ds_read_b64_tr_b16 v[22:23], v37 offset:39168\n"
"s_waitcnt lgkmcnt(4)\n"
"s_waitcnt vmcnt(28)\n"
"ds_read_b64_tr_b16 a[16:17], v36 offset:0\n"
"ds_read_b64_tr_b16 a[18:19], v36 offset:256\n"
"ds_read_b64_tr_b16 a[44:45], v37 offset:0\n"
"ds_read_b64_tr_b16 a[46:47], v37 offset:256\n"
"s_waitcnt lgkmcnt(4)\n"
"s_waitcnt vmcnt(26)\n"
"ds_read_b64_tr_b16 a[72:73], v36 offset:2048\n"
"ds_read_b64_tr_b16 a[74:75], v36 offset:2304\n"
"ds_read_b64_tr_b16 a[100:101], v37 offset:2048\n"
"ds_read_b64_tr_b16 a[102:103], v37 offset:2304\n"
"s_waitcnt lgkmcnt(4)\n"
"s_waitcnt vmcnt(24)\n"
"ds_read_b64_tr_b16 a[128:129], v36 offset:4096\n"
"ds_read_b64_tr_b16 a[130:131], v36 offset:4352\n"
"ds_read_b64_tr_b16 a[156:157], v37 offset:4096\n"
"ds_read_b64_tr_b16 a[158:159], v37 offset:4352\n"
"s_waitcnt lgkmcnt(4)\n"
"s_waitcnt vmcnt(22)\n"
"ds_read_b64_tr_b16 a[184:185], v36 offset:6144\n"
"ds_read_b64_tr_b16 a[186:187], v36 offset:6400\n"
"ds_read_b64_tr_b16 a[212:213], v37 offset:6144\n"
"ds_read_b64_tr_b16 a[214:215], v37 offset:6400\n"
"s_waitcnt lgkmcnt(4)\n"
"s_waitcnt vmcnt(20)\n"
"ds_read_b64_tr_b16 a[240:241], v36 offset:8192\n"
"ds_read_b64_tr_b16 a[242:243], v36 offset:8448\n"
"ds_read_b64_tr_b16 v[24:25], v37 offset:8192\n"
"ds_read_b64_tr_b16 v[26:27], v37 offset:8448\n"
"s_waitcnt lgkmcnt(4)\n"
"s_waitcnt vmcnt(18)\n"
"ds_read_b64_tr_b16 a[20:21], v36 offset:10240\n"
"ds_read_b64_tr_b16 a[22:23], v36 offset:10496\n"
"ds_read_b64_tr_b16 a[48:49], v37 offset:10240\n"
"ds_read_b64_tr_b16 a[50:51], v37 offset:10496\n"
"s_waitcnt lgkmcnt(4)\n"
"s_waitcnt vmcnt(16)\n"
"ds_read_b64_tr_b16 a[76:77], v36 offset:12288\n"
"ds_read_b64_tr_b16 a[78:79], v36 offset:12544\n"
"ds_read_b64_tr_b16 a[104:105], v37 offset:12288\n"
"ds_read_b64_tr_b16 a[106:107], v37 offset:12544\n"
"s_waitcnt lgkmcnt(4)\n"
"s_waitcnt vmcnt(14)\n"
"ds_read_b64_tr_b16 a[132:133], v36 offset:14336\n"
"ds_read_b64_tr_b16 a[134:135], v36 offset:14592\n"
"ds_read_b64_tr_b16 a[160:161], v37 offset:14336\n"
"ds_read_b64_tr_b16 a[162:163], v37 offset:14592\n"
"s_waitcnt lgkmcnt(4)\n"
"s_waitcnt vmcnt(12)\n"
"ds_read_b64_tr_b16 a[188:189], v36 offset:16384\n"
"ds_read_b64_tr_b16 a[190:191], v36 offset:16640\n"
"ds_read_b64_tr_b16 a[216:217], v37 offset:16384\n"
"ds_read_b64_tr_b16 a[218:219], v37 offset:16640\n"
"s_waitcnt lgkmcnt(4)\n"
"s_waitcnt vmcnt(10)\n"
"ds_read_b64_tr_b16 a[244:245], v36 offset:18432\n"
"ds_read_b64_tr_b16 a[246:247], v36 offset:18688\n"
"ds_read_b64_tr_b16 v[28:29], v37 offset:18432\n"
"ds_read_b64_tr_b16 v[30:31], v37 offset:18688\n"
"s_waitcnt lgkmcnt(4)\n"
"s_waitcnt vmcnt(8)\n"
"ds_read_b64_tr_b16 a[24:25], v36 offset:20480\n"
"ds_read_b64_tr_b16 a[26:27], v36 offset:20736\n"
"ds_read_b64_tr_b16 a[52:53], v37 offset:20480\n"
"ds_read_b64_tr_b16 a[54:55], v37 offset:20736\n"
"s_waitcnt lgkmcnt(4)\n"
"s_waitcnt vmcnt(6)\n"
"ds_read_b64_tr_b16 a[80:81], v36 offset:22528\n"
"ds_read_b64_tr_b16 a[82:83], v36 offset:22784\n"
"ds_read_b64_tr_b16 a[108:109], v37 offset:22528\n"
"ds_read_b64_tr_b16 a[110:111], v37 offset:22784\n"
"s_waitcnt lgkmcnt(4)\n"
"s_waitcnt vmcnt(4)\n"
"ds_read_b64_tr_b16 a[136:137], v36 offset:24576\n"
"ds_read_b64_tr_b16 a[138:139], v36 offset:24832\n"
"ds_read_b64_tr_b16 a[164:165], v37 offset:24576\n"
"ds_read_b64_tr_b16 a[166:167], v37 offset:24832\n"
"s_waitcnt lgkmcnt(4)\n"
"s_waitcnt vmcnt(2)\n"
"ds_read_b64_tr_b16 a[192:193], v36 offset:26624\n"
"ds_read_b64_tr_b16 a[194:195], v36 offset:26880\n"
"ds_read_b64_tr_b16 a[220:221], v37 offset:26624\n"
"ds_read_b64_tr_b16 a[222:223], v37 offset:26880\n"
"s_waitcnt lgkmcnt(4)\n"
"s_waitcnt vmcnt(0)\n"
"ds_read_b64_tr_b16 a[248:249], v36 offset:28672\n"
"ds_read_b64_tr_b16 a[250:251], v36 offset:28928\n"
"ds_read_b64_tr_b16 v[32:33], v37 offset:28672\n"
"ds_read_b64_tr_b16 v[34:35], v37 offset:28928\n"
"s_waitcnt lgkmcnt(4)\n"
"s_waitcnt vmcnt(0)\n"
"s_waitcnt lgkmcnt(0)\n"
"s_mov_b32 m0, s57\n"
"s_barrier\n"
"v_mov_b32 v152, 0\n"
"v_mov_b32 v153, 0\n"
"v_mov_b32 v132, 0\n"
"v_mov_b32 v133, 0\n"
"v_mov_b32 v148, 0\n"
"v_mov_b32 v149, 0\n"
"v_mov_b32 v160, 0\n"
"v_mov_b32 v161, 0\n"
"s_mov_b32 s93, 0x80000000\n"
"s_mov_b32 s94, 0x80000000\n"
"s_mov_b32 s94, 0x80000000\n"
"s_mov_b32 s95, 0x80000000\n"
"s_mov_b32 s93, 0x80000000\n"
"s_mov_b32 s95, 0x80000000\n"
"s_mov_b32 s95, 0x80000000\n"
"s_mov_b32 s94, 0x80000000\n"
"s_cmp_eq_u32 s54, 0\n"
"s_cbranch_scc1 .LG_1s\n"
"s_cmp_eq_u32 s54, 1\n"
"s_cbranch_scc1 .LG_2s\n"
"s_cmp_eq_u32 s54, 2\n"
"s_cbranch_scc1 .LG_3s\n"
"s_cmp_eq_u32 s54, 3\n"
"s_cbranch_scc1 .LG_4s\n"
".p2align 6\n"
".LG_top:\n"
"v_mfma_f32_16x16x32_bf16 v[36:39], v[92:95], a[0:3], 0\n"
"v_mfma_f32_16x16x32_bf16 v[36:39], v[96:99], a[4:7], v[36:39]\n"
"s_waitcnt vmcnt(13)\n"
"v_mfma_f32_16x16x32_bf16 v[36:39], v[100:103], a[8:11], v[36:39]\n"
"v_mfma_f32_16x16x32_bf16 v[36:39], v[104:107], a[12:15], v[36:39]\n"
"s_waitcnt vmcnt(11)\n"
"v_mfma_f32_16x16x32_bf16 v[36:39], v[108:111], a[16:19], v[36:39]\n"
"s_waitcnt lgkmcnt(3)\n"
"v_add_f32 v52, v52, v40\n"
"v_add_f32 v53, v53, v41\n"
"v_mfma_f32_16x16x32_bf16 v[36:39], v[112:115], a[20:23], v[36:39]\n"
"s_waitcnt vmcnt(10)\n"
"v_add_f32 v54, v54, v42\n"
"v_add_f32 v55, v55, v43\n"
"v_mfma_f32_16x16x32_bf16 v[36:39], v[116:119], a[24:27], v[36:39]\n"
"v_add_f32 v52, v52, v56\n"
"v_add_f32 v53, v53, v57\n"
"v_mfma_f32_16x16x32_bf16 v[40:43], v[92:95], a[28:31], 0\n"
"v_add_f32 v54, v54, v58\n"
"v_add_f32 v55, v55, v59\n"
"v_mfma_f32_16x16x32_bf16 v[40:43], v[96:99], a[32:35], v[40:43]\n"
"ds_write_addtid_b32 v36 offset:0\n"
"v_add_f32 v52, v52, v44\n"
"v_mfma_f32_16x16x32_bf16 v[40:43], v[100:103], a[36:39], v[40:43]\n"
"ds_write_addtid_b32 v37 offset:4640\n"
"v_add_f32 v53, v53, v45\n"
"v_mfma_f32_16x16x32_bf16 v[40:43], v[104:107], a[40:43], v[40:43]\n"
"ds_write_addtid_b32 v38 offset:9344\n"
"v_add_f32 v54, v54, v46\n"
"v_mfma_f32_16x16x32_bf16 v[40:43], v[108:111], a[44:47], v[40:43]\n"
"ds_write_addtid_b32 v39 offset:13984\n"
"v_add_f32 v55, v55, v47\n"
"v_mfma_f32_16x16x32_bf16 v[40:43], v[112:115], a[48:51], v[40:43]\n"
"s_add_u32 s95, s50, 0xffd00000\n"
"s_and_b32 s94, s95, 0xfbffffff\n"
"v_cvt_pk_bf16_f32 v36, v52, v53\n"
"v_mfma_f32_16x16x32_bf16 v[40:43], v[116:119], a[52:55], v[40:43]\n"
"v_cvt_pk_bf16_f32 v37, v54, v55\n"
"v_mfma_f32_16x16x32_bf16 v[44:47], v[92:95], a[56:59], 0\n"
"s_waitcnt lgkmcnt(4)\n"
"v_add_f32 v60, v60, v64\n"
"v_add_f32 v61, v61, v65\n"
"v_mfma_f32_16x16x32_bf16 v[44:47], v[96:99], a[60:63], v[44:47]\n"
"ds_write_addtid_b32 v40 offset:256\n"
"v_add_f32 v62, v62, v66\n"
"v_mfma_f32_16x16x32_bf16 v[44:47], v[100:103], a[64:67], v[44:47]\n"
"ds_write_addtid_b32 v41 offset:4896\n"
"v_add_f32 v63, v63, v67\n"
"v_mfma_f32_16x16x32_bf16 v[44:47], v[104:107], a[68:71], v[44:47]\n"
"ds_write_addtid_b32 v42 offset:9600\n"
"v_add_f32 v60, v60, v68\n"
"v_mfma_f32_16x16x32_bf16 v[44:47], v[108:111], a[72:75], v[44:47]\n"
"s_waitcnt vmcnt(8)\n"
"buffer_store_dwordx2 v[0:1], v9, s[40:43], s93 offen offset:0 sc1\n"
"ds_write_addtid_b32 v43 offset:14240\n"
"v_add_f32 v61, v61, v69\n"
"v_mfma_f32_16x16x32_bf16 v[44:47], v[112:115], a[76:79], v[44:47]\n"
"v_add_f32 v62, v62, v70\n"
"v_add_f32 v63, v63, v71\n"
"v_mfma_f32_16x16x32_bf16 v[44:47], v[116:119], a[80:83], v[44:47]\n"
"v_add_f32 v60, v60, v48\n"
"v_add_f32 v61, v61, v49\n"
"v_mfma_f32_16x16x32_bf16 v[40:43], v[92:95], a[84:87], 0\n"
"v_add_f32 v62, v62, v50\n"
"v_add_f32 v63, v63, v51\n"
"v_mfma_f32_16x16x32_bf16 v[40:43], v[96:99], a[88:91], v[40:43]\n"
"ds_write_addtid_b32 v44 offset:512\n"
"v_cvt_pk_bf16_f32 v38, v60, v61\n"
"v_mfma_f32_16x16x32_bf16 v[40:43], v[100:103], a[92:95], v[40:43]\n"
"ds_write_addtid_b32 v45 offset:5152\n"
"v_cvt_pk_bf16_f32 v39, v62, v63\n"
"v_mfma_f32_16x16x32_bf16 v[40:43], v[104:107], a[96:99], v[40:43]\n"
"buffer_store_dwordx4 v[36:39], v8, s[40:43], s94 offen sc1\n"
"ds_write_addtid_b32 v46 offset:9856\n"
"v_mfma_f32_16x16x32_bf16 v[40:43], v[108:111], a[100:103], v[40:43]\n"
"ds_write_addtid_b32 v47 offset:14496\n"
"v_mfma_f32_16x16x32_bf16 v[40:43], v[112:115], a[104:107], v[40:43]\n"
"v_mfma_f32_16x16x32_bf16 v[40:43], v[116:119], a[108:111], v[40:43]\n"
"v_mfma_f32_16x16x32_bf16 v[36:39], v[92:95], a[112:115], 0\n"
"v_mfma_f32_16x16x32_bf16 v[36:39], v[96:99], a[116:119], v[36:39]\n"
"ds_write_addtid_b32 v40 offset:768\n"
"v_mfma_f32_16x16x32_bf16 v[36:39], v[100:103], a[120:123], v[36:39]\n"
"ds_write_addtid_b32 v41 offset:5408\n"
"v_mfma_f32_16x16x32_bf16 v[36:39], v[104:107], a[124:127], v[36:39]\n"
"ds_write_addtid_b32 v42 offset:10112\n"
"v_mfma_f32_16x16x32_bf16 v[36:39], v[108:111], a[128:131], v[36:39]\n"
"ds_write_addtid_b32 v43 offset:14752\n"
"v_mfma_f32_16x16x32_bf16 v[36:39], v[112:115], a[132:135], v[36:39]\n"
"v_mfma_f32_16x16x32_bf16 v[36:39], v[116:119], a[136:139], v[36:39]\n"
"v_mfma_f32_16x16x32_bf16 v[44:47], v[92:95], a[140:143], 0\n"
"v_mfma_f32_16x16x32_bf16 v[44:47], v[96:99], a[144:147], v[44:47]\n"
"ds_write_addtid_b32 v36 offset:1024\n"
"v_mfma_f32_16x16x32_bf16 v[44:47], v[100:103], a[148:151], v[44:47]\n"
"ds_write_addtid_b32 v37 offset:5664\n"
"v_mfma_f32_16x16x32_bf16 v[44:47], v[104:107], a[152:155], v[44:47]\n"
"ds_write_addtid_b32 v38 offset:10368\n"
"v_mfma_f32_16x16x32_bf16 v[44:47], v[108:111], a[156:159], v[44:47]\n"
"ds_write_addtid_b32 v39 offset:15008\n"
"v_mfma_f32_16x16x32_bf16 v[44:47], v[112:115], a[160:163], v[44:47]\n"
"s_waitcnt vmcnt(8)\n"
"v_cmp_ne_u64 vcc, s[48:49], v[148:149]\n"
"v_mfma_f32_16x16x32_bf16 v[44:47], v[116:119], a[164:167], v[44:47]\n"
"v_mfma_f32_16x16x32_bf16 v[64:67], a[168:171], v[92:95], 0\n"
"s_cbranch_vccnz .LG_3s\n"
".LG_3r:\n"
"v_mfma_f32_16x16x32_bf16 v[64:67], a[172:175], v[96:99], v[64:67]\n"
"s_add_u32 s50, s50, 0x180000\n"
"s_and_b32 s56, s50, 0xfbffffff\n"
"ds_write_addtid_b32 v44 offset:1280\n"
"v_mfma_f32_16x16x32_bf16 v[64:67], a[176:179], v[100:103], v[64:67]\n"
"ds_write_addtid_b32 v45 offset:5920\n"
"buffer_load_dwordx4 v[36:39], v5, s[40:43], s56 offen offset:0 sc1\n"
"v_mfma_f32_16x16x32_bf16 v[64:67], a[180:183], v[104:107], v[64:67]\n"
"ds_write_addtid_b32 v46 offset:10624\n"
"v_mfma_f32_16x16x32_bf16 v[64:67], a[184:187], v[108:111], v[64:67]\n"
"ds_write_addtid_b32 v47 offset:15264\n"
"buffer_load_dwordx2 v[160:161], v7, s[40:43], s50 offen sc1\n"
"v_mfma_f32_16x16x32_bf16 v[64:67], a[188:191], v[112:115], v[64:67]\n"
"v_mfma_f32_16x16x32_bf16 v[64:67], a[192:195], v[116:119], v[64:67]\n"
"buffer_load_dwordx4 v[40:43], v5, s[40:43], s56 offen offset:1024 sc1\n"
"v_mfma_f32_16x16x32_bf16 v[68:71], a[196:199], v[92:95], 0\n"
"v_mfma_f32_16x16x32_bf16 v[68:71], a[200:203], v[96:99], v[68:71]\n"
"v_mfma_f32_16x16x32_bf16 v[68:71], a[204:207], v[100:103], v[68:71]\n"
"v_mfma_f32_16x16x32_bf16 v[68:71], a[208:211], v[104:107], v[68:71]\n"
"buffer_load_dwordx4 v[44:47], v5, s[40:43], s56 offen offset:2048 sc1\n"
"v_mfma_f32_16x16x32_bf16 v[68:71], a[212:215], v[108:111], v[68:71]\n"
"v_mfma_f32_16x16x32_bf16 v[68:71], a[216:219], v[112:115], v[68:71]\n"
"v_mfma_f32_16x16x32_bf16 v[68:71], a[220:223], v[116:119], v[68:71]\n"
"v_mfma_f32_16x16x32_bf16 v[72:75], a[224:227], v[92:95], 0\n"
"buffer_load_dwordx4 v[48:51], v5, s[40:43], s56 offen offset:3072 sc1\n"
"v_mfma_f32_16x16x32_bf16 v[72:75], a[228:231], v[96:99], v[72:75]\n"
"v_mfma_f32_16x16x32_bf16 v[72:75], a[232:235], v[100:103], v[72:75]\n"
"v_mfma_f32_16x16x32_bf16 v[72:75], a[236:239], v[104:107], v[72:75]\n"
"v_mfma_f32_16x16x32_bf16 v[72:75], a[240:243], v[108:111], v[72:75]\n"
"buffer_load_dwordx4 v[52:55], v6, s[40:43], s56 offen offset:0 sc1\n"
"v_mfma_f32_16x16x32_bf16 v[72:75], a[244:247], v[112:115], v[72:75]\n"
"v_mfma_f32_16x16x32_bf16 v[72:75], a[248:251], v[116:119], v[72:75]\n"
"v_mfma_f32_16x16x32_bf16 v[76:79], a[252:255], v[92:95], 0\n"
"v_mfma_f32_16x16x32_bf16 v[76:79], v[12:15], v[96:99], v[76:79]\n"
"s_waitcnt lgkmcnt(0)\n"
"buffer_load_dwordx4 v[56:59], v6, s[40:43], s56 offen offset:1024 sc1\n"
"v_mfma_f32_16x16x32_bf16 v[76:79], v[16:19], v[100:103], v[76:79]\n"
"s_barrier\n"
"v_mfma_f32_16x16x32_bf16 v[76:79], v[20:23], v[104:107], v[76:79]\n"
"ds_read_b128 v[80:83], v2 offset:2048\n"
"ds_read_b128 v[84:87], v3 offset:512\n"
"v_mfma_f32_16x16x32_bf16 v[76:79], v[24:27], v[108:111], v[76:79]\n"
"ds_read_b128 v[88:91], v4 offset:3072\n"
"ds_read_b128 v[92:95], v2 offset:2304\n"
"v_mfma_f32_16x16x32_bf16 v[76:79], v[28:31], v[112:115], v[76:79]\n"
"buffer_load_dwordx4 v[60:63], v6, s[40:43], s56 offen offset:2048 sc1\n"
"ds_read_b128 v[96:99], v3 offset:768\n"
"v_mfma_f32_16x16x32_bf16 v[76:79], v[32:35], v[116:119], v[76:79]\n"
"s_waitcnt vmcnt(15)\n"
"ds_read_b128 v[100:103], v4 offset:3328\n"
"ds_read_b128 v[104:107], v2 offset:3584\n"
"v_mfma_f32_16x16x32_bf16 v[108:111], v[120:123], a[0:3], 0\n"
"ds_read_b128 v[112:115], v3 offset:1536\n"
"ds_read_b128 v[116:119], v4 offset:3584\n"
"v_mfma_f32_16x16x32_bf16 v[108:111], v[124:127], a[4:7], v[108:111]\n"
"s_waitcnt vmcnt(13)\n"
"ds_read_b128 v[148:151], v2 offset:3840\n"
"ds_read_b128 v[152:155], v3 offset:1792\n"
"v_mfma_f32_16x16x32_bf16 v[108:111], v[128:131], a[8:11], v[108:111]\n"
"ds_read_b128 v[156:159], v4 offset:3840\n"
"v_mfma_f32_16x16x32_bf16 v[108:111], v[132:135], a[12:15], v[108:111]\n"
"s_waitcnt vmcnt(11)\n"
"v_mfma_f32_16x16x32_bf16 v[108:111], v[136:139], a[16:19], v[108:111]\n"
"s_waitcnt lgkmcnt(9)\n"
"v_add_f32 v80, v80, v84\n"
"v_add_f32 v81, v81, v85\n"
"v_mfma_f32_16x16x32_bf16 v[108:111], v[140:143], a[20:23], v[108:111]\n"
"s_waitcnt vmcnt(10)\n"
"v_add_f32 v82, v82, v86\n"
"v_add_f32 v83, v83, v87\n"
"v_mfma_f32_16x16x32_bf16 v[108:111], v[144:147], a[24:27], v[108:111]\n"
"v_add_f32 v80, v80, v88\n"
"v_add_f32 v81, v81, v89\n"
"v_mfma_f32_16x16x32_bf16 v[84:87], v[120:123], a[28:31], 0\n"
"v_add_f32 v82, v82, v90\n"
"v_add_f32 v83, v83, v91\n"
"v_mfma_f32_16x16x32_bf16 v[84:87], v[124:127], a[32:35], v[84:87]\n"
"ds_write_addtid_b32 v108 offset:2048\n"
"v_add_f32 v80, v80, v64\n"
"v_mfma_f32_16x16x32_bf16 v[84:87], v[128:131], a[36:39], v[84:87]\n"
"ds_write_addtid_b32 v109 offset:6688\n"
"v_add_f32 v81, v81, v65\n"
"v_mfma_f32_16x16x32_bf16 v[84:87], v[132:135], a[40:43], v[84:87]\n"
"ds_write_addtid_b32 v110 offset:11392\n"
"v_add_f32 v82, v82, v66\n"
"v_mfma_f32_16x16x32_bf16 v[84:87], v[136:139], a[44:47], v[84:87]\n"
"ds_write_addtid_b32 v111 offset:16032\n"
"v_add_f32 v83, v83, v67\n"
"v_mfma_f32_16x16x32_bf16 v[84:87], v[140:143], a[48:51], v[84:87]\n"
"s_add_u32 s93, s50, 0xffd00000\n"
"s_and_b32 s94, s93, 0xfbffffff\n"
"v_cvt_pk_bf16_f32 v88, v80, v81\n"
"v_mfma_f32_16x16x32_bf16 v[84:87], v[144:147], a[52:55], v[84:87]\n"
"v_cvt_pk_bf16_f32 v89, v82, v83\n"
"v_mfma_f32_16x16x32_bf16 v[64:67], v[120:123], a[56:59], 0\n"
"s_waitcnt lgkmcnt(10)\n"
"v_add_f32 v92, v92, v96\n"
"v_add_f32 v93, v93, v97\n"
"v_mfma_f32_16x16x32_bf16 v[64:67], v[124:127], a[60:63], v[64:67]\n"
"ds_write_addtid_b32 v84 offset:2304\n"
"v_add_f32 v94, v94, v98\n"
"v_mfma_f32_16x16x32_bf16 v[64:67], v[128:131], a[64:67], v[64:67]\n"
"ds_write_addtid_b32 v85 offset:6944\n"
"v_add_f32 v95, v95, v99\n"
"v_mfma_f32_16x16x32_bf16 v[64:67], v[132:135], a[68:71], v[64:67]\n"
"ds_write_addtid_b32 v86 offset:11648\n"
"v_add_f32 v92, v92, v100\n"
"v_mfma_f32_16x16x32_bf16 v[64:67], v[136:139], a[72:75], v[64:67]\n"
"s_waitcnt vmcnt(8)\n"
"buffer_store_dwordx2 v[0:1], v9, s[40:43], s95 offen offset:0 sc1\n"
"ds_write_addtid_b32 v87 offset:16288\n"
"v_add_f32 v93, v93, v101\n"
"v_mfma_f32_16x16x32_bf16 v[64:67], v[140:143], a[76:79], v[64:67]\n"
"v_add_f32 v94, v94, v102\n"
"v_add_f32 v95, v95, v103\n"
"v_mfma_f32_16x16x32_bf16 v[64:67], v[144:147], a[80:83], v[64:67]\n"
"v_add_f32 v92, v92, v68\n"
"v_add_f32 v93, v93, v69\n"
"v_mfma_f32_16x16x32_bf16 v[80:83], v[120:123], a[84:87], 0\n"
"v_add_f32 v94, v94, v70\n"
"v_add_f32 v95, v95, v71\n"
"v_mfma_f32_16x16x32_bf16 v[80:83], v[124:127], a[88:91], v[80:83]\n"
"ds_write_addtid_b32 v64 offset:2560\n"
"v_cvt_pk_bf16_f32 v90, v92, v93\n"
"v_mfma_f32_16x16x32_bf16 v[80:83], v[128:131], a[92:95], v[80:83]\n"
"ds_write_addtid_b32 v65 offset:7200\n"
"v_cvt_pk_bf16_f32 v91, v94, v95\n"
"v_mfma_f32_16x16x32_bf16 v[80:83], v[132:135], a[96:99], v[80:83]\n"
"buffer_store_dwordx4 v[88:91], v8, s[40:43], s94 offen sc1\n"
"ds_write_addtid_b32 v66 offset:11904\n"
"v_mfma_f32_16x16x32_bf16 v[80:83], v[136:139], a[100:103], v[80:83]\n"
"s_waitcnt lgkmcnt(14)\n"
"ds_write_addtid_b32 v67 offset:16544\n"
"v_add_f32 v104, v104, v112\n"
"v_mfma_f32_16x16x32_bf16 v[80:83], v[140:143], a[104:107], v[80:83]\n"
"v_add_f32 v105, v105, v113\n"
"v_add_f32 v106, v106, v114\n"
"v_mfma_f32_16x16x32_bf16 v[80:83], v[144:147], a[108:111], v[80:83]\n"
"v_add_f32 v107, v107, v115\n"
"v_add_f32 v104, v104, v116\n"
"v_mfma_f32_16x16x32_bf16 v[64:67], v[120:123], a[112:115], 0\n"
"v_add_f32 v105, v105, v117\n"
"v_add_f32 v106, v106, v118\n"
"v_mfma_f32_16x16x32_bf16 v[64:67], v[124:127], a[116:119], v[64:67]\n"
"ds_write_addtid_b32 v80 offset:2816\n"
"v_add_f32 v107, v107, v119\n"
"v_mfma_f32_16x16x32_bf16 v[64:67], v[128:131], a[120:123], v[64:67]\n"
"ds_write_addtid_b32 v81 offset:7456\n"
"v_add_f32 v104, v104, v72\n"
"v_mfma_f32_16x16x32_bf16 v[64:67], v[132:135], a[124:127], v[64:67]\n"
"ds_write_addtid_b32 v82 offset:12160\n"
"v_add_f32 v105, v105, v73\n"
"v_mfma_f32_16x16x32_bf16 v[64:67], v[136:139], a[128:131], v[64:67]\n"
"ds_write_addtid_b32 v83 offset:16800\n"
"v_add_f32 v106, v106, v74\n"
"v_mfma_f32_16x16x32_bf16 v[64:67], v[140:143], a[132:135], v[64:67]\n"
"v_add_f32 v107, v107, v75\n"
"v_cvt_pk_bf16_f32 v80, v104, v105\n"
"v_mfma_f32_16x16x32_bf16 v[64:67], v[144:147], a[136:139], v[64:67]\n"
"v_cvt_pk_bf16_f32 v81, v106, v107\n"
"v_mfma_f32_16x16x32_bf16 v[68:71], v[120:123], a[140:143], 0\n"
"s_waitcnt lgkmcnt(15)\n"
"v_add_f32 v148, v148, v152\n"
"v_add_f32 v149, v149, v153\n"
"v_mfma_f32_16x16x32_bf16 v[68:71], v[124:127], a[144:147], v[68:71]\n"
"ds_write_addtid_b32 v64 offset:3072\n"
"v_add_f32 v150, v150, v154\n"
"v_mfma_f32_16x16x32_bf16 v[68:71], v[128:131], a[148:151], v[68:71]\n"
"ds_write_addtid_b32 v65 offset:7712\n"
"v_add_f32 v151, v151, v155\n"
"v_mfma_f32_16x16x32_bf16 v[68:71], v[132:135], a[152:155], v[68:71]\n"
"ds_write_addtid_b32 v66 offset:12416\n"
"v_add_f32 v148, v148, v156\n"
"v_mfma_f32_16x16x32_bf16 v[68:71], v[136:139], a[156:159], v[68:71]\n"
"ds_write_addtid_b32 v67 offset:17056\n"
"v_add_f32 v149, v149, v157\n"
"v_mfma_f32_16x16x32_bf16 v[68:71], v[140:143], a[160:163], v[68:71]\n"
"s_waitcnt vmcnt(8)\n"
"v_cmp_ne_u64 vcc, s[48:49], v[160:161]\n"
"v_add_f32 v150, v150, v158\n"
"v_mfma_f32_16x16x32_bf16 v[68:71], v[144:147], a[164:167], v[68:71]\n"
"v_add_f32 v151, v151, v159\n"
"v_add_f32 v148, v148, v76\n"
"v_mfma_f32_16x16x32_bf16 v[84:87], v[120:123], a[224:227], 0\n"
"s_cbranch_vccnz .LG_4s\n"
".LG_4r:\n"
"v_add_f32 v149, v149, v77\n"
"v_mfma_f32_16x16x32_bf16 v[84:87], v[124:127], a[228:231], v[84:87]\n"
"s_add_u32 s50, s50, 0x180000\n"
"s_and_b32 s56, s50, 0xfbffffff\n"
"ds_write_addtid_b32 v68 offset:3328\n"
"v_mfma_f32_16x16x32_bf16 v[84:87], v[128:131], a[232:235], v[84:87]\n"
"ds_write_addtid_b32 v69 offset:7968\n"
"buffer_load_dwordx4 v[64:67], v5, s[40:43], s56 offen offset:0 sc1\n"
"v_mfma_f32_16x16x32_bf16 v[84:87], v[132:135], a[236:239], v[84:87]\n"
"ds_write_addtid_b32 v70 offset:12672\n"
"v_add_f32 v150, v150, v78\n"
"v_mfma_f32_16x16x32_bf16 v[84:87], v[136:139], a[240:243], v[84:87]\n"
"ds_write_addtid_b32 v71 offset:17312\n"
"buffer_load_dwordx2 v[152:153], v7, s[40:43], s50 offen sc1\n"
"v_mfma_f32_16x16x32_bf16 v[84:87], v[140:143], a[244:247], v[84:87]\n"
"v_add_f32 v151, v151, v79\n"
"v_cvt_pk_bf16_f32 v82, v148, v149\n"
"v_mfma_f32_16x16x32_bf16 v[84:87], v[144:147], a[248:251], v[84:87]\n"
"buffer_load_dwordx4 v[68:71], v5, s[40:43], s56 offen offset:1024 sc1\n"
"v_cvt_pk_bf16_f32 v83, v150, v151\n"
"v_mfma_f32_16x16x32_bf16 v[88:91], v[120:123], a[252:255], 0\n"
"buffer_store_dwordx4 v[80:83], v10, s[40:43], s94 offen sc1\n"
"v_mfma_f32_16x16x32_bf16 v[88:91], v[124:127], v[12:15], v[88:91]\n"
"ds_write_addtid_b32 v84 offset:3584\n"
"v_mfma_f32_16x16x32_bf16 v[88:91], v[128:131], v[16:19], v[88:91]\n"
"ds_write_addtid_b32 v85 offset:8224\n"
"v_mfma_f32_16x16x32_bf16 v[88:91], v[132:135], v[20:23], v[88:91]\n"
"ds_write_addtid_b32 v86 offset:12928\n"
"buffer_load_dwordx4 v[72:75], v5, s[40:43], s56 offen offset:2048 sc1\n"
"v_mfma_f32_16x16x32_bf16 v[88:91], v[136:139], v[24:27], v[88:91]\n"
"ds_write_addtid_b32 v87 offset:17568\n"
"v_mfma_f32_16x16x32_bf16 v[88:91], v[140:143], v[28:31], v[88:91]\n"
"v_mfma_f32_16x16x32_bf16 v[88:91], v[144:147], v[32:35], v[88:91]\n"
"v_mfma_f32_16x16x32_bf16 v[92:95], a[168:171], v[120:123], 0\n"
"buffer_load_dwordx4 v[76:79], v5, s[40:43], s56 offen offset:3072 sc1\n"
"v_mfma_f32_16x16x32_bf16 v[92:95], a[172:175], v[124:127], v[92:95]\n"
"ds_write_addtid_b32 v88 offset:3840\n"
"v_mfma_f32_16x16x32_bf16 v[92:95], a[176:179], v[128:131], v[92:95]\n"
"ds_write_addtid_b32 v89 offset:8480\n"
"v_mfma_f32_16x16x32_bf16 v[92:95], a[180:183], v[132:135], v[92:95]\n"
"ds_write_addtid_b32 v90 offset:13184\n"
"v_mfma_f32_16x16x32_bf16 v[92:95], a[184:187], v[136:139], v[92:95]\n"
"ds_write_addtid_b32 v91 offset:17824\n"
"buffer_load_dwordx4 v[80:83], v6, s[40:43], s56 offen offset:0 sc1\n"
"v_mfma_f32_16x16x32_bf16 v[92:95], a[188:191], v[140:143], v[92:95]\n"
"v_mfma_f32_16x16x32_bf16 v[92:95], a[192:195], v[144:147], v[92:95]\n"
"v_mfma_f32_16x16x32_bf16 v[96:99], a[196:199], v[120:123], 0\n"
"v_mfma_f32_16x16x32_bf16 v[96:99], a[200:203], v[124:127], v[96:99]\n"
"s_waitcnt lgkmcnt(0)\n"
"buffer_load_dwordx4 v[84:87], v6, s[40:43], s56 offen offset:1024 sc1\n"
"v_mfma_f32_16x16x32_bf16 v[96:99], a[204:207], v[128:131], v[96:99]\n"
"s_barrier\n"
"v_mfma_f32_16x16x32_bf16 v[96:99], a[208:211], v[132:135], v[96:99]\n"
"ds_read_b128 v[100:103], v2 offset:0\n"
"ds_read_b128 v[104:107], v3 offset:2560\n"
"v_mfma_f32_16x16x32_bf16 v[96:99], a[212:215], v[136:139], v[96:99]\n"
"ds_read_b128 v[108:111], v4 offset:1024\n"
"ds_read_b128 v[112:115], v2 offset:256\n"
"v_mfma_f32_16x16x32_bf16 v[96:99], a[216:219], v[140:143], v[96:99]\n"
"buffer_load_dwordx4 v[88:91], v6, s[40:43], s56 offen offset:2048 sc1\n"
"ds_read_b128 v[116:119], v3 offset:2816\n"
"v_mfma_f32_16x16x32_bf16 v[96:99], a[220:223], v[144:147], v[96:99]\n"
"s_waitcnt vmcnt(16)\n"
"ds_read_b128 v[120:123], v4 offset:1280\n"
"v_mfma_f32_16x16x32_bf16 v[124:127], v[36:39], a[0:3], 0\n"
"v_mfma_f32_16x16x32_bf16 v[124:127], v[40:43], a[4:7], v[124:127]\n"
"s_waitcnt vmcnt(14)\n"
"v_mfma_f32_16x16x32_bf16 v[124:127], v[44:47], a[8:11], v[124:127]\n"
"v_mfma_f32_16x16x32_bf16 v[124:127], v[48:51], a[12:15], v[124:127]\n"
"s_waitcnt vmcnt(12)\n"
"v_mfma_f32_16x16x32_bf16 v[124:127], v[52:55], a[16:19], v[124:127]\n"
"s_waitcnt lgkmcnt(3)\n"
"v_add_f32 v100, v100, v104\n"
"v_add_f32 v101, v101, v105\n"
"v_mfma_f32_16x16x32_bf16 v[124:127], v[56:59], a[20:23], v[124:127]\n"
"s_waitcnt vmcnt(11)\n"
"v_add_f32 v102, v102, v106\n"
"v_add_f32 v103, v103, v107\n"
"v_mfma_f32_16x16x32_bf16 v[124:127], v[60:63], a[24:27], v[124:127]\n"
"v_add_f32 v100, v100, v108\n"
"v_add_f32 v101, v101, v109\n"
"v_mfma_f32_16x16x32_bf16 v[104:107], v[36:39], a[28:31], 0\n"
"v_add_f32 v102, v102, v110\n"
"v_add_f32 v103, v103, v111\n"
"v_mfma_f32_16x16x32_bf16 v[104:107], v[40:43], a[32:35], v[104:107]\n"
"ds_write_addtid_b32 v124 offset:0\n"
"v_add_f32 v100, v100, v92\n"
"v_mfma_f32_16x16x32_bf16 v[104:107], v[44:47], a[36:39], v[104:107]\n"
"ds_write_addtid_b32 v125 offset:4640\n"
"v_add_f32 v101, v101, v93\n"
"v_mfma_f32_16x16x32_bf16 v[104:107], v[48:51], a[40:43], v[104:107]\n"
"ds_write_addtid_b32 v126 offset:9344\n"
"v_add_f32 v102, v102, v94\n"
"v_mfma_f32_16x16x32_bf16 v[104:107], v[52:55], a[44:47], v[104:107]\n"
"ds_write_addtid_b32 v127 offset:13984\n"
"v_add_f32 v103, v103, v95\n"
"v_mfma_f32_16x16x32_bf16 v[104:107], v[56:59], a[48:51], v[104:107]\n"
"s_add_u32 s94, s50, 0xffd00000\n"
"s_and_b32 s95, s94, 0xfbffffff\n"
"v_cvt_pk_bf16_f32 v108, v100, v101\n"
"v_mfma_f32_16x16x32_bf16 v[104:107], v[60:63], a[52:55], v[104:107]\n"
"v_cvt_pk_bf16_f32 v109, v102, v103\n"
"v_mfma_f32_16x16x32_bf16 v[92:95], v[36:39], a[56:59], 0\n"
"s_waitcnt lgkmcnt(4)\n"
"v_add_f32 v112, v112, v116\n"
"v_add_f32 v113, v113, v117\n"
"v_mfma_f32_16x16x32_bf16 v[92:95], v[40:43], a[60:63], v[92:95]\n"
"ds_write_addtid_b32 v104 offset:256\n"
"v_add_f32 v114, v114, v118\n"
"v_mfma_f32_16x16x32_bf16 v[92:95], v[44:47], a[64:67], v[92:95]\n"
"ds_write_addtid_b32 v105 offset:4896\n"
"v_add_f32 v115, v115, v119\n"
"v_mfma_f32_16x16x32_bf16 v[92:95], v[48:51], a[68:71], v[92:95]\n"
"ds_write_addtid_b32 v106 offset:9600\n"
"v_add_f32 v112, v112, v120\n"
"v_mfma_f32_16x16x32_bf16 v[92:95], v[52:55], a[72:75], v[92:95]\n"
"ds_write_addtid_b32 v107 offset:14240\n"
"v_add_f32 v113, v113, v121\n"
"v_mfma_f32_16x16x32_bf16 v[92:95], v[56:59], a[76:79], v[92:95]\n"
"v_add_f32 v114, v114, v122\n"
"v_add_f32 v115, v115, v123\n"
"v_mfma_f32_16x16x32_bf16 v[92:95], v[60:63], a[80:83], v[92:95]\n"
"v_add_f32 v112, v112, v96\n"
"v_add_f32 v113, v113, v97\n"
"v_mfma_f32_16x16x32_bf16 v[100:103], v[36:39], a[84:87], 0\n"
"v_add_f32 v114, v114, v98\n"
"v_add_f32 v115, v115, v99\n"
"v_mfma_f32_16x16x32_bf16 v[100:103], v[40:43], a[88:91], v[100:103]\n"
"ds_write_addtid_b32 v92 offset:512\n"
"v_cvt_pk_bf16_f32 v110, v112, v113\n"
"v_mfma_f32_16x16x32_bf16 v[100:103], v[44:47], a[92:95], v[100:103]\n"
"ds_write_addtid_b32 v93 offset:5152\n"
"v_cvt_pk_bf16_f32 v111, v114, v115\n"
"v_mfma_f32_16x16x32_bf16 v[100:103], v[48:51], a[96:99], v[100:103]\n"
"buffer_store_dwordx4 v[108:111], v8, s[40:43], s95 offen sc1\n"
"ds_write_addtid_b32 v94 offset:9856\n"
"v_mfma_f32_16x16x32_bf16 v[100:103], v[52:55], a[100:103], v[100:103]\n"
"ds_write_addtid_b32 v95 offset:14496\n"
"v_mfma_f32_16x16x32_bf16 v[100:103], v[56:59], a[104:107], v[100:103]\n"
"v_mfma_f32_16x16x32_bf16 v[100:103], v[60:63], a[108:111], v[100:103]\n"
"v_mfma_f32_16x16x32_bf16 v[96:99], v[36:39], a[112:115], 0\n"
"v_mfma_f32_16x16x32_bf16 v[96:99], v[40:43], a[116:119], v[96:99]\n"
"ds_write_addtid_b32 v100 offset:768\n"
"v_mfma_f32_16x16x32_bf16 v[96:99], v[44:47], a[120:123], v[96:99]\n"
"ds_write_addtid_b32 v101 offset:5408\n"
"v_mfma_f32_16x16x32_bf16 v[96:99], v[48:51], a[124:127], v[96:99]\n"
"ds_write_addtid_b32 v102 offset:10112\n"
"v_mfma_f32_16x16x32_bf16 v[96:99], v[52:55], a[128:131], v[96:99]\n"
"ds_write_addtid_b32 v103 offset:14752\n"
"v_mfma_f32_16x16x32_bf16 v[96:99], v[56:59], a[132:135], v[96:99]\n"
"v_mfma_f32_16x16x32_bf16 v[96:99], v[60:63], a[136:139], v[96:99]\n"
"v_mfma_f32_16x16x32_bf16 v[104:107], v[36:39], a[140:143], 0\n"
"v_mfma_f32_16x16x32_bf16 v[104:107], v[40:43], a[144:147], v[104:107]\n"
"ds_write_addtid_b32 v96 offset:1024\n"
"v_mfma_f32_16x16x32_bf16 v[104:107], v[44:47], a[148:151], v[104:107]\n"
"ds_write_addtid_b32 v97 offset:5664\n"
"v_mfma_f32_16x16x32_bf16 v[104:107], v[48:51], a[152:155], v[104:107]\n"
"ds_write_addtid_b32 v98 offset:10368\n"
"v_mfma_f32_16x16x32_bf16 v[104:107], v[52:55], a[156:159], v[104:107]\n"
"ds_write_addtid_b32 v99 offset:15008\n"
"v_mfma_f32_16x16x32_bf16 v[104:107], v[56:59], a[160:163], v[104:107]\n"
"s_waitcnt vmcnt(8)\n"
"v_cmp_ne_u64 vcc, s[48:49], v[152:153]\n"
"v_mfma_f32_16x16x32_bf16 v[104:107], v[60:63], a[164:167], v[104:107]\n"
"v_mfma_f32_16x16x32_bf16 v[108:111], v[36:39], a[224:227], 0\n"
"s_cbranch_vccnz .LG_1s\n"
".LG_1r:\n"
"v_mfma_f32_16x16x32_bf16 v[108:111], v[40:43], a[228:231], v[108:111]\n"
"s_add_u32 s50, s50, 0x180000\n"
"s_and_b32 s56, s50, 0xfbffffff\n"
"ds_write_addtid_b32 v104 offset:1280\n"
"v_mfma_f32_16x16x32_bf16 v[108:111], v[44:47], a[232:235], v[108:111]\n"
"s_waitcnt vmcnt(6)\n"
"ds_write_addtid_b32 v105 offset:5920\n"
"buffer_load_dwordx4 v[92:95], v5, s[40:43], s56 offen offset:0 sc1\n"
"v_mfma_f32_16x16x32_bf16 v[108:111], v[48:51], a[236:239], v[108:111]\n"
"buffer_store_dwordx2 v[0:1], v11, s[40:43], s93 offen offset:0 sc1\n"
"ds_write_addtid_b32 v106 offset:10624\n"
"v_mfma_f32_16x16x32_bf16 v[108:111], v[52:55], a[240:243], v[108:111]\n"
"ds_write_addtid_b32 v107 offset:15264\n"
"buffer_load_dwordx2 v[132:133], v7, s[40:43], s50 offen sc1\n"
"v_mfma_f32_16x16x32_bf16 v[108:111], v[56:59], a[244:247], v[108:111]\n"
"v_mfma_f32_16x16x32_bf16 v[108:111], v[60:63], a[248:251], v[108:111]\n"
"buffer_load_dwordx4 v[96:99], v5, s[40:43], s56 offen offset:1024 sc1\n"
"v_mfma_f32_16x16x32_bf16 v[112:115], v[36:39], a[252:255], 0\n"
"v_mfma_f32_16x16x32_bf16 v[112:115], v[40:43], v[12:15], v[112:115]\n"
"ds_write_addtid_b32 v108 offset:1536\n"
"v_mfma_f32_16x16x32_bf16 v[112:115], v[44:47], v[16:19], v[112:115]\n"
"ds_write_addtid_b32 v109 offset:6176\n"
"v_mfma_f32_16x16x32_bf16 v[112:115], v[48:51], v[20:23], v[112:115]\n"
"ds_write_addtid_b32 v110 offset:10880\n"
"buffer_load_dwordx4 v[100:103], v5, s[40:43], s56 offen offset:2048 sc1\n"
"v_mfma_f32_16x16x32_bf16 v[112:115], v[52:55], v[24:27], v[112:115]\n"
"ds_write_addtid_b32 v111 offset:15520\n"
"v_mfma_f32_16x16x32_bf16 v[112:115], v[56:59], v[28:31], v[112:115]\n"
"v_mfma_f32_16x16x32_bf16 v[112:115], v[60:63], v[32:35], v[112:115]\n"
"v_mfma_f32_16x16x32_bf16 v[120:123], a[168:171], v[36:39], 0\n"
"buffer_load_dwordx4 v[104:107], v5, s[40:43], s56 offen offset:3072 sc1\n"
"v_mfma_f32_16x16x32_bf16 v[120:123], a[172:175], v[40:43], v[120:123]\n"
"ds_write_addtid_b32 v112 offset:1792\n"
"v_mfma_f32_16x16x32_bf16 v[120:123], a[176:179], v[44:47], v[120:123]\n"
"ds_write_addtid_b32 v113 offset:6432\n"
"v_mfma_f32_16x16x32_bf16 v[120:123], a[180:183], v[48:51], v[120:123]\n"
"ds_write_addtid_b32 v114 offset:11136\n"
"v_mfma_f32_16x16x32_bf16 v[120:123], a[184:187], v[52:55], v[120:123]\n"
"ds_write_addtid_b32 v115 offset:15776\n"
"buffer_load_dwordx4 v[108:111], v6, s[40:43], s56 offen offset:0 sc1\n"
"v_mfma_f32_16x16x32_bf16 v[120:123], a[188:191], v[56:59], v[120:123]\n"
"v_mfma_f32_16x16x32_bf16 v[120:123], a[192:195], v[60:63], v[120:123]\n"
"v_mfma_f32_16x16x32_bf16 v[124:127], a[196:199], v[36:39], 0\n"
"v_mfma_f32_16x16x32_bf16 v[124:127], a[200:203], v[40:43], v[124:127]\n"
"s_waitcnt lgkmcnt(0)\n"
"buffer_load_dwordx4 v[112:115], v6, s[40:43], s56 offen offset:1024 sc1\n"
"v_mfma_f32_16x16x32_bf16 v[124:127], a[204:207], v[44:47], v[124:127]\n"
"s_barrier\n"
"v_mfma_f32_16x16x32_bf16 v[124:127], a[208:211], v[48:51], v[124:127]\n"
"ds_read_b128 v[36:39], v2 offset:2048\n"
"ds_read_b128 v[40:43], v3 offset:512\n"
"v_mfma_f32_16x16x32_bf16 v[124:127], a[212:215], v[52:55], v[124:127]\n"
"ds_read_b128 v[44:47], v4 offset:3072\n"
"ds_read_b128 v[48:51], v2 offset:2304\n"
"v_mfma_f32_16x16x32_bf16 v[124:127], a[216:219], v[56:59], v[124:127]\n"
"buffer_load_dwordx4 v[116:119], v6, s[40:43], s56 offen offset:2048 sc1\n"
"ds_read_b128 v[52:55], v3 offset:768\n"
"v_mfma_f32_16x16x32_bf16 v[124:127], a[220:223], v[60:63], v[124:127]\n"
"ds_read_b128 v[56:59], v4 offset:3328\n"
"v_mfma_f32_16x16x32_bf16 v[128:131], v[64:67], a[0:3], 0\n"
"v_mfma_f32_16x16x32_bf16 v[128:131], v[68:71], a[4:7], v[128:131]\n"
"s_waitcnt vmcnt(13)\n"
"v_mfma_f32_16x16x32_bf16 v[128:131], v[72:75], a[8:11], v[128:131]\n"
"v_mfma_f32_16x16x32_bf16 v[128:131], v[76:79], a[12:15], v[128:131]\n"
"s_waitcnt vmcnt(11)\n"
"v_mfma_f32_16x16x32_bf16 v[128:131], v[80:83], a[16:19], v[128:131]\n"
"s_waitcnt lgkmcnt(3)\n"
"v_add_f32 v36, v36, v40\n"
"v_add_f32 v37, v37, v41\n"
"v_mfma_f32_16x16x32_bf16 v[128:131], v[84:87], a[20:23], v[128:131]\n"
"s_waitcnt vmcnt(10)\n"
"v_add_f32 v38, v38, v42\n"
"v_add_f32 v39, v39, v43\n"
"v_mfma_f32_16x16x32_bf16 v[128:131], v[88:91], a[24:27], v[128:131]\n"
"v_add_f32 v36, v36, v44\n"
"v_add_f32 v37, v37, v45\n"
"v_mfma_f32_16x16x32_bf16 v[40:43], v[64:67], a[28:31], 0\n"
"v_add_f32 v38, v38, v46\n"
"v_add_f32 v39, v39, v47\n"
"v_mfma_f32_16x16x32_bf16 v[40:43], v[68:71], a[32:35], v[40:43]\n"
"ds_write_addtid_b32 v128 offset:2048\n"
"v_add_f32 v36, v36, v120\n"
"v_mfma_f32_16x16x32_bf16 v[40:43], v[72:75], a[36:39], v[40:43]\n"
"ds_write_addtid_b32 v129 offset:6688\n"
"v_add_f32 v37, v37, v121\n"
"v_mfma_f32_16x16x32_bf16 v[40:43], v[76:79], a[40:43], v[40:43]\n"
"ds_write_addtid_b32 v130 offset:11392\n"
"v_add_f32 v38, v38, v122\n"
"v_mfma_f32_16x16x32_bf16 v[40:43], v[80:83], a[44:47], v[40:43]\n"
"ds_write_addtid_b32 v131 offset:16032\n"
"v_add_f32 v39, v39, v123\n"
"v_mfma_f32_16x16x32_bf16 v[40:43], v[84:87], a[48:51], v[40:43]\n"
"s_add_u32 s93, s50, 0xffd00000\n"
"s_and_b32 s95, s93, 0xfbffffff\n"
"v_cvt_pk_bf16_f32 v44, v36, v37\n"
"v_mfma_f32_16x16x32_bf16 v[40:43], v[88:91], a[52:55], v[40:43]\n"
"v_cvt_pk_bf16_f32 v45, v38, v39\n"
"v_mfma_f32_16x16x32_bf16 v[60:63], v[64:67], a[56:59], 0\n"
"s_waitcnt lgkmcnt(4)\n"
"v_add_f32 v48, v48, v52\n"
"v_add_f32 v49, v49, v53\n"
"v_mfma_f32_16x16x32_bf16 v[60:63], v[68:71], a[60:63], v[60:63]\n"
"ds_write_addtid_b32 v40 offset:2304\n"
"v_add_f32 v50, v50, v54\n"
"v_mfma_f32_16x16x32_bf16 v[60:63], v[72:75], a[64:67], v[60:63]\n"
"ds_write_addtid_b32 v41 offset:6944\n"
"v_add_f32 v51, v51, v55\n"
"v_mfma_f32_16x16x32_bf16 v[60:63], v[76:79], a[68:71], v[60:63]\n"
"ds_write_addtid_b32 v42 offset:11648\n"
"v_add_f32 v48, v48, v56\n"
"v_mfma_f32_16x16x32_bf16 v[60:63], v[80:83], a[72:75], v[60:63]\n"
"s_waitcnt vmcnt(9)\n"
"buffer_store_dwordx2 v[0:1], v9, s[40:43], s94 offen offset:0 sc1\n"
"ds_write_addtid_b32 v43 offset:16288\n"
"v_add_f32 v49, v49, v57\n"
"v_mfma_f32_16x16x32_bf16 v[60:63], v[84:87], a[76:79], v[60:63]\n"
"v_add_f32 v50, v50, v58\n"
"v_add_f32 v51, v51, v59\n"
"v_mfma_f32_16x16x32_bf16 v[60:63], v[88:91], a[80:83], v[60:63]\n"
"v_add_f32 v48, v48, v124\n"
"v_add_f32 v49, v49, v125\n"
"v_mfma_f32_16x16x32_bf16 v[36:39], v[64:67], a[84:87], 0\n"
"v_add_f32 v50, v50, v126\n"
"v_add_f32 v51, v51, v127\n"
"v_mfma_f32_16x16x32_bf16 v[36:39], v[68:71], a[88:91], v[36:39]\n"
"ds_write_addtid_b32 v60 offset:2560\n"
"v_cvt_pk_bf16_f32 v46, v48, v49\n"
"v_mfma_f32_16x16x32_bf16 v[36:39], v[72:75], a[92:95], v[36:39]\n"
"ds_write_addtid_b32 v61 offset:7200\n"
"v_cvt_pk_bf16_f32 v47, v50, v51\n"
"v_mfma_f32_16x16x32_bf16 v[36:39], v[76:79], a[96:99], v[36:39]\n"
"buffer_store_dwordx4 v[44:47], v8, s[40:43], s95 offen sc1\n"
"ds_write_addtid_b32 v62 offset:11904\n"
"v_mfma_f32_16x16x32_bf16 v[36:39], v[80:83], a[100:103], v[36:39]\n"
"ds_write_addtid_b32 v63 offset:16544\n"
"v_mfma_f32_16x16x32_bf16 v[36:39], v[84:87], a[104:107], v[36:39]\n"
"v_mfma_f32_16x16x32_bf16 v[36:39], v[88:91], a[108:111], v[36:39]\n"
"v_mfma_f32_16x16x32_bf16 v[40:43], v[64:67], a[112:115], 0\n"
"v_mfma_f32_16x16x32_bf16 v[40:43], v[68:71], a[116:119], v[40:43]\n"
"ds_write_addtid_b32 v36 offset:2816\n"
"v_mfma_f32_16x16x32_bf16 v[40:43], v[72:75], a[120:123], v[40:43]\n"
"ds_write_addtid_b32 v37 offset:7456\n"
"v_mfma_f32_16x16x32_bf16 v[40:43], v[76:79], a[124:127], v[40:43]\n"
"ds_write_addtid_b32 v38 offset:12160\n"
"v_mfma_f32_16x16x32_bf16 v[40:43], v[80:83], a[128:131], v[40:43]\n"
"ds_write_addtid_b32 v39 offset:16800\n"
"v_mfma_f32_16x16x32_bf16 v[40:43], v[84:87], a[132:135], v[40:43]\n"
"v_mfma_f32_16x16x32_bf16 v[40:43], v[88:91], a[136:139], v[40:43]\n"
"v_mfma_f32_16x16x32_bf16 v[44:47], v[64:67], a[140:143], 0\n"
"v_mfma_f32_16x16x32_bf16 v[44:47], v[68:71], a[144:147], v[44:47]\n"
"ds_write_addtid_b32 v40 offset:3072\n"
"v_mfma_f32_16x16x32_bf16 v[44:47], v[72:75], a[148:151], v[44:47]\n"
"ds_write_addtid_b32 v41 offset:7712\n"
"v_mfma_f32_16x16x32_bf16 v[44:47], v[76:79], a[152:155], v[44:47]\n"
"ds_write_addtid_b32 v42 offset:12416\n"
"v_mfma_f32_16x16x32_bf16 v[44:47], v[80:83], a[156:159], v[44:47]\n"
"ds_write_addtid_b32 v43 offset:17056\n"
"v_mfma_f32_16x16x32_bf16 v[44:47], v[84:87], a[160:163], v[44:47]\n"
"s_waitcnt vmcnt(8)\n"
"v_cmp_ne_u64 vcc, s[48:49], v[132:133]\n"
"v_mfma_f32_16x16x32_bf16 v[44:47], v[88:91], a[164:167], v[44:47]\n"
"v_mfma_f32_16x16x32_bf16 v[36:39], v[64:67], a[224:227], 0\n"
"s_cbranch_vccnz .LG_2s\n"
".LG_2r:\n"
"v_mfma_f32_16x16x32_bf16 v[36:39], v[68:71], a[228:231], v[36:39]\n"
"s_add_u32 s50, s50, 0x180000\n"
"s_and_b32 s56, s50, 0xfbffffff\n"
"ds_write_addtid_b32 v44 offset:3328\n"
"v_mfma_f32_16x16x32_bf16 v[36:39], v[72:75], a[232:235], v[36:39]\n"
"ds_write_addtid_b32 v45 offset:7968\n"
"buffer_load_dwordx4 v[120:123], v5, s[40:43], s56 offen offset:0 sc1\n"
"v_mfma_f32_16x16x32_bf16 v[36:39], v[76:79], a[236:239], v[36:39]\n"
"ds_write_addtid_b32 v46 offset:12672\n"
"v_mfma_f32_16x16x32_bf16 v[36:39], v[80:83], a[240:243], v[36:39]\n"
"ds_write_addtid_b32 v47 offset:17312\n"
"buffer_load_dwordx2 v[148:149], v7, s[40:43], s50 offen sc1\n"
"v_mfma_f32_16x16x32_bf16 v[36:39], v[84:87], a[244:247], v[36:39]\n"
"v_mfma_f32_16x16x32_bf16 v[36:39], v[88:91], a[248:251], v[36:39]\n"
"buffer_load_dwordx4 v[124:127], v5, s[40:43], s56 offen offset:1024 sc1\n"
"v_mfma_f32_16x16x32_bf16 v[40:43], v[64:67], a[252:255], 0\n"
"v_mfma_f32_16x16x32_bf16 v[40:43], v[68:71], v[12:15], v[40:43]\n"
"ds_write_addtid_b32 v36 offset:3584\n"
"v_mfma_f32_16x16x32_bf16 v[40:43], v[72:75], v[16:19], v[40:43]\n"
"ds_write_addtid_b32 v37 offset:8224\n"
"v_mfma_f32_16x16x32_bf16 v[40:43], v[76:79], v[20:23], v[40:43]\n"
"ds_write_addtid_b32 v38 offset:12928\n"
"buffer_load_dwordx4 v[128:131], v5, s[40:43], s56 offen offset:2048 sc1\n"
"v_mfma_f32_16x16x32_bf16 v[40:43], v[80:83], v[24:27], v[40:43]\n"
"ds_write_addtid_b32 v39 offset:17568\n"
"v_mfma_f32_16x16x32_bf16 v[40:43], v[84:87], v[28:31], v[40:43]\n"
"v_mfma_f32_16x16x32_bf16 v[40:43], v[88:91], v[32:35], v[40:43]\n"
"v_mfma_f32_16x16x32_bf16 v[44:47], a[168:171], v[64:67], 0\n"
"buffer_load_dwordx4 v[132:135], v5, s[40:43], s56 offen offset:3072 sc1\n"
"v_mfma_f32_16x16x32_bf16 v[44:47], a[172:175], v[68:71], v[44:47]\n"
"ds_write_addtid_b32 v40 offset:3840\n"
"v_mfma_f32_16x16x32_bf16 v[44:47], a[176:179], v[72:75], v[44:47]\n"
"ds_write_addtid_b32 v41 offset:8480\n"
"v_mfma_f32_16x16x32_bf16 v[44:47], a[180:183], v[76:79], v[44:47]\n"
"ds_write_addtid_b32 v42 offset:13184\n"
"v_mfma_f32_16x16x32_bf16 v[44:47], a[184:187], v[80:83], v[44:47]\n"
"ds_write_addtid_b32 v43 offset:17824\n"
"buffer_load_dwordx4 v[136:139], v6, s[40:43], s56 offen offset:0 sc1\n"
"v_mfma_f32_16x16x32_bf16 v[44:47], a[188:191], v[84:87], v[44:47]\n"
"v_mfma_f32_16x16x32_bf16 v[44:47], a[192:195], v[88:91], v[44:47]\n"
"v_mfma_f32_16x16x32_bf16 v[48:51], a[196:199], v[64:67], 0\n"
"v_mfma_f32_16x16x32_bf16 v[48:51], a[200:203], v[68:71], v[48:51]\n"
"s_waitcnt lgkmcnt(0)\n"
"buffer_load_dwordx4 v[140:143], v6, s[40:43], s56 offen offset:1024 sc1\n"
"v_mfma_f32_16x16x32_bf16 v[48:51], a[204:207], v[72:75], v[48:51]\n"
"s_barrier\n"
"v_mfma_f32_16x16x32_bf16 v[48:51], a[208:211], v[76:79], v[48:51]\n"
"ds_read_b128 v[52:55], v2 offset:0\n"
"ds_read_b128 v[40:43], v3 offset:2560\n"
"v_mfma_f32_16x16x32_bf16 v[48:51], a[212:215], v[80:83], v[48:51]\n"
"ds_read_b128 v[56:59], v4 offset:1024\n"
"ds_read_b128 v[60:63], v2 offset:256\n"
"v_mfma_f32_16x16x32_bf16 v[48:51], a[216:219], v[84:87], v[48:51]\n"
"buffer_load_dwordx4 v[144:147], v6, s[40:43], s56 offen offset:2048 sc1\n"
"ds_read_b128 v[64:67], v3 offset:2816\n"
"v_mfma_f32_16x16x32_bf16 v[48:51], a[220:223], v[88:91], v[48:51]\n"
"s_waitcnt vmcnt(15)\n"
"ds_read_b128 v[68:71], v4 offset:1280\n"
"s_branch .LG_top\n"
".LG_1s:\n"
"s_nop 7\n"
"s_cmp_ge_u32 s50, s53\n"
"s_cbranch_scc1 .LG_end\n"
"s_cmp_ge_u32 s50, s52\n"
"s_cbranch_scc1 .LG_1r\n"
"s_sub_u32 s58, s50, 0x0\n"
"s_mov_b32 s59, 200000\n"
".LG_1p:\n"
"buffer_load_dwordx2 v[152:153], v7, s[40:43], s58 offen sc1\n"
"s_waitcnt vmcnt(0)\n"
"v_cmp_ne_u64 vcc, s[48:49], v[152:153]\n"
"s_cbranch_vccz .LG_1px\n"
"s_sleep 1\n"
"s_sub_u32 s59, s59, 1\n"
"s_cbranch_scc0 .LG_1p\n"
"s_branch .LG_end\n"
".LG_1px:\n"
"s_branch .LG_1r\n"
".LG_2s:\n"
"s_nop 7\n"
"s_cmp_ge_u32 s50, s53\n"
"s_cbranch_scc1 .LG_end\n"
"s_cmp_ge_u32 s50, s52\n"
"s_cbranch_scc1 .LG_2r\n"
"s_sub_u32 s58, s50, 0x0\n"
"s_mov_b32 s59, 200000\n"
".LG_2p:\n"
"buffer_load_dwordx2 v[132:133], v7, s[40:43], s58 offen sc1\n"
"s_waitcnt vmcnt(0)\n"
"v_cmp_ne_u64 vcc, s[48:49], v[132:133]\n"
"s_cbranch_vccz .LG_2px\n"
"s_sleep 1\n"
"s_sub_u32 s59, s59, 1\n"
"s_cbranch_scc0 .LG_2p\n"
"s_branch .LG_end\n"
".LG_2px:\n"
"s_branch .LG_2r\n"
".LG_3s:\n"
"s_nop 7\n"
"s_cmp_ge_u32 s50, s53\n"
"s_cbranch_scc1 .LG_end\n"
"s_cmp_ge_u32 s50, s52\n"
"s_cbranch_scc1 .LG_3r\n"
"s_sub_u32 s58, s50, 0x0\n"
"s_mov_b32 s59, 200000\n"
".LG_3p:\n"
"buffer_load_dwordx2 v[148:149], v7, s[40:43], s58 offen sc1\n"
"s_waitcnt vmcnt(0)\n"
"v_cmp_ne_u64 vcc, s[48:49], v[148:149]\n"
"s_cbranch_vccz .LG_3px\n"
"s_sleep 1\n"
"s_sub_u32 s59, s59, 1\n"
"s_cbranch_scc0 .LG_3p\n"
"s_branch .LG_end\n"
".LG_3px:\n"
"s_branch .LG_3r\n"
".LG_4s:\n"
"s_nop 7\n"
"s_cmp_ge_u32 s50, s53\n"
"s_cbranch_scc1 .LG_end\n"
"s_cmp_ge_u32 s50, s52\n"
"s_cbranch_scc1 .LG_4r\n"
"s_sub_u32 s58, s50, 0x0\n"
"s_mov_b32 s59, 200000\n"
".LG_4p:\n"
"buffer_load_dwordx2 v[160:161], v7, s[40:43], s58 offen sc1\n"
"s_waitcnt vmcnt(0)\n"
"v_cmp_ne_u64 vcc, s[48:49], v[160:161]\n"
"s_cbranch_vccz .LG_4px\n"
"s_sleep 1\n"
"s_sub_u32 s59, s59, 1\n"
"s_cbranch_scc0 .LG_4p\n"
"s_branch .LG_end\n"
".LG_4px:\n"
"s_branch .LG_4r\n"
".LG_end:\n"
"s_waitcnt vmcnt(0) lgkmcnt(0)\n"
"s_endpgm\n"
      :: "{v2}"((unsigned)(lds0 + ((wid + 3) & 3) * 18592 + trp)), "{v3}"((unsigned)(lds0 + ((wid + 2) & 3) * 18592 + trp)), "{v4}"((unsigned)(lds0 + ((wid + 1) & 3) * 18592 + trp)), "{v5}"((unsigned)(lane * 16 + f0 * 1024 + 98304)), "{v6}"((unsigned)(lane * 16 + f0 * 1024 + 102400)), "{v7}"((unsigned)((f0 + lane % 7) * 8 + 353024 + G_GL * 1572864)), "{v8}"((unsigned)(lane * 16 + pair * 4096 + kq * 1024 + 212992)), "{v9}"((unsigned)(lane ? DROP : pair * 32 + kq * 8 + 354048)), "{v10}"((unsigned)(lane * 16 + (pg0 + 4) * 4096 + kq * 1024 + 212992)), "{v11}"((unsigned)(lane > 1 ? DROP : (lane ? pg0 + 4 : pair) * 32 + kq * 8 + 354048)), "{v36}"((unsigned)(vT + tsw)), "{v37}"((unsigned)(vT + (tsw ^ 8))), "{v38}"((unsigned)(vD)), "{s[40:43]}"(rS), "{s[44:47]}"(rW), "{s[48:49]}"(wsp), "{s50}"((unsigned)(sA)), "{s51}"((unsigned)(sLo)), "{s52}"((unsigned)(sHi)), "{s53}"((unsigned)(sEx)), "{s54}"((unsigned)(sEnt)), "{s55}"((unsigned)(sLd)), "{s56}"((unsigned)(sA & 0xfbffffff)), "{s57}"((unsigned)(lds0 + wid * 18592)), "{s60}"((unsigned)(sW0 + 64 * ((wid + 1) & 3))), "{s61}"((unsigned)(sW0 + 64 * ((wid + 2) & 3))), "{s62}"((unsigned)(sW0 + 64 * ((wid + 3) & 3))), "{s63}"((unsigned)(sW0 + 64 * ((wid + 4) & 3))), "{s64}"((unsigned)(sW0 + 256))
      : "v255", "a255", "s95", "vcc", "scc", "memory");

    } else {
      const int v16 = kq * 4 + wid;
      const unsigned sEx = sHi + (J_EX + (set == 1)) * 3 * RS, sEnt = grp - 4;
      const u32x4 rO = desc(a.out, 2048 * 2048), bia0 = *(const u32x4*)(a.b2 + v16 * 32 + lg * 8), bia1 = *(const u32x4*)(a.b2 + 512 + v16 * 32 + lg * 8);
      const unsigned se = lg == (li >> 2) ? 0x3f80u << (16 * (li & 1)) : 0u, sel0 = (li & 2) ? 0u : se, sel1 = (li & 2) ? se : 0u;
      const u64 ep = (set == 2 && idx == 27 && wid == 0) ? (u64)a.ws : 0ull;
    asm volatile(
"s_nop 4\n"
"v_mov_b32 v186, 0\n"
"global_load_dwordx2 v[0:1], v186, s[48:49] sc1\n"
"v_mov_b32 v188, 0x3f803f80\n"
"v_mov_b32 v189, 0x3f803f80\n"
"v_mov_b32 v190, 0x3f803f80\n"
"v_mov_b32 v191, 0x3f803f80\n"
"s_add_u32 m0, s55, 0\n"
"s_add_u32 s58, s81, 0\n"
"buffer_load_dwordx4 v38, s[44:47], s58 offen lds\n"
"s_add_u32 m0, s55, 1024\n"
"s_add_u32 s58, s81, 32768\n"
"buffer_load_dwordx4 v38, s[44:47], s58 offen lds\n"
"s_add_u32 m0, s55, 2048\n"
"s_add_u32 s58, s82, 0\n"
"buffer_load_dwordx4 v38, s[44:47], s58 offen lds\n"
"s_add_u32 m0, s55, 3072\n"
"s_add_u32 s58, s82, 32768\n"
"buffer_load_dwordx4 v38, s[44:47], s58 offen lds\n"
"s_add_u32 m0, s55, 4096\n"
"s_add_u32 s58, s83, 0\n"
"buffer_load_dwordx4 v38, s[44:47], s58 offen lds\n"
"s_add_u32 m0, s55, 5120\n"
"s_add_u32 s58, s83, 32768\n"
"buffer_load_dwordx4 v38, s[44:47], s58 offen lds\n"
"s_add_u32 m0, s55, 6144\n"
"s_add_u32 s58, s84, 0\n"
"buffer_load_dwordx4 v38, s[44:47], s58 offen lds\n"
"s_add_u32 m0, s55, 7168\n"
"s_add_u32 s58, s84, 32768\n"
"buffer_load_dwordx4 v38, s[44:47], s58 offen lds\n"
"s_add_u32 m0, s55, 8192\n"
"s_add_u32 s58, s81, 65536\n"
"buffer_load_dwordx4 v38, s[44:47], s58 offen lds\n"
"s_add_u32 m0, s55, 9216\n"
"s_add_u32 s58, s81, 98304\n"
"buffer_load_dwordx4 v38, s[44:47], s58 offen lds\n"
"s_add_u32 m0, s55, 10240\n"
"s_add_u32 s58, s82, 65536\n"
"buffer_load_dwordx4 v38, s[44:47], s58 offen lds\n"
"s_add_u32 m0, s55, 11264\n"
"s_add_u32 s58, s82, 98304\n"
"buffer_load_dwordx4 v38, s[44:47], s58 offen lds\n"
"s_add_u32 m0, s55, 12288\n"
"s_add_u32 s58, s83, 65536\n"
"buffer_load_dwordx4 v38, s[44:47], s58 offen lds\n"
"s_add_u32 m0, s55, 13312\n"
"s_add_u32 s58, s83, 98304\n"
"buffer_load_dwordx4 v38, s[44:47], s58 offen lds\n"
"s_add_u32 m0, s55, 14336\n"
"s_add_u32 s58, s84, 65536\n"
"buffer_load_dwordx4 v38, s[44:47], s58 offen lds\n"
"s_add_u32 m0, s55, 15360\n"
"s_add_u32 s58, s84, 98304\n"
"buffer_load_dwordx4 v38, s[44:47], s58 offen lds\n"
"s_add_u32 m0, s55, 16384\n"
"s_add_u32 s58, s81, 131072\n"
"buffer_load_dwordx4 v38, s[44:47], s58 offen lds\n"
"s_add_u32 m0, s55, 17408\n"
"s_add_u32 s58, s81, 163840\n"
"buffer_load_dwordx4 v38, s[44:47], s58 offen lds\n"
"s_add_u32 m0, s55, 18432\n"
"s_add_u32 s58, s82, 131072\n"
"buffer_load_dwordx4 v38, s[44:47], s58 offen lds\n"
"s_add_u32 m0, s55, 19456\n"
"s_add_u32 s58, s82, 163840\n"
"buffer_load_dwordx4 v38, s[44:47], s58 offen lds\n"
"s_add_u32 m0, s55, 20480\n"
"s_add_u32 s58, s83, 131072\n"
"buffer_load_dwordx4 v38, s[44:47], s58 offen lds\n"
"s_add_u32 m0, s55, 21504\n"
"s_add_u32 s58, s83, 163840\n"
"buffer_load_dwordx4 v38, s[44:47], s58 offen lds\n"
"s_add_u32 m0, s55, 22528\n"
"s_add_u32 s58, s84, 131072\n"
"buffer_load_dwordx4 v38, s[44:47], s58 offen lds\n"
"s_add_u32 m0, s55, 23552\n"
"s_add_u32 s58, s84, 163840\n"
"buffer_load_dwordx4 v38, s[44:47], s58 offen lds\n"
"s_add_u32 m0, s55, 24576\n"
"s_add_u32 s58, s81, 196608\n"
"buffer_load_dwordx4 v38, s[44:47], s58 offen lds\n"
"s_add_u32 m0, s55, 25600\n"
"s_add_u32 s58, s81, 229376\n"
"buffer_load_dwordx4 v38, s[44:47], s58 offen lds\n"
"s_add_u32 m0, s55, 26624\n"
"s_add_u32 s58, s82, 196608\n"
"buffer_load_dwordx4 v38, s[44:47], s58 offen lds\n"
"s_add_u32 m0, s55, 27648\n"
"s_add_u32 s58, s82, 229376\n"
"buffer_load_dwordx4 v38, s[44:47], s58 offen lds\n"
"s_add_u32 m0, s55, 28672\n"
"s_add_u32 s58, s83, 196608\n"
"buffer_load_dwordx4 v38, s[44:47], s58 offen lds\n"
"s_add_u32 m0, s55, 29696\n"
"s_add_u32 s58, s83, 229376\n"
"buffer_load_dwordx4 v38, s[44:47], s58 offen lds\n"
"s_add_u32 m0, s55, 30720\n"
"s_add_u32 s58, s84, 196608\n"
"buffer_load_dwordx4 v38, s[44:47], s58 offen lds\n"
"s_add_u32 m0, s55, 31744\n"
"s_add_u32 s58, s84, 229376\n"
"buffer_load_dwordx4 v38, s[44:47], s58 offen lds\n"
"s_add_u32 m0, s55, 32768\n"
"s_add_u32 s58, s81, 262144\n"
"buffer_load_dwordx4 v38, s[44:47], s58 offen lds\n"
"s_add_u32 m0, s55, 33792\n"
"s_add_u32 s58, s81, 294912\n"
"buffer_load_dwordx4 v38, s[44:47], s58 offen lds\n"
"s_add_u32 m0, s55, 34816\n"
"s_add_u32 s58, s82, 262144\n"
"buffer_load_dwordx4 v38, s[44:47], s58 offen lds\n"
"s_add_u32 m0, s55, 35840\n"
"s_add_u32 s58, s82, 294912\n"
"buffer_load_dwordx4 v38, s[44:47], s58 offen lds\n"
"s_add_u32 m0, s55, 36864\n"
"s_add_u32 s58, s83, 262144\n"
"buffer_load_dwordx4 v38, s[44:47], s58 offen lds\n"
"s_add_u32 m0, s55, 37888\n"
"s_add_u32 s58, s83, 294912\n"
"buffer_load_dwordx4 v38, s[44:47], s58 offen lds\n"
"s_add_u32 m0, s55, 38912\n"
"s_add_u32 s58, s84, 262144\n"
"buffer_load_dwordx4 v38, s[44:47], s58 offen lds\n"
"s_add_u32 m0, s55, 39936\n"
"s_add_u32 s58, s84, 294912\n"
"buffer_load_dwordx4 v38, s[44:47], s58 offen lds\n"
"s_waitcnt vmcnt(38)\n"
"ds_read_b64_tr_b16 a[0:1], v36 offset:0\n"
"ds_read_b64_tr_b16 a[2:3], v36 offset:256\n"
"ds_read_b64_tr_b16 a[28:29], v37 offset:0\n"
"ds_read_b64_tr_b16 a[30:31], v37 offset:256\n"
"v_add_u32 v0, 2, v0\n"
"v_add_u32 v1, 0x9e3779b9, v1\n"
"v_or_b32 v0, 1, v0\n"
"v_readfirstlane_b32 s49, v1\n"
"v_readfirstlane_b32 s48, v0\n"
"s_waitcnt vmcnt(36)\n"
"ds_read_b64_tr_b16 a[56:57], v36 offset:2048\n"
"ds_read_b64_tr_b16 a[58:59], v36 offset:2304\n"
"ds_read_b64_tr_b16 a[84:85], v37 offset:2048\n"
"ds_read_b64_tr_b16 a[86:87], v37 offset:2304\n"
"s_waitcnt lgkmcnt(4)\n"
"s_add_u32 m0, s55, 0\n"
"s_add_u32 s58, s81, 327680\n"
"buffer_load_dwordx4 v38, s[44:47], s58 offen lds\n"
"s_add_u32 m0, s55, 1024\n"
"s_add_u32 s58, s81, 360448\n"
"buffer_load_dwordx4 v38, s[44:47], s58 offen lds\n"
"s_waitcnt vmcnt(36)\n"
"ds_read_b64_tr_b16 a[112:113], v36 offset:4096\n"
"ds_read_b64_tr_b16 a[114:115], v36 offset:4352\n"
"ds_read_b64_tr_b16 a[140:141], v37 offset:4096\n"
"ds_read_b64_tr_b16 a[142:143], v37 offset:4352\n"
"s_waitcnt lgkmcnt(4)\n"
"s_add_u32 m0, s55, 2048\n"
"s_add_u32 s58, s82, 327680\n"
"buffer_load_dwordx4 v38, s[44:47], s58 offen lds\n"
"s_add_u32 m0, s55, 3072\n"
"s_add_u32 s58, s82, 360448\n"
"buffer_load_dwordx4 v38, s[44:47], s58 offen lds\n"
"s_waitcnt vmcnt(36)\n"
"ds_read_b64_tr_b16 a[168:169], v36 offset:6144\n"
"ds_read_b64_tr_b16 a[170:171], v36 offset:6400\n"
"ds_read_b64_tr_b16 a[196:197], v37 offset:6144\n"
"ds_read_b64_tr_b16 a[198:199], v37 offset:6400\n"
"s_waitcnt lgkmcnt(4)\n"
"s_add_u32 m0, s55, 4096\n"
"s_add_u32 s58, s83, 327680\n"
"buffer_load_dwordx4 v38, s[44:47], s58 offen lds\n"
"s_add_u32 m0, s55, 5120\n"
"s_add_u32 s58, s83, 360448\n"
"buffer_load_dwordx4 v38, s[44:47], s58 offen lds\n"
"s_waitcnt vmcnt(36)\n"
"ds_read_b64_tr_b16 a[4:5], v36 offset:8192\n"
"ds_read_b64_tr_b16 a[6:7], v36 offset:8448\n"
"ds_read_b64_tr_b16 a[32:33], v37 offset:8192\n"
"ds_read_b64_tr_b16 a[34:35], v37 offset:8448\n"
"s_waitcnt lgkmcnt(4)\n"
"s_add_u32 m0, s55, 6144\n"
"s_add_u32 s58, s84, 327680\n"
"buffer_load_dwordx4 v38, s[44:47], s58 offen lds\n"
"s_add_u32 m0, s55, 7168\n"
"s_add_u32 s58, s84, 360448\n"
"buffer_load_dwordx4 v38, s[44:47], s58 offen lds\n"
"s_waitcnt vmcnt(36)\n"
"ds_read_b64_tr_b16 a[60:61], v36 offset:10240\n"
"ds_read_b64_tr_b16 a[62:63], v36 offset:10496\n"
"ds_read_b64_tr_b16 a[88:89], v37 offset:10240\n"
"ds_read_b64_tr_b16 a[90:91], v37 offset:10496\n"
"s_waitcnt lgkmcnt(4)\n"
"s_add_u32 m0, s55, 8192\n"
"s_add_u32 s58, s81, 393216\n"
"buffer_load_dwordx4 v38, s[44:47], s58 offen lds\n"
"s_add_u32 m0, s55, 9216\n"
"s_add_u32 s58, s81, 425984\n"
"buffer_load_dwordx4 v38, s[44:47], s58 offen lds\n"
"s_waitcnt vmcnt(36)\n"
"ds_read_b64_tr_b16 a[116:117], v36 offset:12288\n"
"ds_read_b64_tr_b16 a[118:119], v36 offset:12544\n"
"ds_read_b64_tr_b16 a[144:145], v37 offset:12288\n"
"ds_read_b64_tr_b16 a[146:147], v37 offset:12544\n"
"s_waitcnt lgkmcnt(4)\n"
"s_add_u32 m0, s55, 10240\n"
"s_add_u32 s58, s82, 393216\n"
"buffer_load_dwordx4 v38, s[44:47], s58 offen lds\n"
"s_add_u32 m0, s55, 11264\n"
"s_add_u32 s58, s82, 425984\n"
"buffer_load_dwordx4 v38, s[44:47], s58 offen lds\n"
"s_waitcnt vmcnt(36)\n"
"ds_read_b64_tr_b16 a[172:173], v36 offset:14336\n"
"ds_read_b64_tr_b16 a[174:175], v36 offset:14592\n"
"ds_read_b64_tr_b16 a[200:201], v37 offset:14336\n"
"ds_read_b64_tr_b16 a[202:203], v37 offset:14592\n"
"s_waitcnt lgkmcnt(4)\n"
"s_add_u32 m0, s55, 12288\n"
"s_add_u32 s58, s83, 393216\n"
"buffer_load_dwordx4 v38, s[44:47], s58 offen lds\n"
"s_add_u32 m0, s55, 13312\n"
"s_add_u32 s58, s83, 425984\n"
"buffer_load_dwordx4 v38, s[44:47], s58 offen lds\n"
"s_waitcnt vmcnt(36)\n"
"ds_read_b64_tr_b16 a[8:9], v36 offset:16384\n"
"ds_read_b64_tr_b16 a[10:11], v36 offset:16640\n"
"ds_read_b64_tr_b16 a[36:37], v37 offset:16384\n"
"ds_read_b64_tr_b16 a[38:39], v37 offset:16640\n"
"s_waitcnt lgkmcnt(4)\n"
"s_add_u32 m0, s55, 14336\n"
"s_add_u32 s58, s84, 393216\n"
"buffer_load_dwordx4 v38, s[44:47], s58 offen lds\n"
"s_add_u32 m0, s55, 15360\n"
"s_add_u32 s58, s84, 425984\n"
"buffer_load_dwordx4 v38, s[44:47], s58 offen lds\n"
"s_waitcnt vmcnt(36)\n"
"ds_read_b64_tr_b16 a[64:65], v36 offset:18432\n"
"ds_read_b64_tr_b16 a[66:67], v36 offset:18688\n"
"ds_read_b64_tr_b16 a[92:93], v37 offset:18432\n"
"ds_read_b64_tr_b16 a[94:95], v37 offset:18688\n"
"s_waitcnt lgkmcnt(4)\n"
"s_waitcnt vmcnt(34)\n"
"ds_read_b64_tr_b16 a[120:121], v36 offset:20480\n"
"ds_read_b64_tr_b16 a[122:123], v36 offset:20736\n"
"ds_read_b64_tr_b16 a[148:149], v37 offset:20480\n"
"ds_read_b64_tr_b16 a[150:151], v37 offset:20736\n"
"s_waitcnt lgkmcnt(4)\n"
"s_waitcnt vmcnt(32)\n"
"ds_read_b64_tr_b16 a[176:177], v36 offset:22528\n"
"ds_read_b64_tr_b16 a[178:179], v36 offset:22784\n"
"ds_read_b64_tr_b16 a[204:205], v37 offset:22528\n"
"ds_read_b64_tr_b16 a[206:207], v37 offset:22784\n"
"s_waitcnt lgkmcnt(4)\n"
"s_waitcnt vmcnt(30)\n"
"ds_read_b64_tr_b16 a[12:13], v36 offset:24576\n"
"ds_read_b64_tr_b16 a[14:15], v36 offset:24832\n"
"ds_read_b64_tr_b16 a[40:41], v37 offset:24576\n"
"ds_read_b64_tr_b16 a[42:43], v37 offset:24832\n"
"s_waitcnt lgkmcnt(4)\n"
"s_waitcnt vmcnt(28)\n"
"ds_read_b64_tr_b16 a[68:69], v36 offset:26624\n"
"ds_read_b64_tr_b16 a[70:71], v36 offset:26880\n"
"ds_read_b64_tr_b16 a[96:97], v37 offset:26624\n"
"ds_read_b64_tr_b16 a[98:99], v37 offset:26880\n"
"s_waitcnt lgkmcnt(4)\n"
"s_waitcnt vmcnt(26)\n"
"ds_read_b64_tr_b16 a[124:125], v36 offset:28672\n"
"ds_read_b64_tr_b16 a[126:127], v36 offset:28928\n"
"ds_read_b64_tr_b16 a[152:153], v37 offset:28672\n"
"ds_read_b64_tr_b16 a[154:155], v37 offset:28928\n"
"s_waitcnt lgkmcnt(4)\n"
"s_waitcnt vmcnt(24)\n"
"ds_read_b64_tr_b16 a[180:181], v36 offset:30720\n"
"ds_read_b64_tr_b16 a[182:183], v36 offset:30976\n"
"ds_read_b64_tr_b16 a[208:209], v37 offset:30720\n"
"ds_read_b64_tr_b16 a[210:211], v37 offset:30976\n"
"s_waitcnt lgkmcnt(4)\n"
"s_waitcnt vmcnt(22)\n"
"ds_read_b64_tr_b16 a[16:17], v36 offset:32768\n"
"ds_read_b64_tr_b16 a[18:19], v36 offset:33024\n"
"ds_read_b64_tr_b16 a[44:45], v37 offset:32768\n"
"ds_read_b64_tr_b16 a[46:47], v37 offset:33024\n"
"s_waitcnt lgkmcnt(4)\n"
"s_waitcnt vmcnt(20)\n"
"ds_read_b64_tr_b16 a[72:73], v36 offset:34816\n"
"ds_read_b64_tr_b16 a[74:75], v36 offset:35072\n"
"ds_read_b64_tr_b16 a[100:101], v37 offset:34816\n"
"ds_read_b64_tr_b16 a[102:103], v37 offset:35072\n"
"s_waitcnt lgkmcnt(4)\n"
"s_waitcnt vmcnt(18)\n"
"ds_read_b64_tr_b16 a[128:129], v36 offset:36864\n"
"ds_read_b64_tr_b16 a[130:131], v36 offset:37120\n"
"ds_read_b64_tr_b16 a[156:157], v37 offset:36864\n"
"ds_read_b64_tr_b16 a[158:159], v37 offset:37120\n"
"s_waitcnt lgkmcnt(4)\n"
"s_waitcnt vmcnt(16)\n"
"ds_read_b64_tr_b16 a[184:185], v36 offset:38912\n"
"ds_read_b64_tr_b16 a[186:187], v36 offset:39168\n"
"ds_read_b64_tr_b16 a[212:213], v37 offset:38912\n"
"ds_read_b64_tr_b16 a[214:215], v37 offset:39168\n"
"s_waitcnt lgkmcnt(4)\n"
"s_waitcnt vmcnt(14)\n"
"ds_read_b64_tr_b16 a[20:21], v36 offset:0\n"
"ds_read_b64_tr_b16 a[22:23], v36 offset:256\n"
"ds_read_b64_tr_b16 a[48:49], v37 offset:0\n"
"ds_read_b64_tr_b16 a[50:51], v37 offset:256\n"
"s_waitcnt lgkmcnt(4)\n"
"s_waitcnt vmcnt(12)\n"
"ds_read_b64_tr_b16 a[76:77], v36 offset:2048\n"
"ds_read_b64_tr_b16 a[78:79], v36 offset:2304\n"
"ds_read_b64_tr_b16 a[104:105], v37 offset:2048\n"
"ds_read_b64_tr_b16 a[106:107], v37 offset:2304\n"
"s_waitcnt lgkmcnt(4)\n"
"s_waitcnt vmcnt(10)\n"
"ds_read_b64_tr_b16 a[132:133], v36 offset:4096\n"
"ds_read_b64_tr_b16 a[134:135], v36 offset:4352\n"
"ds_read_b64_tr_b16 a[160:161], v37 offset:4096\n"
"ds_read_b64_tr_b16 a[162:163], v37 offset:4352\n"
"s_waitcnt lgkmcnt(4)\n"
"s_waitcnt vmcnt(8)\n"
"ds_read_b64_tr_b16 a[188:189], v36 offset:6144\n"
"ds_read_b64_tr_b16 a[190:191], v36 offset:6400\n"
"ds_read_b64_tr_b16 a[216:217], v37 offset:6144\n"
"ds_read_b64_tr_b16 a[218:219], v37 offset:6400\n"
"s_waitcnt lgkmcnt(4)\n"
"s_waitcnt vmcnt(6)\n"
"ds_read_b64_tr_b16 a[24:25], v36 offset:8192\n"
"ds_read_b64_tr_b16 a[26:27], v36 offset:8448\n"
"ds_read_b64_tr_b16 a[52:53], v37 offset:8192\n"
"ds_read_b64_tr_b16 a[54:55], v37 offset:8448\n"
"s_waitcnt lgkmcnt(4)\n"
"s_waitcnt vmcnt(4)\n"
"ds_read_b64_tr_b16 a[80:81], v36 offset:10240\n"
"ds_read_b64_tr_b16 a[82:83], v36 offset:10496\n"
"ds_read_b64_tr_b16 a[108:109], v37 offset:10240\n"
"ds_read_b64_tr_b16 a[110:111], v37 offset:10496\n"
"s_waitcnt lgkmcnt(4)\n"
"s_waitcnt vmcnt(2)\n"
"ds_read_b64_tr_b16 a[136:137], v36 offset:12288\n"
"ds_read_b64_tr_b16 a[138:139], v36 offset:12544\n"
"ds_read_b64_tr_b16 a[164:165], v37 offset:12288\n"
"ds_read_b64_tr_b16 a[166:167], v37 offset:12544\n"
"s_waitcnt lgkmcnt(4)\n"
"s_waitcnt vmcnt(0)\n"
"ds_read_b64_tr_b16 a[192:193], v36 offset:14336\n"
"ds_read_b64_tr_b16 a[194:195], v36 offset:14592\n"
"ds_read_b64_tr_b16 a[220:221], v37 offset:14336\n"
"ds_read_b64_tr_b16 a[222:223], v37 offset:14592\n"
"s_waitcnt lgkmcnt(4)\n"
"s_waitcnt vmcnt(0)\n"
"s_waitcnt lgkmcnt(0)\n"
"s_mov_b32 m0, s57\n"
"s_barrier\n"
"v_mov_b32 v168, 0\n"
"v_mov_b32 v169, 0\n"
"v_mov_b32 v138, 0\n"
"v_mov_b32 v139, 0\n"
"v_mov_b32 v180, 0\n"
"v_mov_b32 v181, 0\n"
"s_mov_b32 s38, 0x80000000\n"
"s_mov_b32 s95, 0x80000000\n"
"s_mov_b32 s37, 0x80000000\n"
"s_mov_b32 s94, 0x80000000\n"
"s_mov_b32 s94, 0x80000000\n"
"s_mov_b32 s95, 0x80000000\n"
"s_mov_b32 s39, 0x80000000\n"
"v_mov_b32 v136, 0\n"
"v_mov_b32 v137, 0\n"
"s_mov_b32 s94, 0x80000000\n"
"s_mov_b32 s95, 0x80000000\n"
"s_mov_b32 s37, 0x80000000\n"
"s_mov_b32 s35, 0x80000000\n"
"s_mov_b32 s36, 0x80000000\n"
"v_mov_b32 v144, 0\n"
"v_mov_b32 v145, 0\n"
"s_mov_b32 s95, 0x80000000\n"
"s_mov_b32 s38, 0x80000000\n"
"s_mov_b32 s95, 0x80000000\n"
"s_mov_b32 s34, 0x80000000\n"
"s_cmp_eq_u32 s54, 0\n"
"s_cbranch_scc1 .LJ_1s\n"
"s_cmp_eq_u32 s54, 1\n"
"s_cbranch_scc1 .LJ_2s\n"
"s_cmp_eq_u32 s54, 2\n"
"s_cbranch_scc1 .LJ_3s\n"
".p2align 6\n"
".LJ_top:\n"
"v_mfma_f32_16x16x32_bf16 v[36:39], a[252:255], a[0:3], 0\n"
"v_cvt_pk_bf16_f32 v178, v172, v173\n"
"v_cvt_pk_bf16_f32 v179, v174, v175\n"
"v_mfma_f32_16x16x32_bf16 v[36:39], v[84:87], a[4:7], v[36:39]\n"
"buffer_store_dwordx4 v[176:179], v15, s[72:75], s94 offen sc1\n"
"v_mfma_f32_16x16x32_bf16 v[40:43], v[176:179], v[176:179], 0\n"
"v_mfma_f32_16x16x32_bf16 v[44:47], v[28:31], v[176:179], 0\n"
"v_mfma_f32_16x16x32_bf16 v[36:39], v[88:91], a[8:11], v[36:39]\n"
"v_cndmask_b32_e64 v40, v40, v41, s[60:61]\n"
"v_cndmask_b32_e64 v42, v42, v43, s[60:61]\n"
"v_mfma_f32_16x16x32_bf16 v[36:39], v[92:95], a[12:15], v[36:39]\n"
"v_cndmask_b32_e64 v45, v40, v42, s[62:63]\n"
"v_mfma_f32_16x16x32_bf16 v[36:39], v[96:99], a[16:19], v[36:39]\n"
"s_waitcnt lgkmcnt(3)\n"
"buffer_load_dwordx2 v[168:169], v7, s[40:43], s50 offen sc1\n"
"v_add_f32 v144, v144, v148\n"
"v_mfma_f32_16x16x32_bf16 v[36:39], v[100:103], a[20:23], v[36:39]\n"
"buffer_store_dwordx2 v[44:45], v17, s[40:43], s35 offen offset:0 sc1\n"
"v_add_f32 v145, v145, v149\n"
"v_add_f32 v146, v146, v150\n"
"v_mfma_f32_16x16x32_bf16 v[36:39], v[104:107], a[24:27], v[36:39]\n"
"v_add_f32 v147, v147, v151\n"
"v_add_f32 v144, v144, v152\n"
"v_mfma_f32_16x16x32_bf16 v[40:43], a[252:255], a[28:31], 0\n"
"v_add_f32 v145, v145, v153\n"
"v_add_f32 v146, v146, v154\n"
"v_mfma_f32_16x16x32_bf16 v[40:43], v[84:87], a[32:35], v[40:43]\n"
"ds_write_addtid_b32 v36 offset:0\n"
"v_add_f32 v147, v147, v155\n"
"v_mfma_f32_16x16x32_bf16 v[40:43], v[88:91], a[36:39], v[40:43]\n"
"ds_write_addtid_b32 v37 offset:4640\n"
"v_add_f32 v144, v144, v140\n"
"v_mfma_f32_16x16x32_bf16 v[40:43], v[92:95], a[40:43], v[40:43]\n"
"ds_write_addtid_b32 v38 offset:9344\n"
"v_add_f32 v145, v145, v141\n"
"v_mfma_f32_16x16x32_bf16 v[40:43], v[96:99], a[44:47], v[40:43]\n"
"ds_write_addtid_b32 v39 offset:13984\n"
"v_add_f32 v146, v146, v142\n"
"v_mfma_f32_16x16x32_bf16 v[40:43], v[100:103], a[48:51], v[40:43]\n"
"s_waitcnt vmcnt(18)\n"
"v_cmp_ne_u64 vcc, s[48:49], v[180:181]\n"
"v_add_f32 v147, v147, v143\n"
"v_mfma_f32_16x16x32_bf16 v[40:43], v[104:107], a[52:55], v[40:43]\n"
"s_add_u32 s94, s50, 0xffd00000\n"
"s_and_b32 s95, s94, 0xfbffffff\n"
"v_cvt_pk_bf16_f32 v36, v144, v145\n"
"v_mfma_f32_16x16x32_bf16 v[44:47], a[252:255], a[56:59], 0\n"
"s_cbranch_vccnz .LJ_3s\n"
".LJ_3r:\n"
"v_cvt_pk_bf16_f32 v37, v146, v147\n"
"v_mfma_f32_16x16x32_bf16 v[44:47], v[84:87], a[60:63], v[44:47]\n"
"s_add_u32 s50, s50, 0x180000\n"
"s_and_b32 s56, s50, 0xfbffffff\n"
"ds_write_addtid_b32 v40 offset:256\n"
"v_mfma_f32_16x16x32_bf16 v[44:47], v[88:91], a[64:67], v[44:47]\n"
"s_waitcnt lgkmcnt(5)\n"
"ds_write_addtid_b32 v41 offset:4896\n"
"buffer_load_dwordx4 a[224:227], v5, s[40:43], s56 offen offset:0 sc1\n"
"v_mfma_f32_16x16x32_bf16 v[44:47], v[92:95], a[68:71], v[44:47]\n"
"ds_write_addtid_b32 v42 offset:9600\n"
"v_add_f32 v156, v156, v160\n"
"v_mfma_f32_16x16x32_bf16 v[44:47], v[96:99], a[72:75], v[44:47]\n"
"ds_write_addtid_b32 v43 offset:14240\n"
"v_add_f32 v157, v157, v161\n"
"v_mfma_f32_16x16x32_bf16 v[44:47], v[100:103], a[76:79], v[44:47]\n"
"v_add_f32 v158, v158, v162\n"
"v_add_f32 v159, v159, v163\n"
"v_mfma_f32_16x16x32_bf16 v[44:47], v[104:107], a[80:83], v[44:47]\n"
"buffer_load_dwordx4 a[228:231], v5, s[40:43], s56 offen offset:1024 sc1\n"
"v_add_f32 v156, v156, v164\n"
"v_mfma_f32_16x16x32_bf16 v[40:43], a[252:255], a[84:87], 0\n"
"s_add_u32 s39, s50, 0xff800000\n"
"v_add_f32 v157, v157, v165\n"
"v_add_f32 v158, v158, v166\n"
"v_mfma_f32_16x16x32_bf16 v[40:43], v[84:87], a[88:91], v[40:43]\n"
"ds_write_addtid_b32 v44 offset:512\n"
"v_add_f32 v159, v159, v167\n"
"v_mfma_f32_16x16x32_bf16 v[40:43], v[88:91], a[92:95], v[40:43]\n"
"ds_write_addtid_b32 v45 offset:5152\n"
"v_add_f32 v156, v156, v136\n"
"v_mfma_f32_16x16x32_bf16 v[40:43], v[92:95], a[96:99], v[40:43]\n"
"ds_write_addtid_b32 v46 offset:9856\n"
"buffer_load_dwordx4 a[232:235], v5, s[40:43], s56 offen offset:2048 sc1\n"
"v_mfma_f32_16x16x32_bf16 v[40:43], v[96:99], a[100:103], v[40:43]\n"
"ds_write_addtid_b32 v47 offset:14496\n"
"v_add_f32 v157, v157, v137\n"
"v_mfma_f32_16x16x32_bf16 v[40:43], v[100:103], a[104:107], v[40:43]\n"
"v_add_f32 v158, v158, v138\n"
"v_add_f32 v159, v159, v139\n"
"v_mfma_f32_16x16x32_bf16 v[40:43], v[104:107], a[108:111], v[40:43]\n"
"v_cvt_pk_bf16_f32 v38, v156, v157\n"
"v_cvt_pk_bf16_f32 v39, v158, v159\n"
"v_mfma_f32_16x16x32_bf16 v[44:47], a[252:255], a[112:115], 0\n"
"buffer_store_dwordx4 v[36:39], v8, s[40:43], s95 offen sc1\n"
"buffer_load_dwordx4 a[236:239], v5, s[40:43], s56 offen offset:3072 sc1\n"
"v_mfma_f32_16x16x32_bf16 v[44:47], v[84:87], a[116:119], v[44:47]\n"
"s_cmp_lt_u32 s39, 0x8000000\n"
"s_cselect_b32 s39, 0x80000000, s39\n"
"ds_write_addtid_b32 v40 offset:768\n"
"v_mfma_f32_16x16x32_bf16 v[44:47], v[88:91], a[120:123], v[44:47]\n"
"ds_write_addtid_b32 v41 offset:5408\n"
"buffer_load_dwordx2 v[136:137], v10, s[40:43], s39 offen offset:0 sc1\n"
"v_mfma_f32_16x16x32_bf16 v[44:47], v[92:95], a[124:127], v[44:47]\n"
"ds_write_addtid_b32 v42 offset:10112\n"
"v_mfma_f32_16x16x32_bf16 v[44:47], v[96:99], a[128:131], v[44:47]\n"
"ds_write_addtid_b32 v43 offset:14752\n"
"buffer_load_dwordx4 a[240:243], v6, s[40:43], s56 offen offset:0 sc1\n"
"v_mfma_f32_16x16x32_bf16 v[44:47], v[100:103], a[132:135], v[44:47]\n"
"v_mfma_f32_16x16x32_bf16 v[44:47], v[104:107], a[136:139], v[44:47]\n"
"v_mfma_f32_16x16x32_bf16 v[36:39], a[252:255], a[140:143], 0\n"
"v_mfma_f32_16x16x32_bf16 v[36:39], v[84:87], a[144:147], v[36:39]\n"
"s_waitcnt vmcnt(20)\n"
"ds_write_addtid_b32 v44 offset:1024\n"
"buffer_load_dwordx4 a[244:247], v6, s[40:43], s56 offen offset:1024 sc1\n"
"v_mfma_f32_16x16x32_bf16 v[36:39], v[88:91], a[148:151], v[36:39]\n"
"buffer_store_dwordx2 v[0:1], v9, s[40:43], s37 offen offset:0 sc1\n"
"ds_write_addtid_b32 v45 offset:5664\n"
"v_mfma_f32_16x16x32_bf16 v[36:39], v[92:95], a[152:155], v[36:39]\n"
"ds_write_addtid_b32 v46 offset:10368\n"
"v_mfma_f32_16x16x32_bf16 v[36:39], v[96:99], a[156:159], v[36:39]\n"
"ds_write_addtid_b32 v47 offset:15008\n"
"v_mfma_f32_16x16x32_bf16 v[36:39], v[100:103], a[160:163], v[36:39]\n"
"buffer_load_dwordx4 a[248:251], v6, s[40:43], s56 offen offset:2048 sc1\n"
"v_mfma_f32_16x16x32_bf16 v[36:39], v[104:107], a[164:167], v[36:39]\n"
"v_mfma_f32_16x16x32_bf16 v[40:43], a[168:171], a[252:255], 0\n"
"v_mfma_f32_16x16x32_bf16 v[40:43], a[172:175], v[84:87], v[40:43]\n"
"ds_write_addtid_b32 v36 offset:1280\n"
"v_mfma_f32_16x16x32_bf16 v[40:43], a[176:179], v[88:91], v[40:43]\n"
"ds_write_addtid_b32 v37 offset:5920\n"
"v_mfma_f32_16x16x32_bf16 v[40:43], a[180:183], v[92:95], v[40:43]\n"
"ds_write_addtid_b32 v38 offset:10624\n"
"v_mfma_f32_16x16x32_bf16 v[40:43], a[184:187], v[96:99], v[40:43]\n"
"s_waitcnt vmcnt(13)\n"
"ds_write_addtid_b32 v39 offset:15264\n"
"v_mfma_f32_16x16x32_bf16 v[44:47], v[32:35], v[60:63], 0\n"
"s_cmp_ge_i32 s34, s76\n"
"s_cbranch_scc1 .LJ_w1s\n"
".LJ_w1r:\n"
"v_mfma_f32_16x16x32_bf16 v[44:47], v[32:35], v[68:71], v[44:47]\n"
"s_lshr_b32 s95, s34, s77\n"
"v_mfma_f32_16x16x32_bf16 v[44:47], v[32:35], v[76:79], v[44:47]\n"
"s_bitcmp1_b32 s95, 15\n"
"s_cselect_b32 s38, s79, s78\n"
"v_mfma_f32_16x16x32_bf16 v[36:39], v[32:35], v[64:67], 0\n"
"s_add_u32 s95, s95, s38\n"
"v_mfma_f32_16x16x32_bf16 v[36:39], v[32:35], v[72:75], v[36:39]\n"
"s_add_u32 s95, s95, s80\n"
"v_mfma_f32_16x16x32_bf16 v[36:39], v[32:35], v[80:83], v[36:39]\n"
"v_cvt_pk_bf16_f32 v48, v44, v45\n"
"v_cvt_pk_bf16_f32 v49, v46, v47\n"
"v_mfma_f32_16x16x32_bf16 v[40:43], a[188:191], v[100:103], v[40:43]\n"
"v_mfma_f32_16x16x32_bf16 v[40:43], a[192:195], v[104:107], v[40:43]\n"
"v_cvt_pk_bf16_f32 v50, v36, v37\n"
"v_cvt_pk_bf16_f32 v51, v38, v39\n"
"v_mfma_f32_16x16x32_bf16 v[44:47], a[196:199], a[252:255], 0\n"
"buffer_store_dwordx4 v[48:51], v15, s[72:75], s95 offen sc1\n"
"v_mfma_f32_16x16x32_bf16 v[52:55], v[48:51], v[48:51], 0\n"
"v_mfma_f32_16x16x32_bf16 v[36:39], v[28:31], v[48:51], 0\n"
"v_mfma_f32_16x16x32_bf16 v[44:47], a[200:203], v[84:87], v[44:47]\n"
"s_waitcnt lgkmcnt(0)\n"
"v_cndmask_b32_e64 v52, v52, v53, s[60:61]\n"
"v_cndmask_b32_e64 v54, v54, v55, s[60:61]\n"
"v_mfma_f32_16x16x32_bf16 v[44:47], a[204:207], v[88:91], v[44:47]\n"
"s_barrier\n"
"v_mfma_f32_16x16x32_bf16 v[44:47], a[208:211], v[92:95], v[44:47]\n"
"ds_read_b128 v[48:51], v2 offset:0\n"
"ds_read_b128 v[56:59], v3 offset:512\n"
"v_mfma_f32_16x16x32_bf16 v[44:47], a[212:215], v[96:99], v[44:47]\n"
"ds_read_b128 v[60:63], v4 offset:1024\n"
"ds_read_b128 v[64:67], v2 offset:256\n"
"v_mfma_f32_16x16x32_bf16 v[44:47], a[216:219], v[100:103], v[44:47]\n"
"ds_read_b128 v[68:71], v3 offset:768\n"
"ds_read_b128 v[72:75], v4 offset:1280\n"
"v_mfma_f32_16x16x32_bf16 v[44:47], a[220:223], v[104:107], v[44:47]\n"
"v_cndmask_b32_e64 v37, v52, v54, s[62:63]\n"
"v_mfma_f32_16x16x32_bf16 v[76:79], v[108:111], a[0:3], 0\n"
"buffer_store_dwordx2 v[36:37], v17, s[40:43], s34 offen offset:2048 sc1\n"
"v_mfma_f32_16x16x32_bf16 v[76:79], v[112:115], a[4:7], v[76:79]\n"
"v_mfma_f32_16x16x32_bf16 v[76:79], v[116:119], a[8:11], v[76:79]\n"
"v_mfma_f32_16x16x32_bf16 v[76:79], v[120:123], a[12:15], v[76:79]\n"
"v_mfma_f32_16x16x32_bf16 v[76:79], v[124:127], a[16:19], v[76:79]\n"
"s_waitcnt lgkmcnt(3)\n"
"buffer_load_dwordx2 v[138:139], v7, s[40:43], s50 offen sc1\n"
"v_add_f32 v48, v48, v56\n"
"v_mfma_f32_16x16x32_bf16 v[76:79], v[128:131], a[20:23], v[76:79]\n"
"v_add_f32 v49, v49, v57\n"
"v_add_f32 v50, v50, v58\n"
"v_mfma_f32_16x16x32_bf16 v[76:79], v[132:135], a[24:27], v[76:79]\n"
"v_add_f32 v51, v51, v59\n"
"v_add_f32 v48, v48, v60\n"
"v_mfma_f32_16x16x32_bf16 v[36:39], v[108:111], a[28:31], 0\n"
"v_add_f32 v49, v49, v61\n"
"v_add_f32 v50, v50, v62\n"
"v_mfma_f32_16x16x32_bf16 v[36:39], v[112:115], a[32:35], v[36:39]\n"
"ds_write_addtid_b32 v76 offset:1536\n"
"v_add_f32 v51, v51, v63\n"
"v_mfma_f32_16x16x32_bf16 v[36:39], v[116:119], a[36:39], v[36:39]\n"
"ds_write_addtid_b32 v77 offset:6176\n"
"v_add_f32 v48, v48, v40\n"
"v_mfma_f32_16x16x32_bf16 v[36:39], v[120:123], a[40:43], v[36:39]\n"
"ds_write_addtid_b32 v78 offset:10880\n"
"v_add_f32 v49, v49, v41\n"
"v_mfma_f32_16x16x32_bf16 v[36:39], v[124:127], a[44:47], v[36:39]\n"
"ds_write_addtid_b32 v79 offset:15520\n"
"v_add_f32 v50, v50, v42\n"
"v_mfma_f32_16x16x32_bf16 v[36:39], v[128:131], a[48:51], v[36:39]\n"
"s_waitcnt vmcnt(14)\n"
"v_cmp_ne_u64 vcc, s[48:49], v[168:169]\n"
"v_add_f32 v51, v51, v43\n"
"v_mfma_f32_16x16x32_bf16 v[36:39], v[132:135], a[52:55], v[36:39]\n"
"s_add_u32 s38, s50, 0xffd00000\n"
"s_and_b32 s95, s38, 0xfbffffff\n"
"v_cvt_pk_bf16_f32 v52, v48, v49\n"
"v_mfma_f32_16x16x32_bf16 v[40:43], v[108:111], a[56:59], 0\n"
"s_cbranch_vccnz .LJ_1s\n"
".LJ_1r:\n"
"v_cvt_pk_bf16_f32 v53, v50, v51\n"
"v_mfma_f32_16x16x32_bf16 v[40:43], v[112:115], a[60:63], v[40:43]\n"
"s_add_u32 s50, s50, 0x180000\n"
"s_and_b32 s56, s50, 0xfbffffff\n"
"ds_write_addtid_b32 v36 offset:1792\n"
"v_mfma_f32_16x16x32_bf16 v[40:43], v[116:119], a[64:67], v[40:43]\n"
"s_waitcnt lgkmcnt(5)\n"
"ds_write_addtid_b32 v37 offset:6432\n"
"buffer_load_dwordx4 a[252:255], v5, s[40:43], s56 offen offset:0 sc1\n"
"v_mfma_f32_16x16x32_bf16 v[40:43], v[120:123], a[68:71], v[40:43]\n"
"s_waitcnt vmcnt(14)\n"
"buffer_store_dwordx2 v[0:1], v18, s[40:43], s35 offen offset:0 sc1\n"
"ds_write_addtid_b32 v38 offset:11136\n"
"v_add_f32 v64, v64, v68\n"
"v_mfma_f32_16x16x32_bf16 v[40:43], v[124:127], a[72:75], v[40:43]\n"
"ds_write_addtid_b32 v39 offset:15776\n"
"v_add_f32 v65, v65, v69\n"
"v_mfma_f32_16x16x32_bf16 v[40:43], v[128:131], a[76:79], v[40:43]\n"
"v_add_f32 v66, v66, v70\n"
"v_add_f32 v67, v67, v71\n"
"v_mfma_f32_16x16x32_bf16 v[40:43], v[132:135], a[80:83], v[40:43]\n"
"buffer_load_dwordx4 v[84:87], v5, s[40:43], s56 offen offset:1024 sc1\n"
"v_add_f32 v64, v64, v72\n"
"v_mfma_f32_16x16x32_bf16 v[36:39], v[108:111], a[84:87], 0\n"
"s_and_b32 s37, s39, 0xfbffffff\n"
"v_add_f32 v65, v65, v73\n"
"v_add_f32 v66, v66, v74\n"
"v_mfma_f32_16x16x32_bf16 v[36:39], v[112:115], a[88:91], v[36:39]\n"
"s_add_u32 s36, s50, 0xff800000\n"
"ds_write_addtid_b32 v40 offset:2048\n"
"v_add_f32 v67, v67, v75\n"
"v_mfma_f32_16x16x32_bf16 v[36:39], v[116:119], a[92:95], v[36:39]\n"
"ds_write_addtid_b32 v41 offset:6688\n"
"v_add_f32 v64, v64, v44\n"
"v_mfma_f32_16x16x32_bf16 v[36:39], v[120:123], a[96:99], v[36:39]\n"
"ds_write_addtid_b32 v42 offset:11392\n"
"buffer_load_dwordx4 v[88:91], v5, s[40:43], s56 offen offset:2048 sc1\n"
"v_mfma_f32_16x16x32_bf16 v[36:39], v[124:127], a[100:103], v[36:39]\n"
"ds_write_addtid_b32 v43 offset:16032\n"
"v_add_f32 v65, v65, v45\n"
"v_mfma_f32_16x16x32_bf16 v[36:39], v[128:131], a[104:107], v[36:39]\n"
"v_add_f32 v66, v66, v46\n"
"v_add_f32 v67, v67, v47\n"
"v_mfma_f32_16x16x32_bf16 v[36:39], v[132:135], a[108:111], v[36:39]\n"
"v_cvt_pk_bf16_f32 v54, v64, v65\n"
"v_cvt_pk_bf16_f32 v55, v66, v67\n"
"v_mfma_f32_16x16x32_bf16 v[40:43], v[108:111], a[112:115], 0\n"
"buffer_store_dwordx4 v[52:55], v8, s[40:43], s95 offen sc1\n"
"buffer_load_dwordx4 v[92:95], v5, s[40:43], s56 offen offset:3072 sc1\n"
"v_mfma_f32_16x16x32_bf16 v[40:43], v[112:115], a[116:119], v[40:43]\n"
"s_cmp_lt_u32 s36, 0x8000000\n"
"s_cselect_b32 s36, 0x80000000, s36\n"
"ds_write_addtid_b32 v36 offset:2304\n"
"v_mfma_f32_16x16x32_bf16 v[40:43], v[116:119], a[120:123], v[40:43]\n"
"ds_write_addtid_b32 v37 offset:6944\n"
"buffer_load_dwordx2 v[144:145], v10, s[40:43], s36 offen offset:512 sc1\n"
"v_mfma_f32_16x16x32_bf16 v[40:43], v[120:123], a[124:127], v[40:43]\n"
"ds_write_addtid_b32 v38 offset:11648\n"
"v_mfma_f32_16x16x32_bf16 v[40:43], v[124:127], a[128:131], v[40:43]\n"
"ds_write_addtid_b32 v39 offset:16288\n"
"buffer_load_dwordx4 v[96:99], v6, s[40:43], s56 offen offset:0 sc1\n"
"v_mfma_f32_16x16x32_bf16 v[40:43], v[128:131], a[132:135], v[40:43]\n"
"v_mfma_f32_16x16x32_bf16 v[40:43], v[132:135], a[136:139], v[40:43]\n"
"v_mfma_f32_16x16x32_bf16 v[60:63], v[108:111], a[140:143], 0\n"
"v_mfma_f32_16x16x32_bf16 v[60:63], v[112:115], a[144:147], v[60:63]\n"
"s_waitcnt vmcnt(17)\n"
"ds_write_addtid_b32 v40 offset:2560\n"
"buffer_load_dwordx4 v[100:103], v6, s[40:43], s56 offen offset:1024 sc1\n"
"v_mfma_f32_16x16x32_bf16 v[60:63], v[116:119], a[148:151], v[60:63]\n"
"buffer_store_dwordx2 v[0:1], v9, s[40:43], s94 offen offset:0 sc1\n"
"ds_write_addtid_b32 v41 offset:7200\n"
"v_mfma_f32_16x16x32_bf16 v[60:63], v[120:123], a[152:155], v[60:63]\n"
"s_waitcnt vmcnt(17)\n"
"ds_write_addtid_b32 v42 offset:11904\n"
"v_mfma_f32_16x16x32_bf16 v[60:63], v[124:127], a[156:159], v[60:63]\n"
"ds_write_addtid_b32 v43 offset:16544\n"
"v_cmp_ne_u64 vcc, s[48:49], v[136:137]\n"
"v_mfma_f32_16x16x32_bf16 v[60:63], v[128:131], a[160:163], v[60:63]\n"
"buffer_load_dwordx4 v[104:107], v6, s[40:43], s56 offen offset:2048 sc1\n"
"v_mfma_f32_16x16x32_bf16 v[60:63], v[132:135], a[164:167], v[60:63]\n"
"s_cbranch_vccnz .LJ_4s\n"
".LJ_4r:\n"
"v_mfma_f32_16x16x32_bf16 v[64:67], a[168:171], v[108:111], 0\n"
"buffer_load_dwordx4 v[38:41], v11, s[40:43], s37 offen offset:0 sc1\n"
"v_mfma_f32_16x16x32_bf16 v[64:67], a[172:175], v[112:115], v[64:67]\n"
"ds_write_addtid_b32 v60 offset:2816\n"
"buffer_load_dwordx4 v[42:45], v11, s[40:43], s37 offen offset:1024 sc1\n"
"v_mfma_f32_16x16x32_bf16 v[64:67], a[176:179], v[116:119], v[64:67]\n"
"ds_write_addtid_b32 v61 offset:7456\n"
"buffer_load_dwordx4 v[46:49], v11, s[40:43], s37 offen offset:2048 sc1\n"
"v_mfma_f32_16x16x32_bf16 v[64:67], a[180:183], v[120:123], v[64:67]\n"
"ds_write_addtid_b32 v62 offset:12160\n"
"buffer_load_dwordx4 v[50:53], v11, s[40:43], s37 offen offset:3072 sc1\n"
"v_mfma_f32_16x16x32_bf16 v[64:67], a[184:187], v[124:127], v[64:67]\n"
"ds_write_addtid_b32 v63 offset:16800\n"
"buffer_load_dwordx4 v[54:57], v12, s[40:43], s37 offen sc1\n"
"v_mfma_f32_16x16x32_bf16 v[64:67], a[188:191], v[128:131], v[64:67]\n"
"s_mov_b32 s35, s39\n"
"v_mov_b32 v36, v20\n"
"v_mov_b32 v37, v21\n"
"v_mfma_f32_16x16x32_bf16 v[64:67], a[192:195], v[132:135], v[64:67]\n"
"v_mov_b32 v58, v22\n"
"v_mov_b32 v59, v23\n"
"v_mfma_f32_16x16x32_bf16 v[60:63], a[196:199], v[108:111], 0\n"
"v_mfma_f32_16x16x32_bf16 v[60:63], a[200:203], v[112:115], v[60:63]\n"
"s_waitcnt lgkmcnt(0)\n"
"v_mfma_f32_16x16x32_bf16 v[60:63], a[204:207], v[116:119], v[60:63]\n"
"s_barrier\n"
"v_mfma_f32_16x16x32_bf16 v[60:63], a[208:211], v[120:123], v[60:63]\n"
"ds_read_b128 v[68:71], v2 offset:1536\n"
"ds_read_b128 v[72:75], v3 offset:2048\n"
"v_mfma_f32_16x16x32_bf16 v[60:63], a[212:215], v[124:127], v[60:63]\n"
"ds_read_b128 v[76:79], v4 offset:2560\n"
"ds_read_b128 v[80:83], v2 offset:1792\n"
"v_mfma_f32_16x16x32_bf16 v[60:63], a[216:219], v[128:131], v[60:63]\n"
"ds_read_b128 v[116:119], v3 offset:2304\n"
"ds_read_b128 v[120:123], v4 offset:2816\n"
"v_mfma_f32_16x16x32_bf16 v[60:63], a[220:223], v[132:135], v[60:63]\n"
"v_mfma_f32_16x16x32_bf16 v[108:111], a[224:227], a[0:3], 0\n"
"v_mfma_f32_16x16x32_bf16 v[108:111], a[228:231], a[4:7], v[108:111]\n"
"v_mfma_f32_16x16x32_bf16 v[108:111], a[232:235], a[8:11], v[108:111]\n"
"v_mfma_f32_16x16x32_bf16 v[108:111], a[236:239], a[12:15], v[108:111]\n"
"s_waitcnt vmcnt(21)\n"
"v_mfma_f32_16x16x32_bf16 v[108:111], a[240:243], a[16:19], v[108:111]\n"
"s_waitcnt lgkmcnt(3)\n"
"buffer_load_dwordx2 v[180:181], v7, s[40:43], s50 offen sc1\n"
"v_add_f32 v68, v68, v72\n"
"v_mfma_f32_16x16x32_bf16 v[108:111], a[244:247], a[20:23], v[108:111]\n"
"s_waitcnt vmcnt(20)\n"
"v_add_f32 v69, v69, v73\n"
"v_add_f32 v70, v70, v74\n"
"v_mfma_f32_16x16x32_bf16 v[108:111], a[248:251], a[24:27], v[108:111]\n"
"v_add_f32 v71, v71, v75\n"
"v_add_f32 v68, v68, v76\n"
"v_mfma_f32_16x16x32_bf16 v[112:115], a[224:227], a[28:31], 0\n"
"v_add_f32 v69, v69, v77\n"
"v_add_f32 v70, v70, v78\n"
"v_mfma_f32_16x16x32_bf16 v[112:115], a[228:231], a[32:35], v[112:115]\n"
"s_waitcnt vmcnt(18)\n"
"buffer_store_dwordx2 v[0:1], v18, s[40:43], s34 offen offset:128 sc1\n"
"ds_write_addtid_b32 v108 offset:3072\n"
"v_add_f32 v71, v71, v79\n"
"v_mfma_f32_16x16x32_bf16 v[112:115], a[232:235], a[36:39], v[112:115]\n"
"ds_write_addtid_b32 v109 offset:7712\n"
"v_add_f32 v68, v68, v64\n"
"v_mfma_f32_16x16x32_bf16 v[112:115], a[236:239], a[40:43], v[112:115]\n"
"ds_write_addtid_b32 v110 offset:12416\n"
"v_add_f32 v69, v69, v65\n"
"v_mfma_f32_16x16x32_bf16 v[112:115], a[240:243], a[44:47], v[112:115]\n"
"ds_write_addtid_b32 v111 offset:17056\n"
"v_add_f32 v70, v70, v66\n"
"v_mfma_f32_16x16x32_bf16 v[112:115], a[244:247], a[48:51], v[112:115]\n"
"s_waitcnt vmcnt(18)\n"
"v_cmp_ne_u64 vcc, s[48:49], v[138:139]\n"
"v_add_f32 v71, v71, v67\n"
"v_mfma_f32_16x16x32_bf16 v[112:115], a[248:251], a[52:55], v[112:115]\n"
"s_add_u32 s37, s50, 0xffd00000\n"
"s_and_b32 s94, s37, 0xfbffffff\n"
"v_cvt_pk_bf16_f32 v72, v68, v69\n"
"v_mfma_f32_16x16x32_bf16 v[64:67], a[224:227], a[56:59], 0\n"
"s_cbranch_vccnz .LJ_2s\n"
".LJ_2r:\n"
"v_cvt_pk_bf16_f32 v73, v70, v71\n"
"v_mfma_f32_16x16x32_bf16 v[64:67], a[228:231], a[60:63], v[64:67]\n"
"s_add_u32 s50, s50, 0x180000\n"
"s_and_b32 s56, s50, 0xfbffffff\n"
"ds_write_addtid_b32 v112 offset:3328\n"
"v_mfma_f32_16x16x32_bf16 v[64:67], a[232:235], a[64:67], v[64:67]\n"
"s_waitcnt lgkmcnt(5)\n"
"ds_write_addtid_b32 v113 offset:7968\n"
"buffer_load_dwordx4 v[108:111], v5, s[40:43], s56 offen offset:0 sc1\n"
"v_mfma_f32_16x16x32_bf16 v[64:67], a[236:239], a[68:71], v[64:67]\n"
"ds_write_addtid_b32 v114 offset:12672\n"
"v_add_f32 v80, v80, v116\n"
"v_mfma_f32_16x16x32_bf16 v[64:67], a[240:243], a[72:75], v[64:67]\n"
"ds_write_addtid_b32 v115 offset:17312\n"
"v_add_f32 v81, v81, v117\n"
"v_mfma_f32_16x16x32_bf16 v[64:67], a[244:247], a[76:79], v[64:67]\n"
"v_add_f32 v82, v82, v118\n"
"v_add_f32 v83, v83, v119\n"
"v_mfma_f32_16x16x32_bf16 v[64:67], a[248:251], a[80:83], v[64:67]\n"
"buffer_load_dwordx4 v[112:115], v5, s[40:43], s56 offen offset:1024 sc1\n"
"v_add_f32 v80, v80, v120\n"
"v_mfma_f32_16x16x32_bf16 v[68:71], a[224:227], a[84:87], 0\n"
"s_and_b32 s95, s36, 0xfbffffff\n"
"v_add_f32 v81, v81, v121\n"
"v_add_f32 v82, v82, v122\n"
"v_mfma_f32_16x16x32_bf16 v[68:71], a[228:231], a[88:91], v[68:71]\n"
"ds_write_addtid_b32 v64 offset:3584\n"
"v_add_f32 v83, v83, v123\n"
"v_mfma_f32_16x16x32_bf16 v[68:71], a[232:235], a[92:95], v[68:71]\n"
"ds_write_addtid_b32 v65 offset:8224\n"
"v_add_f32 v80, v80, v60\n"
"v_mfma_f32_16x16x32_bf16 v[68:71], a[236:239], a[96:99], v[68:71]\n"
"ds_write_addtid_b32 v66 offset:12928\n"
"buffer_load_dwordx4 v[116:119], v5, s[40:43], s56 offen offset:2048 sc1\n"
"v_mfma_f32_16x16x32_bf16 v[68:71], a[240:243], a[100:103], v[68:71]\n"
"ds_write_addtid_b32 v67 offset:17568\n"
"v_add_f32 v81, v81, v61\n"
"v_mfma_f32_16x16x32_bf16 v[68:71], a[244:247], a[104:107], v[68:71]\n"
"v_add_f32 v82, v82, v62\n"
"v_add_f32 v83, v83, v63\n"
"v_mfma_f32_16x16x32_bf16 v[68:71], a[248:251], a[108:111], v[68:71]\n"
"v_cvt_pk_bf16_f32 v74, v80, v81\n"
"v_cvt_pk_bf16_f32 v75, v82, v83\n"
"v_mfma_f32_16x16x32_bf16 v[60:63], a[224:227], a[112:115], 0\n"
"buffer_store_dwordx4 v[72:75], v8, s[40:43], s94 offen sc1\n"
"buffer_load_dwordx4 v[120:123], v5, s[40:43], s56 offen offset:3072 sc1\n"
"v_mfma_f32_16x16x32_bf16 v[60:63], a[228:231], a[116:119], v[60:63]\n"
"ds_write_addtid_b32 v68 offset:3840\n"
"v_mfma_f32_16x16x32_bf16 v[60:63], a[232:235], a[120:123], v[60:63]\n"
"ds_write_addtid_b32 v69 offset:8480\n"
"v_mfma_f32_16x16x32_bf16 v[60:63], a[236:239], a[124:127], v[60:63]\n"
"ds_write_addtid_b32 v70 offset:13184\n"
"v_mfma_f32_16x16x32_bf16 v[60:63], a[240:243], a[128:131], v[60:63]\n"
"ds_write_addtid_b32 v71 offset:17824\n"
"buffer_load_dwordx4 v[124:127], v6, s[40:43], s56 offen offset:0 sc1\n"
"v_mfma_f32_16x16x32_bf16 v[60:63], a[244:247], a[132:135], v[60:63]\n"
"v_mfma_f32_16x16x32_bf16 v[60:63], a[248:251], a[136:139], v[60:63]\n"
"v_mfma_f32_16x16x32_bf16 v[136:139], a[224:227], a[140:143], 0\n"
"v_mfma_f32_16x16x32_bf16 v[136:139], a[228:231], a[144:147], v[136:139]\n"
"s_waitcnt vmcnt(19)\n"
"ds_write_addtid_b32 v60 offset:4096\n"
"buffer_load_dwordx4 v[128:131], v6, s[40:43], s56 offen offset:1024 sc1\n"
"v_mfma_f32_16x16x32_bf16 v[136:139], a[232:235], a[148:151], v[136:139]\n"
"buffer_store_dwordx2 v[0:1], v9, s[40:43], s38 offen offset:0 sc1\n"
"ds_write_addtid_b32 v61 offset:8736\n"
"v_mfma_f32_16x16x32_bf16 v[136:139], a[236:239], a[152:155], v[136:139]\n"
"s_waitcnt vmcnt(19)\n"
"ds_write_addtid_b32 v62 offset:13440\n"
"v_mfma_f32_16x16x32_bf16 v[136:139], a[240:243], a[156:159], v[136:139]\n"
"ds_write_addtid_b32 v63 offset:18080\n"
"v_cmp_ne_u64 vcc, s[48:49], v[144:145]\n"
"v_mfma_f32_16x16x32_bf16 v[136:139], a[244:247], a[160:163], v[136:139]\n"
"buffer_load_dwordx4 v[132:135], v6, s[40:43], s56 offen offset:2048 sc1\n"
"v_mfma_f32_16x16x32_bf16 v[136:139], a[248:251], a[164:167], v[136:139]\n"
"s_cbranch_vccnz .LJ_5s\n"
".LJ_5r:\n"
"v_mfma_f32_16x16x32_bf16 v[140:143], a[168:171], a[224:227], 0\n"
"buffer_load_dwordx4 v[62:65], v13, s[40:43], s95 offen offset:0 sc1\n"
"v_mfma_f32_16x16x32_bf16 v[140:143], a[172:175], a[228:231], v[140:143]\n"
"ds_write_addtid_b32 v136 offset:4352\n"
"buffer_load_dwordx4 v[66:69], v13, s[40:43], s95 offen offset:1024 sc1\n"
"v_mfma_f32_16x16x32_bf16 v[140:143], a[176:179], a[232:235], v[140:143]\n"
"ds_write_addtid_b32 v137 offset:8992\n"
"buffer_load_dwordx4 v[70:73], v13, s[40:43], s95 offen offset:2048 sc1\n"
"v_mfma_f32_16x16x32_bf16 v[140:143], a[180:183], a[236:239], v[140:143]\n"
"ds_write_addtid_b32 v138 offset:13696\n"
"buffer_load_dwordx4 v[74:77], v13, s[40:43], s95 offen offset:3072 sc1\n"
"v_mfma_f32_16x16x32_bf16 v[140:143], a[184:187], a[240:243], v[140:143]\n"
"ds_write_addtid_b32 v139 offset:18336\n"
"buffer_load_dwordx4 v[78:81], v14, s[40:43], s95 offen sc1\n"
"v_mfma_f32_16x16x32_bf16 v[140:143], a[188:191], a[244:247], v[140:143]\n"
"s_mov_b32 s34, s36\n"
"v_mov_b32 v60, v24\n"
"v_mov_b32 v61, v25\n"
"v_mfma_f32_16x16x32_bf16 v[140:143], a[192:195], a[248:251], v[140:143]\n"
"v_mov_b32 v82, v26\n"
"v_mov_b32 v83, v27\n"
"v_mfma_f32_16x16x32_bf16 v[136:139], a[196:199], a[224:227], 0\n"
"v_mfma_f32_16x16x32_bf16 v[136:139], a[200:203], a[228:231], v[136:139]\n"
"s_waitcnt lgkmcnt(0)\n"
"v_mfma_f32_16x16x32_bf16 v[136:139], a[204:207], a[232:235], v[136:139]\n"
"s_barrier\n"
"v_mfma_f32_16x16x32_bf16 v[136:139], a[208:211], a[236:239], v[136:139]\n"
"ds_read_b128 v[144:147], v2 offset:3072\n"
"ds_read_b128 v[148:151], v3 offset:3584\n"
"v_mfma_f32_16x16x32_bf16 v[136:139], a[212:215], a[240:243], v[136:139]\n"
"ds_read_b128 v[152:155], v4 offset:4096\n"
"ds_read_b128 v[156:159], v2 offset:3328\n"
"v_mfma_f32_16x16x32_bf16 v[136:139], a[216:219], a[244:247], v[136:139]\n"
"s_waitcnt vmcnt(16)\n"
"ds_read_b128 v[160:163], v3 offset:3840\n"
"ds_read_b128 v[164:167], v4 offset:4352\n"
"v_mfma_f32_16x16x32_bf16 v[168:171], v[32:35], v[36:39], 0\n"
"s_cmp_ge_i32 s35, s76\n"
"s_cbranch_scc1 .LJ_w0s\n"
".LJ_w0r:\n"
"v_mfma_f32_16x16x32_bf16 v[168:171], v[32:35], v[44:47], v[168:171]\n"
"s_lshr_b32 s94, s35, s77\n"
"v_mfma_f32_16x16x32_bf16 v[168:171], v[32:35], v[52:55], v[168:171]\n"
"s_bitcmp1_b32 s94, 15\n"
"s_cselect_b32 s95, s79, s78\n"
"v_mfma_f32_16x16x32_bf16 v[172:175], v[32:35], v[40:43], 0\n"
"s_add_u32 s94, s94, s95\n"
"v_mfma_f32_16x16x32_bf16 v[172:175], v[32:35], v[48:51], v[172:175]\n"
"v_mfma_f32_16x16x32_bf16 v[172:175], v[32:35], v[56:59], v[172:175]\n"
"v_cvt_pk_bf16_f32 v176, v168, v169\n"
"v_cvt_pk_bf16_f32 v177, v170, v171\n"
"v_mfma_f32_16x16x32_bf16 v[136:139], a[220:223], a[248:251], v[136:139]\n"
"s_branch .LJ_top\n"
".LJ_1s:\n"
"s_nop 7\n"
"s_cmp_ge_u32 s50, s53\n"
"s_cbranch_scc1 .LJ_end\n"
"s_cmp_ge_u32 s50, s52\n"
"s_cbranch_scc1 .LJ_1r\n"
"s_sub_u32 s58, s50, 0x180000\n"
"s_mov_b32 s59, 200000\n"
".LJ_1p:\n"
"buffer_load_dwordx2 v[168:169], v7, s[40:43], s58 offen sc1\n"
"s_waitcnt vmcnt(0)\n"
"v_cmp_ne_u64 vcc, s[48:49], v[168:169]\n"
"s_cbranch_vccz .LJ_1px\n"
"s_sleep 1\n"
"s_sub_u32 s59, s59, 1\n"
"s_cbranch_scc0 .LJ_1p\n"
"s_branch .LJ_end\n"
".LJ_1px:\n"
"s_branch .LJ_1r\n"
".LJ_2s:\n"
"s_nop 7\n"
"s_cmp_ge_u32 s50, s53\n"
"s_cbranch_scc1 .LJ_end\n"
"s_cmp_ge_u32 s50, s52\n"
"s_cbranch_scc1 .LJ_2r\n"
"s_sub_u32 s58, s50, 0x180000\n"
"s_mov_b32 s59, 200000\n"
".LJ_2p:\n"
"buffer_load_dwordx2 v[138:139], v7, s[40:43], s58 offen sc1\n"
"s_waitcnt vmcnt(0)\n"
"v_cmp_ne_u64 vcc, s[48:49], v[138:139]\n"
"s_cbranch_vccz .LJ_2px\n"
"s_sleep 1\n"
"s_sub_u32 s59, s59, 1\n"
"s_cbranch_scc0 .LJ_2p\n"
"s_branch .LJ_end\n"
".LJ_2px:\n"
"s_branch .LJ_2r\n"
".LJ_3s:\n"
"s_nop 7\n"
"s_cmp_ge_u32 s50, s53\n"
"s_cbranch_scc1 .LJ_end\n"
"s_cmp_ge_u32 s50, s52\n"
"s_cbranch_scc1 .LJ_3r\n"
"s_sub_u32 s58, s50, 0x180000\n"
"s_mov_b32 s59, 200000\n"
".LJ_3p:\n"
"buffer_load_dwordx2 v[180:181], v7, s[40:43], s58 offen sc1\n"
"s_waitcnt vmcnt(0)\n"
"v_cmp_ne_u64 vcc, s[48:49], v[180:181]\n"
"s_cbranch_vccz .LJ_3px\n"
"s_sleep 1\n"
"s_sub_u32 s59, s59, 1\n"
"s_cbranch_scc0 .LJ_3p\n"
"s_branch .LJ_end\n"
".LJ_3px:\n"
"s_branch .LJ_3r\n"
".LJ_4s:\n"
"s_nop 7\n"
"s_cmp_ge_u32 s39, 0x10000000\n"
"s_cbranch_scc1 .LJ_4r\n"
"s_mov_b32 s59, 200000\n"
".LJ_4p:\n"
"buffer_load_dwordx2 v[136:137], v10, s[40:43], s39 offen offset:0 sc1\n"
"s_waitcnt vmcnt(0)\n"
"v_cmp_ne_u64 vcc, s[48:49], v[136:137]\n"
"s_cbranch_vccz .LJ_4px\n"
"s_sleep 1\n"
"s_sub_u32 s59, s59, 1\n"
"s_cbranch_scc0 .LJ_4p\n"
"s_branch .LJ_end\n"
".LJ_4px:\n"
"s_branch .LJ_4r\n"
".LJ_w0s:\n"
"s_nop 7\n"
"s_mov_b64 s[72:73], s[64:65]\n"
"s_mov_b32 s74, s66\n"
"v_mov_b32 v15, v16\n"
"s_mov_b32 s76, 0x7fffffff\n"
"s_mov_b32 s77, 4\n"
"s_mov_b32 s78, 0xff400000\n"
"s_mov_b32 s79, 0xff3f8800\n"
"s_mov_b32 s80, 1024\n"
"s_branch .LJ_w0r\n"
".LJ_5s:\n"
"s_nop 7\n"
"s_cmp_ge_u32 s36, 0x10000000\n"
"s_cbranch_scc1 .LJ_5r\n"
"s_mov_b32 s59, 200000\n"
".LJ_5p:\n"
"buffer_load_dwordx2 v[144:145], v10, s[40:43], s36 offen offset:512 sc1\n"
"s_waitcnt vmcnt(0)\n"
"v_cmp_ne_u64 vcc, s[48:49], v[144:145]\n"
"s_cbranch_vccz .LJ_5px\n"
"s_sleep 1\n"
"s_sub_u32 s59, s59, 1\n"
"s_cbranch_scc0 .LJ_5p\n"
"s_branch .LJ_end\n"
".LJ_5px:\n"
"s_branch .LJ_5r\n"
".LJ_w1s:\n"
"s_nop 7\n"
"s_mov_b64 s[72:73], s[64:65]\n"
"s_mov_b32 s74, s66\n"
"v_mov_b32 v15, v16\n"
"s_mov_b32 s76, 0x7fffffff\n"
"s_mov_b32 s77, 4\n"
"s_mov_b32 s78, 0xff400000\n"
"s_mov_b32 s79, 0xff3f8800\n"
"s_mov_b32 s80, 1024\n"
"s_branch .LJ_w1r\n"
".LJ_end:\n"
"s_cmp_lt_u32 s50, s53\n"
"s_cbranch_scc1 .LJ_ne\n"
"s_cmp_eq_u64 s[68:69], 0\n"
"s_cbranch_scc1 .LJ_ne\n"
"v_mov_b32 v7, 0\n"
"global_store_dwordx2 v7, v[0:1], s[68:69] sc1\n"
".LJ_ne:\n"
"s_waitcnt vmcnt(0) lgkmcnt(0)\n"
"s_endpgm\n"
      :: "{v2}"((unsigned)(lds0 + ((wid + 3) & 3) * 18592 + trp)), "{v3}"((unsigned)(lds0 + ((wid + 2) & 3) * 18592 + trp)), "{v4}"((unsigned)(lds0 + ((wid + 1) & 3) * 18592 + trp)), "{v5}"((unsigned)(lane * 16 + f0 * 1024 + 98304)), "{v6}"((unsigned)(lane * 16 + f0 * 1024 + 102400)), "{v7}"((unsigned)((f0 + lane % 7) * 8 + 353024 + J_GL * 1572864)), "{v8}"((unsigned)(lane * 16 + pair * 4096 + kq * 1024 + 212992)), "{v9}"((unsigned)(lane ? DROP : pair * 32 + kq * 8 + 354048)), "{v10}"((unsigned)(v16 * 32 + (lane & 3) * 8 + 354048)), "{v11}"((unsigned)(lane * 16 + v16 * 4096 + 212992)), "{v12}"((unsigned)(lane * 16 + v16 * 1024 + 65536)), "{v13}"((unsigned)(lane * 16 + v16 * 4096 + 278528)), "{v14}"((unsigned)(lane * 16 + v16 * 1024 + 81920)), "{v15}"((unsigned)(lane * 16 + v16 * 1024 + 0)), "{v16}"((unsigned)(pi * 4096 + lg * 16 + v16 * 64)), "{v17}"((unsigned)(lg == (li >> 2) ? v16 * 128 + li * 8 + 67457024 : DROP)), "{v18}"((unsigned)(lane ? DROP : v16 * 8 + 67463936)), "{v20}"((unsigned)(bia0[0])), "{v21}"((unsigned)(bia0[1])), "{v22}"((unsigned)(bia0[2])), "{v23}"((unsigned)(bia0[3])), "{v24}"((unsigned)(bia1[0])), "{v25}"((unsigned)(bia1[1])), "{v26}"((unsigned)(bia1[2])), "{v27}"((unsigned)(bia1[3])), "{v28}"((unsigned)(0x3f803f80)), "{v29}"((unsigned)(0x3f803f80)), "{v30}"((unsigned)(0x3f803f80)), "{v31}"((unsigned)(0x3f803f80)), "{v32}"((unsigned)(sel0)), "{v33}"((unsigned)(sel1)), "{v34}"((unsigned)(sel0)), "{v35}"((unsigned)(sel1)), "{v36}"((unsigned)(vT + tsw)), "{v37}"((unsigned)(vT + (tsw ^ 8))), "{v38}"((unsigned)(vD)), "{s[40:43]}"(rS), "{s[44:47]}"(rW), "{s[48:49]}"(wsp), "{s50}"((unsigned)(sA)), "{s51}"((unsigned)(sLo)), "{s52}"((unsigned)(sHi)), "{s53}"((unsigned)(sEx)), "{s54}"((unsigned)(sEnt)), "{s55}"((unsigned)(sLd)), "{s56}"((unsigned)(sA & 0xfbffffff)), "{s57}"((unsigned)(lds0 + wid * 18592)), "{s[60:61]}"(0xAAAAAAAAAAAAAAAAull), "{s[62:63]}"(0xCCCCCCCCCCCCCCCCull), "{s[64:67]}"(rO), "{s[68:69]}"(ep), "{s[72:75]}"(rS), "{s76}"((unsigned)(201326592)), "{s77}"((unsigned)(0)), "{s78}"((unsigned)(0)), "{s79}"((unsigned)(0)), "{s80}"((unsigned)(16384)), "{s81}"((unsigned)(sW0 + 64 * ((wid + 1) & 3))), "{s82}"((unsigned)(sW0 + 64 * ((wid + 2) & 3))), "{s83}"((unsigned)(sW0 + 64 * ((wid + 3) & 3))), "{s84}"((unsigned)(sW0 + 64 * ((wid + 4) & 3)))
      : "v255", "a255", "s95", "vcc", "scc", "memory");

    }
  }
}

extern "C" void kernel_launch(void* const* d_in, const int* in_sizes, int n_in, void* d_out, int out_size,
                              void* d_ws, size_t ws_size, hipStream_t stream) {
  Args m{};
  m.x = (const u16*)d_in[0]; m.wqkv = (const u16*)d_in[1]; m.wo = (const u16*)d_in[2]; m.w1 = (const u16*)d_in[4];
  m.b1 = (const u16*)d_in[5]; m.w2 = (const u16*)d_in[6]; m.b2 = (const u16*)d_in[7];
  m.out = (u16*)d_out; m.ws = (char*)d_ws;
  (void)hipFuncSetAttribute((const void*)stream_kernel, hipFuncAttributeMaxDynamicSharedMemorySize, LDS_BYTES);
  hipLaunchKernelGGL(stream_kernel, dim3(256), dim3(256), LDS_BYTES, stream, m);
}
```

```cpp
#include <hip/hip_runtime.h>
#include <stdint.h>
typedef unsigned short u16;
typedef unsigned long long u64;
typedef __attribute__((ext_vector_type(4))) unsigned u32x4;
constexpr unsigned F_X = 0;
constexpr unsigned F_ATT = 32768;
constexpr unsigned F_X1 = 65536;
constexpr unsigned F_H = 98304;
constexpr unsigned F_P = 212992;
constexpr unsigned F_ST1 = 344064;
constexpr unsigned F_ST2 = 348160;
constexpr unsigned F_G = 352256;
constexpr unsigned F_FA = 352512;
constexpr unsigned F_FX1 = 352768;
constexpr unsigned F_FH = 353024;
constexpr unsigned F_FP = 354048;
constexpr unsigned F_FX = 355072;
constexpr unsigned RS = 524288;
constexpr unsigned PADF = 256;
constexpr unsigned LOWB = 64;
constexpr unsigned L_TAB = 98304;
constexpr unsigned LDS_BYTES = 163840;
constexpr int Q_GL = 2;
constexpr int Q_EX = 6;
constexpr int O_GL = 1;
constexpr int O_EX = 3;
constexpr int F_GL = 1;
constexpr int F_EX = 3;
constexpr int G_GL = 1;
constexpr int G_EX = 3;
constexpr int H_GL = 1;
constexpr int H_EX = 3;
constexpr int J_GL = 1;
constexpr int J_EX = 6;
#define MAPV 1
#define FDLY 0
#define PF 7
#define KR 1
#define XP 2

constexpr unsigned DROP = 0x40000000u, WS0 = 1u << 20;
static_assert(WS0 + (PADF + 256 - LOWB) * RS <= (256u << 20), "workspace");
struct Args { const u16 *x, *wqkv, *wo, *w1, *b1, *w2, *b2; u16* out; char* ws;
};
__device__ __forceinline__ u32x4 desc(const void* p, unsigned n) { const u64 a = (u64)p; return (u32x4){(unsigned)a, (unsigned)(a >> 32) & 0xffffu, n, 0x00020000u}; }
__device__ __forceinline__ float lo16(unsigned v) { return __builtin_bit_cast(float, v << 16); }
__device__ __forceinline__ float hi16(unsigned v) { return __builtin_bit_cast(float, v & 0xffff0000u); }

__host__ __device__ inline void decode(int bid, int& role, int& set, int& idx) {
  const int xg = bid & 7, xj = bid >> 3;
  if (xg < 4) {
    if (xj < 16) { role = 0; set = xj & 1; idx = MAPV == 1 ? 8 * xg + (xj >> 1) : MAPV == 2 ? 4 * (xj >> 1) + xg : 4 * (xj >> 1) + ((xg + 2 * set) & 3); }
    else { role = 2; set = xj < 23 ? 1 : xj < 30 ? 2 : 0; idx = 7 * xg + (xj - 16) % 7; }
  } else if (xg == 4) {
    if (xj >= 20) { role = 1; set = xj < 28 ? 2 : 1; idx = xj < 28 ? xj - 20 : xj - 24; }
    else { role = 2; set = 0; idx = 7 * (xj / 5) + 2 + xj % 5; }
  } else if (xj >= 28) { role = 1; set = xg == 7; idx = (xg == 6) * 4 + xj - 28; }
  else { role = 3; set = xg - 5; idx = xj; }
}

__global__ __launch_bounds__(256, 1) void stream_kernel(Args a) {
  extern __shared__ __attribute__((aligned(16))) char smem[];
  const int tid = threadIdx.x, lane = tid & 63, wid = __builtin_amdgcn_readfirstlane(tid >> 6), li = lane & 15, lg = lane >> 4;
  const unsigned lds0 = (unsigned)(size_t)smem;
  const int bp = (blockIdx.x + 2 * (XP - 1)) & 7, bid = (blockIdx.x & ~7) | ((bp & 1) << 2) | (bp >> 1);
  int role, set, idx;
  decode(bid, role, set, idx);
  const u64 wsp = (u64)a.ws;
  const u32x4 rS = desc(a.ws + WS0 - LOWB * RS, (PADF + 256) * RS);
  const int pi = (li >> 2) + 4 * (li & 3);
  const int xj = bid >> 3, xo = (bid & 7) - 4;
  if (role == 2 && xo < 0 && (PF & 1)) {
    const int g = (xj - 16) * 4 + wid;
    const char* p = (const char*)a.wqkv + (((g >> 2) * 2 + 1) * 32 + (g & 3) * 8 + (lane >> 5)) * 6144 + (bid & 7) * 512 + (lane & 31) * 16;
    u32x4 t[12];
    unsigned fl = 0;
    if (PF & 4) fl = __hip_atomic_load((const unsigned*)(a.ws + WS0 + (size_t)(PADF - LOWB + g * 4 + (bid & 7)) * RS + F_ST2 + (lane < 56 ? lane : 0) * 128), __ATOMIC_RELAXED, __HIP_MEMORY_SCOPE_AGENT);
#pragma unroll
    for (int i = 0; i < 12; ++i) t[i] = *(const u32x4*)(p + (i & 3) * 12288 + (i >> 2) * 2048);
#pragma unroll
    for (int i = 0; i < 12; ++i) asm volatile("" ::"v"(t[i]));
    asm volatile("" ::"v"(fl));
  }
  if (role == 2 && xo < 0) for (int i = 0; i < FDLY; ++i) __builtin_amdgcn_s_sleep(16);
  for (int pr = role < 2 || xo < 0 ? 128 : xj < 20 ? xj * 4 + xo : xj * 3 + 19 + xo; pr < 128; pr += pr < 80 ? 128 : 24) {
    const __amdgpu_buffer_rsrc_t rb = __builtin_amdgcn_make_buffer_rsrc(a.ws + WS0 - LOWB * RS, 0, (PADF + 256) * RS, 0x00020000);
    const u32x4* src = (const u32x4*)(a.x + ((pr >> 1) * 32 + 2 * pi + (pr & 1)) * 1024 + wid * 256 + lg * 8);
    const u64 ev = __hip_atomic_load((const u64*)a.ws, __ATOMIC_RELAXED, __HIP_MEMORY_SCOPE_AGENT);
    u32x4 v[8];
#pragma unroll
    for (int k = 0; k < 8; ++k) v[k] = src[k * 4];
    u32x4 pf = {};
    if (pr < 104 && (PF & 2)) {
      const int g = xj * 4 + wid, n = xo ? 7 : 20;
      for (int i = 0; i < n; ++i) {
        const int u = g * n + i, r0 = xo ? u : u >> 1, row = r0 < 768 ? (r0 / 192) * 256 + 64 + r0 % 192 : 0;
        asm volatile("global_load_dwordx4 %0, %1, off" : "+v"(pf) : "v"((const char*)a.wo + row * 2048 + (xo ? xo == 2 : u & 1) * 1024 + lane * 16));
      }
    }
    float s = 0.f, q = 0.f;
#pragma unroll
    for (int k = 0; k < 8; ++k) {
      __builtin_amdgcn_raw_buffer_store_b128(v[k], rb, lane * 16 + k * 1024, (PADF + pr) * RS + F_X + wid * 8192, 16);
#pragma unroll
      for (int d = 0; d < 4; ++d) { const float l = lo16(v[k][d]), h = hi16(v[k][d]); s += l + h; q = fmaf(l, l, fmaf(h, h, q)); }
    }
    s += __shfl_xor(s, 16); q += __shfl_xor(q, 16); s += __shfl_xor(s, 32); q += __shfl_xor(q, 32);
    float2* t = (float2*)smem;
    if (lg == 0) t[wid * 16 + li] = make_float2(s, q);
    asm volatile("s_waitcnt vmcnt(0)" : "+v"(pf)::"memory");
    __syncthreads();
    if (tid < 16) {
      const float2 t0 = t[li], t1 = t[16 + li], t2 = t[32 + li], t3 = t[48 + li];
      const float nm = ((t0.x + t1.x) + (t2.x + t3.x)) * -0x1p-10f;
      const float var = fmaxf(fmaf(-nm, nm, fmaf((t0.y + t1.y) + (t2.y + t3.y), 0x1p-10f, 1e-5f)), 1e-5f);
      const u32x4 g = {__builtin_bit_cast(unsigned, __builtin_amdgcn_rsqf(var)), __builtin_bit_cast(unsigned, nm), ((unsigned)ev + 2u) | 1u, (unsigned)(ev >> 32) + 0x9E3779B9u};
      __builtin_amdgcn_raw_buffer_store_b128(g, rb, li * 16, (PADF + pr) * RS + F_G, 16);
    }
    __syncthreads();
  }
  const unsigned sLo = (PADF + set) * RS, sLd = lds0 + wid * 40960, vT = sLd + (8 * lg + (li >> 2)) * 64 + (li & 3) * 16;
  const unsigned trp = (li & 3) * 4608 + (li & 1) * 32 + (li & 2) * 64 + (li >> 2) * 64 + lg * 16;
  if (role == 0) {
    const unsigned sA = sLo - 2 * RS, sHi = sLo + 127 * 2 * RS, sEx = sHi + Q_EX * 2 * RS, sEnt = -wid & 3, tsw = 0, rot = KR * (set + 2 * (idx & 1));
    const unsigned sW0 = wid * 256 * 6144 + idx * 64, vD = (lane >> 2) * 6144 + (lane & 3) * 16;
    const u32x4 rW = desc(a.wqkv, 1024 * 6144);
    auto ok = [&](int r, int prev) { const int k = lg + 4 * r; return prev ? k >= pi + 9 : (k <= pi && k >= pi - 7); };
    auto qmask = [&](int r, int prev) { return ok(r, prev) ? 0u : 0xff800000u; };
    auto pmask = [&](int d) { return (ok(2 * (d & 1), d >> 1) ? 0xffffu : 0u) | (ok(2 * (d & 1) + 1, d >> 1) ? 0xffff0000u : 0u); };
    asm volatile(
"s_nop 4\n"
"v_mov_b32 v186, 0\n"
"global_load_dwordx2 v[0:1], v186, s[48:49] sc1\n"
"v_mov_b32 v188, 0x3f803f80\n"
"v_mov_b32 v189, 0x3f803f80\n"
"v_mov_b32 v190, 0x3f803f80\n"
"v_mov_b32 v191, 0x3f803f80\n"
"s_add_u32 m0, s55, 0\n"
"s_add_u32 s58, s61, 0\n"
"buffer_load_dwordx4 v50, s[44:47], s58 offen lds\n"
"s_add_u32 m0, s55, 1024\n"
"s_add_u32 s58, s61, 98304\n"
"buffer_load_dwordx4 v50, s[44:47], s58 offen lds\n"
"s_add_u32 m0, s55, 2048\n"
"s_add_u32 s58, s62, 0\n"
"buffer_load_dwordx4 v50, s[44:47], s58 offen lds\n"
"s_add_u32 m0, s55, 3072\n"
"s_add_u32 s58, s62, 98304\n"
"buffer_load_dwordx4 v50, s[44:47], s58 offen lds\n"
"s_add_u32 m0, s55, 4096\n"
"s_add_u32 s58, s63, 0\n"
"buffer_load_dwordx4 v50, s[44:47], s58 offen lds\n"
"s_add_u32 m0, s55, 5120\n"
"s_add_u32 s58, s63, 98304\n"
"buffer_load_dwordx4 v50, s[44:47], s58 offen lds\n"
"s_add_u32 m0, s55, 6144\n"
"s_add_u32 s58, s61, 196608\n"
"buffer_load_dwordx4 v50, s[44:47], s58 offen lds\n"
"s_add_u32 m0, s55, 7168\n"
"s_add_u32 s58, s61, 294912\n"
"buffer_load_dwordx4 v50, s[44:47], s58 offen lds\n"
"s_add_u32 m0, s55, 8192\n"
"s_add_u32 s58, s62, 196608\n"
"buffer_load_dwordx4 v50, s[44:47], s58 offen lds\n"
"s_add_u32 m0, s55, 9216\n"
"s_add_u32 s58, s62, 294912\n"
"buffer_load_dwordx4 v50, s[44:47], s58 offen lds\n"
"s_add_u32 m0, s55, 10240\n"
"s_add_u32 s58, s63, 196608\n"
"buffer_load_dwordx4 v50, s[44:47], s58 offen lds\n"
"s_add_u32 m0, s55, 11264\n"
"s_add_u32 s58, s63, 294912\n"
"buffer_load_dwordx4 v50, s[44:47], s58 offen lds\n"
"s_add_u32 m0, s55, 12288\n"
"s_add_u32 s58, s61, 0\n"
"buffer_load_dwordx4 v51, s[44:47], s58 offen lds\n"
"s_add_u32 m0, s55, 13312\n"
"s_add_u32 s58, s61, 98304\n"
"buffer_load_dwordx4 v51, s[44:47], s58 offen lds\n"
"s_add_u32 m0, s55, 14336\n"
"s_add_u32 s58, s62, 0\n"
"buffer_load_dwordx4 v51, s[44:47], s58 offen lds\n"
"s_add_u32 m0, s55, 15360\n"
"s_add_u32 s58, s62, 98304\n"
"buffer_load_dwordx4 v51, s[44:47], s58 offen lds\n"
"s_add_u32 m0, s55, 16384\n"
"s_add_u32 s58, s63, 0\n"
"buffer_load_dwordx4 v51, s[44:47], s58 offen lds\n"
"s_add_u32 m0, s55, 17408\n"
"s_add_u32 s58, s63, 98304\n"
"buffer_load_dwordx4 v51, s[44:47], s58 offen lds\n"
"s_add_u32 m0, s55, 18432\n"
"s_add_u32 s58, s61, 196608\n"
"buffer_load_dwordx4 v51, s[44:47], s58 offen lds\n"
"s_add_u32 m0, s55, 19456\n"
"s_add_u32 s58, s61, 294912\n"
"buffer_load_dwordx4 v51, s[44:47], s58 offen lds\n"
"s_add_u32 m0, s55, 20480\n"
"s_add_u32 s58, s62, 196608\n"
"buffer_load_dwordx4 v51, s[44:47], s58 offen lds\n"
"s_add_u32 m0, s55, 21504\n"
"s_add_u32 s58, s62, 294912\n"
"buffer_load_dwordx4 v51, s[44:47], s58 offen lds\n"
"s_add_u32 m0, s55, 22528\n"
"s_add_u32 s58, s63, 196608\n"
"buffer_load_dwordx4 v51, s[44:47], s58 offen lds\n"
"s_add_u32 m0, s55, 23552\n"
"s_add_u32 s58, s63, 294912\n"
"buffer_load_dwordx4 v51, s[44:47], s58 offen lds\n"
"s_add_u32 m0, s55, 24576\n"
"s_add_u32 s58, s61, 0\n"
"buffer_load_dwordx4 v52, s[44:47], s58 offen lds\n"
"s_add_u32 m0, s55, 25600\n"
"s_add_u32 s58, s61, 98304\n"
"buffer_load_dwordx4 v52, s[44:47], s58 offen lds\n"
"s_add_u32 m0, s55, 26624\n"
"s_add_u32 s58, s62, 0\n"
"buffer_load_dwordx4 v52, s[44:47], s58 offen lds\n"
"s_add_u32 m0, s55, 27648\n"
"s_add_u32 s58, s62, 98304\n"
"buffer_load_dwordx4 v52, s[44:47], s58 offen lds\n"
"s_add_u32 m0, s55, 28672\n"
"s_add_u32 s58, s63, 0\n"
"buffer_load_dwordx4 v52, s[44:47], s58 offen lds\n"
"s_add_u32 m0, s55, 29696\n"
"s_add_u32 s58, s63, 98304\n"
"buffer_load_dwordx4 v52, s[44:47], s58 offen lds\n"
"s_add_u32 m0, s55, 30720\n"
"s_add_u32 s58, s61, 196608\n"
"buffer_load_dwordx4 v52, s[44:47], s58 offen lds\n"
"s_add_u32 m0, s55, 31744\n"
"s_add_u32 s58, s61, 294912\n"
"buffer_load_dwordx4 v52, s[44:47], s58 offen lds\n"
"s_add_u32 m0, s55, 32768\n"
"s_add_u32 s58, s62, 196608\n"
"buffer_load_dwordx4 v52, s[44:47], s58 offen lds\n"
"s_add_u32 m0, s55, 33792\n"
"s_add_u32 s58, s62, 294912\n"
"buffer_load_dwordx4 v52, s[44:47], s58 offen lds\n"
"s_add_u32 m0, s55, 34816\n"
"s_add_u32 s58, s63, 196608\n"
"buffer_load_dwordx4 v52, s[44:47], s58 offen lds\n"
"s_add_u32 m0, s55, 35840\n"
"s_add_u32 s58, s63, 294912\n"
"buffer_load_dwordx4 v52, s[44:47], s58 offen lds\n"
"s_add_u32 m0, s55, 36864\n"
"s_add_u32 s58, s61, 0\n"
"buffer_load_dwordx4 v53, s[44:47], s58 offen lds\n"
"s_add_u32 m0, s55, 37888\n"
"s_add_u32 s58, s61, 98304\n"
"buffer_load_dwordx4 v53, s[44:47], s58 offen lds\n"
"s_add_u32 m0, s55, 38912\n"
"s_add_u32 s58, s62, 0\n"
"buffer_load_dwordx4 v53, s[44:47], s58 offen lds\n"
"s_add_u32 m0, s55, 39936\n"
"s_add_u32 s58, s62, 98304\n"
"buffer_load_dwordx4 v53, s[44:47], s58 offen lds\n"
"s_waitcnt vmcnt(38)\n"
"ds_read_b64_tr_b16 a[0:1], v48 offset:0\n"
"ds_read_b64_tr_b16 a[2:3], v48 offset:256\n"
"ds_read_b64_tr_b16 a[32:33], v49 offset:0\n"
"ds_read_b64_tr_b16 a[34:35], v49 offset:256\n"
"v_add_u32 v0, 2, v0\n"
"v_add_u32 v1, 0x9e3779b9, v1\n"
"v_or_b32 v0, 1, v0\n"
"v_readfirstlane_b32 s49, v1\n"
"v_readfirstlane_b32 s48, v0\n"
"s_waitcnt vmcnt(36)\n"
"ds_read_b64_tr_b16 a[64:65], v48 offset:2048\n"
"ds_read_b64_tr_b16 a[66:67], v48 offset:2304\n"
"ds_read_b64_tr_b16 a[96:97], v49 offset:2048\n"
"ds_read_b64_tr_b16 a[98:99], v49 offset:2304\n"
"s_waitcnt lgkmcnt(4)\n"
"s_add_u32 m0, s55, 0\n"
"s_add_u32 s58, s63, 0\n"
"buffer_load_dwordx4 v53, s[44:47], s58 offen lds\n"
"s_add_u32 m0, s55, 1024\n"
"s_add_u32 s58, s63, 98304\n"
"buffer_load_dwordx4 v53, s[44:47], s58 offen lds\n"
"v_mfma_f32_16x16x32_bf16 v[192:195], v[188:191], a[0:3], 0\n"
"v_mfma_f32_16x16x32_bf16 v[196:199], v[188:191], a[32:35], 0\n"
"s_waitcnt vmcnt(36)\n"
"ds_read_b64_tr_b16 a[128:129], v48 offset:4096\n"
"ds_read_b64_tr_b16 a[130:131], v48 offset:4352\n"
"ds_read_b64_tr_b16 a[160:161], v49 offset:4096\n"
"ds_read_b64_tr_b16 a[162:163], v49 offset:4352\n"
"s_waitcnt lgkmcnt(4)\n"
"s_add_u32 m0, s55, 2048\n"
"s_add_u32 s58, s61, 196608\n"
"buffer_load_dwordx4 v53, s[44:47], s58 offen lds\n"
"s_add_u32 m0, s55, 3072\n"
"s_add_u32 s58, s61, 294912\n"
"buffer_load_dwordx4 v53, s[44:47], s58 offen lds\n"
"v_mfma_f32_16x16x32_bf16 v[200:203], v[188:191], a[64:67], 0\n"
"v_mfma_f32_16x16x32_bf16 v[204:207], v[188:191], a[96:99], 0\n"
"s_waitcnt vmcnt(36)\n"
"ds_read_b64_tr_b16 a[4:5], v48 offset:6144\n"
"ds_read_b64_tr_b16 a[6:7], v48 offset:6400\n"
"ds_read_b64_tr_b16 a[36:37], v49 offset:6144\n"
"ds_read_b64_tr_b16 a[38:39], v49 offset:6400\n"
"s_waitcnt lgkmcnt(4)\n"
"s_add_u32 m0, s55, 4096\n"
"s_add_u32 s58, s62, 196608\n"
"buffer_load_dwordx4 v53, s[44:47], s58 offen lds\n"
"s_add_u32 m0, s55, 5120\n"
"s_add_u32 s58, s62, 294912\n"
"buffer_load_dwordx4 v53, s[44:47], s58 offen lds\n"
"v_mfma_f32_16x16x32_bf16 v[208:211], a[128:131], v[188:191], 0\n"
"v_mfma_f32_16x16x32_bf16 v[212:215], a[160:163], v[188:191], 0\n"
"s_waitcnt vmcnt(36)\n"
"ds_read_b64_tr_b16 a[68:69], v48 offset:8192\n"
"ds_read_b64_tr_b16 a[70:71], v48 offset:8448\n"
"ds_read_b64_tr_b16 a[100:101], v49 offset:8192\n"
"ds_read_b64_tr_b16 a[102:103], v49 offset:8448\n"
"s_waitcnt lgkmcnt(4)\n"
"s_add_u32 m0, s55, 6144\n"
"s_add_u32 s58, s63, 196608\n"
"buffer_load_dwordx4 v53, s[44:47], s58 offen lds\n"
"s_add_u32 m0, s55, 7168\n"
"s_add_u32 s58, s63, 294912\n"
"buffer_load_dwordx4 v53, s[44:47], s58 offen lds\n"
"v_mfma_f32_16x16x32_bf16 v[192:195], v[188:191], a[4:7], v[192:195]\n"
"v_mfma_f32_16x16x32_bf16 v[196:199], v[188:191], a[36:39], v[196:199]\n"
"s_waitcnt vmcnt(36)\n"
"ds_read_b64_tr_b16 a[132:133], v48 offset:10240\n"
"ds_read_b64_tr_b16 a[134:135], v48 offset:10496\n"
"ds_read_b64_tr_b16 a[164:165], v49 offset:10240\n"
"ds_read_b64_tr_b16 a[166:167], v49 offset:10496\n"
"s_waitcnt lgkmcnt(4)\n"
"v_mfma_f32_16x16x32_bf16 v[200:203], v[188:191], a[68:71], v[200:203]\n"
"v_mfma_f32_16x16x32_bf16 v[204:207], v[188:191], a[100:103], v[204:207]\n"
"s_waitcnt vmcnt(34)\n"
"ds_read_b64_tr_b16 a[8:9], v48 offset:12288\n"
"ds_read_b64_tr_b16 a[10:11], v48 offset:12544\n"
"ds_read_b64_tr_b16 a[40:41], v49 offset:12288\n"
"ds_read_b64_tr_b16 a[42:43], v49 offset:12544\n"
"s_waitcnt lgkmcnt(4)\n"
"v_mfma_f32_16x16x32_bf16 v[208:211], a[132:135], v[188:191], v[208:211]\n"
"v_mfma_f32_16x16x32_bf16 v[212:215], a[164:167], v[188:191], v[212:215]\n"
"s_waitcnt vmcnt(32)\n"
"ds_read_b64_tr_b16 a[72:73], v48 offset:14336\n"
"ds_read_b64_tr_b16 a[74:75], v48 offset:14592\n"
"ds_read_b64_tr_b16 a[104:105], v49 offset:14336\n"
"ds_read_b64_tr_b16 a[106:107], v49 offset:14592\n"
"s_waitcnt lgkmcnt(4)\n"
"v_mfma_f32_16x16x32_bf16 v[192:195], v[188:191], a[8:11], v[192:195]\n"
"v_mfma_f32_16x16x32_bf16 v[196:199], v[188:191], a[40:43], v[196:199]\n"
"s_waitcnt vmcnt(30)\n"
"ds_read_b64_tr_b16 a[136:137], v48 offset:16384\n"
"ds_read_b64_tr_b16 a[138:139], v48 offset:16640\n"
"ds_read_b64_tr_b16 a[168:169], v49 offset:16384\n"
"ds_read_b64_tr_b16 a[170:171], v49 offset:16640\n"
"s_waitcnt lgkmcnt(4)\n"
"v_mfma_f32_16x16x32_bf16 v[200:203], v[188:191], a[72:75], v[200:203]\n"
"v_mfma_f32_16x16x32_bf16 v[204:207], v[188:191], a[104:107], v[204:207]\n"
"s_waitcnt vmcnt(28)\n"
"ds_read_b64_tr_b16 a[12:13], v48 offset:18432\n"
"ds_read_b64_tr_b16 a[14:15], v48 offset:18688\n"
"ds_read_b64_tr_b16 a[44:45], v49 offset:18432\n"
"ds_read_b64_tr_b16 a[46:47], v49 offset:18688\n"
"s_waitcnt lgkmcnt(4)\n"
"v_mfma_f32_16x16x32_bf16 v[208:211], a[136:139], v[188:191], v[208:211]\n"
"v_mfma_f32_16x16x32_bf16 v[212:215], a[168:171], v[188:191], v[212:215]\n"
"s_waitcnt vmcnt(26)\n"
"ds_read_b64_tr_b16 a[76:77], v48 offset:20480\n"
"ds_read_b64_tr_b16 a[78:79], v48 offset:20736\n"
"ds_read_b64_tr_b16 a[108:109], v49 offset:20480\n"
"ds_read_b64_tr_b16 a[110:111], v49 offset:20736\n"
"s_waitcnt lgkmcnt(4)\n"
"v_mfma_f32_16x16x32_bf16 v[192:195], v[188:191], a[12:15], v[192:195]\n"
"v_mfma_f32_16x16x32_bf16 v[196:199], v[188:191], a[44:47], v[196:199]\n"
"s_waitcnt vmcnt(24)\n"
"ds_read_b64_tr_b16 a[140:141], v48 offset:22528\n"
"ds_read_b64_tr_b16 a[142:143], v48 offset:22784\n"
"ds_read_b64_tr_b16 a[172:173], v49 offset:22528\n"
"ds_read_b64_tr_b16 a[174:175], v49 offset:22784\n"
"s_waitcnt lgkmcnt(4)\n"
"v_mfma_f32_16x16x32_bf16 v[200:203], v[188:191], a[76:79], v[200:203]\n"
"v_mfma_f32_16x16x32_bf16 v[204:207], v[188:191], a[108:111], v[204:207]\n"
"s_waitcnt vmcnt(22)\n"
"ds_read_b64_tr_b16 a[16:17], v48 offset:24576\n"
"ds_read_b64_tr_b16 a[18:19], v48 offset:24832\n"
"ds_read_b64_tr_b16 a[48:49], v49 offset:24576\n"
"ds_read_b64_tr_b16 a[50:51], v49 offset:24832\n"
"s_waitcnt lgkmcnt(4)\n"
"v_mfma_f32_16x16x32_bf16 v[208:211], a[140:143], v[188:191], v[208:211]\n"
"v_mfma_f32_16x16x32_bf16 v[212:215], a[172:175], v[188:191], v[212:215]\n"
"s_waitcnt vmcnt(20)\n"
"ds_read_b64_tr_b16 a[80:81], v48 offset:26624\n"
"ds_read_b64_tr_b16 a[82:83], v48 offset:26880\n"
"ds_read_b64_tr_b16 a[112:113], v49 offset:26624\n"
"ds_read_b64_tr_b16 a[114:115], v49 offset:26880\n"
"s_waitcnt lgkmcnt(4)\n"
"v_mfma_f32_16x16x32_bf16 v[192:195], v[188:191], a[16:19], v[192:195]\n"
"v_mfma_f32_16x16x32_bf16 v[196:199], v[188:191], a[48:51], v[196:199]\n"
"s_waitcnt vmcnt(18)\n"
"ds_read_b64_tr_b16 a[144:145], v48 offset:28672\n"
"ds_read_b64_tr_b16 a[146:147], v48 offset:28928\n"
"ds_read_b64_tr_b16 a[176:177], v49 offset:28672\n"
"ds_read_b64_tr_b16 a[178:179], v49 offset:28928\n"
"s_waitcnt lgkmcnt(4)\n"
"v_mfma_f32_16x16x32_bf16 v[200:203], v[188:191], a[80:83], v[200:203]\n"
"v_mfma_f32_16x16x32_bf16 v[204:207], v[188:191], a[112:115], v[204:207]\n"
"s_waitcnt vmcnt(16)\n"
"ds_read_b64_tr_b16 a[20:21], v48 offset:30720\n"
"ds_read_b64_tr_b16 a[22:23], v48 offset:30976\n"
"ds_read_b64_tr_b16 a[52:53], v49 offset:30720\n"
"ds_read_b64_tr_b16 a[54:55], v49 offset:30976\n"
"s_waitcnt lgkmcnt(4)\n"
"v_mfma_f32_16x16x32_bf16 v[208:211], a[144:147], v[188:191], v[208:211]\n"
"v_mfma_f32_16x16x32_bf16 v[212:215], a[176:179], v[188:191], v[212:215]\n"
"s_waitcnt vmcnt(14)\n"
"ds_read_b64_tr_b16 a[84:85], v48 offset:32768\n"
"ds_read_b64_tr_b16 a[86:87], v48 offset:33024\n"
"ds_read_b64_tr_b16 a[116:117], v49 offset:32768\n"
"ds_read_b64_tr_b16 a[118:119], v49 offset:33024\n"
"s_waitcnt lgkmcnt(4)\n"
"v_mfma_f32_16x16x32_bf16 v[192:195], v[188:191], a[20:23], v[192:195]\n"
"v_mfma_f32_16x16x32_bf16 v[196:199], v[188:191], a[52:55], v[196:199]\n"
"s_waitcnt vmcnt(12)\n"
"ds_read_b64_tr_b16 a[148:149], v48 offset:34816\n"
"ds_read_b64_tr_b16 a[150:151], v48 offset:35072\n"
"ds_read_b64_tr_b16 a[180:181], v49 offset:34816\n"
"ds_read_b64_tr_b16 a[182:183], v49 offset:35072\n"
"s_waitcnt lgkmcnt(4)\n"
"v_mfma_f32_16x16x32_bf16 v[200:203], v[188:191], a[84:87], v[200:203]\n"
"v_mfma_f32_16x16x32_bf16 v[204:207], v[188:191], a[116:119], v[204:207]\n"
"s_waitcnt vmcnt(10)\n"
"ds_read_b64_tr_b16 a[24:25], v48 offset:36864\n"
"ds_read_b64_tr_b16 a[26:27], v48 offset:37120\n"
"ds_read_b64_tr_b16 a[56:57], v49 offset:36864\n"
"ds_read_b64_tr_b16 a[58:59], v49 offset:37120\n"
"s_waitcnt lgkmcnt(4)\n"
"v_mfma_f32_16x16x32_bf16 v[208:211], a[148:151], v[188:191], v[208:211]\n"
"v_mfma_f32_16x16x32_bf16 v[212:215], a[180:183], v[188:191], v[212:215]\n"
"s_waitcnt vmcnt(8)\n"
"ds_read_b64_tr_b16 a[88:89], v48 offset:38912\n"
"ds_read_b64_tr_b16 a[90:91], v48 offset:39168\n"
"ds_read_b64_tr_b16 a[120:121], v49 offset:38912\n"
"ds_read_b64_tr_b16 a[122:123], v49 offset:39168\n"
"s_waitcnt lgkmcnt(4)\n"
"v_mfma_f32_16x16x32_bf16 v[192:195], v[188:191], a[24:27], v[192:195]\n"
"v_mfma_f32_16x16x32_bf16 v[196:199], v[188:191], a[56:59], v[196:199]\n"
"s_waitcnt vmcnt(6)\n"
"ds_read_b64_tr_b16 a[152:153], v48 offset:0\n"
"ds_read_b64_tr_b16 a[154:155], v48 offset:256\n"
"ds_read_b64_tr_b16 a[184:185], v49 offset:0\n"
"ds_read_b64_tr_b16 a[186:187], v49 offset:256\n"
"s_waitcnt lgkmcnt(4)\n"
"v_mfma_f32_16x16x32_bf16 v[200:203], v[188:191], a[88:91], v[200:203]\n"
"v_mfma_f32_16x16x32_bf16 v[204:207], v[188:191], a[120:123], v[204:207]\n"
"s_waitcnt vmcnt(4)\n"
"ds_read_b64_tr_b16 a[28:29], v48 offset:2048\n"
"ds_read_b64_tr_b16 a[30:31], v48 offset:2304\n"
"ds_read_b64_tr_b16 a[60:61], v49 offset:2048\n"
"ds_read_b64_tr_b16 a[62:63], v49 offset:2304\n"
"s_waitcnt lgkmcnt(4)\n"
"v_mfma_f32_16x16x32_bf16 v[208:211], a[152:155], v[188:191], v[208:211]\n"
"v_mfma_f32_16x16x32_bf16 v[212:215], a[184:187], v[188:191], v[212:215]\n"
"s_waitcnt vmcnt(2)\n"
"ds_read_b64_tr_b16 a[92:93], v48 offset:4096\n"
"ds_read_b64_tr_b16 a[94:95], v48 offset:4352\n"
"ds_read_b64_tr_b16 a[124:125], v49 offset:4096\n"
"ds_read_b64_tr_b16 a[126:127], v49 offset:4352\n"
"s_waitcnt lgkmcnt(4)\n"
"v_mfma_f32_16x16x32_bf16 v[192:195], v[188:191], a[28:31], v[192:195]\n"
"v_mfma_f32_16x16x32_bf16 v[196:199], v[188:191], a[60:63], v[196:199]\n"
"s_waitcnt vmcnt(0)\n"
"ds_read_b64_tr_b16 a[156:157], v48 offset:6144\n"
"ds_read_b64_tr_b16 a[158:159], v48 offset:6400\n"
"ds_read_b64_tr_b16 a[188:189], v49 offset:6144\n"
"ds_read_b64_tr_b16 a[190:191], v49 offset:6400\n"
"s_waitcnt lgkmcnt(4)\n"
"v_mfma_f32_16x16x32_bf16 v[200:203], v[188:191], a[92:95], v[200:203]\n"
"v_mfma_f32_16x16x32_bf16 v[204:207], v[188:191], a[124:127], v[204:207]\n"
"s_waitcnt lgkmcnt(0)\n"
"v_mfma_f32_16x16x32_bf16 v[208:211], a[156:159], v[188:191], v[208:211]\n"
"v_mfma_f32_16x16x32_bf16 v[212:215], a[188:191], v[188:191], v[212:215]\n"
"s_mov_b32 m0, s57\n"
"s_barrier\n"
"ds_write_addtid_b32 v192 offset:0\n"
"ds_write_addtid_b32 v193 offset:4640\n"
"ds_write_addtid_b32 v194 offset:9344\n"
"ds_write_addtid_b32 v195 offset:13984\n"
"ds_write_addtid_b32 v196 offset:256\n"
"ds_write_addtid_b32 v197 offset:4896\n"
"ds_write_addtid_b32 v198 offset:9600\n"
"ds_write_addtid_b32 v199 offset:14240\n"
"ds_write_addtid_b32 v200 offset:512\n"
"ds_write_addtid_b32 v201 offset:5152\n"
"ds_write_addtid_b32 v202 offset:9856\n"
"ds_write_addtid_b32 v203 offset:14496\n"
"ds_write_addtid_b32 v204 offset:768\n"
"ds_write_addtid_b32 v205 offset:5408\n"
"ds_write_addtid_b32 v206 offset:10112\n"
"ds_write_addtid_b32 v207 offset:14752\n"
"ds_write_addtid_b32 v208 offset:1024\n"
"ds_write_addtid_b32 v209 offset:5664\n"
"ds_write_addtid_b32 v210 offset:10368\n"
"ds_write_addtid_b32 v211 offset:15008\n"
"ds_write_addtid_b32 v212 offset:1280\n"
"ds_write_addtid_b32 v213 offset:5920\n"
"ds_write_addtid_b32 v214 offset:10624\n"
"ds_write_addtid_b32 v215 offset:15264\n"
"s_waitcnt lgkmcnt(0)\n"
"s_barrier\n"
"ds_read_b128 v[96:99], v2 offset:0\n"
"ds_read_b128 v[100:103], v3 offset:0\n"
"ds_read_b128 v[104:107], v4 offset:0\n"
"ds_read_b128 v[108:111], v54 offset:0\n"
"ds_read_b128 v[112:115], v2 offset:256\n"
"ds_read_b128 v[116:119], v3 offset:256\n"
"ds_read_b128 v[120:123], v4 offset:256\n"
"ds_read_b128 v[124:127], v54 offset:256\n"
"ds_read_b128 v[128:131], v2 offset:512\n"
"ds_read_b128 v[132:135], v3 offset:512\n"
"ds_read_b128 v[136:139], v4 offset:512\n"
"ds_read_b128 v[140:143], v54 offset:512\n"
"ds_read_b128 v[144:147], v2 offset:768\n"
"ds_read_b128 v[148:151], v3 offset:768\n"
"ds_read_b128 v[152:155], v4 offset:768\n"
"ds_read_b128 v[156:159], v54 offset:768\n"
"ds_read_b128 v[160:163], v2 offset:1024\n"
"ds_read_b128 v[164:167], v3 offset:1024\n"
"ds_read_b128 v[168:171], v4 offset:1024\n"
"ds_read_b128 v[172:175], v54 offset:1024\n"
"ds_read_b128 v[176:179], v2 offset:1280\n"
"ds_read_b128 v[180:183], v3 offset:1280\n"
"ds_read_b128 v[184:187], v4 offset:1280\n"
"ds_read_b128 v[188:191], v54 offset:1280\n"
"s_waitcnt lgkmcnt(0)\n"
"s_barrier\n"
"v_add_f32 v96, v96, v100\n"
"v_add_f32 v104, v104, v108\n"
"v_add_f32 v28, v96, v104\n"
"v_add_f32 v97, v97, v101\n"
"v_add_f32 v105, v105, v109\n"
"v_add_f32 v29, v97, v105\n"
"v_add_f32 v98, v98, v102\n"
"v_add_f32 v106, v106, v110\n"
"v_add_f32 v30, v98, v106\n"
"v_add_f32 v99, v99, v103\n"
"v_add_f32 v107, v107, v111\n"
"v_add_f32 v31, v99, v107\n"
"v_add_f32 v112, v112, v116\n"
"v_add_f32 v120, v120, v124\n"
"v_add_f32 v32, v112, v120\n"
"v_add_f32 v113, v113, v117\n"
"v_add_f32 v121, v121, v125\n"
"v_add_f32 v33, v113, v121\n"
"v_add_f32 v114, v114, v118\n"
"v_add_f32 v122, v122, v126\n"
"v_add_f32 v34, v114, v122\n"
"v_add_f32 v115, v115, v119\n"
"v_add_f32 v123, v123, v127\n"
"v_add_f32 v35, v115, v123\n"
"v_add_f32 v128, v128, v132\n"
"v_add_f32 v136, v136, v140\n"
"v_add_f32 v36, v128, v136\n"
"v_add_f32 v129, v129, v133\n"
"v_add_f32 v137, v137, v141\n"
"v_add_f32 v37, v129, v137\n"
"v_add_f32 v130, v130, v134\n"
"v_add_f32 v138, v138, v142\n"
"v_add_f32 v38, v130, v138\n"
"v_add_f32 v131, v131, v135\n"
"v_add_f32 v139, v139, v143\n"
"v_add_f32 v39, v131, v139\n"
"v_add_f32 v144, v144, v148\n"
"v_add_f32 v152, v152, v156\n"
"v_add_f32 v40, v144, v152\n"
"v_add_f32 v145, v145, v149\n"
"v_add_f32 v153, v153, v157\n"
"v_add_f32 v41, v145, v153\n"
"v_add_f32 v146, v146, v150\n"
"v_add_f32 v154, v154, v158\n"
"v_add_f32 v42, v146, v154\n"
"v_add_f32 v147, v147, v151\n"
"v_add_f32 v155, v155, v159\n"
"v_add_f32 v43, v147, v155\n"
"v_add_f32 v160, v160, v164\n"
"v_add_f32 v168, v168, v172\n"
"v_add_f32 v44, v160, v168\n"
"v_add_f32 v176, v176, v180\n"
"v_add_f32 v184, v184, v188\n"
"v_add_f32 v45, v176, v184\n"
"v_mov_b32 v128, 0\n"
"v_mov_b32 v129, 0\n"
"v_mov_b32 v130, 0\n"
"v_mov_b32 v131, 0\n"
"v_mov_b32 v136, 0\n"
"v_mov_b32 v137, 0\n"
"v_mov_b32 v138, 0\n"
"v_mov_b32 v139, 0\n"
"v_mov_b32 v144, 0\n"
"v_mov_b32 v145, 0\n"
"v_mov_b32 v146, 0\n"
"v_mov_b32 v147, 0\n"
"v_mov_b32 v160, 0\n"
"v_mov_b32 v161, 0\n"
"v_mov_b32 v162, 0\n"
"v_mov_b32 v163, 0\n"
"s_mov_b32 s95, 0x80000000\n"
"s_mov_b32 s94, 0x80000000\n"
"s_cmp_eq_u32 s54, 0\n"
"s_cbranch_scc1 .LQ_1s\n"
"s_cmp_eq_u32 s54, 1\n"
"s_cbranch_scc1 .LQ_2s\n"
"s_cmp_eq_u32 s54, 2\n"
"s_cbranch_scc1 .LQ_3s\n"
"s_cmp_eq_u32 s54, 3\n"
"s_cbranch_scc1 .LQ_4s\n"
".p2align 6\n"
".LQ_top:\n"
"v_mfma_f32_16x16x32_bf16 v[112:115], a[128:131], v[48:51], 0\n"
"buffer_load_dwordx4 a[192:195], v5, s[40:43], s56 offen offset:0 sc1\n"
"v_mfma_f32_16x16x32_bf16 v[112:115], a[132:135], v[52:55], v[112:115]\n"
"ds_write_addtid_b32 v124 offset:4352\n"
"v_mfma_f32_16x16x32_bf16 v[112:115], a[136:139], v[56:59], v[112:115]\n"
"ds_write_addtid_b32 v125 offset:8992\n"
"v_mfma_f32_16x16x32_bf16 v[112:115], a[140:143], v[60:63], v[112:115]\n"
"ds_write_addtid_b32 v126 offset:13696\n"
"v_mfma_f32_16x16x32_bf16 v[112:115], a[144:147], v[64:67], v[112:115]\n"
"ds_write_addtid_b32 v127 offset:18336\n"
"buffer_load_dwordx4 a[196:199], v5, s[40:43], s56 offen offset:1024 sc1\n"
"v_mfma_f32_16x16x32_bf16 v[112:115], a[148:151], v[68:71], v[112:115]\n"
"v_mul_f32 v119, v129, v39\n"
"v_mov_b32 v255, v128\n"
"v_mfma_f32_16x16x32_bf16 v[112:115], a[152:155], v[72:75], v[112:115]\n"
"v_mul_f32 v117, v129, v37\n"
"v_mul_f32 v118, v129, v38\n"
"v_mfma_f32_16x16x32_bf16 v[112:115], a[156:159], v[76:79], v[112:115]\n"
"v_mul_f32 v123, v129, v43\n"
"v_mul_f32 v116, v129, v36\n"
"v_mfma_f32_16x16x32_bf16 v[124:127], a[160:163], v[48:51], 0\n"
"buffer_load_dwordx4 a[200:203], v6, s[40:43], s56 offen offset:0 sc1\n"
"v_mul_f32 v122, v129, v42\n"
"v_mfma_f32_16x16x32_bf16 v[124:127], a[164:167], v[52:55], v[124:127]\n"
"ds_write_addtid_b32 v112 offset:0\n"
"v_mul_f32 v121, v129, v41\n"
"v_mfma_f32_16x16x32_bf16 v[124:127], a[168:171], v[56:59], v[124:127]\n"
"s_waitcnt lgkmcnt(1)\n"
"ds_write_addtid_b32 v113 offset:4640\n"
"v_mul_f32 v120, v129, v40\n"
"v_mfma_f32_16x16x32_bf16 v[124:127], a[172:175], v[60:63], v[124:127]\n"
"s_barrier\n"
"v_mfma_f32_16x16x32_bf16 v[124:127], a[176:179], v[64:67], v[124:127]\n"
"ds_write_addtid_b32 v114 offset:9344\n"
"buffer_load_dwordx4 a[204:207], v6, s[40:43], s56 offen offset:1024 sc1\n"
"v_mfma_f32_16x16x32_bf16 v[124:127], a[180:183], v[68:71], v[124:127]\n"
"ds_write_addtid_b32 v115 offset:13984\n"
"ds_read2st64_b32 v[128:129], v12 offset0:0 offset1:1\n"
"v_mfma_f32_16x16x32_bf16 v[124:127], a[184:187], v[72:75], v[124:127]\n"
"ds_read2st64_b32 v[130:131], v12 offset0:2 offset1:3\n"
"ds_read2st64_b32 v[140:141], v11 offset0:4 offset1:5\n"
"v_mfma_f32_16x16x32_bf16 v[124:127], a[188:191], v[76:79], v[124:127]\n"
"ds_read2st64_b32 v[148:149], v11 offset0:6 offset1:7\n"
"v_mfma_f32_16x16x32_bf16 v[112:115], v[48:51], a[0:3], 0\n"
"s_add_u32 s95, s50, 0xffc00000\n"
"s_and_b32 s94, s95, 0xfbffffff\n"
"buffer_load_dwordx4 a[208:211], v7, s[40:43], s56 offen offset:0 sc1\n"
"v_mfma_f32_16x16x32_bf16 v[112:115], v[52:55], a[4:7], v[112:115]\n"
"ds_write_addtid_b32 v124 offset:256\n"
"v_mfma_f32_16x16x32_bf16 v[112:115], v[56:59], a[8:11], v[112:115]\n"
"ds_write_addtid_b32 v125 offset:4896\n"
"v_mfma_f32_16x16x32_bf16 v[112:115], v[60:63], a[12:15], v[112:115]\n"
"s_waitcnt lgkmcnt(4)\n"
"ds_write_addtid_b32 v126 offset:9600\n"
"v_mfma_f32_16x16x32_bf16 v[132:135], v[128:131], v[156:159], v[16:19]\n"
"s_cmp_eq_u32 s95, s60\n"
"s_cbranch_scc1 .LQ_fs\n"
".LQ_fr:\n"
"v_mfma_f32_16x16x32_bf16 v[112:115], v[64:67], a[16:19], v[112:115]\n"
"ds_write_addtid_b32 v127 offset:14240\n"
"buffer_load_dwordx4 a[212:215], v7, s[40:43], s56 offen offset:1024 sc1\n"
"v_mfma_f32_16x16x32_bf16 v[112:115], v[68:71], a[20:23], v[112:115]\n"
"v_max_f32 v254, v152, v154\n"
"v_max_f32 v253, v153, v155\n"
"v_mfma_f32_16x16x32_bf16 v[112:115], v[72:75], a[24:27], v[112:115]\n"
"v_max3_f32 v254, v132, v134, v254\n"
"v_max3_f32 v253, v133, v135, v253\n"
"v_mfma_f32_16x16x32_bf16 v[112:115], v[76:79], a[28:31], v[112:115]\n"
"v_max_f32 v252, v254, v253\n"
"v_mov_b32 v251, v252\n"
"v_mfma_f32_16x16x32_bf16 v[124:127], v[48:51], a[32:35], 0\n"
"buffer_load_dwordx4 a[216:219], v8, s[40:43], s56 offen offset:0 sc1\n"
"v_mfma_f32_16x16x32_bf16 v[124:127], v[52:55], a[36:39], v[124:127]\n"
"ds_write_addtid_b32 v112 offset:512\n"
"v_mfma_f32_16x16x32_bf16 v[124:127], v[56:59], a[40:43], v[124:127]\n"
"ds_write_addtid_b32 v113 offset:5152\n"
"v_mfma_f32_16x16x32_bf16 v[124:127], v[60:63], a[44:47], v[124:127]\n"
"ds_write_addtid_b32 v114 offset:9856\n"
"v_mfma_f32_16x16x32_bf16 v[124:127], v[64:67], a[48:51], v[124:127]\n"
"ds_write_addtid_b32 v115 offset:14496\n"
"buffer_load_dwordx4 a[220:223], v8, s[40:43], s56 offen offset:1024 sc1\n"
"v_mfma_f32_16x16x32_bf16 v[124:127], v[68:71], a[52:55], v[124:127]\n"
"v_permlane32_swap_b32 v251, v252\n"
"v_mfma_f32_16x16x32_bf16 v[124:127], v[72:75], a[56:59], v[124:127]\n"
"v_max_f32 v252, v251, v252\n"
"v_mov_b32 v251, v252\n"
"v_mfma_f32_16x16x32_bf16 v[124:127], v[76:79], a[60:63], v[124:127]\n"
"v_permlane16_swap_b32 v251, v252\n"
"v_mfma_f32_16x16x32_bf16 v[116:119], a[64:67], v[48:51], v[116:119]\n"
"v_max_f32 v252, v251, v252\n"
"v_mul_f32 v252, v252, v250\n"
"v_mfma_f32_16x16x32_bf16 v[116:119], a[68:71], v[52:55], v[116:119]\n"
"ds_write_addtid_b32 v124 offset:768\n"
"v_fma_f32 v254, v254, v250, -v252\n"
"v_mfma_f32_16x16x32_bf16 v[116:119], a[72:75], v[56:59], v[116:119]\n"
"ds_write_addtid_b32 v125 offset:5408\n"
"v_fma_f32 v253, v253, v250, -v252\n"
"v_mfma_f32_16x16x32_bf16 v[116:119], a[76:79], v[60:63], v[116:119]\n"
"ds_write_addtid_b32 v126 offset:10112\n"
"v_mfma_f32_16x16x32_bf16 v[116:119], a[80:83], v[64:67], v[116:119]\n"
"ds_write_addtid_b32 v127 offset:14752\n"
"v_mfma_f32_16x16x32_bf16 v[116:119], a[84:87], v[68:71], v[116:119]\n"
"v_exp_f32 v254, v254\n"
"v_mfma_f32_16x16x32_bf16 v[116:119], a[88:91], v[72:75], v[116:119]\n"
"v_exp_f32 v253, v253\n"
"v_mfma_f32_16x16x32_bf16 v[116:119], a[92:95], v[76:79], v[116:119]\n"
"v_add_f32 v252, v254, v253\n"
"v_mov_b32 v251, v252\n"
"v_mfma_f32_16x16x32_bf16 v[120:123], a[96:99], v[48:51], v[120:123]\n"
"v_permlane32_swap_b32 v251, v252\n"
"v_mfma_f32_16x16x32_bf16 v[120:123], a[100:103], v[52:55], v[120:123]\n"
"v_add_f32 v252, v251, v252\n"
"v_mov_b32 v251, v252\n"
"v_mfma_f32_16x16x32_bf16 v[120:123], a[104:107], v[56:59], v[120:123]\n"
"v_permlane16_swap_b32 v251, v252\n"
"v_mfma_f32_16x16x32_bf16 v[120:123], a[108:111], v[60:63], v[120:123]\n"
"s_waitcnt vmcnt(8)\n"
"buffer_load_dwordx4 v[128:131], v9, s[40:43], s50 offen sc1\n"
"v_cmp_ne_u64 vcc, s[48:49], v[162:163]\n"
"v_mfma_f32_16x16x32_bf16 v[120:123], a[112:115], v[64:67], v[120:123]\n"
"v_add_f32 v252, v251, v252\n"
"v_mfma_f32_16x16x32_bf16 v[120:123], a[116:119], v[68:71], v[120:123]\n"
"s_cbranch_vccnz .LQ_4s\n"
".LQ_4r:\n"
"v_mfma_f32_16x16x32_bf16 v[120:123], a[120:123], v[72:75], v[120:123]\n"
"s_add_u32 s50, s50, 0x100000\n"
"s_and_b32 s56, s50, 0xfbffffff\n"
"v_mfma_f32_16x16x32_bf16 v[120:123], a[124:127], v[76:79], v[120:123]\n"
"v_rcp_f32 v252, v252\n"
"v_mfma_f32_16x16x32_bf16 v[48:51], v[80:83], a[64:67], 0\n"
"buffer_load_dwordx4 a[224:227], v5, s[40:43], s56 offen offset:0 sc1\n"
"v_mul_f32 v254, v254, v252\n"
"v_mfma_f32_16x16x32_bf16 v[48:51], v[84:87], a[68:71], v[48:51]\n"
"v_mul_f32 v253, v253, v252\n"
"v_cvt_pk_bf16_f32 v251, v254, v253\n"
"v_mfma_f32_16x16x32_bf16 v[48:51], v[88:91], a[72:75], v[48:51]\n"
"s_waitcnt lgkmcnt(12)\n"
"v_and_b32 v52, v251, v24\n"
"v_and_b32 v53, v251, v25\n"
"v_mfma_f32_16x16x32_bf16 v[48:51], v[92:95], a[76:79], v[48:51]\n"
"v_and_b32 v54, v251, v26\n"
"v_and_b32 v55, v251, v27\n"
"v_mfma_f32_16x16x32_bf16 v[48:51], v[96:99], a[80:83], v[48:51]\n"
"buffer_load_dwordx4 a[228:231], v5, s[40:43], s56 offen offset:1024 sc1\n"
"v_mfma_f32_16x16x32_bf16 v[56:59], v[140:143], v[52:55], 0\n"
"v_mul_f32_dpp v115, v137, v44 row_newbcast:3 row_mask:0xf bank_mask:0xf\n"
"v_mov_b32 v254, v136\n"
"v_mfma_f32_16x16x32_bf16 v[60:63], v[148:151], v[52:55], 0\n"
"v_mul_f32_dpp v113, v137, v44 row_newbcast:1 row_mask:0xf bank_mask:0xf\n"
"v_mul_f32_dpp v114, v137, v44 row_newbcast:2 row_mask:0xf bank_mask:0xf\n"
"v_mfma_f32_16x16x32_bf16 v[48:51], v[100:103], a[84:87], v[48:51]\n"
"v_cvt_pk_bf16_f32 v52, v56, v57\n"
"v_cvt_pk_bf16_f32 v53, v58, v59\n"
"v_mfma_f32_16x16x32_bf16 v[48:51], v[104:107], a[88:91], v[48:51]\n"
"v_cvt_pk_bf16_f32 v54, v60, v61\n"
"v_cvt_pk_bf16_f32 v55, v62, v63\n"
"v_mfma_f32_16x16x32_bf16 v[48:51], v[108:111], a[92:95], v[48:51]\n"
"buffer_store_dwordx4 v[52:55], v13, s[40:43], s94 offen sc1\n"
"v_mul_f32_dpp v125, v137, v45 row_newbcast:1 row_mask:0xf bank_mask:0xf\n"
"v_mul_f32_dpp v126, v137, v45 row_newbcast:2 row_mask:0xf bank_mask:0xf\n"
"v_mul_f32_dpp v127, v137, v45 row_newbcast:3 row_mask:0xf bank_mask:0xf\n"
"v_mul_f32_dpp v112, v137, v44 row_newbcast:0 row_mask:0xf bank_mask:0xf\n"
"v_mfma_f32_16x16x32_bf16 v[52:55], v[80:83], a[96:99], 0\n"
"s_waitcnt lgkmcnt(0)\n"
"buffer_load_dwordx4 a[232:235], v6, s[40:43], s56 offen offset:0 sc1\n"
"v_mul_f32_dpp v124, v137, v45 row_newbcast:0 row_mask:0xf bank_mask:0xf\n"
"v_mfma_f32_16x16x32_bf16 v[52:55], v[84:87], a[100:103], v[52:55]\n"
"s_barrier\n"
"v_mfma_f32_16x16x32_bf16 v[52:55], v[88:91], a[104:107], v[52:55]\n"
"ds_write_addtid_b32 v48 offset:1024\n"
"ds_read_b128 v[56:59], v2 offset:1024\n"
"v_mfma_f32_16x16x32_bf16 v[52:55], v[92:95], a[108:111], v[52:55]\n"
"ds_write_addtid_b32 v49 offset:5664\n"
"ds_read_b128 v[60:63], v3 offset:2048\n"
"v_mfma_f32_16x16x32_bf16 v[52:55], v[96:99], a[112:115], v[52:55]\n"
"ds_write_addtid_b32 v50 offset:10368\n"
"buffer_load_dwordx4 a[236:239], v6, s[40:43], s56 offen offset:1024 sc1\n"
"v_mfma_f32_16x16x32_bf16 v[52:55], v[100:103], a[116:119], v[52:55]\n"
"ds_write_addtid_b32 v51 offset:15008\n"
"ds_read_b128 v[64:67], v4 offset:3072\n"
"v_mfma_f32_16x16x32_bf16 v[52:55], v[104:107], a[120:123], v[52:55]\n"
"ds_read_b128 v[68:71], v2 offset:1280\n"
"ds_read_b128 v[48:51], v3 offset:2304\n"
"v_mfma_f32_16x16x32_bf16 v[52:55], v[108:111], a[124:127], v[52:55]\n"
"ds_read_b128 v[72:75], v4 offset:3328\n"
"v_mfma_f32_16x16x32_bf16 v[76:79], v[80:83], a[0:3], 0\n"
"buffer_load_dwordx4 a[240:243], v7, s[40:43], s56 offen offset:0 sc1\n"
"v_mfma_f32_16x16x32_bf16 v[76:79], v[84:87], a[4:7], v[76:79]\n"
"ds_write_addtid_b32 v52 offset:1280\n"
"v_mfma_f32_16x16x32_bf16 v[76:79], v[88:91], a[8:11], v[76:79]\n"
"ds_write_addtid_b32 v53 offset:5920\n"
"v_mfma_f32_16x16x32_bf16 v[76:79], v[92:95], a[12:15], v[76:79]\n"
"s_waitcnt lgkmcnt(5)\n"
"ds_write_addtid_b32 v54 offset:10624\n"
"v_add_f32 v56, v56, v60\n"
"v_mfma_f32_16x16x32_bf16 v[76:79], v[96:99], a[16:19], v[76:79]\n"
"ds_write_addtid_b32 v55 offset:15264\n"
"buffer_load_dwordx4 a[244:247], v7, s[40:43], s56 offen offset:1024 sc1\n"
"v_mfma_f32_16x16x32_bf16 v[76:79], v[100:103], a[20:23], v[76:79]\n"
"v_add_f32 v57, v57, v61\n"
"v_add_f32 v58, v58, v62\n"
"v_mfma_f32_16x16x32_bf16 v[76:79], v[104:107], a[24:27], v[76:79]\n"
"v_add_f32 v59, v59, v63\n"
"v_add_f32 v56, v56, v64\n"
"v_mfma_f32_16x16x32_bf16 v[76:79], v[108:111], a[28:31], v[76:79]\n"
"v_add_f32 v57, v57, v65\n"
"v_add_f32 v58, v58, v66\n"
"v_mfma_f32_16x16x32_bf16 v[52:55], v[80:83], a[32:35], 0\n"
"buffer_load_dwordx4 a[248:251], v8, s[40:43], s56 offen offset:0 sc1\n"
"v_add_f32 v59, v59, v67\n"
"v_mfma_f32_16x16x32_bf16 v[52:55], v[84:87], a[36:39], v[52:55]\n"
"ds_write_addtid_b32 v76 offset:1536\n"
"v_add_f32 v56, v56, v116\n"
"v_mfma_f32_16x16x32_bf16 v[52:55], v[88:91], a[40:43], v[52:55]\n"
"ds_write_addtid_b32 v77 offset:6176\n"
"v_add_f32 v57, v57, v117\n"
"v_mfma_f32_16x16x32_bf16 v[52:55], v[92:95], a[44:47], v[52:55]\n"
"ds_write_addtid_b32 v78 offset:10880\n"
"v_add_f32 v58, v58, v118\n"
"v_mfma_f32_16x16x32_bf16 v[52:55], v[96:99], a[48:51], v[52:55]\n"
"ds_write_addtid_b32 v79 offset:15520\n"
"buffer_load_dwordx4 a[252:255], v8, s[40:43], s56 offen offset:1024 sc1\n"
"v_mfma_f32_16x16x32_bf16 v[52:55], v[100:103], a[52:55], v[52:55]\n"
"v_add_f32 v59, v59, v119\n"
"v_mul_f32 v56, v255, v56\n"
"v_mfma_f32_16x16x32_bf16 v[52:55], v[104:107], a[56:59], v[52:55]\n"
"v_mul_f32 v57, v255, v57\n"
"v_mul_f32 v58, v255, v58\n"
"v_mfma_f32_16x16x32_bf16 v[52:55], v[108:111], a[60:63], v[52:55]\n"
"v_mul_f32 v59, v255, v59\n"
"v_cvt_pk_bf16_f32 v60, v56, v57\n"
"v_mfma_f32_16x16x32_bf16 v[112:115], v[80:83], a[128:131], v[112:115]\n"
"v_cvt_pk_bf16_f32 v61, v58, v59\n"
"v_mfma_f32_16x16x32_bf16 v[112:115], v[84:87], a[132:135], v[112:115]\n"
"ds_write_addtid_b32 v52 offset:1792\n"
"ds_write_addtid_b32 v60 offset:18592\n"
"v_mfma_f32_16x16x32_bf16 v[112:115], v[88:91], a[136:139], v[112:115]\n"
"ds_write_addtid_b32 v53 offset:6432\n"
"ds_write_addtid_b32 v61 offset:18848\n"
"v_mfma_f32_16x16x32_bf16 v[112:115], v[92:95], a[140:143], v[112:115]\n"
"s_waitcnt lgkmcnt(12)\n"
"ds_write_addtid_b32 v54 offset:11136\n"
"v_add_f32 v68, v68, v48\n"
"v_mfma_f32_16x16x32_bf16 v[112:115], v[96:99], a[144:147], v[112:115]\n"
"ds_write_addtid_b32 v55 offset:15776\n"
"v_add_f32 v69, v69, v49\n"
"v_mfma_f32_16x16x32_bf16 v[112:115], v[100:103], a[148:151], v[112:115]\n"
"v_add_f32 v70, v70, v50\n"
"v_add_f32 v71, v71, v51\n"
"v_mfma_f32_16x16x32_bf16 v[112:115], v[104:107], a[152:155], v[112:115]\n"
"v_add_f32 v68, v68, v72\n"
"v_add_f32 v69, v69, v73\n"
"v_mfma_f32_16x16x32_bf16 v[112:115], v[108:111], a[156:159], v[112:115]\n"
"v_add_f32 v70, v70, v74\n"
"v_add_f32 v71, v71, v75\n"
"v_mfma_f32_16x16x32_bf16 v[124:127], v[80:83], a[160:163], v[124:127]\n"
"v_add_f32 v68, v68, v120\n"
"v_add_f32 v69, v69, v121\n"
"v_mfma_f32_16x16x32_bf16 v[124:127], v[84:87], a[164:167], v[124:127]\n"
"v_add_f32 v70, v70, v122\n"
"v_add_f32 v71, v71, v123\n"
"v_mfma_f32_16x16x32_bf16 v[124:127], v[88:91], a[168:171], v[124:127]\n"
"v_mul_f32 v68, v255, v68\n"
"v_mul_f32 v69, v255, v69\n"
"v_mfma_f32_16x16x32_bf16 v[124:127], v[92:95], a[172:175], v[124:127]\n"
"s_waitcnt vmcnt(9)\n"
"buffer_load_dwordx4 v[136:139], v10, s[40:43], s50 offen sc1\n"
"v_cmp_ne_u64 vcc, s[48:49], v[130:131]\n"
"v_mfma_f32_16x16x32_bf16 v[124:127], v[96:99], a[176:179], v[124:127]\n"
"v_mul_f32 v70, v255, v70\n"
"v_mul_f32 v71, v255, v71\n"
"v_mfma_f32_16x16x32_bf16 v[124:127], v[100:103], a[180:183], v[124:127]\n"
"s_cbranch_vccnz .LQ_1s\n"
".LQ_1r:\n"
"v_cvt_pk_bf16_f32 v62, v68, v69\n"
"v_mfma_f32_16x16x32_bf16 v[124:127], v[104:107], a[184:187], v[124:127]\n"
"s_add_u32 s50, s50, 0x100000\n"
"s_and_b32 s56, s50, 0xfbffffff\n"
"v_cvt_pk_bf16_f32 v63, v70, v71\n"
"v_mfma_f32_16x16x32_bf16 v[124:127], v[108:111], a[188:191], v[124:127]\n"
"ds_write_addtid_b32 v62 offset:19104\n"
"ds_write_addtid_b32 v63 offset:19360\n"
"v_mfma_f32_16x16x32_bf16 v[64:67], a[192:195], a[64:67], 0\n"
"buffer_load_dwordx4 v[48:51], v5, s[40:43], s56 offen offset:0 sc1\n"
"v_mfma_f32_16x16x32_bf16 v[64:67], a[196:199], a[68:71], v[64:67]\n"
"v_mfma_f32_16x16x32_bf16 v[64:67], a[200:203], a[72:75], v[64:67]\n"
"v_mfma_f32_16x16x32_bf16 v[64:67], a[204:207], a[76:79], v[64:67]\n"
"v_mul_f32 v250, 0x3e8293ee, v144\n"
"v_mfma_f32_16x16x32_bf16 v[64:67], a[208:211], a[80:83], v[64:67]\n"
"buffer_load_dwordx4 v[52:55], v5, s[40:43], s56 offen offset:1024 sc1\n"
"v_mul_f32 v119, v145, v31\n"
"v_mfma_f32_16x16x32_bf16 v[64:67], a[212:215], a[84:87], v[64:67]\n"
"v_mul_f32 v117, v145, v29\n"
"v_mul_f32 v118, v145, v30\n"
"v_mfma_f32_16x16x32_bf16 v[64:67], a[216:219], a[88:91], v[64:67]\n"
"v_mul_f32 v123, v145, v35\n"
"v_mul_f32 v116, v145, v28\n"
"v_mfma_f32_16x16x32_bf16 v[64:67], a[220:223], a[92:95], v[64:67]\n"
"s_waitcnt vmcnt(9)\n"
"buffer_store_dwordx2 v[0:1], v14, s[40:43], s95 offen offset:0 sc1\n"
"v_mul_f32 v121, v145, v33\n"
"v_mul_f32 v122, v145, v34\n"
"v_mfma_f32_16x16x32_bf16 v[72:75], a[192:195], a[96:99], 0\n"
"s_waitcnt lgkmcnt(0)\n"
"buffer_load_dwordx4 v[56:59], v6, s[40:43], s56 offen offset:0 sc1\n"
"v_mul_f32 v120, v145, v32\n"
"v_mfma_f32_16x16x32_bf16 v[72:75], a[196:199], a[100:103], v[72:75]\n"
"s_barrier\n"
"v_mfma_f32_16x16x32_bf16 v[72:75], a[200:203], a[104:107], v[72:75]\n"
"ds_write_addtid_b32 v64 offset:2048\n"
"ds_read_b128 v[80:83], v2 offset:2560\n"
"v_mfma_f32_16x16x32_bf16 v[72:75], a[204:207], a[108:111], v[72:75]\n"
"ds_write_addtid_b32 v65 offset:6688\n"
"ds_read_b128 v[76:79], v3 offset:3584\n"
"v_mfma_f32_16x16x32_bf16 v[72:75], a[208:211], a[112:115], v[72:75]\n"
"ds_write_addtid_b32 v66 offset:11392\n"
"buffer_load_dwordx4 v[60:63], v6, s[40:43], s56 offen offset:1024 sc1\n"
"v_mfma_f32_16x16x32_bf16 v[72:75], a[212:215], a[116:119], v[72:75]\n"
"ds_write_addtid_b32 v67 offset:16032\n"
"ds_read_b128 v[84:87], v4 offset:0\n"
"v_mfma_f32_16x16x32_bf16 v[72:75], a[216:219], a[120:123], v[72:75]\n"
"ds_read_b128 v[88:91], v2 offset:2816\n"
"ds_read_b128 v[92:95], v3 offset:3840\n"
"v_mfma_f32_16x16x32_bf16 v[72:75], a[220:223], a[124:127], v[72:75]\n"
"ds_read_b128 v[96:99], v4 offset:256\n"
"v_mfma_f32_16x16x32_bf16 v[100:103], a[128:131], a[192:195], 0\n"
"buffer_load_dwordx4 v[64:67], v7, s[40:43], s56 offen offset:0 sc1\n"
"v_mfma_f32_16x16x32_bf16 v[100:103], a[132:135], a[196:199], v[100:103]\n"
"ds_write_addtid_b32 v72 offset:2304\n"
"v_mfma_f32_16x16x32_bf16 v[100:103], a[136:139], a[200:203], v[100:103]\n"
"ds_write_addtid_b32 v73 offset:6944\n"
"v_mfma_f32_16x16x32_bf16 v[100:103], a[140:143], a[204:207], v[100:103]\n"
"s_waitcnt lgkmcnt(5)\n"
"ds_write_addtid_b32 v74 offset:11648\n"
"v_add_f32 v80, v80, v76\n"
"v_mfma_f32_16x16x32_bf16 v[100:103], a[144:147], a[208:211], v[100:103]\n"
"ds_write_addtid_b32 v75 offset:16288\n"
"buffer_load_dwordx4 v[68:71], v7, s[40:43], s56 offen offset:1024 sc1\n"
"v_mfma_f32_16x16x32_bf16 v[100:103], a[148:151], a[212:215], v[100:103]\n"
"v_add_f32 v81, v81, v77\n"
"v_add_f32 v82, v82, v78\n"
"v_mfma_f32_16x16x32_bf16 v[100:103], a[152:155], a[216:219], v[100:103]\n"
"v_add_f32 v83, v83, v79\n"
"v_add_f32 v80, v80, v84\n"
"v_mfma_f32_16x16x32_bf16 v[100:103], a[156:159], a[220:223], v[100:103]\n"
"v_add_f32 v81, v81, v85\n"
"v_add_f32 v82, v82, v86\n"
"v_mfma_f32_16x16x32_bf16 v[104:107], a[160:163], a[192:195], 0\n"
"buffer_load_dwordx4 v[72:75], v8, s[40:43], s56 offen offset:0 sc1\n"
"v_add_f32 v83, v83, v87\n"
"v_mfma_f32_16x16x32_bf16 v[104:107], a[164:167], a[196:199], v[104:107]\n"
"ds_write_addtid_b32 v100 offset:2560\n"
"v_add_f32 v80, v80, v112\n"
"v_mfma_f32_16x16x32_bf16 v[104:107], a[168:171], a[200:203], v[104:107]\n"
"ds_write_addtid_b32 v101 offset:7200\n"
"v_add_f32 v81, v81, v113\n"
"v_mfma_f32_16x16x32_bf16 v[104:107], a[172:175], a[204:207], v[104:107]\n"
"ds_write_addtid_b32 v102 offset:11904\n"
"v_add_f32 v82, v82, v114\n"
"v_mfma_f32_16x16x32_bf16 v[104:107], a[176:179], a[208:211], v[104:107]\n"
"ds_write_addtid_b32 v103 offset:16544\n"
"buffer_load_dwordx4 v[76:79], v8, s[40:43], s56 offen offset:1024 sc1\n"
"v_mfma_f32_16x16x32_bf16 v[104:107], a[180:183], a[212:215], v[104:107]\n"
"v_add_f32 v83, v83, v115\n"
"v_mul_f32_dpp v80, v254, v80 row_newbcast:0 row_mask:0xf bank_mask:0xf\n"
"v_mfma_f32_16x16x32_bf16 v[104:107], a[184:187], a[216:219], v[104:107]\n"
"v_mul_f32_dpp v81, v254, v81 row_newbcast:1 row_mask:0xf bank_mask:0xf\n"
"v_mul_f32_dpp v82, v254, v82 row_newbcast:2 row_mask:0xf bank_mask:0xf\n"
"v_mfma_f32_16x16x32_bf16 v[104:107], a[188:191], a[220:223], v[104:107]\n"
"v_mul_f32_dpp v83, v254, v83 row_newbcast:3 row_mask:0xf bank_mask:0xf\n"
"v_cvt_pk_bf16_f32 v142, v80, v81\n"
"v_mfma_f32_16x16x32_bf16 v[116:119], a[0:3], a[192:195], v[116:119]\n"
"v_cvt_pk_bf16_f32 v143, v82, v83\n"
"v_mfma_f32_16x16x32_bf16 v[116:119], a[4:7], a[196:199], v[116:119]\n"
"ds_write_addtid_b32 v104 offset:2816\n"
"ds_write_addtid_b32 v142 offset:19616\n"
"v_mfma_f32_16x16x32_bf16 v[116:119], a[8:11], a[200:203], v[116:119]\n"
"ds_write_addtid_b32 v105 offset:7456\n"
"ds_write_addtid_b32 v143 offset:19872\n"
"v_mfma_f32_16x16x32_bf16 v[116:119], a[12:15], a[204:207], v[116:119]\n"
"s_waitcnt lgkmcnt(12)\n"
"ds_write_addtid_b32 v106 offset:12160\n"
"v_add_f32 v88, v88, v92\n"
"v_mfma_f32_16x16x32_bf16 v[116:119], a[16:19], a[208:211], v[116:119]\n"
"ds_write_addtid_b32 v107 offset:16800\n"
"v_add_f32 v89, v89, v93\n"
"v_mfma_f32_16x16x32_bf16 v[116:119], a[20:23], a[212:215], v[116:119]\n"
"v_add_f32 v90, v90, v94\n"
"v_add_f32 v91, v91, v95\n"
"v_mfma_f32_16x16x32_bf16 v[116:119], a[24:27], a[216:219], v[116:119]\n"
"v_add_f32 v88, v88, v96\n"
"v_add_f32 v89, v89, v97\n"
"v_mfma_f32_16x16x32_bf16 v[116:119], a[28:31], a[220:223], v[116:119]\n"
"v_add_f32 v90, v90, v98\n"
"v_add_f32 v91, v91, v99\n"
"v_mfma_f32_16x16x32_bf16 v[120:123], a[32:35], a[192:195], v[120:123]\n"
"v_add_f32 v88, v88, v124\n"
"v_add_f32 v89, v89, v125\n"
"v_mfma_f32_16x16x32_bf16 v[120:123], a[36:39], a[196:199], v[120:123]\n"
"v_add_f32 v90, v90, v126\n"
"v_add_f32 v91, v91, v127\n"
"v_mfma_f32_16x16x32_bf16 v[120:123], a[40:43], a[200:203], v[120:123]\n"
"v_mul_f32_dpp v88, v254, v88 row_newbcast:0 row_mask:0xf bank_mask:0xf\n"
"v_mul_f32_dpp v89, v254, v89 row_newbcast:1 row_mask:0xf bank_mask:0xf\n"
"v_mfma_f32_16x16x32_bf16 v[120:123], a[44:47], a[204:207], v[120:123]\n"
"s_waitcnt vmcnt(9)\n"
"buffer_load_dwordx4 v[144:147], v9, s[40:43], s50 offen sc1\n"
"v_cmp_ne_u64 vcc, s[48:49], v[138:139]\n"
"v_mfma_f32_16x16x32_bf16 v[120:123], a[48:51], a[208:211], v[120:123]\n"
"v_mul_f32_dpp v90, v254, v90 row_newbcast:2 row_mask:0xf bank_mask:0xf\n"
"v_mul_f32_dpp v91, v254, v91 row_newbcast:3 row_mask:0xf bank_mask:0xf\n"
"v_mfma_f32_16x16x32_bf16 v[120:123], a[52:55], a[212:215], v[120:123]\n"
"s_cbranch_vccnz .LQ_2s\n"
".LQ_2r:\n"
"v_cvt_pk_bf16_f32 v150, v88, v89\n"
"v_mfma_f32_16x16x32_bf16 v[120:123], a[56:59], a[216:219], v[120:123]\n"
"s_add_u32 s50, s50, 0x100000\n"
"s_and_b32 s56, s50, 0xfbffffff\n"
"v_cvt_pk_bf16_f32 v151, v90, v91\n"
"v_mfma_f32_16x16x32_bf16 v[120:123], a[60:63], a[220:223], v[120:123]\n"
"ds_write_addtid_b32 v150 offset:20128\n"
"ds_write_addtid_b32 v151 offset:20384\n"
"v_mfma_f32_16x16x32_bf16 v[96:99], a[224:227], a[64:67], 0\n"
"buffer_load_dwordx4 v[80:83], v5, s[40:43], s56 offen offset:0 sc1\n"
"v_mfma_f32_16x16x32_bf16 v[96:99], a[228:231], a[68:71], v[96:99]\n"
"v_mfma_f32_16x16x32_bf16 v[96:99], a[232:235], a[72:75], v[96:99]\n"
"v_mfma_f32_16x16x32_bf16 v[96:99], a[236:239], a[76:79], v[96:99]\n"
"v_mfma_f32_16x16x32_bf16 v[96:99], a[240:243], a[80:83], v[96:99]\n"
"buffer_load_dwordx4 v[84:87], v5, s[40:43], s56 offen offset:1024 sc1\n"
"v_mfma_f32_16x16x32_bf16 v[96:99], a[244:247], a[84:87], v[96:99]\n"
"v_mfma_f32_16x16x32_bf16 v[96:99], a[248:251], a[88:91], v[96:99]\n"
"v_mfma_f32_16x16x32_bf16 v[96:99], a[252:255], a[92:95], v[96:99]\n"
"v_mfma_f32_16x16x32_bf16 v[104:107], a[224:227], a[96:99], 0\n"
"buffer_load_dwordx4 v[88:91], v6, s[40:43], s56 offen offset:0 sc1\n"
"v_mfma_f32_16x16x32_bf16 v[104:107], a[228:231], a[100:103], v[104:107]\n"
"ds_write_addtid_b32 v96 offset:3072\n"
"v_mfma_f32_16x16x32_bf16 v[104:107], a[232:235], a[104:107], v[104:107]\n"
"s_waitcnt lgkmcnt(1)\n"
"ds_write_addtid_b32 v97 offset:7712\n"
"v_mfma_f32_16x16x32_bf16 v[104:107], a[236:239], a[108:111], v[104:107]\n"
"s_barrier\n"
"v_mfma_f32_16x16x32_bf16 v[104:107], a[240:243], a[112:115], v[104:107]\n"
"ds_write_addtid_b32 v98 offset:12416\n"
"buffer_load_dwordx4 v[92:95], v6, s[40:43], s56 offen offset:1024 sc1\n"
"v_mfma_f32_16x16x32_bf16 v[104:107], a[244:247], a[116:119], v[104:107]\n"
"ds_write_addtid_b32 v99 offset:17056\n"
"ds_read_b128 v[112:115], v2 offset:4096\n"
"v_mfma_f32_16x16x32_bf16 v[104:107], a[248:251], a[120:123], v[104:107]\n"
"ds_read_b128 v[108:111], v3 offset:512\n"
"ds_read_b128 v[124:127], v4 offset:1536\n"
"v_mfma_f32_16x16x32_bf16 v[104:107], a[252:255], a[124:127], v[104:107]\n"
"ds_read_b128 v[132:135], v2 offset:4352\n"
"ds_read_b128 v[152:155], v3 offset:768\n"
"v_mfma_f32_16x16x32_bf16 v[156:159], a[128:131], a[224:227], 0\n"
"buffer_load_dwordx4 v[96:99], v7, s[40:43], s56 offen offset:0 sc1\n"
"ds_read_b128 v[160:163], v4 offset:1792\n"
"v_mfma_f32_16x16x32_bf16 v[156:159], a[132:135], a[228:231], v[156:159]\n"
"ds_write_addtid_b32 v104 offset:3328\n"
"v_mfma_f32_16x16x32_bf16 v[156:159], a[136:139], a[232:235], v[156:159]\n"
"ds_write_addtid_b32 v105 offset:7968\n"
"v_mfma_f32_16x16x32_bf16 v[156:159], a[140:143], a[236:239], v[156:159]\n"
"ds_write_addtid_b32 v106 offset:12672\n"
"v_mfma_f32_16x16x32_bf16 v[156:159], a[144:147], a[240:243], v[156:159]\n"
"s_waitcnt lgkmcnt(6)\n"
"ds_write_addtid_b32 v107 offset:17312\n"
"buffer_load_dwordx4 v[100:103], v7, s[40:43], s56 offen offset:1024 sc1\n"
"v_mfma_f32_16x16x32_bf16 v[156:159], a[148:151], a[244:247], v[156:159]\n"
"v_add_f32 v112, v112, v108\n"
"v_add_f32 v113, v113, v109\n"
"v_mfma_f32_16x16x32_bf16 v[156:159], a[152:155], a[248:251], v[156:159]\n"
"v_add_f32 v114, v114, v110\n"
"v_add_f32 v115, v115, v111\n"
"v_mfma_f32_16x16x32_bf16 v[156:159], a[156:159], a[252:255], v[156:159]\n"
"v_add_f32 v112, v112, v124\n"
"v_add_f32 v113, v113, v125\n"
"v_mfma_f32_16x16x32_bf16 v[164:167], a[160:163], a[224:227], 0\n"
"buffer_load_dwordx4 v[104:107], v8, s[40:43], s56 offen offset:0 sc1\n"
"v_add_f32 v114, v114, v126\n"
"v_mfma_f32_16x16x32_bf16 v[164:167], a[164:167], a[228:231], v[164:167]\n"
"ds_write_addtid_b32 v156 offset:3584\n"
"v_add_f32 v115, v115, v127\n"
"v_mfma_f32_16x16x32_bf16 v[164:167], a[168:171], a[232:235], v[164:167]\n"
"ds_write_addtid_b32 v157 offset:8224\n"
"v_add_f32 v112, v112, v116\n"
"v_mfma_f32_16x16x32_bf16 v[164:167], a[172:175], a[236:239], v[164:167]\n"
"ds_write_addtid_b32 v158 offset:12928\n"
"v_add_f32 v113, v113, v117\n"
"v_mfma_f32_16x16x32_bf16 v[164:167], a[176:179], a[240:243], v[164:167]\n"
"ds_write_addtid_b32 v159 offset:17568\n"
"buffer_load_dwordx4 v[108:111], v8, s[40:43], s56 offen offset:1024 sc1\n"
"v_mfma_f32_16x16x32_bf16 v[164:167], a[180:183], a[244:247], v[164:167]\n"
"v_add_f32 v114, v114, v118\n"
"v_add_f32 v115, v115, v119\n"
"v_mfma_f32_16x16x32_bf16 v[164:167], a[184:187], a[248:251], v[164:167]\n"
"v_cvt_pk_bf16_f32 v156, v112, v113\n"
"v_cvt_pk_bf16_f32 v157, v114, v115\n"
"v_mfma_f32_16x16x32_bf16 v[164:167], a[188:191], a[252:255], v[164:167]\n"
"ds_read2st64_b32 v[116:117], v11 offset0:0 offset1:1\n"
"ds_read2st64_b32 v[118:119], v11 offset0:2 offset1:3\n"
"v_mfma_f32_16x16x32_bf16 v[112:115], a[224:227], a[0:3], 0\n"
"s_waitcnt lgkmcnt(10)\n"
"v_add_f32 v132, v132, v152\n"
"v_add_f32 v133, v133, v153\n"
"v_mfma_f32_16x16x32_bf16 v[112:115], a[228:231], a[4:7], v[112:115]\n"
"ds_write_addtid_b32 v164 offset:3840\n"
"v_add_f32 v134, v134, v154\n"
"v_mfma_f32_16x16x32_bf16 v[112:115], a[232:235], a[8:11], v[112:115]\n"
"ds_write_addtid_b32 v165 offset:8480\n"
"v_add_f32 v135, v135, v155\n"
"v_mfma_f32_16x16x32_bf16 v[112:115], a[236:239], a[12:15], v[112:115]\n"
"ds_write_addtid_b32 v166 offset:13184\n"
"v_add_f32 v132, v132, v160\n"
"v_mfma_f32_16x16x32_bf16 v[112:115], a[240:243], a[16:19], v[112:115]\n"
"ds_write_addtid_b32 v167 offset:17824\n"
"v_add_f32 v133, v133, v161\n"
"v_mfma_f32_16x16x32_bf16 v[112:115], a[244:247], a[20:23], v[112:115]\n"
"v_add_f32 v134, v134, v162\n"
"v_add_f32 v135, v135, v163\n"
"v_mfma_f32_16x16x32_bf16 v[112:115], a[248:251], a[24:27], v[112:115]\n"
"v_add_f32 v132, v132, v120\n"
"v_add_f32 v133, v133, v121\n"
"v_mfma_f32_16x16x32_bf16 v[112:115], a[252:255], a[28:31], v[112:115]\n"
"v_add_f32 v134, v134, v122\n"
"v_add_f32 v135, v135, v123\n"
"v_mfma_f32_16x16x32_bf16 v[124:127], a[224:227], a[32:35], 0\n"
"v_cvt_pk_bf16_f32 v158, v132, v133\n"
"v_cvt_pk_bf16_f32 v159, v134, v135\n"
"v_mfma_f32_16x16x32_bf16 v[124:127], a[228:231], a[36:39], v[124:127]\n"
"s_waitcnt lgkmcnt(4)\n"
"ds_write_addtid_b32 v112 offset:4096\n"
"v_mfma_f32_16x16x32_bf16 v[152:155], v[116:119], v[156:159], v[20:23]\n"
"v_mfma_f32_16x16x32_bf16 v[124:127], a[232:235], a[40:43], v[124:127]\n"
"ds_write_addtid_b32 v113 offset:8736\n"
"buffer_load_dwordx4 v[160:163], v9, s[40:43], s50 offen sc1\n"
"v_mfma_f32_16x16x32_bf16 v[124:127], a[236:239], a[44:47], v[124:127]\n"
"s_waitcnt vmcnt(9)\n"
"ds_write_addtid_b32 v114 offset:13440\n"
"v_cmp_ne_u64 vcc, s[48:49], v[146:147]\n"
"v_mfma_f32_16x16x32_bf16 v[124:127], a[240:243], a[48:51], v[124:127]\n"
"ds_write_addtid_b32 v115 offset:18080\n"
"v_mfma_f32_16x16x32_bf16 v[124:127], a[244:247], a[52:55], v[124:127]\n"
"s_cbranch_vccnz .LQ_3s\n"
".LQ_3r:\n"
"v_mfma_f32_16x16x32_bf16 v[124:127], a[248:251], a[56:59], v[124:127]\n"
"s_add_u32 s50, s50, 0x100000\n"
"s_and_b32 s56, s50, 0xfbffffff\n"
"v_mfma_f32_16x16x32_bf16 v[124:127], a[252:255], a[60:63], v[124:127]\n"
"s_branch .LQ_top\n"
".LQ_1s:\n"
"s_nop 7\n"
"s_cmp_ge_u32 s50, s53\n"
"s_cbranch_scc1 .LQ_end\n"
"s_cmp_ge_u32 s50, s52\n"
"s_cbranch_scc1 .LQ_1r\n"
"s_sub_u32 s58, s50, 0x100000\n"
"s_mov_b32 s59, 200000\n"
".LQ_1p:\n"
"buffer_load_dwordx4 v[128:131], v9, s[40:43], s58 offen sc1\n"
"s_waitcnt vmcnt(0)\n"
"v_cmp_ne_u64 vcc, s[48:49], v[130:131]\n"
"s_cbranch_vccz .LQ_1px\n"
"s_sleep 1\n"
"s_sub_u32 s59, s59, 1\n"
"s_cbranch_scc0 .LQ_1p\n"
"s_branch .LQ_end\n"
".LQ_1px:\n"
"s_branch .LQ_1r\n"
".LQ_2s:\n"
"s_nop 7\n"
"s_cmp_ge_u32 s50, s53\n"
"s_cbranch_scc1 .LQ_end\n"
"s_cmp_ge_u32 s50, s52\n"
"s_cbranch_scc1 .LQ_2r\n"
"s_sub_u32 s58, s50, 0x100000\n"
"s_mov_b32 s59, 200000\n"
".LQ_2p:\n"
"buffer_load_dwordx4 v[136:139], v10, s[40:43], s58 offen sc1\n"
"s_waitcnt vmcnt(0)\n"
"v_cmp_ne_u64 vcc, s[48:49], v[138:139]\n"
"s_cbranch_vccz .LQ_2px\n"
"s_sleep 1\n"
"s_sub_u32 s59, s59, 1\n"
"s_cbranch_scc0 .LQ_2p\n"
"s_branch .LQ_end\n"
".LQ_2px:\n"
"s_branch .LQ_2r\n"
".LQ_3s:\n"
"s_nop 7\n"
"s_cmp_ge_u32 s50, s53\n"
"s_cbranch_scc1 .LQ_end\n"
"s_cmp_ge_u32 s50, s52\n"
"s_cbranch_scc1 .LQ_3r\n"
"s_sub_u32 s58, s50, 0x100000\n"
"s_mov_b32 s59, 200000\n"
".LQ_3p:\n"
"buffer_load_dwordx4 v[144:147], v9, s[40:43], s58 offen sc1\n"
"s_waitcnt vmcnt(0)\n"
"v_cmp_ne_u64 vcc, s[48:49], v[146:147]\n"
"s_cbranch_vccz .LQ_3px\n"
"s_sleep 1\n"
"s_sub_u32 s59, s59, 1\n"
"s_cbranch_scc0 .LQ_3p\n"
"s_branch .LQ_end\n"
".LQ_3px:\n"
"s_branch .LQ_3r\n"
".LQ_4s:\n"
"s_nop 7\n"
"s_cmp_ge_u32 s50, s53\n"
"s_cbranch_scc1 .LQ_end\n"
"s_cmp_ge_u32 s50, s52\n"
"s_cbranch_scc1 .LQ_4r\n"
"s_sub_u32 s58, s50, 0x100000\n"
"s_mov_b32 s59, 200000\n"
".LQ_4p:\n"
"buffer_load_dwordx4 v[160:163], v9, s[40:43], s58 offen sc1\n"
"s_waitcnt vmcnt(0)\n"
"v_cmp_ne_u64 vcc, s[48:49], v[162:163]\n"
"s_cbranch_vccz .LQ_4px\n"
"s_sleep 1\n"
"s_sub_u32 s59, s59, 1\n"
"s_cbranch_scc0 .LQ_4p\n"
"s_branch .LQ_end\n"
".LQ_4px:\n"
"s_branch .LQ_4r\n"
".LQ_fs:\n"
"s_nop 7\n"
"v_mov_b32 v152, 0xff800000\n"
"v_mov_b32 v153, 0xff800000\n"
"v_mov_b32 v154, 0xff800000\n"
"v_mov_b32 v155, 0xff800000\n"
"v_mov_b32 v142, 0\n"
"v_mov_b32 v143, 0\n"
"v_mov_b32 v150, 0\n"
"v_mov_b32 v151, 0\n"
"s_add_u32 s60, s60, 0x4000000\n"
"s_branch .LQ_fr\n"
".LQ_end:\n"
"s_waitcnt vmcnt(0) lgkmcnt(0)\n"
"s_endpgm\n"
      :: "{v2}"((unsigned)(lds0 + ((wid + 3) & 3) * 20640 + trp)), "{v3}"((unsigned)(lds0 + ((wid + 2) & 3) * 20640 + trp)), "{v4}"((unsigned)(lds0 + ((wid + 1) & 3) * 20640 + trp)), "{v5}"((unsigned)(lane * 16 + wid * 8192 + ((0 + rot) & 3) * 2048 + 0)), "{v6}"((unsigned)(lane * 16 + wid * 8192 + ((1 + rot) & 3) * 2048 + 0)), "{v7}"((unsigned)(lane * 16 + wid * 8192 + ((2 + rot) & 3) * 2048 + 0)), "{v8}"((unsigned)(lane * 16 + wid * 8192 + ((3 + rot) & 3) * 2048 + 0)), "{v9}"((unsigned)(li * 16 + 352256 + Q_GL * 1048576)), "{v10}"((unsigned)(((li + 4 * lg) & 15) * 16 + 352256 + Q_GL * 1048576)), "{v11}"((unsigned)(lds0 + ((wid + 1) & 3) * 20640 + 18592 + lane * 4)), "{v12}"((unsigned)(lds0 + ((wid + 2) & 3) * 20640 + 18592 + lane * 4)), "{v13}"((unsigned)(lane * 16 + idx * 1024 + 32768)), "{v14}"((unsigned)(lane ? DROP : idx * 8 + 352512)), "{v16}"((unsigned)(qmask(0, 0))), "{v17}"((unsigned)(qmask(1, 0))), "{v18}"((unsigned)(qmask(2, 0))), "{v19}"((unsigned)(qmask(3, 0))), "{v20}"((unsigned)(qmask(0, 1))), "{v21}"((unsigned)(qmask(1, 1))), "{v22}"((unsigned)(qmask(2, 1))), "{v23}"((unsigned)(qmask(3, 1))), "{v24}"((unsigned)(pmask(0))), "{v25}"((unsigned)(pmask(1))), "{v26}"((unsigned)(pmask(2))), "{v27}"((unsigned)(pmask(3))), "{v48}"((unsigned)(vT + tsw)), "{v49}"((unsigned)(vT + (tsw ^ 8))), "{v50}"((unsigned)(vD + ((0 + rot) & 3) * 393216)), "{v51}"((unsigned)(vD + ((1 + rot) & 3) * 393216)), "{v52}"((unsigned)(vD + ((2 + rot) & 3) * 393216)), "{v53}"((unsigned)(vD + ((3 + rot) & 3) * 393216)), "{v54}"((unsigned)(lds0 + wid * 20640 + trp)), "{s[40:43]}"(rS), "{s[44:47]}"(rW), "{s[48:49]}"(wsp), "{s50}"((unsigned)(sA)), "{s51}"((unsigned)(sLo)), "{s52}"((unsigned)(sHi)), "{s53}"((unsigned)(sEx)), "{s54}"((unsigned)(sEnt)), "{s55}"((unsigned)(sLd)), "{s56}"((unsigned)(sA & 0xfbffffff)), "{s57}"((unsigned)(lds0 + wid * 20640)), "{s60}"((unsigned)(sLo)), "{s61}"((unsigned)(sW0 + 0)), "{s62}"((unsigned)(sW0 + 2048)), "{s63}"((unsigned)(sW0 + 4096))
      : "v255", "a255", "s95", "vcc", "scc", "memory");

  }
  const unsigned sA = sLo - 3 * RS, sHi = sLo + (set ? 84 : 85) * 3 * RS;
  if (role == 1) {
    const unsigned sEx = sHi + O_EX * 3 * RS, sEnt = 0, tsw = 0, pair = idx * 4 + wid, rot = 0;
    const unsigned sW0 = wid * 256 * 2048 + idx * 256, vD = (lane >> 2) * 2048 + (lane & 3) * 16;
    const u32x4 rW = desc(a.wo, 1024 * 2048);
    asm volatile(
"s_nop 4\n"
"v_mov_b32 v186, 0\n"
"global_load_dwordx2 v[0:1], v186, s[48:49] sc1\n"
"v_mov_b32 v188, 0x3f803f80\n"
"v_mov_b32 v189, 0x3f803f80\n"
"v_mov_b32 v190, 0x3f803f80\n"
"v_mov_b32 v191, 0x3f803f80\n"
"s_add_u32 m0, s55, 0\n"
"s_add_u32 s58, s64, 0\n"
"buffer_load_dwordx4 v22, s[44:47], s58 offen lds\n"
"s_add_u32 m0, s55, 1024\n"
"s_add_u32 s58, s64, 32768\n"
"buffer_load_dwordx4 v22, s[44:47], s58 offen lds\n"
"s_add_u32 m0, s55, 2048\n"
"s_add_u32 s58, s65, 0\n"
"buffer_load_dwordx4 v22, s[44:47], s58 offen lds\n"
"s_add_u32 m0, s55, 3072\n"
"s_add_u32 s58, s65, 32768\n"
"buffer_load_dwordx4 v22, s[44:47], s58 offen lds\n"
"s_add_u32 m0, s55, 4096\n"
"s_add_u32 s58, s66, 0\n"
"buffer_load_dwordx4 v22, s[44:47], s58 offen lds\n"
"s_add_u32 m0, s55, 5120\n"
"s_add_u32 s58, s66, 32768\n"
"buffer_load_dwordx4 v22, s[44:47], s58 offen lds\n"
"s_add_u32 m0, s55, 6144\n"
"s_add_u32 s58, s67, 0\n"
"buffer_load_dwordx4 v22, s[44:47], s58 offen lds\n"
"s_add_u32 m0, s55, 7168\n"
"s_add_u32 s58, s67, 32768\n"
"buffer_load_dwordx4 v22, s[44:47], s58 offen lds\n"
"s_add_u32 m0, s55, 8192\n"
"s_add_u32 s58, s64, 65536\n"
"buffer_load_dwordx4 v22, s[44:47], s58 offen lds\n"
"s_add_u32 m0, s55, 9216\n"
"s_add_u32 s58, s64, 98304\n"
"buffer_load_dwordx4 v22, s[44:47], s58 offen lds\n"
"s_add_u32 m0, s55, 10240\n"
"s_add_u32 s58, s65, 65536\n"
"buffer_load_dwordx4 v22, s[44:47], s58 offen lds\n"
"s_add_u32 m0, s55, 11264\n"
"s_add_u32 s58, s65, 98304\n"
"buffer_load_dwordx4 v22, s[44:47], s58 offen lds\n"
"s_add_u32 m0, s55, 12288\n"
"s_add_u32 s58, s66, 65536\n"
"buffer_load_dwordx4 v22, s[44:47], s58 offen lds\n"
"s_add_u32 m0, s55, 13312\n"
"s_add_u32 s58, s66, 98304\n"
"buffer_load_dwordx4 v22, s[44:47], s58 offen lds\n"
"s_add_u32 m0, s55, 14336\n"
"s_add_u32 s58, s67, 65536\n"
"buffer_load_dwordx4 v22, s[44:47], s58 offen lds\n"
"s_add_u32 m0, s55, 15360\n"
"s_add_u32 s58, s67, 98304\n"
"buffer_load_dwordx4 v22, s[44:47], s58 offen lds\n"
"s_add_u32 m0, s55, 16384\n"
"s_add_u32 s58, s64, 0\n"
"buffer_load_dwordx4 v23, s[44:47], s58 offen lds\n"
"s_add_u32 m0, s55, 17408\n"
"s_add_u32 s58, s64, 32768\n"
"buffer_load_dwordx4 v23, s[44:47], s58 offen lds\n"
"s_add_u32 m0, s55, 18432\n"
"s_add_u32 s58, s65, 0\n"
"buffer_load_dwordx4 v23, s[44:47], s58 offen lds\n"
"s_add_u32 m0, s55, 19456\n"
"s_add_u32 s58, s65, 32768\n"
"buffer_load_dwordx4 v23, s[44:47], s58 offen lds\n"
"s_add_u32 m0, s55, 20480\n"
"s_add_u32 s58, s66, 0\n"
"buffer_load_dwordx4 v23, s[44:47], s58 offen lds\n"
"s_add_u32 m0, s55, 21504\n"
"s_add_u32 s58, s66, 32768\n"
"buffer_load_dwordx4 v23, s[44:47], s58 offen lds\n"
"s_add_u32 m0, s55, 22528\n"
"s_add_u32 s58, s67, 0\n"
"buffer_load_dwordx4 v23, s[44:47], s58 offen lds\n"
"s_add_u32 m0, s55, 23552\n"
"s_add_u32 s58, s67, 32768\n"
"buffer_load_dwordx4 v23, s[44:47], s58 offen lds\n"
"s_add_u32 m0, s55, 24576\n"
"s_add_u32 s58, s64, 65536\n"
"buffer_load_dwordx4 v23, s[44:47], s58 offen lds\n"
"s_add_u32 m0, s55, 25600\n"
"s_add_u32 s58, s64, 98304\n"
"buffer_load_dwordx4 v23, s[44:47], s58 offen lds\n"
"s_add_u32 m0, s55, 26624\n"
"s_add_u32 s58, s65, 65536\n"
"buffer_load_dwordx4 v23, s[44:47], s58 offen lds\n"
"s_add_u32 m0, s55, 27648\n"
"s_add_u32 s58, s65, 98304\n"
"buffer_load_dwordx4 v23, s[44:47], s58 offen lds\n"
"s_add_u32 m0, s55, 28672\n"
"s_add_u32 s58, s66, 65536\n"
"buffer_load_dwordx4 v23, s[44:47], s58 offen lds\n"
"s_add_u32 m0, s55, 29696\n"
"s_add_u32 s58, s66, 98304\n"
"buffer_load_dwordx4 v23, s[44:47], s58 offen lds\n"
"s_add_u32 m0, s55, 30720\n"
"s_add_u32 s58, s67, 65536\n"
"buffer_load_dwordx4 v23, s[44:47], s58 offen lds\n"
"s_add_u32 m0, s55, 31744\n"
"s_add_u32 s58, s67, 98304\n"
"buffer_load_dwordx4 v23, s[44:47], s58 offen lds\n"
"s_add_u32 m0, s55, 32768\n"
"s_add_u32 s58, s64, 0\n"
"buffer_load_dwordx4 v24, s[44:47], s58 offen lds\n"
"s_add_u32 m0, s55, 33792\n"
"s_add_u32 s58, s64, 32768\n"
"buffer_load_dwordx4 v24, s[44:47], s58 offen lds\n"
"s_add_u32 m0, s55, 34816\n"
"s_add_u32 s58, s65, 0\n"
"buffer_load_dwordx4 v24, s[44:47], s58 offen lds\n"
"s_add_u32 m0, s55, 35840\n"
"s_add_u32 s58, s65, 32768\n"
"buffer_load_dwordx4 v24, s[44:47], s58 offen lds\n"
"s_add_u32 m0, s55, 36864\n"
"s_add_u32 s58, s66, 0\n"
"buffer_load_dwordx4 v24, s[44:47], s58 offen lds\n"
"s_add_u32 m0, s55, 37888\n"
"s_add_u32 s58, s66, 32768\n"
"buffer_load_dwordx4 v24, s[44:47], s58 offen lds\n"
"s_add_u32 m0, s55, 38912\n"
"s_add_u32 s58, s67, 0\n"
"buffer_load_dwordx4 v24, s[44:47], s58 offen lds\n"
"s_add_u32 m0, s55, 39936\n"
"s_add_u32 s58, s67, 32768\n"
"buffer_load_dwordx4 v24, s[44:47], s58 offen lds\n"
"s_waitcnt vmcnt(38)\n"
"ds_read_b64_tr_b16 a[0:1], v20 offset:0\n"
"ds_read_b64_tr_b16 a[2:3], v20 offset:256\n"
"ds_read_b64_tr_b16 a[32:33], v21 offset:0\n"
"ds_read_b64_tr_b16 a[34:35], v21 offset:256\n"
"v_add_u32 v0, 2, v0\n"
"v_add_u32 v1, 0x9e3779b9, v1\n"
"v_or_b32 v0, 1, v0\n"
"v_readfirstlane_b32 s49, v1\n"
"v_readfirstlane_b32 s48, v0\n"
"s_waitcnt vmcnt(36)\n"
"ds_read_b64_tr_b16 a[64:65], v20 offset:2048\n"
"ds_read_b64_tr_b16 a[66:67], v20 offset:2304\n"
"ds_read_b64_tr_b16 a[96:97], v21 offset:2048\n"
"ds_read_b64_tr_b16 a[98:99], v21 offset:2304\n"
"s_waitcnt lgkmcnt(4)\n"
"s_add_u32 m0, s55, 0\n"
"s_add_u32 s58, s64, 65536\n"
"buffer_load_dwordx4 v24, s[44:47], s58 offen lds\n"
"s_add_u32 m0, s55, 1024\n"
"s_add_u32 s58, s64, 98304\n"
"buffer_load_dwordx4 v24, s[44:47], s58 offen lds\n"
"s_waitcnt vmcnt(36)\n"
"ds_read_b64_tr_b16 a[128:129], v20 offset:4096\n"
"ds_read_b64_tr_b16 a[130:131], v20 offset:4352\n"
"ds_read_b64_tr_b16 a[160:161], v21 offset:4096\n"
"ds_read_b64_tr_b16 a[162:163], v21 offset:4352\n"
"s_waitcnt lgkmcnt(4)\n"
"s_add_u32 m0, s55, 2048\n"
"s_add_u32 s58, s65, 65536\n"
"buffer_load_dwordx4 v24, s[44:47], s58 offen lds\n"
"s_add_u32 m0, s55, 3072\n"
"s_add_u32 s58, s65, 98304\n"
"buffer_load_dwordx4 v24, s[44:47], s58 offen lds\n"
"s_waitcnt vmcnt(36)\n"
"ds_read_b64_tr_b16 a[192:193], v20 offset:6144\n"
"ds_read_b64_tr_b16 a[194:195], v20 offset:6400\n"
"ds_read_b64_tr_b16 a[224:225], v21 offset:6144\n"
"ds_read_b64_tr_b16 a[226:227], v21 offset:6400\n"
"s_waitcnt lgkmcnt(4)\n"
"s_add_u32 m0, s55, 4096\n"
"s_add_u32 s58, s66, 65536\n"
"buffer_load_dwordx4 v24, s[44:47], s58 offen lds\n"
"s_add_u32 m0, s55, 5120\n"
"s_add_u32 s58, s66, 98304\n"
"buffer_load_dwordx4 v24, s[44:47], s58 offen lds\n"
"s_waitcnt vmcnt(36)\n"
"ds_read_b64_tr_b16 a[4:5], v20 offset:8192\n"
"ds_read_b64_tr_b16 a[6:7], v20 offset:8448\n"
"ds_read_b64_tr_b16 a[36:37], v21 offset:8192\n"
"ds_read_b64_tr_b16 a[38:39], v21 offset:8448\n"
"s_waitcnt lgkmcnt(4)\n"
"s_add_u32 m0, s55, 6144\n"
"s_add_u32 s58, s67, 65536\n"
"buffer_load_dwordx4 v24, s[44:47], s58 offen lds\n"
"s_add_u32 m0, s55, 7168\n"
"s_add_u32 s58, s67, 98304\n"
"buffer_load_dwordx4 v24, s[44:47], s58 offen lds\n"
"s_waitcnt vmcnt(36)\n"
"ds_read_b64_tr_b16 a[68:69], v20 offset:10240\n"
"ds_read_b64_tr_b16 a[70:71], v20 offset:10496\n"
"ds_read_b64_tr_b16 a[100:101], v21 offset:10240\n"
"ds_read_b64_tr_b16 a[102:103], v21 offset:10496\n"
"s_waitcnt lgkmcnt(4)\n"
"s_add_u32 m0, s55, 8192\n"
"s_add_u32 s58, s64, 0\n"
"buffer_load_dwordx4 v25, s[44:47], s58 offen lds\n"
"s_add_u32 m0, s55, 9216\n"
"s_add_u32 s58, s64, 32768\n"
"buffer_load_dwordx4 v25, s[44:47], s58 offen lds\n"
"s_waitcnt vmcnt(36)\n"
"ds_read_b64_tr_b16 a[132:133], v20 offset:12288\n"
"ds_read_b64_tr_b16 a[134:135], v20 offset:12544\n"
"ds_read_b64_tr_b16 a[164:165], v21 offset:12288\n"
"ds_read_b64_tr_b16 a[166:167], v21 offset:12544\n"
"s_waitcnt lgkmcnt(4)\n"
"s_add_u32 m0, s55, 10240\n"
"s_add_u32 s58, s65, 0\n"
"buffer_load_dwordx4 v25, s[44:47], s58 offen lds\n"
"s_add_u32 m0, s55, 11264\n"
"s_add_u32 s58, s65, 32768\n"
"buffer_load_dwordx4 v25, s[44:47], s58 offen lds\n"
"s_waitcnt vmcnt(36)\n"
"ds_read_b64_tr_b16 a[196:197], v20 offset:14336\n"
"ds_read_b64_tr_b16 a[198:199], v20 offset:14592\n"
"ds_read_b64_tr_b16 a[228:229], v21 offset:14336\n"
"ds_read_b64_tr_b16 a[230:231], v21 offset:14592\n"
"s_waitcnt lgkmcnt(4)\n"
"s_add_u32 m0, s55, 12288\n"
"s_add_u32 s58, s66, 0\n"
"buffer_load_dwordx4 v25, s[44:47], s58 offen lds\n"
"s_add_u32 m0, s55, 13312\n"
"s_add_u32 s58, s66, 32768\n"
"buffer_load_dwordx4 v25, s[44:47], s58 offen lds\n"
"s_waitcnt vmcnt(36)\n"
"ds_read_b64_tr_b16 a[8:9], v20 offset:16384\n"
"ds_read_b64_tr_b16 a[10:11], v20 offset:16640\n"
"ds_read_b64_tr_b16 a[40:41], v21 offset:16384\n"
"ds_read_b64_tr_b16 a[42:43], v21 offset:16640\n"
"s_waitcnt lgkmcnt(4)\n"
"s_add_u32 m0, s55, 14336\n"
"s_add_u32 s58, s67, 0\n"
"buffer_load_dwordx4 v25, s[44:47], s58 offen lds\n"
"s_add_u32 m0, s55, 15360\n"
"s_add_u32 s58, s67, 32768\n"
"buffer_load_dwordx4 v25, s[44:47], s58 offen lds\n"
"s_waitcnt vmcnt(36)\n"
"ds_read_b64_tr_b16 a[72:73], v20 offset:18432\n"
"ds_read_b64_tr_b16 a[74:75], v20 offset:18688\n"
"ds_read_b64_tr_b16 a[104:105], v21 offset:18432\n"
"ds_read_b64_tr_b16 a[106:107], v21 offset:18688\n"
"s_waitcnt lgkmcnt(4)\n"
"s_add_u32 m0, s55, 16384\n"
"s_add_u32 s58, s64, 65536\n"
"buffer_load_dwordx4 v25, s[44:47], s58 offen lds\n"
"s_add_u32 m0, s55, 17408\n"
"s_add_u32 s58, s64, 98304\n"
"buffer_load_dwordx4 v25, s[44:47], s58 offen lds\n"
"s_waitcnt vmcnt(36)\n"
"ds_read_b64_tr_b16 a[136:137], v20 offset:20480\n"
"ds_read_b64_tr_b16 a[138:139], v20 offset:20736\n"
"ds_read_b64_tr_b16 a[168:169], v21 offset:20480\n"
"ds_read_b64_tr_b16 a[170:171], v21 offset:20736\n"
"s_waitcnt lgkmcnt(4)\n"
"s_add_u32 m0, s55, 18432\n"
"s_add_u32 s58, s65, 65536\n"
"buffer_load_dwordx4 v25, s[44:47], s58 offen lds\n"
"s_add_u32 m0, s55, 19456\n"
"s_add_u32 s58, s65, 98304\n"
"buffer_load_dwordx4 v25, s[44:47], s58 offen lds\n"
"s_waitcnt vmcnt(36)\n"
"ds_read_b64_tr_b16 a[200:201], v20 offset:22528\n"
"ds_read_b64_tr_b16 a[202:203], v20 offset:22784\n"
"ds_read_b64_tr_b16 a[232:233], v21 offset:22528\n"
"ds_read_b64_tr_b16 a[234:235], v21 offset:22784\n"
"s_waitcnt lgkmcnt(4)\n"
"s_add_u32 m0, s55, 20480\n"
"s_add_u32 s58, s66, 65536\n"
"buffer_load_dwordx4 v25, s[44:47], s58 offen lds\n"
"s_add_u32 m0, s55, 21504\n"
"s_add_u32 s58, s66, 98304\n"
"buffer_load_dwordx4 v25, s[44:47], s58 offen lds\n"
"s_waitcnt vmcnt(36)\n"
"ds_read_b64_tr_b16 a[12:13], v20 offset:24576\n"
"ds_read_b64_tr_b16 a[14:15], v20 offset:24832\n"
"ds_read_b64_tr_b16 a[44:45], v21 offset:24576\n"
"ds_read_b64_tr_b16 a[46:47], v21 offset:24832\n"
"s_waitcnt lgkmcnt(4)\n"
"s_add_u32 m0, s55, 22528\n"
"s_add_u32 s58, s67, 65536\n"
"buffer_load_dwordx4 v25, s[44:47], s58 offen lds\n"
"s_add_u32 m0, s55, 23552\n"
"s_add_u32 s58, s67, 98304\n"
"buffer_load_dwordx4 v25, s[44:47], s58 offen lds\n"
"s_waitcnt vmcnt(36)\n"
"ds_read_b64_tr_b16 a[76:77], v20 offset:26624\n"
"ds_read_b64_tr_b16 a[78:79], v20 offset:26880\n"
"ds_read_b64_tr_b16 a[108:109], v21 offset:26624\n"
"ds_read_b64_tr_b16 a[110:111], v21 offset:26880\n"
"s_waitcnt lgkmcnt(4)\n"
"s_waitcnt vmcnt(34)\n"
"ds_read_b64_tr_b16 a[140:141], v20 offset:28672\n"
"ds_read_b64_tr_b16 a[142:143], v20 offset:28928\n"
"ds_read_b64_tr_b16 a[172:173], v21 offset:28672\n"
"ds_read_b64_tr_b16 a[174:175], v21 offset:28928\n"
"s_waitcnt lgkmcnt(4)\n"
"s_waitcnt vmcnt(32)\n"
"ds_read_b64_tr_b16 a[204:205], v20 offset:30720\n"
"ds_read_b64_tr_b16 a[206:207], v20 offset:30976\n"
"ds_read_b64_tr_b16 a[236:237], v21 offset:30720\n"
"ds_read_b64_tr_b16 a[238:239], v21 offset:30976\n"
"s_waitcnt lgkmcnt(4)\n"
"s_waitcnt vmcnt(30)\n"
"ds_read_b64_tr_b16 a[16:17], v20 offset:32768\n"
"ds_read_b64_tr_b16 a[18:19], v20 offset:33024\n"
"ds_read_b64_tr_b16 a[48:49], v21 offset:32768\n"
"ds_read_b64_tr_b16 a[50:51], v21 offset:33024\n"
"s_waitcnt lgkmcnt(4)\n"
"s_waitcnt vmcnt(28)\n"
"ds_read_b64_tr_b16 a[80:81], v20 offset:34816\n"
"ds_read_b64_tr_b16 a[82:83], v20 offset:35072\n"
"ds_read_b64_tr_b16 a[112:113], v21 offset:34816\n"
"ds_read_b64_tr_b16 a[114:115], v21 offset:35072\n"
"s_waitcnt lgkmcnt(4)\n"
"s_waitcnt vmcnt(26)\n"
"ds_read_b64_tr_b16 a[144:145], v20 offset:36864\n"
"ds_read_b64_tr_b16 a[146:147], v20 offset:37120\n"
"ds_read_b64_tr_b16 a[176:177], v21 offset:36864\n"
"ds_read_b64_tr_b16 a[178:179], v21 offset:37120\n"
"s_waitcnt lgkmcnt(4)\n"
"s_waitcnt vmcnt(24)\n"
"ds_read_b64_tr_b16 a[208:209], v20 offset:38912\n"
"ds_read_b64_tr_b16 a[210:211], v20 offset:39168\n"
"ds_read_b64_tr_b16 a[240:241], v21 offset:38912\n"
"ds_read_b64_tr_b16 a[242:243], v21 offset:39168\n"
"s_waitcnt lgkmcnt(4)\n"
"s_waitcnt vmcnt(22)\n"
"ds_read_b64_tr_b16 a[20:21], v20 offset:0\n"
"ds_read_b64_tr_b16 a[22:23], v20 offset:256\n"
"ds_read_b64_tr_b16 a[52:53], v21 offset:0\n"
"ds_read_b64_tr_b16 a[54:55], v21 offset:256\n"
"s_waitcnt lgkmcnt(4)\n"
"s_waitcnt vmcnt(20)\n"
"ds_read_b64_tr_b16 a[84:85], v20 offset:2048\n"
"ds_read_b64_tr_b16 a[86:87], v20 offset:2304\n"
"ds_read_b64_tr_b16 a[116:117], v21 offset:2048\n"
"ds_read_b64_tr_b16 a[118:119], v21 offset:2304\n"
"s_waitcnt lgkmcnt(4)\n"
"s_waitcnt vmcnt(18)\n"
"ds_read_b64_tr_b16 a[148:149], v20 offset:4096\n"
"ds_read_b64_tr_b16 a[150:151], v20 offset:4352\n"
"ds_read_b64_tr_b16 a[180:181], v21 offset:4096\n"
"ds_read_b64_tr_b16 a[182:183], v21 offset:4352\n"
"s_waitcnt lgkmcnt(4)\n"
"s_waitcnt vmcnt(16)\n"
"ds_read_b64_tr_b16 a[212:213], v20 offset:6144\n"
"ds_read_b64_tr_b16 a[214:215], v20 offset:6400\n"
"ds_read_b64_tr_b16 a[244:245], v21 offset:6144\n"
"ds_read_b64_tr_b16 a[246:247], v21 offset:6400\n"
"s_waitcnt lgkmcnt(4)\n"
"s_waitcnt vmcnt(14)\n"
"ds_read_b64_tr_b16 a[24:25], v20 offset:8192\n"
"ds_read_b64_tr_b16 a[26:27], v20 offset:8448\n"
"ds_read_b64_tr_b16 a[56:57], v21 offset:8192\n"
"ds_read_b64_tr_b16 a[58:59], v21 offset:8448\n"
"s_waitcnt lgkmcnt(4)\n"
"s_waitcnt vmcnt(12)\n"
"ds_read_b64_tr_b16 a[88:89], v20 offset:10240\n"
"ds_read_b64_tr_b16 a[90:91], v20 offset:10496\n"
"ds_read_b64_tr_b16 a[120:121], v21 offset:10240\n"
"ds_read_b64_tr_b16 a[122:123], v21 offset:10496\n"
"s_waitcnt lgkmcnt(4)\n"
"s_waitcnt vmcnt(10)\n"
"ds_read_b64_tr_b16 a[152:153], v20 offset:12288\n"
"ds_read_b64_tr_b16 a[154:155], v20 offset:12544\n"
"ds_read_b64_tr_b16 a[184:185], v21 offset:12288\n"
"ds_read_b64_tr_b16 a[186:187], v21 offset:12544\n"
"s_waitcnt lgkmcnt(4)\n"
"s_waitcnt vmcnt(8)\n"
"ds_read_b64_tr_b16 a[216:217], v20 offset:14336\n"
"ds_read_b64_tr_b16 a[218:219], v20 offset:14592\n"
"ds_read_b64_tr_b16 a[248:249], v21 offset:14336\n"
"ds_read_b64_tr_b16 a[250:251], v21 offset:14592\n"
"s_waitcnt lgkmcnt(4)\n"
"s_waitcnt vmcnt(6)\n"
"ds_read_b64_tr_b16 a[28:29], v20 offset:16384\n"
"ds_read_b64_tr_b16 a[30:31], v20 offset:16640\n"
"ds_read_b64_tr_b16 a[60:61], v21 offset:16384\n"
"ds_read_b64_tr_b16 a[62:63], v21 offset:16640\n"
"s_waitcnt lgkmcnt(4)\n"
"s_waitcnt vmcnt(4)\n"
"ds_read_b64_tr_b16 a[92:93], v20 offset:18432\n"
"ds_read_b64_tr_b16 a[94:95], v20 offset:18688\n"
"ds_read_b64_tr_b16 a[124:125], v21 offset:18432\n"
"ds_read_b64_tr_b16 a[126:127], v21 offset:18688\n"
"s_waitcnt lgkmcnt(4)\n"
"s_waitcnt vmcnt(2)\n"
"ds_read_b64_tr_b16 a[156:157], v20 offset:20480\n"
"ds_read_b64_tr_b16 a[158:159], v20 offset:20736\n"
"ds_read_b64_tr_b16 a[188:189], v21 offset:20480\n"
"ds_read_b64_tr_b16 a[190:191], v21 offset:20736\n"
"s_waitcnt lgkmcnt(4)\n"
"s_waitcnt vmcnt(0)\n"
"ds_read_b64_tr_b16 a[220:221], v20 offset:22528\n"
"ds_read_b64_tr_b16 a[222:223], v20 offset:22784\n"
"ds_read_b64_tr_b16 a[252:253], v21 offset:22528\n"
"ds_read_b64_tr_b16 a[254:255], v21 offset:22784\n"
"s_waitcnt lgkmcnt(4)\n"
"s_waitcnt lgkmcnt(0)\n"
"s_mov_b32 m0, s57\n"
"s_barrier\n"
"v_mov_b32 v120, 0\n"
"v_mov_b32 v121, 0\n"
"v_mov_b32 v124, 0\n"
"v_mov_b32 v125, 0\n"
"v_mov_b32 v124, 0\n"
"v_mov_b32 v125, 0\n"
"v_mov_b32 v124, 0\n"
"v_mov_b32 v125, 0\n"
"s_mov_b32 s95, 0x80000000\n"
"s_mov_b32 s93, 0x80000000\n"
"s_mov_b32 s93, 0x80000000\n"
"s_mov_b32 s94, 0x80000000\n"
"s_mov_b32 s92, 0x80000000\n"
"s_mov_b32 s95, 0x80000000\n"
"s_mov_b32 s94, 0x80000000\n"
"s_mov_b32 s95, 0x80000000\n"
"s_mov_b32 s95, 0x80000000\n"
"s_mov_b32 s95, 0x80000000\n"
"s_mov_b32 s94, 0x80000000\n"
"s_mov_b32 s95, 0x80000000\n"
"s_cmp_eq_u32 s54, 0\n"
"s_cbranch_scc1 .LO_1s\n"
"s_cmp_eq_u32 s54, 1\n"
"s_cbranch_scc1 .LO_2s\n"
"s_cmp_eq_u32 s54, 2\n"
"s_cbranch_scc1 .LO_3s\n"
"s_cmp_eq_u32 s54, 3\n"
"s_cbranch_scc1 .LO_4s\n"
".p2align 6\n"
".LO_top:\n"
"v_mfma_f32_16x16x32_bf16 v[36:39], v[84:87], a[0:3], 0\n"
"buffer_load_dwordx4 v[20:23], v6, s[40:43], s56 offen offset:0 sc1\n"
"v_mfma_f32_16x16x32_bf16 v[36:39], v[48:51], a[4:7], v[36:39]\n"
"s_waitcnt vmcnt(11)\n"
"v_mfma_f32_16x16x32_bf16 v[36:39], v[52:55], a[8:11], v[36:39]\n"
"s_waitcnt lgkmcnt(3)\n"
"v_add_f32 v96, v96, v44\n"
"v_add_f32 v97, v97, v45\n"
"v_mfma_f32_16x16x32_bf16 v[36:39], v[56:59], a[12:15], v[36:39]\n"
"s_waitcnt vmcnt(9)\n"
"buffer_load_dwordx2 v[124:125], v9, s[40:43], s50 offen sc1\n"
"v_add_f32 v98, v98, v46\n"
"v_mfma_f32_16x16x32_bf16 v[36:39], v[60:63], a[16:19], v[36:39]\n"
"buffer_load_dwordx4 v[24:27], v6, s[40:43], s56 offen offset:1024 sc1\n"
"v_add_f32 v99, v99, v47\n"
"v_mfma_f32_16x16x32_bf16 v[36:39], v[64:67], a[20:23], v[36:39]\n"
"s_waitcnt vmcnt(8)\n"
"v_add_f32 v96, v96, v76\n"
"v_add_f32 v97, v97, v77\n"
"v_mfma_f32_16x16x32_bf16 v[36:39], v[68:71], a[24:27], v[36:39]\n"
"v_add_f32 v98, v98, v78\n"
"v_add_f32 v99, v99, v79\n"
"v_mfma_f32_16x16x32_bf16 v[36:39], v[72:75], a[28:31], v[36:39]\n"
"v_add_f32 v96, v96, v104\n"
"v_add_f32 v97, v97, v105\n"
"v_mfma_f32_16x16x32_bf16 v[44:47], v[84:87], a[32:35], 0\n"
"buffer_load_dwordx4 v[28:31], v7, s[40:43], s56 offen offset:0 sc1\n"
"v_add_f32 v98, v98, v106\n"
"v_mfma_f32_16x16x32_bf16 v[44:47], v[48:51], a[36:39], v[44:47]\n"
"ds_write_addtid_b32 v36 offset:0\n"
"v_add_f32 v99, v99, v107\n"
"v_mfma_f32_16x16x32_bf16 v[44:47], v[52:55], a[40:43], v[44:47]\n"
"s_add_u32 s94, s50, 0xffd00000\n"
"s_and_b32 s95, s94, 0xfbffffff\n"
"ds_write_addtid_b32 v37 offset:4640\n"
"v_mfma_f32_16x16x32_bf16 v[44:47], v[56:59], a[44:47], v[44:47]\n"
"ds_write_addtid_b32 v38 offset:9344\n"
"v_cvt_pk_bf16_f32 v76, v96, v97\n"
"v_mfma_f32_16x16x32_bf16 v[44:47], v[60:63], a[48:51], v[44:47]\n"
"s_waitcnt vmcnt(7)\n"
"ds_write_addtid_b32 v39 offset:13984\n"
"buffer_load_dwordx4 v[32:35], v7, s[40:43], s56 offen offset:1024 sc1\n"
"v_mfma_f32_16x16x32_bf16 v[44:47], v[64:67], a[52:55], v[44:47]\n"
"buffer_store_dwordx2 v[0:1], v12, s[40:43], s92 offen offset:0 sc1\n"
"v_cvt_pk_bf16_f32 v77, v98, v99\n"
"v_mfma_f32_16x16x32_bf16 v[44:47], v[68:71], a[56:59], v[44:47]\n"
"s_waitcnt lgkmcnt(4)\n"
"v_add_f32 v100, v100, v116\n"
"v_add_f32 v101, v101, v117\n"
"v_mfma_f32_16x16x32_bf16 v[44:47], v[72:75], a[60:63], v[44:47]\n"
"v_add_f32 v102, v102, v118\n"
"v_add_f32 v103, v103, v119\n"
"v_mfma_f32_16x16x32_bf16 v[80:83], v[84:87], a[64:67], 0\n"
"buffer_load_dwordx4 v[36:39], v8, s[40:43], s56 offen offset:0 sc1\n"
"v_add_f32 v100, v100, v120\n"
"v_mfma_f32_16x16x32_bf16 v[80:83], v[48:51], a[68:71], v[80:83]\n"
"ds_write_addtid_b32 v44 offset:256\n"
"v_add_f32 v101, v101, v121\n"
"v_mfma_f32_16x16x32_bf16 v[80:83], v[52:55], a[72:75], v[80:83]\n"
"ds_write_addtid_b32 v45 offset:4896\n"
"v_add_f32 v102, v102, v122\n"
"v_mfma_f32_16x16x32_bf16 v[80:83], v[56:59], a[76:79], v[80:83]\n"
"ds_write_addtid_b32 v46 offset:9600\n"
"v_add_f32 v103, v103, v123\n"
"v_mfma_f32_16x16x32_bf16 v[80:83], v[60:63], a[80:83], v[80:83]\n"
"ds_write_addtid_b32 v47 offset:14240\n"
"buffer_load_dwordx4 v[40:43], v8, s[40:43], s56 offen offset:1024 sc1\n"
"v_mfma_f32_16x16x32_bf16 v[80:83], v[64:67], a[84:87], v[80:83]\n"
"s_waitcnt vmcnt(10)\n"
"v_add_f32 v100, v100, v108\n"
"v_add_f32 v101, v101, v109\n"
"v_mfma_f32_16x16x32_bf16 v[80:83], v[68:71], a[88:91], v[80:83]\n"
"v_add_f32 v102, v102, v110\n"
"v_add_f32 v103, v103, v111\n"
"v_mfma_f32_16x16x32_bf16 v[80:83], v[72:75], a[92:95], v[80:83]\n"
"v_cvt_pk_bf16_f32 v78, v100, v101\n"
"v_cvt_pk_bf16_f32 v79, v102, v103\n"
"v_mfma_f32_16x16x32_bf16 v[44:47], v[84:87], a[96:99], 0\n"
"buffer_store_dwordx4 v[76:79], v11, s[40:43], s95 offen sc1\n"
"v_lshlrev_b32 v96, 16, v112\n"
"v_and_b32 v97, 0xffff0000, v112\n"
"v_lshlrev_b32 v98, 16, v113\n"
"v_and_b32 v99, 0xffff0000, v113\n"
"v_mfma_f32_16x16x32_bf16 v[100:103], v[76:79], v[76:79], 0\n"
"v_lshlrev_b32 v104, 16, v114\n"
"v_and_b32 v105, 0xffff0000, v114\n"
"v_mfma_f32_16x16x32_bf16 v[108:111], v[14:17], v[76:79], 0\n"
"v_lshlrev_b32 v106, 16, v115\n"
"v_and_b32 v107, 0xffff0000, v115\n"
"v_mfma_f32_16x16x32_bf16 v[44:47], v[48:51], a[100:103], v[44:47]\n"
"ds_write_addtid_b32 v80 offset:512\n"
"v_cndmask_b32_e64 v100, v100, v101, s[60:61]\n"
"v_mfma_f32_16x16x32_bf16 v[44:47], v[52:55], a[104:107], v[44:47]\n"
"ds_write_addtid_b32 v81 offset:5152\n"
"v_cndmask_b32_e64 v102, v102, v103, s[60:61]\n"
"v_mfma_f32_16x16x32_bf16 v[44:47], v[56:59], a[108:111], v[44:47]\n"
"ds_write_addtid_b32 v82 offset:9856\n"
"v_cndmask_b32_e64 v109, v100, v102, s[62:63]\n"
"v_mfma_f32_16x16x32_bf16 v[44:47], v[60:63], a[112:115], v[44:47]\n"
"buffer_store_dwordx2 v[108:109], v13, s[40:43], s95 offen offset:0 sc1\n"
"ds_write_addtid_b32 v83 offset:14496\n"
"v_mfma_f32_16x16x32_bf16 v[44:47], v[64:67], a[116:119], v[44:47]\n"
"v_mfma_f32_16x16x32_bf16 v[44:47], v[68:71], a[120:123], v[44:47]\n"
"v_mfma_f32_16x16x32_bf16 v[44:47], v[72:75], a[124:127], v[44:47]\n"
"v_mfma_f32_16x16x32_bf16 v[76:79], v[84:87], a[128:131], 0\n"
"v_mfma_f32_16x16x32_bf16 v[76:79], v[48:51], a[132:135], v[76:79]\n"
"ds_write_addtid_b32 v44 offset:768\n"
"v_mfma_f32_16x16x32_bf16 v[76:79], v[52:55], a[136:139], v[76:79]\n"
"ds_write_addtid_b32 v45 offset:5408\n"
"v_mfma_f32_16x16x32_bf16 v[76:79], v[56:59], a[140:143], v[76:79]\n"
"ds_write_addtid_b32 v46 offset:10112\n"
"v_mfma_f32_16x16x32_bf16 v[76:79], v[60:63], a[144:147], v[76:79]\n"
"s_add_u32 s95, s50, 0x0\n"
"s_and_b32 s95, s95, 0xfbffffff\n"
"ds_write_addtid_b32 v47 offset:14752\n"
"v_mfma_f32_16x16x32_bf16 v[76:79], v[64:67], a[148:151], v[76:79]\n"
"buffer_load_dwordx4 v[80:83], v10, s[40:43], s95 offen sc1\n"
"v_mfma_f32_16x16x32_bf16 v[76:79], v[68:71], a[152:155], v[76:79]\n"
"v_mfma_f32_16x16x32_bf16 v[76:79], v[72:75], a[156:159], v[76:79]\n"
"v_mfma_f32_16x16x32_bf16 v[44:47], v[84:87], a[160:163], 0\n"
"v_mfma_f32_16x16x32_bf16 v[44:47], v[48:51], a[164:167], v[44:47]\n"
"ds_write_addtid_b32 v76 offset:1024\n"
"v_mfma_f32_16x16x32_bf16 v[44:47], v[52:55], a[168:171], v[44:47]\n"
"ds_write_addtid_b32 v77 offset:5664\n"
"v_mfma_f32_16x16x32_bf16 v[44:47], v[56:59], a[172:175], v[44:47]\n"
"ds_write_addtid_b32 v78 offset:10368\n"
"v_mfma_f32_16x16x32_bf16 v[44:47], v[60:63], a[176:179], v[44:47]\n"
"ds_write_addtid_b32 v79 offset:15008\n"
"v_mfma_f32_16x16x32_bf16 v[44:47], v[64:67], a[180:183], v[44:47]\n"
"v_mfma_f32_16x16x32_bf16 v[44:47], v[68:71], a[184:187], v[44:47]\n"
"v_mfma_f32_16x16x32_bf16 v[44:47], v[72:75], a[188:191], v[44:47]\n"
"v_mfma_f32_16x16x32_bf16 v[96:99], a[192:195], v[84:87], v[96:99]\n"
"v_mfma_f32_16x16x32_bf16 v[96:99], a[196:199], v[48:51], v[96:99]\n"
"ds_write_addtid_b32 v44 offset:1280\n"
"v_mfma_f32_16x16x32_bf16 v[96:99], a[200:203], v[52:55], v[96:99]\n"
"ds_write_addtid_b32 v45 offset:5920\n"
"v_mfma_f32_16x16x32_bf16 v[96:99], a[204:207], v[56:59], v[96:99]\n"
"ds_write_addtid_b32 v46 offset:10624\n"
"v_mfma_f32_16x16x32_bf16 v[96:99], a[208:211], v[60:63], v[96:99]\n"
"s_waitcnt vmcnt(9)\n"
"ds_write_addtid_b32 v47 offset:15264\n"
"v_cmp_ne_u64 vcc, s[48:49], v[124:125]\n"
"v_mfma_f32_16x16x32_bf16 v[96:99], a[212:215], v[64:67], v[96:99]\n"
"v_mfma_f32_16x16x32_bf16 v[96:99], a[216:219], v[68:71], v[96:99]\n"
"s_cbranch_vccnz .LO_3s\n"
".LO_3r:\n"
"v_mfma_f32_16x16x32_bf16 v[96:99], a[220:223], v[72:75], v[96:99]\n"
"s_add_u32 s50, s50, 0x180000\n"
"s_and_b32 s56, s50, 0xfbffffff\n"
"v_mfma_f32_16x16x32_bf16 v[104:107], a[224:227], v[84:87], v[104:107]\n"
"s_waitcnt lgkmcnt(0)\n"
"buffer_load_dwordx4 v[44:47], v5, s[40:43], s56 offen offset:0 sc1\n"
"v_mfma_f32_16x16x32_bf16 v[104:107], a[228:231], v[48:51], v[104:107]\n"
"s_barrier\n"
"v_mfma_f32_16x16x32_bf16 v[104:107], a[232:235], v[52:55], v[104:107]\n"
"ds_read_b128 v[76:79], v2 offset:0\n"
"ds_read_b128 v[84:87], v3 offset:512\n"
"v_mfma_f32_16x16x32_bf16 v[104:107], a[236:239], v[56:59], v[104:107]\n"
"ds_read_b128 v[100:103], v4 offset:1024\n"
"ds_read_b128 v[108:111], v2 offset:256\n"
"v_mfma_f32_16x16x32_bf16 v[104:107], a[240:243], v[60:63], v[104:107]\n"
"buffer_load_dwordx4 v[48:51], v5, s[40:43], s56 offen offset:1024 sc1\n"
"ds_read_b128 v[112:115], v3 offset:768\n"
"v_mfma_f32_16x16x32_bf16 v[104:107], a[244:247], v[64:67], v[104:107]\n"
"ds_read_b128 v[116:119], v4 offset:1280\n"
"v_mfma_f32_16x16x32_bf16 v[104:107], a[248:251], v[68:71], v[104:107]\n"
"v_mfma_f32_16x16x32_bf16 v[104:107], a[252:255], v[72:75], v[104:107]\n"
"v_mfma_f32_16x16x32_bf16 v[120:123], v[88:91], a[0:3], 0\n"
"buffer_load_dwordx4 v[52:55], v6, s[40:43], s56 offen offset:0 sc1\n"
"v_mfma_f32_16x16x32_bf16 v[120:123], v[92:95], a[4:7], v[120:123]\n"
"s_waitcnt vmcnt(11)\n"
"v_mfma_f32_16x16x32_bf16 v[120:123], v[20:23], a[8:11], v[120:123]\n"
"s_waitcnt lgkmcnt(3)\n"
"v_add_f32 v76, v76, v84\n"
"v_add_f32 v77, v77, v85\n"
"v_mfma_f32_16x16x32_bf16 v[120:123], v[24:27], a[12:15], v[120:123]\n"
"s_waitcnt vmcnt(9)\n"
"buffer_load_dwordx2 v[124:125], v9, s[40:43], s50 offen sc1\n"
"v_add_f32 v78, v78, v86\n"
"v_mfma_f32_16x16x32_bf16 v[120:123], v[28:31], a[16:19], v[120:123]\n"
"buffer_load_dwordx4 v[56:59], v6, s[40:43], s56 offen offset:1024 sc1\n"
"v_add_f32 v79, v79, v87\n"
"v_mfma_f32_16x16x32_bf16 v[120:123], v[32:35], a[20:23], v[120:123]\n"
"s_waitcnt vmcnt(8)\n"
"v_add_f32 v76, v76, v100\n"
"v_add_f32 v77, v77, v101\n"
"v_mfma_f32_16x16x32_bf16 v[120:123], v[36:39], a[24:27], v[120:123]\n"
"v_add_f32 v78, v78, v102\n"
"v_add_f32 v79, v79, v103\n"
"v_mfma_f32_16x16x32_bf16 v[120:123], v[40:43], a[28:31], v[120:123]\n"
"v_add_f32 v76, v76, v96\n"
"v_add_f32 v77, v77, v97\n"
"v_mfma_f32_16x16x32_bf16 v[84:87], v[88:91], a[32:35], 0\n"
"buffer_load_dwordx4 v[60:63], v7, s[40:43], s56 offen offset:0 sc1\n"
"v_add_f32 v78, v78, v98\n"
"v_mfma_f32_16x16x32_bf16 v[84:87], v[92:95], a[36:39], v[84:87]\n"
"ds_write_addtid_b32 v120 offset:1536\n"
"v_add_f32 v79, v79, v99\n"
"v_mfma_f32_16x16x32_bf16 v[84:87], v[20:23], a[40:43], v[84:87]\n"
"s_add_u32 s95, s50, 0xffd00000\n"
"s_and_b32 s93, s95, 0xfbffffff\n"
"ds_write_addtid_b32 v121 offset:6176\n"
"v_mfma_f32_16x16x32_bf16 v[84:87], v[24:27], a[44:47], v[84:87]\n"
"ds_write_addtid_b32 v122 offset:10880\n"
"v_cvt_pk_bf16_f32 v96, v76, v77\n"
"v_mfma_f32_16x16x32_bf16 v[84:87], v[28:31], a[48:51], v[84:87]\n"
"s_waitcnt vmcnt(7)\n"
"ds_write_addtid_b32 v123 offset:15520\n"
"buffer_load_dwordx4 v[64:67], v7, s[40:43], s56 offen offset:1024 sc1\n"
"v_mfma_f32_16x16x32_bf16 v[84:87], v[32:35], a[52:55], v[84:87]\n"
"buffer_store_dwordx2 v[0:1], v12, s[40:43], s94 offen offset:0 sc1\n"
"v_cvt_pk_bf16_f32 v97, v78, v79\n"
"v_mfma_f32_16x16x32_bf16 v[84:87], v[36:39], a[56:59], v[84:87]\n"
"s_waitcnt lgkmcnt(4)\n"
"v_add_f32 v108, v108, v112\n"
"v_add_f32 v109, v109, v113\n"
"v_mfma_f32_16x16x32_bf16 v[84:87], v[40:43], a[60:63], v[84:87]\n"
"v_add_f32 v110, v110, v114\n"
"v_add_f32 v111, v111, v115\n"
"v_mfma_f32_16x16x32_bf16 v[76:79], v[88:91], a[64:67], 0\n"
"buffer_load_dwordx4 v[68:71], v8, s[40:43], s56 offen offset:0 sc1\n"
"v_add_f32 v108, v108, v116\n"
"v_mfma_f32_16x16x32_bf16 v[76:79], v[92:95], a[68:71], v[76:79]\n"
"ds_write_addtid_b32 v84 offset:1792\n"
"v_add_f32 v109, v109, v117\n"
"v_mfma_f32_16x16x32_bf16 v[76:79], v[20:23], a[72:75], v[76:79]\n"
"ds_write_addtid_b32 v85 offset:6432\n"
"v_add_f32 v110, v110, v118\n"
"v_mfma_f32_16x16x32_bf16 v[76:79], v[24:27], a[76:79], v[76:79]\n"
"ds_write_addtid_b32 v86 offset:11136\n"
"v_add_f32 v111, v111, v119\n"
"v_mfma_f32_16x16x32_bf16 v[76:79], v[28:31], a[80:83], v[76:79]\n"
"ds_write_addtid_b32 v87 offset:15776\n"
"buffer_load_dwordx4 v[72:75], v8, s[40:43], s56 offen offset:1024 sc1\n"
"v_mfma_f32_16x16x32_bf16 v[76:79], v[32:35], a[84:87], v[76:79]\n"
"s_waitcnt vmcnt(10)\n"
"v_add_f32 v108, v108, v104\n"
"v_add_f32 v109, v109, v105\n"
"v_mfma_f32_16x16x32_bf16 v[76:79], v[36:39], a[88:91], v[76:79]\n"
"v_add_f32 v110, v110, v106\n"
"v_add_f32 v111, v111, v107\n"
"v_mfma_f32_16x16x32_bf16 v[76:79], v[40:43], a[92:95], v[76:79]\n"
"v_cvt_pk_bf16_f32 v98, v108, v109\n"
"v_cvt_pk_bf16_f32 v99, v110, v111\n"
"v_mfma_f32_16x16x32_bf16 v[84:87], v[88:91], a[96:99], 0\n"
"buffer_store_dwordx4 v[96:99], v11, s[40:43], s93 offen sc1\n"
"v_lshlrev_b32 v100, 16, v80\n"
"v_and_b32 v101, 0xffff0000, v80\n"
"v_lshlrev_b32 v102, 16, v81\n"
"v_and_b32 v103, 0xffff0000, v81\n"
"v_mfma_f32_16x16x32_bf16 v[104:107], v[96:99], v[96:99], 0\n"
"v_lshlrev_b32 v108, 16, v82\n"
"v_and_b32 v109, 0xffff0000, v82\n"
"v_mfma_f32_16x16x32_bf16 v[112:115], v[14:17], v[96:99], 0\n"
"v_lshlrev_b32 v110, 16, v83\n"
"v_and_b32 v111, 0xffff0000, v83\n"
"v_mfma_f32_16x16x32_bf16 v[84:87], v[92:95], a[100:103], v[84:87]\n"
"ds_write_addtid_b32 v76 offset:2048\n"
"v_cndmask_b32_e64 v104, v104, v105, s[60:61]\n"
"v_mfma_f32_16x16x32_bf16 v[84:87], v[20:23], a[104:107], v[84:87]\n"
"ds_write_addtid_b32 v77 offset:6688\n"
"v_cndmask_b32_e64 v106, v106, v107, s[60:61]\n"
"v_mfma_f32_16x16x32_bf16 v[84:87], v[24:27], a[108:111], v[84:87]\n"
"ds_write_addtid_b32 v78 offset:11392\n"
"v_cndmask_b32_e64 v113, v104, v106, s[62:63]\n"
"v_mfma_f32_16x16x32_bf16 v[84:87], v[28:31], a[112:115], v[84:87]\n"
"buffer_store_dwordx2 v[112:113], v13, s[40:43], s93 offen offset:0 sc1\n"
"ds_write_addtid_b32 v79 offset:16032\n"
"v_mfma_f32_16x16x32_bf16 v[84:87], v[32:35], a[116:119], v[84:87]\n"
"v_mfma_f32_16x16x32_bf16 v[84:87], v[36:39], a[120:123], v[84:87]\n"
"v_mfma_f32_16x16x32_bf16 v[84:87], v[40:43], a[124:127], v[84:87]\n"
"v_mfma_f32_16x16x32_bf16 v[76:79], v[88:91], a[128:131], 0\n"
"v_mfma_f32_16x16x32_bf16 v[76:79], v[92:95], a[132:135], v[76:79]\n"
"ds_write_addtid_b32 v84 offset:2304\n"
"v_mfma_f32_16x16x32_bf16 v[76:79], v[20:23], a[136:139], v[76:79]\n"
"ds_write_addtid_b32 v85 offset:6944\n"
"v_mfma_f32_16x16x32_bf16 v[76:79], v[24:27], a[140:143], v[76:79]\n"
"ds_write_addtid_b32 v86 offset:11648\n"
"v_mfma_f32_16x16x32_bf16 v[76:79], v[28:31], a[144:147], v[76:79]\n"
"s_add_u32 s94, s50, 0x0\n"
"s_and_b32 s94, s94, 0xfbffffff\n"
"ds_write_addtid_b32 v87 offset:16288\n"
"v_mfma_f32_16x16x32_bf16 v[76:79], v[32:35], a[148:151], v[76:79]\n"
"buffer_load_dwordx4 v[96:99], v10, s[40:43], s94 offen sc1\n"
"v_mfma_f32_16x16x32_bf16 v[76:79], v[36:39], a[152:155], v[76:79]\n"
"v_mfma_f32_16x16x32_bf16 v[76:79], v[40:43], a[156:159], v[76:79]\n"
"v_mfma_f32_16x16x32_bf16 v[80:83], v[88:91], a[160:163], 0\n"
"v_mfma_f32_16x16x32_bf16 v[80:83], v[92:95], a[164:167], v[80:83]\n"
"ds_write_addtid_b32 v76 offset:2560\n"
"v_mfma_f32_16x16x32_bf16 v[80:83], v[20:23], a[168:171], v[80:83]\n"
"ds_write_addtid_b32 v77 offset:7200\n"
"v_mfma_f32_16x16x32_bf16 v[80:83], v[24:27], a[172:175], v[80:83]\n"
"ds_write_addtid_b32 v78 offset:11904\n"
"v_mfma_f32_16x16x32_bf16 v[80:83], v[28:31], a[176:179], v[80:83]\n"
"ds_write_addtid_b32 v79 offset:16544\n"
"v_mfma_f32_16x16x32_bf16 v[80:83], v[32:35], a[180:183], v[80:83]\n"
"v_mfma_f32_16x16x32_bf16 v[80:83], v[36:39], a[184:187], v[80:83]\n"
"v_mfma_f32_16x16x32_bf16 v[80:83], v[40:43], a[188:191], v[80:83]\n"
"v_mfma_f32_16x16x32_bf16 v[100:103], a[192:195], v[88:91], v[100:103]\n"
"v_mfma_f32_16x16x32_bf16 v[100:103], a[196:199], v[92:95], v[100:103]\n"
"ds_write_addtid_b32 v80 offset:2816\n"
"v_mfma_f32_16x16x32_bf16 v[100:103], a[200:203], v[20:23], v[100:103]\n"
"ds_write_addtid_b32 v81 offset:7456\n"
"v_mfma_f32_16x16x32_bf16 v[100:103], a[204:207], v[24:27], v[100:103]\n"
"ds_write_addtid_b32 v82 offset:12160\n"
"v_mfma_f32_16x16x32_bf16 v[100:103], a[208:211], v[28:31], v[100:103]\n"
"s_waitcnt vmcnt(9)\n"
"ds_write_addtid_b32 v83 offset:16800\n"
"v_cmp_ne_u64 vcc, s[48:49], v[124:125]\n"
"v_mfma_f32_16x16x32_bf16 v[100:103], a[212:215], v[32:35], v[100:103]\n"
"v_mfma_f32_16x16x32_bf16 v[100:103], a[216:219], v[36:39], v[100:103]\n"
"s_cbranch_vccnz .LO_4s\n"
".LO_4r:\n"
"v_mfma_f32_16x16x32_bf16 v[100:103], a[220:223], v[40:43], v[100:103]\n"
"s_add_u32 s50, s50, 0x180000\n"
"s_and_b32 s56, s50, 0xfbffffff\n"
"v_mfma_f32_16x16x32_bf16 v[108:111], a[224:227], v[88:91], v[108:111]\n"
"s_waitcnt lgkmcnt(0)\n"
"buffer_load_dwordx4 v[76:79], v5, s[40:43], s56 offen offset:0 sc1\n"
"v_mfma_f32_16x16x32_bf16 v[108:111], a[228:231], v[92:95], v[108:111]\n"
"s_barrier\n"
"v_mfma_f32_16x16x32_bf16 v[108:111], a[232:235], v[20:23], v[108:111]\n"
"ds_read_b128 v[80:83], v2 offset:1536\n"
"ds_read_b128 v[84:87], v3 offset:2048\n"
"v_mfma_f32_16x16x32_bf16 v[108:111], a[236:239], v[24:27], v[108:111]\n"
"ds_read_b128 v[88:91], v4 offset:2560\n"
"ds_read_b128 v[92:95], v2 offset:1792\n"
"v_mfma_f32_16x16x32_bf16 v[108:111], a[240:243], v[28:31], v[108:111]\n"
"buffer_load_dwordx4 v[20:23], v5, s[40:43], s56 offen offset:1024 sc1\n"
"ds_read_b128 v[104:107], v3 offset:2304\n"
"v_mfma_f32_16x16x32_bf16 v[108:111], a[244:247], v[32:35], v[108:111]\n"
"ds_read_b128 v[112:115], v4 offset:2816\n"
"v_mfma_f32_16x16x32_bf16 v[108:111], a[248:251], v[36:39], v[108:111]\n"
"v_mfma_f32_16x16x32_bf16 v[108:111], a[252:255], v[40:43], v[108:111]\n"
"v_mfma_f32_16x16x32_bf16 v[116:119], v[44:47], a[0:3], 0\n"
"buffer_load_dwordx4 v[24:27], v6, s[40:43], s56 offen offset:0 sc1\n"
"v_mfma_f32_16x16x32_bf16 v[116:119], v[48:51], a[4:7], v[116:119]\n"
"s_waitcnt vmcnt(11)\n"
"v_mfma_f32_16x16x32_bf16 v[116:119], v[52:55], a[8:11], v[116:119]\n"
"s_waitcnt lgkmcnt(3)\n"
"v_add_f32 v80, v80, v84\n"
"v_add_f32 v81, v81, v85\n"
"v_mfma_f32_16x16x32_bf16 v[116:119], v[56:59], a[12:15], v[116:119]\n"
"s_waitcnt vmcnt(9)\n"
"buffer_load_dwordx2 v[120:121], v9, s[40:43], s50 offen sc1\n"
"v_add_f32 v82, v82, v86\n"
"v_mfma_f32_16x16x32_bf16 v[116:119], v[60:63], a[16:19], v[116:119]\n"
"buffer_load_dwordx4 v[28:31], v6, s[40:43], s56 offen offset:1024 sc1\n"
"v_add_f32 v83, v83, v87\n"
"v_mfma_f32_16x16x32_bf16 v[116:119], v[64:67], a[20:23], v[116:119]\n"
"s_waitcnt vmcnt(8)\n"
"v_add_f32 v80, v80, v88\n"
"v_add_f32 v81, v81, v89\n"
"v_mfma_f32_16x16x32_bf16 v[116:119], v[68:71], a[24:27], v[116:119]\n"
"v_add_f32 v82, v82, v90\n"
"v_add_f32 v83, v83, v91\n"
"v_mfma_f32_16x16x32_bf16 v[116:119], v[72:75], a[28:31], v[116:119]\n"
"v_add_f32 v80, v80, v100\n"
"v_add_f32 v81, v81, v101\n"
"v_mfma_f32_16x16x32_bf16 v[84:87], v[44:47], a[32:35], 0\n"
"buffer_load_dwordx4 v[32:35], v7, s[40:43], s56 offen offset:0 sc1\n"
"v_add_f32 v82, v82, v102\n"
"v_mfma_f32_16x16x32_bf16 v[84:87], v[48:51], a[36:39], v[84:87]\n"
"ds_write_addtid_b32 v116 offset:0\n"
"v_add_f32 v83, v83, v103\n"
"v_mfma_f32_16x16x32_bf16 v[84:87], v[52:55], a[40:43], v[84:87]\n"
"s_add_u32 s93, s50, 0xffd00000\n"
"s_and_b32 s94, s93, 0xfbffffff\n"
"ds_write_addtid_b32 v117 offset:4640\n"
"v_mfma_f32_16x16x32_bf16 v[84:87], v[56:59], a[44:47], v[84:87]\n"
"ds_write_addtid_b32 v118 offset:9344\n"
"v_cvt_pk_bf16_f32 v88, v80, v81\n"
"v_mfma_f32_16x16x32_bf16 v[84:87], v[60:63], a[48:51], v[84:87]\n"
"s_waitcnt vmcnt(7)\n"
"ds_write_addtid_b32 v119 offset:13984\n"
"buffer_load_dwordx4 v[36:39], v7, s[40:43], s56 offen offset:1024 sc1\n"
"v_mfma_f32_16x16x32_bf16 v[84:87], v[64:67], a[52:55], v[84:87]\n"
"buffer_store_dwordx2 v[0:1], v12, s[40:43], s95 offen offset:0 sc1\n"
"v_cvt_pk_bf16_f32 v89, v82, v83\n"
"v_mfma_f32_16x16x32_bf16 v[84:87], v[68:71], a[56:59], v[84:87]\n"
"s_waitcnt lgkmcnt(4)\n"
"v_add_f32 v92, v92, v104\n"
"v_add_f32 v93, v93, v105\n"
"v_mfma_f32_16x16x32_bf16 v[84:87], v[72:75], a[60:63], v[84:87]\n"
"v_add_f32 v94, v94, v106\n"
"v_add_f32 v95, v95, v107\n"
"v_mfma_f32_16x16x32_bf16 v[100:103], v[44:47], a[64:67], 0\n"
"buffer_load_dwordx4 v[40:43], v8, s[40:43], s56 offen offset:0 sc1\n"
"v_add_f32 v92, v92, v112\n"
"v_mfma_f32_16x16x32_bf16 v[100:103], v[48:51], a[68:71], v[100:103]\n"
"ds_write_addtid_b32 v84 offset:256\n"
"v_add_f32 v93, v93, v113\n"
"v_mfma_f32_16x16x32_bf16 v[100:103], v[52:55], a[72:75], v[100:103]\n"
"ds_write_addtid_b32 v85 offset:4896\n"
"v_add_f32 v94, v94, v114\n"
"v_mfma_f32_16x16x32_bf16 v[100:103], v[56:59], a[76:79], v[100:103]\n"
"ds_write_addtid_b32 v86 offset:9600\n"
"v_add_f32 v95, v95, v115\n"
"v_mfma_f32_16x16x32_bf16 v[100:103], v[60:63], a[80:83], v[100:103]\n"
"ds_write_addtid_b32 v87 offset:14240\n"
"buffer_load_dwordx4 v[80:83], v8, s[40:43], s56 offen offset:1024 sc1\n"
"v_mfma_f32_16x16x32_bf16 v[100:103], v[64:67], a[84:87], v[100:103]\n"
"s_waitcnt vmcnt(10)\n"
"v_add_f32 v92, v92, v108\n"
"v_add_f32 v93, v93, v109\n"
"v_mfma_f32_16x16x32_bf16 v[100:103], v[68:71], a[88:91], v[100:103]\n"
"v_add_f32 v94, v94, v110\n"
"v_add_f32 v95, v95, v111\n"
"v_mfma_f32_16x16x32_bf16 v[100:103], v[72:75], a[92:95], v[100:103]\n"
"v_cvt_pk_bf16_f32 v90, v92, v93\n"
"v_cvt_pk_bf16_f32 v91, v94, v95\n"
"v_mfma_f32_16x16x32_bf16 v[84:87], v[44:47], a[96:99], 0\n"
"buffer_store_dwordx4 v[88:91], v11, s[40:43], s94 offen sc1\n"
"v_lshlrev_b32 v104, 16, v96\n"
"v_and_b32 v105, 0xffff0000, v96\n"
"v_lshlrev_b32 v106, 16, v97\n"
"v_and_b32 v107, 0xffff0000, v97\n"
"v_mfma_f32_16x16x32_bf16 v[92:95], v[88:91], v[88:91], 0\n"
"v_lshlrev_b32 v108, 16, v98\n"
"v_and_b32 v109, 0xffff0000, v98\n"
"v_mfma_f32_16x16x32_bf16 v[112:115], v[14:17], v[88:91], 0\n"
"v_lshlrev_b32 v110, 16, v99\n"
"v_and_b32 v111, 0xffff0000, v99\n"
"v_mfma_f32_16x16x32_bf16 v[84:87], v[48:51], a[100:103], v[84:87]\n"
"ds_write_addtid_b32 v100 offset:512\n"
"v_cndmask_b32_e64 v92, v92, v93, s[60:61]\n"
"v_mfma_f32_16x16x32_bf16 v[84:87], v[52:55], a[104:107], v[84:87]\n"
"ds_write_addtid_b32 v101 offset:5152\n"
"v_cndmask_b32_e64 v94, v94, v95, s[60:61]\n"
"v_mfma_f32_16x16x32_bf16 v[84:87], v[56:59], a[108:111], v[84:87]\n"
"ds_write_addtid_b32 v102 offset:9856\n"
"v_cndmask_b32_e64 v113, v92, v94, s[62:63]\n"
"v_mfma_f32_16x16x32_bf16 v[84:87], v[60:63], a[112:115], v[84:87]\n"
"buffer_store_dwordx2 v[112:113], v13, s[40:43], s94 offen offset:0 sc1\n"
"ds_write_addtid_b32 v103 offset:14496\n"
"v_mfma_f32_16x16x32_bf16 v[84:87], v[64:67], a[116:119], v[84:87]\n"
"v_mfma_f32_16x16x32_bf16 v[84:87], v[68:71], a[120:123], v[84:87]\n"
"v_mfma_f32_16x16x32_bf16 v[84:87], v[72:75], a[124:127], v[84:87]\n"
"v_mfma_f32_16x16x32_bf16 v[88:91], v[44:47], a[128:131], 0\n"
"v_mfma_f32_16x16x32_bf16 v[88:91], v[48:51], a[132:135], v[88:91]\n"
"ds_write_addtid_b32 v84 offset:768\n"
"v_mfma_f32_16x16x32_bf16 v[88:91], v[52:55], a[136:139], v[88:91]\n"
"ds_write_addtid_b32 v85 offset:5408\n"
"v_mfma_f32_16x16x32_bf16 v[88:91], v[56:59], a[140:143], v[88:91]\n"
"ds_write_addtid_b32 v86 offset:10112\n"
"v_mfma_f32_16x16x32_bf16 v[88:91], v[60:63], a[144:147], v[88:91]\n"
"s_add_u32 s95, s50, 0x0\n"
"s_and_b32 s95, s95, 0xfbffffff\n"
"ds_write_addtid_b32 v87 offset:14752\n"
"v_mfma_f32_16x16x32_bf16 v[88:91], v[64:67], a[148:151], v[88:91]\n"
"buffer_load_dwordx4 v[92:95], v10, s[40:43], s95 offen sc1\n"
"v_mfma_f32_16x16x32_bf16 v[88:91], v[68:71], a[152:155], v[88:91]\n"
"v_mfma_f32_16x16x32_bf16 v[88:91], v[72:75], a[156:159], v[88:91]\n"
"v_mfma_f32_16x16x32_bf16 v[84:87], v[44:47], a[160:163], 0\n"
"v_mfma_f32_16x16x32_bf16 v[84:87], v[48:51], a[164:167], v[84:87]\n"
"ds_write_addtid_b32 v88 offset:1024\n"
"v_mfma_f32_16x16x32_bf16 v[84:87], v[52:55], a[168:171], v[84:87]\n"
"ds_write_addtid_b32 v89 offset:5664\n"
"v_mfma_f32_16x16x32_bf16 v[84:87], v[56:59], a[172:175], v[84:87]\n"
"ds_write_addtid_b32 v90 offset:10368\n"
"v_mfma_f32_16x16x32_bf16 v[84:87], v[60:63], a[176:179], v[84:87]\n"
"ds_write_addtid_b32 v91 offset:15008\n"
"v_mfma_f32_16x16x32_bf16 v[84:87], v[64:67], a[180:183], v[84:87]\n"
"v_mfma_f32_16x16x32_bf16 v[84:87], v[68:71], a[184:187], v[84:87]\n"
"v_mfma_f32_16x16x32_bf16 v[84:87], v[72:75], a[188:191], v[84:87]\n"
"v_mfma_f32_16x16x32_bf16 v[104:107], a[192:195], v[44:47], v[104:107]\n"
"v_mfma_f32_16x16x32_bf16 v[104:107], a[196:199], v[48:51], v[104:107]\n"
"ds_write_addtid_b32 v84 offset:1280\n"
"v_mfma_f32_16x16x32_bf16 v[104:107], a[200:203], v[52:55], v[104:107]\n"
"ds_write_addtid_b32 v85 offset:5920\n"
"v_mfma_f32_16x16x32_bf16 v[104:107], a[204:207], v[56:59], v[104:107]\n"
"ds_write_addtid_b32 v86 offset:10624\n"
"v_mfma_f32_16x16x32_bf16 v[104:107], a[208:211], v[60:63], v[104:107]\n"
"s_waitcnt vmcnt(9)\n"
"ds_write_addtid_b32 v87 offset:15264\n"
"v_cmp_ne_u64 vcc, s[48:49], v[120:121]\n"
"v_mfma_f32_16x16x32_bf16 v[104:107], a[212:215], v[64:67], v[104:107]\n"
"v_mfma_f32_16x16x32_bf16 v[104:107], a[216:219], v[68:71], v[104:107]\n"
"s_cbranch_vccnz .LO_1s\n"
".LO_1r:\n"
"v_mfma_f32_16x16x32_bf16 v[104:107], a[220:223], v[72:75], v[104:107]\n"
"s_add_u32 s50, s50, 0x180000\n"
"s_and_b32 s56, s50, 0xfbffffff\n"
"v_mfma_f32_16x16x32_bf16 v[108:111], a[224:227], v[44:47], v[108:111]\n"
"s_waitcnt lgkmcnt(0)\n"
"buffer_load_dwordx4 v[84:87], v5, s[40:43], s56 offen offset:0 sc1\n"
"v_mfma_f32_16x16x32_bf16 v[108:111], a[228:231], v[48:51], v[108:111]\n"
"s_barrier\n"
"v_mfma_f32_16x16x32_bf16 v[108:111], a[232:235], v[52:55], v[108:111]\n"
"ds_read_b128 v[44:47], v2 offset:0\n"
"ds_read_b128 v[88:91], v3 offset:512\n"
"v_mfma_f32_16x16x32_bf16 v[108:111], a[236:239], v[56:59], v[108:111]\n"
"ds_read_b128 v[96:99], v4 offset:1024\n"
"ds_read_b128 v[100:103], v2 offset:256\n"
"v_mfma_f32_16x16x32_bf16 v[108:111], a[240:243], v[60:63], v[108:111]\n"
"buffer_load_dwordx4 v[48:51], v5, s[40:43], s56 offen offset:1024 sc1\n"
"ds_read_b128 v[112:115], v3 offset:768\n"
"v_mfma_f32_16x16x32_bf16 v[108:111], a[244:247], v[64:67], v[108:111]\n"
"ds_read_b128 v[116:119], v4 offset:1280\n"
"v_mfma_f32_16x16x32_bf16 v[108:111], a[248:251], v[68:71], v[108:111]\n"
"v_mfma_f32_16x16x32_bf16 v[108:111], a[252:255], v[72:75], v[108:111]\n"
"v_mfma_f32_16x16x32_bf16 v[120:123], v[76:79], a[0:3], 0\n"
"buffer_load_dwordx4 v[52:55], v6, s[40:43], s56 offen offset:0 sc1\n"
"v_mfma_f32_16x16x32_bf16 v[120:123], v[20:23], a[4:7], v[120:123]\n"
"s_waitcnt vmcnt(11)\n"
"v_mfma_f32_16x16x32_bf16 v[120:123], v[24:27], a[8:11], v[120:123]\n"
"s_waitcnt lgkmcnt(3)\n"
"v_add_f32 v44, v44, v88\n"
"v_add_f32 v45, v45, v89\n"
"v_mfma_f32_16x16x32_bf16 v[120:123], v[28:31], a[12:15], v[120:123]\n"
"s_waitcnt vmcnt(9)\n"
"buffer_load_dwordx2 v[124:125], v9, s[40:43], s50 offen sc1\n"
"v_add_f32 v46, v46, v90\n"
"v_mfma_f32_16x16x32_bf16 v[120:123], v[32:35], a[16:19], v[120:123]\n"
"buffer_load_dwordx4 v[56:59], v6, s[40:43], s56 offen offset:1024 sc1\n"
"v_add_f32 v47, v47, v91\n"
"v_mfma_f32_16x16x32_bf16 v[120:123], v[36:39], a[20:23], v[120:123]\n"
"s_waitcnt vmcnt(8)\n"
"v_add_f32 v44, v44, v96\n"
"v_add_f32 v45, v45, v97\n"
"v_mfma_f32_16x16x32_bf16 v[120:123], v[40:43], a[24:27], v[120:123]\n"
"v_add_f32 v46, v46, v98\n"
"v_add_f32 v47, v47, v99\n"
"v_mfma_f32_16x16x32_bf16 v[120:123], v[80:83], a[28:31], v[120:123]\n"
"v_add_f32 v44, v44, v104\n"
"v_add_f32 v45, v45, v105\n"
"v_mfma_f32_16x16x32_bf16 v[88:91], v[76:79], a[32:35], 0\n"
"buffer_load_dwordx4 v[60:63], v7, s[40:43], s56 offen offset:0 sc1\n"
"v_add_f32 v46, v46, v106\n"
"v_mfma_f32_16x16x32_bf16 v[88:91], v[20:23], a[36:39], v[88:91]\n"
"ds_write_addtid_b32 v120 offset:1536\n"
"v_add_f32 v47, v47, v107\n"
"v_mfma_f32_16x16x32_bf16 v[88:91], v[24:27], a[40:43], v[88:91]\n"
"s_add_u32 s92, s50, 0xffd00000\n"
"s_and_b32 s95, s92, 0xfbffffff\n"
"ds_write_addtid_b32 v121 offset:6176\n"
"v_mfma_f32_16x16x32_bf16 v[88:91], v[28:31], a[44:47], v[88:91]\n"
"ds_write_addtid_b32 v122 offset:10880\n"
"v_cvt_pk_bf16_f32 v96, v44, v45\n"
"v_mfma_f32_16x16x32_bf16 v[88:91], v[32:35], a[48:51], v[88:91]\n"
"s_waitcnt vmcnt(7)\n"
"ds_write_addtid_b32 v123 offset:15520\n"
"buffer_load_dwordx4 v[64:67], v7, s[40:43], s56 offen offset:1024 sc1\n"
"v_mfma_f32_16x16x32_bf16 v[88:91], v[36:39], a[52:55], v[88:91]\n"
"buffer_store_dwordx2 v[0:1], v12, s[40:43], s93 offen offset:0 sc1\n"
"v_cvt_pk_bf16_f32 v97, v46, v47\n"
"v_mfma_f32_16x16x32_bf16 v[88:91], v[40:43], a[56:59], v[88:91]\n"
"s_waitcnt lgkmcnt(4)\n"
"v_add_f32 v100, v100, v112\n"
"v_add_f32 v101, v101, v113\n"
"v_mfma_f32_16x16x32_bf16 v[88:91], v[80:83], a[60:63], v[88:91]\n"
"v_add_f32 v102, v102, v114\n"
"v_add_f32 v103, v103, v115\n"
"v_mfma_f32_16x16x32_bf16 v[44:47], v[76:79], a[64:67], 0\n"
"buffer_load_dwordx4 v[68:71], v8, s[40:43], s56 offen offset:0 sc1\n"
"v_add_f32 v100, v100, v116\n"
"v_mfma_f32_16x16x32_bf16 v[44:47], v[20:23], a[68:71], v[44:47]\n"
"ds_write_addtid_b32 v88 offset:1792\n"
"v_add_f32 v101, v101, v117\n"
"v_mfma_f32_16x16x32_bf16 v[44:47], v[24:27], a[72:75], v[44:47]\n"
"ds_write_addtid_b32 v89 offset:6432\n"
"v_add_f32 v102, v102, v118\n"
"v_mfma_f32_16x16x32_bf16 v[44:47], v[28:31], a[76:79], v[44:47]\n"
"ds_write_addtid_b32 v90 offset:11136\n"
"v_add_f32 v103, v103, v119\n"
"v_mfma_f32_16x16x32_bf16 v[44:47], v[32:35], a[80:83], v[44:47]\n"
"ds_write_addtid_b32 v91 offset:15776\n"
"buffer_load_dwordx4 v[72:75], v8, s[40:43], s56 offen offset:1024 sc1\n"
"v_mfma_f32_16x16x32_bf16 v[44:47], v[36:39], a[84:87], v[44:47]\n"
"s_waitcnt vmcnt(10)\n"
"v_add_f32 v100, v100, v108\n"
"v_add_f32 v101, v101, v109\n"
"v_mfma_f32_16x16x32_bf16 v[44:47], v[40:43], a[88:91], v[44:47]\n"
"v_add_f32 v102, v102, v110\n"
"v_add_f32 v103, v103, v111\n"
"v_mfma_f32_16x16x32_bf16 v[44:47], v[80:83], a[92:95], v[44:47]\n"
"v_cvt_pk_bf16_f32 v98, v100, v101\n"
"v_cvt_pk_bf16_f32 v99, v102, v103\n"
"v_mfma_f32_16x16x32_bf16 v[88:91], v[76:79], a[96:99], 0\n"
"buffer_store_dwordx4 v[96:99], v11, s[40:43], s95 offen sc1\n"
"v_lshlrev_b32 v104, 16, v92\n"
"v_and_b32 v105, 0xffff0000, v92\n"
"v_lshlrev_b32 v106, 16, v93\n"
"v_and_b32 v107, 0xffff0000, v93\n"
"v_mfma_f32_16x16x32_bf16 v[100:103], v[96:99], v[96:99], 0\n"
"v_lshlrev_b32 v108, 16, v94\n"
"v_and_b32 v109, 0xffff0000, v94\n"
"v_mfma_f32_16x16x32_bf16 v[112:115], v[14:17], v[96:99], 0\n"
"v_lshlrev_b32 v110, 16, v95\n"
"v_and_b32 v111, 0xffff0000, v95\n"
"v_mfma_f32_16x16x32_bf16 v[88:91], v[20:23], a[100:103], v[88:91]\n"
"ds_write_addtid_b32 v44 offset:2048\n"
"v_cndmask_b32_e64 v100, v100, v101, s[60:61]\n"
"v_mfma_f32_16x16x32_bf16 v[88:91], v[24:27], a[104:107], v[88:91]\n"
"ds_write_addtid_b32 v45 offset:6688\n"
"v_cndmask_b32_e64 v102, v102, v103, s[60:61]\n"
"v_mfma_f32_16x16x32_bf16 v[88:91], v[28:31], a[108:111], v[88:91]\n"
"ds_write_addtid_b32 v46 offset:11392\n"
"v_cndmask_b32_e64 v113, v100, v102, s[62:63]\n"
"v_mfma_f32_16x16x32_bf16 v[88:91], v[32:35], a[112:115], v[88:91]\n"
"buffer_store_dwordx2 v[112:113], v13, s[40:43], s95 offen offset:0 sc1\n"
"ds_write_addtid_b32 v47 offset:16032\n"
"v_mfma_f32_16x16x32_bf16 v[88:91], v[36:39], a[116:119], v[88:91]\n"
"v_mfma_f32_16x16x32_bf16 v[88:91], v[40:43], a[120:123], v[88:91]\n"
"v_mfma_f32_16x16x32_bf16 v[88:91], v[80:83], a[124:127], v[88:91]\n"
"v_mfma_f32_16x16x32_bf16 v[44:47], v[76:79], a[128:131], 0\n"
"v_mfma_f32_16x16x32_bf16 v[44:47], v[20:23], a[132:135], v[44:47]\n"
"ds_write_addtid_b32 v88 offset:2304\n"
"v_mfma_f32_16x16x32_bf16 v[44:47], v[24:27], a[136:139], v[44:47]\n"
"ds_write_addtid_b32 v89 offset:6944\n"
"v_mfma_f32_16x16x32_bf16 v[44:47], v[28:31], a[140:143], v[44:47]\n"
"ds_write_addtid_b32 v90 offset:11648\n"
"v_mfma_f32_16x16x32_bf16 v[44:47], v[32:35], a[144:147], v[44:47]\n"
"s_add_u32 s95, s50, 0x0\n"
"s_and_b32 s95, s95, 0xfbffffff\n"
"ds_write_addtid_b32 v91 offset:16288\n"
"v_mfma_f32_16x16x32_bf16 v[44:47], v[36:39], a[148:151], v[44:47]\n"
"buffer_load_dwordx4 v[112:115], v10, s[40:43], s95 offen sc1\n"
"v_mfma_f32_16x16x32_bf16 v[44:47], v[40:43], a[152:155], v[44:47]\n"
"v_mfma_f32_16x16x32_bf16 v[44:47], v[80:83], a[156:159], v[44:47]\n"
"v_mfma_f32_16x16x32_bf16 v[88:91], v[76:79], a[160:163], 0\n"
"v_mfma_f32_16x16x32_bf16 v[88:91], v[20:23], a[164:167], v[88:91]\n"
"ds_write_addtid_b32 v44 offset:2560\n"
"v_mfma_f32_16x16x32_bf16 v[88:91], v[24:27], a[168:171], v[88:91]\n"
"ds_write_addtid_b32 v45 offset:7200\n"
"v_mfma_f32_16x16x32_bf16 v[88:91], v[28:31], a[172:175], v[88:91]\n"
"ds_write_addtid_b32 v46 offset:11904\n"
"v_mfma_f32_16x16x32_bf16 v[88:91], v[32:35], a[176:179], v[88:91]\n"
"ds_write_addtid_b32 v47 offset:16544\n"
"v_mfma_f32_16x16x32_bf16 v[88:91], v[36:39], a[180:183], v[88:91]\n"
"v_mfma_f32_16x16x32_bf16 v[88:91], v[40:43], a[184:187], v[88:91]\n"
"v_mfma_f32_16x16x32_bf16 v[88:91], v[80:83], a[188:191], v[88:91]\n"
"v_mfma_f32_16x16x32_bf16 v[104:107], a[192:195], v[76:79], v[104:107]\n"
"v_mfma_f32_16x16x32_bf16 v[104:107], a[196:199], v[20:23], v[104:107]\n"
"ds_write_addtid_b32 v88 offset:2816\n"
"v_mfma_f32_16x16x32_bf16 v[104:107], a[200:203], v[24:27], v[104:107]\n"
"ds_write_addtid_b32 v89 offset:7456\n"
"v_mfma_f32_16x16x32_bf16 v[104:107], a[204:207], v[28:31], v[104:107]\n"
"ds_write_addtid_b32 v90 offset:12160\n"
"v_mfma_f32_16x16x32_bf16 v[104:107], a[208:211], v[32:35], v[104:107]\n"
"s_waitcnt vmcnt(9)\n"
"ds_write_addtid_b32 v91 offset:16800\n"
"v_cmp_ne_u64 vcc, s[48:49], v[124:125]\n"
"v_mfma_f32_16x16x32_bf16 v[104:107], a[212:215], v[36:39], v[104:107]\n"
"v_mfma_f32_16x16x32_bf16 v[104:107], a[216:219], v[40:43], v[104:107]\n"
"s_cbranch_vccnz .LO_2s\n"
".LO_2r:\n"
"v_mfma_f32_16x16x32_bf16 v[104:107], a[220:223], v[80:83], v[104:107]\n"
"s_add_u32 s50, s50, 0x180000\n"
"s_and_b32 s56, s50, 0xfbffffff\n"
"v_mfma_f32_16x16x32_bf16 v[108:111], a[224:227], v[76:79], v[108:111]\n"
"s_waitcnt lgkmcnt(0)\n"
"buffer_load_dwordx4 v[88:91], v5, s[40:43], s56 offen offset:0 sc1\n"
"v_mfma_f32_16x16x32_bf16 v[108:111], a[228:231], v[20:23], v[108:111]\n"
"s_barrier\n"
"v_mfma_f32_16x16x32_bf16 v[108:111], a[232:235], v[24:27], v[108:111]\n"
"ds_read_b128 v[96:99], v2 offset:1536\n"
"ds_read_b128 v[44:47], v3 offset:2048\n"
"v_mfma_f32_16x16x32_bf16 v[108:111], a[236:239], v[28:31], v[108:111]\n"
"ds_read_b128 v[76:79], v4 offset:2560\n"
"ds_read_b128 v[100:103], v2 offset:1792\n"
"v_mfma_f32_16x16x32_bf16 v[108:111], a[240:243], v[32:35], v[108:111]\n"
"buffer_load_dwordx4 v[92:95], v5, s[40:43], s56 offen offset:1024 sc1\n"
"ds_read_b128 v[116:119], v3 offset:2304\n"
"v_mfma_f32_16x16x32_bf16 v[108:111], a[244:247], v[36:39], v[108:111]\n"
"ds_read_b128 v[120:123], v4 offset:2816\n"
"v_mfma_f32_16x16x32_bf16 v[108:111], a[248:251], v[40:43], v[108:111]\n"
"v_mfma_f32_16x16x32_bf16 v[108:111], a[252:255], v[80:83], v[108:111]\n"
"s_branch .LO_top\n"
".LO_1s:\n"
"s_nop 7\n"
"s_cmp_ge_u32 s50, s53\n"
"s_cbranch_scc1 .LO_end\n"
"s_cmp_ge_u32 s50, s52\n"
"s_cbranch_scc1 .LO_1r\n"
"s_sub_u32 s58, s50, 0x0\n"
"s_mov_b32 s59, 200000\n"
".LO_1p:\n"
"buffer_load_dwordx2 v[120:121], v9, s[40:43], s58 offen sc1\n"
"s_waitcnt vmcnt(0)\n"
"v_cmp_ne_u64 vcc, s[48:49], v[120:121]\n"
"s_cbranch_vccz .LO_1px\n"
"s_sleep 1\n"
"s_sub_u32 s59, s59, 1\n"
"s_cbranch_scc0 .LO_1p\n"
"s_branch .LO_end\n"
".LO_1px:\n"
"s_branch .LO_1r\n"
".LO_2s:\n"
"s_nop 7\n"
"s_cmp_ge_u32 s50, s53\n"
"s_cbranch_scc1 .LO_end\n"
"s_cmp_ge_u32 s50, s52\n"
"s_cbranch_scc1 .LO_2r\n"
"s_sub_u32 s58, s50, 0x0\n"
"s_mov_b32 s59, 200000\n"
".LO_2p:\n"
"buffer_load_dwordx2 v[124:125], v9, s[40:43], s58 offen sc1\n"
"s_waitcnt vmcnt(0)\n"
"v_cmp_ne_u64 vcc, s[48:49], v[124:125]\n"
"s_cbranch_vccz .LO_2px\n"
"s_sleep 1\n"
"s_sub_u32 s59, s59, 1\n"
"s_cbranch_scc0 .LO_2p\n"
"s_branch .LO_end\n"
".LO_2px:\n"
"s_branch .LO_2r\n"
".LO_3s:\n"
"s_nop 7\n"
"s_cmp_ge_u32 s50, s53\n"
"s_cbranch_scc1 .LO_end\n"
"s_cmp_ge_u32 s50, s52\n"
"s_cbranch_scc1 .LO_3r\n"
"s_sub_u32 s58, s50, 0x0\n"
"s_mov_b32 s59, 200000\n"
".LO_3p:\n"
"buffer_load_dwordx2 v[124:125], v9, s[40:43], s58 offen sc1\n"
"s_waitcnt vmcnt(0)\n"
"v_cmp_ne_u64 vcc, s[48:49], v[124:125]\n"
"s_cbranch_vccz .LO_3px\n"
"s_sleep 1\n"
"s_sub_u32 s59, s59, 1\n"
"s_cbranch_scc0 .LO_3p\n"
"s_branch .LO_end\n"
".LO_3px:\n"
"s_branch .LO_3r\n"
".LO_4s:\n"
"s_nop 7\n"
"s_cmp_ge_u32 s50, s53\n"
"s_cbranch_scc1 .LO_end\n"
"s_cmp_ge_u32 s50, s52\n"
"s_cbranch_scc1 .LO_4r\n"
"s_sub_u32 s58, s50, 0x0\n"
"s_mov_b32 s59, 200000\n"
".LO_4p:\n"
"buffer_load_dwordx2 v[124:125], v9, s[40:43], s58 offen sc1\n"
"s_waitcnt vmcnt(0)\n"
"v_cmp_ne_u64 vcc, s[48:49], v[124:125]\n"
"s_cbranch_vccz .LO_4px\n"
"s_sleep 1\n"
"s_sub_u32 s59, s59, 1\n"
"s_cbranch_scc0 .LO_4p\n"
"s_branch .LO_end\n"
".LO_4px:\n"
"s_branch .LO_4r\n"
".LO_end:\n"
"s_waitcnt vmcnt(0) lgkmcnt(0)\n"
"s_endpgm\n"
      :: "{v2}"((unsigned)(lds0 + ((wid + 3) & 3) * 18592 + trp)), "{v3}"((unsigned)(lds0 + ((wid + 2) & 3) * 18592 + trp)), "{v4}"((unsigned)(lds0 + ((wid + 1) & 3) * 18592 + trp)), "{v5}"((unsigned)(lane * 16 + wid * 8192 + ((0 + rot) & 3) * 2048 + 32768)), "{v6}"((unsigned)(lane * 16 + wid * 8192 + ((1 + rot) & 3) * 2048 + 32768)), "{v7}"((unsigned)(lane * 16 + wid * 8192 + ((2 + rot) & 3) * 2048 + 32768)), "{v8}"((unsigned)(lane * 16 + wid * 8192 + ((3 + rot) & 3) * 2048 + 32768)), "{v9}"((unsigned)((wid * 8 + (lane & 7)) * 8 + 352512 + O_GL * 1572864)), "{v10}"((unsigned)(lane * 16 + pair * 1024 + 0)), "{v11}"((unsigned)(lane * 16 + pair * 1024 + 65536)), "{v12}"((unsigned)(lane ? DROP : pair * 8 + 352768)), "{v13}"((unsigned)(lg == (li >> 2) ? pair * 128 + li * 8 + 344064 : DROP)), "{v14}"((unsigned)(0x3f803f80)), "{v15}"((unsigned)(0x3f803f80)), "{v16}"((unsigned)(0x3f803f80)), "{v17}"((unsigned)(0x3f803f80)), "{v20}"((unsigned)(vT + tsw)), "{v21}"((unsigned)(vT + (tsw ^ 8))), "{v22}"((unsigned)(vD + ((0 + rot) & 3) * 131072)), "{v23}"((unsigned)(vD + ((1 + rot) & 3) * 131072)), "{v24}"((unsigned)(vD + ((2 + rot) & 3) * 131072)), "{v25}"((unsigned)(vD + ((3 + rot) & 3) * 131072)), "{s[40:43]}"(rS), "{s[44:47]}"(rW), "{s[48:49]}"(wsp), "{s50}"((unsigned)(sA)), "{s51}"((unsigned)(sLo)), "{s52}"((unsigned)(sHi)), "{s53}"((unsigned)(sEx)), "{s54}"((unsigned)(sEnt)), "{s55}"((unsigned)(sLd)), "{s56}"((unsigned)(sA & 0xfbffffff)), "{s57}"((unsigned)(lds0 + wid * 18592)), "{s[60:61]}"(0xAAAAAAAAAAAAAAAAull), "{s[62:63]}"(0xCCCCCCCCCCCCCCCCull), "{s64}"((unsigned)(sW0 + 64 * ((wid + 1) & 3))), "{s65}"((unsigned)(sW0 + 64 * ((wid + 2) & 3))), "{s66}"((unsigned)(sW0 + 64 * ((wid + 3) & 3))), "{s67}"((unsigned)(sW0 + 64 * ((wid + 4) & 3)))
      : "v255", "a255", "s95", "vcc", "scc", "memory");

  } else if (role == 2) {
    const unsigned sEx = sHi + F_EX * 3 * RS, sEnt = -wid & 3, tsw = 0, pair = idx * 4 + wid, rot = KR * (set ? set & 2 : 1);
    const unsigned sW0 = wid * 256 * 7168 + idx * 256, vD = (lane >> 2) * 7168 + (lane & 3) * 16;
    const u32x4 rW = desc(a.w1, 1024 * 7168), bp = *(const u32x4*)(a.b1 + pair * 32 + lg * 8);
    const unsigned bia[8] = {bp[0] << 16, bp[0] & 0xffff0000u, bp[1] << 16, bp[1] & 0xffff0000u, bp[2] << 16, bp[2] & 0xffff0000u, bp[3] << 16, bp[3] & 0xffff0000u};
    asm volatile(
"s_nop 4\n"
"v_mov_b32 v186, 0\n"
"global_load_dwordx2 v[0:1], v186, s[48:49] sc1\n"
"v_mov_b32 v188, 0x3f803f80\n"
"v_mov_b32 v189, 0x3f803f80\n"
"v_mov_b32 v190, 0x3f803f80\n"
"v_mov_b32 v191, 0x3f803f80\n"
"s_add_u32 m0, s55, 0\n"
"s_add_u32 s58, s61, 0\n"
"buffer_load_dwordx4 v38, s[44:47], s58 offen lds\n"
"s_add_u32 m0, s55, 1024\n"
"s_add_u32 s58, s61, 114688\n"
"buffer_load_dwordx4 v38, s[44:47], s58 offen lds\n"
"s_add_u32 m0, s55, 2048\n"
"s_add_u32 s58, s62, 0\n"
"buffer_load_dwordx4 v38, s[44:47], s58 offen lds\n"
"s_add_u32 m0, s55, 3072\n"
"s_add_u32 s58, s62, 114688\n"
"buffer_load_dwordx4 v38, s[44:47], s58 offen lds\n"
"s_add_u32 m0, s55, 4096\n"
"s_add_u32 s58, s63, 0\n"
"buffer_load_dwordx4 v38, s[44:47], s58 offen lds\n"
"s_add_u32 m0, s55, 5120\n"
"s_add_u32 s58, s63, 114688\n"
"buffer_load_dwordx4 v38, s[44:47], s58 offen lds\n"
"s_add_u32 m0, s55, 6144\n"
"s_add_u32 s58, s64, 0\n"
"buffer_load_dwordx4 v38, s[44:47], s58 offen lds\n"
"s_add_u32 m0, s55, 7168\n"
"s_add_u32 s58, s64, 114688\n"
"buffer_load_dwordx4 v38, s[44:47], s58 offen lds\n"
"s_add_u32 m0, s55, 8192\n"
"s_add_u32 s58, s61, 229376\n"
"buffer_load_dwordx4 v38, s[44:47], s58 offen lds\n"
"s_add_u32 m0, s55, 9216\n"
"s_add_u32 s58, s61, 344064\n"
"buffer_load_dwordx4 v38, s[44:47], s58 offen lds\n"
"s_add_u32 m0, s55, 10240\n"
"s_add_u32 s58, s62, 229376\n"
"buffer_load_dwordx4 v38, s[44:47], s58 offen lds\n"
"s_add_u32 m0, s55, 11264\n"
"s_add_u32 s58, s62, 344064\n"
"buffer_load_dwordx4 v38, s[44:47], s58 offen lds\n"
"s_add_u32 m0, s55, 12288\n"
"s_add_u32 s58, s63, 229376\n"
"buffer_load_dwordx4 v38, s[44:47], s58 offen lds\n"
"s_add_u32 m0, s55, 13312\n"
"s_add_u32 s58, s63, 344064\n"
"buffer_load_dwordx4 v38, s[44:47], s58 offen lds\n"
"s_add_u32 m0, s55, 14336\n"
"s_add_u32 s58, s64, 229376\n"
"buffer_load_dwordx4 v38, s[44:47], s58 offen lds\n"
"s_add_u32 m0, s55, 15360\n"
"s_add_u32 s58, s64, 344064\n"
"buffer_load_dwordx4 v38, s[44:47], s58 offen lds\n"
"s_add_u32 m0, s55, 16384\n"
"s_add_u32 s58, s61, 0\n"
"buffer_load_dwordx4 v39, s[44:47], s58 offen lds\n"
"s_add_u32 m0, s55, 17408\n"
"s_add_u32 s58, s61, 114688\n"
"buffer_load_dwordx4 v39, s[44:47], s58 offen lds\n"
"s_add_u32 m0, s55, 18432\n"
"s_add_u32 s58, s62, 0\n"
"buffer_load_dwordx4 v39, s[44:47], s58 offen lds\n"
"s_add_u32 m0, s55, 19456\n"
"s_add_u32 s58, s62, 114688\n"
"buffer_load_dwordx4 v39, s[44:47], s58 offen lds\n"
"s_add_u32 m0, s55, 20480\n"
"s_add_u32 s58, s63, 0\n"
"buffer_load_dwordx4 v39, s[44:47], s58 offen lds\n"
"s_add_u32 m0, s55, 21504\n"
"s_add_u32 s58, s63, 114688\n"
"buffer_load_dwordx4 v39, s[44:47], s58 offen lds\n"
"s_add_u32 m0, s55, 22528\n"
"s_add_u32 s58, s64, 0\n"
"buffer_load_dwordx4 v39, s[44:47], s58 offen lds\n"
"s_add_u32 m0, s55, 23552\n"
"s_add_u32 s58, s64, 114688\n"
"buffer_load_dwordx4 v39, s[44:47], s58 offen lds\n"
"s_add_u32 m0, s55, 24576\n"
"s_add_u32 s58, s61, 229376\n"
"buffer_load_dwordx4 v39, s[44:47], s58 offen lds\n"
"s_add_u32 m0, s55, 25600\n"
"s_add_u32 s58, s61, 344064\n"
"buffer_load_dwordx4 v39, s[44:47], s58 offen lds\n"
"s_add_u32 m0, s55, 26624\n"
"s_add_u32 s58, s62, 229376\n"
"buffer_load_dwordx4 v39, s[44:47], s58 offen lds\n"
"s_add_u32 m0, s55, 27648\n"
"s_add_u32 s58, s62, 344064\n"
"buffer_load_dwordx4 v39, s[44:47], s58 offen lds\n"
"s_add_u32 m0, s55, 28672\n"
"s_add_u32 s58, s63, 229376\n"
"buffer_load_dwordx4 v39, s[44:47], s58 offen lds\n"
"s_add_u32 m0, s55, 29696\n"
"s_add_u32 s58, s63, 344064\n"
"buffer_load_dwordx4 v39, s[44:47], s58 offen lds\n"
"s_add_u32 m0, s55, 30720\n"
"s_add_u32 s58, s64, 229376\n"
"buffer_load_dwordx4 v39, s[44:47], s58 offen lds\n"
"s_add_u32 m0, s55, 31744\n"
"s_add_u32 s58, s64, 344064\n"
"buffer_load_dwordx4 v39, s[44:47], s58 offen lds\n"
"s_add_u32 m0, s55, 32768\n"
"s_add_u32 s58, s61, 0\n"
"buffer_load_dwordx4 v40, s[44:47], s58 offen lds\n"
"s_add_u32 m0, s55, 33792\n"
"s_add_u32 s58, s61, 114688\n"
"buffer_load_dwordx4 v40, s[44:47], s58 offen lds\n"
"s_add_u32 m0, s55, 34816\n"
"s_add_u32 s58, s62, 0\n"
"buffer_load_dwordx4 v40, s[44:47], s58 offen lds\n"
"s_add_u32 m0, s55, 35840\n"
"s_add_u32 s58, s62, 114688\n"
"buffer_load_dwordx4 v40, s[44:47], s58 offen lds\n"
"s_add_u32 m0, s55, 36864\n"
"s_add_u32 s58, s63, 0\n"
"buffer_load_dwordx4 v40, s[44:47], s58 offen lds\n"
"s_add_u32 m0, s55, 37888\n"
"s_add_u32 s58, s63, 114688\n"
"buffer_load_dwordx4 v40, s[44:47], s58 offen lds\n"
"s_add_u32 m0, s55, 38912\n"
"s_add_u32 s58, s64, 0\n"
"buffer_load_dwordx4 v40, s[44:47], s58 offen lds\n"
"s_add_u32 m0, s55, 39936\n"
"s_add_u32 s58, s64, 114688\n"
"buffer_load_dwordx4 v40, s[44:47], s58 offen lds\n"
"s_waitcnt vmcnt(38)\n"
"ds_read_b64_tr_b16 a[0:1], v36 offset:0\n"
"ds_read_b64_tr_b16 a[2:3], v36 offset:256\n"
"ds_read_b64_tr_b16 a[32:33], v37 offset:0\n"
"ds_read_b64_tr_b16 a[34:35], v37 offset:256\n"
"v_add_u32 v0, 2, v0\n"
"v_add_u32 v1, 0x9e3779b9, v1\n"
"v_or_b32 v0, 1, v0\n"
"v_readfirstlane_b32 s49, v1\n"
"v_readfirstlane_b32 s48, v0\n"
"s_waitcnt vmcnt(36)\n"
"ds_read_b64_tr_b16 a[64:65], v36 offset:2048\n"
"ds_read_b64_tr_b16 a[66:67], v36 offset:2304\n"
"ds_read_b64_tr_b16 a[96:97], v37 offset:2048\n"
"ds_read_b64_tr_b16 a[98:99], v37 offset:2304\n"
"s_waitcnt lgkmcnt(4)\n"
"s_add_u32 m0, s55, 0\n"
"s_add_u32 s58, s61, 229376\n"
"buffer_load_dwordx4 v40, s[44:47], s58 offen lds\n"
"s_add_u32 m0, s55, 1024\n"
"s_add_u32 s58, s61, 344064\n"
"buffer_load_dwordx4 v40, s[44:47], s58 offen lds\n"
"v_mfma_f32_16x16x32_bf16 v[192:195], v[188:191], a[0:3], 0\n"
"v_mfma_f32_16x16x32_bf16 v[196:199], v[188:191], a[32:35], 0\n"
"s_waitcnt vmcnt(36)\n"
"ds_read_b64_tr_b16 a[128:129], v36 offset:4096\n"
"ds_read_b64_tr_b16 a[130:131], v36 offset:4352\n"
"ds_read_b64_tr_b16 a[160:161], v37 offset:4096\n"
"ds_read_b64_tr_b16 a[162:163], v37 offset:4352\n"
"s_waitcnt lgkmcnt(4)\n"
"s_add_u32 m0, s55, 2048\n"
"s_add_u32 s58, s62, 229376\n"
"buffer_load_dwordx4 v40, s[44:47], s58 offen lds\n"
"s_add_u32 m0, s55, 3072\n"
"s_add_u32 s58, s62, 344064\n"
"buffer_load_dwordx4 v40, s[44:47], s58 offen lds\n"
"v_mfma_f32_16x16x32_bf16 v[200:203], v[188:191], a[64:67], 0\n"
"v_mfma_f32_16x16x32_bf16 v[204:207], v[188:191], a[96:99], 0\n"
"s_waitcnt vmcnt(36)\n"
"ds_read_b64_tr_b16 a[192:193], v36 offset:6144\n"
"ds_read_b64_tr_b16 a[194:195], v36 offset:6400\n"
"ds_read_b64_tr_b16 a[224:225], v37 offset:6144\n"
"ds_read_b64_tr_b16 a[226:227], v37 offset:6400\n"
"s_waitcnt lgkmcnt(4)\n"
"s_add_u32 m0, s55, 4096\n"
"s_add_u32 s58, s63, 229376\n"
"buffer_load_dwordx4 v40, s[44:47], s58 offen lds\n"
"s_add_u32 m0, s55, 5120\n"
"s_add_u32 s58, s63, 344064\n"
"buffer_load_dwordx4 v40, s[44:47], s58 offen lds\n"
"v_mfma_f32_16x16x32_bf16 v[208:211], v[188:191], a[128:131], 0\n"
"v_mfma_f32_16x16x32_bf16 v[212:215], v[188:191], a[160:163], 0\n"
"s_waitcnt vmcnt(36)\n"
"ds_read_b64_tr_b16 a[4:5], v36 offset:8192\n"
"ds_read_b64_tr_b16 a[6:7], v36 offset:8448\n"
"ds_read_b64_tr_b16 a[36:37], v37 offset:8192\n"
"ds_read_b64_tr_b16 a[38:39], v37 offset:8448\n"
"s_waitcnt lgkmcnt(4)\n"
"s_add_u32 m0, s55, 6144\n"
"s_add_u32 s58, s64, 229376\n"
"buffer_load_dwordx4 v40, s[44:47], s58 offen lds\n"
"s_add_u32 m0, s55, 7168\n"
"s_add_u32 s58, s64, 344064\n"
"buffer_load_dwordx4 v40, s[44:47], s58 offen lds\n"
"v_mfma_f32_16x16x32_bf16 v[216:219], v[188:191], a[192:195], 0\n"
"v_mfma_f32_16x16x32_bf16 v[220:223], v[188:191], a[224:227], 0\n"
"s_waitcnt vmcnt(36)\n"
"ds_read_b64_tr_b16 a[68:69], v36 offset:10240\n"
"ds_read_b64_tr_b16 a[70:71], v36 offset:10496\n"
"ds_read_b64_tr_b16 a[100:101], v37 offset:10240\n"
"ds_read_b64_tr_b16 a[102:103], v37 offset:10496\n"
"s_waitcnt lgkmcnt(4)\n"
"s_add_u32 m0, s55, 8192\n"
"s_add_u32 s58, s61, 0\n"
"buffer_load_dwordx4 v41, s[44:47], s58 offen lds\n"
"s_add_u32 m0, s55, 9216\n"
"s_add_u32 s58, s61, 114688\n"
"buffer_load_dwordx4 v41, s[44:47], s58 offen lds\n"
"v_mfma_f32_16x16x32_bf16 v[192:195], v[188:191], a[4:7], v[192:195]\n"
"v_mfma_f32_16x16x32_bf16 v[196:199], v[188:191], a[36:39], v[196:199]\n"
"s_waitcnt vmcnt(36)\n"
"ds_read_b64_tr_b16 a[132:133], v36 offset:12288\n"
"ds_read_b64_tr_b16 a[134:135], v36 offset:12544\n"
"ds_read_b64_tr_b16 a[164:165], v37 offset:12288\n"
"ds_read_b64_tr_b16 a[166:167], v37 offset:12544\n"
"s_waitcnt lgkmcnt(4)\n"
"s_add_u32 m0, s55, 10240\n"
"s_add_u32 s58, s62, 0\n"
"buffer_load_dwordx4 v41, s[44:47], s58 offen lds\n"
"s_add_u32 m0, s55, 11264\n"
"s_add_u32 s58, s62, 114688\n"
"buffer_load_dwordx4 v41, s[44:47], s58 offen lds\n"
"v_mfma_f32_16x16x32_bf16 v[200:203], v[188:191], a[68:71], v[200:203]\n"
"v_mfma_f32_16x16x32_bf16 v[204:207], v[188:191], a[100:103], v[204:207]\n"
"s_waitcnt vmcnt(36)\n"
"ds_read_b64_tr_b16 a[196:197], v36 offset:14336\n"
"ds_read_b64_tr_b16 a[198:199], v36 offset:14592\n"
"ds_read_b64_tr_b16 a[228:229], v37 offset:14336\n"
"ds_read_b64_tr_b16 a[230:231], v37 offset:14592\n"
"s_waitcnt lgkmcnt(4)\n"
"s_add_u32 m0, s55, 12288\n"
"s_add_u32 s58, s63, 0\n"
"buffer_load_dwordx4 v41, s[44:47], s58 offen lds\n"
"s_add_u32 m0, s55, 13312\n"
"s_add_u32 s58, s63, 114688\n"
"buffer_load_dwordx4 v41, s[44:47], s58 offen lds\n"
"v_mfma_f32_16x16x32_bf16 v[208:211], v[188:191], a[132:135], v[208:211]\n"
"v_mfma_f32_16x16x32_bf16 v[212:215], v[188:191], a[164:167], v[212:215]\n"
"s_waitcnt vmcnt(36)\n"
"ds_read_b64_tr_b16 a[8:9], v36 offset:16384\n"
"ds_read_b64_tr_b16 a[10:11], v36 offset:16640\n"
"ds_read_b64_tr_b16 a[40:41], v37 offset:16384\n"
"ds_read_b64_tr_b16 a[42:43], v37 offset:16640\n"
"s_waitcnt lgkmcnt(4)\n"
"s_add_u32 m0, s55, 14336\n"
"s_add_u32 s58, s64, 0\n"
"buffer_load_dwordx4 v41, s[44:47], s58 offen lds\n"
"s_add_u32 m0, s55, 15360\n"
"s_add_u32 s58, s64, 114688\n"
"buffer_load_dwordx4 v41, s[44:47], s58 offen lds\n"
"v_mfma_f32_16x16x32_bf16 v[216:219], v[188:191], a[196:199], v[216:219]\n"
"v_mfma_f32_16x16x32_bf16 v[220:223], v[188:191], a[228:231], v[220:223]\n"
"s_waitcnt vmcnt(36)\n"
"ds_read_b64_tr_b16 a[72:73], v36 offset:18432\n"
"ds_read_b64_tr_b16 a[74:75], v36 offset:18688\n"
"ds_read_b64_tr_b16 a[104:105], v37 offset:18432\n"
"ds_read_b64_tr_b16 a[106:107], v37 offset:18688\n"
"s_waitcnt lgkmcnt(4)\n"
"s_add_u32 m0, s55, 16384\n"
"s_add_u32 s58, s61, 229376\n"
"buffer_load_dwordx4 v41, s[44:47], s58 offen lds\n"
"s_add_u32 m0, s55, 17408\n"
"s_add_u32 s58, s61, 344064\n"
"buffer_load_dwordx4 v41, s[44:47], s58 offen lds\n"
"v_mfma_f32_16x16x32_bf16 v[192:195], v[188:191], a[8:11], v[192:195]\n"
"v_mfma_f32_16x16x32_bf16 v[196:199], v[188:191], a[40:43], v[196:199]\n"
"s_waitcnt vmcnt(36)\n"
"ds_read_b64_tr_b16 a[136:137], v36 offset:20480\n"
"ds_read_b64_tr_b16 a[138:139], v36 offset:20736\n"
"ds_read_b64_tr_b16 a[168:169], v37 offset:20480\n"
"ds_read_b64_tr_b16 a[170:171], v37 offset:20736\n"
"s_waitcnt lgkmcnt(4)\n"
"s_add_u32 m0, s55, 18432\n"
"s_add_u32 s58, s62, 229376\n"
"buffer_load_dwordx4 v41, s[44:47], s58 offen lds\n"
"s_add_u32 m0, s55, 19456\n"
"s_add_u32 s58, s62, 344064\n"
"buffer_load_dwordx4 v41, s[44:47], s58 offen lds\n"
"v_mfma_f32_16x16x32_bf16 v[200:203], v[188:191], a[72:75], v[200:203]\n"
"v_mfma_f32_16x16x32_bf16 v[204:207], v[188:191], a[104:107], v[204:207]\n"
"s_waitcnt vmcnt(36)\n"
"ds_read_b64_tr_b16 a[200:201], v36 offset:22528\n"
"ds_read_b64_tr_b16 a[202:203], v36 offset:22784\n"
"ds_read_b64_tr_b16 a[232:233], v37 offset:22528\n"
"ds_read_b64_tr_b16 a[234:235], v37 offset:22784\n"
"s_waitcnt lgkmcnt(4)\n"
"s_add_u32 m0, s55, 20480\n"
"s_add_u32 s58, s63, 229376\n"
"buffer_load_dwordx4 v41, s[44:47], s58 offen lds\n"
"s_add_u32 m0, s55, 21504\n"
"s_add_u32 s58, s63, 344064\n"
"buffer_load_dwordx4 v41, s[44:47], s58 offen lds\n"
"v_mfma_f32_16x16x32_bf16 v[208:211], v[188:191], a[136:139], v[208:211]\n"
"v_mfma_f32_16x16x32_bf16 v[212:215], v[188:191], a[168:171], v[212:215]\n"
"s_waitcnt vmcnt(36)\n"
"ds_read_b64_tr_b16 a[12:13], v36 offset:24576\n"
"ds_read_b64_tr_b16 a[14:15], v36 offset:24832\n"
"ds_read_b64_tr_b16 a[44:45], v37 offset:24576\n"
"ds_read_b64_tr_b16 a[46:47], v37 offset:24832\n"
"s_waitcnt lgkmcnt(4)\n"
"s_add_u32 m0, s55, 22528\n"
"s_add_u32 s58, s64, 229376\n"
"buffer_load_dwordx4 v41, s[44:47], s58 offen lds\n"
"s_add_u32 m0, s55, 23552\n"
"s_add_u32 s58, s64, 344064\n"
"buffer_load_dwordx4 v41, s[44:47], s58 offen lds\n"
"v_mfma_f32_16x16x32_bf16 v[216:219], v[188:191], a[200:203], v[216:219]\n"
"v_mfma_f32_16x16x32_bf16 v[220:223], v[188:191], a[232:235], v[220:223]\n"
"s_waitcnt vmcnt(36)\n"
"ds_read_b64_tr_b16 a[76:77], v36 offset:26624\n"
"ds_read_b64_tr_b16 a[78:79], v36 offset:26880\n"
"ds_read_b64_tr_b16 a[108:109], v37 offset:26624\n"
"ds_read_b64_tr_b16 a[110:111], v37 offset:26880\n"
"s_waitcnt lgkmcnt(4)\n"
"v_mfma_f32_16x16x32_bf16 v[192:195], v[188:191], a[12:15], v[192:195]\n"
"v_mfma_f32_16x16x32_bf16 v[196:199], v[188:191], a[44:47], v[196:199]\n"
"s_waitcnt vmcnt(34)\n"
"ds_read_b64_tr_b16 a[140:141], v36 offset:28672\n"
"ds_read_b64_tr_b16 a[142:143], v36 offset:28928\n"
"ds_read_b64_tr_b16 a[172:173], v37 offset:28672\n"
"ds_read_b64_tr_b16 a[174:175], v37 offset:28928\n"
"s_waitcnt lgkmcnt(4)\n"
"v_mfma_f32_16x16x32_bf16 v[200:203], v[188:191], a[76:79], v[200:203]\n"
"v_mfma_f32_16x16x32_bf16 v[204:207], v[188:191], a[108:111], v[204:207]\n"
"s_waitcnt vmcnt(32)\n"
"ds_read_b64_tr_b16 a[204:205], v36 offset:30720\n"
"ds_read_b64_tr_b16 a[206:207], v36 offset:30976\n"
"ds_read_b64_tr_b16 a[236:237], v37 offset:30720\n"
"ds_read_b64_tr_b16 a[238:239], v37 offset:30976\n"
"s_waitcnt lgkmcnt(4)\n"
"v_mfma_f32_16x16x32_bf16 v[208:211], v[188:191], a[140:143], v[208:211]\n"
"v_mfma_f32_16x16x32_bf16 v[212:215], v[188:191], a[172:175], v[212:215]\n"
"s_waitcnt vmcnt(30)\n"
"ds_read_b64_tr_b16 a[16:17], v36 offset:32768\n"
"ds_read_b64_tr_b16 a[18:19], v36 offset:33024\n"
"ds_read_b64_tr_b16 a[48:49], v37 offset:32768\n"
"ds_read_b64_tr_b16 a[50:51], v37 offset:33024\n"
"s_waitcnt lgkmcnt(4)\n"
"v_mfma_f32_16x16x32_bf16 v[216:219], v[188:191], a[204:207], v[216:219]\n"
"v_mfma_f32_16x16x32_bf16 v[220:223], v[188:191], a[236:239], v[220:223]\n"
"s_waitcnt vmcnt(28)\n"
"ds_read_b64_tr_b16 a[80:81], v36 offset:34816\n"
"ds_read_b64_tr_b16 a[82:83], v36 offset:35072\n"
"ds_read_b64_tr_b16 a[112:113], v37 offset:34816\n"
"ds_read_b64_tr_b16 a[114:115], v37 offset:35072\n"
"s_waitcnt lgkmcnt(4)\n"
"v_mfma_f32_16x16x32_bf16 v[192:195], v[188:191], a[16:19], v[192:195]\n"
"v_mfma_f32_16x16x32_bf16 v[196:199], v[188:191], a[48:51], v[196:199]\n"
"s_waitcnt vmcnt(26)\n"
"ds_read_b64_tr_b16 a[144:145], v36 offset:36864\n"
"ds_read_b64_tr_b16 a[146:147], v36 offset:37120\n"
"ds_read_b64_tr_b16 a[176:177], v37 offset:36864\n"
"ds_read_b64_tr_b16 a[178:179], v37 offset:37120\n"
"s_waitcnt lgkmcnt(4)\n"
"v_mfma_f32_16x16x32_bf16 v[200:203], v[188:191], a[80:83], v[200:203]\n"
"v_mfma_f32_16x16x32_bf16 v[204:207], v[188:191], a[112:115], v[204:207]\n"
"s_waitcnt vmcnt(24)\n"
"ds_read_b64_tr_b16 a[208:209], v36 offset:38912\n"
"ds_read_b64_tr_b16 a[210:211], v36 offset:39168\n"
"ds_read_b64_tr_b16 a[240:241], v37 offset:38912\n"
"ds_read_b64_tr_b16 a[242:243], v37 offset:39168\n"
"s_waitcnt lgkmcnt(4)\n"
"v_mfma_f32_16x16x32_bf16 v[208:211], v[188:191], a[144:147], v[208:211]\n"
"v_mfma_f32_16x16x32_bf16 v[212:215], v[188:191], a[176:179], v[212:215]\n"
"s_waitcnt vmcnt(22)\n"
"ds_read_b64_tr_b16 a[20:21], v36 offset:0\n"
"ds_read_b64_tr_b16 a[22:23], v36 offset:256\n"
"ds_read_b64_tr_b16 a[52:53], v37 offset:0\n"
"ds_read_b64_tr_b16 a[54:55], v37 offset:256\n"
"s_waitcnt lgkmcnt(4)\n"
"v_mfma_f32_16x16x32_bf16 v[216:219], v[188:191], a[208:211], v[216:219]\n"
"v_mfma_f32_16x16x32_bf16 v[220:223], v[188:191], a[240:243], v[220:223]\n"
"s_waitcnt vmcnt(20)\n"
"ds_read_b64_tr_b16 a[84:85], v36 offset:2048\n"
"ds_read_b64_tr_b16 a[86:87], v36 offset:2304\n"
"ds_read_b64_tr_b16 a[116:117], v37 offset:2048\n"
"ds_read_b64_tr_b16 a[118:119], v37 offset:2304\n"
"s_waitcnt lgkmcnt(4)\n"
"v_mfma_f32_16x16x32_bf16 v[192:195], v[188:191], a[20:23], v[192:195]\n"
"v_mfma_f32_16x16x32_bf16 v[196:199], v[188:191], a[52:55], v[196:199]\n"
"s_waitcnt vmcnt(18)\n"
"ds_read_b64_tr_b16 a[148:149], v36 offset:4096\n"
"ds_read_b64_tr_b16 a[150:151], v36 offset:4352\n"
"ds_read_b64_tr_b16 a[180:181], v37 offset:4096\n"
"ds_read_b64_tr_b16 a[182:183], v37 offset:4352\n"
"s_waitcnt lgkmcnt(4)\n"
"v_mfma_f32_16x16x32_bf16 v[200:203], v[188:191], a[84:87], v[200:203]\n"
"v_mfma_f32_16x16x32_bf16 v[204:207], v[188:191], a[116:119], v[204:207]\n"
"s_waitcnt vmcnt(16)\n"
"ds_read_b64_tr_b16 a[212:213], v36 offset:6144\n"
"ds_read_b64_tr_b16 a[214:215], v36 offset:6400\n"
"ds_read_b64_tr_b16 a[244:245], v37 offset:6144\n"
"ds_read_b64_tr_b16 a[246:247], v37 offset:6400\n"
"s_waitcnt lgkmcnt(4)\n"
"v_mfma_f32_16x16x32_bf16 v[208:211], v[188:191], a[148:151], v[208:211]\n"
"v_mfma_f32_16x16x32_bf16 v[212:215], v[188:191], a[180:183], v[212:215]\n"
"s_waitcnt vmcnt(14)\n"
"ds_read_b64_tr_b16 a[24:25], v36 offset:8192\n"
"ds_read_b64_tr_b16 a[26:27], v36 offset:8448\n"
"ds_read_b64_tr_b16 a[56:57], v37 offset:8192\n"
"ds_read_b64_tr_b16 a[58:59], v37 offset:8448\n"
"s_waitcnt lgkmcnt(4)\n"
"v_mfma_f32_16x16x32_bf16 v[216:219], v[188:191], a[212:215], v[216:219]\n"
"v_mfma_f32_16x16x32_bf16 v[220:223], v[188:191], a[244:247], v[220:223]\n"
"s_waitcnt vmcnt(12)\n"
"ds_read_b64_tr_b16 a[88:89], v36 offset:10240\n"
"ds_read_b64_tr_b16 a[90:91], v36 offset:10496\n"
"ds_read_b64_tr_b16 a[120:121], v37 offset:10240\n"
"ds_read_b64_tr_b16 a[122:123], v37 offset:10496\n"
"s_waitcnt lgkmcnt(4)\n"
"v_mfma_f32_16x16x32_bf16 v[192:195], v[188:191], a[24:27], v[192:195]\n"
"v_mfma_f32_16x16x32_bf16 v[196:199], v[188:191], a[56:59], v[196:199]\n"
"s_waitcnt vmcnt(10)\n"
"ds_read_b64_tr_b16 a[152:153], v36 offset:12288\n"
"ds_read_b64_tr_b16 a[154:155], v36 offset:12544\n"
"ds_read_b64_tr_b16 a[184:185], v37 offset:12288\n"
"ds_read_b64_tr_b16 a[186:187], v37 offset:12544\n"
"s_waitcnt lgkmcnt(4)\n"
"v_mfma_f32_16x16x32_bf16 v[200:203], v[188:191], a[88:91], v[200:203]\n"
"v_mfma_f32_16x16x32_bf16 v[204:207], v[188:191], a[120:123], v[204:207]\n"
"s_waitcnt vmcnt(8)\n"
"ds_read_b64_tr_b16 a[216:217], v36 offset:14336\n"
"ds_read_b64_tr_b16 a[218:219], v36 offset:14592\n"
"ds_read_b64_tr_b16 a[248:249], v37 offset:14336\n"
"ds_read_b64_tr_b16 a[250:251], v37 offset:14592\n"
"s_waitcnt lgkmcnt(4)\n"
"v_mfma_f32_16x16x32_bf16 v[208:211], v[188:191], a[152:155], v[208:211]\n"
"v_mfma_f32_16x16x32_bf16 v[212:215], v[188:191], a[184:187], v[212:215]\n"
"s_waitcnt vmcnt(6)\n"
"ds_read_b64_tr_b16 a[28:29], v36 offset:16384\n"
"ds_read_b64_tr_b16 a[30:31], v36 offset:16640\n"
"ds_read_b64_tr_b16 a[60:61], v37 offset:16384\n"
"ds_read_b64_tr_b16 a[62:63], v37 offset:16640\n"
"s_waitcnt lgkmcnt(4)\n"
"v_mfma_f32_16x16x32_bf16 v[216:219], v[188:191], a[216:219], v[216:219]\n"
"v_mfma_f32_16x16x32_bf16 v[220:223], v[188:191], a[248:251], v[220:223]\n"
"s_waitcnt vmcnt(4)\n"
"ds_read_b64_tr_b16 a[92:93], v36 offset:18432\n"
"ds_read_b64_tr_b16 a[94:95], v36 offset:18688\n"
"ds_read_b64_tr_b16 a[124:125], v37 offset:18432\n"
"ds_read_b64_tr_b16 a[126:127], v37 offset:18688\n"
"s_waitcnt lgkmcnt(4)\n"
"v_mfma_f32_16x16x32_bf16 v[192:195], v[188:191], a[28:31], v[192:195]\n"
"v_mfma_f32_16x16x32_bf16 v[196:199], v[188:191], a[60:63], v[196:199]\n"
"s_waitcnt vmcnt(2)\n"
"ds_read_b64_tr_b16 a[156:157], v36 offset:20480\n"
"ds_read_b64_tr_b16 a[158:159], v36 offset:20736\n"
"ds_read_b64_tr_b16 a[188:189], v37 offset:20480\n"
"ds_read_b64_tr_b16 a[190:191], v37 offset:20736\n"
"s_waitcnt lgkmcnt(4)\n"
"v_mfma_f32_16x16x32_bf16 v[200:203], v[188:191], a[92:95], v[200:203]\n"
"v_mfma_f32_16x16x32_bf16 v[204:207], v[188:191], a[124:127], v[204:207]\n"
"s_waitcnt vmcnt(0)\n"
"ds_read_b64_tr_b16 a[220:221], v36 offset:22528\n"
"ds_read_b64_tr_b16 a[222:223], v36 offset:22784\n"
"ds_read_b64_tr_b16 a[252:253], v37 offset:22528\n"
"ds_read_b64_tr_b16 a[254:255], v37 offset:22784\n"
"s_waitcnt lgkmcnt(4)\n"
"v_mfma_f32_16x16x32_bf16 v[208:211], v[188:191], a[156:159], v[208:211]\n"
"v_mfma_f32_16x16x32_bf16 v[212:215], v[188:191], a[188:191], v[212:215]\n"
"s_waitcnt lgkmcnt(0)\n"
"v_mfma_f32_16x16x32_bf16 v[216:219], v[188:191], a[220:223], v[216:219]\n"
"v_mfma_f32_16x16x32_bf16 v[220:223], v[188:191], a[252:255], v[220:223]\n"
"s_mov_b32 m0, s57\n"
"s_barrier\n"
"ds_write_addtid_b32 v192 offset:0\n"
"ds_write_addtid_b32 v193 offset:4640\n"
"ds_write_addtid_b32 v194 offset:9344\n"
"ds_write_addtid_b32 v195 offset:13984\n"
"ds_write_addtid_b32 v196 offset:256\n"
"ds_write_addtid_b32 v197 offset:4896\n"
"ds_write_addtid_b32 v198 offset:9600\n"
"ds_write_addtid_b32 v199 offset:14240\n"
"ds_write_addtid_b32 v200 offset:512\n"
"ds_write_addtid_b32 v201 offset:5152\n"
"ds_write_addtid_b32 v202 offset:9856\n"
"ds_write_addtid_b32 v203 offset:14496\n"
"ds_write_addtid_b32 v204 offset:768\n"
"ds_write_addtid_b32 v205 offset:5408\n"
"ds_write_addtid_b32 v206 offset:10112\n"
"ds_write_addtid_b32 v207 offset:14752\n"
"ds_write_addtid_b32 v208 offset:1024\n"
"ds_write_addtid_b32 v209 offset:5664\n"
"ds_write_addtid_b32 v210 offset:10368\n"
"ds_write_addtid_b32 v211 offset:15008\n"
"ds_write_addtid_b32 v212 offset:1280\n"
"ds_write_addtid_b32 v213 offset:5920\n"
"ds_write_addtid_b32 v214 offset:10624\n"
"ds_write_addtid_b32 v215 offset:15264\n"
"ds_write_addtid_b32 v216 offset:1536\n"
"ds_write_addtid_b32 v217 offset:6176\n"
"ds_write_addtid_b32 v218 offset:10880\n"
"ds_write_addtid_b32 v219 offset:15520\n"
"ds_write_addtid_b32 v220 offset:1792\n"
"ds_write_addtid_b32 v221 offset:6432\n"
"ds_write_addtid_b32 v222 offset:11136\n"
"ds_write_addtid_b32 v223 offset:15776\n"
"s_waitcnt lgkmcnt(0)\n"
"s_barrier\n"
"ds_read_b128 v[96:99], v2 offset:0\n"
"ds_read_b128 v[100:103], v3 offset:512\n"
"ds_read_b128 v[104:107], v4 offset:1024\n"
"ds_read_b128 v[108:111], v42 offset:1536\n"
"ds_read_b128 v[112:115], v2 offset:256\n"
"ds_read_b128 v[116:119], v3 offset:768\n"
"ds_read_b128 v[120:123], v4 offset:1280\n"
"ds_read_b128 v[124:127], v42 offset:1792\n"
"s_waitcnt lgkmcnt(0)\n"
"s_barrier\n"
"v_add_f32 v96, v96, v100\n"
"v_add_f32 v104, v104, v108\n"
"v_add_f32 v18, v96, v104\n"
"v_add_f32 v97, v97, v101\n"
"v_add_f32 v105, v105, v109\n"
"v_add_f32 v19, v97, v105\n"
"v_add_f32 v98, v98, v102\n"
"v_add_f32 v106, v106, v110\n"
"v_add_f32 v20, v98, v106\n"
"v_add_f32 v99, v99, v103\n"
"v_add_f32 v107, v107, v111\n"
"v_add_f32 v21, v99, v107\n"
"v_add_f32 v112, v112, v116\n"
"v_add_f32 v120, v120, v124\n"
"v_add_f32 v22, v112, v120\n"
"v_add_f32 v113, v113, v117\n"
"v_add_f32 v121, v121, v125\n"
"v_add_f32 v23, v113, v121\n"
"v_add_f32 v114, v114, v118\n"
"v_add_f32 v122, v122, v126\n"
"v_add_f32 v24, v114, v122\n"
"v_add_f32 v115, v115, v119\n"
"v_add_f32 v123, v123, v127\n"
"v_add_f32 v25, v115, v123\n"
"v_mov_b32 v134, 0\n"
"v_mov_b32 v135, 0\n"
"v_mov_b32 v150, 0\n"
"v_mov_b32 v151, 0\n"
"v_mov_b32 v136, 0\n"
"v_mov_b32 v137, 0\n"
"v_mov_b32 v138, 0\n"
"v_mov_b32 v139, 0\n"
"s_mov_b32 s95, 0x80000000\n"
"s_mov_b32 s95, 0x80000000\n"
"s_mov_b32 s93, 0x80000000\n"
"s_mov_b32 s93, 0x80000000\n"
"s_mov_b32 s94, 0x80000000\n"
"s_mov_b32 s92, 0x80000000\n"
"s_mov_b32 s95, 0x80000000\n"
"s_mov_b32 s94, 0x80000000\n"
"s_mov_b32 s95, 0x80000000\n"
"s_cmp_eq_u32 s54, 0\n"
"s_cbranch_scc1 .LF_1s\n"
"s_cmp_eq_u32 s54, 1\n"
"s_cbranch_scc1 .LF_2s\n"
"s_cmp_eq_u32 s54, 2\n"
"s_cbranch_scc1 .LF_3s\n"
"s_cmp_eq_u32 s54, 3\n"
"s_cbranch_scc1 .LF_4s\n"
".p2align 6\n"
".LF_top:\n"
"v_mfma_f32_16x16x32_bf16 v[52:55], v[100:103], a[0:3], 0\n"
"buffer_load_dwordx4 v[36:39], v6, s[40:43], s56 offen offset:0 sc1\n"
"v_add_f32_dpp v113, v112, v112 row_ror:8 row_mask:0xf bank_mask:0xf\n"
"v_mfma_f32_16x16x32_bf16 v[52:55], v[64:67], a[4:7], v[52:55]\n"
"s_waitcnt vmcnt(9)\n"
"ds_read_b128 v[56:59], v3 offset:2048\n"
"v_mov_b32 v112, v113\n"
"v_mfma_f32_16x16x32_bf16 v[52:55], v[68:71], a[8:11], v[52:55]\n"
"v_permlane16_swap_b32 v113, v112\n"
"v_mfma_f32_16x16x32_bf16 v[52:55], v[72:75], a[12:15], v[52:55]\n"
"s_waitcnt vmcnt(7)\n"
"buffer_load_dwordx2 v[136:137], v9, s[40:43], s50 offen sc1\n"
"v_mul_f32 v113, 0xba800000, v113\n"
"v_mfma_f32_16x16x32_bf16 v[52:55], v[76:79], a[16:19], v[52:55]\n"
"buffer_load_dwordx4 v[40:43], v6, s[40:43], s56 offen offset:1024 sc1\n"
"v_fma_f32 v112, v112, s60, v17\n"
"v_mfma_f32_16x16x32_bf16 v[52:55], v[80:83], a[20:23], v[52:55]\n"
"s_waitcnt vmcnt(7)\n"
"v_fma_f32 v112, -v113, v113, v112\n"
"v_max_f32 v112, v17, v112\n"
"v_mfma_f32_16x16x32_bf16 v[52:55], v[84:87], a[24:27], v[52:55]\n"
"v_rsq_f32 v112, v112\n"
"v_mfma_f32_16x16x32_bf16 v[52:55], v[88:91], a[28:31], v[52:55]\n"
"ds_read_b128 v[60:63], v4 offset:1024\n"
"v_mul_f32 v113, v113, v112\n"
"v_mfma_f32_16x16x32_bf16 v[92:95], v[100:103], a[32:35], 0\n"
"buffer_load_dwordx4 v[44:47], v7, s[40:43], s56 offen offset:0 sc1\n"
"ds_write_addtid_b32 v112 offset:18592\n"
"v_mfma_f32_16x16x32_bf16 v[92:95], v[64:67], a[36:39], v[92:95]\n"
"ds_write_addtid_b32 v52 offset:0\n"
"ds_write_addtid_b32 v113 offset:18848\n"
"v_mfma_f32_16x16x32_bf16 v[92:95], v[68:71], a[40:43], v[92:95]\n"
"ds_write_addtid_b32 v53 offset:4640\n"
"ds_read_b128 v[96:99], v2 offset:256\n"
"v_mfma_f32_16x16x32_bf16 v[92:95], v[72:75], a[44:47], v[92:95]\n"
"ds_write_addtid_b32 v54 offset:9344\n"
"ds_read_b128 v[112:115], v3 offset:2304\n"
"v_mfma_f32_16x16x32_bf16 v[92:95], v[76:79], a[48:51], v[92:95]\n"
"ds_write_addtid_b32 v55 offset:13984\n"
"buffer_load_dwordx4 v[48:51], v7, s[40:43], s56 offen offset:1024 sc1\n"
"v_mfma_f32_16x16x32_bf16 v[92:95], v[80:83], a[52:55], v[92:95]\n"
"ds_read_b128 v[116:119], v4 offset:1280\n"
"v_mfma_f32_16x16x32_bf16 v[92:95], v[84:87], a[56:59], v[92:95]\n"
"s_waitcnt lgkmcnt(9)\n"
"v_add_f32 v128, v128, v56\n"
"v_add_f32 v129, v129, v57\n"
"v_mfma_f32_16x16x32_bf16 v[92:95], v[88:91], a[60:63], v[92:95]\n"
"v_add_f32 v130, v130, v58\n"
"v_add_f32 v131, v131, v59\n"
"v_mfma_f32_16x16x32_bf16 v[120:123], v[100:103], a[64:67], 0\n"
"buffer_load_dwordx4 v[52:55], v8, s[40:43], s56 offen offset:0 sc1\n"
"v_add_f32 v128, v128, v60\n"
"v_mfma_f32_16x16x32_bf16 v[120:123], v[64:67], a[68:71], v[120:123]\n"
"ds_write_addtid_b32 v92 offset:256\n"
"v_add_f32 v129, v129, v61\n"
"v_mfma_f32_16x16x32_bf16 v[120:123], v[68:71], a[72:75], v[120:123]\n"
"ds_write_addtid_b32 v93 offset:4896\n"
"v_add_f32 v130, v130, v62\n"
"v_mfma_f32_16x16x32_bf16 v[120:123], v[72:75], a[76:79], v[120:123]\n"
"ds_write_addtid_b32 v94 offset:9600\n"
"v_add_f32 v131, v131, v63\n"
"v_mfma_f32_16x16x32_bf16 v[120:123], v[76:79], a[80:83], v[120:123]\n"
"ds_write_addtid_b32 v95 offset:14240\n"
"buffer_load_dwordx4 v[56:59], v8, s[40:43], s56 offen offset:1024 sc1\n"
"v_mfma_f32_16x16x32_bf16 v[120:123], v[80:83], a[84:87], v[120:123]\n"
"v_add_f32 v128, v128, v124\n"
"v_add_f32 v129, v129, v125\n"
"v_mfma_f32_16x16x32_bf16 v[120:123], v[84:87], a[88:91], v[120:123]\n"
"v_add_f32 v130, v130, v126\n"
"v_add_f32 v131, v131, v127\n"
"v_mfma_f32_16x16x32_bf16 v[120:123], v[88:91], a[92:95], v[120:123]\n"
"s_add_u32 s94, s50, 0xffd00000\n"
"s_and_b32 s95, s94, 0xfbffffff\n"
"v_fma_f32 v60, v18, v139, v26\n"
"v_mfma_f32_16x16x32_bf16 v[92:95], v[100:103], a[96:99], 0\n"
"v_fma_f32 v61, v19, v139, v27\n"
"v_fma_f32 v62, v20, v139, v28\n"
"v_mfma_f32_16x16x32_bf16 v[92:95], v[64:67], a[100:103], v[92:95]\n"
"ds_write_addtid_b32 v120 offset:512\n"
"v_fma_f32 v63, v21, v139, v29\n"
"v_mfma_f32_16x16x32_bf16 v[92:95], v[68:71], a[104:107], v[92:95]\n"
"ds_write_addtid_b32 v121 offset:5152\n"
"v_fma_f32 v128, v128, v138, v60\n"
"v_mfma_f32_16x16x32_bf16 v[92:95], v[72:75], a[108:111], v[92:95]\n"
"ds_write_addtid_b32 v122 offset:9856\n"
"v_fma_f32 v129, v129, v138, v61\n"
"v_mfma_f32_16x16x32_bf16 v[92:95], v[76:79], a[112:115], v[92:95]\n"
"ds_write_addtid_b32 v123 offset:14496\n"
"v_fma_f32 v130, v130, v138, v62\n"
"v_mfma_f32_16x16x32_bf16 v[92:95], v[80:83], a[116:119], v[92:95]\n"
"v_fma_f32 v131, v131, v138, v63\n"
"v_mul_f32_e64 v128, v128, |v128|\n"
"v_mfma_f32_16x16x32_bf16 v[92:95], v[84:87], a[120:123], v[92:95]\n"
"s_waitcnt vmcnt(9)\n"
"buffer_store_dwordx2 v[0:1], v16, s[40:43], s92 offen offset:0 sc1\n"
"v_mul_f32_e64 v129, v129, |v129|\n"
"v_mul_f32_e64 v130, v130, |v130|\n"
"v_mfma_f32_16x16x32_bf16 v[92:95], v[88:91], a[124:127], v[92:95]\n"
"v_mul_f32_e64 v131, v131, |v131|\n"
"v_cvt_pk_bf16_f32 v60, v128, v129\n"
"v_mfma_f32_16x16x32_bf16 v[120:123], v[100:103], a[128:131], 0\n"
"v_cvt_pk_bf16_f32 v61, v130, v131\n"
"v_pk_max_i16 v60, v60, 0\n"
"v_mfma_f32_16x16x32_bf16 v[120:123], v[64:67], a[132:135], v[120:123]\n"
"ds_write_addtid_b32 v92 offset:768\n"
"v_pk_max_i16 v61, v61, 0\n"
"v_mfma_f32_16x16x32_bf16 v[120:123], v[68:71], a[136:139], v[120:123]\n"
"s_waitcnt lgkmcnt(9)\n"
"ds_write_addtid_b32 v93 offset:5408\n"
"v_add_f32 v96, v96, v112\n"
"v_mfma_f32_16x16x32_bf16 v[120:123], v[72:75], a[140:143], v[120:123]\n"
"ds_write_addtid_b32 v94 offset:10112\n"
"v_add_f32 v97, v97, v113\n"
"v_mfma_f32_16x16x32_bf16 v[120:123], v[76:79], a[144:147], v[120:123]\n"
"ds_write_addtid_b32 v95 offset:14752\n"
"v_add_f32 v98, v98, v114\n"
"v_mfma_f32_16x16x32_bf16 v[120:123], v[80:83], a[148:151], v[120:123]\n"
"v_add_f32 v99, v99, v115\n"
"v_add_f32 v96, v96, v116\n"
"v_mfma_f32_16x16x32_bf16 v[120:123], v[84:87], a[152:155], v[120:123]\n"
"v_add_f32 v97, v97, v117\n"
"v_add_f32 v98, v98, v118\n"
"v_mfma_f32_16x16x32_bf16 v[120:123], v[88:91], a[156:159], v[120:123]\n"
"v_add_f32 v99, v99, v119\n"
"v_add_f32 v96, v96, v132\n"
"v_mfma_f32_16x16x32_bf16 v[92:95], v[100:103], a[160:163], 0\n"
"v_add_f32 v97, v97, v133\n"
"v_add_f32 v98, v98, v134\n"
"v_mfma_f32_16x16x32_bf16 v[92:95], v[64:67], a[164:167], v[92:95]\n"
"ds_write_addtid_b32 v120 offset:1024\n"
"v_add_f32 v99, v99, v135\n"
"v_mfma_f32_16x16x32_bf16 v[92:95], v[68:71], a[168:171], v[92:95]\n"
"ds_write_addtid_b32 v121 offset:5664\n"
"v_fma_f32 v112, v22, v139, v30\n"
"v_mfma_f32_16x16x32_bf16 v[92:95], v[72:75], a[172:175], v[92:95]\n"
"ds_write_addtid_b32 v122 offset:10368\n"
"v_fma_f32 v113, v23, v139, v31\n"
"v_mfma_f32_16x16x32_bf16 v[92:95], v[76:79], a[176:179], v[92:95]\n"
"ds_write_addtid_b32 v123 offset:15008\n"
"v_fma_f32 v114, v24, v139, v32\n"
"v_mfma_f32_16x16x32_bf16 v[92:95], v[80:83], a[180:183], v[92:95]\n"
"v_fma_f32 v115, v25, v139, v33\n"
"v_fma_f32 v96, v96, v138, v112\n"
"v_mfma_f32_16x16x32_bf16 v[92:95], v[84:87], a[184:187], v[92:95]\n"
"v_fma_f32 v97, v97, v138, v113\n"
"v_fma_f32 v98, v98, v138, v114\n"
"v_mfma_f32_16x16x32_bf16 v[92:95], v[88:91], a[188:191], v[92:95]\n"
"v_fma_f32 v99, v99, v138, v115\n"
"v_mul_f32_e64 v96, v96, |v96|\n"
"v_mfma_f32_16x16x32_bf16 v[116:119], a[192:195], v[100:103], 0\n"
"v_mul_f32_e64 v97, v97, |v97|\n"
"v_mul_f32_e64 v98, v98, |v98|\n"
"v_mfma_f32_16x16x32_bf16 v[116:119], a[196:199], v[64:67], v[116:119]\n"
"ds_write_addtid_b32 v92 offset:1280\n"
"v_mul_f32_e64 v99, v99, |v99|\n"
"v_mfma_f32_16x16x32_bf16 v[116:119], a[200:203], v[68:71], v[116:119]\n"
"ds_write_addtid_b32 v93 offset:5920\n"
"v_cvt_pk_bf16_f32 v62, v96, v97\n"
"v_mfma_f32_16x16x32_bf16 v[116:119], a[204:207], v[72:75], v[116:119]\n"
"ds_write_addtid_b32 v94 offset:10624\n"
"v_cvt_pk_bf16_f32 v63, v98, v99\n"
"v_mfma_f32_16x16x32_bf16 v[116:119], a[208:211], v[76:79], v[116:119]\n"
"s_waitcnt vmcnt(6)\n"
"ds_write_addtid_b32 v95 offset:15264\n"
"v_cmp_ne_u64 vcc, s[48:49], v[136:137]\n"
"v_mfma_f32_16x16x32_bf16 v[116:119], a[212:215], v[80:83], v[116:119]\n"
"v_pk_max_i16 v62, v62, 0\n"
"v_pk_max_i16 v63, v63, 0\n"
"v_mfma_f32_16x16x32_bf16 v[116:119], a[216:219], v[84:87], v[116:119]\n"
"buffer_store_dwordx4 v[60:63], v15, s[40:43], s95 offen sc1\n"
"s_cbranch_vccnz .LF_3s\n"
".LF_3r:\n"
"v_mfma_f32_16x16x32_bf16 v[116:119], a[220:223], v[88:91], v[116:119]\n"
"s_add_u32 s50, s50, 0x180000\n"
"s_and_b32 s56, s50, 0xfbffffff\n"
"v_mfma_f32_16x16x32_bf16 v[92:95], a[224:227], v[100:103], 0\n"
"s_waitcnt lgkmcnt(0)\n"
"buffer_load_dwordx4 v[60:63], v5, s[40:43], s56 offen offset:0 sc1\n"
"v_mfma_f32_16x16x32_bf16 v[92:95], a[228:231], v[64:67], v[92:95]\n"
"s_barrier\n"
"v_mfma_f32_16x16x32_bf16 v[92:95], a[232:235], v[68:71], v[92:95]\n"
"ds_read2st64_b32 v[136:137], v11 offset0:0 offset1:1\n"
"ds_read_b128 v[96:99], v2 offset:1536\n"
"v_mfma_f32_16x16x32_bf16 v[92:95], a[236:239], v[72:75], v[92:95]\n"
"ds_read_b128 v[100:103], v3 offset:512\n"
"ds_read_b128 v[112:115], v4 offset:2560\n"
"v_mfma_f32_16x16x32_bf16 v[92:95], a[240:243], v[76:79], v[92:95]\n"
"buffer_load_dwordx4 v[64:67], v5, s[40:43], s56 offen offset:1024 sc1\n"
"ds_read_b128 v[120:123], v2 offset:1792\n"
"v_mfma_f32_16x16x32_bf16 v[92:95], a[244:247], v[80:83], v[92:95]\n"
"ds_read_b128 v[124:127], v3 offset:768\n"
"ds_read_b128 v[128:131], v4 offset:2816\n"
"v_mfma_f32_16x16x32_bf16 v[92:95], a[248:251], v[84:87], v[92:95]\n"
"v_mfma_f32_16x16x32_bf16 v[92:95], a[252:255], v[88:91], v[92:95]\n"
"v_mfma_f32_16x16x32_bf16 v[132:135], v[104:107], a[0:3], 0\n"
"buffer_load_dwordx4 v[68:71], v6, s[40:43], s56 offen offset:0 sc1\n"
"v_mfma_f32_16x16x32_bf16 v[132:135], v[108:111], a[4:7], v[132:135]\n"
"s_waitcnt vmcnt(9)\n"
"v_mfma_f32_16x16x32_bf16 v[132:135], v[36:39], a[8:11], v[132:135]\n"
"s_waitcnt lgkmcnt(3)\n"
"v_add_f32 v96, v96, v100\n"
"v_add_f32 v97, v97, v101\n"
"v_mfma_f32_16x16x32_bf16 v[132:135], v[40:43], a[12:15], v[132:135]\n"
"s_waitcnt vmcnt(7)\n"
"buffer_load_dwordx2 v[138:139], v9, s[40:43], s50 offen sc1\n"
"v_add_f32 v98, v98, v102\n"
"v_mfma_f32_16x16x32_bf16 v[132:135], v[44:47], a[16:19], v[132:135]\n"
"buffer_load_dwordx4 v[72:75], v6, s[40:43], s56 offen offset:1024 sc1\n"
"v_add_f32 v99, v99, v103\n"
"v_mfma_f32_16x16x32_bf16 v[132:135], v[48:51], a[20:23], v[132:135]\n"
"s_waitcnt vmcnt(7)\n"
"v_add_f32 v96, v96, v112\n"
"v_add_f32 v97, v97, v113\n"
"v_mfma_f32_16x16x32_bf16 v[132:135], v[52:55], a[24:27], v[132:135]\n"
"v_add_f32 v98, v98, v114\n"
"v_add_f32 v99, v99, v115\n"
"v_mfma_f32_16x16x32_bf16 v[132:135], v[56:59], a[28:31], v[132:135]\n"
"v_add_f32 v96, v96, v116\n"
"v_add_f32 v97, v97, v117\n"
"v_mfma_f32_16x16x32_bf16 v[100:103], v[104:107], a[32:35], 0\n"
"buffer_load_dwordx4 v[76:79], v7, s[40:43], s56 offen offset:0 sc1\n"
"v_add_f32 v98, v98, v118\n"
"v_mfma_f32_16x16x32_bf16 v[100:103], v[108:111], a[36:39], v[100:103]\n"
"ds_write_addtid_b32 v132 offset:1536\n"
"v_add_f32 v99, v99, v119\n"
"v_mfma_f32_16x16x32_bf16 v[100:103], v[36:39], a[40:43], v[100:103]\n"
"s_add_u32 s95, s50, 0xffd00000\n"
"s_and_b32 s93, s95, 0xfbffffff\n"
"ds_write_addtid_b32 v133 offset:6176\n"
"v_mfma_f32_16x16x32_bf16 v[100:103], v[40:43], a[44:47], v[100:103]\n"
"ds_write_addtid_b32 v134 offset:10880\n"
"v_fma_f32 v88, v18, v137, v26\n"
"v_mfma_f32_16x16x32_bf16 v[100:103], v[44:47], a[48:51], v[100:103]\n"
"ds_write_addtid_b32 v135 offset:15520\n"
"buffer_load_dwordx4 v[80:83], v7, s[40:43], s56 offen offset:1024 sc1\n"
"v_mfma_f32_16x16x32_bf16 v[100:103], v[48:51], a[52:55], v[100:103]\n"
"v_fma_f32 v89, v19, v137, v27\n"
"v_fma_f32 v90, v20, v137, v28\n"
"v_mfma_f32_16x16x32_bf16 v[100:103], v[52:55], a[56:59], v[100:103]\n"
"v_fma_f32 v91, v21, v137, v29\n"
"v_fma_f32 v96, v96, v136, v88\n"
"v_mfma_f32_16x16x32_bf16 v[100:103], v[56:59], a[60:63], v[100:103]\n"
"v_fma_f32 v97, v97, v136, v89\n"
"v_fma_f32 v98, v98, v136, v90\n"
"v_mfma_f32_16x16x32_bf16 v[112:115], v[104:107], a[64:67], 0\n"
"buffer_load_dwordx4 v[84:87], v8, s[40:43], s56 offen offset:0 sc1\n"
"v_fma_f32 v99, v99, v136, v91\n"
"v_mfma_f32_16x16x32_bf16 v[112:115], v[108:111], a[68:71], v[112:115]\n"
"ds_write_addtid_b32 v100 offset:1792\n"
"v_mul_f32_e64 v96, v96, |v96|\n"
"v_mfma_f32_16x16x32_bf16 v[112:115], v[36:39], a[72:75], v[112:115]\n"
"ds_write_addtid_b32 v101 offset:6432\n"
"v_mul_f32_e64 v97, v97, |v97|\n"
"v_mfma_f32_16x16x32_bf16 v[112:115], v[40:43], a[76:79], v[112:115]\n"
"ds_write_addtid_b32 v102 offset:11136\n"
"v_mul_f32_e64 v98, v98, |v98|\n"
"v_mfma_f32_16x16x32_bf16 v[112:115], v[44:47], a[80:83], v[112:115]\n"
"ds_write_addtid_b32 v103 offset:15776\n"
"buffer_load_dwordx4 v[88:91], v8, s[40:43], s56 offen offset:1024 sc1\n"
"v_mfma_f32_16x16x32_bf16 v[112:115], v[48:51], a[84:87], v[112:115]\n"
"v_mul_f32_e64 v99, v99, |v99|\n"
"v_cvt_pk_bf16_f32 v100, v96, v97\n"
"v_mfma_f32_16x16x32_bf16 v[112:115], v[52:55], a[88:91], v[112:115]\n"
"v_cvt_pk_bf16_f32 v101, v98, v99\n"
"v_pk_max_i16 v100, v100, 0\n"
"v_mfma_f32_16x16x32_bf16 v[112:115], v[56:59], a[92:95], v[112:115]\n"
"v_pk_max_i16 v101, v101, 0\n"
"v_mfma_f32_16x16x32_bf16 v[96:99], v[104:107], a[96:99], 0\n"
"s_waitcnt lgkmcnt(8)\n"
"v_add_f32 v120, v120, v124\n"
"v_add_f32 v121, v121, v125\n"
"v_mfma_f32_16x16x32_bf16 v[96:99], v[108:111], a[100:103], v[96:99]\n"
"ds_write_addtid_b32 v112 offset:2048\n"
"v_add_f32 v122, v122, v126\n"
"v_mfma_f32_16x16x32_bf16 v[96:99], v[36:39], a[104:107], v[96:99]\n"
"ds_write_addtid_b32 v113 offset:6688\n"
"v_add_f32 v123, v123, v127\n"
"v_mfma_f32_16x16x32_bf16 v[96:99], v[40:43], a[108:111], v[96:99]\n"
"ds_write_addtid_b32 v114 offset:11392\n"
"v_add_f32 v120, v120, v128\n"
"v_mfma_f32_16x16x32_bf16 v[96:99], v[44:47], a[112:115], v[96:99]\n"
"ds_write_addtid_b32 v115 offset:16032\n"
"v_add_f32 v121, v121, v129\n"
"v_mfma_f32_16x16x32_bf16 v[96:99], v[48:51], a[116:119], v[96:99]\n"
"v_add_f32 v122, v122, v130\n"
"v_add_f32 v123, v123, v131\n"
"v_mfma_f32_16x16x32_bf16 v[96:99], v[52:55], a[120:123], v[96:99]\n"
"v_add_f32 v120, v120, v92\n"
"v_add_f32 v121, v121, v93\n"
"v_mfma_f32_16x16x32_bf16 v[96:99], v[56:59], a[124:127], v[96:99]\n"
"v_add_f32 v122, v122, v94\n"
"v_add_f32 v123, v123, v95\n"
"v_mfma_f32_16x16x32_bf16 v[112:115], v[104:107], a[128:131], 0\n"
"v_fma_f32 v116, v22, v137, v30\n"
"v_fma_f32 v117, v23, v137, v31\n"
"v_mfma_f32_16x16x32_bf16 v[112:115], v[108:111], a[132:135], v[112:115]\n"
"ds_write_addtid_b32 v96 offset:2304\n"
"v_fma_f32 v118, v24, v137, v32\n"
"v_mfma_f32_16x16x32_bf16 v[112:115], v[36:39], a[136:139], v[112:115]\n"
"ds_write_addtid_b32 v97 offset:6944\n"
"v_fma_f32 v119, v25, v137, v33\n"
"v_mfma_f32_16x16x32_bf16 v[112:115], v[40:43], a[140:143], v[112:115]\n"
"ds_write_addtid_b32 v98 offset:11648\n"
"v_fma_f32 v120, v120, v136, v116\n"
"v_mfma_f32_16x16x32_bf16 v[112:115], v[44:47], a[144:147], v[112:115]\n"
"ds_write_addtid_b32 v99 offset:16288\n"
"v_fma_f32 v121, v121, v136, v117\n"
"v_mfma_f32_16x16x32_bf16 v[112:115], v[48:51], a[148:151], v[112:115]\n"
"v_fma_f32 v122, v122, v136, v118\n"
"v_fma_f32 v123, v123, v136, v119\n"
"v_mfma_f32_16x16x32_bf16 v[112:115], v[52:55], a[152:155], v[112:115]\n"
"s_waitcnt vmcnt(9)\n"
"buffer_store_dwordx2 v[0:1], v16, s[40:43], s94 offen offset:0 sc1\n"
"v_mul_f32_e64 v120, v120, |v120|\n"
"v_mul_f32_e64 v121, v121, |v121|\n"
"v_mfma_f32_16x16x32_bf16 v[112:115], v[56:59], a[156:159], v[112:115]\n"
"v_mul_f32_e64 v122, v122, |v122|\n"
"v_mul_f32_e64 v123, v123, |v123|\n"
"v_mfma_f32_16x16x32_bf16 v[92:95], v[104:107], a[160:163], 0\n"
"v_cvt_pk_bf16_f32 v102, v120, v121\n"
"v_cvt_pk_bf16_f32 v103, v122, v123\n"
"v_mfma_f32_16x16x32_bf16 v[92:95], v[108:111], a[164:167], v[92:95]\n"
"ds_write_addtid_b32 v112 offset:2560\n"
"v_pk_max_i16 v102, v102, 0\n"
"v_mfma_f32_16x16x32_bf16 v[92:95], v[36:39], a[168:171], v[92:95]\n"
"ds_write_addtid_b32 v113 offset:7200\n"
"v_pk_max_i16 v103, v103, 0\n"
"v_mfma_f32_16x16x32_bf16 v[92:95], v[40:43], a[172:175], v[92:95]\n"
"buffer_store_dwordx4 v[100:103], v15, s[40:43], s93 offen sc1\n"
"ds_write_addtid_b32 v114 offset:11904\n"
"v_mfma_f32_16x16x32_bf16 v[92:95], v[44:47], a[176:179], v[92:95]\n"
"ds_write_addtid_b32 v115 offset:16544\n"
"v_mfma_f32_16x16x32_bf16 v[92:95], v[48:51], a[180:183], v[92:95]\n"
"v_mfma_f32_16x16x32_bf16 v[92:95], v[52:55], a[184:187], v[92:95]\n"
"v_mfma_f32_16x16x32_bf16 v[92:95], v[56:59], a[188:191], v[92:95]\n"
"v_mfma_f32_16x16x32_bf16 v[96:99], a[192:195], v[104:107], 0\n"
"v_mfma_f32_16x16x32_bf16 v[96:99], a[196:199], v[108:111], v[96:99]\n"
"ds_write_addtid_b32 v92 offset:2816\n"
"v_mfma_f32_16x16x32_bf16 v[96:99], a[200:203], v[36:39], v[96:99]\n"
"ds_write_addtid_b32 v93 offset:7456\n"
"v_mfma_f32_16x16x32_bf16 v[96:99], a[204:207], v[40:43], v[96:99]\n"
"ds_write_addtid_b32 v94 offset:12160\n"
"v_mfma_f32_16x16x32_bf16 v[96:99], a[208:211], v[44:47], v[96:99]\n"
"s_waitcnt vmcnt(7)\n"
"ds_write_addtid_b32 v95 offset:16800\n"
"v_cmp_ne_u64 vcc, s[48:49], v[138:139]\n"
"v_mfma_f32_16x16x32_bf16 v[96:99], a[212:215], v[48:51], v[96:99]\n"
"v_mfma_f32_16x16x32_bf16 v[96:99], a[216:219], v[52:55], v[96:99]\n"
"s_cbranch_vccnz .LF_4s\n"
".LF_4r:\n"
"v_mfma_f32_16x16x32_bf16 v[96:99], a[220:223], v[56:59], v[96:99]\n"
"s_add_u32 s50, s50, 0x180000\n"
"s_and_b32 s56, s50, 0xfbffffff\n"
"v_mfma_f32_16x16x32_bf16 v[100:103], a[224:227], v[104:107], 0\n"
"s_waitcnt lgkmcnt(0)\n"
"buffer_load_dwordx4 v[92:95], v5, s[40:43], s56 offen offset:0 sc1\n"
"v_mfma_f32_16x16x32_bf16 v[100:103], a[228:231], v[108:111], v[100:103]\n"
"s_barrier\n"
"v_mfma_f32_16x16x32_bf16 v[100:103], a[232:235], v[36:39], v[100:103]\n"
"ds_read2st64_b32 v[132:133], v12 offset0:0 offset1:1\n"
"ds_read_b128 v[104:107], v2 offset:0\n"
"v_mfma_f32_16x16x32_bf16 v[100:103], a[236:239], v[40:43], v[100:103]\n"
"ds_read_b128 v[108:111], v3 offset:2048\n"
"ds_read_b128 v[112:115], v4 offset:1024\n"
"v_mfma_f32_16x16x32_bf16 v[100:103], a[240:243], v[44:47], v[100:103]\n"
"buffer_load_dwordx4 v[36:39], v5, s[40:43], s56 offen offset:1024 sc1\n"
"ds_read_b128 v[116:119], v2 offset:256\n"
"v_mfma_f32_16x16x32_bf16 v[100:103], a[244:247], v[48:51], v[100:103]\n"
"ds_read_b128 v[120:123], v3 offset:2304\n"
"ds_read_b128 v[124:127], v4 offset:1280\n"
"v_mfma_f32_16x16x32_bf16 v[100:103], a[248:251], v[52:55], v[100:103]\n"
"v_mfma_f32_16x16x32_bf16 v[100:103], a[252:255], v[56:59], v[100:103]\n"
"v_mfma_f32_16x16x32_bf16 v[128:131], v[60:63], a[0:3], 0\n"
"buffer_load_dwordx4 v[40:43], v6, s[40:43], s56 offen offset:0 sc1\n"
"v_mfma_f32_16x16x32_bf16 v[128:131], v[64:67], a[4:7], v[128:131]\n"
"s_waitcnt vmcnt(9)\n"
"v_mfma_f32_16x16x32_bf16 v[128:131], v[68:71], a[8:11], v[128:131]\n"
"s_waitcnt lgkmcnt(3)\n"
"v_add_f32 v104, v104, v108\n"
"v_add_f32 v105, v105, v109\n"
"v_mfma_f32_16x16x32_bf16 v[128:131], v[72:75], a[12:15], v[128:131]\n"
"s_waitcnt vmcnt(7)\n"
"buffer_load_dwordx2 v[134:135], v9, s[40:43], s50 offen sc1\n"
"v_add_f32 v106, v106, v110\n"
"v_mfma_f32_16x16x32_bf16 v[128:131], v[76:79], a[16:19], v[128:131]\n"
"buffer_load_dwordx4 v[44:47], v6, s[40:43], s56 offen offset:1024 sc1\n"
"v_add_f32 v107, v107, v111\n"
"v_mfma_f32_16x16x32_bf16 v[128:131], v[80:83], a[20:23], v[128:131]\n"
"s_waitcnt vmcnt(7)\n"
"v_add_f32 v104, v104, v112\n"
"v_add_f32 v105, v105, v113\n"
"v_mfma_f32_16x16x32_bf16 v[128:131], v[84:87], a[24:27], v[128:131]\n"
"v_add_f32 v106, v106, v114\n"
"v_add_f32 v107, v107, v115\n"
"v_mfma_f32_16x16x32_bf16 v[128:131], v[88:91], a[28:31], v[128:131]\n"
"v_add_f32 v104, v104, v96\n"
"v_add_f32 v105, v105, v97\n"
"v_mfma_f32_16x16x32_bf16 v[108:111], v[60:63], a[32:35], 0\n"
"buffer_load_dwordx4 v[48:51], v7, s[40:43], s56 offen offset:0 sc1\n"
"v_add_f32 v106, v106, v98\n"
"v_mfma_f32_16x16x32_bf16 v[108:111], v[64:67], a[36:39], v[108:111]\n"
"ds_write_addtid_b32 v128 offset:0\n"
"v_add_f32 v107, v107, v99\n"
"v_mfma_f32_16x16x32_bf16 v[108:111], v[68:71], a[40:43], v[108:111]\n"
"s_add_u32 s93, s50, 0xffd00000\n"
"s_and_b32 s94, s93, 0xfbffffff\n"
"ds_write_addtid_b32 v129 offset:4640\n"
"v_mfma_f32_16x16x32_bf16 v[108:111], v[72:75], a[44:47], v[108:111]\n"
"ds_write_addtid_b32 v130 offset:9344\n"
"v_fma_f32 v96, v18, v133, v26\n"
"v_mfma_f32_16x16x32_bf16 v[108:111], v[76:79], a[48:51], v[108:111]\n"
"ds_write_addtid_b32 v131 offset:13984\n"
"buffer_load_dwordx4 v[52:55], v7, s[40:43], s56 offen offset:1024 sc1\n"
"v_mfma_f32_16x16x32_bf16 v[108:111], v[80:83], a[52:55], v[108:111]\n"
"v_fma_f32 v97, v19, v133, v27\n"
"v_fma_f32 v98, v20, v133, v28\n"
"v_mfma_f32_16x16x32_bf16 v[108:111], v[84:87], a[56:59], v[108:111]\n"
"v_fma_f32 v99, v21, v133, v29\n"
"v_fma_f32 v104, v104, v132, v96\n"
"v_mfma_f32_16x16x32_bf16 v[108:111], v[88:91], a[60:63], v[108:111]\n"
"v_fma_f32 v105, v105, v132, v97\n"
"v_fma_f32 v106, v106, v132, v98\n"
"v_mfma_f32_16x16x32_bf16 v[112:115], v[60:63], a[64:67], 0\n"
"buffer_load_dwordx4 v[56:59], v8, s[40:43], s56 offen offset:0 sc1\n"
"v_fma_f32 v107, v107, v132, v99\n"
"v_mfma_f32_16x16x32_bf16 v[112:115], v[64:67], a[68:71], v[112:115]\n"
"ds_write_addtid_b32 v108 offset:256\n"
"v_mul_f32_e64 v104, v104, |v104|\n"
"v_mfma_f32_16x16x32_bf16 v[112:115], v[68:71], a[72:75], v[112:115]\n"
"ds_write_addtid_b32 v109 offset:4896\n"
"v_mul_f32_e64 v105, v105, |v105|\n"
"v_mfma_f32_16x16x32_bf16 v[112:115], v[72:75], a[76:79], v[112:115]\n"
"ds_write_addtid_b32 v110 offset:9600\n"
"v_mul_f32_e64 v106, v106, |v106|\n"
"v_mfma_f32_16x16x32_bf16 v[112:115], v[76:79], a[80:83], v[112:115]\n"
"ds_write_addtid_b32 v111 offset:14240\n"
"buffer_load_dwordx4 v[96:99], v8, s[40:43], s56 offen offset:1024 sc1\n"
"v_mfma_f32_16x16x32_bf16 v[112:115], v[80:83], a[84:87], v[112:115]\n"
"v_mul_f32_e64 v107, v107, |v107|\n"
"v_cvt_pk_bf16_f32 v108, v104, v105\n"
"v_mfma_f32_16x16x32_bf16 v[112:115], v[84:87], a[88:91], v[112:115]\n"
"v_cvt_pk_bf16_f32 v109, v106, v107\n"
"v_pk_max_i16 v108, v108, 0\n"
"v_mfma_f32_16x16x32_bf16 v[112:115], v[88:91], a[92:95], v[112:115]\n"
"v_pk_max_i16 v109, v109, 0\n"
"v_mfma_f32_16x16x32_bf16 v[104:107], v[60:63], a[96:99], 0\n"
"s_waitcnt lgkmcnt(8)\n"
"v_add_f32 v116, v116, v120\n"
"v_add_f32 v117, v117, v121\n"
"v_mfma_f32_16x16x32_bf16 v[104:107], v[64:67], a[100:103], v[104:107]\n"
"ds_write_addtid_b32 v112 offset:512\n"
"v_add_f32 v118, v118, v122\n"
"v_mfma_f32_16x16x32_bf16 v[104:107], v[68:71], a[104:107], v[104:107]\n"
"ds_write_addtid_b32 v113 offset:5152\n"
"v_add_f32 v119, v119, v123\n"
"v_mfma_f32_16x16x32_bf16 v[104:107], v[72:75], a[108:111], v[104:107]\n"
"s_waitcnt vmcnt(9)\n"
"buffer_store_dwordx2 v[0:1], v16, s[40:43], s95 offen offset:0 sc1\n"
"ds_write_addtid_b32 v114 offset:9856\n"
"v_add_f32 v116, v116, v124\n"
"v_mfma_f32_16x16x32_bf16 v[104:107], v[76:79], a[112:115], v[104:107]\n"
"ds_write_addtid_b32 v115 offset:14496\n"
"v_add_f32 v117, v117, v125\n"
"v_mfma_f32_16x16x32_bf16 v[104:107], v[80:83], a[116:119], v[104:107]\n"
"v_add_f32 v118, v118, v126\n"
"v_add_f32 v119, v119, v127\n"
"v_mfma_f32_16x16x32_bf16 v[104:107], v[84:87], a[120:123], v[104:107]\n"
"v_add_f32 v116, v116, v100\n"
"v_add_f32 v117, v117, v101\n"
"v_mfma_f32_16x16x32_bf16 v[104:107], v[88:91], a[124:127], v[104:107]\n"
"v_add_f32 v118, v118, v102\n"
"v_add_f32 v119, v119, v103\n"
"v_mfma_f32_16x16x32_bf16 v[112:115], v[60:63], a[128:131], 0\n"
"v_fma_f32 v120, v22, v133, v30\n"
"v_fma_f32 v121, v23, v133, v31\n"
"v_mfma_f32_16x16x32_bf16 v[112:115], v[64:67], a[132:135], v[112:115]\n"
"ds_write_addtid_b32 v104 offset:768\n"
"v_fma_f32 v122, v24, v133, v32\n"
"v_mfma_f32_16x16x32_bf16 v[112:115], v[68:71], a[136:139], v[112:115]\n"
"ds_write_addtid_b32 v105 offset:5408\n"
"v_fma_f32 v123, v25, v133, v33\n"
"v_mfma_f32_16x16x32_bf16 v[112:115], v[72:75], a[140:143], v[112:115]\n"
"ds_write_addtid_b32 v106 offset:10112\n"
"v_fma_f32 v116, v116, v132, v120\n"
"v_mfma_f32_16x16x32_bf16 v[112:115], v[76:79], a[144:147], v[112:115]\n"
"ds_write_addtid_b32 v107 offset:14752\n"
"v_fma_f32 v117, v117, v132, v121\n"
"v_mfma_f32_16x16x32_bf16 v[112:115], v[80:83], a[148:151], v[112:115]\n"
"v_fma_f32 v118, v118, v132, v122\n"
"v_fma_f32 v119, v119, v132, v123\n"
"v_mfma_f32_16x16x32_bf16 v[112:115], v[84:87], a[152:155], v[112:115]\n"
"v_mul_f32_e64 v116, v116, |v116|\n"
"v_mul_f32_e64 v117, v117, |v117|\n"
"v_mfma_f32_16x16x32_bf16 v[112:115], v[88:91], a[156:159], v[112:115]\n"
"v_mul_f32_e64 v118, v118, |v118|\n"
"v_mul_f32_e64 v119, v119, |v119|\n"
"v_mfma_f32_16x16x32_bf16 v[100:103], v[60:63], a[160:163], 0\n"
"v_cvt_pk_bf16_f32 v110, v116, v117\n"
"v_cvt_pk_bf16_f32 v111, v118, v119\n"
"v_mfma_f32_16x16x32_bf16 v[100:103], v[64:67], a[164:167], v[100:103]\n"
"ds_write_addtid_b32 v112 offset:1024\n"
"v_pk_max_i16 v110, v110, 0\n"
"v_mfma_f32_16x16x32_bf16 v[100:103], v[68:71], a[168:171], v[100:103]\n"
"ds_write_addtid_b32 v113 offset:5664\n"
"v_pk_max_i16 v111, v111, 0\n"
"v_mfma_f32_16x16x32_bf16 v[100:103], v[72:75], a[172:175], v[100:103]\n"
"buffer_store_dwordx4 v[108:111], v15, s[40:43], s94 offen sc1\n"
"ds_write_addtid_b32 v114 offset:10368\n"
"v_mfma_f32_16x16x32_bf16 v[100:103], v[76:79], a[176:179], v[100:103]\n"
"ds_write_addtid_b32 v115 offset:15008\n"
"v_mfma_f32_16x16x32_bf16 v[100:103], v[80:83], a[180:183], v[100:103]\n"
"v_mfma_f32_16x16x32_bf16 v[100:103], v[84:87], a[184:187], v[100:103]\n"
"v_mfma_f32_16x16x32_bf16 v[100:103], v[88:91], a[188:191], v[100:103]\n"
"v_mfma_f32_16x16x32_bf16 v[104:107], a[192:195], v[60:63], 0\n"
"v_mfma_f32_16x16x32_bf16 v[104:107], a[196:199], v[64:67], v[104:107]\n"
"ds_write_addtid_b32 v100 offset:1280\n"
"v_mfma_f32_16x16x32_bf16 v[104:107], a[200:203], v[68:71], v[104:107]\n"
"ds_write_addtid_b32 v101 offset:5920\n"
"v_mfma_f32_16x16x32_bf16 v[104:107], a[204:207], v[72:75], v[104:107]\n"
"ds_write_addtid_b32 v102 offset:10624\n"
"v_mfma_f32_16x16x32_bf16 v[104:107], a[208:211], v[76:79], v[104:107]\n"
"s_waitcnt vmcnt(7)\n"
"ds_write_addtid_b32 v103 offset:15264\n"
"v_cmp_ne_u64 vcc, s[48:49], v[134:135]\n"
"v_mfma_f32_16x16x32_bf16 v[104:107], a[212:215], v[80:83], v[104:107]\n"
"v_mfma_f32_16x16x32_bf16 v[104:107], a[216:219], v[84:87], v[104:107]\n"
"s_cbranch_vccnz .LF_1s\n"
".LF_1r:\n"
"v_mfma_f32_16x16x32_bf16 v[104:107], a[220:223], v[88:91], v[104:107]\n"
"s_add_u32 s50, s50, 0x180000\n"
"s_and_b32 s56, s50, 0xfbffffff\n"
"v_mfma_f32_16x16x32_bf16 v[108:111], a[224:227], v[60:63], 0\n"
"s_waitcnt lgkmcnt(0)\n"
"buffer_load_dwordx4 v[100:103], v5, s[40:43], s56 offen offset:0 sc1\n"
"v_mfma_f32_16x16x32_bf16 v[108:111], a[228:231], v[64:67], v[108:111]\n"
"s_barrier\n"
"v_mfma_f32_16x16x32_bf16 v[108:111], a[232:235], v[68:71], v[108:111]\n"
"s_add_u32 s95, s50, 0x0\n"
"s_and_b32 s95, s95, 0xfbffffff\n"
"ds_read2st64_b32 v[148:149], v13 offset0:0 offset1:1\n"
"v_mfma_f32_16x16x32_bf16 v[108:111], a[236:239], v[72:75], v[108:111]\n"
"buffer_load_dwordx4 v[112:115], v10, s[40:43], s95 offen offset:0 sc1\n"
"ds_read_b128 v[60:63], v2 offset:1536\n"
"v_mfma_f32_16x16x32_bf16 v[108:111], a[240:243], v[76:79], v[108:111]\n"
"buffer_load_dwordx4 v[64:67], v5, s[40:43], s56 offen offset:1024 sc1\n"
"ds_read_b128 v[116:119], v3 offset:512\n"
"v_mfma_f32_16x16x32_bf16 v[108:111], a[244:247], v[80:83], v[108:111]\n"
"buffer_load_dwordx4 v[120:123], v10, s[40:43], s95 offen offset:1024 sc1\n"
"ds_read_b128 v[124:127], v4 offset:2560\n"
"v_mfma_f32_16x16x32_bf16 v[108:111], a[248:251], v[84:87], v[108:111]\n"
"buffer_load_dwordx4 v[128:131], v10, s[40:43], s95 offen offset:2048 sc1\n"
"ds_read_b128 v[132:135], v2 offset:1792\n"
"v_mfma_f32_16x16x32_bf16 v[108:111], a[252:255], v[88:91], v[108:111]\n"
"buffer_load_dwordx4 v[136:139], v10, s[40:43], s95 offen offset:3072 sc1\n"
"ds_read_b128 v[140:143], v3 offset:768\n"
"v_mfma_f32_16x16x32_bf16 v[84:87], v[92:95], a[0:3], 0\n"
"buffer_load_dwordx4 v[68:71], v6, s[40:43], s56 offen offset:0 sc1\n"
"ds_read_b128 v[144:147], v4 offset:2816\n"
"v_mfma_f32_16x16x32_bf16 v[84:87], v[36:39], a[4:7], v[84:87]\n"
"s_waitcnt vmcnt(13)\n"
"v_mfma_f32_16x16x32_bf16 v[84:87], v[40:43], a[8:11], v[84:87]\n"
"v_mfma_f32_16x16x32_bf16 v[84:87], v[44:47], a[12:15], v[84:87]\n"
"s_waitcnt vmcnt(11)\n"
"buffer_load_dwordx2 v[150:151], v9, s[40:43], s50 offen sc1\n"
"v_mfma_f32_16x16x32_bf16 v[84:87], v[48:51], a[16:19], v[84:87]\n"
"s_waitcnt lgkmcnt(3)\n"
"buffer_load_dwordx4 v[72:75], v6, s[40:43], s56 offen offset:1024 sc1\n"
"v_add_f32 v60, v60, v116\n"
"v_mfma_f32_16x16x32_bf16 v[84:87], v[52:55], a[20:23], v[84:87]\n"
"s_waitcnt vmcnt(11)\n"
"v_add_f32 v61, v61, v117\n"
"v_add_f32 v62, v62, v118\n"
"v_mfma_f32_16x16x32_bf16 v[84:87], v[56:59], a[24:27], v[84:87]\n"
"v_add_f32 v63, v63, v119\n"
"v_add_f32 v60, v60, v124\n"
"v_mfma_f32_16x16x32_bf16 v[84:87], v[96:99], a[28:31], v[84:87]\n"
"v_add_f32 v61, v61, v125\n"
"v_add_f32 v62, v62, v126\n"
"v_mfma_f32_16x16x32_bf16 v[116:119], v[92:95], a[32:35], 0\n"
"buffer_load_dwordx4 v[76:79], v7, s[40:43], s56 offen offset:0 sc1\n"
"v_add_f32 v63, v63, v127\n"
"v_mfma_f32_16x16x32_bf16 v[116:119], v[36:39], a[36:39], v[116:119]\n"
"ds_write_addtid_b32 v84 offset:1536\n"
"v_add_f32 v60, v60, v104\n"
"v_mfma_f32_16x16x32_bf16 v[116:119], v[40:43], a[40:43], v[116:119]\n"
"ds_write_addtid_b32 v85 offset:6176\n"
"v_add_f32 v61, v61, v105\n"
"v_mfma_f32_16x16x32_bf16 v[116:119], v[44:47], a[44:47], v[116:119]\n"
"ds_write_addtid_b32 v86 offset:10880\n"
"v_add_f32 v62, v62, v106\n"
"v_mfma_f32_16x16x32_bf16 v[116:119], v[48:51], a[48:51], v[116:119]\n"
"ds_write_addtid_b32 v87 offset:15520\n"
"buffer_load_dwordx4 v[80:83], v7, s[40:43], s56 offen offset:1024 sc1\n"
"v_mfma_f32_16x16x32_bf16 v[116:119], v[52:55], a[52:55], v[116:119]\n"
"v_add_f32 v63, v63, v107\n"
"v_mfma_f32_16x16x32_bf16 v[116:119], v[56:59], a[56:59], v[116:119]\n"
"s_add_u32 s92, s50, 0xffd00000\n"
"s_and_b32 s95, s92, 0xfbffffff\n"
"v_fma_f32 v124, v18, v149, v26\n"
"v_mfma_f32_16x16x32_bf16 v[116:119], v[96:99], a[60:63], v[116:119]\n"
"v_fma_f32 v125, v19, v149, v27\n"
"v_fma_f32 v126, v20, v149, v28\n"
"v_mfma_f32_16x16x32_bf16 v[104:107], v[92:95], a[64:67], 0\n"
"buffer_load_dwordx4 v[84:87], v8, s[40:43], s56 offen offset:0 sc1\n"
"v_fma_f32 v127, v21, v149, v29\n"
"v_mfma_f32_16x16x32_bf16 v[104:107], v[36:39], a[68:71], v[104:107]\n"
"ds_write_addtid_b32 v116 offset:1792\n"
"v_fma_f32 v60, v60, v148, v124\n"
"v_mfma_f32_16x16x32_bf16 v[104:107], v[40:43], a[72:75], v[104:107]\n"
"ds_write_addtid_b32 v117 offset:6432\n"
"v_fma_f32 v61, v61, v148, v125\n"
"v_mfma_f32_16x16x32_bf16 v[104:107], v[44:47], a[76:79], v[104:107]\n"
"ds_write_addtid_b32 v118 offset:11136\n"
"v_fma_f32 v62, v62, v148, v126\n"
"v_mfma_f32_16x16x32_bf16 v[104:107], v[48:51], a[80:83], v[104:107]\n"
"ds_write_addtid_b32 v119 offset:15776\n"
"buffer_load_dwordx4 v[88:91], v8, s[40:43], s56 offen offset:1024 sc1\n"
"v_mfma_f32_16x16x32_bf16 v[104:107], v[52:55], a[84:87], v[104:107]\n"
"v_fma_f32 v63, v63, v148, v127\n"
"v_mul_f32_e64 v60, v60, |v60|\n"
"v_mfma_f32_16x16x32_bf16 v[104:107], v[56:59], a[88:91], v[104:107]\n"
"v_mul_f32_e64 v61, v61, |v61|\n"
"v_mul_f32_e64 v62, v62, |v62|\n"
"v_mfma_f32_16x16x32_bf16 v[104:107], v[96:99], a[92:95], v[104:107]\n"
"v_mul_f32_e64 v63, v63, |v63|\n"
"v_cvt_pk_bf16_f32 v116, v60, v61\n"
"v_mfma_f32_16x16x32_bf16 v[124:127], v[92:95], a[96:99], 0\n"
"v_cvt_pk_bf16_f32 v117, v62, v63\n"
"v_pk_max_i16 v116, v116, 0\n"
"v_mfma_f32_16x16x32_bf16 v[124:127], v[36:39], a[100:103], v[124:127]\n"
"ds_write_addtid_b32 v104 offset:2048\n"
"v_pk_max_i16 v117, v117, 0\n"
"v_mfma_f32_16x16x32_bf16 v[124:127], v[40:43], a[104:107], v[124:127]\n"
"s_waitcnt lgkmcnt(9)\n"
"ds_write_addtid_b32 v105 offset:6688\n"
"v_add_f32 v132, v132, v140\n"
"v_mfma_f32_16x16x32_bf16 v[124:127], v[44:47], a[108:111], v[124:127]\n"
"s_waitcnt vmcnt(13)\n"
"buffer_store_dwordx2 v[0:1], v16, s[40:43], s93 offen offset:0 sc1\n"
"ds_write_addtid_b32 v106 offset:11392\n"
"v_add_f32 v133, v133, v141\n"
"v_mfma_f32_16x16x32_bf16 v[124:127], v[48:51], a[112:115], v[124:127]\n"
"ds_write_addtid_b32 v107 offset:16032\n"
"v_add_f32 v134, v134, v142\n"
"v_mfma_f32_16x16x32_bf16 v[124:127], v[52:55], a[116:119], v[124:127]\n"
"v_add_f32 v135, v135, v143\n"
"v_add_f32 v132, v132, v144\n"
"v_mfma_f32_16x16x32_bf16 v[124:127], v[56:59], a[120:123], v[124:127]\n"
"v_add_f32 v133, v133, v145\n"
"v_add_f32 v134, v134, v146\n"
"v_mfma_f32_16x16x32_bf16 v[124:127], v[96:99], a[124:127], v[124:127]\n"
"v_add_f32 v135, v135, v147\n"
"v_add_f32 v132, v132, v108\n"
"v_mfma_f32_16x16x32_bf16 v[60:63], v[92:95], a[128:131], 0\n"
"v_add_f32 v133, v133, v109\n"
"v_add_f32 v134, v134, v110\n"
"v_mfma_f32_16x16x32_bf16 v[60:63], v[36:39], a[132:135], v[60:63]\n"
"ds_write_addtid_b32 v124 offset:2304\n"
"v_add_f32 v135, v135, v111\n"
"v_mfma_f32_16x16x32_bf16 v[60:63], v[40:43], a[136:139], v[60:63]\n"
"ds_write_addtid_b32 v125 offset:6944\n"
"v_fma_f32 v104, v22, v149, v30\n"
"v_mfma_f32_16x16x32_bf16 v[60:63], v[44:47], a[140:143], v[60:63]\n"
"ds_write_addtid_b32 v126 offset:11648\n"
"v_fma_f32 v105, v23, v149, v31\n"
"v_mfma_f32_16x16x32_bf16 v[60:63], v[48:51], a[144:147], v[60:63]\n"
"ds_write_addtid_b32 v127 offset:16288\n"
"v_fma_f32 v106, v24, v149, v32\n"
"v_mfma_f32_16x16x32_bf16 v[60:63], v[52:55], a[148:151], v[60:63]\n"
"v_fma_f32 v107, v25, v149, v33\n"
"v_fma_f32 v132, v132, v148, v104\n"
"v_mfma_f32_16x16x32_bf16 v[60:63], v[56:59], a[152:155], v[60:63]\n"
"v_fma_f32 v133, v133, v148, v105\n"
"v_fma_f32 v134, v134, v148, v106\n"
"v_mfma_f32_16x16x32_bf16 v[60:63], v[96:99], a[156:159], v[60:63]\n"
"v_fma_f32 v135, v135, v148, v107\n"
"v_mul_f32_e64 v132, v132, |v132|\n"
"v_mfma_f32_16x16x32_bf16 v[108:111], v[92:95], a[160:163], 0\n"
"v_mul_f32_e64 v133, v133, |v133|\n"
"v_mul_f32_e64 v134, v134, |v134|\n"
"v_mfma_f32_16x16x32_bf16 v[108:111], v[36:39], a[164:167], v[108:111]\n"
"ds_write_addtid_b32 v60 offset:2560\n"
"v_mul_f32_e64 v135, v135, |v135|\n"
"v_mfma_f32_16x16x32_bf16 v[108:111], v[40:43], a[168:171], v[108:111]\n"
"ds_write_addtid_b32 v61 offset:7200\n"
"v_cvt_pk_bf16_f32 v118, v132, v133\n"
"v_mfma_f32_16x16x32_bf16 v[108:111], v[44:47], a[172:175], v[108:111]\n"
"ds_write_addtid_b32 v62 offset:11904\n"
"v_cvt_pk_bf16_f32 v119, v134, v135\n"
"v_mfma_f32_16x16x32_bf16 v[108:111], v[48:51], a[176:179], v[108:111]\n"
"ds_write_addtid_b32 v63 offset:16544\n"
"v_pk_max_i16 v118, v118, 0\n"
"v_mfma_f32_16x16x32_bf16 v[108:111], v[52:55], a[180:183], v[108:111]\n"
"v_pk_max_i16 v119, v119, 0\n"
"v_mfma_f32_16x16x32_bf16 v[108:111], v[56:59], a[184:187], v[108:111]\n"
"buffer_store_dwordx4 v[116:119], v15, s[40:43], s95 offen sc1\n"
"v_mfma_f32_16x16x32_bf16 v[108:111], v[96:99], a[188:191], v[108:111]\n"
"s_waitcnt vmcnt(9)\n"
"v_add_f32 v112, v112, v120\n"
"v_add_f32 v113, v113, v121\n"
"v_mfma_f32_16x16x32_bf16 v[124:127], a[192:195], v[92:95], 0\n"
"v_add_f32 v114, v114, v122\n"
"v_add_f32 v115, v115, v123\n"
"v_mfma_f32_16x16x32_bf16 v[124:127], a[196:199], v[36:39], v[124:127]\n"
"ds_write_addtid_b32 v108 offset:2816\n"
"v_add_f32 v128, v128, v136\n"
"v_mfma_f32_16x16x32_bf16 v[124:127], a[200:203], v[40:43], v[124:127]\n"
"ds_write_addtid_b32 v109 offset:7456\n"
"v_add_f32 v129, v129, v137\n"
"v_mfma_f32_16x16x32_bf16 v[124:127], a[204:207], v[44:47], v[124:127]\n"
"ds_write_addtid_b32 v110 offset:12160\n"
"v_add_f32 v130, v130, v138\n"
"v_mfma_f32_16x16x32_bf16 v[124:127], a[208:211], v[48:51], v[124:127]\n"
"s_waitcnt vmcnt(7)\n"
"ds_write_addtid_b32 v111 offset:16800\n"
"v_cmp_ne_u64 vcc, s[48:49], v[150:151]\n"
"v_mfma_f32_16x16x32_bf16 v[124:127], a[212:215], v[52:55], v[124:127]\n"
"v_add_f32 v131, v131, v139\n"
"v_add_f32 v112, v112, v128\n"
"v_mfma_f32_16x16x32_bf16 v[124:127], a[216:219], v[56:59], v[124:127]\n"
"s_cbranch_vccnz .LF_2s\n"
".LF_2r:\n"
"v_add_f32 v113, v113, v129\n"
"v_mfma_f32_16x16x32_bf16 v[124:127], a[220:223], v[96:99], v[124:127]\n"
"s_add_u32 s50, s50, 0x180000\n"
"s_and_b32 s56, s50, 0xfbffffff\n"
"v_add_f32 v114, v114, v130\n"
"v_mfma_f32_16x16x32_bf16 v[132:135], a[224:227], v[92:95], 0\n"
"s_waitcnt lgkmcnt(0)\n"
"buffer_load_dwordx4 v[104:107], v5, s[40:43], s56 offen offset:0 sc1\n"
"v_add_f32 v115, v115, v131\n"
"v_mfma_f32_16x16x32_bf16 v[132:135], a[228:231], v[36:39], v[132:135]\n"
"s_barrier\n"
"v_mfma_f32_16x16x32_bf16 v[132:135], a[232:235], v[40:43], v[132:135]\n"
"v_permlane32_swap_b32 v112, v114\n"
"v_mfma_f32_16x16x32_bf16 v[132:135], a[236:239], v[44:47], v[132:135]\n"
"v_permlane32_swap_b32 v113, v115\n"
"v_mfma_f32_16x16x32_bf16 v[132:135], a[240:243], v[48:51], v[132:135]\n"
"buffer_load_dwordx4 v[108:111], v5, s[40:43], s56 offen offset:1024 sc1\n"
"v_add_f32 v112, v112, v114\n"
"v_mfma_f32_16x16x32_bf16 v[132:135], a[244:247], v[52:55], v[132:135]\n"
"ds_read2st64_b32 v[138:139], v14 offset0:0 offset1:1\n"
"v_add_f32 v113, v113, v115\n"
"v_mfma_f32_16x16x32_bf16 v[132:135], a[248:251], v[56:59], v[132:135]\n"
"v_permlane16_swap_b32 v112, v113\n"
"v_mfma_f32_16x16x32_bf16 v[132:135], a[252:255], v[96:99], v[132:135]\n"
"ds_read_b128 v[128:131], v2 offset:0\n"
"v_add_f32 v112, v112, v113\n"
"s_branch .LF_top\n"
".LF_1s:\n"
"s_nop 7\n"
"s_cmp_ge_u32 s50, s53\n"
"s_cbranch_scc1 .LF_end\n"
"s_cmp_ge_u32 s50, s52\n"
"s_cbranch_scc1 .LF_1r\n"
"s_sub_u32 s58, s50, 0x0\n"
"s_mov_b32 s59, 200000\n"
".LF_1p:\n"
"buffer_load_dwordx2 v[134:135], v9, s[40:43], s58 offen sc1\n"
"s_waitcnt vmcnt(0)\n"
"v_cmp_ne_u64 vcc, s[48:49], v[134:135]\n"
"s_cbranch_vccz .LF_1px\n"
"s_sleep 1\n"
"s_sub_u32 s59, s59, 1\n"
"s_cbranch_scc0 .LF_1p\n"
"s_branch .LF_end\n"
".LF_1px:\n"
"s_branch .LF_1r\n"
".LF_2s:\n"
"s_nop 7\n"
"s_cmp_ge_u32 s50, s53\n"
"s_cbranch_scc1 .LF_end\n"
"s_cmp_ge_u32 s50, s52\n"
"s_cbranch_scc1 .LF_2r\n"
"s_sub_u32 s58, s50, 0x0\n"
"s_mov_b32 s59, 200000\n"
".LF_2p:\n"
"buffer_load_dwordx2 v[150:151], v9, s[40:43], s58 offen sc1\n"
"s_waitcnt vmcnt(0)\n"
"v_cmp_ne_u64 vcc, s[48:49], v[150:151]\n"
"s_cbranch_vccz .LF_2px\n"
"s_sleep 1\n"
"s_sub_u32 s59, s59, 1\n"
"s_cbranch_scc0 .LF_2p\n"
"s_branch .LF_end\n"
".LF_2px:\n"
"s_branch .LF_2r\n"
".LF_3s:\n"
"s_nop 7\n"
"s_cmp_ge_u32 s50, s53\n"
"s_cbranch_scc1 .LF_end\n"
"s_cmp_ge_u32 s50, s52\n"
"s_cbranch_scc1 .LF_3r\n"
"s_sub_u32 s58, s50, 0x0\n"
"s_mov_b32 s59, 200000\n"
".LF_3p:\n"
"buffer_load_dwordx2 v[136:137], v9, s[40:43], s58 offen sc1\n"
"s_waitcnt vmcnt(0)\n"
"v_cmp_ne_u64 vcc, s[48:49], v[136:137]\n"
"s_cbranch_vccz .LF_3px\n"
"s_sleep 1\n"
"s_sub_u32 s59, s59, 1\n"
"s_cbranch_scc0 .LF_3p\n"
"s_branch .LF_end\n"
".LF_3px:\n"
"s_branch .LF_3r\n"
".LF_4s:\n"
"s_nop 7\n"
"s_cmp_ge_u32 s50, s53\n"
"s_cbranch_scc1 .LF_end\n"
"s_cmp_ge_u32 s50, s52\n"
"s_cbranch_scc1 .LF_4r\n"
"s_sub_u32 s58, s50, 0x0\n"
"s_mov_b32 s59, 200000\n"
".LF_4p:\n"
"buffer_load_dwordx2 v[138:139], v9, s[40:43], s58 offen sc1\n"
"s_waitcnt vmcnt(0)\n"
"v_cmp_ne_u64 vcc, s[48:49], v[138:139]\n"
"s_cbranch_vccz .LF_4px\n"
"s_sleep 1\n"
"s_sub_u32 s59, s59, 1\n"
"s_cbranch_scc0 .LF_4p\n"
"s_branch .LF_end\n"
".LF_4px:\n"
"s_branch .LF_4r\n"
".LF_end:\n"
"s_waitcnt vmcnt(0) lgkmcnt(0)\n"
"s_endpgm\n"
      :: "{v2}"((unsigned)(lds0 + ((wid + 3) & 3) * 19104 + trp)), "{v3}"((unsigned)(lds0 + ((wid + 2) & 3) * 19104 + trp)), "{v4}"((unsigned)(lds0 + ((wid + 1) & 3) * 19104 + trp)), "{v5}"((unsigned)(lane * 16 + wid * 8192 + ((0 + rot) & 3) * 2048 + 65536)), "{v6}"((unsigned)(lane * 16 + wid * 8192 + ((1 + rot) & 3) * 2048 + 65536)), "{v7}"((unsigned)(lane * 16 + wid * 8192 + ((2 + rot) & 3) * 2048 + 65536)), "{v8}"((unsigned)(lane * 16 + wid * 8192 + ((3 + rot) & 3) * 2048 + 65536)), "{v9}"((unsigned)((lane & 31) * 8 + 352768 + F_GL * 1572864)), "{v10}"((unsigned)(lane * 16 + 344064)), "{v11}"((unsigned)(lds0 + ((wid + 0) & 3) * 19104 + 18592 + ((li >> 1) + 32 * (li & 1)) * 4)), "{v12}"((unsigned)(lds0 + ((wid + 1) & 3) * 19104 + 18592 + ((li >> 1) + 32 * (li & 1)) * 4)), "{v13}"((unsigned)(lds0 + ((wid + 2) & 3) * 19104 + 18592 + ((li >> 1) + 32 * (li & 1)) * 4)), "{v14}"((unsigned)(lds0 + ((wid + 3) & 3) * 19104 + 18592 + ((li >> 1) + 32 * (li & 1)) * 4)), "{v15}"((unsigned)(lane * 16 + pair * 1024 + 98304)), "{v16}"((unsigned)(lane ? DROP : pair * 8 + 353024)), "{v17}"((unsigned)(0x3727c5ac)), "{v26}"((unsigned)(bia[0])), "{v27}"((unsigned)(bia[1])), "{v28}"((unsigned)(bia[2])), "{v29}"((unsigned)(bia[3])), "{v30}"((unsigned)(bia[4])), "{v31}"((unsigned)(bia[5])), "{v32}"((unsigned)(bia[6])), "{v33}"((unsigned)(bia[7])), "{v36}"((unsigned)(vT + tsw)), "{v37}"((unsigned)(vT + (tsw ^ 8))), "{v38}"((unsigned)(vD + ((0 + rot) & 3) * 458752)), "{v39}"((unsigned)(vD + ((1 + rot) & 3) * 458752)), "{v40}"((unsigned)(vD + ((2 + rot) & 3) * 458752)), "{v41}"((unsigned)(vD + ((3 + rot) & 3) * 458752)), "{v42}"((unsigned)(lds0 + wid * 19104 + trp)), "{s[40:43]}"(rS), "{s[44:47]}"(rW), "{s[48:49]}"(wsp), "{s50}"((unsigned)(sA)), "{s51}"((unsigned)(sLo)), "{s52}"((unsigned)(sHi)), "{s53}"((unsigned)(sEx)), "{s54}"((unsigned)(sEnt)), "{s55}"((unsigned)(sLd)), "{s56}"((unsigned)(sA & 0xfbffffff)), "{s57}"((unsigned)(lds0 + wid * 19104)), "{s60}"((unsigned)(0x3a800000)), "{s61}"((unsigned)(sW0 + 64 * ((wid + 1) & 3))), "{s62}"((unsigned)(sW0 + 64 * ((wid + 2) & 3))), "{s63}"((unsigned)(sW0 + 64 * ((wid + 3) & 3))), "{s64}"((unsigned)(sW0 + 64 * ((wid + 4) & 3)))
      : "v255", "a255", "s95", "vcc", "scc", "memory");

  } else {
    const int kq = idx / 7, grp = idx % 7, f0 = kq * 28 + wid * 7, pg0 = grp < 4 ? grp * 5 : 4 + grp * 4, pair = pg0 + wid;
    const unsigned tsw = (kq & 1) * 8, sW0 = f0 * 32 * 2048 + pg0 * 64, vD = (lane >> 2) * 2048 + (lane & 3) * 16;
    const u32x4 rW = desc(a.w2, 3584 * 2048);
    if (idx == 0) {
      const unsigned sEx = sHi + H_EX * 3 * RS, sEnt = -wid & 3;
    asm volatile(
"s_nop 4\n"
"v_mov_b32 v186, 0\n"
"global_load_dwordx2 v[0:1], v186, s[48:49] sc1\n"
"v_mov_b32 v188, 0x3f803f80\n"
"v_mov_b32 v189, 0x3f803f80\n"
"v_mov_b32 v190, 0x3f803f80\n"
"v_mov_b32 v191, 0x3f803f80\n"
"s_add_u32 m0, s55, 0\n"
"s_add_u32 s58, s61, 0\n"
"buffer_load_dwordx4 v42, s[44:47], s58 offen lds\n"
"s_add_u32 m0, s55, 1024\n"
"s_add_u32 s58, s61, 32768\n"
"buffer_load_dwordx4 v42, s[44:47], s58 offen lds\n"
"s_add_u32 m0, s55, 2048\n"
"s_add_u32 s58, s62, 0\n"
"buffer_load_dwordx4 v42, s[44:47], s58 offen lds\n"
"s_add_u32 m0, s55, 3072\n"
"s_add_u32 s58, s62, 32768\n"
"buffer_load_dwordx4 v42, s[44:47], s58 offen lds\n"
"s_add_u32 m0, s55, 4096\n"
"s_add_u32 s58, s63, 0\n"
"buffer_load_dwordx4 v42, s[44:47], s58 offen lds\n"
"s_add_u32 m0, s55, 5120\n"
"s_add_u32 s58, s63, 32768\n"
"buffer_load_dwordx4 v42, s[44:47], s58 offen lds\n"
"s_add_u32 m0, s55, 6144\n"
"s_add_u32 s58, s64, 0\n"
"buffer_load_dwordx4 v42, s[44:47], s58 offen lds\n"
"s_add_u32 m0, s55, 7168\n"
"s_add_u32 s58, s64, 32768\n"
"buffer_load_dwordx4 v42, s[44:47], s58 offen lds\n"
"s_add_u32 m0, s55, 8192\n"
"s_add_u32 s58, s65, 0\n"
"buffer_load_dwordx4 v42, s[44:47], s58 offen lds\n"
"s_add_u32 m0, s55, 9216\n"
"s_add_u32 s58, s65, 32768\n"
"buffer_load_dwordx4 v42, s[44:47], s58 offen lds\n"
"s_add_u32 m0, s55, 10240\n"
"s_add_u32 s58, s61, 65536\n"
"buffer_load_dwordx4 v42, s[44:47], s58 offen lds\n"
"s_add_u32 m0, s55, 11264\n"
"s_add_u32 s58, s61, 98304\n"
"buffer_load_dwordx4 v42, s[44:47], s58 offen lds\n"
"s_add_u32 m0, s55, 12288\n"
"s_add_u32 s58, s62, 65536\n"
"buffer_load_dwordx4 v42, s[44:47], s58 offen lds\n"
"s_add_u32 m0, s55, 13312\n"
"s_add_u32 s58, s62, 98304\n"
"buffer_load_dwordx4 v42, s[44:47], s58 offen lds\n"
"s_add_u32 m0, s55, 14336\n"
"s_add_u32 s58, s63, 65536\n"
"buffer_load_dwordx4 v42, s[44:47], s58 offen lds\n"
"s_add_u32 m0, s55, 15360\n"
"s_add_u32 s58, s63, 98304\n"
"buffer_load_dwordx4 v42, s[44:47], s58 offen lds\n"
"s_add_u32 m0, s55, 16384\n"
"s_add_u32 s58, s64, 65536\n"
"buffer_load_dwordx4 v42, s[44:47], s58 offen lds\n"
"s_add_u32 m0, s55, 17408\n"
"s_add_u32 s58, s64, 98304\n"
"buffer_load_dwordx4 v42, s[44:47], s58 offen lds\n"
"s_add_u32 m0, s55, 18432\n"
"s_add_u32 s58, s65, 65536\n"
"buffer_load_dwordx4 v42, s[44:47], s58 offen lds\n"
"s_add_u32 m0, s55, 19456\n"
"s_add_u32 s58, s65, 98304\n"
"buffer_load_dwordx4 v42, s[44:47], s58 offen lds\n"
"s_add_u32 m0, s55, 20480\n"
"s_add_u32 s58, s61, 131072\n"
"buffer_load_dwordx4 v42, s[44:47], s58 offen lds\n"
"s_add_u32 m0, s55, 21504\n"
"s_add_u32 s58, s61, 163840\n"
"buffer_load_dwordx4 v42, s[44:47], s58 offen lds\n"
"s_add_u32 m0, s55, 22528\n"
"s_add_u32 s58, s62, 131072\n"
"buffer_load_dwordx4 v42, s[44:47], s58 offen lds\n"
"s_add_u32 m0, s55, 23552\n"
"s_add_u32 s58, s62, 163840\n"
"buffer_load_dwordx4 v42, s[44:47], s58 offen lds\n"
"s_add_u32 m0, s55, 24576\n"
"s_add_u32 s58, s63, 131072\n"
"buffer_load_dwordx4 v42, s[44:47], s58 offen lds\n"
"s_add_u32 m0, s55, 25600\n"
"s_add_u32 s58, s63, 163840\n"
"buffer_load_dwordx4 v42, s[44:47], s58 offen lds\n"
"s_add_u32 m0, s55, 26624\n"
"s_add_u32 s58, s64, 131072\n"
"buffer_load_dwordx4 v42, s[44:47], s58 offen lds\n"
"s_add_u32 m0, s55, 27648\n"
"s_add_u32 s58, s64, 163840\n"
"buffer_load_dwordx4 v42, s[44:47], s58 offen lds\n"
"s_add_u32 m0, s55, 28672\n"
"s_add_u32 s58, s65, 131072\n"
"buffer_load_dwordx4 v42, s[44:47], s58 offen lds\n"
"s_add_u32 m0, s55, 29696\n"
"s_add_u32 s58, s65, 163840\n"
"buffer_load_dwordx4 v42, s[44:47], s58 offen lds\n"
"s_add_u32 m0, s55, 30720\n"
"s_add_u32 s58, s61, 196608\n"
"buffer_load_dwordx4 v42, s[44:47], s58 offen lds\n"
"s_add_u32 m0, s55, 31744\n"
"s_add_u32 s58, s61, 229376\n"
"buffer_load_dwordx4 v42, s[44:47], s58 offen lds\n"
"s_add_u32 m0, s55, 32768\n"
"s_add_u32 s58, s62, 196608\n"
"buffer_load_dwordx4 v42, s[44:47], s58 offen lds\n"
"s_add_u32 m0, s55, 33792\n"
"s_add_u32 s58, s62, 229376\n"
"buffer_load_dwordx4 v42, s[44:47], s58 offen lds\n"
"s_add_u32 m0, s55, 34816\n"
"s_add_u32 s58, s63, 196608\n"
"buffer_load_dwordx4 v42, s[44:47], s58 offen lds\n"
"s_add_u32 m0, s55, 35840\n"
"s_add_u32 s58, s63, 229376\n"
"buffer_load_dwordx4 v42, s[44:47], s58 offen lds\n"
"s_add_u32 m0, s55, 36864\n"
"s_add_u32 s58, s64, 196608\n"
"buffer_load_dwordx4 v42, s[44:47], s58 offen lds\n"
"s_add_u32 m0, s55, 37888\n"
"s_add_u32 s58, s64, 229376\n"
"buffer_load_dwordx4 v42, s[44:47], s58 offen lds\n"
"s_add_u32 m0, s55, 38912\n"
"s_add_u32 s58, s65, 196608\n"
"buffer_load_dwordx4 v42, s[44:47], s58 offen lds\n"
"s_add_u32 m0, s55, 39936\n"
"s_add_u32 s58, s65, 229376\n"
"buffer_load_dwordx4 v42, s[44:47], s58 offen lds\n"
"s_waitcnt vmcnt(38)\n"
"ds_read_b64_tr_b16 a[0:1], v40 offset:0\n"
"ds_read_b64_tr_b16 a[2:3], v40 offset:256\n"
"ds_read_b64_tr_b16 a[28:29], v41 offset:0\n"
"ds_read_b64_tr_b16 a[30:31], v41 offset:256\n"
"v_add_u32 v0, 2, v0\n"
"v_add_u32 v1, 0x9e3779b9, v1\n"
"v_or_b32 v0, 1, v0\n"
"v_readfirstlane_b32 s49, v1\n"
"v_readfirstlane_b32 s48, v0\n"
"s_waitcnt vmcnt(36)\n"
"ds_read_b64_tr_b16 a[56:57], v40 offset:2048\n"
"ds_read_b64_tr_b16 a[58:59], v40 offset:2304\n"
"ds_read_b64_tr_b16 a[84:85], v41 offset:2048\n"
"ds_read_b64_tr_b16 a[86:87], v41 offset:2304\n"
"s_waitcnt lgkmcnt(4)\n"
"s_add_u32 m0, s55, 0\n"
"s_add_u32 s58, s61, 262144\n"
"buffer_load_dwordx4 v42, s[44:47], s58 offen lds\n"
"s_add_u32 m0, s55, 1024\n"
"s_add_u32 s58, s61, 294912\n"
"buffer_load_dwordx4 v42, s[44:47], s58 offen lds\n"
"s_waitcnt vmcnt(36)\n"
"ds_read_b64_tr_b16 a[112:113], v40 offset:4096\n"
"ds_read_b64_tr_b16 a[114:115], v40 offset:4352\n"
"ds_read_b64_tr_b16 a[140:141], v41 offset:4096\n"
"ds_read_b64_tr_b16 a[142:143], v41 offset:4352\n"
"s_waitcnt lgkmcnt(4)\n"
"s_add_u32 m0, s55, 2048\n"
"s_add_u32 s58, s62, 262144\n"
"buffer_load_dwordx4 v42, s[44:47], s58 offen lds\n"
"s_add_u32 m0, s55, 3072\n"
"s_add_u32 s58, s62, 294912\n"
"buffer_load_dwordx4 v42, s[44:47], s58 offen lds\n"
"s_waitcnt vmcnt(36)\n"
"ds_read_b64_tr_b16 a[168:169], v40 offset:6144\n"
"ds_read_b64_tr_b16 a[170:171], v40 offset:6400\n"
"ds_read_b64_tr_b16 a[196:197], v41 offset:6144\n"
"ds_read_b64_tr_b16 a[198:199], v41 offset:6400\n"
"s_waitcnt lgkmcnt(4)\n"
"s_add_u32 m0, s55, 4096\n"
"s_add_u32 s58, s63, 262144\n"
"buffer_load_dwordx4 v42, s[44:47], s58 offen lds\n"
"s_add_u32 m0, s55, 5120\n"
"s_add_u32 s58, s63, 294912\n"
"buffer_load_dwordx4 v42, s[44:47], s58 offen lds\n"
"s_waitcnt vmcnt(36)\n"
"ds_read_b64_tr_b16 a[224:225], v40 offset:8192\n"
"ds_read_b64_tr_b16 a[226:227], v40 offset:8448\n"
"ds_read_b64_tr_b16 a[252:253], v41 offset:8192\n"
"ds_read_b64_tr_b16 a[254:255], v41 offset:8448\n"
"s_waitcnt lgkmcnt(4)\n"
"s_add_u32 m0, s55, 6144\n"
"s_add_u32 s58, s64, 262144\n"
"buffer_load_dwordx4 v42, s[44:47], s58 offen lds\n"
"s_add_u32 m0, s55, 7168\n"
"s_add_u32 s58, s64, 294912\n"
"buffer_load_dwordx4 v42, s[44:47], s58 offen lds\n"
"s_waitcnt vmcnt(36)\n"
"ds_read_b64_tr_b16 a[4:5], v40 offset:10240\n"
"ds_read_b64_tr_b16 a[6:7], v40 offset:10496\n"
"ds_read_b64_tr_b16 a[32:33], v41 offset:10240\n"
"ds_read_b64_tr_b16 a[34:35], v41 offset:10496\n"
"s_waitcnt lgkmcnt(4)\n"
"s_add_u32 m0, s55, 8192\n"
"s_add_u32 s58, s65, 262144\n"
"buffer_load_dwordx4 v42, s[44:47], s58 offen lds\n"
"s_add_u32 m0, s55, 9216\n"
"s_add_u32 s58, s65, 294912\n"
"buffer_load_dwordx4 v42, s[44:47], s58 offen lds\n"
"s_waitcnt vmcnt(36)\n"
"ds_read_b64_tr_b16 a[60:61], v40 offset:12288\n"
"ds_read_b64_tr_b16 a[62:63], v40 offset:12544\n"
"ds_read_b64_tr_b16 a[88:89], v41 offset:12288\n"
"ds_read_b64_tr_b16 a[90:91], v41 offset:12544\n"
"s_waitcnt lgkmcnt(4)\n"
"s_add_u32 m0, s55, 10240\n"
"s_add_u32 s58, s61, 327680\n"
"buffer_load_dwordx4 v42, s[44:47], s58 offen lds\n"
"s_add_u32 m0, s55, 11264\n"
"s_add_u32 s58, s61, 360448\n"
"buffer_load_dwordx4 v42, s[44:47], s58 offen lds\n"
"s_waitcnt vmcnt(36)\n"
"ds_read_b64_tr_b16 a[116:117], v40 offset:14336\n"
"ds_read_b64_tr_b16 a[118:119], v40 offset:14592\n"
"ds_read_b64_tr_b16 a[144:145], v41 offset:14336\n"
"ds_read_b64_tr_b16 a[146:147], v41 offset:14592\n"
"s_waitcnt lgkmcnt(4)\n"
"s_add_u32 m0, s55, 12288\n"
"s_add_u32 s58, s62, 327680\n"
"buffer_load_dwordx4 v42, s[44:47], s58 offen lds\n"
"s_add_u32 m0, s55, 13312\n"
"s_add_u32 s58, s62, 360448\n"
"buffer_load_dwordx4 v42, s[44:47], s58 offen lds\n"
"s_waitcnt vmcnt(36)\n"
"ds_read_b64_tr_b16 a[172:173], v40 offset:16384\n"
"ds_read_b64_tr_b16 a[174:175], v40 offset:16640\n"
"ds_read_b64_tr_b16 a[200:201], v41 offset:16384\n"
"ds_read_b64_tr_b16 a[202:203], v41 offset:16640\n"
"s_waitcnt lgkmcnt(4)\n"
"s_add_u32 m0, s55, 14336\n"
"s_add_u32 s58, s63, 327680\n"
"buffer_load_dwordx4 v42, s[44:47], s58 offen lds\n"
"s_add_u32 m0, s55, 15360\n"
"s_add_u32 s58, s63, 360448\n"
"buffer_load_dwordx4 v42, s[44:47], s58 offen lds\n"
"s_waitcnt vmcnt(36)\n"
"ds_read_b64_tr_b16 a[228:229], v40 offset:18432\n"
"ds_read_b64_tr_b16 a[230:231], v40 offset:18688\n"
"ds_read_b64_tr_b16 v[16:17], v41 offset:18432\n"
"ds_read_b64_tr_b16 v[18:19], v41 offset:18688\n"
"s_waitcnt lgkmcnt(4)\n"
"s_add_u32 m0, s55, 16384\n"
"s_add_u32 s58, s64, 327680\n"
"buffer_load_dwordx4 v42, s[44:47], s58 offen lds\n"
"s_add_u32 m0, s55, 17408\n"
"s_add_u32 s58, s64, 360448\n"
"buffer_load_dwordx4 v42, s[44:47], s58 offen lds\n"
"s_waitcnt vmcnt(36)\n"
"ds_read_b64_tr_b16 a[8:9], v40 offset:20480\n"
"ds_read_b64_tr_b16 a[10:11], v40 offset:20736\n"
"ds_read_b64_tr_b16 a[36:37], v41 offset:20480\n"
"ds_read_b64_tr_b16 a[38:39], v41 offset:20736\n"
"s_waitcnt lgkmcnt(4)\n"
"s_add_u32 m0, s55, 18432\n"
"s_add_u32 s58, s65, 327680\n"
"buffer_load_dwordx4 v42, s[44:47], s58 offen lds\n"
"s_add_u32 m0, s55, 19456\n"
"s_add_u32 s58, s65, 360448\n"
"buffer_load_dwordx4 v42, s[44:47], s58 offen lds\n"
"s_waitcnt vmcnt(36)\n"
"ds_read_b64_tr_b16 a[64:65], v40 offset:22528\n"
"ds_read_b64_tr_b16 a[66:67], v40 offset:22784\n"
"ds_read_b64_tr_b16 a[92:93], v41 offset:22528\n"
"ds_read_b64_tr_b16 a[94:95], v41 offset:22784\n"
"s_waitcnt lgkmcnt(4)\n"
"s_add_u32 m0, s55, 20480\n"
"s_add_u32 s58, s61, 393216\n"
"buffer_load_dwordx4 v42, s[44:47], s58 offen lds\n"
"s_add_u32 m0, s55, 21504\n"
"s_add_u32 s58, s61, 425984\n"
"buffer_load_dwordx4 v42, s[44:47], s58 offen lds\n"
"s_waitcnt vmcnt(36)\n"
"ds_read_b64_tr_b16 a[120:121], v40 offset:24576\n"
"ds_read_b64_tr_b16 a[122:123], v40 offset:24832\n"
"ds_read_b64_tr_b16 a[148:149], v41 offset:24576\n"
"ds_read_b64_tr_b16 a[150:151], v41 offset:24832\n"
"s_waitcnt lgkmcnt(4)\n"
"s_add_u32 m0, s55, 22528\n"
"s_add_u32 s58, s62, 393216\n"
"buffer_load_dwordx4 v42, s[44:47], s58 offen lds\n"
"s_add_u32 m0, s55, 23552\n"
"s_add_u32 s58, s62, 425984\n"
"buffer_load_dwordx4 v42, s[44:47], s58 offen lds\n"
"s_waitcnt vmcnt(36)\n"
"ds_read_b64_tr_b16 a[176:177], v40 offset:26624\n"
"ds_read_b64_tr_b16 a[178:179], v40 offset:26880\n"
"ds_read_b64_tr_b16 a[204:205], v41 offset:26624\n"
"ds_read_b64_tr_b16 a[206:207], v41 offset:26880\n"
"s_waitcnt lgkmcnt(4)\n"
"s_add_u32 m0, s55, 24576\n"
"s_add_u32 s58, s63, 393216\n"
"buffer_load_dwordx4 v42, s[44:47], s58 offen lds\n"
"s_add_u32 m0, s55, 25600\n"
"s_add_u32 s58, s63, 425984\n"
"buffer_load_dwordx4 v42, s[44:47], s58 offen lds\n"
"s_waitcnt vmcnt(36)\n"
"ds_read_b64_tr_b16 a[232:233], v40 offset:28672\n"
"ds_read_b64_tr_b16 a[234:235], v40 offset:28928\n"
"ds_read_b64_tr_b16 v[20:21], v41 offset:28672\n"
"ds_read_b64_tr_b16 v[22:23], v41 offset:28928\n"
"s_waitcnt lgkmcnt(4)\n"
"s_add_u32 m0, s55, 26624\n"
"s_add_u32 s58, s64, 393216\n"
"buffer_load_dwordx4 v42, s[44:47], s58 offen lds\n"
"s_add_u32 m0, s55, 27648\n"
"s_add_u32 s58, s64, 425984\n"
"buffer_load_dwordx4 v42, s[44:47], s58 offen lds\n"
"s_waitcnt vmcnt(36)\n"
"ds_read_b64_tr_b16 a[12:13], v40 offset:30720\n"
"ds_read_b64_tr_b16 a[14:15], v40 offset:30976\n"
"ds_read_b64_tr_b16 a[40:41], v41 offset:30720\n"
"ds_read_b64_tr_b16 a[42:43], v41 offset:30976\n"
"s_waitcnt lgkmcnt(4)\n"
"s_add_u32 m0, s55, 28672\n"
"s_add_u32 s58, s65, 393216\n"
"buffer_load_dwordx4 v42, s[44:47], s58 offen lds\n"
"s_add_u32 m0, s55, 29696\n"
"s_add_u32 s58, s65, 425984\n"
"buffer_load_dwordx4 v42, s[44:47], s58 offen lds\n"
"s_waitcnt vmcnt(36)\n"
"ds_read_b64_tr_b16 a[68:69], v40 offset:32768\n"
"ds_read_b64_tr_b16 a[70:71], v40 offset:33024\n"
"ds_read_b64_tr_b16 a[96:97], v41 offset:32768\n"
"ds_read_b64_tr_b16 a[98:99], v41 offset:33024\n"
"s_waitcnt lgkmcnt(4)\n"
"s_waitcnt vmcnt(34)\n"
"ds_read_b64_tr_b16 a[124:125], v40 offset:34816\n"
"ds_read_b64_tr_b16 a[126:127], v40 offset:35072\n"
"ds_read_b64_tr_b16 a[152:153], v41 offset:34816\n"
"ds_read_b64_tr_b16 a[154:155], v41 offset:35072\n"
"s_waitcnt lgkmcnt(4)\n"
"s_waitcnt vmcnt(32)\n"
"ds_read_b64_tr_b16 a[180:181], v40 offset:36864\n"
"ds_read_b64_tr_b16 a[182:183], v40 offset:37120\n"
"ds_read_b64_tr_b16 a[208:209], v41 offset:36864\n"
"ds_read_b64_tr_b16 a[210:211], v41 offset:37120\n"
"s_waitcnt lgkmcnt(4)\n"
"s_waitcnt vmcnt(30)\n"
"ds_read_b64_tr_b16 a[236:237], v40 offset:38912\n"
"ds_read_b64_tr_b16 a[238:239], v40 offset:39168\n"
"ds_read_b64_tr_b16 v[24:25], v41 offset:38912\n"
"ds_read_b64_tr_b16 v[26:27], v41 offset:39168\n"
"s_waitcnt lgkmcnt(4)\n"
"s_waitcnt vmcnt(28)\n"
"ds_read_b64_tr_b16 a[16:17], v40 offset:0\n"
"ds_read_b64_tr_b16 a[18:19], v40 offset:256\n"
"ds_read_b64_tr_b16 a[44:45], v41 offset:0\n"
"ds_read_b64_tr_b16 a[46:47], v41 offset:256\n"
"s_waitcnt lgkmcnt(4)\n"
"s_waitcnt vmcnt(26)\n"
"ds_read_b64_tr_b16 a[72:73], v40 offset:2048\n"
"ds_read_b64_tr_b16 a[74:75], v40 offset:2304\n"
"ds_read_b64_tr_b16 a[100:101], v41 offset:2048\n"
"ds_read_b64_tr_b16 a[102:103], v41 offset:2304\n"
"s_waitcnt lgkmcnt(4)\n"
"s_waitcnt vmcnt(24)\n"
"ds_read_b64_tr_b16 a[128:129], v40 offset:4096\n"
"ds_read_b64_tr_b16 a[130:131], v40 offset:4352\n"
"ds_read_b64_tr_b16 a[156:157], v41 offset:4096\n"
"ds_read_b64_tr_b16 a[158:159], v41 offset:4352\n"
"s_waitcnt lgkmcnt(4)\n"
"s_waitcnt vmcnt(22)\n"
"ds_read_b64_tr_b16 a[184:185], v40 offset:6144\n"
"ds_read_b64_tr_b16 a[186:187], v40 offset:6400\n"
"ds_read_b64_tr_b16 a[212:213], v41 offset:6144\n"
"ds_read_b64_tr_b16 a[214:215], v41 offset:6400\n"
"s_waitcnt lgkmcnt(4)\n"
"s_waitcnt vmcnt(20)\n"
"ds_read_b64_tr_b16 a[240:241], v40 offset:8192\n"
"ds_read_b64_tr_b16 a[242:243], v40 offset:8448\n"
"ds_read_b64_tr_b16 v[28:29], v41 offset:8192\n"
"ds_read_b64_tr_b16 v[30:31], v41 offset:8448\n"
"s_waitcnt lgkmcnt(4)\n"
"s_waitcnt vmcnt(18)\n"
"ds_read_b64_tr_b16 a[20:21], v40 offset:10240\n"
"ds_read_b64_tr_b16 a[22:23], v40 offset:10496\n"
"ds_read_b64_tr_b16 a[48:49], v41 offset:10240\n"
"ds_read_b64_tr_b16 a[50:51], v41 offset:10496\n"
"s_waitcnt lgkmcnt(4)\n"
"s_waitcnt vmcnt(16)\n"
"ds_read_b64_tr_b16 a[76:77], v40 offset:12288\n"
"ds_read_b64_tr_b16 a[78:79], v40 offset:12544\n"
"ds_read_b64_tr_b16 a[104:105], v41 offset:12288\n"
"ds_read_b64_tr_b16 a[106:107], v41 offset:12544\n"
"s_waitcnt lgkmcnt(4)\n"
"s_waitcnt vmcnt(14)\n"
"ds_read_b64_tr_b16 a[132:133], v40 offset:14336\n"
"ds_read_b64_tr_b16 a[134:135], v40 offset:14592\n"
"ds_read_b64_tr_b16 a[160:161], v41 offset:14336\n"
"ds_read_b64_tr_b16 a[162:163], v41 offset:14592\n"
"s_waitcnt lgkmcnt(4)\n"
"s_waitcnt vmcnt(12)\n"
"ds_read_b64_tr_b16 a[188:189], v40 offset:16384\n"
"ds_read_b64_tr_b16 a[190:191], v40 offset:16640\n"
"ds_read_b64_tr_b16 a[216:217], v41 offset:16384\n"
"ds_read_b64_tr_b16 a[218:219], v41 offset:16640\n"
"s_waitcnt lgkmcnt(4)\n"
"s_waitcnt vmcnt(10)\n"
"ds_read_b64_tr_b16 a[244:245], v40 offset:18432\n"
"ds_read_b64_tr_b16 a[246:247], v40 offset:18688\n"
"ds_read_b64_tr_b16 v[32:33], v41 offset:18432\n"
"ds_read_b64_tr_b16 v[34:35], v41 offset:18688\n"
"s_waitcnt lgkmcnt(4)\n"
"s_waitcnt vmcnt(8)\n"
"ds_read_b64_tr_b16 a[24:25], v40 offset:20480\n"
"ds_read_b64_tr_b16 a[26:27], v40 offset:20736\n"
"ds_read_b64_tr_b16 a[52:53], v41 offset:20480\n"
"ds_read_b64_tr_b16 a[54:55], v41 offset:20736\n"
"s_waitcnt lgkmcnt(4)\n"
"s_waitcnt vmcnt(6)\n"
"ds_read_b64_tr_b16 a[80:81], v40 offset:22528\n"
"ds_read_b64_tr_b16 a[82:83], v40 offset:22784\n"
"ds_read_b64_tr_b16 a[108:109], v41 offset:22528\n"
"ds_read_b64_tr_b16 a[110:111], v41 offset:22784\n"
"s_waitcnt lgkmcnt(4)\n"
"s_waitcnt vmcnt(4)\n"
"ds_read_b64_tr_b16 a[136:137], v40 offset:24576\n"
"ds_read_b64_tr_b16 a[138:139], v40 offset:24832\n"
"ds_read_b64_tr_b16 a[164:165], v41 offset:24576\n"
"ds_read_b64_tr_b16 a[166:167], v41 offset:24832\n"
"s_waitcnt lgkmcnt(4)\n"
"s_waitcnt vmcnt(2)\n"
"ds_read_b64_tr_b16 a[192:193], v40 offset:26624\n"
"ds_read_b64_tr_b16 a[194:195], v40 offset:26880\n"
"ds_read_b64_tr_b16 a[220:221], v41 offset:26624\n"
"ds_read_b64_tr_b16 a[222:223], v41 offset:26880\n"
"s_waitcnt lgkmcnt(4)\n"
"s_waitcnt vmcnt(0)\n"
"ds_read_b64_tr_b16 a[248:249], v40 offset:28672\n"
"ds_read_b64_tr_b16 a[250:251], v40 offset:28928\n"
"ds_read_b64_tr_b16 v[36:37], v41 offset:28672\n"
"ds_read_b64_tr_b16 v[38:39], v41 offset:28928\n"
"s_waitcnt lgkmcnt(4)\n"
"s_waitcnt vmcnt(0)\n"
"s_waitcnt lgkmcnt(0)\n"
"s_mov_b32 m0, s57\n"
"s_barrier\n"
"v_mov_b32 v126, 0\n"
"v_mov_b32 v127, 0\n"
"v_mov_b32 v140, 0\n"
"v_mov_b32 v141, 0\n"
"v_mov_b32 v112, 0\n"
"v_mov_b32 v113, 0\n"
"v_mov_b32 v156, 0\n"
"v_mov_b32 v157, 0\n"
"s_mov_b32 s95, 0x80000000\n"
"s_mov_b32 s94, 0x80000000\n"
"s_mov_b32 s92, 0x80000000\n"
"s_mov_b32 s94, 0x80000000\n"
"s_mov_b32 s95, 0x80000000\n"
"s_mov_b32 s94, 0x80000000\n"
"s_mov_b32 s94, 0x80000000\n"
"s_mov_b32 s95, 0x80000000\n"
"s_mov_b32 s93, 0x80000000\n"
"v_mov_b32 v124, 0\n"
"v_mov_b32 v125, 0\n"
"s_cmp_eq_u32 s54, 0\n"
"s_cbranch_scc1 .LH_1s\n"
"s_cmp_eq_u32 s54, 1\n"
"s_cbranch_scc1 .LH_2s\n"
"s_cmp_eq_u32 s54, 2\n"
"s_cbranch_scc1 .LH_3s\n"
"s_cmp_eq_u32 s54, 3\n"
"s_cbranch_scc1 .LH_4s\n"
".p2align 6\n"
".LH_top:\n"
"v_mfma_f32_16x16x32_bf16 v[52:55], v[64:67], a[0:3], 0\n"
"v_mfma_f32_16x16x32_bf16 v[52:55], v[68:71], a[4:7], v[52:55]\n"
"v_mfma_f32_16x16x32_bf16 v[52:55], v[72:75], a[8:11], v[52:55]\n"
"buffer_load_dwordx4 v[40:43], v5, s[40:43], s56 offen offset:1024 sc1\n"
"v_mfma_f32_16x16x32_bf16 v[52:55], v[76:79], a[12:15], v[52:55]\n"
"s_waitcnt vmcnt(5)\n"
"v_mfma_f32_16x16x32_bf16 v[52:55], v[80:83], a[16:19], v[52:55]\n"
"s_waitcnt lgkmcnt(3)\n"
"v_add_f32 v104, v104, v48\n"
"v_add_f32 v105, v105, v49\n"
"v_mfma_f32_16x16x32_bf16 v[52:55], v[84:87], a[20:23], v[52:55]\n"
"s_waitcnt vmcnt(4)\n"
"v_add_f32 v106, v106, v50\n"
"v_add_f32 v107, v107, v51\n"
"v_mfma_f32_16x16x32_bf16 v[52:55], v[88:91], a[24:27], v[52:55]\n"
"s_waitcnt vmcnt(3)\n"
"buffer_load_dwordx4 v[44:47], v5, s[40:43], s56 offen offset:2048 sc1\n"
"v_add_f32 v104, v104, v112\n"
"v_mfma_f32_16x16x32_bf16 v[60:63], v[64:67], a[28:31], 0\n"
"buffer_store_dwordx2 v[0:1], v9, s[40:43], s95 offen offset:0 sc1\n"
"v_add_f32 v105, v105, v113\n"
"v_add_f32 v106, v106, v114\n"
"v_mfma_f32_16x16x32_bf16 v[60:63], v[68:71], a[32:35], v[60:63]\n"
"ds_write_addtid_b32 v52 offset:0\n"
"v_add_f32 v107, v107, v115\n"
"v_mfma_f32_16x16x32_bf16 v[60:63], v[72:75], a[36:39], v[60:63]\n"
"ds_write_addtid_b32 v53 offset:4640\n"
"v_add_f32 v104, v104, v100\n"
"v_mfma_f32_16x16x32_bf16 v[60:63], v[76:79], a[40:43], v[60:63]\n"
"ds_write_addtid_b32 v54 offset:9344\n"
"buffer_load_dwordx4 v[48:51], v5, s[40:43], s56 offen offset:3072 sc1\n"
"v_mfma_f32_16x16x32_bf16 v[60:63], v[80:83], a[44:47], v[60:63]\n"
"ds_write_addtid_b32 v55 offset:13984\n"
"v_add_f32 v105, v105, v101\n"
"v_mfma_f32_16x16x32_bf16 v[60:63], v[84:87], a[48:51], v[60:63]\n"
"v_add_f32 v106, v106, v102\n"
"v_add_f32 v107, v107, v103\n"
"v_mfma_f32_16x16x32_bf16 v[60:63], v[88:91], a[52:55], v[60:63]\n"
"s_add_u32 s94, s50, 0xffd00000\n"
"s_and_b32 s95, s94, 0xfbffffff\n"
"buffer_load_dwordx2 v[112:113], v7, s[40:43], s50 offen sc1\n"
"v_mfma_f32_16x16x32_bf16 v[92:95], v[64:67], a[56:59], 0\n"
"buffer_load_dwordx4 v[52:55], v6, s[40:43], s56 offen offset:0 sc1\n"
"v_cvt_pk_bf16_f32 v100, v104, v105\n"
"v_mfma_f32_16x16x32_bf16 v[92:95], v[68:71], a[60:63], v[92:95]\n"
"ds_write_addtid_b32 v60 offset:256\n"
"v_cvt_pk_bf16_f32 v101, v106, v107\n"
"v_mfma_f32_16x16x32_bf16 v[92:95], v[72:75], a[64:67], v[92:95]\n"
"s_waitcnt lgkmcnt(5)\n"
"ds_write_addtid_b32 v61 offset:4896\n"
"v_add_f32 v116, v116, v120\n"
"v_mfma_f32_16x16x32_bf16 v[92:95], v[76:79], a[68:71], v[92:95]\n"
"ds_write_addtid_b32 v62 offset:9600\n"
"v_add_f32 v117, v117, v121\n"
"v_mfma_f32_16x16x32_bf16 v[92:95], v[80:83], a[72:75], v[92:95]\n"
"ds_write_addtid_b32 v63 offset:14240\n"
"buffer_load_dwordx4 v[56:59], v6, s[40:43], s56 offen offset:1024 sc1\n"
"v_mfma_f32_16x16x32_bf16 v[92:95], v[84:87], a[76:79], v[92:95]\n"
"v_add_f32 v118, v118, v122\n"
"v_add_f32 v119, v119, v123\n"
"v_mfma_f32_16x16x32_bf16 v[92:95], v[88:91], a[80:83], v[92:95]\n"
"v_add_f32 v116, v116, v124\n"
"v_add_f32 v117, v117, v125\n"
"v_mfma_f32_16x16x32_bf16 v[104:107], v[64:67], a[84:87], 0\n"
"v_add_f32 v118, v118, v126\n"
"v_add_f32 v119, v119, v127\n"
"v_mfma_f32_16x16x32_bf16 v[104:107], v[68:71], a[88:91], v[104:107]\n"
"ds_write_addtid_b32 v92 offset:512\n"
"buffer_load_dwordx4 v[60:63], v6, s[40:43], s56 offen offset:2048 sc1\n"
"v_mfma_f32_16x16x32_bf16 v[104:107], v[72:75], a[92:95], v[104:107]\n"
"ds_write_addtid_b32 v93 offset:5152\n"
"v_add_f32 v116, v116, v108\n"
"v_mfma_f32_16x16x32_bf16 v[104:107], v[76:79], a[96:99], v[104:107]\n"
"ds_write_addtid_b32 v94 offset:9856\n"
"v_add_f32 v117, v117, v109\n"
"v_mfma_f32_16x16x32_bf16 v[104:107], v[80:83], a[100:103], v[104:107]\n"
"ds_write_addtid_b32 v95 offset:14496\n"
"v_add_f32 v118, v118, v110\n"
"v_mfma_f32_16x16x32_bf16 v[104:107], v[84:87], a[104:107], v[104:107]\n"
"v_add_f32 v119, v119, v111\n"
"v_cvt_pk_bf16_f32 v102, v116, v117\n"
"v_mfma_f32_16x16x32_bf16 v[104:107], v[88:91], a[108:111], v[104:107]\n"
"v_cvt_pk_bf16_f32 v103, v118, v119\n"
"v_mfma_f32_16x16x32_bf16 v[92:95], v[64:67], a[112:115], 0\n"
"buffer_store_dwordx4 v[100:103], v8, s[40:43], s95 offen sc1\n"
"v_mfma_f32_16x16x32_bf16 v[92:95], v[68:71], a[116:119], v[92:95]\n"
"ds_write_addtid_b32 v104 offset:768\n"
"v_mfma_f32_16x16x32_bf16 v[92:95], v[72:75], a[120:123], v[92:95]\n"
"ds_write_addtid_b32 v105 offset:5408\n"
"v_mfma_f32_16x16x32_bf16 v[92:95], v[76:79], a[124:127], v[92:95]\n"
"ds_write_addtid_b32 v106 offset:10112\n"
"v_mfma_f32_16x16x32_bf16 v[92:95], v[80:83], a[128:131], v[92:95]\n"
"ds_write_addtid_b32 v107 offset:14752\n"
"v_mfma_f32_16x16x32_bf16 v[92:95], v[84:87], a[132:135], v[92:95]\n"
"v_mfma_f32_16x16x32_bf16 v[92:95], v[88:91], a[136:139], v[92:95]\n"
"v_mfma_f32_16x16x32_bf16 v[100:103], v[64:67], a[140:143], 0\n"
"v_mfma_f32_16x16x32_bf16 v[100:103], v[68:71], a[144:147], v[100:103]\n"
"ds_write_addtid_b32 v92 offset:1024\n"
"v_mfma_f32_16x16x32_bf16 v[100:103], v[72:75], a[148:151], v[100:103]\n"
"ds_write_addtid_b32 v93 offset:5664\n"
"v_mfma_f32_16x16x32_bf16 v[100:103], v[76:79], a[152:155], v[100:103]\n"
"ds_write_addtid_b32 v94 offset:10368\n"
"v_mfma_f32_16x16x32_bf16 v[100:103], v[80:83], a[156:159], v[100:103]\n"
"ds_write_addtid_b32 v95 offset:15008\n"
"v_mfma_f32_16x16x32_bf16 v[100:103], v[84:87], a[160:163], v[100:103]\n"
"v_mfma_f32_16x16x32_bf16 v[100:103], v[88:91], a[164:167], v[100:103]\n"
"v_mfma_f32_16x16x32_bf16 v[104:107], a[168:171], v[64:67], 0\n"
"v_mfma_f32_16x16x32_bf16 v[104:107], a[172:175], v[68:71], v[104:107]\n"
"ds_write_addtid_b32 v100 offset:1280\n"
"v_mfma_f32_16x16x32_bf16 v[104:107], a[176:179], v[72:75], v[104:107]\n"
"ds_write_addtid_b32 v101 offset:5920\n"
"v_mfma_f32_16x16x32_bf16 v[104:107], a[180:183], v[76:79], v[104:107]\n"
"ds_write_addtid_b32 v102 offset:10624\n"
"v_mfma_f32_16x16x32_bf16 v[104:107], a[184:187], v[80:83], v[104:107]\n"
"ds_write_addtid_b32 v103 offset:15264\n"
"v_mfma_f32_16x16x32_bf16 v[104:107], a[188:191], v[84:87], v[104:107]\n"
"v_mfma_f32_16x16x32_bf16 v[104:107], a[192:195], v[88:91], v[104:107]\n"
"v_mfma_f32_16x16x32_bf16 v[92:95], a[196:199], v[64:67], 0\n"
"v_mfma_f32_16x16x32_bf16 v[92:95], a[200:203], v[68:71], v[92:95]\n"
"v_mfma_f32_16x16x32_bf16 v[92:95], a[204:207], v[72:75], v[92:95]\n"
"v_mfma_f32_16x16x32_bf16 v[92:95], a[208:211], v[76:79], v[92:95]\n"
"v_mfma_f32_16x16x32_bf16 v[92:95], a[212:215], v[80:83], v[92:95]\n"
"v_mfma_f32_16x16x32_bf16 v[92:95], a[216:219], v[84:87], v[92:95]\n"
"v_mfma_f32_16x16x32_bf16 v[92:95], a[220:223], v[88:91], v[92:95]\n"
"v_mfma_f32_16x16x32_bf16 v[100:103], a[224:227], v[64:67], 0\n"
"v_mfma_f32_16x16x32_bf16 v[100:103], a[228:231], v[68:71], v[100:103]\n"
"v_mfma_f32_16x16x32_bf16 v[100:103], a[232:235], v[72:75], v[100:103]\n"
"v_mfma_f32_16x16x32_bf16 v[100:103], a[236:239], v[76:79], v[100:103]\n"
"v_mfma_f32_16x16x32_bf16 v[100:103], a[240:243], v[80:83], v[100:103]\n"
"v_mfma_f32_16x16x32_bf16 v[100:103], a[244:247], v[84:87], v[100:103]\n"
"s_waitcnt vmcnt(4)\n"
"v_cmp_ne_u64 vcc, s[48:49], v[112:113]\n"
"v_mfma_f32_16x16x32_bf16 v[100:103], a[248:251], v[88:91], v[100:103]\n"
"v_mfma_f32_16x16x32_bf16 v[108:111], a[252:255], v[64:67], 0\n"
"s_cbranch_vccnz .LH_3s\n"
".LH_3r:\n"
"v_mfma_f32_16x16x32_bf16 v[108:111], v[16:19], v[68:71], v[108:111]\n"
"s_waitcnt lgkmcnt(0)\n"
"v_mfma_f32_16x16x32_bf16 v[108:111], v[20:23], v[72:75], v[108:111]\n"
"s_barrier\n"
"v_mfma_f32_16x16x32_bf16 v[108:111], v[24:27], v[76:79], v[108:111]\n"
"ds_read_b128 v[112:115], v2 offset:2048\n"
"ds_read_b128 v[116:119], v3 offset:512\n"
"v_mfma_f32_16x16x32_bf16 v[108:111], v[28:31], v[80:83], v[108:111]\n"
"s_add_u32 s50, s50, 0x180000\n"
"s_and_b32 s56, s50, 0xfbffffff\n"
"ds_read_b128 v[76:79], v4 offset:3072\n"
"v_mfma_f32_16x16x32_bf16 v[108:111], v[32:35], v[84:87], v[108:111]\n"
"buffer_load_dwordx4 v[64:67], v5, s[40:43], s56 offen offset:0 sc1\n"
"ds_read_b128 v[120:123], v2 offset:2304\n"
"v_mfma_f32_16x16x32_bf16 v[108:111], v[36:39], v[88:91], v[108:111]\n"
"ds_read_b128 v[124:127], v3 offset:768\n"
"ds_read_b128 v[128:131], v4 offset:3328\n"
"v_mfma_f32_16x16x32_bf16 v[80:83], v[96:99], a[0:3], 0\n"
"ds_read_b128 v[132:135], v2 offset:3584\n"
"ds_read_b128 v[136:139], v3 offset:1536\n"
"v_mfma_f32_16x16x32_bf16 v[80:83], v[40:43], a[4:7], v[80:83]\n"
"ds_read_b128 v[140:143], v4 offset:3584\n"
"ds_read_b128 v[144:147], v2 offset:3840\n"
"v_mfma_f32_16x16x32_bf16 v[80:83], v[44:47], a[8:11], v[80:83]\n"
"buffer_load_dwordx4 v[68:71], v5, s[40:43], s56 offen offset:1024 sc1\n"
"ds_read_b128 v[148:151], v3 offset:1792\n"
"v_mfma_f32_16x16x32_bf16 v[80:83], v[48:51], a[12:15], v[80:83]\n"
"s_waitcnt vmcnt(4)\n"
"ds_read_b128 v[152:155], v4 offset:3840\n"
"v_mfma_f32_16x16x32_bf16 v[80:83], v[52:55], a[16:19], v[80:83]\n"
"s_waitcnt lgkmcnt(9)\n"
"v_add_f32 v112, v112, v116\n"
"v_add_f32 v113, v113, v117\n"
"v_mfma_f32_16x16x32_bf16 v[80:83], v[56:59], a[20:23], v[80:83]\n"
"s_waitcnt vmcnt(3)\n"
"v_add_f32 v114, v114, v118\n"
"v_add_f32 v115, v115, v119\n"
"v_mfma_f32_16x16x32_bf16 v[80:83], v[60:63], a[24:27], v[80:83]\n"
"s_waitcnt vmcnt(2)\n"
"buffer_load_dwordx4 v[72:75], v5, s[40:43], s56 offen offset:2048 sc1\n"
"v_add_f32 v112, v112, v76\n"
"v_mfma_f32_16x16x32_bf16 v[88:91], v[96:99], a[28:31], 0\n"
"buffer_store_dwordx2 v[0:1], v9, s[40:43], s94 offen offset:0 sc1\n"
"v_add_f32 v113, v113, v77\n"
"v_add_f32 v114, v114, v78\n"
"v_mfma_f32_16x16x32_bf16 v[88:91], v[40:43], a[32:35], v[88:91]\n"
"ds_write_addtid_b32 v80 offset:2048\n"
"v_add_f32 v115, v115, v79\n"
"v_mfma_f32_16x16x32_bf16 v[88:91], v[44:47], a[36:39], v[88:91]\n"
"ds_write_addtid_b32 v81 offset:6688\n"
"v_add_f32 v112, v112, v104\n"
"v_mfma_f32_16x16x32_bf16 v[88:91], v[48:51], a[40:43], v[88:91]\n"
"ds_write_addtid_b32 v82 offset:11392\n"
"buffer_load_dwordx4 v[76:79], v5, s[40:43], s56 offen offset:3072 sc1\n"
"v_mfma_f32_16x16x32_bf16 v[88:91], v[52:55], a[44:47], v[88:91]\n"
"ds_write_addtid_b32 v83 offset:16032\n"
"v_add_f32 v113, v113, v105\n"
"v_mfma_f32_16x16x32_bf16 v[88:91], v[56:59], a[48:51], v[88:91]\n"
"v_add_f32 v114, v114, v106\n"
"v_add_f32 v115, v115, v107\n"
"v_mfma_f32_16x16x32_bf16 v[88:91], v[60:63], a[52:55], v[88:91]\n"
"s_add_u32 s95, s50, 0xffd00000\n"
"s_and_b32 s94, s95, 0xfbffffff\n"
"buffer_load_dwordx2 v[156:157], v7, s[40:43], s50 offen sc1\n"
"v_mfma_f32_16x16x32_bf16 v[104:107], v[96:99], a[56:59], 0\n"
"s_add_u32 s93, s50, 0xff000000\n"
"buffer_load_dwordx4 v[80:83], v6, s[40:43], s56 offen offset:0 sc1\n"
"v_cvt_pk_bf16_f32 v116, v112, v113\n"
"v_mfma_f32_16x16x32_bf16 v[104:107], v[40:43], a[60:63], v[104:107]\n"
"ds_write_addtid_b32 v88 offset:2304\n"
"v_cvt_pk_bf16_f32 v117, v114, v115\n"
"v_mfma_f32_16x16x32_bf16 v[104:107], v[44:47], a[64:67], v[104:107]\n"
"s_waitcnt lgkmcnt(11)\n"
"ds_write_addtid_b32 v89 offset:6944\n"
"v_add_f32 v120, v120, v124\n"
"v_mfma_f32_16x16x32_bf16 v[104:107], v[48:51], a[68:71], v[104:107]\n"
"ds_write_addtid_b32 v90 offset:11648\n"
"v_add_f32 v121, v121, v125\n"
"v_mfma_f32_16x16x32_bf16 v[104:107], v[52:55], a[72:75], v[104:107]\n"
"ds_write_addtid_b32 v91 offset:16288\n"
"buffer_load_dwordx4 v[84:87], v6, s[40:43], s56 offen offset:1024 sc1\n"
"v_mfma_f32_16x16x32_bf16 v[104:107], v[56:59], a[76:79], v[104:107]\n"
"v_add_f32 v122, v122, v126\n"
"v_add_f32 v123, v123, v127\n"
"v_mfma_f32_16x16x32_bf16 v[104:107], v[60:63], a[80:83], v[104:107]\n"
"v_add_f32 v120, v120, v128\n"
"v_add_f32 v121, v121, v129\n"
"v_mfma_f32_16x16x32_bf16 v[112:115], v[96:99], a[84:87], 0\n"
"v_add_f32 v122, v122, v130\n"
"v_add_f32 v123, v123, v131\n"
"v_mfma_f32_16x16x32_bf16 v[112:115], v[40:43], a[88:91], v[112:115]\n"
"ds_write_addtid_b32 v104 offset:2560\n"
"buffer_load_dwordx4 v[88:91], v6, s[40:43], s56 offen offset:2048 sc1\n"
"v_mfma_f32_16x16x32_bf16 v[112:115], v[44:47], a[92:95], v[112:115]\n"
"ds_write_addtid_b32 v105 offset:7200\n"
"v_add_f32 v120, v120, v92\n"
"v_mfma_f32_16x16x32_bf16 v[112:115], v[48:51], a[96:99], v[112:115]\n"
"ds_write_addtid_b32 v106 offset:11904\n"
"v_add_f32 v121, v121, v93\n"
"v_mfma_f32_16x16x32_bf16 v[112:115], v[52:55], a[100:103], v[112:115]\n"
"ds_write_addtid_b32 v107 offset:16544\n"
"v_add_f32 v122, v122, v94\n"
"v_mfma_f32_16x16x32_bf16 v[112:115], v[56:59], a[104:107], v[112:115]\n"
"v_add_f32 v123, v123, v95\n"
"v_cvt_pk_bf16_f32 v118, v120, v121\n"
"v_mfma_f32_16x16x32_bf16 v[112:115], v[60:63], a[108:111], v[112:115]\n"
"s_cmp_lt_u32 s93, 0x8000000\n"
"s_cselect_b32 s93, 0x80000000, s93\n"
"v_cvt_pk_bf16_f32 v119, v122, v123\n"
"v_mfma_f32_16x16x32_bf16 v[92:95], v[96:99], a[112:115], 0\n"
"buffer_store_dwordx4 v[116:119], v8, s[40:43], s94 offen sc1\n"
"buffer_load_dwordx2 v[124:125], v12, s[40:43], s93 offen offset:0 sc1\n"
"v_mfma_f32_16x16x32_bf16 v[92:95], v[40:43], a[116:119], v[92:95]\n"
"s_waitcnt lgkmcnt(15)\n"
"ds_write_addtid_b32 v112 offset:2816\n"
"v_add_f32 v132, v132, v136\n"
"v_mfma_f32_16x16x32_bf16 v[92:95], v[44:47], a[120:123], v[92:95]\n"
"ds_write_addtid_b32 v113 offset:7456\n"
"v_add_f32 v133, v133, v137\n"
"v_mfma_f32_16x16x32_bf16 v[92:95], v[48:51], a[124:127], v[92:95]\n"
"ds_write_addtid_b32 v114 offset:12160\n"
"v_add_f32 v134, v134, v138\n"
"v_mfma_f32_16x16x32_bf16 v[92:95], v[52:55], a[128:131], v[92:95]\n"
"ds_write_addtid_b32 v115 offset:16800\n"
"v_add_f32 v135, v135, v139\n"
"v_mfma_f32_16x16x32_bf16 v[92:95], v[56:59], a[132:135], v[92:95]\n"
"v_add_f32 v132, v132, v140\n"
"v_add_f32 v133, v133, v141\n"
"v_mfma_f32_16x16x32_bf16 v[92:95], v[60:63], a[136:139], v[92:95]\n"
"v_add_f32 v134, v134, v142\n"
"v_add_f32 v135, v135, v143\n"
"v_mfma_f32_16x16x32_bf16 v[104:107], v[96:99], a[140:143], 0\n"
"v_add_f32 v132, v132, v100\n"
"v_add_f32 v133, v133, v101\n"
"v_mfma_f32_16x16x32_bf16 v[104:107], v[40:43], a[144:147], v[104:107]\n"
"ds_write_addtid_b32 v92 offset:3072\n"
"v_add_f32 v134, v134, v102\n"
"v_mfma_f32_16x16x32_bf16 v[104:107], v[44:47], a[148:151], v[104:107]\n"
"ds_write_addtid_b32 v93 offset:7712\n"
"v_add_f32 v135, v135, v103\n"
"v_mfma_f32_16x16x32_bf16 v[104:107], v[48:51], a[152:155], v[104:107]\n"
"ds_write_addtid_b32 v94 offset:12416\n"
"v_cvt_pk_bf16_f32 v112, v132, v133\n"
"v_mfma_f32_16x16x32_bf16 v[104:107], v[52:55], a[156:159], v[104:107]\n"
"ds_write_addtid_b32 v95 offset:17056\n"
"v_cvt_pk_bf16_f32 v113, v134, v135\n"
"v_mfma_f32_16x16x32_bf16 v[104:107], v[56:59], a[160:163], v[104:107]\n"
"s_waitcnt lgkmcnt(15)\n"
"v_add_f32 v144, v144, v148\n"
"v_add_f32 v145, v145, v149\n"
"v_mfma_f32_16x16x32_bf16 v[104:107], v[60:63], a[164:167], v[104:107]\n"
"v_add_f32 v146, v146, v150\n"
"v_add_f32 v147, v147, v151\n"
"v_mfma_f32_16x16x32_bf16 v[92:95], v[96:99], a[224:227], 0\n"
"v_add_f32 v144, v144, v152\n"
"v_add_f32 v145, v145, v153\n"
"v_mfma_f32_16x16x32_bf16 v[92:95], v[40:43], a[228:231], v[92:95]\n"
"ds_write_addtid_b32 v104 offset:3328\n"
"v_add_f32 v146, v146, v154\n"
"v_mfma_f32_16x16x32_bf16 v[92:95], v[44:47], a[232:235], v[92:95]\n"
"ds_write_addtid_b32 v105 offset:7968\n"
"v_add_f32 v147, v147, v155\n"
"v_mfma_f32_16x16x32_bf16 v[92:95], v[48:51], a[236:239], v[92:95]\n"
"ds_write_addtid_b32 v106 offset:12672\n"
"v_add_f32 v144, v144, v108\n"
"v_mfma_f32_16x16x32_bf16 v[92:95], v[52:55], a[240:243], v[92:95]\n"
"ds_write_addtid_b32 v107 offset:17312\n"
"v_add_f32 v145, v145, v109\n"
"v_mfma_f32_16x16x32_bf16 v[92:95], v[56:59], a[244:247], v[92:95]\n"
"v_add_f32 v146, v146, v110\n"
"v_add_f32 v147, v147, v111\n"
"v_mfma_f32_16x16x32_bf16 v[92:95], v[60:63], a[248:251], v[92:95]\n"
"v_cvt_pk_bf16_f32 v114, v144, v145\n"
"v_cvt_pk_bf16_f32 v115, v146, v147\n"
"v_mfma_f32_16x16x32_bf16 v[100:103], v[96:99], a[252:255], 0\n"
"buffer_store_dwordx4 v[112:115], v10, s[40:43], s94 offen sc1\n"
"v_mfma_f32_16x16x32_bf16 v[100:103], v[40:43], v[16:19], v[100:103]\n"
"ds_write_addtid_b32 v92 offset:3584\n"
"v_mfma_f32_16x16x32_bf16 v[100:103], v[44:47], v[20:23], v[100:103]\n"
"ds_write_addtid_b32 v93 offset:8224\n"
"v_mfma_f32_16x16x32_bf16 v[100:103], v[48:51], v[24:27], v[100:103]\n"
"ds_write_addtid_b32 v94 offset:12928\n"
"v_mfma_f32_16x16x32_bf16 v[100:103], v[52:55], v[28:31], v[100:103]\n"
"ds_write_addtid_b32 v95 offset:17568\n"
"v_mfma_f32_16x16x32_bf16 v[100:103], v[56:59], v[32:35], v[100:103]\n"
"v_mfma_f32_16x16x32_bf16 v[100:103], v[60:63], v[36:39], v[100:103]\n"
"v_mfma_f32_16x16x32_bf16 v[104:107], a[168:171], v[96:99], 0\n"
"v_mfma_f32_16x16x32_bf16 v[104:107], a[172:175], v[40:43], v[104:107]\n"
"ds_write_addtid_b32 v100 offset:3840\n"
"v_mfma_f32_16x16x32_bf16 v[104:107], a[176:179], v[44:47], v[104:107]\n"
"ds_write_addtid_b32 v101 offset:8480\n"
"v_mfma_f32_16x16x32_bf16 v[104:107], a[180:183], v[48:51], v[104:107]\n"
"ds_write_addtid_b32 v102 offset:13184\n"
"v_mfma_f32_16x16x32_bf16 v[104:107], a[184:187], v[52:55], v[104:107]\n"
"ds_write_addtid_b32 v103 offset:17824\n"
"v_mfma_f32_16x16x32_bf16 v[104:107], a[188:191], v[56:59], v[104:107]\n"
"s_waitcnt vmcnt(6)\n"
"v_cmp_ne_u64 vcc, s[48:49], v[156:157]\n"
"v_mfma_f32_16x16x32_bf16 v[104:107], a[192:195], v[60:63], v[104:107]\n"
"v_mfma_f32_16x16x32_bf16 v[100:103], a[196:199], v[96:99], 0\n"
"s_cbranch_vccnz .LH_4s\n"
".LH_4r:\n"
"v_mfma_f32_16x16x32_bf16 v[100:103], a[200:203], v[40:43], v[100:103]\n"
"s_waitcnt lgkmcnt(0)\n"
"v_mfma_f32_16x16x32_bf16 v[100:103], a[204:207], v[44:47], v[100:103]\n"
"s_barrier\n"
"v_mfma_f32_16x16x32_bf16 v[100:103], a[208:211], v[48:51], v[100:103]\n"
"ds_read_b128 v[92:95], v2 offset:0\n"
"ds_read_b128 v[96:99], v3 offset:2560\n"
"v_mfma_f32_16x16x32_bf16 v[100:103], a[212:215], v[52:55], v[100:103]\n"
"s_add_u32 s50, s50, 0x180000\n"
"s_and_b32 s56, s50, 0xfbffffff\n"
"ds_read_b128 v[108:111], v4 offset:1024\n"
"v_mfma_f32_16x16x32_bf16 v[100:103], a[216:219], v[56:59], v[100:103]\n"
"buffer_load_dwordx4 v[40:43], v5, s[40:43], s56 offen offset:0 sc1\n"
"ds_read_b128 v[112:115], v2 offset:256\n"
"v_mfma_f32_16x16x32_bf16 v[100:103], a[220:223], v[60:63], v[100:103]\n"
"ds_read_b128 v[116:119], v3 offset:2816\n"
"ds_read_b128 v[120:123], v4 offset:1280\n"
"v_mfma_f32_16x16x32_bf16 v[56:59], v[64:67], a[0:3], 0\n"
"v_mfma_f32_16x16x32_bf16 v[56:59], v[68:71], a[4:7], v[56:59]\n"
"v_mfma_f32_16x16x32_bf16 v[56:59], v[72:75], a[8:11], v[56:59]\n"
"buffer_load_dwordx4 v[44:47], v5, s[40:43], s56 offen offset:1024 sc1\n"
"v_mfma_f32_16x16x32_bf16 v[56:59], v[76:79], a[12:15], v[56:59]\n"
"s_waitcnt vmcnt(6)\n"
"v_mfma_f32_16x16x32_bf16 v[56:59], v[80:83], a[16:19], v[56:59]\n"
"s_waitcnt lgkmcnt(3)\n"
"v_add_f32 v92, v92, v96\n"
"v_add_f32 v93, v93, v97\n"
"v_mfma_f32_16x16x32_bf16 v[56:59], v[84:87], a[20:23], v[56:59]\n"
"s_waitcnt vmcnt(5)\n"
"v_add_f32 v94, v94, v98\n"
"v_add_f32 v95, v95, v99\n"
"v_mfma_f32_16x16x32_bf16 v[56:59], v[88:91], a[24:27], v[56:59]\n"
"s_waitcnt vmcnt(3)\n"
"buffer_load_dwordx4 v[48:51], v5, s[40:43], s56 offen offset:2048 sc1\n"
"v_add_f32 v92, v92, v108\n"
"v_mfma_f32_16x16x32_bf16 v[96:99], v[64:67], a[28:31], 0\n"
"v_add_f32 v93, v93, v109\n"
"v_add_f32 v94, v94, v110\n"
"v_mfma_f32_16x16x32_bf16 v[96:99], v[68:71], a[32:35], v[96:99]\n"
"ds_write_addtid_b32 v56 offset:0\n"
"v_add_f32 v95, v95, v111\n"
"v_mfma_f32_16x16x32_bf16 v[96:99], v[72:75], a[36:39], v[96:99]\n"
"ds_write_addtid_b32 v57 offset:4640\n"
"v_add_f32 v92, v92, v104\n"
"v_mfma_f32_16x16x32_bf16 v[96:99], v[76:79], a[40:43], v[96:99]\n"
"ds_write_addtid_b32 v58 offset:9344\n"
"buffer_load_dwordx4 v[52:55], v5, s[40:43], s56 offen offset:3072 sc1\n"
"v_mfma_f32_16x16x32_bf16 v[96:99], v[80:83], a[44:47], v[96:99]\n"
"ds_write_addtid_b32 v59 offset:13984\n"
"v_add_f32 v93, v93, v105\n"
"v_mfma_f32_16x16x32_bf16 v[96:99], v[84:87], a[48:51], v[96:99]\n"
"v_add_f32 v94, v94, v106\n"
"v_add_f32 v95, v95, v107\n"
"v_mfma_f32_16x16x32_bf16 v[96:99], v[88:91], a[52:55], v[96:99]\n"
"s_add_u32 s92, s50, 0xffd00000\n"
"s_and_b32 s94, s92, 0xfbffffff\n"
"buffer_load_dwordx2 v[126:127], v7, s[40:43], s50 offen sc1\n"
"v_mfma_f32_16x16x32_bf16 v[104:107], v[64:67], a[56:59], 0\n"
"buffer_load_dwordx4 v[56:59], v6, s[40:43], s56 offen offset:0 sc1\n"
"v_cvt_pk_bf16_f32 v108, v92, v93\n"
"v_mfma_f32_16x16x32_bf16 v[104:107], v[68:71], a[60:63], v[104:107]\n"
"ds_write_addtid_b32 v96 offset:256\n"
"v_cvt_pk_bf16_f32 v109, v94, v95\n"
"v_mfma_f32_16x16x32_bf16 v[104:107], v[72:75], a[64:67], v[104:107]\n"
"s_waitcnt lgkmcnt(5)\n"
"ds_write_addtid_b32 v97 offset:4896\n"
"v_add_f32 v112, v112, v116\n"
"v_mfma_f32_16x16x32_bf16 v[104:107], v[76:79], a[68:71], v[104:107]\n"
"ds_write_addtid_b32 v98 offset:9600\n"
"v_add_f32 v113, v113, v117\n"
"v_mfma_f32_16x16x32_bf16 v[104:107], v[80:83], a[72:75], v[104:107]\n"
"ds_write_addtid_b32 v99 offset:14240\n"
"buffer_load_dwordx4 v[60:63], v6, s[40:43], s56 offen offset:1024 sc1\n"
"v_mfma_f32_16x16x32_bf16 v[104:107], v[84:87], a[76:79], v[104:107]\n"
"v_add_f32 v114, v114, v118\n"
"v_add_f32 v115, v115, v119\n"
"v_mfma_f32_16x16x32_bf16 v[104:107], v[88:91], a[80:83], v[104:107]\n"
"v_add_f32 v112, v112, v120\n"
"v_add_f32 v113, v113, v121\n"
"v_mfma_f32_16x16x32_bf16 v[96:99], v[64:67], a[84:87], 0\n"
"v_add_f32 v114, v114, v122\n"
"v_add_f32 v115, v115, v123\n"
"v_mfma_f32_16x16x32_bf16 v[96:99], v[68:71], a[88:91], v[96:99]\n"
"ds_write_addtid_b32 v104 offset:512\n"
"buffer_load_dwordx4 v[92:95], v6, s[40:43], s56 offen offset:2048 sc1\n"
"v_mfma_f32_16x16x32_bf16 v[96:99], v[72:75], a[92:95], v[96:99]\n"
"ds_write_addtid_b32 v105 offset:5152\n"
"v_add_f32 v112, v112, v100\n"
"v_mfma_f32_16x16x32_bf16 v[96:99], v[76:79], a[96:99], v[96:99]\n"
"ds_write_addtid_b32 v106 offset:9856\n"
"v_add_f32 v113, v113, v101\n"
"v_mfma_f32_16x16x32_bf16 v[96:99], v[80:83], a[100:103], v[96:99]\n"
"ds_write_addtid_b32 v107 offset:14496\n"
"v_add_f32 v114, v114, v102\n"
"v_mfma_f32_16x16x32_bf16 v[96:99], v[84:87], a[104:107], v[96:99]\n"
"v_add_f32 v115, v115, v103\n"
"v_cvt_pk_bf16_f32 v110, v112, v113\n"
"v_mfma_f32_16x16x32_bf16 v[96:99], v[88:91], a[108:111], v[96:99]\n"
"s_waitcnt vmcnt(8)\n"
"buffer_store_dwordx2 v[0:1], v11, s[40:43], s95 offen offset:0 sc1\n"
"v_cvt_pk_bf16_f32 v111, v114, v115\n"
"v_mfma_f32_16x16x32_bf16 v[100:103], v[64:67], a[112:115], 0\n"
"buffer_store_dwordx4 v[108:111], v8, s[40:43], s94 offen sc1\n"
"v_mfma_f32_16x16x32_bf16 v[100:103], v[68:71], a[116:119], v[100:103]\n"
"ds_write_addtid_b32 v96 offset:768\n"
"v_mfma_f32_16x16x32_bf16 v[100:103], v[72:75], a[120:123], v[100:103]\n"
"ds_write_addtid_b32 v97 offset:5408\n"
"v_mfma_f32_16x16x32_bf16 v[100:103], v[76:79], a[124:127], v[100:103]\n"
"ds_write_addtid_b32 v98 offset:10112\n"
"v_cmp_ne_u64 vcc, s[48:49], v[124:125]\n"
"v_mfma_f32_16x16x32_bf16 v[100:103], v[80:83], a[128:131], v[100:103]\n"
"ds_write_addtid_b32 v99 offset:14752\n"
"v_mfma_f32_16x16x32_bf16 v[100:103], v[84:87], a[132:135], v[100:103]\n"
"s_cbranch_vccnz .LH_5s\n"
".LH_5r:\n"
"v_mfma_f32_16x16x32_bf16 v[100:103], v[88:91], a[136:139], v[100:103]\n"
"buffer_load_dwordx4 v[96:99], v13, s[40:43], s93 offen offset:0 sc1\n"
"v_mfma_f32_16x16x32_bf16 v[104:107], v[64:67], a[140:143], 0\n"
"buffer_load_dwordx4 v[108:111], v13, s[40:43], s93 offen offset:1024 sc1\n"
"v_mfma_f32_16x16x32_bf16 v[104:107], v[68:71], a[144:147], v[104:107]\n"
"ds_write_addtid_b32 v100 offset:1024\n"
"buffer_load_dwordx4 v[112:115], v13, s[40:43], s93 offen offset:2048 sc1\n"
"v_mfma_f32_16x16x32_bf16 v[104:107], v[72:75], a[148:151], v[104:107]\n"
"ds_write_addtid_b32 v101 offset:5664\n"
"buffer_load_dwordx4 v[116:119], v13, s[40:43], s93 offen offset:3072 sc1\n"
"v_mfma_f32_16x16x32_bf16 v[104:107], v[76:79], a[152:155], v[104:107]\n"
"ds_write_addtid_b32 v102 offset:10368\n"
"v_mfma_f32_16x16x32_bf16 v[104:107], v[80:83], a[156:159], v[104:107]\n"
"ds_write_addtid_b32 v103 offset:15008\n"
"v_mfma_f32_16x16x32_bf16 v[104:107], v[84:87], a[160:163], v[104:107]\n"
"v_mfma_f32_16x16x32_bf16 v[104:107], v[88:91], a[164:167], v[104:107]\n"
"v_mfma_f32_16x16x32_bf16 v[120:123], v[64:67], a[224:227], 0\n"
"v_mfma_f32_16x16x32_bf16 v[120:123], v[68:71], a[228:231], v[120:123]\n"
"ds_write_addtid_b32 v104 offset:1280\n"
"v_mfma_f32_16x16x32_bf16 v[120:123], v[72:75], a[232:235], v[120:123]\n"
"ds_write_addtid_b32 v105 offset:5920\n"
"v_mfma_f32_16x16x32_bf16 v[120:123], v[76:79], a[236:239], v[120:123]\n"
"ds_write_addtid_b32 v106 offset:10624\n"
"v_mfma_f32_16x16x32_bf16 v[120:123], v[80:83], a[240:243], v[120:123]\n"
"ds_write_addtid_b32 v107 offset:15264\n"
"v_mfma_f32_16x16x32_bf16 v[120:123], v[84:87], a[244:247], v[120:123]\n"
"v_mfma_f32_16x16x32_bf16 v[120:123], v[88:91], a[248:251], v[120:123]\n"
"v_mfma_f32_16x16x32_bf16 v[100:103], v[64:67], a[252:255], 0\n"
"v_mfma_f32_16x16x32_bf16 v[100:103], v[68:71], v[16:19], v[100:103]\n"
"ds_write_addtid_b32 v120 offset:1536\n"
"v_mfma_f32_16x16x32_bf16 v[100:103], v[72:75], v[20:23], v[100:103]\n"
"ds_write_addtid_b32 v121 offset:6176\n"
"v_mfma_f32_16x16x32_bf16 v[100:103], v[76:79], v[24:27], v[100:103]\n"
"ds_write_addtid_b32 v122 offset:10880\n"
"v_mfma_f32_16x16x32_bf16 v[100:103], v[80:83], v[28:31], v[100:103]\n"
"ds_write_addtid_b32 v123 offset:15520\n"
"v_mfma_f32_16x16x32_bf16 v[100:103], v[84:87], v[32:35], v[100:103]\n"
"v_mfma_f32_16x16x32_bf16 v[100:103], v[88:91], v[36:39], v[100:103]\n"
"v_mfma_f32_16x16x32_bf16 v[104:107], a[168:171], v[64:67], 0\n"
"v_mfma_f32_16x16x32_bf16 v[104:107], a[172:175], v[68:71], v[104:107]\n"
"ds_write_addtid_b32 v100 offset:1792\n"
"v_mfma_f32_16x16x32_bf16 v[104:107], a[176:179], v[72:75], v[104:107]\n"
"ds_write_addtid_b32 v101 offset:6432\n"
"v_mfma_f32_16x16x32_bf16 v[104:107], a[180:183], v[76:79], v[104:107]\n"
"ds_write_addtid_b32 v102 offset:11136\n"
"v_mfma_f32_16x16x32_bf16 v[104:107], a[184:187], v[80:83], v[104:107]\n"
"ds_write_addtid_b32 v103 offset:15776\n"
"v_mfma_f32_16x16x32_bf16 v[104:107], a[188:191], v[84:87], v[104:107]\n"
"s_waitcnt vmcnt(9)\n"
"v_cmp_ne_u64 vcc, s[48:49], v[126:127]\n"
"v_mfma_f32_16x16x32_bf16 v[104:107], a[192:195], v[88:91], v[104:107]\n"
"v_mfma_f32_16x16x32_bf16 v[100:103], a[196:199], v[64:67], 0\n"
"s_cbranch_vccnz .LH_1s\n"
".LH_1r:\n"
"v_mfma_f32_16x16x32_bf16 v[100:103], a[200:203], v[68:71], v[100:103]\n"
"s_waitcnt lgkmcnt(0)\n"
"v_mfma_f32_16x16x32_bf16 v[100:103], a[204:207], v[72:75], v[100:103]\n"
"s_barrier\n"
"v_mfma_f32_16x16x32_bf16 v[100:103], a[208:211], v[76:79], v[100:103]\n"
"ds_read_b128 v[120:123], v2 offset:2048\n"
"ds_read_b128 v[124:127], v3 offset:512\n"
"v_mfma_f32_16x16x32_bf16 v[100:103], a[212:215], v[80:83], v[100:103]\n"
"s_add_u32 s50, s50, 0x180000\n"
"s_and_b32 s56, s50, 0xfbffffff\n"
"ds_read_b128 v[76:79], v4 offset:3072\n"
"v_mfma_f32_16x16x32_bf16 v[100:103], a[216:219], v[84:87], v[100:103]\n"
"buffer_load_dwordx4 v[64:67], v5, s[40:43], s56 offen offset:0 sc1\n"
"ds_read_b128 v[128:131], v2 offset:2304\n"
"v_mfma_f32_16x16x32_bf16 v[100:103], a[220:223], v[88:91], v[100:103]\n"
"ds_read_b128 v[132:135], v3 offset:768\n"
"ds_read_b128 v[136:139], v4 offset:3328\n"
"v_mfma_f32_16x16x32_bf16 v[80:83], v[40:43], a[0:3], 0\n"
"v_mfma_f32_16x16x32_bf16 v[80:83], v[44:47], a[4:7], v[80:83]\n"
"v_mfma_f32_16x16x32_bf16 v[80:83], v[48:51], a[8:11], v[80:83]\n"
"buffer_load_dwordx4 v[68:71], v5, s[40:43], s56 offen offset:1024 sc1\n"
"v_mfma_f32_16x16x32_bf16 v[80:83], v[52:55], a[12:15], v[80:83]\n"
"s_waitcnt vmcnt(9)\n"
"v_mfma_f32_16x16x32_bf16 v[80:83], v[56:59], a[16:19], v[80:83]\n"
"s_waitcnt lgkmcnt(3)\n"
"v_add_f32 v120, v120, v124\n"
"v_add_f32 v121, v121, v125\n"
"v_mfma_f32_16x16x32_bf16 v[80:83], v[60:63], a[20:23], v[80:83]\n"
"s_waitcnt vmcnt(8)\n"
"v_add_f32 v122, v122, v126\n"
"v_add_f32 v123, v123, v127\n"
"v_mfma_f32_16x16x32_bf16 v[80:83], v[92:95], a[24:27], v[80:83]\n"
"s_waitcnt vmcnt(6)\n"
"buffer_load_dwordx4 v[72:75], v5, s[40:43], s56 offen offset:2048 sc1\n"
"v_add_f32 v120, v120, v76\n"
"v_mfma_f32_16x16x32_bf16 v[88:91], v[40:43], a[28:31], 0\n"
"buffer_store_dwordx2 v[0:1], v9, s[40:43], s92 offen offset:0 sc1\n"
"v_add_f32 v121, v121, v77\n"
"v_add_f32 v122, v122, v78\n"
"v_mfma_f32_16x16x32_bf16 v[88:91], v[44:47], a[32:35], v[88:91]\n"
"ds_write_addtid_b32 v80 offset:2048\n"
"v_add_f32 v123, v123, v79\n"
"v_mfma_f32_16x16x32_bf16 v[88:91], v[48:51], a[36:39], v[88:91]\n"
"ds_write_addtid_b32 v81 offset:6688\n"
"v_add_f32 v120, v120, v104\n"
"v_mfma_f32_16x16x32_bf16 v[88:91], v[52:55], a[40:43], v[88:91]\n"
"ds_write_addtid_b32 v82 offset:11392\n"
"buffer_load_dwordx4 v[76:79], v5, s[40:43], s56 offen offset:3072 sc1\n"
"v_mfma_f32_16x16x32_bf16 v[88:91], v[56:59], a[44:47], v[88:91]\n"
"ds_write_addtid_b32 v83 offset:16032\n"
"v_add_f32 v121, v121, v105\n"
"v_mfma_f32_16x16x32_bf16 v[88:91], v[60:63], a[48:51], v[88:91]\n"
"v_add_f32 v122, v122, v106\n"
"v_add_f32 v123, v123, v107\n"
"v_mfma_f32_16x16x32_bf16 v[88:91], v[92:95], a[52:55], v[88:91]\n"
"s_add_u32 s95, s50, 0xffd00000\n"
"s_and_b32 s94, s95, 0xfbffffff\n"
"buffer_load_dwordx2 v[140:141], v7, s[40:43], s50 offen sc1\n"
"v_mfma_f32_16x16x32_bf16 v[104:107], v[40:43], a[56:59], 0\n"
"buffer_load_dwordx4 v[80:83], v6, s[40:43], s56 offen offset:0 sc1\n"
"v_cvt_pk_bf16_f32 v124, v120, v121\n"
"v_mfma_f32_16x16x32_bf16 v[104:107], v[44:47], a[60:63], v[104:107]\n"
"s_waitcnt vmcnt(7)\n"
"ds_write_addtid_b32 v88 offset:2304\n"
"v_cvt_pk_bf16_f32 v125, v122, v123\n"
"v_mfma_f32_16x16x32_bf16 v[104:107], v[48:51], a[64:67], v[104:107]\n"
"s_waitcnt lgkmcnt(5)\n"
"ds_write_addtid_b32 v89 offset:6944\n"
"v_add_f32 v128, v128, v132\n"
"v_mfma_f32_16x16x32_bf16 v[104:107], v[52:55], a[68:71], v[104:107]\n"
"ds_write_addtid_b32 v90 offset:11648\n"
"v_add_f32 v129, v129, v133\n"
"v_mfma_f32_16x16x32_bf16 v[104:107], v[56:59], a[72:75], v[104:107]\n"
"ds_write_addtid_b32 v91 offset:16288\n"
"buffer_load_dwordx4 v[84:87], v6, s[40:43], s56 offen offset:1024 sc1\n"
"v_mfma_f32_16x16x32_bf16 v[104:107], v[60:63], a[76:79], v[104:107]\n"
"v_add_f32 v130, v130, v134\n"
"v_add_f32 v131, v131, v135\n"
"v_mfma_f32_16x16x32_bf16 v[104:107], v[92:95], a[80:83], v[104:107]\n"
"v_add_f32 v128, v128, v136\n"
"v_add_f32 v129, v129, v137\n"
"v_mfma_f32_16x16x32_bf16 v[120:123], v[40:43], a[84:87], 0\n"
"v_add_f32 v130, v130, v138\n"
"v_add_f32 v131, v131, v139\n"
"v_mfma_f32_16x16x32_bf16 v[120:123], v[44:47], a[88:91], v[120:123]\n"
"ds_write_addtid_b32 v104 offset:2560\n"
"buffer_load_dwordx4 v[88:91], v6, s[40:43], s56 offen offset:2048 sc1\n"
"v_mfma_f32_16x16x32_bf16 v[120:123], v[48:51], a[92:95], v[120:123]\n"
"ds_write_addtid_b32 v105 offset:7200\n"
"v_add_f32 v128, v128, v100\n"
"v_mfma_f32_16x16x32_bf16 v[120:123], v[52:55], a[96:99], v[120:123]\n"
"ds_write_addtid_b32 v106 offset:11904\n"
"v_add_f32 v129, v129, v101\n"
"v_mfma_f32_16x16x32_bf16 v[120:123], v[56:59], a[100:103], v[120:123]\n"
"ds_write_addtid_b32 v107 offset:16544\n"
"v_add_f32 v130, v130, v102\n"
"v_mfma_f32_16x16x32_bf16 v[120:123], v[60:63], a[104:107], v[120:123]\n"
"v_add_f32 v131, v131, v103\n"
"v_cvt_pk_bf16_f32 v126, v128, v129\n"
"v_mfma_f32_16x16x32_bf16 v[120:123], v[92:95], a[108:111], v[120:123]\n"
"v_cvt_pk_bf16_f32 v127, v130, v131\n"
"v_add_f32 v96, v96, v108\n"
"v_mfma_f32_16x16x32_bf16 v[100:103], v[40:43], a[112:115], 0\n"
"buffer_store_dwordx4 v[124:127], v8, s[40:43], s94 offen sc1\n"
"v_add_f32 v97, v97, v109\n"
"v_add_f32 v98, v98, v110\n"
"v_add_f32 v99, v99, v111\n"
"v_add_f32 v112, v112, v116\n"
"v_mfma_f32_16x16x32_bf16 v[100:103], v[44:47], a[116:119], v[100:103]\n"
"ds_write_addtid_b32 v120 offset:2816\n"
"v_add_f32 v113, v113, v117\n"
"v_mfma_f32_16x16x32_bf16 v[100:103], v[48:51], a[120:123], v[100:103]\n"
"ds_write_addtid_b32 v121 offset:7456\n"
"v_add_f32 v114, v114, v118\n"
"v_mfma_f32_16x16x32_bf16 v[100:103], v[52:55], a[124:127], v[100:103]\n"
"ds_write_addtid_b32 v122 offset:12160\n"
"v_add_f32 v115, v115, v119\n"
"v_mfma_f32_16x16x32_bf16 v[100:103], v[56:59], a[128:131], v[100:103]\n"
"ds_write_addtid_b32 v123 offset:16800\n"
"v_add_f32 v96, v96, v112\n"
"v_mfma_f32_16x16x32_bf16 v[100:103], v[60:63], a[132:135], v[100:103]\n"
"v_add_f32 v97, v97, v113\n"
"v_add_f32 v98, v98, v114\n"
"v_mfma_f32_16x16x32_bf16 v[100:103], v[92:95], a[136:139], v[100:103]\n"
"v_add_f32 v99, v99, v115\n"
"v_mfma_f32_16x16x32_bf16 v[104:107], v[40:43], a[140:143], 0\n"
"v_permlane32_swap_b32 v96, v98\n"
"v_mfma_f32_16x16x32_bf16 v[104:107], v[44:47], a[144:147], v[104:107]\n"
"ds_write_addtid_b32 v100 offset:3072\n"
"v_mfma_f32_16x16x32_bf16 v[104:107], v[48:51], a[148:151], v[104:107]\n"
"ds_write_addtid_b32 v101 offset:7712\n"
"v_mfma_f32_16x16x32_bf16 v[104:107], v[52:55], a[152:155], v[104:107]\n"
"ds_write_addtid_b32 v102 offset:12416\n"
"v_mfma_f32_16x16x32_bf16 v[104:107], v[56:59], a[156:159], v[104:107]\n"
"ds_write_addtid_b32 v103 offset:17056\n"
"v_mfma_f32_16x16x32_bf16 v[104:107], v[60:63], a[160:163], v[104:107]\n"
"v_permlane32_swap_b32 v97, v99\n"
"v_mfma_f32_16x16x32_bf16 v[104:107], v[92:95], a[164:167], v[104:107]\n"
"v_add_f32 v96, v96, v98\n"
"v_add_f32 v97, v97, v99\n"
"v_mfma_f32_16x16x32_bf16 v[100:103], v[40:43], a[224:227], 0\n"
"v_permlane16_swap_b32 v96, v97\n"
"v_mfma_f32_16x16x32_bf16 v[100:103], v[44:47], a[228:231], v[100:103]\n"
"ds_write_addtid_b32 v104 offset:3328\n"
"v_add_f32 v96, v96, v97\n"
"v_mfma_f32_16x16x32_bf16 v[100:103], v[48:51], a[232:235], v[100:103]\n"
"ds_write_addtid_b32 v105 offset:7968\n"
"v_add_f32_dpp v97, v96, v96 row_ror:8 row_mask:0xf bank_mask:0xf\n"
"v_mfma_f32_16x16x32_bf16 v[100:103], v[52:55], a[236:239], v[100:103]\n"
"ds_write_addtid_b32 v106 offset:12672\n"
"v_mov_b32 v96, v97\n"
"v_mfma_f32_16x16x32_bf16 v[100:103], v[56:59], a[240:243], v[100:103]\n"
"ds_write_addtid_b32 v107 offset:17312\n"
"v_mfma_f32_16x16x32_bf16 v[100:103], v[60:63], a[244:247], v[100:103]\n"
"v_permlane16_swap_b32 v97, v96\n"
"v_mfma_f32_16x16x32_bf16 v[100:103], v[92:95], a[248:251], v[100:103]\n"
"v_mul_f32 v97, 0xba800000, v97\n"
"v_fma_f32 v96, v96, s60, v15\n"
"v_mfma_f32_16x16x32_bf16 v[104:107], v[40:43], a[252:255], 0\n"
"v_fma_f32 v96, -v97, v97, v96\n"
"v_max_f32 v96, v15, v96\n"
"v_mfma_f32_16x16x32_bf16 v[104:107], v[44:47], v[16:19], v[104:107]\n"
"ds_write_addtid_b32 v100 offset:3584\n"
"v_mfma_f32_16x16x32_bf16 v[104:107], v[48:51], v[20:23], v[104:107]\n"
"ds_write_addtid_b32 v101 offset:8224\n"
"v_mfma_f32_16x16x32_bf16 v[104:107], v[52:55], v[24:27], v[104:107]\n"
"ds_write_addtid_b32 v102 offset:12928\n"
"v_mfma_f32_16x16x32_bf16 v[104:107], v[56:59], v[28:31], v[104:107]\n"
"ds_write_addtid_b32 v103 offset:17568\n"
"v_mfma_f32_16x16x32_bf16 v[104:107], v[60:63], v[32:35], v[104:107]\n"
"v_rsq_f32 v96, v96\n"
"v_mfma_f32_16x16x32_bf16 v[104:107], v[92:95], v[36:39], v[104:107]\n"
"v_mov_b32 v98, v0\n"
"v_mov_b32 v99, v1\n"
"v_mfma_f32_16x16x32_bf16 v[100:103], a[168:171], v[40:43], 0\n"
"buffer_store_dwordx4 v[96:99], v14, s[40:43], s93 offen sc1\n"
"v_mfma_f32_16x16x32_bf16 v[100:103], a[172:175], v[44:47], v[100:103]\n"
"ds_write_addtid_b32 v104 offset:3840\n"
"v_mfma_f32_16x16x32_bf16 v[100:103], a[176:179], v[48:51], v[100:103]\n"
"ds_write_addtid_b32 v105 offset:8480\n"
"v_mfma_f32_16x16x32_bf16 v[100:103], a[180:183], v[52:55], v[100:103]\n"
"ds_write_addtid_b32 v106 offset:13184\n"
"v_mfma_f32_16x16x32_bf16 v[100:103], a[184:187], v[56:59], v[100:103]\n"
"ds_write_addtid_b32 v107 offset:17824\n"
"v_mfma_f32_16x16x32_bf16 v[100:103], a[188:191], v[60:63], v[100:103]\n"
"s_waitcnt vmcnt(5)\n"
"v_cmp_ne_u64 vcc, s[48:49], v[140:141]\n"
"v_mfma_f32_16x16x32_bf16 v[100:103], a[192:195], v[92:95], v[100:103]\n"
"v_mfma_f32_16x16x32_bf16 v[108:111], a[196:199], v[40:43], 0\n"
"s_cbranch_vccnz .LH_2s\n"
".LH_2r:\n"
"v_mfma_f32_16x16x32_bf16 v[108:111], a[200:203], v[44:47], v[108:111]\n"
"s_waitcnt lgkmcnt(0)\n"
"v_mfma_f32_16x16x32_bf16 v[108:111], a[204:207], v[48:51], v[108:111]\n"
"s_barrier\n"
"v_mfma_f32_16x16x32_bf16 v[108:111], a[208:211], v[52:55], v[108:111]\n"
"ds_read_b128 v[104:107], v2 offset:0\n"
"ds_read_b128 v[48:51], v3 offset:2560\n"
"v_mfma_f32_16x16x32_bf16 v[108:111], a[212:215], v[56:59], v[108:111]\n"
"s_add_u32 s50, s50, 0x180000\n"
"s_and_b32 s56, s50, 0xfbffffff\n"
"ds_read_b128 v[112:115], v4 offset:1024\n"
"v_mfma_f32_16x16x32_bf16 v[108:111], a[216:219], v[60:63], v[108:111]\n"
"buffer_load_dwordx4 v[96:99], v5, s[40:43], s56 offen offset:0 sc1\n"
"ds_read_b128 v[116:119], v2 offset:256\n"
"v_mfma_f32_16x16x32_bf16 v[108:111], a[220:223], v[92:95], v[108:111]\n"
"ds_read_b128 v[120:123], v3 offset:2816\n"
"ds_read_b128 v[124:127], v4 offset:1280\n"
"s_branch .LH_top\n"
".LH_1s:\n"
"s_nop 7\n"
"s_cmp_ge_u32 s50, s53\n"
"s_cbranch_scc1 .LH_end\n"
"s_cmp_ge_u32 s50, s52\n"
"s_cbranch_scc1 .LH_1r\n"
"s_sub_u32 s58, s50, 0x0\n"
"s_mov_b32 s59, 200000\n"
".LH_1p:\n"
"buffer_load_dwordx2 v[126:127], v7, s[40:43], s58 offen sc1\n"
"s_waitcnt vmcnt(0)\n"
"v_cmp_ne_u64 vcc, s[48:49], v[126:127]\n"
"s_cbranch_vccz .LH_1px\n"
"s_sleep 1\n"
"s_sub_u32 s59, s59, 1\n"
"s_cbranch_scc0 .LH_1p\n"
"s_branch .LH_end\n"
".LH_1px:\n"
"s_branch .LH_1r\n"
".LH_2s:\n"
"s_nop 7\n"
"s_cmp_ge_u32 s50, s53\n"
"s_cbranch_scc1 .LH_end\n"
"s_cmp_ge_u32 s50, s52\n"
"s_cbranch_scc1 .LH_2r\n"
"s_sub_u32 s58, s50, 0x0\n"
"s_mov_b32 s59, 200000\n"
".LH_2p:\n"
"buffer_load_dwordx2 v[140:141], v7, s[40:43], s58 offen sc1\n"
"s_waitcnt vmcnt(0)\n"
"v_cmp_ne_u64 vcc, s[48:49], v[140:141]\n"
"s_cbranch_vccz .LH_2px\n"
"s_sleep 1\n"
"s_sub_u32 s59, s59, 1\n"
"s_cbranch_scc0 .LH_2p\n"
"s_branch .LH_end\n"
".LH_2px:\n"
"s_branch .LH_2r\n"
".LH_3s:\n"
"s_nop 7\n"
"s_cmp_ge_u32 s50, s53\n"
"s_cbranch_scc1 .LH_end\n"
"s_cmp_ge_u32 s50, s52\n"
"s_cbranch_scc1 .LH_3r\n"
"s_sub_u32 s58, s50, 0x0\n"
"s_mov_b32 s59, 200000\n"
".LH_3p:\n"
"buffer_load_dwordx2 v[112:113], v7, s[40:43], s58 offen sc1\n"
"s_waitcnt vmcnt(0)\n"
"v_cmp_ne_u64 vcc, s[48:49], v[112:113]\n"
"s_cbranch_vccz .LH_3px\n"
"s_sleep 1\n"
"s_sub_u32 s59, s59, 1\n"
"s_cbranch_scc0 .LH_3p\n"
"s_branch .LH_end\n"
".LH_3px:\n"
"s_branch .LH_3r\n"
".LH_4s:\n"
"s_nop 7\n"
"s_cmp_ge_u32 s50, s53\n"
"s_cbranch_scc1 .LH_end\n"
"s_cmp_ge_u32 s50, s52\n"
"s_cbranch_scc1 .LH_4r\n"
"s_sub_u32 s58, s50, 0x0\n"
"s_mov_b32 s59, 200000\n"
".LH_4p:\n"
"buffer_load_dwordx2 v[156:157], v7, s[40:43], s58 offen sc1\n"
"s_waitcnt vmcnt(0)\n"
"v_cmp_ne_u64 vcc, s[48:49], v[156:157]\n"
"s_cbranch_vccz .LH_4px\n"
"s_sleep 1\n"
"s_sub_u32 s59, s59, 1\n"
"s_cbranch_scc0 .LH_4p\n"
"s_branch .LH_end\n"
".LH_4px:\n"
"s_branch .LH_4r\n"
".LH_5s:\n"
"s_nop 7\n"
"s_cmp_ge_u32 s93, 0xc000000\n"
"s_cbranch_scc1 .LH_5r\n"
"s_mov_b32 s59, 200000\n"
".LH_5p:\n"
"buffer_load_dwordx2 v[124:125], v12, s[40:43], s93 offen offset:0 sc1\n"
"s_waitcnt vmcnt(0)\n"
"v_cmp_ne_u64 vcc, s[48:49], v[124:125]\n"
"s_cbranch_vccz .LH_5px\n"
"s_sleep 1\n"
"s_sub_u32 s59, s59, 1\n"
"s_cbranch_scc0 .LH_5p\n"
"s_branch .LH_end\n"
".LH_5px:\n"
"s_branch .LH_5r\n"
".LH_end:\n"
"s_waitcnt vmcnt(0) lgkmcnt(0)\n"
"s_endpgm\n"
      :: "{v2}"((unsigned)(lds0 + ((wid + 3) & 3) * 18592 + trp)), "{v3}"((unsigned)(lds0 + ((wid + 2) & 3) * 18592 + trp)), "{v4}"((unsigned)(lds0 + ((wid + 1) & 3) * 18592 + trp)), "{v5}"((unsigned)(lane * 16 + f0 * 1024 + 98304)), "{v6}"((unsigned)(lane * 16 + f0 * 1024 + 102400)), "{v7}"((unsigned)((f0 + lane % 7) * 8 + 353024 + H_GL * 1572864)), "{v8}"((unsigned)(lane * 16 + pair * 4096 + kq * 1024 + 212992)), "{v9}"((unsigned)(lane ? DROP : pair * 32 + kq * 8 + 354048)), "{v10}"((unsigned)(lane * 16 + (pg0 + 4) * 4096 + kq * 1024 + 212992)), "{v11}"((unsigned)(lane > 1 ? DROP : (lane ? pg0 + 4 : pair) * 32 + kq * 8 + 354048)), "{v12}"((unsigned)((lane & 31) * 8 + 67463936)), "{v13}"((unsigned)(lane * 16 + 67457024)), "{v14}"((unsigned)((lane & 24) ? DROP : (2 * (lane & 7) + (lane >> 5)) * 16 + 67461120)), "{v15}"((unsigned)(0x3727c5ac)), "{v40}"((unsigned)(vT + tsw)), "{v41}"((unsigned)(vT + (tsw ^ 8))), "{v42}"((unsigned)(vD)), "{s[40:43]}"(rS), "{s[44:47]}"(rW), "{s[48:49]}"(wsp), "{s50}"((unsigned)(sA)), "{s51}"((unsigned)(sLo)), "{s52}"((unsigned)(sHi)), "{s53}"((unsigned)(sEx)), "{s54}"((unsigned)(sEnt)), "{s55}"((unsigned)(sLd)), "{s56}"((unsigned)(sA & 0xfbffffff)), "{s57}"((unsigned)(lds0 + wid * 18592)), "{s60}"((unsigned)(0x3a800000)), "{s61}"((unsigned)(sW0 + 64 * ((wid + 1) & 3))), "{s62}"((unsigned)(sW0 + 64 * ((wid + 2) & 3))), "{s63}"((unsigned)(sW0 + 64 * ((wid + 3) & 3))), "{s64}"((unsigned)(sW0 + 64 * ((wid + 4) & 3))), "{s65}"((unsigned)(sW0 + 256))
      : "v255", "a255", "s95", "vcc", "scc", "memory");

    } else if (grp < 4) {
      const unsigned sEx = sHi + G_EX * 3 * RS, sEnt = -wid & 3;
    asm volatile(
"s_nop 4\n"
"v_mov_b32 v186, 0\n"
"global_load_dwordx2 v[0:1], v186, s[48:49] sc1\n"
"v_mov_b32 v188, 0x3f803f80\n"
"v_mov_b32 v189, 0x3f803f80\n"
"v_mov_b32 v190, 0x3f803f80\n"
"v_mov_b32 v191, 0x3f803f80\n"
"s_add_u32 m0, s55, 0\n"
"s_add_u32 s58, s60, 0\n"
"buffer_load_dwordx4 v38, s[44:47], s58 offen lds\n"
"s_add_u32 m0, s55, 1024\n"
"s_add_u32 s58, s60, 32768\n"
"buffer_load_dwordx4 v38, s[44:47], s58 offen lds\n"
"s_add_u32 m0, s55, 2048\n"
"s_add_u32 s58, s61, 0\n"
"buffer_load_dwordx4 v38, s[44:47], s58 offen lds\n"
"s_add_u32 m0, s55, 3072\n"
"s_add_u32 s58, s61, 32768\n"
"buffer_load_dwordx4 v38, s[44:47], s58 offen lds\n"
"s_add_u32 m0, s55, 4096\n"
"s_add_u32 s58, s62, 0\n"
"buffer_load_dwordx4 v38, s[44:47], s58 offen lds\n"
"s_add_u32 m0, s55, 5120\n"
"s_add_u32 s58, s62, 32768\n"
"buffer_load_dwordx4 v38, s[44:47], s58 offen lds\n"
"s_add_u32 m0, s55, 6144\n"
"s_add_u32 s58, s63, 0\n"
"buffer_load_dwordx4 v38, s[44:47], s58 offen lds\n"
"s_add_u32 m0, s55, 7168\n"
"s_add_u32 s58, s63, 32768\n"
"buffer_load_dwordx4 v38, s[44:47], s58 offen lds\n"
"s_add_u32 m0, s55, 8192\n"
"s_add_u32 s58, s64, 0\n"
"buffer_load_dwordx4 v38, s[44:47], s58 offen lds\n"
"s_add_u32 m0, s55, 9216\n"
"s_add_u32 s58, s64, 32768\n"
"buffer_load_dwordx4 v38, s[44:47], s58 offen lds\n"
"s_add_u32 m0, s55, 10240\n"
"s_add_u32 s58, s60, 65536\n"
"buffer_load_dwordx4 v38, s[44:47], s58 offen lds\n"
"s_add_u32 m0, s55, 11264\n"
"s_add_u32 s58, s60, 98304\n"
"buffer_load_dwordx4 v38, s[44:47], s58 offen lds\n"
"s_add_u32 m0, s55, 12288\n"
"s_add_u32 s58, s61, 65536\n"
"buffer_load_dwordx4 v38, s[44:47], s58 offen lds\n"
"s_add_u32 m0, s55, 13312\n"
"s_add_u32 s58, s61, 98304\n"
"buffer_load_dwordx4 v38, s[44:47], s58 offen lds\n"
"s_add_u32 m0, s55, 14336\n"
"s_add_u32 s58, s62, 65536\n"
"buffer_load_dwordx4 v38, s[44:47], s58 offen lds\n"
"s_add_u32 m0, s55, 15360\n"
"s_add_u32 s58, s62, 98304\n"
"buffer_load_dwordx4 v38, s[44:47], s58 offen lds\n"
"s_add_u32 m0, s55, 16384\n"
"s_add_u32 s58, s63, 65536\n"
"buffer_load_dwordx4 v38, s[44:47], s58 offen lds\n"
"s_add_u32 m0, s55, 17408\n"
"s_add_u32 s58, s63, 98304\n"
"buffer_load_dwordx4 v38, s[44:47], s58 offen lds\n"
"s_add_u32 m0, s55, 18432\n"
"s_add_u32 s58, s64, 65536\n"
"buffer_load_dwordx4 v38, s[44:47], s58 offen lds\n"
"s_add_u32 m0, s55, 19456\n"
"s_add_u32 s58, s64, 98304\n"
"buffer_load_dwordx4 v38, s[44:47], s58 offen lds\n"
"s_add_u32 m0, s55, 20480\n"
"s_add_u32 s58, s60, 131072\n"
"buffer_load_dwordx4 v38, s[44:47], s58 offen lds\n"
"s_add_u32 m0, s55, 21504\n"
"s_add_u32 s58, s60, 163840\n"
"buffer_load_dwordx4 v38, s[44:47], s58 offen lds\n"
"s_add_u32 m0, s55, 22528\n"
"s_add_u32 s58, s61, 131072\n"
"buffer_load_dwordx4 v38, s[44:47], s58 offen lds\n"
"s_add_u32 m0, s55, 23552\n"
"s_add_u32 s58, s61, 163840\n"
"buffer_load_dwordx4 v38, s[44:47], s58 offen lds\n"
"s_add_u32 m0, s55, 24576\n"
"s_add_u32 s58, s62, 131072\n"
"buffer_load_dwordx4 v38, s[44:47], s58 offen lds\n"
"s_add_u32 m0, s55, 25600\n"
"s_add_u32 s58, s62, 163840\n"
"buffer_load_dwordx4 v38, s[44:47], s58 offen lds\n"
"s_add_u32 m0, s55, 26624\n"
"s_add_u32 s58, s63, 131072\n"
"buffer_load_dwordx4 v38, s[44:47], s58 offen lds\n"
"s_add_u32 m0, s55, 27648\n"
"s_add_u32 s58, s63, 163840\n"
"buffer_load_dwordx4 v38, s[44:47], s58 offen lds\n"
"s_add_u32 m0, s55, 28672\n"
"s_add_u32 s58, s64, 131072\n"
"buffer_load_dwordx4 v38, s[44:47], s58 offen lds\n"
"s_add_u32 m0, s55, 29696\n"
"s_add_u32 s58, s64, 163840\n"
"buffer_load_dwordx4 v38, s[44:47], s58 offen lds\n"
"s_add_u32 m0, s55, 30720\n"
"s_add_u32 s58, s60, 196608\n"
"buffer_load_dwordx4 v38, s[44:47], s58 offen lds\n"
"s_add_u32 m0, s55, 31744\n"
"s_add_u32 s58, s60, 229376\n"
"buffer_load_dwordx4 v38, s[44:47], s58 offen lds\n"
"s_add_u32 m0, s55, 32768\n"
"s_add_u32 s58, s61, 196608\n"
"buffer_load_dwordx4 v38, s[44:47], s58 offen lds\n"
"s_add_u32 m0, s55, 33792\n"
"s_add_u32 s58, s61, 229376\n"
"buffer_load_dwordx4 v38, s[44:47], s58 offen lds\n"
"s_add_u32 m0, s55, 34816\n"
"s_add_u32 s58, s62, 196608\n"
"buffer_load_dwordx4 v38, s[44:47], s58 offen lds\n"
"s_add_u32 m0, s55, 35840\n"
"s_add_u32 s58, s62, 229376\n"
"buffer_load_dwordx4 v38, s[44:47], s58 offen lds\n"
"s_add_u32 m0, s55, 36864\n"
"s_add_u32 s58, s63, 196608\n"
"buffer_load_dwordx4 v38, s[44:47], s58 offen lds\n"
"s_add_u32 m0, s55, 37888\n"
"s_add_u32 s58, s63, 229376\n"
"buffer_load_dwordx4 v38, s[44:47], s58 offen lds\n"
"s_add_u32 m0, s55, 38912\n"
"s_add_u32 s58, s64, 196608\n"
"buffer_load_dwordx4 v38, s[44:47], s58 offen lds\n"
"s_add_u32 m0, s55, 39936\n"
"s_add_u32 s58, s64, 229376\n"
"buffer_load_dwordx4 v38, s[44:47], s58 offen lds\n"
"s_waitcnt vmcnt(38)\n"
"ds_read_b64_tr_b16 a[0:1], v36 offset:0\n"
"ds_read_b64_tr_b16 a[2:3], v36 offset:256\n"
"ds_read_b64_tr_b16 a[28:29], v37 offset:0\n"
"ds_read_b64_tr_b16 a[30:31], v37 offset:256\n"
"v_add_u32 v0, 2, v0\n"
"v_add_u32 v1, 0x9e3779b9, v1\n"
"v_or_b32 v0, 1, v0\n"
"v_readfirstlane_b32 s49, v1\n"
"v_readfirstlane_b32 s48, v0\n"
"s_waitcnt vmcnt(36)\n"
"ds_read_b64_tr_b16 a[56:57], v36 offset:2048\n"
"ds_read_b64_tr_b16 a[58:59], v36 offset:2304\n"
"ds_read_b64_tr_b16 a[84:85], v37 offset:2048\n"
"ds_read_b64_tr_b16 a[86:87], v37 offset:2304\n"
"s_waitcnt lgkmcnt(4)\n"
"s_add_u32 m0, s55, 0\n"
"s_add_u32 s58, s60, 262144\n"
"buffer_load_dwordx4 v38, s[44:47], s58 offen lds\n"
"s_add_u32 m0, s55, 1024\n"
"s_add_u32 s58, s60, 294912\n"
"buffer_load_dwordx4 v38, s[44:47], s58 offen lds\n"
"s_waitcnt vmcnt(36)\n"
"ds_read_b64_tr_b16 a[112:113], v36 offset:4096\n"
"ds_read_b64_tr_b16 a[114:115], v36 offset:4352\n"
"ds_read_b64_tr_b16 a[140:141], v37 offset:4096\n"
"ds_read_b64_tr_b16 a[142:143], v37 offset:4352\n"
"s_waitcnt lgkmcnt(4)\n"
"s_add_u32 m0, s55, 2048\n"
"s_add_u32 s58, s61, 262144\n"
"buffer_load_dwordx4 v38, s[44:47], s58 offen lds\n"
"s_add_u32 m0, s55, 3072\n"
"s_add_u32 s58, s61, 294912\n"
"buffer_load_dwordx4 v38, s[44:47], s58 offen lds\n"
"s_waitcnt vmcnt(36)\n"
"ds_read_b64_tr_b16 a[168:169], v36 offset:6144\n"
"ds_read_b64_tr_b16 a[170:171], v36 offset:6400\n"
"ds_read_b64_tr_b16 a[196:197], v37 offset:6144\n"
"ds_read_b64_tr_b16 a[198:199], v37 offset:6400\n"
"s_waitcnt lgkmcnt(4)\n"
"s_add_u32 m0, s55, 4096\n"
"s_add_u32 s58, s62, 262144\n"
"buffer_load_dwordx4 v38, s[44:47], s58 offen lds\n"
"s_add_u32 m0, s55, 5120\n"
"s_add_u32 s58, s62, 294912\n"
"buffer_load_dwordx4 v38, s[44:47], s58 offen lds\n"
"s_waitcnt vmcnt(36)\n"
"ds_read_b64_tr_b16 a[224:225], v36 offset:8192\n"
"ds_read_b64_tr_b16 a[226:227], v36 offset:8448\n"
"ds_read_b64_tr_b16 a[252:253], v37 offset:8192\n"
"ds_read_b64_tr_b16 a[254:255], v37 offset:8448\n"
"s_waitcnt lgkmcnt(4)\n"
"s_add_u32 m0, s55, 6144\n"
"s_add_u32 s58, s63, 262144\n"
"buffer_load_dwordx4 v38, s[44:47], s58 offen lds\n"
"s_add_u32 m0, s55, 7168\n"
"s_add_u32 s58, s63, 294912\n"
"buffer_load_dwordx4 v38, s[44:47], s58 offen lds\n"
"s_waitcnt vmcnt(36)\n"
"ds_read_b64_tr_b16 a[4:5], v36 offset:10240\n"
"ds_read_b64_tr_b16 a[6:7], v36 offset:10496\n"
"ds_read_b64_tr_b16 a[32:33], v37 offset:10240\n"
"ds_read_b64_tr_b16 a[34:35], v37 offset:10496\n"
"s_waitcnt lgkmcnt(4)\n"
"s_add_u32 m0, s55, 8192\n"
"s_add_u32 s58, s64, 262144\n"
"buffer_load_dwordx4 v38, s[44:47], s58 offen lds\n"
"s_add_u32 m0, s55, 9216\n"
"s_add_u32 s58, s64, 294912\n"
"buffer_load_dwordx4 v38, s[44:47], s58 offen lds\n"
"s_waitcnt vmcnt(36)\n"
"ds_read_b64_tr_b16 a[60:61], v36 offset:12288\n"
"ds_read_b64_tr_b16 a[62:63], v36 offset:12544\n"
"ds_read_b64_tr_b16 a[88:89], v37 offset:12288\n"
"ds_read_b64_tr_b16 a[90:91], v37 offset:12544\n"
"s_waitcnt lgkmcnt(4)\n"
"s_add_u32 m0, s55, 10240\n"
"s_add_u32 s58, s60, 327680\n"
"buffer_load_dwordx4 v38, s[44:47], s58 offen lds\n"
"s_add_u32 m0, s55, 11264\n"
"s_add_u32 s58, s60, 360448\n"
"buffer_load_dwordx4 v38, s[44:47], s58 offen lds\n"
"s_waitcnt vmcnt(36)\n"
"ds_read_b64_tr_b16 a[116:117], v36 offset:14336\n"
"ds_read_b64_tr_b16 a[118:119], v36 offset:14592\n"
"ds_read_b64_tr_b16 a[144:145], v37 offset:14336\n"
"ds_read_b64_tr_b16 a[146:147], v37 offset:14592\n"
"s_waitcnt lgkmcnt(4)\n"
"s_add_u32 m0, s55, 12288\n"
"s_add_u32 s58, s61, 327680\n"
"buffer_load_dwordx4 v38, s[44:47], s58 offen lds\n"
"s_add_u32 m0, s55, 13312\n"
"s_add_u32 s58, s61, 360448\n"
"buffer_load_dwordx4 v38, s[44:47], s58 offen lds\n"
"s_waitcnt vmcnt(36)\n"
"ds_read_b64_tr_b16 a[172:173], v36 offset:16384\n"
"ds_read_b64_tr_b16 a[174:175], v36 offset:16640\n"
"ds_read_b64_tr_b16 a[200:201], v37 offset:16384\n"
"ds_read_b64_tr_b16 a[202:203], v37 offset:16640\n"
"s_waitcnt lgkmcnt(4)\n"
"s_add_u32 m0, s55, 14336\n"
"s_add_u32 s58, s62, 327680\n"
"buffer_load_dwordx4 v38, s[44:47], s58 offen lds\n"
"s_add_u32 m0, s55, 15360\n"
"s_add_u32 s58, s62, 360448\n"
"buffer_load_dwordx4 v38, s[44:47], s58 offen lds\n"
"s_waitcnt vmcnt(36)\n"
"ds_read_b64_tr_b16 a[228:229], v36 offset:18432\n"
"ds_read_b64_tr_b16 a[230:231], v36 offset:18688\n"
"ds_read_b64_tr_b16 v[12:13], v37 offset:18432\n"
"ds_read_b64_tr_b16 v[14:15], v37 offset:18688\n"
"s_waitcnt lgkmcnt(4)\n"
"s_add_u32 m0, s55, 16384\n"
"s_add_u32 s58, s63, 327680\n"
"buffer_load_dwordx4 v38, s[44:47], s58 offen lds\n"
"s_add_u32 m0, s55, 17408\n"
"s_add_u32 s58, s63, 360448\n"
"buffer_load_dwordx4 v38, s[44:47], s58 offen lds\n"
"s_waitcnt vmcnt(36)\n"
"ds_read_b64_tr_b16 a[8:9], v36 offset:20480\n"
"ds_read_b64_tr_b16 a[10:11], v36 offset:20736\n"
"ds_read_b64_tr_b16 a[36:37], v37 offset:20480\n"
"ds_read_b64_tr_b16 a[38:39], v37 offset:20736\n"
"s_waitcnt lgkmcnt(4)\n"
"s_add_u32 m0, s55, 18432\n"
"s_add_u32 s58, s64, 327680\n"
"buffer_load_dwordx4 v38, s[44:47], s58 offen lds\n"
"s_add_u32 m0, s55, 19456\n"
"s_add_u32 s58, s64, 360448\n"
"buffer_load_dwordx4 v38, s[44:47], s58 offen lds\n"
"s_waitcnt vmcnt(36)\n"
"ds_read_b64_tr_b16 a[64:65], v36 offset:22528\n"
"ds_read_b64_tr_b16 a[66:67], v36 offset:22784\n"
"ds_read_b64_tr_b16 a[92:93], v37 offset:22528\n"
"ds_read_b64_tr_b16 a[94:95], v37 offset:22784\n"
"s_waitcnt lgkmcnt(4)\n"
"s_add_u32 m0, s55, 20480\n"
"s_add_u32 s58, s60, 393216\n"
"buffer_load_dwordx4 v38, s[44:47], s58 offen lds\n"
"s_add_u32 m0, s55, 21504\n"
"s_add_u32 s58, s60, 425984\n"
"buffer_load_dwordx4 v38, s[44:47], s58 offen lds\n"
"s_waitcnt vmcnt(36)\n"
"ds_read_b64_tr_b16 a[120:121], v36 offset:24576\n"
"ds_read_b64_tr_b16 a[122:123], v36 offset:24832\n"
"ds_read_b64_tr_b16 a[148:149], v37 offset:24576\n"
"ds_read_b64_tr_b16 a[150:151], v37 offset:24832\n"
"s_waitcnt lgkmcnt(4)\n"
"s_add_u32 m0, s55, 22528\n"
"s_add_u32 s58, s61, 393216\n"
"buffer_load_dwordx4 v38, s[44:47], s58 offen lds\n"
"s_add_u32 m0, s55, 23552\n"
"s_add_u32 s58, s61, 425984\n"
"buffer_load_dwordx4 v38, s[44:47], s58 offen lds\n"
"s_waitcnt vmcnt(36)\n"
"ds_read_b64_tr_b16 a[176:177], v36 offset:26624\n"
"ds_read_b64_tr_b16 a[178:179], v36 offset:26880\n"
"ds_read_b64_tr_b16 a[204:205], v37 offset:26624\n"
"ds_read_b64_tr_b16 a[206:207], v37 offset:26880\n"
"s_waitcnt lgkmcnt(4)\n"
"s_add_u32 m0, s55, 24576\n"
"s_add_u32 s58, s62, 393216\n"
"buffer_load_dwordx4 v38, s[44:47], s58 offen lds\n"
"s_add_u32 m0, s55, 25600\n"
"s_add_u32 s58, s62, 425984\n"
"buffer_load_dwordx4 v38, s[44:47], s58 offen lds\n"
"s_waitcnt vmcnt(36)\n"
"ds_read_b64_tr_b16 a[232:233], v36 offset:28672\n"
"ds_read_b64_tr_b16 a[234:235], v36 offset:28928\n"
"ds_read_b64_tr_b16 v[16:17], v37 offset:28672\n"
"ds_read_b64_tr_b16 v[18:19], v37 offset:28928\n"
"s_waitcnt lgkmcnt(4)\n"
"s_add_u32 m0, s55, 26624\n"
"s_add_u32 s58, s63, 393216\n"
"buffer_load_dwordx4 v38, s[44:47], s58 offen lds\n"
"s_add_u32 m0, s55, 27648\n"
"s_add_u32 s58, s63, 425984\n"
"buffer_load_dwordx4 v38, s[44:47], s58 offen lds\n"
"s_waitcnt vmcnt(36)\n"
"ds_read_b64_tr_b16 a[12:13], v36 offset:30720\n"
"ds_read_b64_tr_b16 a[14:15], v36 offset:30976\n"
"ds_read_b64_tr_b16 a[40:41], v37 offset:30720\n"
"ds_read_b64_tr_b16 a[42:43], v37 offset:30976\n"
"s_waitcnt lgkmcnt(4)\n"
"s_add_u32 m0, s55, 28672\n"
"s_add_u32 s58, s64, 393216\n"
"buffer_load_dwordx4 v38, s[44:47], s58 offen lds\n"
"s_add_u32 m0, s55, 29696\n"
"s_add_u32 s58, s64, 425984\n"
"buffer_load_dwordx4 v38, s[44:47], s58 offen lds\n"
"s_waitcnt vmcnt(36)\n"
"ds_read_b64_tr_b16 a[68:69], v36 offset:32768\n"
"ds_read_b64_tr_b16 a[70:71], v36 offset:33024\n"
"ds_read_b64_tr_b16 a[96:97], v37 offset:32768\n"
"ds_read_b64_tr_b16 a[98:99], v37 offset:33024\n"
"s_waitcnt lgkmcnt(4)\n"
"s_waitcnt vmcnt(34)\n"
"ds_read_b64_tr_b16 a[124:125], v36 offset:34816\n"
"ds_read_b64_tr_b16 a[126:127], v36 offset:35072\n"
"ds_read_b64_tr_b16 a[152:153], v37 offset:34816\n"
"ds_read_b64_tr_b16 a[154:155], v37 offset:35072\n"
"s_waitcnt lgkmcnt(4)\n"
"s_waitcnt vmcnt(32)\n"
"ds_read_b64_tr_b16 a[180:181], v36 offset:36864\n"
"ds_read_b64_tr_b16 a[182:183], v36 offset:37120\n"
"ds_read_b64_tr_b16 a[208:209], v37 offset:36864\n"
"ds_read_b64_tr_b16 a[210:211], v37 offset:37120\n"
"s_waitcnt lgkmcnt(4)\n"
"s_waitcnt vmcnt(30)\n"
"ds_read_b64_tr_b16 a[236:237], v36 offset:38912\n"
"ds_read_b64_tr_b16 a[238:239], v36 offset:39168\n"
"ds_read_b64_tr_b16 v[20:21], v37 offset:38912\n"
"ds_read_b64_tr_b16 v[22:23], v37 offset:39168\n"
"s_waitcnt lgkmcnt(4)\n"
"s_waitcnt vmcnt(28)\n"
"ds_read_b64_tr_b16 a[16:17], v36 offset:0\n"
"ds_read_b64_tr_b16 a[18:19], v36 offset:256\n"
"ds_read_b64_tr_b16 a[44:45], v37 offset:0\n"
"ds_read_b64_tr_b16 a[46:47], v37 offset:256\n"
"s_waitcnt lgkmcnt(4)\n"
"s_waitcnt vmcnt(26)\n"
"ds_read_b64_tr_b16 a[72:73], v36 offset:2048\n"
"ds_read_b64_tr_b16 a[74:75], v36 offset:2304\n"
"ds_read_b64_tr_b16 a[100:101], v37 offset:2048\n"
"ds_read_b64_tr_b16 a[102:103], v37 offset:2304\n"
"s_waitcnt lgkmcnt(4)\n"
"s_waitcnt vmcnt(24)\n"
"ds_read_b64_tr_b16 a[128:129], v36 offset:4096\n"
"ds_read_b64_tr_b16 a[130:131], v36 offset:4352\n"
"ds_read_b64_tr_b16 a[156:157], v37 offset:4096\n"
"ds_read_b64_tr_b16 a[158:159], v37 offset:4352\n"
"s_waitcnt lgkmcnt(4)\n"
"s_waitcnt vmcnt(22)\n"
"ds_read_b64_tr_b16 a[184:185], v36 offset:6144\n"
"ds_read_b64_tr_b16 a[186:187], v36 offset:6400\n"
"ds_read_b64_tr_b16 a[212:213], v37 offset:6144\n"
"ds_read_b64_tr_b16 a[214:215], v37 offset:6400\n"
"s_waitcnt lgkmcnt(4)\n"
"s_waitcnt vmcnt(20)\n"
"ds_read_b64_tr_b16 a[240:241], v36 offset:8192\n"
"ds_read_b64_tr_b16 a[242:243], v36 offset:8448\n"
"ds_read_b64_tr_b16 v[24:25], v37 offset:8192\n"
"ds_read_b64_tr_b16 v[26:27], v37 offset:8448\n"
"s_waitcnt lgkmcnt(4)\n"
"s_waitcnt vmcnt(18)\n"
"ds_read_b64_tr_b16 a[20:21], v36 offset:10240\n"
"ds_read_b64_tr_b16 a[22:23], v36 offset:10496\n"
"ds_read_b64_tr_b16 a[48:49], v37 offset:10240\n"
"ds_read_b64_tr_b16 a[50:51], v37 offset:10496\n"
"s_waitcnt lgkmcnt(4)\n"
"s_waitcnt vmcnt(16)\n"
"ds_read_b64_tr_b16 a[76:77], v36 offset:12288\n"
"ds_read_b64_tr_b16 a[78:79], v36 offset:12544\n"
"ds_read_b64_tr_b16 a[104:105], v37 offset:12288\n"
"ds_read_b64_tr_b16 a[106:107], v37 offset:12544\n"
"s_waitcnt lgkmcnt(4)\n"
"s_waitcnt vmcnt(14)\n"
"ds_read_b64_tr_b16 a[132:133], v36 offset:14336\n"
"ds_read_b64_tr_b16 a[134:135], v36 offset:14592\n"
"ds_read_b64_tr_b16 a[160:161], v37 offset:14336\n"
"ds_read_b64_tr_b16 a[162:163], v37 offset:14592\n"
"s_waitcnt lgkmcnt(4)\n"
"s_waitcnt vmcnt(12)\n"
"ds_read_b64_tr_b16 a[188:189], v36 offset:16384\n"
"ds_read_b64_tr_b16 a[190:191], v36 offset:16640\n"
"ds_read_b64_tr_b16 a[216:217], v37 offset:16384\n"
"ds_read_b64_tr_b16 a[218:219], v37 offset:16640\n"
"s_waitcnt lgkmcnt(4)\n"
"s_waitcnt vmcnt(10)\n"
"ds_read_b64_tr_b16 a[244:245], v36 offset:18432\n"
"ds_read_b64_tr_b16 a[246:247], v36 offset:18688\n"
"ds_read_b64_tr_b16 v[28:29], v37 offset:18432\n"
"ds_read_b64_tr_b16 v[30:31], v37 offset:18688\n"
"s_waitcnt lgkmcnt(4)\n"
"s_waitcnt vmcnt(8)\n"
"ds_read_b64_tr_b16 a[24:25], v36 offset:20480\n"
"ds_read_b64_tr_b16 a[26:27], v36 offset:20736\n"
"ds_read_b64_tr_b16 a[52:53], v37 offset:20480\n"
"ds_read_b64_tr_b16 a[54:55], v37 offset:20736\n"
"s_waitcnt lgkmcnt(4)\n"
"s_waitcnt vmcnt(6)\n"
"ds_read_b64_tr_b16 a[80:81], v36 offset:22528\n"
"ds_read_b64_tr_b16 a[82:83], v36 offset:22784\n"
"ds_read_b64_tr_b16 a[108:109], v37 offset:22528\n"
"ds_read_b64_tr_b16 a[110:111], v37 offset:22784\n"
"s_waitcnt lgkmcnt(4)\n"
"s_waitcnt vmcnt(4)\n"
"ds_read_b64_tr_b16 a[136:137], v36 offset:24576\n"
"ds_read_b64_tr_b16 a[138:139], v36 offset:24832\n"
"ds_read_b64_tr_b16 a[164:165], v37 offset:24576\n"
"ds_read_b64_tr_b16 a[166:167], v37 offset:24832\n"
"s_waitcnt lgkmcnt(4)\n"
"s_waitcnt vmcnt(2)\n"
"ds_read_b64_tr_b16 a[192:193], v36 offset:26624\n"
"ds_read_b64_tr_b16 a[194:195], v36 offset:26880\n"
"ds_read_b64_tr_b16 a[220:221], v37 offset:26624\n"
"ds_read_b64_tr_b16 a[222:223], v37 offset:26880\n"
"s_waitcnt lgkmcnt(4)\n"
"s_waitcnt vmcnt(0)\n"
"ds_read_b64_tr_b16 a[248:249], v36 offset:28672\n"
"ds_read_b64_tr_b16 a[250:251], v36 offset:28928\n"
"ds_read_b64_tr_b16 v[32:33], v37 offset:28672\n"
"ds_read_b64_tr_b16 v[34:35], v37 offset:28928\n"
"s_waitcnt lgkmcnt(4)\n"
"s_waitcnt vmcnt(0)\n"
"s_waitcnt lgkmcnt(0)\n"
"s_mov_b32 m0, s57\n"
"s_barrier\n"
"v_mov_b32 v120, 0\n"
"v_mov_b32 v121, 0\n"
"v_mov_b32 v120, 0\n"
"v_mov_b32 v121, 0\n"
"v_mov_b32 v124, 0\n"
"v_mov_b32 v125, 0\n"
"v_mov_b32 v152, 0\n"
"v_mov_b32 v153, 0\n"
"s_mov_b32 s95, 0x80000000\n"
"s_mov_b32 s94, 0x80000000\n"
"s_mov_b32 s93, 0x80000000\n"
"s_mov_b32 s94, 0x80000000\n"
"s_mov_b32 s95, 0x80000000\n"
"s_mov_b32 s94, 0x80000000\n"
"s_mov_b32 s94, 0x80000000\n"
"s_mov_b32 s95, 0x80000000\n"
"s_cmp_eq_u32 s54, 0\n"
"s_cbranch_scc1 .LG_1s\n"
"s_cmp_eq_u32 s54, 1\n"
"s_cbranch_scc1 .LG_2s\n"
"s_cmp_eq_u32 s54, 2\n"
"s_cbranch_scc1 .LG_3s\n"
"s_cmp_eq_u32 s54, 3\n"
"s_cbranch_scc1 .LG_4s\n"
".p2align 6\n"
".LG_top:\n"
"v_mfma_f32_16x16x32_bf16 v[48:51], v[60:63], a[0:3], 0\n"
"v_mfma_f32_16x16x32_bf16 v[48:51], v[64:67], a[4:7], v[48:51]\n"
"v_mfma_f32_16x16x32_bf16 v[48:51], v[68:71], a[8:11], v[48:51]\n"
"buffer_load_dwordx4 v[36:39], v5, s[40:43], s56 offen offset:1024 sc1\n"
"v_mfma_f32_16x16x32_bf16 v[48:51], v[72:75], a[12:15], v[48:51]\n"
"s_waitcnt vmcnt(4)\n"
"v_mfma_f32_16x16x32_bf16 v[48:51], v[76:79], a[16:19], v[48:51]\n"
"s_waitcnt lgkmcnt(3)\n"
"v_add_f32 v108, v108, v44\n"
"v_add_f32 v109, v109, v45\n"
"v_mfma_f32_16x16x32_bf16 v[48:51], v[80:83], a[20:23], v[48:51]\n"
"s_waitcnt vmcnt(3)\n"
"v_add_f32 v110, v110, v46\n"
"v_add_f32 v111, v111, v47\n"
"v_mfma_f32_16x16x32_bf16 v[48:51], v[84:87], a[24:27], v[48:51]\n"
"s_waitcnt vmcnt(2)\n"
"buffer_load_dwordx4 v[40:43], v5, s[40:43], s56 offen offset:2048 sc1\n"
"v_add_f32 v108, v108, v96\n"
"v_mfma_f32_16x16x32_bf16 v[56:59], v[60:63], a[28:31], 0\n"
"buffer_store_dwordx2 v[0:1], v9, s[40:43], s95 offen offset:0 sc1\n"
"v_add_f32 v109, v109, v97\n"
"v_add_f32 v110, v110, v98\n"
"v_mfma_f32_16x16x32_bf16 v[56:59], v[64:67], a[32:35], v[56:59]\n"
"ds_write_addtid_b32 v48 offset:0\n"
"v_add_f32 v111, v111, v99\n"
"v_mfma_f32_16x16x32_bf16 v[56:59], v[68:71], a[36:39], v[56:59]\n"
"ds_write_addtid_b32 v49 offset:4640\n"
"v_add_f32 v108, v108, v100\n"
"v_mfma_f32_16x16x32_bf16 v[56:59], v[72:75], a[40:43], v[56:59]\n"
"ds_write_addtid_b32 v50 offset:9344\n"
"buffer_load_dwordx4 v[44:47], v5, s[40:43], s56 offen offset:3072 sc1\n"
"v_mfma_f32_16x16x32_bf16 v[56:59], v[76:79], a[44:47], v[56:59]\n"
"ds_write_addtid_b32 v51 offset:13984\n"
"v_add_f32 v109, v109, v101\n"
"v_mfma_f32_16x16x32_bf16 v[56:59], v[80:83], a[48:51], v[56:59]\n"
"v_add_f32 v110, v110, v102\n"
"v_add_f32 v111, v111, v103\n"
"v_mfma_f32_16x16x32_bf16 v[56:59], v[84:87], a[52:55], v[56:59]\n"
"s_add_u32 s94, s50, 0xffd00000\n"
"s_and_b32 s95, s94, 0xfbffffff\n"
"buffer_load_dwordx2 v[124:125], v7, s[40:43], s50 offen sc1\n"
"v_mfma_f32_16x16x32_bf16 v[88:91], v[60:63], a[56:59], 0\n"
"buffer_load_dwordx4 v[48:51], v6, s[40:43], s56 offen offset:0 sc1\n"
"v_cvt_pk_bf16_f32 v96, v108, v109\n"
"v_mfma_f32_16x16x32_bf16 v[88:91], v[64:67], a[60:63], v[88:91]\n"
"ds_write_addtid_b32 v56 offset:256\n"
"v_cvt_pk_bf16_f32 v97, v110, v111\n"
"v_mfma_f32_16x16x32_bf16 v[88:91], v[68:71], a[64:67], v[88:91]\n"
"s_waitcnt lgkmcnt(5)\n"
"ds_write_addtid_b32 v57 offset:4896\n"
"v_add_f32 v112, v112, v116\n"
"v_mfma_f32_16x16x32_bf16 v[88:91], v[72:75], a[68:71], v[88:91]\n"
"ds_write_addtid_b32 v58 offset:9600\n"
"v_add_f32 v113, v113, v117\n"
"v_mfma_f32_16x16x32_bf16 v[88:91], v[76:79], a[72:75], v[88:91]\n"
"ds_write_addtid_b32 v59 offset:14240\n"
"buffer_load_dwordx4 v[52:55], v6, s[40:43], s56 offen offset:1024 sc1\n"
"v_mfma_f32_16x16x32_bf16 v[88:91], v[80:83], a[76:79], v[88:91]\n"
"v_add_f32 v114, v114, v118\n"
"v_add_f32 v115, v115, v119\n"
"v_mfma_f32_16x16x32_bf16 v[88:91], v[84:87], a[80:83], v[88:91]\n"
"v_add_f32 v112, v112, v120\n"
"v_add_f32 v113, v113, v121\n"
"v_mfma_f32_16x16x32_bf16 v[100:103], v[60:63], a[84:87], 0\n"
"v_add_f32 v114, v114, v122\n"
"v_add_f32 v115, v115, v123\n"
"v_mfma_f32_16x16x32_bf16 v[100:103], v[64:67], a[88:91], v[100:103]\n"
"ds_write_addtid_b32 v88 offset:512\n"
"buffer_load_dwordx4 v[56:59], v6, s[40:43], s56 offen offset:2048 sc1\n"
"v_mfma_f32_16x16x32_bf16 v[100:103], v[68:71], a[92:95], v[100:103]\n"
"ds_write_addtid_b32 v89 offset:5152\n"
"v_add_f32 v112, v112, v104\n"
"v_mfma_f32_16x16x32_bf16 v[100:103], v[72:75], a[96:99], v[100:103]\n"
"ds_write_addtid_b32 v90 offset:9856\n"
"v_add_f32 v113, v113, v105\n"
"v_mfma_f32_16x16x32_bf16 v[100:103], v[76:79], a[100:103], v[100:103]\n"
"ds_write_addtid_b32 v91 offset:14496\n"
"v_add_f32 v114, v114, v106\n"
"v_mfma_f32_16x16x32_bf16 v[100:103], v[80:83], a[104:107], v[100:103]\n"
"v_add_f32 v115, v115, v107\n"
"v_cvt_pk_bf16_f32 v98, v112, v113\n"
"v_mfma_f32_16x16x32_bf16 v[100:103], v[84:87], a[108:111], v[100:103]\n"
"v_cvt_pk_bf16_f32 v99, v114, v115\n"
"v_mfma_f32_16x16x32_bf16 v[88:91], v[60:63], a[112:115], 0\n"
"buffer_store_dwordx4 v[96:99], v8, s[40:43], s95 offen sc1\n"
"v_mfma_f32_16x16x32_bf16 v[88:91], v[64:67], a[116:119], v[88:91]\n"
"ds_write_addtid_b32 v100 offset:768\n"
"v_mfma_f32_16x16x32_bf16 v[88:91], v[68:71], a[120:123], v[88:91]\n"
"ds_write_addtid_b32 v101 offset:5408\n"
"v_mfma_f32_16x16x32_bf16 v[88:91], v[72:75], a[124:127], v[88:91]\n"
"ds_write_addtid_b32 v102 offset:10112\n"
"v_mfma_f32_16x16x32_bf16 v[88:91], v[76:79], a[128:131], v[88:91]\n"
"ds_write_addtid_b32 v103 offset:14752\n"
"v_mfma_f32_16x16x32_bf16 v[88:91], v[80:83], a[132:135], v[88:91]\n"
"v_mfma_f32_16x16x32_bf16 v[88:91], v[84:87], a[136:139], v[88:91]\n"
"v_mfma_f32_16x16x32_bf16 v[96:99], v[60:63], a[140:143], 0\n"
"v_mfma_f32_16x16x32_bf16 v[96:99], v[64:67], a[144:147], v[96:99]\n"
"ds_write_addtid_b32 v88 offset:1024\n"
"v_mfma_f32_16x16x32_bf16 v[96:99], v[68:71], a[148:151], v[96:99]\n"
"ds_write_addtid_b32 v89 offset:5664\n"
"v_mfma_f32_16x16x32_bf16 v[96:99], v[72:75], a[152:155], v[96:99]\n"
"ds_write_addtid_b32 v90 offset:10368\n"
"v_mfma_f32_16x16x32_bf16 v[96:99], v[76:79], a[156:159], v[96:99]\n"
"ds_write_addtid_b32 v91 offset:15008\n"
"v_mfma_f32_16x16x32_bf16 v[96:99], v[80:83], a[160:163], v[96:99]\n"
"v_mfma_f32_16x16x32_bf16 v[96:99], v[84:87], a[164:167], v[96:99]\n"
"v_mfma_f32_16x16x32_bf16 v[100:103], a[168:171], v[60:63], 0\n"
"v_mfma_f32_16x16x32_bf16 v[100:103], a[172:175], v[64:67], v[100:103]\n"
"ds_write_addtid_b32 v96 offset:1280\n"
"v_mfma_f32_16x16x32_bf16 v[100:103], a[176:179], v[68:71], v[100:103]\n"
"ds_write_addtid_b32 v97 offset:5920\n"
"v_mfma_f32_16x16x32_bf16 v[100:103], a[180:183], v[72:75], v[100:103]\n"
"ds_write_addtid_b32 v98 offset:10624\n"
"v_mfma_f32_16x16x32_bf16 v[100:103], a[184:187], v[76:79], v[100:103]\n"
"ds_write_addtid_b32 v99 offset:15264\n"
"v_mfma_f32_16x16x32_bf16 v[100:103], a[188:191], v[80:83], v[100:103]\n"
"v_mfma_f32_16x16x32_bf16 v[100:103], a[192:195], v[84:87], v[100:103]\n"
"v_mfma_f32_16x16x32_bf16 v[88:91], a[196:199], v[60:63], 0\n"
"v_mfma_f32_16x16x32_bf16 v[88:91], a[200:203], v[64:67], v[88:91]\n"
"v_mfma_f32_16x16x32_bf16 v[88:91], a[204:207], v[68:71], v[88:91]\n"
"v_mfma_f32_16x16x32_bf16 v[88:91], a[208:211], v[72:75], v[88:91]\n"
"v_mfma_f32_16x16x32_bf16 v[88:91], a[212:215], v[76:79], v[88:91]\n"
"v_mfma_f32_16x16x32_bf16 v[88:91], a[216:219], v[80:83], v[88:91]\n"
"v_mfma_f32_16x16x32_bf16 v[88:91], a[220:223], v[84:87], v[88:91]\n"
"v_mfma_f32_16x16x32_bf16 v[96:99], a[224:227], v[60:63], 0\n"
"v_mfma_f32_16x16x32_bf16 v[96:99], a[228:231], v[64:67], v[96:99]\n"
"v_mfma_f32_16x16x32_bf16 v[96:99], a[232:235], v[68:71], v[96:99]\n"
"v_mfma_f32_16x16x32_bf16 v[96:99], a[236:239], v[72:75], v[96:99]\n"
"v_mfma_f32_16x16x32_bf16 v[96:99], a[240:243], v[76:79], v[96:99]\n"
"v_mfma_f32_16x16x32_bf16 v[96:99], a[244:247], v[80:83], v[96:99]\n"
"s_waitcnt vmcnt(4)\n"
"v_cmp_ne_u64 vcc, s[48:49], v[124:125]\n"
"v_mfma_f32_16x16x32_bf16 v[96:99], a[248:251], v[84:87], v[96:99]\n"
"v_mfma_f32_16x16x32_bf16 v[104:107], a[252:255], v[60:63], 0\n"
"s_cbranch_vccnz .LG_3s\n"
".LG_3r:\n"
"v_mfma_f32_16x16x32_bf16 v[104:107], v[12:15], v[64:67], v[104:107]\n"
"s_waitcnt lgkmcnt(0)\n"
"v_mfma_f32_16x16x32_bf16 v[104:107], v[16:19], v[68:71], v[104:107]\n"
"s_barrier\n"
"v_mfma_f32_16x16x32_bf16 v[104:107], v[20:23], v[72:75], v[104:107]\n"
"ds_read_b128 v[108:111], v2 offset:2048\n"
"ds_read_b128 v[112:115], v3 offset:512\n"
"v_mfma_f32_16x16x32_bf16 v[104:107], v[24:27], v[76:79], v[104:107]\n"
"s_add_u32 s50, s50, 0x180000\n"
"s_and_b32 s56, s50, 0xfbffffff\n"
"ds_read_b128 v[72:75], v4 offset:3072\n"
"v_mfma_f32_16x16x32_bf16 v[104:107], v[28:31], v[80:83], v[104:107]\n"
"buffer_load_dwordx4 v[60:63], v5, s[40:43], s56 offen offset:0 sc1\n"
"ds_read_b128 v[116:119], v2 offset:2304\n"
"v_mfma_f32_16x16x32_bf16 v[104:107], v[32:35], v[84:87], v[104:107]\n"
"ds_read_b128 v[120:123], v3 offset:768\n"
"ds_read_b128 v[124:127], v4 offset:3328\n"
"v_mfma_f32_16x16x32_bf16 v[76:79], v[92:95], a[0:3], 0\n"
"ds_read_b128 v[128:131], v2 offset:3584\n"
"ds_read_b128 v[132:135], v3 offset:1536\n"
"v_mfma_f32_16x16x32_bf16 v[76:79], v[36:39], a[4:7], v[76:79]\n"
"ds_read_b128 v[136:139], v4 offset:3584\n"
"ds_read_b128 v[140:143], v2 offset:3840\n"
"v_mfma_f32_16x16x32_bf16 v[76:79], v[40:43], a[8:11], v[76:79]\n"
"buffer_load_dwordx4 v[64:67], v5, s[40:43], s56 offen offset:1024 sc1\n"
"ds_read_b128 v[144:147], v3 offset:1792\n"
"v_mfma_f32_16x16x32_bf16 v[76:79], v[44:47], a[12:15], v[76:79]\n"
"s_waitcnt vmcnt(4)\n"
"ds_read_b128 v[148:151], v4 offset:3840\n"
"v_mfma_f32_16x16x32_bf16 v[76:79], v[48:51], a[16:19], v[76:79]\n"
"s_waitcnt lgkmcnt(9)\n"
"v_add_f32 v108, v108, v112\n"
"v_add_f32 v109, v109, v113\n"
"v_mfma_f32_16x16x32_bf16 v[76:79], v[52:55], a[20:23], v[76:79]\n"
"s_waitcnt vmcnt(3)\n"
"v_add_f32 v110, v110, v114\n"
"v_add_f32 v111, v111, v115\n"
"v_mfma_f32_16x16x32_bf16 v[76:79], v[56:59], a[24:27], v[76:79]\n"
"s_waitcnt vmcnt(2)\n"
"buffer_load_dwordx4 v[68:71], v5, s[40:43], s56 offen offset:2048 sc1\n"
"v_add_f32 v108, v108, v72\n"
"v_mfma_f32_16x16x32_bf16 v[84:87], v[92:95], a[28:31], 0\n"
"buffer_store_dwordx2 v[0:1], v9, s[40:43], s94 offen offset:0 sc1\n"
"v_add_f32 v109, v109, v73\n"
"v_add_f32 v110, v110, v74\n"
"v_mfma_f32_16x16x32_bf16 v[84:87], v[36:39], a[32:35], v[84:87]\n"
"ds_write_addtid_b32 v76 offset:2048\n"
"v_add_f32 v111, v111, v75\n"
"v_mfma_f32_16x16x32_bf16 v[84:87], v[40:43], a[36:39], v[84:87]\n"
"ds_write_addtid_b32 v77 offset:6688\n"
"v_add_f32 v108, v108, v100\n"
"v_mfma_f32_16x16x32_bf16 v[84:87], v[44:47], a[40:43], v[84:87]\n"
"ds_write_addtid_b32 v78 offset:11392\n"
"buffer_load_dwordx4 v[72:75], v5, s[40:43], s56 offen offset:3072 sc1\n"
"v_mfma_f32_16x16x32_bf16 v[84:87], v[48:51], a[44:47], v[84:87]\n"
"ds_write_addtid_b32 v79 offset:16032\n"
"v_add_f32 v109, v109, v101\n"
"v_mfma_f32_16x16x32_bf16 v[84:87], v[52:55], a[48:51], v[84:87]\n"
"v_add_f32 v110, v110, v102\n"
"v_add_f32 v111, v111, v103\n"
"v_mfma_f32_16x16x32_bf16 v[84:87], v[56:59], a[52:55], v[84:87]\n"
"s_add_u32 s95, s50, 0xffd00000\n"
"s_and_b32 s94, s95, 0xfbffffff\n"
"buffer_load_dwordx2 v[152:153], v7, s[40:43], s50 offen sc1\n"
"v_mfma_f32_16x16x32_bf16 v[100:103], v[92:95], a[56:59], 0\n"
"buffer_load_dwordx4 v[76:79], v6, s[40:43], s56 offen offset:0 sc1\n"
"v_cvt_pk_bf16_f32 v112, v108, v109\n"
"v_mfma_f32_16x16x32_bf16 v[100:103], v[36:39], a[60:63], v[100:103]\n"
"ds_write_addtid_b32 v84 offset:2304\n"
"v_cvt_pk_bf16_f32 v113, v110, v111\n"
"v_mfma_f32_16x16x32_bf16 v[100:103], v[40:43], a[64:67], v[100:103]\n"
"s_waitcnt lgkmcnt(11)\n"
"ds_write_addtid_b32 v85 offset:6944\n"
"v_add_f32 v116, v116, v120\n"
"v_mfma_f32_16x16x32_bf16 v[100:103], v[44:47], a[68:71], v[100:103]\n"
"ds_write_addtid_b32 v86 offset:11648\n"
"v_add_f32 v117, v117, v121\n"
"v_mfma_f32_16x16x32_bf16 v[100:103], v[48:51], a[72:75], v[100:103]\n"
"ds_write_addtid_b32 v87 offset:16288\n"
"buffer_load_dwordx4 v[80:83], v6, s[40:43], s56 offen offset:1024 sc1\n"
"v_mfma_f32_16x16x32_bf16 v[100:103], v[52:55], a[76:79], v[100:103]\n"
"v_add_f32 v118, v118, v122\n"
"v_add_f32 v119, v119, v123\n"
"v_mfma_f32_16x16x32_bf16 v[100:103], v[56:59], a[80:83], v[100:103]\n"
"v_add_f32 v116, v116, v124\n"
"v_add_f32 v117, v117, v125\n"
"v_mfma_f32_16x16x32_bf16 v[108:111], v[92:95], a[84:87], 0\n"
"v_add_f32 v118, v118, v126\n"
"v_add_f32 v119, v119, v127\n"
"v_mfma_f32_16x16x32_bf16 v[108:111], v[36:39], a[88:91], v[108:111]\n"
"ds_write_addtid_b32 v100 offset:2560\n"
"buffer_load_dwordx4 v[84:87], v6, s[40:43], s56 offen offset:2048 sc1\n"
"v_mfma_f32_16x16x32_bf16 v[108:111], v[40:43], a[92:95], v[108:111]\n"
"ds_write_addtid_b32 v101 offset:7200\n"
"v_add_f32 v116, v116, v88\n"
"v_mfma_f32_16x16x32_bf16 v[108:111], v[44:47], a[96:99], v[108:111]\n"
"ds_write_addtid_b32 v102 offset:11904\n"
"v_add_f32 v117, v117, v89\n"
"v_mfma_f32_16x16x32_bf16 v[108:111], v[48:51], a[100:103], v[108:111]\n"
"ds_write_addtid_b32 v103 offset:16544\n"
"v_add_f32 v118, v118, v90\n"
"v_mfma_f32_16x16x32_bf16 v[108:111], v[52:55], a[104:107], v[108:111]\n"
"v_add_f32 v119, v119, v91\n"
"v_cvt_pk_bf16_f32 v114, v116, v117\n"
"v_mfma_f32_16x16x32_bf16 v[108:111], v[56:59], a[108:111], v[108:111]\n"
"v_cvt_pk_bf16_f32 v115, v118, v119\n"
"v_mfma_f32_16x16x32_bf16 v[88:91], v[92:95], a[112:115], 0\n"
"buffer_store_dwordx4 v[112:115], v8, s[40:43], s94 offen sc1\n"
"v_mfma_f32_16x16x32_bf16 v[88:91], v[36:39], a[116:119], v[88:91]\n"
"s_waitcnt lgkmcnt(15)\n"
"ds_write_addtid_b32 v108 offset:2816\n"
"v_add_f32 v128, v128, v132\n"
"v_mfma_f32_16x16x32_bf16 v[88:91], v[40:43], a[120:123], v[88:91]\n"
"ds_write_addtid_b32 v109 offset:7456\n"
"v_add_f32 v129, v129, v133\n"
"v_mfma_f32_16x16x32_bf16 v[88:91], v[44:47], a[124:127], v[88:91]\n"
"ds_write_addtid_b32 v110 offset:12160\n"
"v_add_f32 v130, v130, v134\n"
"v_mfma_f32_16x16x32_bf16 v[88:91], v[48:51], a[128:131], v[88:91]\n"
"ds_write_addtid_b32 v111 offset:16800\n"
"v_add_f32 v131, v131, v135\n"
"v_mfma_f32_16x16x32_bf16 v[88:91], v[52:55], a[132:135], v[88:91]\n"
"v_add_f32 v128, v128, v136\n"
"v_add_f32 v129, v129, v137\n"
"v_mfma_f32_16x16x32_bf16 v[88:91], v[56:59], a[136:139], v[88:91]\n"
"v_add_f32 v130, v130, v138\n"
"v_add_f32 v131, v131, v139\n"
"v_mfma_f32_16x16x32_bf16 v[100:103], v[92:95], a[140:143], 0\n"
"v_add_f32 v128, v128, v96\n"
"v_add_f32 v129, v129, v97\n"
"v_mfma_f32_16x16x32_bf16 v[100:103], v[36:39], a[144:147], v[100:103]\n"
"ds_write_addtid_b32 v88 offset:3072\n"
"v_add_f32 v130, v130, v98\n"
"v_mfma_f32_16x16x32_bf16 v[100:103], v[40:43], a[148:151], v[100:103]\n"
"ds_write_addtid_b32 v89 offset:7712\n"
"v_add_f32 v131, v131, v99\n"
"v_mfma_f32_16x16x32_bf16 v[100:103], v[44:47], a[152:155], v[100:103]\n"
"ds_write_addtid_b32 v90 offset:12416\n"
"v_cvt_pk_bf16_f32 v108, v128, v129\n"
"v_mfma_f32_16x16x32_bf16 v[100:103], v[48:51], a[156:159], v[100:103]\n"
"ds_write_addtid_b32 v91 offset:17056\n"
"v_cvt_pk_bf16_f32 v109, v130, v131\n"
"v_mfma_f32_16x16x32_bf16 v[100:103], v[52:55], a[160:163], v[100:103]\n"
"s_waitcnt lgkmcnt(15)\n"
"v_add_f32 v140, v140, v144\n"
"v_add_f32 v141, v141, v145\n"
"v_mfma_f32_16x16x32_bf16 v[100:103], v[56:59], a[164:167], v[100:103]\n"
"v_add_f32 v142, v142, v146\n"
"v_add_f32 v143, v143, v147\n"
"v_mfma_f32_16x16x32_bf16 v[88:91], v[92:95], a[224:227], 0\n"
"v_add_f32 v140, v140, v148\n"
"v_add_f32 v141, v141, v149\n"
"v_mfma_f32_16x16x32_bf16 v[88:91], v[36:39], a[228:231], v[88:91]\n"
"ds_write_addtid_b32 v100 offset:3328\n"
"v_add_f32 v142, v142, v150\n"
"v_mfma_f32_16x16x32_bf16 v[88:91], v[40:43], a[232:235], v[88:91]\n"
"ds_write_addtid_b32 v101 offset:7968\n"
"v_add_f32 v143, v143, v151\n"
"v_mfma_f32_16x16x32_bf16 v[88:91], v[44:47], a[236:239], v[88:91]\n"
"ds_write_addtid_b32 v102 offset:12672\n"
"v_add_f32 v140, v140, v104\n"
"v_mfma_f32_16x16x32_bf16 v[88:91], v[48:51], a[240:243], v[88:91]\n"
"ds_write_addtid_b32 v103 offset:17312\n"
"v_add_f32 v141, v141, v105\n"
"v_mfma_f32_16x16x32_bf16 v[88:91], v[52:55], a[244:247], v[88:91]\n"
"v_add_f32 v142, v142, v106\n"
"v_add_f32 v143, v143, v107\n"
"v_mfma_f32_16x16x32_bf16 v[88:91], v[56:59], a[248:251], v[88:91]\n"
"v_cvt_pk_bf16_f32 v110, v140, v141\n"
"v_cvt_pk_bf16_f32 v111, v142, v143\n"
"v_mfma_f32_16x16x32_bf16 v[96:99], v[92:95], a[252:255], 0\n"
"buffer_store_dwordx4 v[108:111], v10, s[40:43], s94 offen sc1\n"
"v_mfma_f32_16x16x32_bf16 v[96:99], v[36:39], v[12:15], v[96:99]\n"
"ds_write_addtid_b32 v88 offset:3584\n"
"v_mfma_f32_16x16x32_bf16 v[96:99], v[40:43], v[16:19], v[96:99]\n"
"ds_write_addtid_b32 v89 offset:8224\n"
"v_mfma_f32_16x16x32_bf16 v[96:99], v[44:47], v[20:23], v[96:99]\n"
"ds_write_addtid_b32 v90 offset:12928\n"
"v_mfma_f32_16x16x32_bf16 v[96:99], v[48:51], v[24:27], v[96:99]\n"
"ds_write_addtid_b32 v91 offset:17568\n"
"v_mfma_f32_16x16x32_bf16 v[96:99], v[52:55], v[28:31], v[96:99]\n"
"v_mfma_f32_16x16x32_bf16 v[96:99], v[56:59], v[32:35], v[96:99]\n"
"v_mfma_f32_16x16x32_bf16 v[100:103], a[168:171], v[92:95], 0\n"
"v_mfma_f32_16x16x32_bf16 v[100:103], a[172:175], v[36:39], v[100:103]\n"
"ds_write_addtid_b32 v96 offset:3840\n"
"v_mfma_f32_16x16x32_bf16 v[100:103], a[176:179], v[40:43], v[100:103]\n"
"ds_write_addtid_b32 v97 offset:8480\n"
"v_mfma_f32_16x16x32_bf16 v[100:103], a[180:183], v[44:47], v[100:103]\n"
"ds_write_addtid_b32 v98 offset:13184\n"
"v_mfma_f32_16x16x32_bf16 v[100:103], a[184:187], v[48:51], v[100:103]\n"
"ds_write_addtid_b32 v99 offset:17824\n"
"v_mfma_f32_16x16x32_bf16 v[100:103], a[188:191], v[52:55], v[100:103]\n"
"s_waitcnt vmcnt(5)\n"
"v_cmp_ne_u64 vcc, s[48:49], v[152:153]\n"
"v_mfma_f32_16x16x32_bf16 v[100:103], a[192:195], v[56:59], v[100:103]\n"
"v_mfma_f32_16x16x32_bf16 v[96:99], a[196:199], v[92:95], 0\n"
"s_cbranch_vccnz .LG_4s\n"
".LG_4r:\n"
"v_mfma_f32_16x16x32_bf16 v[96:99], a[200:203], v[36:39], v[96:99]\n"
"s_waitcnt lgkmcnt(0)\n"
"v_mfma_f32_16x16x32_bf16 v[96:99], a[204:207], v[40:43], v[96:99]\n"
"s_barrier\n"
"v_mfma_f32_16x16x32_bf16 v[96:99], a[208:211], v[44:47], v[96:99]\n"
"ds_read_b128 v[88:91], v2 offset:0\n"
"ds_read_b128 v[92:95], v3 offset:2560\n"
"v_mfma_f32_16x16x32_bf16 v[96:99], a[212:215], v[48:51], v[96:99]\n"
"s_add_u32 s50, s50, 0x180000\n"
"s_and_b32 s56, s50, 0xfbffffff\n"
"ds_read_b128 v[104:107], v4 offset:1024\n"
"v_mfma_f32_16x16x32_bf16 v[96:99], a[216:219], v[52:55], v[96:99]\n"
"buffer_load_dwordx4 v[36:39], v5, s[40:43], s56 offen offset:0 sc1\n"
"ds_read_b128 v[108:111], v2 offset:256\n"
"v_mfma_f32_16x16x32_bf16 v[96:99], a[220:223], v[56:59], v[96:99]\n"
"ds_read_b128 v[112:115], v3 offset:2816\n"
"ds_read_b128 v[116:119], v4 offset:1280\n"
"v_mfma_f32_16x16x32_bf16 v[52:55], v[60:63], a[0:3], 0\n"
"v_mfma_f32_16x16x32_bf16 v[52:55], v[64:67], a[4:7], v[52:55]\n"
"v_mfma_f32_16x16x32_bf16 v[52:55], v[68:71], a[8:11], v[52:55]\n"
"buffer_load_dwordx4 v[40:43], v5, s[40:43], s56 offen offset:1024 sc1\n"
"v_mfma_f32_16x16x32_bf16 v[52:55], v[72:75], a[12:15], v[52:55]\n"
"s_waitcnt vmcnt(5)\n"
"v_mfma_f32_16x16x32_bf16 v[52:55], v[76:79], a[16:19], v[52:55]\n"
"s_waitcnt lgkmcnt(3)\n"
"v_add_f32 v88, v88, v92\n"
"v_add_f32 v89, v89, v93\n"
"v_mfma_f32_16x16x32_bf16 v[52:55], v[80:83], a[20:23], v[52:55]\n"
"s_waitcnt vmcnt(4)\n"
"v_add_f32 v90, v90, v94\n"
"v_add_f32 v91, v91, v95\n"
"v_mfma_f32_16x16x32_bf16 v[52:55], v[84:87], a[24:27], v[52:55]\n"
"buffer_load_dwordx4 v[44:47], v5, s[40:43], s56 offen offset:2048 sc1\n"
"v_add_f32 v88, v88, v104\n"
"v_mfma_f32_16x16x32_bf16 v[92:95], v[60:63], a[28:31], 0\n"
"v_add_f32 v89, v89, v105\n"
"v_add_f32 v90, v90, v106\n"
"v_mfma_f32_16x16x32_bf16 v[92:95], v[64:67], a[32:35], v[92:95]\n"
"ds_write_addtid_b32 v52 offset:0\n"
"v_add_f32 v91, v91, v107\n"
"v_mfma_f32_16x16x32_bf16 v[92:95], v[68:71], a[36:39], v[92:95]\n"
"ds_write_addtid_b32 v53 offset:4640\n"
"v_add_f32 v88, v88, v100\n"
"v_mfma_f32_16x16x32_bf16 v[92:95], v[72:75], a[40:43], v[92:95]\n"
"ds_write_addtid_b32 v54 offset:9344\n"
"buffer_load_dwordx4 v[48:51], v5, s[40:43], s56 offen offset:3072 sc1\n"
"v_mfma_f32_16x16x32_bf16 v[92:95], v[76:79], a[44:47], v[92:95]\n"
"ds_write_addtid_b32 v55 offset:13984\n"
"v_add_f32 v89, v89, v101\n"
"v_mfma_f32_16x16x32_bf16 v[92:95], v[80:83], a[48:51], v[92:95]\n"
"v_add_f32 v90, v90, v102\n"
"v_add_f32 v91, v91, v103\n"
"v_mfma_f32_16x16x32_bf16 v[92:95], v[84:87], a[52:55], v[92:95]\n"
"s_add_u32 s93, s50, 0xffd00000\n"
"s_and_b32 s94, s93, 0xfbffffff\n"
"buffer_load_dwordx2 v[120:121], v7, s[40:43], s50 offen sc1\n"
"v_mfma_f32_16x16x32_bf16 v[100:103], v[60:63], a[56:59], 0\n"
"buffer_load_dwordx4 v[52:55], v6, s[40:43], s56 offen offset:0 sc1\n"
"v_cvt_pk_bf16_f32 v104, v88, v89\n"
"v_mfma_f32_16x16x32_bf16 v[100:103], v[64:67], a[60:63], v[100:103]\n"
"ds_write_addtid_b32 v92 offset:256\n"
"v_cvt_pk_bf16_f32 v105, v90, v91\n"
"v_mfma_f32_16x16x32_bf16 v[100:103], v[68:71], a[64:67], v[100:103]\n"
"s_waitcnt lgkmcnt(5)\n"
"ds_write_addtid_b32 v93 offset:4896\n"
"v_add_f32 v108, v108, v112\n"
"v_mfma_f32_16x16x32_bf16 v[100:103], v[72:75], a[68:71], v[100:103]\n"
"ds_write_addtid_b32 v94 offset:9600\n"
"v_add_f32 v109, v109, v113\n"
"v_mfma_f32_16x16x32_bf16 v[100:103], v[76:79], a[72:75], v[100:103]\n"
"ds_write_addtid_b32 v95 offset:14240\n"
"buffer_load_dwordx4 v[56:59], v6, s[40:43], s56 offen offset:1024 sc1\n"
"v_mfma_f32_16x16x32_bf16 v[100:103], v[80:83], a[76:79], v[100:103]\n"
"v_add_f32 v110, v110, v114\n"
"v_add_f32 v111, v111, v115\n"
"v_mfma_f32_16x16x32_bf16 v[100:103], v[84:87], a[80:83], v[100:103]\n"
"v_add_f32 v108, v108, v116\n"
"v_add_f32 v109, v109, v117\n"
"v_mfma_f32_16x16x32_bf16 v[92:95], v[60:63], a[84:87], 0\n"
"v_add_f32 v110, v110, v118\n"
"v_add_f32 v111, v111, v119\n"
"v_mfma_f32_16x16x32_bf16 v[92:95], v[64:67], a[88:91], v[92:95]\n"
"ds_write_addtid_b32 v100 offset:512\n"
"buffer_load_dwordx4 v[88:91], v6, s[40:43], s56 offen offset:2048 sc1\n"
"v_mfma_f32_16x16x32_bf16 v[92:95], v[68:71], a[92:95], v[92:95]\n"
"ds_write_addtid_b32 v101 offset:5152\n"
"v_add_f32 v108, v108, v96\n"
"v_mfma_f32_16x16x32_bf16 v[92:95], v[72:75], a[96:99], v[92:95]\n"
"ds_write_addtid_b32 v102 offset:9856\n"
"v_add_f32 v109, v109, v97\n"
"v_mfma_f32_16x16x32_bf16 v[92:95], v[76:79], a[100:103], v[92:95]\n"
"ds_write_addtid_b32 v103 offset:14496\n"
"v_add_f32 v110, v110, v98\n"
"v_mfma_f32_16x16x32_bf16 v[92:95], v[80:83], a[104:107], v[92:95]\n"
"v_add_f32 v111, v111, v99\n"
"v_cvt_pk_bf16_f32 v106, v108, v109\n"
"v_mfma_f32_16x16x32_bf16 v[92:95], v[84:87], a[108:111], v[92:95]\n"
"s_waitcnt vmcnt(8)\n"
"buffer_store_dwordx2 v[0:1], v11, s[40:43], s95 offen offset:0 sc1\n"
"v_cvt_pk_bf16_f32 v107, v110, v111\n"
"v_mfma_f32_16x16x32_bf16 v[96:99], v[60:63], a[112:115], 0\n"
"buffer_store_dwordx4 v[104:107], v8, s[40:43], s94 offen sc1\n"
"v_mfma_f32_16x16x32_bf16 v[96:99], v[64:67], a[116:119], v[96:99]\n"
"ds_write_addtid_b32 v92 offset:768\n"
"v_mfma_f32_16x16x32_bf16 v[96:99], v[68:71], a[120:123], v[96:99]\n"
"ds_write_addtid_b32 v93 offset:5408\n"
"v_mfma_f32_16x16x32_bf16 v[96:99], v[72:75], a[124:127], v[96:99]\n"
"ds_write_addtid_b32 v94 offset:10112\n"
"v_mfma_f32_16x16x32_bf16 v[96:99], v[76:79], a[128:131], v[96:99]\n"
"ds_write_addtid_b32 v95 offset:14752\n"
"v_mfma_f32_16x16x32_bf16 v[96:99], v[80:83], a[132:135], v[96:99]\n"
"v_mfma_f32_16x16x32_bf16 v[96:99], v[84:87], a[136:139], v[96:99]\n"
"v_mfma_f32_16x16x32_bf16 v[100:103], v[60:63], a[140:143], 0\n"
"v_mfma_f32_16x16x32_bf16 v[100:103], v[64:67], a[144:147], v[100:103]\n"
"ds_write_addtid_b32 v96 offset:1024\n"
"v_mfma_f32_16x16x32_bf16 v[100:103], v[68:71], a[148:151], v[100:103]\n"
"ds_write_addtid_b32 v97 offset:5664\n"
"v_mfma_f32_16x16x32_bf16 v[100:103], v[72:75], a[152:155], v[100:103]\n"
"ds_write_addtid_b32 v98 offset:10368\n"
"v_mfma_f32_16x16x32_bf16 v[100:103], v[76:79], a[156:159], v[100:103]\n"
"ds_write_addtid_b32 v99 offset:15008\n"
"v_mfma_f32_16x16x32_bf16 v[100:103], v[80:83], a[160:163], v[100:103]\n"
"v_mfma_f32_16x16x32_bf16 v[100:103], v[84:87], a[164:167], v[100:103]\n"
"v_mfma_f32_16x16x32_bf16 v[92:95], v[60:63], a[224:227], 0\n"
"v_mfma_f32_16x16x32_bf16 v[92:95], v[64:67], a[228:231], v[92:95]\n"
"ds_write_addtid_b32 v100 offset:1280\n"
"v_mfma_f32_16x16x32_bf16 v[92:95], v[68:71], a[232:235], v[92:95]\n"
"ds_write_addtid_b32 v101 offset:5920\n"
"v_mfma_f32_16x16x32_bf16 v[92:95], v[72:75], a[236:239], v[92:95]\n"
"ds_write_addtid_b32 v102 offset:10624\n"
"v_mfma_f32_16x16x32_bf16 v[92:95], v[76:79], a[240:243], v[92:95]\n"
"ds_write_addtid_b32 v103 offset:15264\n"
"v_mfma_f32_16x16x32_bf16 v[92:95], v[80:83], a[244:247], v[92:95]\n"
"v_mfma_f32_16x16x32_bf16 v[92:95], v[84:87], a[248:251], v[92:95]\n"
"v_mfma_f32_16x16x32_bf16 v[96:99], v[60:63], a[252:255], 0\n"
"v_mfma_f32_16x16x32_bf16 v[96:99], v[64:67], v[12:15], v[96:99]\n"
"ds_write_addtid_b32 v92 offset:1536\n"
"v_mfma_f32_16x16x32_bf16 v[96:99], v[68:71], v[16:19], v[96:99]\n"
"ds_write_addtid_b32 v93 offset:6176\n"
"v_mfma_f32_16x16x32_bf16 v[96:99], v[72:75], v[20:23], v[96:99]\n"
"ds_write_addtid_b32 v94 offset:10880\n"
"v_mfma_f32_16x16x32_bf16 v[96:99], v[76:79], v[24:27], v[96:99]\n"
"ds_write_addtid_b32 v95 offset:15520\n"
"v_mfma_f32_16x16x32_bf16 v[96:99], v[80:83], v[28:31], v[96:99]\n"
"v_mfma_f32_16x16x32_bf16 v[96:99], v[84:87], v[32:35], v[96:99]\n"
"v_mfma_f32_16x16x32_bf16 v[100:103], a[168:171], v[60:63], 0\n"
"v_mfma_f32_16x16x32_bf16 v[100:103], a[172:175], v[64:67], v[100:103]\n"
"ds_write_addtid_b32 v96 offset:1792\n"
"v_mfma_f32_16x16x32_bf16 v[100:103], a[176:179], v[68:71], v[100:103]\n"
"ds_write_addtid_b32 v97 offset:6432\n"
"v_mfma_f32_16x16x32_bf16 v[100:103], a[180:183], v[72:75], v[100:103]\n"
"ds_write_addtid_b32 v98 offset:11136\n"
"v_mfma_f32_16x16x32_bf16 v[100:103], a[184:187], v[76:79], v[100:103]\n"
"ds_write_addtid_b32 v99 offset:15776\n"
"v_mfma_f32_16x16x32_bf16 v[100:103], a[188:191], v[80:83], v[100:103]\n"
"s_waitcnt vmcnt(5)\n"
"v_cmp_ne_u64 vcc, s[48:49], v[120:121]\n"
"v_mfma_f32_16x16x32_bf16 v[100:103], a[192:195], v[84:87], v[100:103]\n"
"v_mfma_f32_16x16x32_bf16 v[92:95], a[196:199], v[60:63], 0\n"
"s_cbranch_vccnz .LG_1s\n"
".LG_1r:\n"
"v_mfma_f32_16x16x32_bf16 v[92:95], a[200:203], v[64:67], v[92:95]\n"
"s_waitcnt lgkmcnt(0)\n"
"v_mfma_f32_16x16x32_bf16 v[92:95], a[204:207], v[68:71], v[92:95]\n"
"s_barrier\n"
"v_mfma_f32_16x16x32_bf16 v[92:95], a[208:211], v[72:75], v[92:95]\n"
"ds_read_b128 v[96:99], v2 offset:2048\n"
"ds_read_b128 v[104:107], v3 offset:512\n"
"v_mfma_f32_16x16x32_bf16 v[92:95], a[212:215], v[76:79], v[92:95]\n"
"s_add_u32 s50, s50, 0x180000\n"
"s_and_b32 s56, s50, 0xfbffffff\n"
"ds_read_b128 v[72:75], v4 offset:3072\n"
"v_mfma_f32_16x16x32_bf16 v[92:95], a[216:219], v[80:83], v[92:95]\n"
"buffer_load_dwordx4 v[60:63], v5, s[40:43], s56 offen offset:0 sc1\n"
"ds_read_b128 v[108:111], v2 offset:2304\n"
"v_mfma_f32_16x16x32_bf16 v[92:95], a[220:223], v[84:87], v[92:95]\n"
"ds_read_b128 v[112:115], v3 offset:768\n"
"ds_read_b128 v[116:119], v4 offset:3328\n"
"v_mfma_f32_16x16x32_bf16 v[76:79], v[36:39], a[0:3], 0\n"
"v_mfma_f32_16x16x32_bf16 v[76:79], v[40:43], a[4:7], v[76:79]\n"
"v_mfma_f32_16x16x32_bf16 v[76:79], v[44:47], a[8:11], v[76:79]\n"
"buffer_load_dwordx4 v[64:67], v5, s[40:43], s56 offen offset:1024 sc1\n"
"v_mfma_f32_16x16x32_bf16 v[76:79], v[48:51], a[12:15], v[76:79]\n"
"s_waitcnt vmcnt(5)\n"
"v_mfma_f32_16x16x32_bf16 v[76:79], v[52:55], a[16:19], v[76:79]\n"
"s_waitcnt lgkmcnt(3)\n"
"v_add_f32 v96, v96, v104\n"
"v_add_f32 v97, v97, v105\n"
"v_mfma_f32_16x16x32_bf16 v[76:79], v[56:59], a[20:23], v[76:79]\n"
"s_waitcnt vmcnt(4)\n"
"v_add_f32 v98, v98, v106\n"
"v_add_f32 v99, v99, v107\n"
"v_mfma_f32_16x16x32_bf16 v[76:79], v[88:91], a[24:27], v[76:79]\n"
"s_waitcnt vmcnt(2)\n"
"buffer_load_dwordx4 v[68:71], v5, s[40:43], s56 offen offset:2048 sc1\n"
"v_add_f32 v96, v96, v72\n"
"v_mfma_f32_16x16x32_bf16 v[84:87], v[36:39], a[28:31], 0\n"
"buffer_store_dwordx2 v[0:1], v9, s[40:43], s93 offen offset:0 sc1\n"
"v_add_f32 v97, v97, v73\n"
"v_add_f32 v98, v98, v74\n"
"v_mfma_f32_16x16x32_bf16 v[84:87], v[40:43], a[32:35], v[84:87]\n"
"ds_write_addtid_b32 v76 offset:2048\n"
"v_add_f32 v99, v99, v75\n"
"v_mfma_f32_16x16x32_bf16 v[84:87], v[44:47], a[36:39], v[84:87]\n"
"ds_write_addtid_b32 v77 offset:6688\n"
"v_add_f32 v96, v96, v100\n"
"v_mfma_f32_16x16x32_bf16 v[84:87], v[48:51], a[40:43], v[84:87]\n"
"ds_write_addtid_b32 v78 offset:11392\n"
"buffer_load_dwordx4 v[72:75], v5, s[40:43], s56 offen offset:3072 sc1\n"
"v_mfma_f32_16x16x32_bf16 v[84:87], v[52:55], a[44:47], v[84:87]\n"
"ds_write_addtid_b32 v79 offset:16032\n"
"v_add_f32 v97, v97, v101\n"
"v_mfma_f32_16x16x32_bf16 v[84:87], v[56:59], a[48:51], v[84:87]\n"
"v_add_f32 v98, v98, v102\n"
"v_add_f32 v99, v99, v103\n"
"v_mfma_f32_16x16x32_bf16 v[84:87], v[88:91], a[52:55], v[84:87]\n"
"s_add_u32 s95, s50, 0xffd00000\n"
"s_and_b32 s94, s95, 0xfbffffff\n"
"buffer_load_dwordx2 v[120:121], v7, s[40:43], s50 offen sc1\n"
"v_mfma_f32_16x16x32_bf16 v[100:103], v[36:39], a[56:59], 0\n"
"buffer_load_dwordx4 v[76:79], v6, s[40:43], s56 offen offset:0 sc1\n"
"v_cvt_pk_bf16_f32 v104, v96, v97\n"
"v_mfma_f32_16x16x32_bf16 v[100:103], v[40:43], a[60:63], v[100:103]\n"
"ds_write_addtid_b32 v84 offset:2304\n"
"v_cvt_pk_bf16_f32 v105, v98, v99\n"
"v_mfma_f32_16x16x32_bf16 v[100:103], v[44:47], a[64:67], v[100:103]\n"
"s_waitcnt lgkmcnt(5)\n"
"ds_write_addtid_b32 v85 offset:6944\n"
"v_add_f32 v108, v108, v112\n"
"v_mfma_f32_16x16x32_bf16 v[100:103], v[48:51], a[68:71], v[100:103]\n"
"ds_write_addtid_b32 v86 offset:11648\n"
"v_add_f32 v109, v109, v113\n"
"v_mfma_f32_16x16x32_bf16 v[100:103], v[52:55], a[72:75], v[100:103]\n"
"ds_write_addtid_b32 v87 offset:16288\n"
"buffer_load_dwordx4 v[80:83], v6, s[40:43], s56 offen offset:1024 sc1\n"
"v_mfma_f32_16x16x32_bf16 v[100:103], v[56:59], a[76:79], v[100:103]\n"
"v_add_f32 v110, v110, v114\n"
"v_add_f32 v111, v111, v115\n"
"v_mfma_f32_16x16x32_bf16 v[100:103], v[88:91], a[80:83], v[100:103]\n"
"v_add_f32 v108, v108, v116\n"
"v_add_f32 v109, v109, v117\n"
"v_mfma_f32_16x16x32_bf16 v[96:99], v[36:39], a[84:87], 0\n"
"v_add_f32 v110, v110, v118\n"
"v_add_f32 v111, v111, v119\n"
"v_mfma_f32_16x16x32_bf16 v[96:99], v[40:43], a[88:91], v[96:99]\n"
"ds_write_addtid_b32 v100 offset:2560\n"
"buffer_load_dwordx4 v[84:87], v6, s[40:43], s56 offen offset:2048 sc1\n"
"v_mfma_f32_16x16x32_bf16 v[96:99], v[44:47], a[92:95], v[96:99]\n"
"ds_write_addtid_b32 v101 offset:7200\n"
"v_add_f32 v108, v108, v92\n"
"v_mfma_f32_16x16x32_bf16 v[96:99], v[48:51], a[96:99], v[96:99]\n"
"ds_write_addtid_b32 v102 offset:11904\n"
"v_add_f32 v109, v109, v93\n"
"v_mfma_f32_16x16x32_bf16 v[96:99], v[52:55], a[100:103], v[96:99]\n"
"ds_write_addtid_b32 v103 offset:16544\n"
"v_add_f32 v110, v110, v94\n"
"v_mfma_f32_16x16x32_bf16 v[96:99], v[56:59], a[104:107], v[96:99]\n"
"v_add_f32 v111, v111, v95\n"
"v_cvt_pk_bf16_f32 v106, v108, v109\n"
"v_mfma_f32_16x16x32_bf16 v[96:99], v[88:91], a[108:111], v[96:99]\n"
"v_cvt_pk_bf16_f32 v107, v110, v111\n"
"v_mfma_f32_16x16x32_bf16 v[92:95], v[36:39], a[112:115], 0\n"
"buffer_store_dwordx4 v[104:107], v8, s[40:43], s94 offen sc1\n"
"v_mfma_f32_16x16x32_bf16 v[92:95], v[40:43], a[116:119], v[92:95]\n"
"ds_write_addtid_b32 v96 offset:2816\n"
"v_mfma_f32_16x16x32_bf16 v[92:95], v[44:47], a[120:123], v[92:95]\n"
"ds_write_addtid_b32 v97 offset:7456\n"
"v_mfma_f32_16x16x32_bf16 v[92:95], v[48:51], a[124:127], v[92:95]\n"
"ds_write_addtid_b32 v98 offset:12160\n"
"v_mfma_f32_16x16x32_bf16 v[92:95], v[52:55], a[128:131], v[92:95]\n"
"ds_write_addtid_b32 v99 offset:16800\n"
"v_mfma_f32_16x16x32_bf16 v[92:95], v[56:59], a[132:135], v[92:95]\n"
"v_mfma_f32_16x16x32_bf16 v[92:95], v[88:91], a[136:139], v[92:95]\n"
"v_mfma_f32_16x16x32_bf16 v[100:103], v[36:39], a[140:143], 0\n"
"v_mfma_f32_16x16x32_bf16 v[100:103], v[40:43], a[144:147], v[100:103]\n"
"ds_write_addtid_b32 v92 offset:3072\n"
"v_mfma_f32_16x16x32_bf16 v[100:103], v[44:47], a[148:151], v[100:103]\n"
"ds_write_addtid_b32 v93 offset:7712\n"
"v_mfma_f32_16x16x32_bf16 v[100:103], v[48:51], a[152:155], v[100:103]\n"
"ds_write_addtid_b32 v94 offset:12416\n"
"v_mfma_f32_16x16x32_bf16 v[100:103], v[52:55], a[156:159], v[100:103]\n"
"ds_write_addtid_b32 v95 offset:17056\n"
"v_mfma_f32_16x16x32_bf16 v[100:103], v[56:59], a[160:163], v[100:103]\n"
"v_mfma_f32_16x16x32_bf16 v[100:103], v[88:91], a[164:167], v[100:103]\n"
"v_mfma_f32_16x16x32_bf16 v[96:99], v[36:39], a[224:227], 0\n"
"v_mfma_f32_16x16x32_bf16 v[96:99], v[40:43], a[228:231], v[96:99]\n"
"ds_write_addtid_b32 v100 offset:3328\n"
"v_mfma_f32_16x16x32_bf16 v[96:99], v[44:47], a[232:235], v[96:99]\n"
"ds_write_addtid_b32 v101 offset:7968\n"
"v_mfma_f32_16x16x32_bf16 v[96:99], v[48:51], a[236:239], v[96:99]\n"
"ds_write_addtid_b32 v102 offset:12672\n"
"v_mfma_f32_16x16x32_bf16 v[96:99], v[52:55], a[240:243], v[96:99]\n"
"ds_write_addtid_b32 v103 offset:17312\n"
"v_mfma_f32_16x16x32_bf16 v[96:99], v[56:59], a[244:247], v[96:99]\n"
"v_mfma_f32_16x16x32_bf16 v[96:99], v[88:91], a[248:251], v[96:99]\n"
"v_mfma_f32_16x16x32_bf16 v[92:95], v[36:39], a[252:255], 0\n"
"v_mfma_f32_16x16x32_bf16 v[92:95], v[40:43], v[12:15], v[92:95]\n"
"ds_write_addtid_b32 v96 offset:3584\n"
"v_mfma_f32_16x16x32_bf16 v[92:95], v[44:47], v[16:19], v[92:95]\n"
"ds_write_addtid_b32 v97 offset:8224\n"
"v_mfma_f32_16x16x32_bf16 v[92:95], v[48:51], v[20:23], v[92:95]\n"
"ds_write_addtid_b32 v98 offset:12928\n"
"v_mfma_f32_16x16x32_bf16 v[92:95], v[52:55], v[24:27], v[92:95]\n"
"ds_write_addtid_b32 v99 offset:17568\n"
"v_mfma_f32_16x16x32_bf16 v[92:95], v[56:59], v[28:31], v[92:95]\n"
"v_mfma_f32_16x16x32_bf16 v[92:95], v[88:91], v[32:35], v[92:95]\n"
"v_mfma_f32_16x16x32_bf16 v[100:103], a[168:171], v[36:39], 0\n"
"v_mfma_f32_16x16x32_bf16 v[100:103], a[172:175], v[40:43], v[100:103]\n"
"ds_write_addtid_b32 v92 offset:3840\n"
"v_mfma_f32_16x16x32_bf16 v[100:103], a[176:179], v[44:47], v[100:103]\n"
"ds_write_addtid_b32 v93 offset:8480\n"
"v_mfma_f32_16x16x32_bf16 v[100:103], a[180:183], v[48:51], v[100:103]\n"
"ds_write_addtid_b32 v94 offset:13184\n"
"v_mfma_f32_16x16x32_bf16 v[100:103], a[184:187], v[52:55], v[100:103]\n"
"ds_write_addtid_b32 v95 offset:17824\n"
"v_mfma_f32_16x16x32_bf16 v[100:103], a[188:191], v[56:59], v[100:103]\n"
"s_waitcnt vmcnt(4)\n"
"v_cmp_ne_u64 vcc, s[48:49], v[120:121]\n"
"v_mfma_f32_16x16x32_bf16 v[100:103], a[192:195], v[88:91], v[100:103]\n"
"v_mfma_f32_16x16x32_bf16 v[104:107], a[196:199], v[36:39], 0\n"
"s_cbranch_vccnz .LG_2s\n"
".LG_2r:\n"
"v_mfma_f32_16x16x32_bf16 v[104:107], a[200:203], v[40:43], v[104:107]\n"
"s_waitcnt lgkmcnt(0)\n"
"v_mfma_f32_16x16x32_bf16 v[104:107], a[204:207], v[44:47], v[104:107]\n"
"s_barrier\n"
"v_mfma_f32_16x16x32_bf16 v[104:107], a[208:211], v[48:51], v[104:107]\n"
"ds_read_b128 v[108:111], v2 offset:0\n"
"ds_read_b128 v[44:47], v3 offset:2560\n"
"v_mfma_f32_16x16x32_bf16 v[104:107], a[212:215], v[52:55], v[104:107]\n"
"s_add_u32 s50, s50, 0x180000\n"
"s_and_b32 s56, s50, 0xfbffffff\n"
"ds_read_b128 v[96:99], v4 offset:1024\n"
"v_mfma_f32_16x16x32_bf16 v[104:107], a[216:219], v[56:59], v[104:107]\n"
"buffer_load_dwordx4 v[92:95], v5, s[40:43], s56 offen offset:0 sc1\n"
"ds_read_b128 v[112:115], v2 offset:256\n"
"v_mfma_f32_16x16x32_bf16 v[104:107], a[220:223], v[88:91], v[104:107]\n"
"ds_read_b128 v[116:119], v3 offset:2816\n"
"ds_read_b128 v[120:123], v4 offset:1280\n"
"s_branch .LG_top\n"
".LG_1s:\n"
"s_nop 7\n"
"s_cmp_ge_u32 s50, s53\n"
"s_cbranch_scc1 .LG_end\n"
"s_cmp_ge_u32 s50, s52\n"
"s_cbranch_scc1 .LG_1r\n"
"s_sub_u32 s58, s50, 0x0\n"
"s_mov_b32 s59, 200000\n"
".LG_1p:\n"
"buffer_load_dwordx2 v[120:121], v7, s[40:43], s58 offen sc1\n"
"s_waitcnt vmcnt(0)\n"
"v_cmp_ne_u64 vcc, s[48:49], v[120:121]\n"
"s_cbranch_vccz .LG_1px\n"
"s_sleep 1\n"
"s_sub_u32 s59, s59, 1\n"
"s_cbranch_scc0 .LG_1p\n"
"s_branch .LG_end\n"
".LG_1px:\n"
"s_branch .LG_1r\n"
".LG_2s:\n"
"s_nop 7\n"
"s_cmp_ge_u32 s50, s53\n"
"s_cbranch_scc1 .LG_end\n"
"s_cmp_ge_u32 s50, s52\n"
"s_cbranch_scc1 .LG_2r\n"
"s_sub_u32 s58, s50, 0x0\n"
"s_mov_b32 s59, 200000\n"
".LG_2p:\n"
"buffer_load_dwordx2 v[120:121], v7, s[40:43], s58 offen sc1\n"
"s_waitcnt vmcnt(0)\n"
"v_cmp_ne_u64 vcc, s[48:49], v[120:121]\n"
"s_cbranch_vccz .LG_2px\n"
"s_sleep 1\n"
"s_sub_u32 s59, s59, 1\n"
"s_cbranch_scc0 .LG_2p\n"
"s_branch .LG_end\n"
".LG_2px:\n"
"s_branch .LG_2r\n"
".LG_3s:\n"
"s_nop 7\n"
"s_cmp_ge_u32 s50, s53\n"
"s_cbranch_scc1 .LG_end\n"
"s_cmp_ge_u32 s50, s52\n"
"s_cbranch_scc1 .LG_3r\n"
"s_sub_u32 s58, s50, 0x0\n"
"s_mov_b32 s59, 200000\n"
".LG_3p:\n"
"buffer_load_dwordx2 v[124:125], v7, s[40:43], s58 offen sc1\n"
"s_waitcnt vmcnt(0)\n"
"v_cmp_ne_u64 vcc, s[48:49], v[124:125]\n"
"s_cbranch_vccz .LG_3px\n"
"s_sleep 1\n"
"s_sub_u32 s59, s59, 1\n"
"s_cbranch_scc0 .LG_3p\n"
"s_branch .LG_end\n"
".LG_3px:\n"
"s_branch .LG_3r\n"
".LG_4s:\n"
"s_nop 7\n"
"s_cmp_ge_u32 s50, s53\n"
"s_cbranch_scc1 .LG_end\n"
"s_cmp_ge_u32 s50, s52\n"
"s_cbranch_scc1 .LG_4r\n"
"s_sub_u32 s58, s50, 0x0\n"
"s_mov_b32 s59, 200000\n"
".LG_4p:\n"
"buffer_load_dwordx2 v[152:153], v7, s[40:43], s58 offen sc1\n"
"s_waitcnt vmcnt(0)\n"
"v_cmp_ne_u64 vcc, s[48:49], v[152:153]\n"
"s_cbranch_vccz .LG_4px\n"
"s_sleep 1\n"
"s_sub_u32 s59, s59, 1\n"
"s_cbranch_scc0 .LG_4p\n"
"s_branch .LG_end\n"
".LG_4px:\n"
"s_branch .LG_4r\n"
".LG_end:\n"
"s_waitcnt vmcnt(0) lgkmcnt(0)\n"
"s_endpgm\n"
      :: "{v2}"((unsigned)(lds0 + ((wid + 3) & 3) * 18592 + trp)), "{v3}"((unsigned)(lds0 + ((wid + 2) & 3) * 18592 + trp)), "{v4}"((unsigned)(lds0 + ((wid + 1) & 3) * 18592 + trp)), "{v5}"((unsigned)(lane * 16 + f0 * 1024 + 98304)), "{v6}"((unsigned)(lane * 16 + f0 * 1024 + 102400)), "{v7}"((unsigned)((f0 + lane % 7) * 8 + 353024 + G_GL * 1572864)), "{v8}"((unsigned)(lane * 16 + pair * 4096 + kq * 1024 + 212992)), "{v9}"((unsigned)(lane ? DROP : pair * 32 + kq * 8 + 354048)), "{v10}"((unsigned)(lane * 16 + (pg0 + 4) * 4096 + kq * 1024 + 212992)), "{v11}"((unsigned)(lane > 1 ? DROP : (lane ? pg0 + 4 : pair) * 32 + kq * 8 + 354048)), "{v36}"((unsigned)(vT + tsw)), "{v37}"((unsigned)(vT + (tsw ^ 8))), "{v38}"((unsigned)(vD)), "{s[40:43]}"(rS), "{s[44:47]}"(rW), "{s[48:49]}"(wsp), "{s50}"((unsigned)(sA)), "{s51}"((unsigned)(sLo)), "{s52}"((unsigned)(sHi)), "{s53}"((unsigned)(sEx)), "{s54}"((unsigned)(sEnt)), "{s55}"((unsigned)(sLd)), "{s56}"((unsigned)(sA & 0xfbffffff)), "{s57}"((unsigned)(lds0 + wid * 18592)), "{s60}"((unsigned)(sW0 + 64 * ((wid + 1) & 3))), "{s61}"((unsigned)(sW0 + 64 * ((wid + 2) & 3))), "{s62}"((unsigned)(sW0 + 64 * ((wid + 3) & 3))), "{s63}"((unsigned)(sW0 + 64 * ((wid + 4) & 3))), "{s64}"((unsigned)(sW0 + 256))
      : "v255", "a255", "s95", "vcc", "scc", "memory");

    } else {
      const int v16 = kq * 4 + wid;
      const unsigned sEx = sHi + (J_EX + (set == 1)) * 3 * RS, sEnt = grp - 4;
      const u32x4 rO = desc(a.out, 2048 * 2048), bia0 = *(const u32x4*)(a.b2 + v16 * 32 + lg * 8), bia1 = *(const u32x4*)(a.b2 + 512 + v16 * 32 + lg * 8);
      const unsigned se = lg == (li >> 2) ? 0x3f80u << (16 * (li & 1)) : 0u, sel0 = (li & 2) ? 0u : se, sel1 = (li & 2) ? se : 0u;
      const u64 ep = (set == 2 && idx == 27 && wid == 0) ? (u64)a.ws : 0ull;
    asm volatile(
"s_nop 4\n"
"v_mov_b32 v186, 0\n"
"global_load_dwordx2 v[0:1], v186, s[48:49] sc1\n"
"v_mov_b32 v188, 0x3f803f80\n"
"v_mov_b32 v189, 0x3f803f80\n"
"v_mov_b32 v190, 0x3f803f80\n"
"v_mov_b32 v191, 0x3f803f80\n"
"s_add_u32 m0, s55, 0\n"
"s_add_u32 s58, s81, 0\n"
"buffer_load_dwordx4 v38, s[44:47], s58 offen lds\n"
"s_add_u32 m0, s55, 1024\n"
"s_add_u32 s58, s81, 32768\n"
"buffer_load_dwordx4 v38, s[44:47], s58 offen lds\n"
"s_add_u32 m0, s55, 2048\n"
"s_add_u32 s58, s82, 0\n"
"buffer_load_dwordx4 v38, s[44:47], s58 offen lds\n"
"s_add_u32 m0, s55, 3072\n"
"s_add_u32 s58, s82, 32768\n"
"buffer_load_dwordx4 v38, s[44:47], s58 offen lds\n"
"s_add_u32 m0, s55, 4096\n"
"s_add_u32 s58, s83, 0\n"
"buffer_load_dwordx4 v38, s[44:47], s58 offen lds\n"
"s_add_u32 m0, s55, 5120\n"
"s_add_u32 s58, s83, 32768\n"
"buffer_load_dwordx4 v38, s[44:47], s58 offen lds\n"
"s_add_u32 m0, s55, 6144\n"
"s_add_u32 s58, s84, 0\n"
"buffer_load_dwordx4 v38, s[44:47], s58 offen lds\n"
"s_add_u32 m0, s55, 7168\n"
"s_add_u32 s58, s84, 32768\n"
"buffer_load_dwordx4 v38, s[44:47], s58 offen lds\n"
"s_add_u32 m0, s55, 8192\n"
"s_add_u32 s58, s81, 65536\n"
"buffer_load_dwordx4 v38, s[44:47], s58 offen lds\n"
"s_add_u32 m0, s55, 9216\n"
"s_add_u32 s58, s81, 98304\n"
"buffer_load_dwordx4 v38, s[44:47], s58 offen lds\n"
"s_add_u32 m0, s55, 10240\n"
"s_add_u32 s58, s82, 65536\n"
"buffer_load_dwordx4 v38, s[44:47], s58 offen lds\n"
"s_add_u32 m0, s55, 11264\n"
"s_add_u32 s58, s82, 98304\n"
"buffer_load_dwordx4 v38, s[44:47], s58 offen lds\n"
"s_add_u32 m0, s55, 12288\n"
"s_add_u32 s58, s83, 65536\n"
"buffer_load_dwordx4 v38, s[44:47], s58 offen lds\n"
"s_add_u32 m0, s55, 13312\n"
"s_add_u32 s58, s83, 98304\n"
"buffer_load_dwordx4 v38, s[44:47], s58 offen lds\n"
"s_add_u32 m0, s55, 14336\n"
"s_add_u32 s58, s84, 65536\n"
"buffer_load_dwordx4 v38, s[44:47], s58 offen lds\n"
"s_add_u32 m0, s55, 15360\n"
"s_add_u32 s58, s84, 98304\n"
"buffer_load_dwordx4 v38, s[44:47], s58 offen lds\n"
"s_add_u32 m0, s55, 16384\n"
"s_add_u32 s58, s81, 131072\n"
"buffer_load_dwordx4 v38, s[44:47], s58 offen lds\n"
"s_add_u32 m0, s55, 17408\n"
"s_add_u32 s58, s81, 163840\n"
"buffer_load_dwordx4 v38, s[44:47], s58 offen lds\n"
"s_add_u32 m0, s55, 18432\n"
"s_add_u32 s58, s82, 131072\n"
"buffer_load_dwordx4 v38, s[44:47], s58 offen lds\n"
"s_add_u32 m0, s55, 19456\n"
"s_add_u32 s58, s82, 163840\n"
"buffer_load_dwordx4 v38, s[44:47], s58 offen lds\n"
"s_add_u32 m0, s55, 20480\n"
"s_add_u32 s58, s83, 131072\n"
"buffer_load_dwordx4 v38, s[44:47], s58 offen lds\n"
"s_add_u32 m0, s55, 21504\n"
"s_add_u32 s58, s83, 163840\n"
"buffer_load_dwordx4 v38, s[44:47], s58 offen lds\n"
"s_add_u32 m0, s55, 22528\n"
"s_add_u32 s58, s84, 131072\n"
"buffer_load_dwordx4 v38, s[44:47], s58 offen lds\n"
"s_add_u32 m0, s55, 23552\n"
"s_add_u32 s58, s84, 163840\n"
"buffer_load_dwordx4 v38, s[44:47], s58 offen lds\n"
"s_add_u32 m0, s55, 24576\n"
"s_add_u32 s58, s81, 196608\n"
"buffer_load_dwordx4 v38, s[44:47], s58 offen lds\n"
"s_add_u32 m0, s55, 25600\n"
"s_add_u32 s58, s81, 229376\n"
"buffer_load_dwordx4 v38, s[44:47], s58 offen lds\n"
"s_add_u32 m0, s55, 26624\n"
"s_add_u32 s58, s82, 196608\n"
"buffer_load_dwordx4 v38, s[44:47], s58 offen lds\n"
"s_add_u32 m0, s55, 27648\n"
"s_add_u32 s58, s82, 229376\n"
"buffer_load_dwordx4 v38, s[44:47], s58 offen lds\n"
"s_add_u32 m0, s55, 28672\n"
"s_add_u32 s58, s83, 196608\n"
"buffer_load_dwordx4 v38, s[44:47], s58 offen lds\n"
"s_add_u32 m0, s55, 29696\n"
"s_add_u32 s58, s83, 229376\n"
"buffer_load_dwordx4 v38, s[44:47], s58 offen lds\n"
"s_add_u32 m0, s55, 30720\n"
"s_add_u32 s58, s84, 196608\n"
"buffer_load_dwordx4 v38, s[44:47], s58 offen lds\n"
"s_add_u32 m0, s55, 31744\n"
"s_add_u32 s58, s84, 229376\n"
"buffer_load_dwordx4 v38, s[44:47], s58 offen lds\n"
"s_add_u32 m0, s55, 32768\n"
"s_add_u32 s58, s81, 262144\n"
"buffer_load_dwordx4 v38, s[44:47], s58 offen lds\n"
"s_add_u32 m0, s55, 33792\n"
"s_add_u32 s58, s81, 294912\n"
"buffer_load_dwordx4 v38, s[44:47], s58 offen lds\n"
"s_add_u32 m0, s55, 34816\n"
"s_add_u32 s58, s82, 262144\n"
"buffer_load_dwordx4 v38, s[44:47], s58 offen lds\n"
"s_add_u32 m0, s55, 35840\n"
"s_add_u32 s58, s82, 294912\n"
"buffer_load_dwordx4 v38, s[44:47], s58 offen lds\n"
"s_add_u32 m0, s55, 36864\n"
"s_add_u32 s58, s83, 262144\n"
"buffer_load_dwordx4 v38, s[44:47], s58 offen lds\n"
"s_add_u32 m0, s55, 37888\n"
"s_add_u32 s58, s83, 294912\n"
"buffer_load_dwordx4 v38, s[44:47], s58 offen lds\n"
"s_add_u32 m0, s55, 38912\n"
"s_add_u32 s58, s84, 262144\n"
"buffer_load_dwordx4 v38, s[44:47], s58 offen lds\n"
"s_add_u32 m0, s55, 39936\n"
"s_add_u32 s58, s84, 294912\n"
"buffer_load_dwordx4 v38, s[44:47], s58 offen lds\n"
"s_waitcnt vmcnt(38)\n"
"ds_read_b64_tr_b16 a[0:1], v36 offset:0\n"
"ds_read_b64_tr_b16 a[2:3], v36 offset:256\n"
"ds_read_b64_tr_b16 a[28:29], v37 offset:0\n"
"ds_read_b64_tr_b16 a[30:31], v37 offset:256\n"
"v_add_u32 v0, 2, v0\n"
"v_add_u32 v1, 0x9e3779b9, v1\n"
"v_or_b32 v0, 1, v0\n"
"v_readfirstlane_b32 s49, v1\n"
"v_readfirstlane_b32 s48, v0\n"
"s_waitcnt vmcnt(36)\n"
"ds_read_b64_tr_b16 a[56:57], v36 offset:2048\n"
"ds_read_b64_tr_b16 a[58:59], v36 offset:2304\n"
"ds_read_b64_tr_b16 a[84:85], v37 offset:2048\n"
"ds_read_b64_tr_b16 a[86:87], v37 offset:2304\n"
"s_waitcnt lgkmcnt(4)\n"
"s_add_u32 m0, s55, 0\n"
"s_add_u32 s58, s81, 327680\n"
"buffer_load_dwordx4 v38, s[44:47], s58 offen lds\n"
"s_add_u32 m0, s55, 1024\n"
"s_add_u32 s58, s81, 360448\n"
"buffer_load_dwordx4 v38, s[44:47], s58 offen lds\n"
"s_waitcnt vmcnt(36)\n"
"ds_read_b64_tr_b16 a[112:113], v36 offset:4096\n"
"ds_read_b64_tr_b16 a[114:115], v36 offset:4352\n"
"ds_read_b64_tr_b16 a[140:141], v37 offset:4096\n"
"ds_read_b64_tr_b16 a[142:143], v37 offset:4352\n"
"s_waitcnt lgkmcnt(4)\n"
"s_add_u32 m0, s55, 2048\n"
"s_add_u32 s58, s82, 327680\n"
"buffer_load_dwordx4 v38, s[44:47], s58 offen lds\n"
"s_add_u32 m0, s55, 3072\n"
"s_add_u32 s58, s82, 360448\n"
"buffer_load_dwordx4 v38, s[44:47], s58 offen lds\n"
"s_waitcnt vmcnt(36)\n"
"ds_read_b64_tr_b16 a[168:169], v36 offset:6144\n"
"ds_read_b64_tr_b16 a[170:171], v36 offset:6400\n"
"ds_read_b64_tr_b16 a[196:197], v37 offset:6144\n"
"ds_read_b64_tr_b16 a[198:199], v37 offset:6400\n"
"s_waitcnt lgkmcnt(4)\n"
"s_add_u32 m0, s55, 4096\n"
"s_add_u32 s58, s83, 327680\n"
"buffer_load_dwordx4 v38, s[44:47], s58 offen lds\n"
"s_add_u32 m0, s55, 5120\n"
"s_add_u32 s58, s83, 360448\n"
"buffer_load_dwordx4 v38, s[44:47], s58 offen lds\n"
"s_waitcnt vmcnt(36)\n"
"ds_read_b64_tr_b16 a[4:5], v36 offset:8192\n"
"ds_read_b64_tr_b16 a[6:7], v36 offset:8448\n"
"ds_read_b64_tr_b16 a[32:33], v37 offset:8192\n"
"ds_read_b64_tr_b16 a[34:35], v37 offset:8448\n"
"s_waitcnt lgkmcnt(4)\n"
"s_add_u32 m0, s55, 6144\n"
"s_add_u32 s58, s84, 327680\n"
"buffer_load_dwordx4 v38, s[44:47], s58 offen lds\n"
"s_add_u32 m0, s55, 7168\n"
"s_add_u32 s58, s84, 360448\n"
"buffer_load_dwordx4 v38, s[44:47], s58 offen lds\n"
"s_waitcnt vmcnt(36)\n"
"ds_read_b64_tr_b16 a[60:61], v36 offset:10240\n"
"ds_read_b64_tr_b16 a[62:63], v36 offset:10496\n"
"ds_read_b64_tr_b16 a[88:89], v37 offset:10240\n"
"ds_read_b64_tr_b16 a[90:91], v37 offset:10496\n"
"s_waitcnt lgkmcnt(4)\n"
"s_add_u32 m0, s55, 8192\n"
"s_add_u32 s58, s81, 393216\n"
"buffer_load_dwordx4 v38, s[44:47], s58 offen lds\n"
"s_add_u32 m0, s55, 9216\n"
"s_add_u32 s58, s81, 425984\n"
"buffer_load_dwordx4 v38, s[44:47], s58 offen lds\n"
"s_waitcnt vmcnt(36)\n"
"ds_read_b64_tr_b16 a[116:117], v36 offset:12288\n"
"ds_read_b64_tr_b16 a[118:119], v36 offset:12544\n"
"ds_read_b64_tr_b16 a[144:145], v37 offset:12288\n"
"ds_read_b64_tr_b16 a[146:147], v37 offset:12544\n"
"s_waitcnt lgkmcnt(4)\n"
"s_add_u32 m0, s55, 10240\n"
"s_add_u32 s58, s82, 393216\n"
"buffer_load_dwordx4 v38, s[44:47], s58 offen lds\n"
"s_add_u32 m0, s55, 11264\n"
"s_add_u32 s58, s82, 425984\n"
"buffer_load_dwordx4 v38, s[44:47], s58 offen lds\n"
"s_waitcnt vmcnt(36)\n"
"ds_read_b64_tr_b16 a[172:173], v36 offset:14336\n"
"ds_read_b64_tr_b16 a[174:175], v36 offset:14592\n"
"ds_read_b64_tr_b16 a[200:201], v37 offset:14336\n"
"ds_read_b64_tr_b16 a[202:203], v37 offset:14592\n"
"s_waitcnt lgkmcnt(4)\n"
"s_add_u32 m0, s55, 12288\n"
"s_add_u32 s58, s83, 393216\n"
"buffer_load_dwordx4 v38, s[44:47], s58 offen lds\n"
"s_add_u32 m0, s55, 13312\n"
"s_add_u32 s58, s83, 425984\n"
"buffer_load_dwordx4 v38, s[44:47], s58 offen lds\n"
"s_waitcnt vmcnt(36)\n"
"ds_read_b64_tr_b16 a[8:9], v36 offset:16384\n"
"ds_read_b64_tr_b16 a[10:11], v36 offset:16640\n"
"ds_read_b64_tr_b16 a[36:37], v37 offset:16384\n"
"ds_read_b64_tr_b16 a[38:39], v37 offset:16640\n"
"s_waitcnt lgkmcnt(4)\n"
"s_add_u32 m0, s55, 14336\n"
"s_add_u32 s58, s84, 393216\n"
"buffer_load_dwordx4 v38, s[44:47], s58 offen lds\n"
"s_add_u32 m0, s55, 15360\n"
"s_add_u32 s58, s84, 425984\n"
"buffer_load_dwordx4 v38, s[44:47], s58 offen lds\n"
"s_waitcnt vmcnt(36)\n"
"ds_read_b64_tr_b16 a[64:65], v36 offset:18432\n"
"ds_read_b64_tr_b16 a[66:67], v36 offset:18688\n"
"ds_read_b64_tr_b16 a[92:93], v37 offset:18432\n"
"ds_read_b64_tr_b16 a[94:95], v37 offset:18688\n"
"s_waitcnt lgkmcnt(4)\n"
"s_waitcnt vmcnt(34)\n"
"ds_read_b64_tr_b16 a[120:121], v36 offset:20480\n"
"ds_read_b64_tr_b16 a[122:123], v36 offset:20736\n"
"ds_read_b64_tr_b16 a[148:149], v37 offset:20480\n"
"ds_read_b64_tr_b16 a[150:151], v37 offset:20736\n"
"s_waitcnt lgkmcnt(4)\n"
"s_waitcnt vmcnt(32)\n"
"ds_read_b64_tr_b16 a[176:177], v36 offset:22528\n"
"ds_read_b64_tr_b16 a[178:179], v36 offset:22784\n"
"ds_read_b64_tr_b16 a[204:205], v37 offset:22528\n"
"ds_read_b64_tr_b16 a[206:207], v37 offset:22784\n"
"s_waitcnt lgkmcnt(4)\n"
"s_waitcnt vmcnt(30)\n"
"ds_read_b64_tr_b16 a[12:13], v36 offset:24576\n"
"ds_read_b64_tr_b16 a[14:15], v36 offset:24832\n"
"ds_read_b64_tr_b16 a[40:41], v37 offset:24576\n"
"ds_read_b64_tr_b16 a[42:43], v37 offset:24832\n"
"s_waitcnt lgkmcnt(4)\n"
"s_waitcnt vmcnt(28)\n"
"ds_read_b64_tr_b16 a[68:69], v36 offset:26624\n"
"ds_read_b64_tr_b16 a[70:71], v36 offset:26880\n"
"ds_read_b64_tr_b16 a[96:97], v37 offset:26624\n"
"ds_read_b64_tr_b16 a[98:99], v37 offset:26880\n"
"s_waitcnt lgkmcnt(4)\n"
"s_waitcnt vmcnt(26)\n"
"ds_read_b64_tr_b16 a[124:125], v36 offset:28672\n"
"ds_read_b64_tr_b16 a[126:127], v36 offset:28928\n"
"ds_read_b64_tr_b16 a[152:153], v37 offset:28672\n"
"ds_read_b64_tr_b16 a[154:155], v37 offset:28928\n"
"s_waitcnt lgkmcnt(4)\n"
"s_waitcnt vmcnt(24)\n"
"ds_read_b64_tr_b16 a[180:181], v36 offset:30720\n"
"ds_read_b64_tr_b16 a[182:183], v36 offset:30976\n"
"ds_read_b64_tr_b16 a[208:209], v37 offset:30720\n"
"ds_read_b64_tr_b16 a[210:211], v37 offset:30976\n"
"s_waitcnt lgkmcnt(4)\n"
"s_waitcnt vmcnt(22)\n"
"ds_read_b64_tr_b16 a[16:17], v36 offset:32768\n"
"ds_read_b64_tr_b16 a[18:19], v36 offset:33024\n"
"ds_read_b64_tr_b16 a[44:45], v37 offset:32768\n"
"ds_read_b64_tr_b16 a[46:47], v37 offset:33024\n"
"s_waitcnt lgkmcnt(4)\n"
"s_waitcnt vmcnt(20)\n"
"ds_read_b64_tr_b16 a[72:73], v36 offset:34816\n"
"ds_read_b64_tr_b16 a[74:75], v36 offset:35072\n"
"ds_read_b64_tr_b16 a[100:101], v37 offset:34816\n"
"ds_read_b64_tr_b16 a[102:103], v37 offset:35072\n"
"s_waitcnt lgkmcnt(4)\n"
"s_waitcnt vmcnt(18)\n"
"ds_read_b64_tr_b16 a[128:129], v36 offset:36864\n"
"ds_read_b64_tr_b16 a[130:131], v36 offset:37120\n"
"ds_read_b64_tr_b16 a[156:157], v37 offset:36864\n"
"ds_read_b64_tr_b16 a[158:159], v37 offset:37120\n"
"s_waitcnt lgkmcnt(4)\n"
"s_waitcnt vmcnt(16)\n"
"ds_read_b64_tr_b16 a[184:185], v36 offset:38912\n"
"ds_read_b64_tr_b16 a[186:187], v36 offset:39168\n"
"ds_read_b64_tr_b16 a[212:213], v37 offset:38912\n"
"ds_read_b64_tr_b16 a[214:215], v37 offset:39168\n"
"s_waitcnt lgkmcnt(4)\n"
"s_waitcnt vmcnt(14)\n"
"ds_read_b64_tr_b16 a[20:21], v36 offset:0\n"
"ds_read_b64_tr_b16 a[22:23], v36 offset:256\n"
"ds_read_b64_tr_b16 a[48:49], v37 offset:0\n"
"ds_read_b64_tr_b16 a[50:51], v37 offset:256\n"
"s_waitcnt lgkmcnt(4)\n"
"s_waitcnt vmcnt(12)\n"
"ds_read_b64_tr_b16 a[76:77], v36 offset:2048\n"
"ds_read_b64_tr_b16 a[78:79], v36 offset:2304\n"
"ds_read_b64_tr_b16 a[104:105], v37 offset:2048\n"
"ds_read_b64_tr_b16 a[106:107], v37 offset:2304\n"
"s_waitcnt lgkmcnt(4)\n"
"s_waitcnt vmcnt(10)\n"
"ds_read_b64_tr_b16 a[132:133], v36 offset:4096\n"
"ds_read_b64_tr_b16 a[134:135], v36 offset:4352\n"
"ds_read_b64_tr_b16 a[160:161], v37 offset:4096\n"
"ds_read_b64_tr_b16 a[162:163], v37 offset:4352\n"
"s_waitcnt lgkmcnt(4)\n"
"s_waitcnt vmcnt(8)\n"
"ds_read_b64_tr_b16 a[188:189], v36 offset:6144\n"
"ds_read_b64_tr_b16 a[190:191], v36 offset:6400\n"
"ds_read_b64_tr_b16 a[216:217], v37 offset:6144\n"
"ds_read_b64_tr_b16 a[218:219], v37 offset:6400\n"
"s_waitcnt lgkmcnt(4)\n"
"s_waitcnt vmcnt(6)\n"
"ds_read_b64_tr_b16 a[24:25], v36 offset:8192\n"
"ds_read_b64_tr_b16 a[26:27], v36 offset:8448\n"
"ds_read_b64_tr_b16 a[52:53], v37 offset:8192\n"
"ds_read_b64_tr_b16 a[54:55], v37 offset:8448\n"
"s_waitcnt lgkmcnt(4)\n"
"s_waitcnt vmcnt(4)\n"
"ds_read_b64_tr_b16 a[80:81], v36 offset:10240\n"
"ds_read_b64_tr_b16 a[82:83], v36 offset:10496\n"
"ds_read_b64_tr_b16 a[108:109], v37 offset:10240\n"
"ds_read_b64_tr_b16 a[110:111], v37 offset:10496\n"
"s_waitcnt lgkmcnt(4)\n"
"s_waitcnt vmcnt(2)\n"
"ds_read_b64_tr_b16 a[136:137], v36 offset:12288\n"
"ds_read_b64_tr_b16 a[138:139], v36 offset:12544\n"
"ds_read_b64_tr_b16 a[164:165], v37 offset:12288\n"
"ds_read_b64_tr_b16 a[166:167], v37 offset:12544\n"
"s_waitcnt lgkmcnt(4)\n"
"s_waitcnt vmcnt(0)\n"
"ds_read_b64_tr_b16 a[192:193], v36 offset:14336\n"
"ds_read_b64_tr_b16 a[194:195], v36 offset:14592\n"
"ds_read_b64_tr_b16 a[220:221], v37 offset:14336\n"
"ds_read_b64_tr_b16 a[222:223], v37 offset:14592\n"
"s_waitcnt lgkmcnt(4)\n"
"s_waitcnt vmcnt(0)\n"
"s_waitcnt lgkmcnt(0)\n"
"s_mov_b32 m0, s57\n"
"s_barrier\n"
"v_mov_b32 v120, 0\n"
"v_mov_b32 v121, 0\n"
"v_mov_b32 v132, 0\n"
"v_mov_b32 v133, 0\n"
"v_mov_b32 v128, 0\n"
"v_mov_b32 v129, 0\n"
"s_mov_b32 s38, 0x80000000\n"
"s_mov_b32 s37, 0x80000000\n"
"s_mov_b32 s36, 0x80000000\n"
"s_mov_b32 s94, 0x80000000\n"
"s_mov_b32 s94, 0x80000000\n"
"s_mov_b32 s95, 0x80000000\n"
"s_mov_b32 s38, 0x80000000\n"
"v_mov_b32 v130, 0\n"
"v_mov_b32 v131, 0\n"
"s_mov_b32 s37, 0x80000000\n"
"s_mov_b32 s36, 0x80000000\n"
"s_mov_b32 s39, 0x80000000\n"
"s_mov_b32 s39, 0x80000000\n"
"s_mov_b32 s95, 0x80000000\n"
"v_mov_b32 v122, 0\n"
"v_mov_b32 v123, 0\n"
"s_mov_b32 s94, 0x80000000\n"
"s_mov_b32 s95, 0x80000000\n"
"s_mov_b32 s94, 0x80000000\n"
"s_mov_b32 s37, 0x80000000\n"
"s_cmp_eq_u32 s54, 0\n"
"s_cbranch_scc1 .LJ_1s\n"
"s_cmp_eq_u32 s54, 1\n"
"s_cbranch_scc1 .LJ_2s\n"
"s_cmp_eq_u32 s54, 2\n"
"s_cbranch_scc1 .LJ_3s\n"
".p2align 6\n"
".LJ_top:\n"
"v_mfma_f32_16x16x32_bf16 v[36:39], v[68:71], a[0:3], 0\n"
"buffer_load_dwordx4 a[224:227], v6, s[40:43], s56 offen offset:0 sc1\n"
"v_cndmask_b32_e64 v42, v42, v43, s[60:61]\n"
"v_mfma_f32_16x16x32_bf16 v[36:39], v[72:75], a[4:7], v[36:39]\n"
"v_cndmask_b32_e64 v45, v40, v42, s[62:63]\n"
"v_mfma_f32_16x16x32_bf16 v[36:39], v[76:79], a[8:11], v[36:39]\n"
"buffer_store_dwordx2 v[44:45], v17, s[40:43], s37 offen offset:2048 sc1\n"
"v_mfma_f32_16x16x32_bf16 v[36:39], v[80:83], a[12:15], v[36:39]\n"
"s_waitcnt vmcnt(13)\n"
"v_mfma_f32_16x16x32_bf16 v[36:39], v[84:87], a[16:19], v[36:39]\n"
"s_waitcnt lgkmcnt(3)\n"
"buffer_load_dwordx4 a[228:231], v6, s[40:43], s56 offen offset:1024 sc1\n"
"v_add_f32 v52, v52, v56\n"
"v_mfma_f32_16x16x32_bf16 v[36:39], v[88:91], a[20:23], v[36:39]\n"
"s_waitcnt vmcnt(13)\n"
"v_add_f32 v53, v53, v57\n"
"v_add_f32 v54, v54, v58\n"
"v_mfma_f32_16x16x32_bf16 v[36:39], v[92:95], a[24:27], v[36:39]\n"
"v_add_f32 v55, v55, v59\n"
"v_add_f32 v52, v52, v116\n"
"v_mfma_f32_16x16x32_bf16 v[40:43], v[68:71], a[28:31], 0\n"
"v_add_f32 v53, v53, v117\n"
"v_add_f32 v54, v54, v118\n"
"v_mfma_f32_16x16x32_bf16 v[40:43], v[72:75], a[32:35], v[40:43]\n"
"ds_write_addtid_b32 v36 offset:0\n"
"buffer_load_dwordx4 a[232:235], v6, s[40:43], s56 offen offset:2048 sc1\n"
"v_mfma_f32_16x16x32_bf16 v[40:43], v[76:79], a[36:39], v[40:43]\n"
"ds_write_addtid_b32 v37 offset:4640\n"
"v_add_f32 v55, v55, v119\n"
"v_mfma_f32_16x16x32_bf16 v[40:43], v[80:83], a[40:43], v[40:43]\n"
"ds_write_addtid_b32 v38 offset:9344\n"
"v_add_f32 v52, v52, v112\n"
"v_mfma_f32_16x16x32_bf16 v[40:43], v[84:87], a[44:47], v[40:43]\n"
"ds_write_addtid_b32 v39 offset:13984\n"
"v_add_f32 v53, v53, v113\n"
"v_mfma_f32_16x16x32_bf16 v[40:43], v[88:91], a[48:51], v[40:43]\n"
"v_add_f32 v54, v54, v114\n"
"v_add_f32 v55, v55, v115\n"
"v_mfma_f32_16x16x32_bf16 v[40:43], v[92:95], a[52:55], v[40:43]\n"
"s_add_u32 s94, s50, 0xffd00000\n"
"s_and_b32 s95, s94, 0xfbffffff\n"
"v_cvt_pk_bf16_f32 v36, v52, v53\n"
"v_mfma_f32_16x16x32_bf16 v[44:47], v[68:71], a[56:59], 0\n"
"v_cvt_pk_bf16_f32 v37, v54, v55\n"
"v_mfma_f32_16x16x32_bf16 v[44:47], v[72:75], a[60:63], v[44:47]\n"
"s_waitcnt lgkmcnt(4)\n"
"ds_write_addtid_b32 v40 offset:256\n"
"v_add_f32 v120, v120, v124\n"
"v_mfma_f32_16x16x32_bf16 v[44:47], v[76:79], a[64:67], v[44:47]\n"
"ds_write_addtid_b32 v41 offset:4896\n"
"v_add_f32 v121, v121, v125\n"
"v_mfma_f32_16x16x32_bf16 v[44:47], v[80:83], a[68:71], v[44:47]\n"
"ds_write_addtid_b32 v42 offset:9600\n"
"v_add_f32 v122, v122, v126\n"
"v_mfma_f32_16x16x32_bf16 v[44:47], v[84:87], a[72:75], v[44:47]\n"
"s_waitcnt vmcnt(12)\n"
"buffer_store_dwordx2 v[0:1], v9, s[40:43], s36 offen offset:0 sc1\n"
"ds_write_addtid_b32 v43 offset:14240\n"
"v_add_f32 v123, v123, v127\n"
"v_mfma_f32_16x16x32_bf16 v[44:47], v[88:91], a[76:79], v[44:47]\n"
"v_add_f32 v120, v120, v60\n"
"v_add_f32 v121, v121, v61\n"
"v_mfma_f32_16x16x32_bf16 v[44:47], v[92:95], a[80:83], v[44:47]\n"
"v_add_f32 v122, v122, v62\n"
"v_add_f32 v123, v123, v63\n"
"v_mfma_f32_16x16x32_bf16 v[40:43], v[68:71], a[84:87], 0\n"
"v_add_f32 v120, v120, v48\n"
"v_add_f32 v121, v121, v49\n"
"v_mfma_f32_16x16x32_bf16 v[40:43], v[72:75], a[88:91], v[40:43]\n"
"s_and_b32 s39, s38, 0xfbffffff\n"
"ds_write_addtid_b32 v44 offset:512\n"
"v_add_f32 v122, v122, v50\n"
"v_mfma_f32_16x16x32_bf16 v[40:43], v[76:79], a[92:95], v[40:43]\n"
"ds_write_addtid_b32 v45 offset:5152\n"
"v_add_f32 v123, v123, v51\n"
"v_mfma_f32_16x16x32_bf16 v[40:43], v[80:83], a[96:99], v[40:43]\n"
"ds_write_addtid_b32 v46 offset:9856\n"
"v_cvt_pk_bf16_f32 v38, v120, v121\n"
"v_mfma_f32_16x16x32_bf16 v[40:43], v[84:87], a[100:103], v[40:43]\n"
"ds_write_addtid_b32 v47 offset:14496\n"
"v_cvt_pk_bf16_f32 v39, v122, v123\n"
"v_mfma_f32_16x16x32_bf16 v[40:43], v[88:91], a[104:107], v[40:43]\n"
"buffer_store_dwordx4 v[36:39], v8, s[40:43], s95 offen sc1\n"
"v_mfma_f32_16x16x32_bf16 v[40:43], v[92:95], a[108:111], v[40:43]\n"
"v_mfma_f32_16x16x32_bf16 v[44:47], v[68:71], a[112:115], 0\n"
"v_mfma_f32_16x16x32_bf16 v[44:47], v[72:75], a[116:119], v[44:47]\n"
"ds_write_addtid_b32 v40 offset:768\n"
"v_mfma_f32_16x16x32_bf16 v[44:47], v[76:79], a[120:123], v[44:47]\n"
"ds_write_addtid_b32 v41 offset:5408\n"
"v_mfma_f32_16x16x32_bf16 v[44:47], v[80:83], a[124:127], v[44:47]\n"
"ds_write_addtid_b32 v42 offset:10112\n"
"v_mfma_f32_16x16x32_bf16 v[44:47], v[84:87], a[128:131], v[44:47]\n"
"ds_write_addtid_b32 v43 offset:14752\n"
"v_mfma_f32_16x16x32_bf16 v[44:47], v[88:91], a[132:135], v[44:47]\n"
"s_waitcnt vmcnt(12)\n"
"v_cmp_ne_u64 vcc, s[48:49], v[128:129]\n"
"v_mfma_f32_16x16x32_bf16 v[44:47], v[92:95], a[136:139], v[44:47]\n"
"v_mfma_f32_16x16x32_bf16 v[60:63], v[68:71], a[140:143], 0\n"
"s_cbranch_vccnz .LJ_3s\n"
".LJ_3r:\n"
"v_mfma_f32_16x16x32_bf16 v[60:63], v[72:75], a[144:147], v[60:63]\n"
"ds_write_addtid_b32 v44 offset:1024\n"
"v_mfma_f32_16x16x32_bf16 v[60:63], v[76:79], a[148:151], v[60:63]\n"
"s_waitcnt vmcnt(9)\n"
"ds_write_addtid_b32 v45 offset:5664\n"
"v_mfma_f32_16x16x32_bf16 v[60:63], v[80:83], a[152:155], v[60:63]\n"
"ds_write_addtid_b32 v46 offset:10368\n"
"v_mfma_f32_16x16x32_bf16 v[60:63], v[84:87], a[156:159], v[60:63]\n"
"s_add_u32 s50, s50, 0x180000\n"
"s_and_b32 s56, s50, 0xfbffffff\n"
"ds_write_addtid_b32 v47 offset:15008\n"
"v_mfma_f32_16x16x32_bf16 v[60:63], v[88:91], a[160:163], v[60:63]\n"
"buffer_load_dwordx4 a[236:239], v5, s[40:43], s56 offen offset:0 sc1\n"
"v_cmp_ne_u64 vcc, s[48:49], v[130:131]\n"
"v_mfma_f32_16x16x32_bf16 v[60:63], v[92:95], a[164:167], v[60:63]\n"
"buffer_load_dwordx2 v[120:121], v7, s[40:43], s50 offen sc1\n"
"v_mfma_f32_16x16x32_bf16 v[112:115], a[168:171], v[68:71], 0\n"
"s_cbranch_vccnz .LJ_4s\n"
".LJ_4r:\n"
"v_mfma_f32_16x16x32_bf16 v[112:115], a[172:175], v[72:75], v[112:115]\n"
"s_add_u32 s95, s50, 0xff980000\n"
"ds_write_addtid_b32 v60 offset:1280\n"
"buffer_load_dwordx4 v[38:41], v11, s[40:43], s39 offen offset:0 sc1\n"
"v_mfma_f32_16x16x32_bf16 v[112:115], a[176:179], v[76:79], v[112:115]\n"
"ds_write_addtid_b32 v61 offset:5920\n"
"buffer_load_dwordx4 a[240:243], v5, s[40:43], s56 offen offset:1024 sc1\n"
"v_mfma_f32_16x16x32_bf16 v[112:115], a[180:183], v[80:83], v[112:115]\n"
"ds_write_addtid_b32 v62 offset:10624\n"
"buffer_load_dwordx4 v[42:45], v11, s[40:43], s39 offen offset:1024 sc1\n"
"v_mfma_f32_16x16x32_bf16 v[112:115], a[184:187], v[84:87], v[112:115]\n"
"ds_write_addtid_b32 v63 offset:15264\n"
"buffer_load_dwordx4 v[46:49], v11, s[40:43], s39 offen offset:2048 sc1\n"
"v_mfma_f32_16x16x32_bf16 v[112:115], a[188:191], v[88:91], v[112:115]\n"
"s_cmp_lt_u32 s95, 0x8000000\n"
"s_cselect_b32 s95, 0x80000000, s95\n"
"buffer_load_dwordx4 v[50:53], v11, s[40:43], s39 offen offset:3072 sc1\n"
"v_mfma_f32_16x16x32_bf16 v[112:115], a[192:195], v[92:95], v[112:115]\n"
"buffer_load_dwordx4 a[244:247], v5, s[40:43], s56 offen offset:2048 sc1\n"
"v_mfma_f32_16x16x32_bf16 v[116:119], a[196:199], v[68:71], 0\n"
"buffer_load_dwordx4 v[54:57], v12, s[40:43], s39 offen sc1\n"
"v_mfma_f32_16x16x32_bf16 v[116:119], a[200:203], v[72:75], v[116:119]\n"
"s_waitcnt lgkmcnt(0)\n"
"buffer_load_dwordx2 v[122:123], v10, s[40:43], s95 offen offset:512 sc1\n"
"v_mfma_f32_16x16x32_bf16 v[116:119], a[204:207], v[76:79], v[116:119]\n"
"s_barrier\n"
"v_mfma_f32_16x16x32_bf16 v[116:119], a[208:211], v[80:83], v[116:119]\n"
"s_mov_b32 s39, s38\n"
"buffer_load_dwordx4 a[248:251], v5, s[40:43], s56 offen offset:3072 sc1\n"
"ds_read_b128 v[68:71], v2 offset:0\n"
"v_mfma_f32_16x16x32_bf16 v[116:119], a[212:215], v[84:87], v[116:119]\n"
"s_waitcnt vmcnt(15)\n"
"buffer_store_dwordx2 v[0:1], v18, s[40:43], s37 offen offset:128 sc1\n"
"ds_read_b128 v[64:67], v3 offset:512\n"
"ds_read_b128 v[72:75], v4 offset:1024\n"
"v_mfma_f32_16x16x32_bf16 v[116:119], a[216:219], v[88:91], v[116:119]\n"
"ds_read_b128 v[76:79], v2 offset:256\n"
"ds_read_b128 v[80:83], v3 offset:768\n"
"v_mfma_f32_16x16x32_bf16 v[116:119], a[220:223], v[92:95], v[116:119]\n"
"ds_read_b128 v[84:87], v4 offset:1280\n"
"v_mov_b32 v36, v20\n"
"v_mfma_f32_16x16x32_bf16 v[88:91], v[96:99], a[0:3], 0\n"
"buffer_load_dwordx4 a[252:255], v6, s[40:43], s56 offen offset:0 sc1\n"
"v_mov_b32 v37, v21\n"
"v_mfma_f32_16x16x32_bf16 v[88:91], v[100:103], a[4:7], v[88:91]\n"
"v_mov_b32 v58, v22\n"
"v_mov_b32 v59, v23\n"
"v_mfma_f32_16x16x32_bf16 v[88:91], v[104:107], a[8:11], v[88:91]\n"
"v_mfma_f32_16x16x32_bf16 v[88:91], v[108:111], a[12:15], v[88:91]\n"
"s_waitcnt vmcnt(16)\n"
"v_mfma_f32_16x16x32_bf16 v[88:91], a[224:227], a[16:19], v[88:91]\n"
"s_waitcnt lgkmcnt(3)\n"
"buffer_load_dwordx4 v[60:63], v6, s[40:43], s56 offen offset:1024 sc1\n"
"v_add_f32 v68, v68, v64\n"
"v_mfma_f32_16x16x32_bf16 v[88:91], a[228:231], a[20:23], v[88:91]\n"
"s_waitcnt vmcnt(16)\n"
"v_add_f32 v69, v69, v65\n"
"v_add_f32 v70, v70, v66\n"
"v_mfma_f32_16x16x32_bf16 v[88:91], a[232:235], a[24:27], v[88:91]\n"
"v_add_f32 v71, v71, v67\n"
"v_add_f32 v68, v68, v72\n"
"v_mfma_f32_16x16x32_bf16 v[92:95], v[96:99], a[28:31], 0\n"
"v_add_f32 v69, v69, v73\n"
"v_add_f32 v70, v70, v74\n"
"v_mfma_f32_16x16x32_bf16 v[92:95], v[100:103], a[32:35], v[92:95]\n"
"ds_write_addtid_b32 v88 offset:1536\n"
"buffer_load_dwordx4 v[64:67], v6, s[40:43], s56 offen offset:2048 sc1\n"
"v_mfma_f32_16x16x32_bf16 v[92:95], v[104:107], a[36:39], v[92:95]\n"
"ds_write_addtid_b32 v89 offset:6176\n"
"v_add_f32 v71, v71, v75\n"
"v_mfma_f32_16x16x32_bf16 v[92:95], v[108:111], a[40:43], v[92:95]\n"
"ds_write_addtid_b32 v90 offset:10880\n"
"v_add_f32 v68, v68, v112\n"
"v_mfma_f32_16x16x32_bf16 v[92:95], a[224:227], a[44:47], v[92:95]\n"
"ds_write_addtid_b32 v91 offset:15520\n"
"v_add_f32 v69, v69, v113\n"
"v_mfma_f32_16x16x32_bf16 v[92:95], a[228:231], a[48:51], v[92:95]\n"
"v_add_f32 v70, v70, v114\n"
"v_add_f32 v71, v71, v115\n"
"v_mfma_f32_16x16x32_bf16 v[92:95], a[232:235], a[52:55], v[92:95]\n"
"s_add_u32 s38, s50, 0xffd00000\n"
"s_and_b32 s37, s38, 0xfbffffff\n"
"v_cvt_pk_bf16_f32 v72, v68, v69\n"
"v_mfma_f32_16x16x32_bf16 v[88:91], v[96:99], a[56:59], 0\n"
"v_cvt_pk_bf16_f32 v73, v70, v71\n"
"v_mfma_f32_16x16x32_bf16 v[88:91], v[100:103], a[60:63], v[88:91]\n"
"s_waitcnt lgkmcnt(4)\n"
"ds_write_addtid_b32 v92 offset:1792\n"
"v_add_f32 v76, v76, v80\n"
"v_mfma_f32_16x16x32_bf16 v[88:91], v[104:107], a[64:67], v[88:91]\n"
"ds_write_addtid_b32 v93 offset:6432\n"
"v_add_f32 v77, v77, v81\n"
"v_mfma_f32_16x16x32_bf16 v[88:91], v[108:111], a[68:71], v[88:91]\n"
"ds_write_addtid_b32 v94 offset:11136\n"
"v_add_f32 v78, v78, v82\n"
"v_mfma_f32_16x16x32_bf16 v[88:91], a[224:227], a[72:75], v[88:91]\n"
"s_waitcnt vmcnt(15)\n"
"buffer_store_dwordx2 v[0:1], v9, s[40:43], s94 offen offset:0 sc1\n"
"ds_write_addtid_b32 v95 offset:15776\n"
"v_add_f32 v79, v79, v83\n"
"v_mfma_f32_16x16x32_bf16 v[88:91], a[228:231], a[76:79], v[88:91]\n"
"v_add_f32 v76, v76, v84\n"
"v_add_f32 v77, v77, v85\n"
"v_mfma_f32_16x16x32_bf16 v[88:91], a[232:235], a[80:83], v[88:91]\n"
"v_add_f32 v78, v78, v86\n"
"v_add_f32 v79, v79, v87\n"
"v_mfma_f32_16x16x32_bf16 v[68:71], v[96:99], a[84:87], 0\n"
"v_add_f32 v76, v76, v116\n"
"v_add_f32 v77, v77, v117\n"
"v_mfma_f32_16x16x32_bf16 v[68:71], v[100:103], a[88:91], v[68:71]\n"
"ds_write_addtid_b32 v88 offset:2048\n"
"v_add_f32 v78, v78, v118\n"
"v_mfma_f32_16x16x32_bf16 v[68:71], v[104:107], a[92:95], v[68:71]\n"
"ds_write_addtid_b32 v89 offset:6688\n"
"v_add_f32 v79, v79, v119\n"
"v_mfma_f32_16x16x32_bf16 v[68:71], v[108:111], a[96:99], v[68:71]\n"
"ds_write_addtid_b32 v90 offset:11392\n"
"v_cvt_pk_bf16_f32 v74, v76, v77\n"
"v_mfma_f32_16x16x32_bf16 v[68:71], a[224:227], a[100:103], v[68:71]\n"
"ds_write_addtid_b32 v91 offset:16032\n"
"v_cvt_pk_bf16_f32 v75, v78, v79\n"
"v_mfma_f32_16x16x32_bf16 v[68:71], a[228:231], a[104:107], v[68:71]\n"
"buffer_store_dwordx4 v[72:75], v8, s[40:43], s37 offen sc1\n"
"v_mfma_f32_16x16x32_bf16 v[68:71], a[232:235], a[108:111], v[68:71]\n"
"v_mfma_f32_16x16x32_bf16 v[76:79], v[96:99], a[112:115], 0\n"
"s_and_b32 s94, s95, 0xfbffffff\n"
"v_mfma_f32_16x16x32_bf16 v[76:79], v[100:103], a[116:119], v[76:79]\n"
"ds_write_addtid_b32 v68 offset:2304\n"
"v_mfma_f32_16x16x32_bf16 v[76:79], v[104:107], a[120:123], v[76:79]\n"
"ds_write_addtid_b32 v69 offset:6944\n"
"v_mfma_f32_16x16x32_bf16 v[76:79], v[108:111], a[124:127], v[76:79]\n"
"ds_write_addtid_b32 v70 offset:11648\n"
"v_mfma_f32_16x16x32_bf16 v[76:79], a[224:227], a[128:131], v[76:79]\n"
"ds_write_addtid_b32 v71 offset:16288\n"
"v_mfma_f32_16x16x32_bf16 v[76:79], a[228:231], a[132:135], v[76:79]\n"
"s_waitcnt vmcnt(15)\n"
"v_cmp_ne_u64 vcc, s[48:49], v[120:121]\n"
"v_mfma_f32_16x16x32_bf16 v[76:79], a[232:235], a[136:139], v[76:79]\n"
"v_mfma_f32_16x16x32_bf16 v[80:83], v[96:99], a[140:143], 0\n"
"s_cbranch_vccnz .LJ_1s\n"
".LJ_1r:\n"
"v_mfma_f32_16x16x32_bf16 v[80:83], v[100:103], a[144:147], v[80:83]\n"
"ds_write_addtid_b32 v76 offset:2560\n"
"v_mfma_f32_16x16x32_bf16 v[80:83], v[104:107], a[148:151], v[80:83]\n"
"ds_write_addtid_b32 v77 offset:7200\n"
"v_mfma_f32_16x16x32_bf16 v[80:83], v[108:111], a[152:155], v[80:83]\n"
"ds_write_addtid_b32 v78 offset:11904\n"
"v_mfma_f32_16x16x32_bf16 v[80:83], a[224:227], a[156:159], v[80:83]\n"
"s_add_u32 s50, s50, 0x180000\n"
"s_and_b32 s56, s50, 0xfbffffff\n"
"ds_write_addtid_b32 v79 offset:16544\n"
"v_mfma_f32_16x16x32_bf16 v[80:83], a[228:231], a[160:163], v[80:83]\n"
"buffer_load_dwordx4 v[68:71], v5, s[40:43], s56 offen offset:0 sc1\n"
"v_mfma_f32_16x16x32_bf16 v[80:83], a[232:235], a[164:167], v[80:83]\n"
"s_waitcnt vmcnt(9)\n"
"buffer_load_dwordx2 v[132:133], v7, s[40:43], s50 offen sc1\n"
"v_mfma_f32_16x16x32_bf16 v[72:75], v[32:35], v[36:39], 0\n"
"s_cmp_ge_i32 s39, s76\n"
"s_cbranch_scc1 .LJ_w0s\n"
".LJ_w0r:\n"
"v_mfma_f32_16x16x32_bf16 v[72:75], v[32:35], v[44:47], v[72:75]\n"
"s_lshr_b32 s37, s39, s77\n"
"v_mfma_f32_16x16x32_bf16 v[72:75], v[32:35], v[52:55], v[72:75]\n"
"s_bitcmp1_b32 s37, 15\n"
"s_cselect_b32 s36, s79, s78\n"
"v_mfma_f32_16x16x32_bf16 v[76:79], v[32:35], v[40:43], 0\n"
"s_add_u32 s37, s37, s36\n"
"v_mfma_f32_16x16x32_bf16 v[76:79], v[32:35], v[48:51], v[76:79]\n"
"s_waitcnt vmcnt(9)\n"
"v_cmp_ne_u64 vcc, s[48:49], v[122:123]\n"
"v_mfma_f32_16x16x32_bf16 v[76:79], v[32:35], v[56:59], v[76:79]\n"
"v_cvt_pk_bf16_f32 v84, v72, v73\n"
"v_cvt_pk_bf16_f32 v85, v74, v75\n"
"v_mfma_f32_16x16x32_bf16 v[112:115], a[168:171], v[96:99], 0\n"
"s_cbranch_vccnz .LJ_5s\n"
".LJ_5r:\n"
"v_mfma_f32_16x16x32_bf16 v[112:115], a[172:175], v[100:103], v[112:115]\n"
"ds_write_addtid_b32 v80 offset:2816\n"
"v_cvt_pk_bf16_f32 v86, v76, v77\n"
"v_mfma_f32_16x16x32_bf16 v[112:115], a[176:179], v[104:107], v[112:115]\n"
"ds_write_addtid_b32 v81 offset:7456\n"
"buffer_load_dwordx4 v[72:75], v5, s[40:43], s56 offen offset:1024 sc1\n"
"v_mfma_f32_16x16x32_bf16 v[112:115], a[180:183], v[108:111], v[112:115]\n"
"ds_write_addtid_b32 v82 offset:12160\n"
"v_cvt_pk_bf16_f32 v87, v78, v79\n"
"v_mfma_f32_16x16x32_bf16 v[112:115], a[184:187], a[224:227], v[112:115]\n"
"buffer_store_dwordx4 v[84:87], v15, s[72:75], s37 offen sc1\n"
"ds_write_addtid_b32 v83 offset:16800\n"
"buffer_load_dwordx4 v[38:41], v13, s[40:43], s94 offen offset:0 sc1\n"
"v_mfma_f32_16x16x32_bf16 v[88:91], v[84:87], v[84:87], 0\n"
"buffer_load_dwordx4 v[42:45], v13, s[40:43], s94 offen offset:1024 sc1\n"
"v_mfma_f32_16x16x32_bf16 v[80:83], v[28:31], v[84:87], 0\n"
"buffer_load_dwordx4 v[46:49], v13, s[40:43], s94 offen offset:2048 sc1\n"
"v_mfma_f32_16x16x32_bf16 v[112:115], a[188:191], a[228:231], v[112:115]\n"
"v_cndmask_b32_e64 v88, v88, v89, s[60:61]\n"
"v_cndmask_b32_e64 v90, v90, v91, s[60:61]\n"
"v_mfma_f32_16x16x32_bf16 v[112:115], a[192:195], a[232:235], v[112:115]\n"
"buffer_load_dwordx4 v[76:79], v5, s[40:43], s56 offen offset:2048 sc1\n"
"v_cndmask_b32_e64 v81, v88, v90, s[62:63]\n"
"v_mfma_f32_16x16x32_bf16 v[116:119], a[196:199], v[96:99], 0\n"
"buffer_store_dwordx2 v[80:81], v17, s[40:43], s39 offen offset:0 sc1\n"
"v_mfma_f32_16x16x32_bf16 v[116:119], a[200:203], v[100:103], v[116:119]\n"
"s_waitcnt lgkmcnt(0)\n"
"buffer_load_dwordx4 v[50:53], v13, s[40:43], s94 offen offset:3072 sc1\n"
"v_mfma_f32_16x16x32_bf16 v[116:119], a[204:207], v[104:107], v[116:119]\n"
"s_barrier\n"
"v_mfma_f32_16x16x32_bf16 v[116:119], a[208:211], v[108:111], v[116:119]\n"
"buffer_load_dwordx4 v[80:83], v5, s[40:43], s56 offen offset:3072 sc1\n"
"ds_read_b128 v[96:99], v2 offset:1536\n"
"v_mfma_f32_16x16x32_bf16 v[116:119], a[212:215], a[224:227], v[116:119]\n"
"ds_read_b128 v[92:95], v3 offset:2048\n"
"ds_read_b128 v[100:103], v4 offset:2560\n"
"v_mfma_f32_16x16x32_bf16 v[116:119], a[216:219], a[228:231], v[116:119]\n"
"ds_read_b128 v[104:107], v2 offset:1792\n"
"ds_read_b128 v[108:111], v3 offset:2304\n"
"v_mfma_f32_16x16x32_bf16 v[116:119], a[220:223], a[232:235], v[116:119]\n"
"ds_read_b128 v[120:123], v4 offset:2816\n"
"buffer_load_dwordx4 v[54:57], v14, s[40:43], s94 offen sc1\n"
"v_mfma_f32_16x16x32_bf16 v[124:127], a[236:239], a[0:3], 0\n"
"s_mov_b32 s37, s95\n"
"buffer_load_dwordx4 v[84:87], v6, s[40:43], s56 offen offset:0 sc1\n"
"v_mov_b32 v36, v24\n"
"v_mfma_f32_16x16x32_bf16 v[124:127], a[240:243], a[4:7], v[124:127]\n"
"s_waitcnt vmcnt(19)\n"
"v_mov_b32 v37, v25\n"
"v_mov_b32 v58, v26\n"
"v_mfma_f32_16x16x32_bf16 v[124:127], a[244:247], a[8:11], v[124:127]\n"
"v_mov_b32 v59, v27\n"
"v_mfma_f32_16x16x32_bf16 v[124:127], a[248:251], a[12:15], v[124:127]\n"
"s_waitcnt vmcnt(16)\n"
"v_mfma_f32_16x16x32_bf16 v[124:127], a[252:255], a[16:19], v[124:127]\n"
"s_waitcnt lgkmcnt(3)\n"
"buffer_load_dwordx4 v[88:91], v6, s[40:43], s56 offen offset:1024 sc1\n"
"v_add_f32 v96, v96, v92\n"
"v_mfma_f32_16x16x32_bf16 v[124:127], v[60:63], a[20:23], v[124:127]\n"
"s_waitcnt vmcnt(16)\n"
"v_add_f32 v97, v97, v93\n"
"v_add_f32 v98, v98, v94\n"
"v_mfma_f32_16x16x32_bf16 v[124:127], v[64:67], a[24:27], v[124:127]\n"
"v_add_f32 v99, v99, v95\n"
"v_add_f32 v96, v96, v100\n"
"v_mfma_f32_16x16x32_bf16 v[128:131], a[236:239], a[28:31], 0\n"
"v_add_f32 v97, v97, v101\n"
"v_add_f32 v98, v98, v102\n"
"v_mfma_f32_16x16x32_bf16 v[128:131], a[240:243], a[32:35], v[128:131]\n"
"ds_write_addtid_b32 v124 offset:3072\n"
"buffer_load_dwordx4 v[92:95], v6, s[40:43], s56 offen offset:2048 sc1\n"
"v_mfma_f32_16x16x32_bf16 v[128:131], a[244:247], a[36:39], v[128:131]\n"
"ds_write_addtid_b32 v125 offset:7712\n"
"v_add_f32 v99, v99, v103\n"
"v_mfma_f32_16x16x32_bf16 v[128:131], a[248:251], a[40:43], v[128:131]\n"
"ds_write_addtid_b32 v126 offset:12416\n"
"v_add_f32 v96, v96, v112\n"
"v_mfma_f32_16x16x32_bf16 v[128:131], a[252:255], a[44:47], v[128:131]\n"
"ds_write_addtid_b32 v127 offset:17056\n"
"v_add_f32 v97, v97, v113\n"
"v_mfma_f32_16x16x32_bf16 v[128:131], v[60:63], a[48:51], v[128:131]\n"
"v_add_f32 v98, v98, v114\n"
"v_add_f32 v99, v99, v115\n"
"v_mfma_f32_16x16x32_bf16 v[128:131], v[64:67], a[52:55], v[128:131]\n"
"s_add_u32 s36, s50, 0xffd00000\n"
"s_and_b32 s94, s36, 0xfbffffff\n"
"v_cvt_pk_bf16_f32 v100, v96, v97\n"
"v_mfma_f32_16x16x32_bf16 v[112:115], a[236:239], a[56:59], 0\n"
"v_cvt_pk_bf16_f32 v101, v98, v99\n"
"v_mfma_f32_16x16x32_bf16 v[112:115], a[240:243], a[60:63], v[112:115]\n"
"s_waitcnt lgkmcnt(4)\n"
"ds_write_addtid_b32 v128 offset:3328\n"
"v_add_f32 v104, v104, v108\n"
"v_mfma_f32_16x16x32_bf16 v[112:115], a[244:247], a[64:67], v[112:115]\n"
"ds_write_addtid_b32 v129 offset:7968\n"
"v_add_f32 v105, v105, v109\n"
"v_mfma_f32_16x16x32_bf16 v[112:115], a[248:251], a[68:71], v[112:115]\n"
"ds_write_addtid_b32 v130 offset:12672\n"
"v_add_f32 v106, v106, v110\n"
"v_mfma_f32_16x16x32_bf16 v[112:115], a[252:255], a[72:75], v[112:115]\n"
"s_waitcnt vmcnt(15)\n"
"buffer_store_dwordx2 v[0:1], v9, s[40:43], s38 offen offset:0 sc1\n"
"ds_write_addtid_b32 v131 offset:17312\n"
"v_add_f32 v107, v107, v111\n"
"v_mfma_f32_16x16x32_bf16 v[112:115], v[60:63], a[76:79], v[112:115]\n"
"v_add_f32 v104, v104, v120\n"
"v_add_f32 v105, v105, v121\n"
"v_mfma_f32_16x16x32_bf16 v[112:115], v[64:67], a[80:83], v[112:115]\n"
"v_add_f32 v106, v106, v122\n"
"v_add_f32 v107, v107, v123\n"
"v_mfma_f32_16x16x32_bf16 v[96:99], a[236:239], a[84:87], 0\n"
"v_add_f32 v104, v104, v116\n"
"v_add_f32 v105, v105, v117\n"
"v_mfma_f32_16x16x32_bf16 v[96:99], a[240:243], a[88:91], v[96:99]\n"
"ds_write_addtid_b32 v112 offset:3584\n"
"v_add_f32 v106, v106, v118\n"
"v_mfma_f32_16x16x32_bf16 v[96:99], a[244:247], a[92:95], v[96:99]\n"
"ds_write_addtid_b32 v113 offset:8224\n"
"v_add_f32 v107, v107, v119\n"
"v_mfma_f32_16x16x32_bf16 v[96:99], a[248:251], a[96:99], v[96:99]\n"
"ds_write_addtid_b32 v114 offset:12928\n"
"v_cvt_pk_bf16_f32 v102, v104, v105\n"
"v_mfma_f32_16x16x32_bf16 v[96:99], a[252:255], a[100:103], v[96:99]\n"
"ds_write_addtid_b32 v115 offset:17568\n"
"v_cvt_pk_bf16_f32 v103, v106, v107\n"
"v_mfma_f32_16x16x32_bf16 v[96:99], v[60:63], a[104:107], v[96:99]\n"
"buffer_store_dwordx4 v[100:103], v8, s[40:43], s94 offen sc1\n"
"v_mfma_f32_16x16x32_bf16 v[96:99], v[64:67], a[108:111], v[96:99]\n"
"v_mfma_f32_16x16x32_bf16 v[104:107], a[236:239], a[112:115], 0\n"
"v_mfma_f32_16x16x32_bf16 v[104:107], a[240:243], a[116:119], v[104:107]\n"
"ds_write_addtid_b32 v96 offset:3840\n"
"v_mfma_f32_16x16x32_bf16 v[104:107], a[244:247], a[120:123], v[104:107]\n"
"ds_write_addtid_b32 v97 offset:8480\n"
"v_mfma_f32_16x16x32_bf16 v[104:107], a[248:251], a[124:127], v[104:107]\n"
"ds_write_addtid_b32 v98 offset:13184\n"
"v_mfma_f32_16x16x32_bf16 v[104:107], a[252:255], a[128:131], v[104:107]\n"
"ds_write_addtid_b32 v99 offset:17824\n"
"v_mfma_f32_16x16x32_bf16 v[104:107], v[60:63], a[132:135], v[104:107]\n"
"s_waitcnt vmcnt(15)\n"
"v_cmp_ne_u64 vcc, s[48:49], v[132:133]\n"
"v_mfma_f32_16x16x32_bf16 v[104:107], v[64:67], a[136:139], v[104:107]\n"
"v_mfma_f32_16x16x32_bf16 v[108:111], a[236:239], a[140:143], 0\n"
"s_cbranch_vccnz .LJ_2s\n"
".LJ_2r:\n"
"v_mfma_f32_16x16x32_bf16 v[108:111], a[240:243], a[144:147], v[108:111]\n"
"ds_write_addtid_b32 v104 offset:4096\n"
"v_mfma_f32_16x16x32_bf16 v[108:111], a[244:247], a[148:151], v[108:111]\n"
"ds_write_addtid_b32 v105 offset:8736\n"
"v_mfma_f32_16x16x32_bf16 v[108:111], a[248:251], a[152:155], v[108:111]\n"
"ds_write_addtid_b32 v106 offset:13440\n"
"v_mfma_f32_16x16x32_bf16 v[108:111], a[252:255], a[156:159], v[108:111]\n"
"s_add_u32 s50, s50, 0x180000\n"
"s_and_b32 s56, s50, 0xfbffffff\n"
"ds_write_addtid_b32 v107 offset:18080\n"
"v_mfma_f32_16x16x32_bf16 v[108:111], v[60:63], a[160:163], v[108:111]\n"
"buffer_load_dwordx4 v[96:99], v5, s[40:43], s56 offen offset:0 sc1\n"
"v_mfma_f32_16x16x32_bf16 v[108:111], v[64:67], a[164:167], v[108:111]\n"
"s_waitcnt vmcnt(9)\n"
"buffer_load_dwordx2 v[128:129], v7, s[40:43], s50 offen sc1\n"
"v_mfma_f32_16x16x32_bf16 v[112:115], a[168:171], a[236:239], 0\n"
"s_add_u32 s38, s50, 0xff980000\n"
"buffer_store_dwordx2 v[0:1], v18, s[40:43], s39 offen offset:0 sc1\n"
"v_mfma_f32_16x16x32_bf16 v[112:115], a[172:175], a[240:243], v[112:115]\n"
"s_cmp_lt_u32 s38, 0x8000000\n"
"s_cselect_b32 s38, 0x80000000, s38\n"
"ds_write_addtid_b32 v108 offset:4352\n"
"v_mfma_f32_16x16x32_bf16 v[112:115], a[176:179], a[244:247], v[112:115]\n"
"ds_write_addtid_b32 v109 offset:8992\n"
"buffer_load_dwordx4 v[100:103], v5, s[40:43], s56 offen offset:1024 sc1\n"
"v_mfma_f32_16x16x32_bf16 v[112:115], a[180:183], a[248:251], v[112:115]\n"
"ds_write_addtid_b32 v110 offset:13696\n"
"buffer_load_dwordx2 v[130:131], v10, s[40:43], s38 offen offset:0 sc1\n"
"v_mfma_f32_16x16x32_bf16 v[112:115], a[184:187], a[252:255], v[112:115]\n"
"ds_write_addtid_b32 v111 offset:18336\n"
"v_mfma_f32_16x16x32_bf16 v[112:115], a[188:191], v[60:63], v[112:115]\n"
"s_waitcnt vmcnt(10)\n"
"v_mfma_f32_16x16x32_bf16 v[116:119], v[32:35], v[36:39], 0\n"
"s_cmp_ge_i32 s37, s76\n"
"s_cbranch_scc1 .LJ_w1s\n"
".LJ_w1r:\n"
"v_mfma_f32_16x16x32_bf16 v[116:119], v[32:35], v[44:47], v[116:119]\n"
"s_lshr_b32 s94, s37, s77\n"
"v_mfma_f32_16x16x32_bf16 v[116:119], v[32:35], v[52:55], v[116:119]\n"
"s_bitcmp1_b32 s94, 15\n"
"s_cselect_b32 s95, s79, s78\n"
"v_mfma_f32_16x16x32_bf16 v[108:111], v[32:35], v[40:43], 0\n"
"s_add_u32 s94, s94, s95\n"
"v_mfma_f32_16x16x32_bf16 v[108:111], v[32:35], v[48:51], v[108:111]\n"
"s_add_u32 s94, s94, s80\n"
"v_mfma_f32_16x16x32_bf16 v[108:111], v[32:35], v[56:59], v[108:111]\n"
"v_cvt_pk_bf16_f32 v120, v116, v117\n"
"v_cvt_pk_bf16_f32 v121, v118, v119\n"
"v_mfma_f32_16x16x32_bf16 v[112:115], a[192:195], v[64:67], v[112:115]\n"
"buffer_load_dwordx4 v[104:107], v5, s[40:43], s56 offen offset:2048 sc1\n"
"v_mfma_f32_16x16x32_bf16 v[48:51], a[196:199], a[236:239], 0\n"
"v_cvt_pk_bf16_f32 v122, v108, v109\n"
"v_cvt_pk_bf16_f32 v123, v110, v111\n"
"v_mfma_f32_16x16x32_bf16 v[48:51], a[200:203], a[240:243], v[48:51]\n"
"s_waitcnt lgkmcnt(0)\n"
"buffer_store_dwordx4 v[120:123], v15, s[72:75], s94 offen sc1\n"
"v_mfma_f32_16x16x32_bf16 v[40:43], v[120:123], v[120:123], 0\n"
"v_mfma_f32_16x16x32_bf16 v[44:47], v[28:31], v[120:123], 0\n"
"v_mfma_f32_16x16x32_bf16 v[48:51], a[204:207], a[244:247], v[48:51]\n"
"s_barrier\n"
"v_mfma_f32_16x16x32_bf16 v[48:51], a[208:211], a[248:251], v[48:51]\n"
"buffer_load_dwordx4 v[108:111], v5, s[40:43], s56 offen offset:3072 sc1\n"
"ds_read_b128 v[52:55], v2 offset:3072\n"
"v_mfma_f32_16x16x32_bf16 v[48:51], a[212:215], a[252:255], v[48:51]\n"
"ds_read_b128 v[56:59], v3 offset:3584\n"
"ds_read_b128 v[116:119], v4 offset:4096\n"
"v_mfma_f32_16x16x32_bf16 v[48:51], a[216:219], v[60:63], v[48:51]\n"
"ds_read_b128 v[120:123], v2 offset:3328\n"
"ds_read_b128 v[124:127], v3 offset:3840\n"
"v_mfma_f32_16x16x32_bf16 v[48:51], a[220:223], v[64:67], v[48:51]\n"
"ds_read_b128 v[60:63], v4 offset:4352\n"
"v_cndmask_b32_e64 v40, v40, v41, s[60:61]\n"
"s_branch .LJ_top\n"
".LJ_1s:\n"
"s_nop 7\n"
"s_cmp_ge_u32 s50, s53\n"
"s_cbranch_scc1 .LJ_end\n"
"s_cmp_ge_u32 s50, s52\n"
"s_cbranch_scc1 .LJ_1r\n"
"s_sub_u32 s58, s50, 0x0\n"
"s_mov_b32 s59, 200000\n"
".LJ_1p:\n"
"buffer_load_dwordx2 v[120:121], v7, s[40:43], s58 offen sc1\n"
"s_waitcnt vmcnt(0)\n"
"v_cmp_ne_u64 vcc, s[48:49], v[120:121]\n"
"s_cbranch_vccz .LJ_1px\n"
"s_sleep 1\n"
"s_sub_u32 s59, s59, 1\n"
"s_cbranch_scc0 .LJ_1p\n"
"s_branch .LJ_end\n"
".LJ_1px:\n"
"s_branch .LJ_1r\n"
".LJ_2s:\n"
"s_nop 7\n"
"s_cmp_ge_u32 s50, s53\n"
"s_cbranch_scc1 .LJ_end\n"
"s_cmp_ge_u32 s50, s52\n"
"s_cbranch_scc1 .LJ_2r\n"
"s_sub_u32 s58, s50, 0x0\n"
"s_mov_b32 s59, 200000\n"
".LJ_2p:\n"
"buffer_load_dwordx2 v[132:133], v7, s[40:43], s58 offen sc1\n"
"s_waitcnt vmcnt(0)\n"
"v_cmp_ne_u64 vcc, s[48:49], v[132:133]\n"
"s_cbranch_vccz .LJ_2px\n"
"s_sleep 1\n"
"s_sub_u32 s59, s59, 1\n"
"s_cbranch_scc0 .LJ_2p\n"
"s_branch .LJ_end\n"
".LJ_2px:\n"
"s_branch .LJ_2r\n"
".LJ_3s:\n"
"s_nop 7\n"
"s_cmp_ge_u32 s50, s53\n"
"s_cbranch_scc1 .LJ_end\n"
"s_cmp_ge_u32 s50, s52\n"
"s_cbranch_scc1 .LJ_3r\n"
"s_sub_u32 s58, s50, 0x0\n"
"s_mov_b32 s59, 200000\n"
".LJ_3p:\n"
"buffer_load_dwordx2 v[128:129], v7, s[40:43], s58 offen sc1\n"
"s_waitcnt vmcnt(0)\n"
"v_cmp_ne_u64 vcc, s[48:49], v[128:129]\n"
"s_cbranch_vccz .LJ_3px\n"
"s_sleep 1\n"
"s_sub_u32 s59, s59, 1\n"
"s_cbranch_scc0 .LJ_3p\n"
"s_branch .LJ_end\n"
".LJ_3px:\n"
"s_branch .LJ_3r\n"
".LJ_4s:\n"
"s_nop 7\n"
"s_cmp_ge_u32 s38, 0x10000000\n"
"s_cbranch_scc1 .LJ_4r\n"
"s_mov_b32 s59, 200000\n"
".LJ_4p:\n"
"buffer_load_dwordx2 v[130:131], v10, s[40:43], s38 offen offset:0 sc1\n"
"s_waitcnt vmcnt(0)\n"
"v_cmp_ne_u64 vcc, s[48:49], v[130:131]\n"
"s_cbranch_vccz .LJ_4px\n"
"s_sleep 1\n"
"s_sub_u32 s59, s59, 1\n"
"s_cbranch_scc0 .LJ_4p\n"
"s_branch .LJ_end\n"
".LJ_4px:\n"
"s_branch .LJ_4r\n"
".LJ_w0s:\n"
"s_nop 7\n"
"s_mov_b64 s[72:73], s[64:65]\n"
"s_mov_b32 s74, s66\n"
"v_mov_b32 v15, v16\n"
"s_mov_b32 s76, 0x7fffffff\n"
"s_mov_b32 s77, 4\n"
"s_mov_b32 s78, 0xff400000\n"
"s_mov_b32 s79, 0xff3f8800\n"
"s_mov_b32 s80, 1024\n"
"s_branch .LJ_w0r\n"
".LJ_5s:\n"
"s_nop 7\n"
"s_cmp_ge_u32 s95, 0x10000000\n"
"s_cbranch_scc1 .LJ_5r\n"
"s_mov_b32 s59, 200000\n"
".LJ_5p:\n"
"buffer_load_dwordx2 v[122:123], v10, s[40:43], s95 offen offset:512 sc1\n"
"s_waitcnt vmcnt(0)\n"
"v_cmp_ne_u64 vcc, s[48:49], v[122:123]\n"
"s_cbranch_vccz .LJ_5px\n"
"s_sleep 1\n"
"s_sub_u32 s59, s59, 1\n"
"s_cbranch_scc0 .LJ_5p\n"
"s_branch .LJ_end\n"
".LJ_5px:\n"
"s_branch .LJ_5r\n"
".LJ_w1s:\n"
"s_nop 7\n"
"s_mov_b64 s[72:73], s[64:65]\n"
"s_mov_b32 s74, s66\n"
"v_mov_b32 v15, v16\n"
"s_mov_b32 s76, 0x7fffffff\n"
"s_mov_b32 s77, 4\n"
"s_mov_b32 s78, 0xff400000\n"
"s_mov_b32 s79, 0xff3f8800\n"
"s_mov_b32 s80, 1024\n"
"s_branch .LJ_w1r\n"
".LJ_end:\n"
"s_cmp_lt_u32 s50, s53\n"
"s_cbranch_scc1 .LJ_ne\n"
"s_cmp_eq_u64 s[68:69], 0\n"
"s_cbranch_scc1 .LJ_ne\n"
"v_mov_b32 v7, 0\n"
"global_store_dwordx2 v7, v[0:1], s[68:69] sc1\n"
".LJ_ne:\n"
"s_waitcnt vmcnt(0) lgkmcnt(0)\n"
"s_endpgm\n"
      :: "{v2}"((unsigned)(lds0 + ((wid + 3) & 3) * 18592 + trp)), "{v3}"((unsigned)(lds0 + ((wid + 2) & 3) * 18592 + trp)), "{v4}"((unsigned)(lds0 + ((wid + 1) & 3) * 18592 + trp)), "{v5}"((unsigned)(lane * 16 + f0 * 1024 + 98304)), "{v6}"((unsigned)(lane * 16 + f0 * 1024 + 102400)), "{v7}"((unsigned)((f0 + lane % 7) * 8 + 353024 + J_GL * 1572864)), "{v8}"((unsigned)(lane * 16 + pair * 4096 + kq * 1024 + 212992)), "{v9}"((unsigned)(lane ? DROP : pair * 32 + kq * 8 + 354048)), "{v10}"((unsigned)(v16 * 32 + (lane & 3) * 8 + 354048)), "{v11}"((unsigned)(lane * 16 + v16 * 4096 + 212992)), "{v12}"((unsigned)(lane * 16 + v16 * 1024 + 65536)), "{v13}"((unsigned)(lane * 16 + v16 * 4096 + 278528)), "{v14}"((unsigned)(lane * 16 + v16 * 1024 + 81920)), "{v15}"((unsigned)(lane * 16 + v16 * 1024 + 0)), "{v16}"((unsigned)(pi * 4096 + lg * 16 + v16 * 64)), "{v17}"((unsigned)(lg == (li >> 2) ? v16 * 128 + li * 8 + 67457024 : DROP)), "{v18}"((unsigned)(lane ? DROP : v16 * 8 + 67463936)), "{v20}"((unsigned)(bia0[0])), "{v21}"((unsigned)(bia0[1])), "{v22}"((unsigned)(bia0[2])), "{v23}"((unsigned)(bia0[3])), "{v24}"((unsigned)(bia1[0])), "{v25}"((unsigned)(bia1[1])), "{v26}"((unsigned)(bia1[2])), "{v27}"((unsigned)(bia1[3])), "{v28}"((unsigned)(0x3f803f80)), "{v29}"((unsigned)(0x3f803f80)), "{v30}"((unsigned)(0x3f803f80)), "{v31}"((unsigned)(0x3f803f80)), "{v32}"((unsigned)(sel0)), "{v33}"((unsigned)(sel1)), "{v34}"((unsigned)(sel0)), "{v35}"((unsigned)(sel1)), "{v36}"((unsigned)(vT + tsw)), "{v37}"((unsigned)(vT + (tsw ^ 8))), "{v38}"((unsigned)(vD)), "{s[40:43]}"(rS), "{s[44:47]}"(rW), "{s[48:49]}"(wsp), "{s50}"((unsigned)(sA)), "{s51}"((unsigned)(sLo)), "{s52}"((unsigned)(sHi)), "{s53}"((unsigned)(sEx)), "{s54}"((unsigned)(sEnt)), "{s55}"((unsigned)(sLd)), "{s56}"((unsigned)(sA & 0xfbffffff)), "{s57}"((unsigned)(lds0 + wid * 18592)), "{s[60:61]}"(0xAAAAAAAAAAAAAAAAull), "{s[62:63]}"(0xCCCCCCCCCCCCCCCCull), "{s[64:67]}"(rO), "{s[68:69]}"(ep), "{s[72:75]}"(rS), "{s76}"((unsigned)(201326592)), "{s77}"((unsigned)(0)), "{s78}"((unsigned)(0)), "{s79}"((unsigned)(0)), "{s80}"((unsigned)(16384)), "{s81}"((unsigned)(sW0 + 64 * ((wid + 1) & 3))), "{s82}"((unsigned)(sW0 + 64 * ((wid + 2) & 3))), "{s83}"((unsigned)(sW0 + 64 * ((wid + 3) & 3))), "{s84}"((unsigned)(sW0 + 64 * ((wid + 4) & 3)))
      : "v255", "a255", "s95", "vcc", "scc", "memory");

    }
  }
}

extern "C" void kernel_launch(void* const* d_in, const int* in_sizes, int n_in, void* d_out, int out_size,
                              void* d_ws, size_t ws_size, hipStream_t stream) {
  Args m{};
  m.x = (const u16*)d_in[0]; m.wqkv = (const u16*)d_in[1]; m.wo = (const u16*)d_in[2]; m.w1 = (const u16*)d_in[4];
  m.b1 = (const u16*)d_in[5]; m.w2 = (const u16*)d_in[6]; m.b2 = (const u16*)d_in[7];
  m.out = (u16*)d_out; m.ws = (char*)d_ws;
  (void)hipFuncSetAttribute((const void*)stream_kernel, hipFuncAttributeMaxDynamicSharedMemorySize, LDS_BYTES);
  hipLaunchKernelGGL(stream_kernel, dim3(256), dim3(256), LDS_BYTES, stream, m);
}
```

```cpp
#include <hip/hip_runtime.h>
#include <stdint.h>
typedef unsigned short u16;
typedef unsigned long long u64;
typedef __attribute__((ext_vector_type(4))) unsigned u32x4;
constexpr unsigned F_X = 0;
constexpr unsigned F_ATT = 32768;
constexpr unsigned F_X1 = 65536;
constexpr unsigned F_H = 98304;
constexpr unsigned F_P = 212992;
constexpr unsigned F_ST1 = 344064;
constexpr unsigned F_ST2 = 348160;
constexpr unsigned F_G = 352256;
constexpr unsigned F_FA = 352512;
constexpr unsigned F_FX1 = 352768;
constexpr unsigned F_FH = 353024;
constexpr unsigned F_FP = 354048;
constexpr unsigned F_FX = 355072;
constexpr unsigned RS = 524288;
constexpr unsigned PADF = 256;
constexpr unsigned LOWB = 64;
constexpr unsigned L_TAB = 98304;
constexpr unsigned LDS_BYTES = 163840;
constexpr int Q_GL = 2;
constexpr int Q_EX = 6;
constexpr int O_GL = 1;
constexpr int O_EX = 3;
constexpr int F_GL = 1;
constexpr int F_EX = 4;
constexpr int G_GL = 1;
constexpr int G_EX = 3;
constexpr int H_GL = 1;
constexpr int H_EX = 3;
constexpr int J_GL = 1;
constexpr int J_EX = 8;
#define MAPV 1
#define FDLY 0
#define PF 10
#define KR 1
#define XP 2
#define OR 1
#define JJ 1
#define DLO 0
#define DLF 0
#define DLG 0

constexpr unsigned DROP = 0x40000000u, WS0 = 1u << 20;
static_assert(WS0 + (PADF + 256 - LOWB) * RS <= (256u << 20), "workspace");
struct Args { const u16 *x, *wqkv, *wo, *w1, *b1, *w2, *b2; u16* out; char* ws;
};
__device__ __forceinline__ u32x4 desc(const void* p, unsigned n) { const u64 a = (u64)p; return (u32x4){(unsigned)a, (unsigned)(a >> 32) & 0xffffu, n, 0x00020000u}; }
__device__ __forceinline__ float lo16(unsigned v) { return __builtin_bit_cast(float, v << 16); }
__device__ __forceinline__ float hi16(unsigned v) { return __builtin_bit_cast(float, v & 0xffff0000u); }

__host__ __device__ inline void decode(int bid, int& role, int& set, int& idx) {
  const int xg = bid & 7, xj = bid >> 3;
  if (xg < 4) {
    if (xj < 16) { role = 0; set = xj & 1; idx = MAPV == 1 ? 8 * xg + (xj >> 1) : MAPV == 2 ? 4 * (xj >> 1) + xg : 4 * (xj >> 1) + ((xg + 2 * set) & 3); }
    else { role = 2; set = xj < 23 ? 1 : xj < 30 ? 2 : 0; idx = 7 * xg + (xj - 16) % 7; }
  } else if (xg == 4) {
    if (xj >= 20) { role = 1; set = xj < 28 ? 2 : 1; idx = xj < 28 ? xj - 20 : xj - 24; }
    else { role = 2; set = 0; idx = 7 * (xj / 5) + 2 + xj % 5; }
  } else if (xj >= 28) { role = 1; set = xg == 7; idx = (xg == 6) * 4 + xj - 28; }
  else { role = 3; set = xg - 5; idx = xj; }
}

__global__ __launch_bounds__(256, 1) void stream_kernel(Args a) {
  extern __shared__ __attribute__((aligned(16))) char smem[];
  const int tid = threadIdx.x, lane = tid & 63, wid = __builtin_amdgcn_readfirstlane(tid >> 6), li = lane & 15, lg = lane >> 4;
  const unsigned lds0 = (unsigned)(size_t)smem;
  const int bp = (blockIdx.x + 2 * (XP - 1)) & 7, bid = (blockIdx.x & ~7) | ((bp & 1) << 2) | (bp >> 1);
  int role, set, idx;
  decode(bid, role, set, idx);
  const u64 wsp = (u64)a.ws, wsb = wsp + WS0 - LOWB * RS;
  const u32x4 rS = desc(a.ws + WS0 - LOWB * RS, (PADF + 256) * RS);
  const u64 tz = __builtin_amdgcn_s_memrealtime();
  const int pi = (li >> 2) + 4 * (li & 3);
  const int xj = bid >> 3, xo = (bid & 7) - 4;
  if (role == 2 && xo < 0 && (PF & 1)) {
    const int g = (xj - 16) * 4 + wid;
    const char* p = (const char*)a.wqkv + (((g >> 2) * 2 + 1) * 32 + (g & 3) * 8 + (lane >> 5)) * 6144 + (bid & 7) * 512 + (lane & 31) * 16;
    u32x4 t[12];
    unsigned fl = 0;
    if (PF & 4) fl = __hip_atomic_load((const unsigned*)(a.ws + WS0 + (size_t)(PADF - LOWB + g * 4 + (bid & 7)) * RS + F_ST2 + (lane < 56 ? lane : 0) * 128), __ATOMIC_RELAXED, __HIP_MEMORY_SCOPE_AGENT);
#pragma unroll
    for (int i = 0; i < 12; ++i) t[i] = *(const u32x4*)(p + (i & 3) * 12288 + (i >> 2) * 2048);
#pragma unroll
    for (int i = 0; i < 12; ++i) asm volatile("" ::"v"(t[i]));
    asm volatile("" ::"v"(fl));
  }
  if (xo < 0 && (PF & 8)) {
    const int g = xj * 4 + wid;
    const char* p = (const char*)a.wqkv + (g * 8 + (lane >> 5)) * 6144 + (bid & 7) * 512 + (lane & 31) * 16;
    u32x4 t[12];
#pragma unroll
    for (int i = 0; i < 12; ++i) t[i] = *(const u32x4*)(p + (i & 3) * 12288 + (i >> 2) * 2048);
    unsigned fl = 0;
    if (role == 2) fl = __hip_atomic_load((const unsigned*)(a.ws + WS0 + (size_t)(PADF - LOWB + (g - 64) * 4 + (bid & 7)) * RS + F_ST2 + (lane < 56 ? lane : 0) * 128), __ATOMIC_RELAXED, __HIP_MEMORY_SCOPE_AGENT);
#pragma unroll
    for (int i = 0; i < 12; ++i) asm volatile("" ::"v"(t[i]));
    asm volatile("" ::"v"(fl));
  }
  if (role == 2 && xo < 0) for (int i = 0; i < FDLY; ++i) __builtin_amdgcn_s_sleep(16);
  for (int pr = role < 2 || xo < 0 ? 128 : xj < 20 ? xj * 4 + xo : xj * 3 + 19 + xo; pr < 128; pr += pr < 80 ? 128 : 24) {
    const __amdgpu_buffer_rsrc_t rb = __builtin_amdgcn_make_buffer_rsrc(a.ws + WS0 - LOWB * RS, 0, (PADF + 256) * RS, 0x00020000);
    const u32x4* src = (const u32x4*)(a.x + ((pr >> 1) * 32 + 2 * pi + (pr & 1)) * 1024 + wid * 256 + lg * 8);
    const u64 ev = __hip_atomic_load((const u64*)a.ws, __ATOMIC_RELAXED, __HIP_MEMORY_SCOPE_AGENT);
    u32x4 v[8];
#pragma unroll
    for (int k = 0; k < 8; ++k) v[k] = src[k * 4];
    u32x4 pf = {};
    if (pr < 104 && (PF & 2)) {
      const int g = xj * 4 + wid, n = xo ? 7 : 20;
      for (int i = 0; i < n; ++i) {
        const int u = g * n + i, r0 = xo ? u : u >> 1, row = r0 < 768 ? (r0 / 192) * 256 + 64 + r0 % 192 : 0;
        asm volatile("global_load_dwordx4 %0, %1, off" : "+v"(pf) : "v"((const char*)a.wo + row * 2048 + (xo ? xo == 2 : u & 1) * 1024 + lane * 16));
      }
    }
    float s = 0.f, q = 0.f;
#pragma unroll
    for (int k = 0; k < 8; ++k) {
      __builtin_amdgcn_raw_buffer_store_b128(v[k], rb, lane * 16 + k * 1024, (PADF + pr) * RS + F_X + wid * 8192, 16);
#pragma unroll
      for (int d = 0; d < 4; ++d) { const float l = lo16(v[k][d]), h = hi16(v[k][d]); s += l + h; q = fmaf(l, l, fmaf(h, h, q)); }
    }
    s += __shfl_xor(s, 16); q += __shfl_xor(q, 16); s += __shfl_xor(s, 32); q += __shfl_xor(q, 32);
    float2* t = (float2*)smem;
    if (lg == 0) t[wid * 16 + li] = make_float2(s, q);
    asm volatile("s_waitcnt vmcnt(0)" : "+v"(pf)::"memory");
    __syncthreads();
    if (tid < 16) {
      const float2 t0 = t[li], t1 = t[16 + li], t2 = t[32 + li], t3 = t[48 + li];
      const float nm = ((t0.x + t1.x) + (t2.x + t3.x)) * -0x1p-10f;
      const float var = fmaxf(fmaf(-nm, nm, fmaf((t0.y + t1.y) + (t2.y + t3.y), 0x1p-10f, 1e-5f)), 1e-5f);
      const u32x4 g = {__builtin_bit_cast(unsigned, __builtin_amdgcn_rsqf(var)), __builtin_bit_cast(unsigned, nm), ((unsigned)ev + 2u) | 1u, (unsigned)(ev >> 32) + 0x9E3779B9u};
      __builtin_amdgcn_raw_buffer_store_b128(g, rb, li * 16, (PADF + pr) * RS + F_G, 16);
    }
    __syncthreads();
  }
  const unsigned sLo = (PADF + set) * RS, sLd = lds0 + wid * 40960, vT = sLd + (8 * lg + (li >> 2)) * 64 + (li & 3) * 16;
  const unsigned trp = (li & 3) * 4608 + (li & 1) * 32 + (li & 2) * 64 + (li >> 2) * 64 + lg * 16;
  if (role == 0) {
    const unsigned sA = sLo - 2 * RS, sHi = sLo + 127 * 2 * RS, sEx = sHi + Q_EX * 2 * RS, sEnt = -wid & 3, tsw = 0, rot = KR * (set + 2 * (idx & 1));
    const unsigned sW0 = wid * 256 * 6144 + idx * 64, vD = (lane >> 2) * 6144 + (lane & 3) * 16;
    const u32x4 rW = desc(a.wqkv, 1024 * 6144);
    auto ok = [&](int r, int prev) { const int k = lg + 4 * r; return prev ? k >= pi + 9 : (k <= pi && k >= pi - 7); };
    auto qmask = [&](int r, int prev) { return ok(r, prev) ? 0u : 0xff800000u; };
    auto pmask = [&](int d) { return (ok(2 * (d & 1), d >> 1) ? 0xffffu : 0u) | (ok(2 * (d & 1) + 1, d >> 1) ? 0xffff0000u : 0u); };
    asm volatile(
"s_nop 4\n"
"v_mov_b32 v186, 0\n"
"global_load_dwordx2 v[0:1], v186, s[48:49] sc1\n"
"v_mov_b32 v188, 0x3f803f80\n"
"v_mov_b32 v189, 0x3f803f80\n"
"v_mov_b32 v190, 0x3f803f80\n"
"v_mov_b32 v191, 0x3f803f80\n"
"s_add_u32 m0, s55, 0\n"
"s_add_u32 s60, s63, 0\n"
"buffer_load_dwordx4 v50, s[44:47], s60 offen lds\n"
"s_add_u32 m0, s55, 1024\n"
"s_add_u32 s60, s63, 98304\n"
"buffer_load_dwordx4 v50, s[44:47], s60 offen lds\n"
"s_add_u32 m0, s55, 2048\n"
"s_add_u32 s60, s64, 0\n"
"buffer_load_dwordx4 v50, s[44:47], s60 offen lds\n"
"s_add_u32 m0, s55, 3072\n"
"s_add_u32 s60, s64, 98304\n"
"buffer_load_dwordx4 v50, s[44:47], s60 offen lds\n"
"s_add_u32 m0, s55, 4096\n"
"s_add_u32 s60, s65, 0\n"
"buffer_load_dwordx4 v50, s[44:47], s60 offen lds\n"
"s_add_u32 m0, s55, 5120\n"
"s_add_u32 s60, s65, 98304\n"
"buffer_load_dwordx4 v50, s[44:47], s60 offen lds\n"
"s_add_u32 m0, s55, 6144\n"
"s_add_u32 s60, s63, 196608\n"
"buffer_load_dwordx4 v50, s[44:47], s60 offen lds\n"
"s_add_u32 m0, s55, 7168\n"
"s_add_u32 s60, s63, 294912\n"
"buffer_load_dwordx4 v50, s[44:47], s60 offen lds\n"
"s_add_u32 m0, s55, 8192\n"
"s_add_u32 s60, s64, 196608\n"
"buffer_load_dwordx4 v50, s[44:47], s60 offen lds\n"
"s_add_u32 m0, s55, 9216\n"
"s_add_u32 s60, s64, 294912\n"
"buffer_load_dwordx4 v50, s[44:47], s60 offen lds\n"
"s_add_u32 m0, s55, 10240\n"
"s_add_u32 s60, s65, 196608\n"
"buffer_load_dwordx4 v50, s[44:47], s60 offen lds\n"
"s_add_u32 m0, s55, 11264\n"
"s_add_u32 s60, s65, 294912\n"
"buffer_load_dwordx4 v50, s[44:47], s60 offen lds\n"
"s_add_u32 m0, s55, 12288\n"
"s_add_u32 s60, s63, 0\n"
"buffer_load_dwordx4 v51, s[44:47], s60 offen lds\n"
"s_add_u32 m0, s55, 13312\n"
"s_add_u32 s60, s63, 98304\n"
"buffer_load_dwordx4 v51, s[44:47], s60 offen lds\n"
"s_add_u32 m0, s55, 14336\n"
"s_add_u32 s60, s64, 0\n"
"buffer_load_dwordx4 v51, s[44:47], s60 offen lds\n"
"s_add_u32 m0, s55, 15360\n"
"s_add_u32 s60, s64, 98304\n"
"buffer_load_dwordx4 v51, s[44:47], s60 offen lds\n"
"s_add_u32 m0, s55, 16384\n"
"s_add_u32 s60, s65, 0\n"
"buffer_load_dwordx4 v51, s[44:47], s60 offen lds\n"
"s_add_u32 m0, s55, 17408\n"
"s_add_u32 s60, s65, 98304\n"
"buffer_load_dwordx4 v51, s[44:47], s60 offen lds\n"
"s_add_u32 m0, s55, 18432\n"
"s_add_u32 s60, s63, 196608\n"
"buffer_load_dwordx4 v51, s[44:47], s60 offen lds\n"
"s_add_u32 m0, s55, 19456\n"
"s_add_u32 s60, s63, 294912\n"
"buffer_load_dwordx4 v51, s[44:47], s60 offen lds\n"
"s_add_u32 m0, s55, 20480\n"
"s_add_u32 s60, s64, 196608\n"
"buffer_load_dwordx4 v51, s[44:47], s60 offen lds\n"
"s_add_u32 m0, s55, 21504\n"
"s_add_u32 s60, s64, 294912\n"
"buffer_load_dwordx4 v51, s[44:47], s60 offen lds\n"
"s_add_u32 m0, s55, 22528\n"
"s_add_u32 s60, s65, 196608\n"
"buffer_load_dwordx4 v51, s[44:47], s60 offen lds\n"
"s_add_u32 m0, s55, 23552\n"
"s_add_u32 s60, s65, 294912\n"
"buffer_load_dwordx4 v51, s[44:47], s60 offen lds\n"
"s_add_u32 m0, s55, 24576\n"
"s_add_u32 s60, s63, 0\n"
"buffer_load_dwordx4 v52, s[44:47], s60 offen lds\n"
"s_add_u32 m0, s55, 25600\n"
"s_add_u32 s60, s63, 98304\n"
"buffer_load_dwordx4 v52, s[44:47], s60 offen lds\n"
"s_add_u32 m0, s55, 26624\n"
"s_add_u32 s60, s64, 0\n"
"buffer_load_dwordx4 v52, s[44:47], s60 offen lds\n"
"s_add_u32 m0, s55, 27648\n"
"s_add_u32 s60, s64, 98304\n"
"buffer_load_dwordx4 v52, s[44:47], s60 offen lds\n"
"s_add_u32 m0, s55, 28672\n"
"s_add_u32 s60, s65, 0\n"
"buffer_load_dwordx4 v52, s[44:47], s60 offen lds\n"
"s_add_u32 m0, s55, 29696\n"
"s_add_u32 s60, s65, 98304\n"
"buffer_load_dwordx4 v52, s[44:47], s60 offen lds\n"
"s_add_u32 m0, s55, 30720\n"
"s_add_u32 s60, s63, 196608\n"
"buffer_load_dwordx4 v52, s[44:47], s60 offen lds\n"
"s_add_u32 m0, s55, 31744\n"
"s_add_u32 s60, s63, 294912\n"
"buffer_load_dwordx4 v52, s[44:47], s60 offen lds\n"
"s_add_u32 m0, s55, 32768\n"
"s_add_u32 s60, s64, 196608\n"
"buffer_load_dwordx4 v52, s[44:47], s60 offen lds\n"
"s_add_u32 m0, s55, 33792\n"
"s_add_u32 s60, s64, 294912\n"
"buffer_load_dwordx4 v52, s[44:47], s60 offen lds\n"
"s_add_u32 m0, s55, 34816\n"
"s_add_u32 s60, s65, 196608\n"
"buffer_load_dwordx4 v52, s[44:47], s60 offen lds\n"
"s_add_u32 m0, s55, 35840\n"
"s_add_u32 s60, s65, 294912\n"
"buffer_load_dwordx4 v52, s[44:47], s60 offen lds\n"
"s_add_u32 m0, s55, 36864\n"
"s_add_u32 s60, s63, 0\n"
"buffer_load_dwordx4 v53, s[44:47], s60 offen lds\n"
"s_add_u32 m0, s55, 37888\n"
"s_add_u32 s60, s63, 98304\n"
"buffer_load_dwordx4 v53, s[44:47], s60 offen lds\n"
"s_add_u32 m0, s55, 38912\n"
"s_add_u32 s60, s64, 0\n"
"buffer_load_dwordx4 v53, s[44:47], s60 offen lds\n"
"s_add_u32 m0, s55, 39936\n"
"s_add_u32 s60, s64, 98304\n"
"buffer_load_dwordx4 v53, s[44:47], s60 offen lds\n"
"s_waitcnt vmcnt(38)\n"
"ds_read_b64_tr_b16 a[0:1], v48 offset:0\n"
"ds_read_b64_tr_b16 a[2:3], v48 offset:256\n"
"ds_read_b64_tr_b16 a[32:33], v49 offset:0\n"
"ds_read_b64_tr_b16 a[34:35], v49 offset:256\n"
"v_add_u32 v0, 2, v0\n"
"v_add_u32 v1, 0x9e3779b9, v1\n"
"v_or_b32 v0, 1, v0\n"
"v_readfirstlane_b32 s49, v1\n"
"v_readfirstlane_b32 s48, v0\n"
"s_waitcnt vmcnt(36)\n"
"ds_read_b64_tr_b16 a[64:65], v48 offset:2048\n"
"ds_read_b64_tr_b16 a[66:67], v48 offset:2304\n"
"ds_read_b64_tr_b16 a[96:97], v49 offset:2048\n"
"ds_read_b64_tr_b16 a[98:99], v49 offset:2304\n"
"s_waitcnt lgkmcnt(4)\n"
"s_add_u32 m0, s55, 0\n"
"s_add_u32 s60, s65, 0\n"
"buffer_load_dwordx4 v53, s[44:47], s60 offen lds\n"
"s_add_u32 m0, s55, 1024\n"
"s_add_u32 s60, s65, 98304\n"
"buffer_load_dwordx4 v53, s[44:47], s60 offen lds\n"
"v_mfma_f32_16x16x32_bf16 v[192:195], v[188:191], a[0:3], 0\n"
"v_mfma_f32_16x16x32_bf16 v[196:199], v[188:191], a[32:35], 0\n"
"s_waitcnt vmcnt(36)\n"
"ds_read_b64_tr_b16 a[128:129], v48 offset:4096\n"
"ds_read_b64_tr_b16 a[130:131], v48 offset:4352\n"
"ds_read_b64_tr_b16 a[160:161], v49 offset:4096\n"
"ds_read_b64_tr_b16 a[162:163], v49 offset:4352\n"
"s_waitcnt lgkmcnt(4)\n"
"s_add_u32 m0, s55, 2048\n"
"s_add_u32 s60, s63, 196608\n"
"buffer_load_dwordx4 v53, s[44:47], s60 offen lds\n"
"s_add_u32 m0, s55, 3072\n"
"s_add_u32 s60, s63, 294912\n"
"buffer_load_dwordx4 v53, s[44:47], s60 offen lds\n"
"v_mfma_f32_16x16x32_bf16 v[200:203], v[188:191], a[64:67], 0\n"
"v_mfma_f32_16x16x32_bf16 v[204:207], v[188:191], a[96:99], 0\n"
"s_waitcnt vmcnt(36)\n"
"ds_read_b64_tr_b16 a[4:5], v48 offset:6144\n"
"ds_read_b64_tr_b16 a[6:7], v48 offset:6400\n"
"ds_read_b64_tr_b16 a[36:37], v49 offset:6144\n"
"ds_read_b64_tr_b16 a[38:39], v49 offset:6400\n"
"s_waitcnt lgkmcnt(4)\n"
"s_add_u32 m0, s55, 4096\n"
"s_add_u32 s60, s64, 196608\n"
"buffer_load_dwordx4 v53, s[44:47], s60 offen lds\n"
"s_add_u32 m0, s55, 5120\n"
"s_add_u32 s60, s64, 294912\n"
"buffer_load_dwordx4 v53, s[44:47], s60 offen lds\n"
"v_mfma_f32_16x16x32_bf16 v[208:211], a[128:131], v[188:191], 0\n"
"v_mfma_f32_16x16x32_bf16 v[212:215], a[160:163], v[188:191], 0\n"
"s_waitcnt vmcnt(36)\n"
"ds_read_b64_tr_b16 a[68:69], v48 offset:8192\n"
"ds_read_b64_tr_b16 a[70:71], v48 offset:8448\n"
"ds_read_b64_tr_b16 a[100:101], v49 offset:8192\n"
"ds_read_b64_tr_b16 a[102:103], v49 offset:8448\n"
"s_waitcnt lgkmcnt(4)\n"
"s_add_u32 m0, s55, 6144\n"
"s_add_u32 s60, s65, 196608\n"
"buffer_load_dwordx4 v53, s[44:47], s60 offen lds\n"
"s_add_u32 m0, s55, 7168\n"
"s_add_u32 s60, s65, 294912\n"
"buffer_load_dwordx4 v53, s[44:47], s60 offen lds\n"
"v_mfma_f32_16x16x32_bf16 v[192:195], v[188:191], a[4:7], v[192:195]\n"
"v_mfma_f32_16x16x32_bf16 v[196:199], v[188:191], a[36:39], v[196:199]\n"
"s_waitcnt vmcnt(36)\n"
"ds_read_b64_tr_b16 a[132:133], v48 offset:10240\n"
"ds_read_b64_tr_b16 a[134:135], v48 offset:10496\n"
"ds_read_b64_tr_b16 a[164:165], v49 offset:10240\n"
"ds_read_b64_tr_b16 a[166:167], v49 offset:10496\n"
"s_waitcnt lgkmcnt(4)\n"
"v_mfma_f32_16x16x32_bf16 v[200:203], v[188:191], a[68:71], v[200:203]\n"
"v_mfma_f32_16x16x32_bf16 v[204:207], v[188:191], a[100:103], v[204:207]\n"
"s_waitcnt vmcnt(34)\n"
"ds_read_b64_tr_b16 a[8:9], v48 offset:12288\n"
"ds_read_b64_tr_b16 a[10:11], v48 offset:12544\n"
"ds_read_b64_tr_b16 a[40:41], v49 offset:12288\n"
"ds_read_b64_tr_b16 a[42:43], v49 offset:12544\n"
"s_waitcnt lgkmcnt(4)\n"
"v_mfma_f32_16x16x32_bf16 v[208:211], a[132:135], v[188:191], v[208:211]\n"
"v_mfma_f32_16x16x32_bf16 v[212:215], a[164:167], v[188:191], v[212:215]\n"
"s_waitcnt vmcnt(32)\n"
"ds_read_b64_tr_b16 a[72:73], v48 offset:14336\n"
"ds_read_b64_tr_b16 a[74:75], v48 offset:14592\n"
"ds_read_b64_tr_b16 a[104:105], v49 offset:14336\n"
"ds_read_b64_tr_b16 a[106:107], v49 offset:14592\n"
"s_waitcnt lgkmcnt(4)\n"
"v_mfma_f32_16x16x32_bf16 v[192:195], v[188:191], a[8:11], v[192:195]\n"
"v_mfma_f32_16x16x32_bf16 v[196:199], v[188:191], a[40:43], v[196:199]\n"
"s_waitcnt vmcnt(30)\n"
"ds_read_b64_tr_b16 a[136:137], v48 offset:16384\n"
"ds_read_b64_tr_b16 a[138:139], v48 offset:16640\n"
"ds_read_b64_tr_b16 a[168:169], v49 offset:16384\n"
"ds_read_b64_tr_b16 a[170:171], v49 offset:16640\n"
"s_waitcnt lgkmcnt(4)\n"
"v_mfma_f32_16x16x32_bf16 v[200:203], v[188:191], a[72:75], v[200:203]\n"
"v_mfma_f32_16x16x32_bf16 v[204:207], v[188:191], a[104:107], v[204:207]\n"
"s_waitcnt vmcnt(28)\n"
"ds_read_b64_tr_b16 a[12:13], v48 offset:18432\n"
"ds_read_b64_tr_b16 a[14:15], v48 offset:18688\n"
"ds_read_b64_tr_b16 a[44:45], v49 offset:18432\n"
"ds_read_b64_tr_b16 a[46:47], v49 offset:18688\n"
"s_waitcnt lgkmcnt(4)\n"
"v_mfma_f32_16x16x32_bf16 v[208:211], a[136:139], v[188:191], v[208:211]\n"
"v_mfma_f32_16x16x32_bf16 v[212:215], a[168:171], v[188:191], v[212:215]\n"
"s_waitcnt vmcnt(26)\n"
"ds_read_b64_tr_b16 a[76:77], v48 offset:20480\n"
"ds_read_b64_tr_b16 a[78:79], v48 offset:20736\n"
"ds_read_b64_tr_b16 a[108:109], v49 offset:20480\n"
"ds_read_b64_tr_b16 a[110:111], v49 offset:20736\n"
"s_waitcnt lgkmcnt(4)\n"
"v_mfma_f32_16x16x32_bf16 v[192:195], v[188:191], a[12:15], v[192:195]\n"
"v_mfma_f32_16x16x32_bf16 v[196:199], v[188:191], a[44:47], v[196:199]\n"
"s_waitcnt vmcnt(24)\n"
"ds_read_b64_tr_b16 a[140:141], v48 offset:22528\n"
"ds_read_b64_tr_b16 a[142:143], v48 offset:22784\n"
"ds_read_b64_tr_b16 a[172:173], v49 offset:22528\n"
"ds_read_b64_tr_b16 a[174:175], v49 offset:22784\n"
"s_waitcnt lgkmcnt(4)\n"
"v_mfma_f32_16x16x32_bf16 v[200:203], v[188:191], a[76:79], v[200:203]\n"
"v_mfma_f32_16x16x32_bf16 v[204:207], v[188:191], a[108:111], v[204:207]\n"
"s_waitcnt vmcnt(22)\n"
"ds_read_b64_tr_b16 a[16:17], v48 offset:24576\n"
"ds_read_b64_tr_b16 a[18:19], v48 offset:24832\n"
"ds_read_b64_tr_b16 a[48:49], v49 offset:24576\n"
"ds_read_b64_tr_b16 a[50:51], v49 offset:24832\n"
"s_waitcnt lgkmcnt(4)\n"
"v_mfma_f32_16x16x32_bf16 v[208:211], a[140:143], v[188:191], v[208:211]\n"
"v_mfma_f32_16x16x32_bf16 v[212:215], a[172:175], v[188:191], v[212:215]\n"
"s_waitcnt vmcnt(20)\n"
"ds_read_b64_tr_b16 a[80:81], v48 offset:26624\n"
"ds_read_b64_tr_b16 a[82:83], v48 offset:26880\n"
"ds_read_b64_tr_b16 a[112:113], v49 offset:26624\n"
"ds_read_b64_tr_b16 a[114:115], v49 offset:26880\n"
"s_waitcnt lgkmcnt(4)\n"
"v_mfma_f32_16x16x32_bf16 v[192:195], v[188:191], a[16:19], v[192:195]\n"
"v_mfma_f32_16x16x32_bf16 v[196:199], v[188:191], a[48:51], v[196:199]\n"
"s_waitcnt vmcnt(18)\n"
"ds_read_b64_tr_b16 a[144:145], v48 offset:28672\n"
"ds_read_b64_tr_b16 a[146:147], v48 offset:28928\n"
"ds_read_b64_tr_b16 a[176:177], v49 offset:28672\n"
"ds_read_b64_tr_b16 a[178:179], v49 offset:28928\n"
"s_waitcnt lgkmcnt(4)\n"
"v_mfma_f32_16x16x32_bf16 v[200:203], v[188:191], a[80:83], v[200:203]\n"
"v_mfma_f32_16x16x32_bf16 v[204:207], v[188:191], a[112:115], v[204:207]\n"
"s_waitcnt vmcnt(16)\n"
"ds_read_b64_tr_b16 a[20:21], v48 offset:30720\n"
"ds_read_b64_tr_b16 a[22:23], v48 offset:30976\n"
"ds_read_b64_tr_b16 a[52:53], v49 offset:30720\n"
"ds_read_b64_tr_b16 a[54:55], v49 offset:30976\n"
"s_waitcnt lgkmcnt(4)\n"
"v_mfma_f32_16x16x32_bf16 v[208:211], a[144:147], v[188:191], v[208:211]\n"
"v_mfma_f32_16x16x32_bf16 v[212:215], a[176:179], v[188:191], v[212:215]\n"
"s_waitcnt vmcnt(14)\n"
"ds_read_b64_tr_b16 a[84:85], v48 offset:32768\n"
"ds_read_b64_tr_b16 a[86:87], v48 offset:33024\n"
"ds_read_b64_tr_b16 a[116:117], v49 offset:32768\n"
"ds_read_b64_tr_b16 a[118:119], v49 offset:33024\n"
"s_waitcnt lgkmcnt(4)\n"
"v_mfma_f32_16x16x32_bf16 v[192:195], v[188:191], a[20:23], v[192:195]\n"
"v_mfma_f32_16x16x32_bf16 v[196:199], v[188:191], a[52:55], v[196:199]\n"
"s_waitcnt vmcnt(12)\n"
"ds_read_b64_tr_b16 a[148:149], v48 offset:34816\n"
"ds_read_b64_tr_b16 a[150:151], v48 offset:35072\n"
"ds_read_b64_tr_b16 a[180:181], v49 offset:34816\n"
"ds_read_b64_tr_b16 a[182:183], v49 offset:35072\n"
"s_waitcnt lgkmcnt(4)\n"
"v_mfma_f32_16x16x32_bf16 v[200:203], v[188:191], a[84:87], v[200:203]\n"
"v_mfma_f32_16x16x32_bf16 v[204:207], v[188:191], a[116:119], v[204:207]\n"
"s_waitcnt vmcnt(10)\n"
"ds_read_b64_tr_b16 a[24:25], v48 offset:36864\n"
"ds_read_b64_tr_b16 a[26:27], v48 offset:37120\n"
"ds_read_b64_tr_b16 a[56:57], v49 offset:36864\n"
"ds_read_b64_tr_b16 a[58:59], v49 offset:37120\n"
"s_waitcnt lgkmcnt(4)\n"
"v_mfma_f32_16x16x32_bf16 v[208:211], a[148:151], v[188:191], v[208:211]\n"
"v_mfma_f32_16x16x32_bf16 v[212:215], a[180:183], v[188:191], v[212:215]\n"
"s_waitcnt vmcnt(8)\n"
"ds_read_b64_tr_b16 a[88:89], v48 offset:38912\n"
"ds_read_b64_tr_b16 a[90:91], v48 offset:39168\n"
"ds_read_b64_tr_b16 a[120:121], v49 offset:38912\n"
"ds_read_b64_tr_b16 a[122:123], v49 offset:39168\n"
"s_waitcnt lgkmcnt(4)\n"
"v_mfma_f32_16x16x32_bf16 v[192:195], v[188:191], a[24:27], v[192:195]\n"
"v_mfma_f32_16x16x32_bf16 v[196:199], v[188:191], a[56:59], v[196:199]\n"
"s_waitcnt vmcnt(6)\n"
"ds_read_b64_tr_b16 a[152:153], v48 offset:0\n"
"ds_read_b64_tr_b16 a[154:155], v48 offset:256\n"
"ds_read_b64_tr_b16 a[184:185], v49 offset:0\n"
"ds_read_b64_tr_b16 a[186:187], v49 offset:256\n"
"s_waitcnt lgkmcnt(4)\n"
"v_mfma_f32_16x16x32_bf16 v[200:203], v[188:191], a[88:91], v[200:203]\n"
"v_mfma_f32_16x16x32_bf16 v[204:207], v[188:191], a[120:123], v[204:207]\n"
"s_waitcnt vmcnt(4)\n"
"ds_read_b64_tr_b16 a[28:29], v48 offset:2048\n"
"ds_read_b64_tr_b16 a[30:31], v48 offset:2304\n"
"ds_read_b64_tr_b16 a[60:61], v49 offset:2048\n"
"ds_read_b64_tr_b16 a[62:63], v49 offset:2304\n"
"s_waitcnt lgkmcnt(4)\n"
"v_mfma_f32_16x16x32_bf16 v[208:211], a[152:155], v[188:191], v[208:211]\n"
"v_mfma_f32_16x16x32_bf16 v[212:215], a[184:187], v[188:191], v[212:215]\n"
"s_waitcnt vmcnt(2)\n"
"ds_read_b64_tr_b16 a[92:93], v48 offset:4096\n"
"ds_read_b64_tr_b16 a[94:95], v48 offset:4352\n"
"ds_read_b64_tr_b16 a[124:125], v49 offset:4096\n"
"ds_read_b64_tr_b16 a[126:127], v49 offset:4352\n"
"s_waitcnt lgkmcnt(4)\n"
"v_mfma_f32_16x16x32_bf16 v[192:195], v[188:191], a[28:31], v[192:195]\n"
"v_mfma_f32_16x16x32_bf16 v[196:199], v[188:191], a[60:63], v[196:199]\n"
"s_waitcnt vmcnt(0)\n"
"ds_read_b64_tr_b16 a[156:157], v48 offset:6144\n"
"ds_read_b64_tr_b16 a[158:159], v48 offset:6400\n"
"ds_read_b64_tr_b16 a[188:189], v49 offset:6144\n"
"ds_read_b64_tr_b16 a[190:191], v49 offset:6400\n"
"s_waitcnt lgkmcnt(4)\n"
"v_mfma_f32_16x16x32_bf16 v[200:203], v[188:191], a[92:95], v[200:203]\n"
"v_mfma_f32_16x16x32_bf16 v[204:207], v[188:191], a[124:127], v[204:207]\n"
"s_waitcnt lgkmcnt(0)\n"
"v_mfma_f32_16x16x32_bf16 v[208:211], a[156:159], v[188:191], v[208:211]\n"
"v_mfma_f32_16x16x32_bf16 v[212:215], a[188:191], v[188:191], v[212:215]\n"
"s_mov_b32 m0, s59\n"
"s_barrier\n"
"ds_write_addtid_b32 v192 offset:0\n"
"ds_write_addtid_b32 v193 offset:4640\n"
"ds_write_addtid_b32 v194 offset:9344\n"
"ds_write_addtid_b32 v195 offset:13984\n"
"ds_write_addtid_b32 v196 offset:256\n"
"ds_write_addtid_b32 v197 offset:4896\n"
"ds_write_addtid_b32 v198 offset:9600\n"
"ds_write_addtid_b32 v199 offset:14240\n"
"ds_write_addtid_b32 v200 offset:512\n"
"ds_write_addtid_b32 v201 offset:5152\n"
"ds_write_addtid_b32 v202 offset:9856\n"
"ds_write_addtid_b32 v203 offset:14496\n"
"ds_write_addtid_b32 v204 offset:768\n"
"ds_write_addtid_b32 v205 offset:5408\n"
"ds_write_addtid_b32 v206 offset:10112\n"
"ds_write_addtid_b32 v207 offset:14752\n"
"ds_write_addtid_b32 v208 offset:1024\n"
"ds_write_addtid_b32 v209 offset:5664\n"
"ds_write_addtid_b32 v210 offset:10368\n"
"ds_write_addtid_b32 v211 offset:15008\n"
"ds_write_addtid_b32 v212 offset:1280\n"
"ds_write_addtid_b32 v213 offset:5920\n"
"ds_write_addtid_b32 v214 offset:10624\n"
"ds_write_addtid_b32 v215 offset:15264\n"
"s_waitcnt lgkmcnt(0)\n"
"s_barrier\n"
"ds_read_b128 v[96:99], v2 offset:0\n"
"ds_read_b128 v[100:103], v3 offset:0\n"
"ds_read_b128 v[104:107], v4 offset:0\n"
"ds_read_b128 v[108:111], v54 offset:0\n"
"ds_read_b128 v[112:115], v2 offset:256\n"
"ds_read_b128 v[116:119], v3 offset:256\n"
"ds_read_b128 v[120:123], v4 offset:256\n"
"ds_read_b128 v[124:127], v54 offset:256\n"
"ds_read_b128 v[128:131], v2 offset:512\n"
"ds_read_b128 v[132:135], v3 offset:512\n"
"ds_read_b128 v[136:139], v4 offset:512\n"
"ds_read_b128 v[140:143], v54 offset:512\n"
"ds_read_b128 v[144:147], v2 offset:768\n"
"ds_read_b128 v[148:151], v3 offset:768\n"
"ds_read_b128 v[152:155], v4 offset:768\n"
"ds_read_b128 v[156:159], v54 offset:768\n"
"ds_read_b128 v[160:163], v2 offset:1024\n"
"ds_read_b128 v[164:167], v3 offset:1024\n"
"ds_read_b128 v[168:171], v4 offset:1024\n"
"ds_read_b128 v[172:175], v54 offset:1024\n"
"ds_read_b128 v[176:179], v2 offset:1280\n"
"ds_read_b128 v[180:183], v3 offset:1280\n"
"ds_read_b128 v[184:187], v4 offset:1280\n"
"ds_read_b128 v[188:191], v54 offset:1280\n"
"s_waitcnt lgkmcnt(0)\n"
"s_barrier\n"
"v_add_f32 v96, v96, v100\n"
"v_add_f32 v104, v104, v108\n"
"v_add_f32 v28, v96, v104\n"
"v_add_f32 v97, v97, v101\n"
"v_add_f32 v105, v105, v109\n"
"v_add_f32 v29, v97, v105\n"
"v_add_f32 v98, v98, v102\n"
"v_add_f32 v106, v106, v110\n"
"v_add_f32 v30, v98, v106\n"
"v_add_f32 v99, v99, v103\n"
"v_add_f32 v107, v107, v111\n"
"v_add_f32 v31, v99, v107\n"
"v_add_f32 v112, v112, v116\n"
"v_add_f32 v120, v120, v124\n"
"v_add_f32 v32, v112, v120\n"
"v_add_f32 v113, v113, v117\n"
"v_add_f32 v121, v121, v125\n"
"v_add_f32 v33, v113, v121\n"
"v_add_f32 v114, v114, v118\n"
"v_add_f32 v122, v122, v126\n"
"v_add_f32 v34, v114, v122\n"
"v_add_f32 v115, v115, v119\n"
"v_add_f32 v123, v123, v127\n"
"v_add_f32 v35, v115, v123\n"
"v_add_f32 v128, v128, v132\n"
"v_add_f32 v136, v136, v140\n"
"v_add_f32 v36, v128, v136\n"
"v_add_f32 v129, v129, v133\n"
"v_add_f32 v137, v137, v141\n"
"v_add_f32 v37, v129, v137\n"
"v_add_f32 v130, v130, v134\n"
"v_add_f32 v138, v138, v142\n"
"v_add_f32 v38, v130, v138\n"
"v_add_f32 v131, v131, v135\n"
"v_add_f32 v139, v139, v143\n"
"v_add_f32 v39, v131, v139\n"
"v_add_f32 v144, v144, v148\n"
"v_add_f32 v152, v152, v156\n"
"v_add_f32 v40, v144, v152\n"
"v_add_f32 v145, v145, v149\n"
"v_add_f32 v153, v153, v157\n"
"v_add_f32 v41, v145, v153\n"
"v_add_f32 v146, v146, v150\n"
"v_add_f32 v154, v154, v158\n"
"v_add_f32 v42, v146, v154\n"
"v_add_f32 v147, v147, v151\n"
"v_add_f32 v155, v155, v159\n"
"v_add_f32 v43, v147, v155\n"
"v_add_f32 v160, v160, v164\n"
"v_add_f32 v168, v168, v172\n"
"v_add_f32 v44, v160, v168\n"
"v_add_f32 v176, v176, v180\n"
"v_add_f32 v184, v184, v188\n"
"v_add_f32 v45, v176, v184\n"
"v_mov_b32 v128, 0\n"
"v_mov_b32 v129, 0\n"
"v_mov_b32 v130, 0\n"
"v_mov_b32 v131, 0\n"
"v_mov_b32 v136, 0\n"
"v_mov_b32 v137, 0\n"
"v_mov_b32 v138, 0\n"
"v_mov_b32 v139, 0\n"
"v_mov_b32 v144, 0\n"
"v_mov_b32 v145, 0\n"
"v_mov_b32 v146, 0\n"
"v_mov_b32 v147, 0\n"
"v_mov_b32 v152, 0\n"
"v_mov_b32 v153, 0\n"
"v_mov_b32 v154, 0\n"
"v_mov_b32 v155, 0\n"
"s_mov_b32 s95, 0x80000000\n"
"s_mov_b32 s94, 0x80000000\n"
"s_cmp_eq_u32 s54, 0\n"
"s_cbranch_scc1 .LQ_1s\n"
"s_cmp_eq_u32 s54, 1\n"
"s_cbranch_scc1 .LQ_2s\n"
"s_cmp_eq_u32 s54, 2\n"
"s_cbranch_scc1 .LQ_3s\n"
"s_cmp_eq_u32 s54, 3\n"
"s_cbranch_scc1 .LQ_4s\n"
".p2align 6\n"
".LQ_top:\n"
"v_mfma_f32_16x16x32_bf16 v[112:115], a[128:131], v[48:51], 0\n"
"s_add_u32 s50, s50, 0x100000\n"
"global_load_dwordx4 a[192:195], v5, s[56:57] offset:0 sc1\n"
"v_mfma_f32_16x16x32_bf16 v[112:115], a[132:135], v[52:55], v[112:115]\n"
"ds_write_addtid_b32 v132 offset:4352\n"
"v_mfma_f32_16x16x32_bf16 v[112:115], a[136:139], v[56:59], v[112:115]\n"
"ds_write_addtid_b32 v133 offset:8992\n"
"v_mfma_f32_16x16x32_bf16 v[112:115], a[140:143], v[60:63], v[112:115]\n"
"ds_write_addtid_b32 v134 offset:13696\n"
"v_mfma_f32_16x16x32_bf16 v[112:115], a[144:147], v[64:67], v[112:115]\n"
"ds_write_addtid_b32 v135 offset:18336\n"
"v_mov_b32 v255, v128\n"
"v_mfma_f32_16x16x32_bf16 v[112:115], a[148:151], v[68:71], v[112:115]\n"
"global_load_dwordx4 a[196:199], v5, s[56:57] offset:1024 sc1\n"
"v_mul_f32 v119, v129, v39\n"
"v_mfma_f32_16x16x32_bf16 v[112:115], a[152:155], v[72:75], v[112:115]\n"
"v_mul_f32 v117, v129, v37\n"
"v_mul_f32 v118, v129, v38\n"
"v_mfma_f32_16x16x32_bf16 v[112:115], a[156:159], v[76:79], v[112:115]\n"
"v_mul_f32 v123, v129, v43\n"
"v_mul_f32 v116, v129, v36\n"
"v_mfma_f32_16x16x32_bf16 v[124:127], a[160:163], v[48:51], 0\n"
"v_mul_f32 v121, v129, v41\n"
"v_mul_f32 v122, v129, v42\n"
"v_mfma_f32_16x16x32_bf16 v[124:127], a[164:167], v[52:55], v[124:127]\n"
"ds_write_addtid_b32 v112 offset:0\n"
"v_mul_f32 v120, v129, v40\n"
"v_mfma_f32_16x16x32_bf16 v[124:127], a[168:171], v[56:59], v[124:127]\n"
"s_waitcnt lgkmcnt(1)\n"
"ds_write_addtid_b32 v113 offset:4640\n"
"global_load_dwordx4 a[200:203], v6, s[56:57] offset:0 sc1\n"
"v_mfma_f32_16x16x32_bf16 v[124:127], a[172:175], v[60:63], v[124:127]\n"
"s_barrier\n"
"v_mfma_f32_16x16x32_bf16 v[124:127], a[176:179], v[64:67], v[124:127]\n"
"ds_write_addtid_b32 v114 offset:9344\n"
"ds_read2st64_b32 v[128:129], v12 offset0:0 offset1:1\n"
"v_mfma_f32_16x16x32_bf16 v[124:127], a[180:183], v[68:71], v[124:127]\n"
"ds_write_addtid_b32 v115 offset:13984\n"
"ds_read2st64_b32 v[130:131], v12 offset0:2 offset1:3\n"
"v_mfma_f32_16x16x32_bf16 v[124:127], a[184:187], v[72:75], v[124:127]\n"
"ds_read2st64_b32 v[140:141], v11 offset0:4 offset1:5\n"
"ds_read2st64_b32 v[148:149], v11 offset0:6 offset1:7\n"
"v_mfma_f32_16x16x32_bf16 v[124:127], a[188:191], v[76:79], v[124:127]\n"
"s_add_u32 s95, s50, 0xffc00000\n"
"s_and_b32 s94, s95, 0xfbffffff\n"
"v_mfma_f32_16x16x32_bf16 v[112:115], v[48:51], a[0:3], 0\n"
"global_load_dwordx4 a[204:207], v6, s[56:57] offset:1024 sc1\n"
"v_mfma_f32_16x16x32_bf16 v[112:115], v[52:55], a[4:7], v[112:115]\n"
"ds_write_addtid_b32 v124 offset:256\n"
"v_mfma_f32_16x16x32_bf16 v[112:115], v[56:59], a[8:11], v[112:115]\n"
"s_waitcnt lgkmcnt(3)\n"
"ds_write_addtid_b32 v125 offset:4896\n"
"v_mfma_f32_16x16x32_bf16 v[132:135], v[128:131], v[164:167], v[16:19]\n"
"s_cmp_eq_u32 s95, s62\n"
"s_cbranch_scc1 .LQ_fs\n"
".LQ_fr:\n"
"v_mfma_f32_16x16x32_bf16 v[112:115], v[60:63], a[12:15], v[112:115]\n"
"ds_write_addtid_b32 v126 offset:9600\n"
"v_max_f32 v254, v156, v158\n"
"v_mfma_f32_16x16x32_bf16 v[112:115], v[64:67], a[16:19], v[112:115]\n"
"ds_write_addtid_b32 v127 offset:14240\n"
"global_load_dwordx4 a[208:211], v7, s[56:57] offset:0 sc1\n"
"v_mfma_f32_16x16x32_bf16 v[112:115], v[68:71], a[20:23], v[112:115]\n"
"v_max_f32 v253, v157, v159\n"
"v_max3_f32 v254, v132, v134, v254\n"
"v_mfma_f32_16x16x32_bf16 v[112:115], v[72:75], a[24:27], v[112:115]\n"
"v_max3_f32 v253, v133, v135, v253\n"
"v_max_f32 v252, v254, v253\n"
"v_mfma_f32_16x16x32_bf16 v[112:115], v[76:79], a[28:31], v[112:115]\n"
"v_mov_b32 v251, v252\n"
"v_mfma_f32_16x16x32_bf16 v[124:127], v[48:51], a[32:35], 0\n"
"v_permlane32_swap_b32 v251, v252\n"
"v_mfma_f32_16x16x32_bf16 v[124:127], v[52:55], a[36:39], v[124:127]\n"
"ds_write_addtid_b32 v112 offset:512\n"
"global_load_dwordx4 a[212:215], v7, s[56:57] offset:1024 sc1\n"
"v_mfma_f32_16x16x32_bf16 v[124:127], v[56:59], a[40:43], v[124:127]\n"
"ds_write_addtid_b32 v113 offset:5152\n"
"v_max_f32 v252, v251, v252\n"
"v_mfma_f32_16x16x32_bf16 v[124:127], v[60:63], a[44:47], v[124:127]\n"
"ds_write_addtid_b32 v114 offset:9856\n"
"v_mov_b32 v251, v252\n"
"v_mfma_f32_16x16x32_bf16 v[124:127], v[64:67], a[48:51], v[124:127]\n"
"ds_write_addtid_b32 v115 offset:14496\n"
"v_mfma_f32_16x16x32_bf16 v[124:127], v[68:71], a[52:55], v[124:127]\n"
"v_permlane16_swap_b32 v251, v252\n"
"v_mfma_f32_16x16x32_bf16 v[124:127], v[72:75], a[56:59], v[124:127]\n"
"global_load_dwordx4 a[216:219], v8, s[56:57] offset:0 sc1\n"
"v_max_f32 v252, v251, v252\n"
"v_mfma_f32_16x16x32_bf16 v[124:127], v[76:79], a[60:63], v[124:127]\n"
"v_mul_f32 v252, v252, v250\n"
"v_fma_f32 v254, v254, v250, -v252\n"
"v_mfma_f32_16x16x32_bf16 v[116:119], a[64:67], v[48:51], v[116:119]\n"
"v_fma_f32 v253, v253, v250, -v252\n"
"v_mfma_f32_16x16x32_bf16 v[116:119], a[68:71], v[52:55], v[116:119]\n"
"ds_write_addtid_b32 v124 offset:768\n"
"v_mfma_f32_16x16x32_bf16 v[116:119], a[72:75], v[56:59], v[116:119]\n"
"ds_write_addtid_b32 v125 offset:5408\n"
"v_mfma_f32_16x16x32_bf16 v[116:119], a[76:79], v[60:63], v[116:119]\n"
"ds_write_addtid_b32 v126 offset:10112\n"
"global_load_dwordx4 a[220:223], v8, s[56:57] offset:1024 sc1\n"
"v_mfma_f32_16x16x32_bf16 v[116:119], a[80:83], v[64:67], v[116:119]\n"
"ds_write_addtid_b32 v127 offset:14752\n"
"v_mfma_f32_16x16x32_bf16 v[116:119], a[84:87], v[68:71], v[116:119]\n"
"v_exp_f32 v254, v254\n"
"v_mfma_f32_16x16x32_bf16 v[116:119], a[88:91], v[72:75], v[116:119]\n"
"v_exp_f32 v253, v253\n"
"v_mfma_f32_16x16x32_bf16 v[116:119], a[92:95], v[76:79], v[116:119]\n"
"s_add_u32 s58, s50, 0x100000\n"
"v_add_f32 v252, v254, v253\n"
"v_mov_b32 v251, v252\n"
"v_mfma_f32_16x16x32_bf16 v[120:123], a[96:99], v[48:51], v[120:123]\n"
"s_and_b32 s58, s58, 0x3ffffff\n"
"v_permlane32_swap_b32 v251, v252\n"
"v_mfma_f32_16x16x32_bf16 v[120:123], a[100:103], v[52:55], v[120:123]\n"
"s_add_u32 s56, s40, s58\n"
"v_add_f32 v252, v251, v252\n"
"v_mov_b32 v251, v252\n"
"v_mfma_f32_16x16x32_bf16 v[120:123], a[104:107], v[56:59], v[120:123]\n"
"s_addc_u32 s57, s41, 0\n"
"v_permlane16_swap_b32 v251, v252\n"
"v_mfma_f32_16x16x32_bf16 v[120:123], a[108:111], v[60:63], v[120:123]\n"
"s_waitcnt vmcnt(8)\n"
"v_cmp_ne_u64 vcc, s[48:49], v[154:155]\n"
"v_add_f32 v252, v251, v252\n"
"v_mfma_f32_16x16x32_bf16 v[120:123], a[112:115], v[64:67], v[120:123]\n"
"buffer_load_dwordx4 v[128:131], v9, s[40:43], s50 offen sc1\n"
"v_mfma_f32_16x16x32_bf16 v[120:123], a[116:119], v[68:71], v[120:123]\n"
"s_cbranch_vccnz .LQ_4s\n"
".LQ_4r:\n"
"v_mfma_f32_16x16x32_bf16 v[120:123], a[120:123], v[72:75], v[120:123]\n"
"s_add_u32 s50, s50, 0x100000\n"
"v_rcp_f32 v252, v252\n"
"v_mfma_f32_16x16x32_bf16 v[120:123], a[124:127], v[76:79], v[120:123]\n"
"global_load_dwordx4 a[224:227], v5, s[56:57] offset:0 sc1\n"
"v_mul_f32 v254, v254, v252\n"
"v_mfma_f32_16x16x32_bf16 v[48:51], v[80:83], a[64:67], 0\n"
"v_mul_f32 v253, v253, v252\n"
"v_cvt_pk_bf16_f32 v251, v254, v253\n"
"v_mfma_f32_16x16x32_bf16 v[48:51], v[84:87], a[68:71], v[48:51]\n"
"v_mfma_f32_16x16x32_bf16 v[48:51], v[88:91], a[72:75], v[48:51]\n"
"s_waitcnt lgkmcnt(12)\n"
"v_and_b32 v52, v251, v24\n"
"v_and_b32 v53, v251, v25\n"
"v_mfma_f32_16x16x32_bf16 v[48:51], v[92:95], a[76:79], v[48:51]\n"
"v_and_b32 v54, v251, v26\n"
"v_and_b32 v55, v251, v27\n"
"v_mfma_f32_16x16x32_bf16 v[48:51], v[96:99], a[80:83], v[48:51]\n"
"global_load_dwordx4 a[228:231], v5, s[56:57] offset:1024 sc1\n"
"v_mfma_f32_16x16x32_bf16 v[56:59], v[140:143], v[52:55], 0\n"
"v_mul_f32_dpp v75, v137, v44 row_newbcast:3 row_mask:0xf bank_mask:0xf\n"
"v_mov_b32 v254, v136\n"
"v_mfma_f32_16x16x32_bf16 v[60:63], v[148:151], v[52:55], 0\n"
"v_mul_f32_dpp v73, v137, v44 row_newbcast:1 row_mask:0xf bank_mask:0xf\n"
"v_mul_f32_dpp v74, v137, v44 row_newbcast:2 row_mask:0xf bank_mask:0xf\n"
"v_mfma_f32_16x16x32_bf16 v[48:51], v[100:103], a[84:87], v[48:51]\n"
"v_cvt_pk_bf16_f32 v52, v56, v57\n"
"v_cvt_pk_bf16_f32 v53, v58, v59\n"
"v_mfma_f32_16x16x32_bf16 v[48:51], v[104:107], a[88:91], v[48:51]\n"
"v_cvt_pk_bf16_f32 v54, v60, v61\n"
"v_cvt_pk_bf16_f32 v55, v62, v63\n"
"v_mfma_f32_16x16x32_bf16 v[48:51], v[108:111], a[92:95], v[48:51]\n"
"buffer_store_dwordx4 v[52:55], v13, s[40:43], s94 offen sc1\n"
"v_mul_f32_dpp v114, v137, v45 row_newbcast:2 row_mask:0xf bank_mask:0xf\n"
"v_mul_f32_dpp v115, v137, v45 row_newbcast:3 row_mask:0xf bank_mask:0xf\n"
"v_mul_f32_dpp v72, v137, v44 row_newbcast:0 row_mask:0xf bank_mask:0xf\n"
"v_mfma_f32_16x16x32_bf16 v[52:55], v[80:83], a[96:99], 0\n"
"s_waitcnt lgkmcnt(0)\n"
"v_mul_f32_dpp v112, v137, v45 row_newbcast:0 row_mask:0xf bank_mask:0xf\n"
"v_mul_f32_dpp v113, v137, v45 row_newbcast:1 row_mask:0xf bank_mask:0xf\n"
"v_mfma_f32_16x16x32_bf16 v[52:55], v[84:87], a[100:103], v[52:55]\n"
"s_barrier\n"
"v_mfma_f32_16x16x32_bf16 v[52:55], v[88:91], a[104:107], v[52:55]\n"
"ds_write_addtid_b32 v48 offset:1024\n"
"ds_read_b128 v[56:59], v2 offset:1024\n"
"v_mfma_f32_16x16x32_bf16 v[52:55], v[92:95], a[108:111], v[52:55]\n"
"ds_write_addtid_b32 v49 offset:5664\n"
"ds_read_b128 v[60:63], v3 offset:2048\n"
"v_mfma_f32_16x16x32_bf16 v[52:55], v[96:99], a[112:115], v[52:55]\n"
"ds_write_addtid_b32 v50 offset:10368\n"
"global_load_dwordx4 a[232:235], v6, s[56:57] offset:0 sc1\n"
"v_mfma_f32_16x16x32_bf16 v[52:55], v[100:103], a[116:119], v[52:55]\n"
"ds_write_addtid_b32 v51 offset:15008\n"
"ds_read_b128 v[64:67], v4 offset:3072\n"
"v_mfma_f32_16x16x32_bf16 v[52:55], v[104:107], a[120:123], v[52:55]\n"
"ds_read_b128 v[68:71], v2 offset:1280\n"
"ds_read_b128 v[48:51], v3 offset:2304\n"
"v_mfma_f32_16x16x32_bf16 v[52:55], v[108:111], a[124:127], v[52:55]\n"
"ds_read_b128 v[76:79], v4 offset:3328\n"
"v_mfma_f32_16x16x32_bf16 v[124:127], v[80:83], a[0:3], 0\n"
"global_load_dwordx4 a[236:239], v6, s[56:57] offset:1024 sc1\n"
"v_mfma_f32_16x16x32_bf16 v[124:127], v[84:87], a[4:7], v[124:127]\n"
"ds_write_addtid_b32 v52 offset:1280\n"
"v_mfma_f32_16x16x32_bf16 v[124:127], v[88:91], a[8:11], v[124:127]\n"
"ds_write_addtid_b32 v53 offset:5920\n"
"v_mfma_f32_16x16x32_bf16 v[124:127], v[92:95], a[12:15], v[124:127]\n"
"s_waitcnt lgkmcnt(5)\n"
"ds_write_addtid_b32 v54 offset:10624\n"
"v_add_f32 v56, v56, v60\n"
"v_mfma_f32_16x16x32_bf16 v[124:127], v[96:99], a[16:19], v[124:127]\n"
"ds_write_addtid_b32 v55 offset:15264\n"
"v_add_f32 v57, v57, v61\n"
"v_mfma_f32_16x16x32_bf16 v[124:127], v[100:103], a[20:23], v[124:127]\n"
"global_load_dwordx4 a[240:243], v7, s[56:57] offset:0 sc1\n"
"v_add_f32 v58, v58, v62\n"
"v_mfma_f32_16x16x32_bf16 v[124:127], v[104:107], a[24:27], v[124:127]\n"
"v_add_f32 v59, v59, v63\n"
"v_add_f32 v56, v56, v64\n"
"v_mfma_f32_16x16x32_bf16 v[124:127], v[108:111], a[28:31], v[124:127]\n"
"v_add_f32 v57, v57, v65\n"
"v_add_f32 v58, v58, v66\n"
"v_mfma_f32_16x16x32_bf16 v[52:55], v[80:83], a[32:35], 0\n"
"v_add_f32 v59, v59, v67\n"
"v_add_f32 v56, v56, v116\n"
"v_mfma_f32_16x16x32_bf16 v[52:55], v[84:87], a[36:39], v[52:55]\n"
"ds_write_addtid_b32 v124 offset:1536\n"
"v_add_f32 v57, v57, v117\n"
"v_mfma_f32_16x16x32_bf16 v[52:55], v[88:91], a[40:43], v[52:55]\n"
"ds_write_addtid_b32 v125 offset:6176\n"
"global_load_dwordx4 a[244:247], v7, s[56:57] offset:1024 sc1\n"
"v_mfma_f32_16x16x32_bf16 v[52:55], v[92:95], a[44:47], v[52:55]\n"
"ds_write_addtid_b32 v126 offset:10880\n"
"v_add_f32 v58, v58, v118\n"
"v_mfma_f32_16x16x32_bf16 v[52:55], v[96:99], a[48:51], v[52:55]\n"
"ds_write_addtid_b32 v127 offset:15520\n"
"v_add_f32 v59, v59, v119\n"
"v_mfma_f32_16x16x32_bf16 v[52:55], v[100:103], a[52:55], v[52:55]\n"
"v_mul_f32 v56, v255, v56\n"
"v_mul_f32 v57, v255, v57\n"
"v_mfma_f32_16x16x32_bf16 v[52:55], v[104:107], a[56:59], v[52:55]\n"
"v_mul_f32 v58, v255, v58\n"
"v_mul_f32 v59, v255, v59\n"
"v_mfma_f32_16x16x32_bf16 v[52:55], v[108:111], a[60:63], v[52:55]\n"
"global_load_dwordx4 a[248:251], v8, s[56:57] offset:0 sc1\n"
"v_cvt_pk_bf16_f32 v60, v56, v57\n"
"v_mfma_f32_16x16x32_bf16 v[72:75], v[80:83], a[128:131], v[72:75]\n"
"v_cvt_pk_bf16_f32 v61, v58, v59\n"
"v_mfma_f32_16x16x32_bf16 v[72:75], v[84:87], a[132:135], v[72:75]\n"
"ds_write_addtid_b32 v52 offset:1792\n"
"ds_write_addtid_b32 v60 offset:18592\n"
"v_mfma_f32_16x16x32_bf16 v[72:75], v[88:91], a[136:139], v[72:75]\n"
"ds_write_addtid_b32 v53 offset:6432\n"
"ds_write_addtid_b32 v61 offset:18848\n"
"v_mfma_f32_16x16x32_bf16 v[72:75], v[92:95], a[140:143], v[72:75]\n"
"s_waitcnt lgkmcnt(12)\n"
"ds_write_addtid_b32 v54 offset:11136\n"
"v_add_f32 v68, v68, v48\n"
"v_mfma_f32_16x16x32_bf16 v[72:75], v[96:99], a[144:147], v[72:75]\n"
"ds_write_addtid_b32 v55 offset:15776\n"
"global_load_dwordx4 a[252:255], v8, s[56:57] offset:1024 sc1\n"
"v_mfma_f32_16x16x32_bf16 v[72:75], v[100:103], a[148:151], v[72:75]\n"
"v_add_f32 v69, v69, v49\n"
"v_add_f32 v70, v70, v50\n"
"v_mfma_f32_16x16x32_bf16 v[72:75], v[104:107], a[152:155], v[72:75]\n"
"v_add_f32 v71, v71, v51\n"
"v_add_f32 v68, v68, v76\n"
"v_mfma_f32_16x16x32_bf16 v[72:75], v[108:111], a[156:159], v[72:75]\n"
"v_add_f32 v69, v69, v77\n"
"v_add_f32 v70, v70, v78\n"
"v_mfma_f32_16x16x32_bf16 v[112:115], v[80:83], a[160:163], v[112:115]\n"
"s_add_u32 s58, s50, 0x100000\n"
"v_add_f32 v71, v71, v79\n"
"v_add_f32 v68, v68, v120\n"
"v_mfma_f32_16x16x32_bf16 v[112:115], v[84:87], a[164:167], v[112:115]\n"
"s_and_b32 s58, s58, 0x3ffffff\n"
"v_add_f32 v69, v69, v121\n"
"v_add_f32 v70, v70, v122\n"
"v_mfma_f32_16x16x32_bf16 v[112:115], v[88:91], a[168:171], v[112:115]\n"
"s_add_u32 s56, s40, s58\n"
"v_add_f32 v71, v71, v123\n"
"v_mul_f32 v68, v255, v68\n"
"v_mfma_f32_16x16x32_bf16 v[112:115], v[92:95], a[172:175], v[112:115]\n"
"s_addc_u32 s57, s41, 0\n"
"v_mul_f32 v69, v255, v69\n"
"v_mul_f32 v70, v255, v70\n"
"v_mfma_f32_16x16x32_bf16 v[112:115], v[96:99], a[176:179], v[112:115]\n"
"s_waitcnt vmcnt(9)\n"
"v_cmp_ne_u64 vcc, s[48:49], v[130:131]\n"
"v_mul_f32 v71, v255, v71\n"
"v_mfma_f32_16x16x32_bf16 v[112:115], v[100:103], a[180:183], v[112:115]\n"
"buffer_load_dwordx4 v[136:139], v10, s[40:43], s50 offen sc1\n"
"v_mfma_f32_16x16x32_bf16 v[112:115], v[104:107], a[184:187], v[112:115]\n"
"s_cbranch_vccnz .LQ_1s\n"
".LQ_1r:\n"
"v_cvt_pk_bf16_f32 v62, v68, v69\n"
"v_mfma_f32_16x16x32_bf16 v[112:115], v[108:111], a[188:191], v[112:115]\n"
"v_cvt_pk_bf16_f32 v63, v70, v71\n"
"v_mfma_f32_16x16x32_bf16 v[64:67], a[192:195], a[64:67], 0\n"
"s_add_u32 s50, s50, 0x100000\n"
"global_load_dwordx4 v[48:51], v5, s[56:57] offset:0 sc1\n"
"ds_write_addtid_b32 v62 offset:19104\n"
"v_mfma_f32_16x16x32_bf16 v[64:67], a[196:199], a[68:71], v[64:67]\n"
"ds_write_addtid_b32 v63 offset:19360\n"
"v_mfma_f32_16x16x32_bf16 v[64:67], a[200:203], a[72:75], v[64:67]\n"
"v_mfma_f32_16x16x32_bf16 v[64:67], a[204:207], a[76:79], v[64:67]\n"
"v_mul_f32 v250, 0x3e8293ee, v144\n"
"v_mfma_f32_16x16x32_bf16 v[64:67], a[208:211], a[80:83], v[64:67]\n"
"v_mul_f32 v110, v145, v30\n"
"v_mul_f32 v111, v145, v31\n"
"v_mfma_f32_16x16x32_bf16 v[64:67], a[212:215], a[84:87], v[64:67]\n"
"global_load_dwordx4 v[52:55], v5, s[56:57] offset:1024 sc1\n"
"v_mul_f32 v109, v145, v29\n"
"v_mfma_f32_16x16x32_bf16 v[64:67], a[216:219], a[88:91], v[64:67]\n"
"v_mul_f32 v119, v145, v35\n"
"v_mul_f32 v108, v145, v28\n"
"v_mfma_f32_16x16x32_bf16 v[64:67], a[220:223], a[92:95], v[64:67]\n"
"s_waitcnt vmcnt(9)\n"
"buffer_store_dwordx2 v[0:1], v14, s[40:43], s95 offen offset:0 sc1\n"
"v_mul_f32 v118, v145, v34\n"
"v_mfma_f32_16x16x32_bf16 v[68:71], a[192:195], a[96:99], 0\n"
"s_waitcnt lgkmcnt(0)\n"
"v_mul_f32 v116, v145, v32\n"
"v_mul_f32 v117, v145, v33\n"
"v_mfma_f32_16x16x32_bf16 v[68:71], a[196:199], a[100:103], v[68:71]\n"
"s_barrier\n"
"v_mfma_f32_16x16x32_bf16 v[68:71], a[200:203], a[104:107], v[68:71]\n"
"ds_write_addtid_b32 v64 offset:2048\n"
"global_load_dwordx4 v[56:59], v6, s[56:57] offset:0 sc1\n"
"v_mfma_f32_16x16x32_bf16 v[68:71], a[204:207], a[108:111], v[68:71]\n"
"ds_write_addtid_b32 v65 offset:6688\n"
"ds_read_b128 v[76:79], v2 offset:2560\n"
"v_mfma_f32_16x16x32_bf16 v[68:71], a[208:211], a[112:115], v[68:71]\n"
"ds_write_addtid_b32 v66 offset:11392\n"
"ds_read_b128 v[80:83], v3 offset:3584\n"
"v_mfma_f32_16x16x32_bf16 v[68:71], a[212:215], a[116:119], v[68:71]\n"
"ds_write_addtid_b32 v67 offset:16032\n"
"ds_read_b128 v[84:87], v4 offset:0\n"
"v_mfma_f32_16x16x32_bf16 v[68:71], a[216:219], a[120:123], v[68:71]\n"
"ds_read_b128 v[88:91], v2 offset:2816\n"
"ds_read_b128 v[92:95], v3 offset:3840\n"
"v_mfma_f32_16x16x32_bf16 v[68:71], a[220:223], a[124:127], v[68:71]\n"
"global_load_dwordx4 v[60:63], v6, s[56:57] offset:1024 sc1\n"
"ds_read_b128 v[96:99], v4 offset:256\n"
"v_mfma_f32_16x16x32_bf16 v[100:103], a[128:131], a[192:195], 0\n"
"v_mfma_f32_16x16x32_bf16 v[100:103], a[132:135], a[196:199], v[100:103]\n"
"ds_write_addtid_b32 v68 offset:2304\n"
"v_mfma_f32_16x16x32_bf16 v[100:103], a[136:139], a[200:203], v[100:103]\n"
"ds_write_addtid_b32 v69 offset:6944\n"
"v_mfma_f32_16x16x32_bf16 v[100:103], a[140:143], a[204:207], v[100:103]\n"
"s_waitcnt lgkmcnt(5)\n"
"ds_write_addtid_b32 v70 offset:11648\n"
"v_add_f32 v76, v76, v80\n"
"v_mfma_f32_16x16x32_bf16 v[100:103], a[144:147], a[208:211], v[100:103]\n"
"ds_write_addtid_b32 v71 offset:16288\n"
"global_load_dwordx4 v[64:67], v7, s[56:57] offset:0 sc1\n"
"v_mfma_f32_16x16x32_bf16 v[100:103], a[148:151], a[212:215], v[100:103]\n"
"v_add_f32 v77, v77, v81\n"
"v_add_f32 v78, v78, v82\n"
"v_mfma_f32_16x16x32_bf16 v[100:103], a[152:155], a[216:219], v[100:103]\n"
"v_add_f32 v79, v79, v83\n"
"v_add_f32 v76, v76, v84\n"
"v_mfma_f32_16x16x32_bf16 v[100:103], a[156:159], a[220:223], v[100:103]\n"
"v_add_f32 v77, v77, v85\n"
"v_add_f32 v78, v78, v86\n"
"v_mfma_f32_16x16x32_bf16 v[80:83], a[160:163], a[192:195], 0\n"
"v_add_f32 v79, v79, v87\n"
"v_add_f32 v76, v76, v72\n"
"v_mfma_f32_16x16x32_bf16 v[80:83], a[164:167], a[196:199], v[80:83]\n"
"ds_write_addtid_b32 v100 offset:2560\n"
"global_load_dwordx4 v[68:71], v7, s[56:57] offset:1024 sc1\n"
"v_mfma_f32_16x16x32_bf16 v[80:83], a[168:171], a[200:203], v[80:83]\n"
"ds_write_addtid_b32 v101 offset:7200\n"
"v_add_f32 v77, v77, v73\n"
"v_mfma_f32_16x16x32_bf16 v[80:83], a[172:175], a[204:207], v[80:83]\n"
"ds_write_addtid_b32 v102 offset:11904\n"
"v_add_f32 v78, v78, v74\n"
"v_mfma_f32_16x16x32_bf16 v[80:83], a[176:179], a[208:211], v[80:83]\n"
"ds_write_addtid_b32 v103 offset:16544\n"
"v_add_f32 v79, v79, v75\n"
"v_mfma_f32_16x16x32_bf16 v[80:83], a[180:183], a[212:215], v[80:83]\n"
"v_mul_f32_dpp v76, v254, v76 row_newbcast:0 row_mask:0xf bank_mask:0xf\n"
"v_mul_f32_dpp v77, v254, v77 row_newbcast:1 row_mask:0xf bank_mask:0xf\n"
"v_mfma_f32_16x16x32_bf16 v[80:83], a[184:187], a[216:219], v[80:83]\n"
"global_load_dwordx4 v[72:75], v8, s[56:57] offset:0 sc1\n"
"v_mul_f32_dpp v78, v254, v78 row_newbcast:2 row_mask:0xf bank_mask:0xf\n"
"v_mfma_f32_16x16x32_bf16 v[80:83], a[188:191], a[220:223], v[80:83]\n"
"v_mul_f32_dpp v79, v254, v79 row_newbcast:3 row_mask:0xf bank_mask:0xf\n"
"v_cvt_pk_bf16_f32 v142, v76, v77\n"
"v_mfma_f32_16x16x32_bf16 v[108:111], a[0:3], a[192:195], v[108:111]\n"
"v_cvt_pk_bf16_f32 v143, v78, v79\n"
"v_mfma_f32_16x16x32_bf16 v[108:111], a[4:7], a[196:199], v[108:111]\n"
"ds_write_addtid_b32 v80 offset:2816\n"
"ds_write_addtid_b32 v142 offset:19616\n"
"v_mfma_f32_16x16x32_bf16 v[108:111], a[8:11], a[200:203], v[108:111]\n"
"ds_write_addtid_b32 v81 offset:7456\n"
"ds_write_addtid_b32 v143 offset:19872\n"
"v_mfma_f32_16x16x32_bf16 v[108:111], a[12:15], a[204:207], v[108:111]\n"
"s_waitcnt lgkmcnt(12)\n"
"ds_write_addtid_b32 v82 offset:12160\n"
"global_load_dwordx4 v[76:79], v8, s[56:57] offset:1024 sc1\n"
"v_mfma_f32_16x16x32_bf16 v[108:111], a[16:19], a[208:211], v[108:111]\n"
"ds_write_addtid_b32 v83 offset:16800\n"
"v_add_f32 v88, v88, v92\n"
"v_mfma_f32_16x16x32_bf16 v[108:111], a[20:23], a[212:215], v[108:111]\n"
"v_add_f32 v89, v89, v93\n"
"v_add_f32 v90, v90, v94\n"
"v_mfma_f32_16x16x32_bf16 v[108:111], a[24:27], a[216:219], v[108:111]\n"
"v_add_f32 v91, v91, v95\n"
"v_add_f32 v88, v88, v96\n"
"v_mfma_f32_16x16x32_bf16 v[108:111], a[28:31], a[220:223], v[108:111]\n"
"v_add_f32 v89, v89, v97\n"
"v_add_f32 v90, v90, v98\n"
"v_mfma_f32_16x16x32_bf16 v[116:119], a[32:35], a[192:195], v[116:119]\n"
"v_add_f32 v91, v91, v99\n"
"v_add_f32 v88, v88, v112\n"
"v_mfma_f32_16x16x32_bf16 v[116:119], a[36:39], a[196:199], v[116:119]\n"
"v_add_f32 v89, v89, v113\n"
"v_add_f32 v90, v90, v114\n"
"v_mfma_f32_16x16x32_bf16 v[116:119], a[40:43], a[200:203], v[116:119]\n"
"s_add_u32 s58, s50, 0x100000\n"
"buffer_load_dwordx4 v[144:147], v9, s[40:43], s50 offen sc1\n"
"v_mfma_f32_16x16x32_bf16 v[116:119], a[44:47], a[204:207], v[116:119]\n"
"s_and_b32 s58, s58, 0x3ffffff\n"
"v_add_f32 v91, v91, v115\n"
"v_mul_f32_dpp v88, v254, v88 row_newbcast:0 row_mask:0xf bank_mask:0xf\n"
"v_mfma_f32_16x16x32_bf16 v[116:119], a[48:51], a[208:211], v[116:119]\n"
"s_add_u32 s56, s40, s58\n"
"v_mul_f32_dpp v89, v254, v89 row_newbcast:1 row_mask:0xf bank_mask:0xf\n"
"v_mul_f32_dpp v90, v254, v90 row_newbcast:2 row_mask:0xf bank_mask:0xf\n"
"v_mfma_f32_16x16x32_bf16 v[116:119], a[52:55], a[212:215], v[116:119]\n"
"s_addc_u32 s57, s41, 0\n"
"v_mul_f32_dpp v91, v254, v91 row_newbcast:3 row_mask:0xf bank_mask:0xf\n"
"v_cvt_pk_bf16_f32 v150, v88, v89\n"
"v_mfma_f32_16x16x32_bf16 v[116:119], a[56:59], a[216:219], v[116:119]\n"
"s_waitcnt vmcnt(10)\n"
"v_cmp_ne_u64 vcc, s[48:49], v[138:139]\n"
"v_cvt_pk_bf16_f32 v151, v90, v91\n"
"v_mfma_f32_16x16x32_bf16 v[116:119], a[60:63], a[220:223], v[116:119]\n"
"ds_write_addtid_b32 v150 offset:20128\n"
"ds_write_addtid_b32 v151 offset:20384\n"
"v_mfma_f32_16x16x32_bf16 v[92:95], a[224:227], a[64:67], 0\n"
"s_cbranch_vccnz .LQ_2s\n"
".LQ_2r:\n"
"v_mfma_f32_16x16x32_bf16 v[92:95], a[228:231], a[68:71], v[92:95]\n"
"global_load_dwordx4 v[80:83], v5, s[56:57] offset:0 sc1\n"
"v_mfma_f32_16x16x32_bf16 v[92:95], a[232:235], a[72:75], v[92:95]\n"
"s_add_u32 s50, s50, 0x100000\n"
"v_mfma_f32_16x16x32_bf16 v[92:95], a[236:239], a[76:79], v[92:95]\n"
"v_mfma_f32_16x16x32_bf16 v[92:95], a[240:243], a[80:83], v[92:95]\n"
"v_mfma_f32_16x16x32_bf16 v[92:95], a[244:247], a[84:87], v[92:95]\n"
"v_mfma_f32_16x16x32_bf16 v[92:95], a[248:251], a[88:91], v[92:95]\n"
"global_load_dwordx4 v[84:87], v5, s[56:57] offset:1024 sc1\n"
"v_mfma_f32_16x16x32_bf16 v[92:95], a[252:255], a[92:95], v[92:95]\n"
"v_mfma_f32_16x16x32_bf16 v[100:103], a[224:227], a[96:99], 0\n"
"v_mfma_f32_16x16x32_bf16 v[100:103], a[228:231], a[100:103], v[100:103]\n"
"ds_write_addtid_b32 v92 offset:3072\n"
"v_mfma_f32_16x16x32_bf16 v[100:103], a[232:235], a[104:107], v[100:103]\n"
"s_waitcnt lgkmcnt(1)\n"
"ds_write_addtid_b32 v93 offset:7712\n"
"v_mfma_f32_16x16x32_bf16 v[100:103], a[236:239], a[108:111], v[100:103]\n"
"s_barrier\n"
"v_mfma_f32_16x16x32_bf16 v[100:103], a[240:243], a[112:115], v[100:103]\n"
"ds_write_addtid_b32 v94 offset:12416\n"
"ds_read_b128 v[112:115], v2 offset:4096\n"
"v_mfma_f32_16x16x32_bf16 v[100:103], a[244:247], a[116:119], v[100:103]\n"
"ds_write_addtid_b32 v95 offset:17056\n"
"global_load_dwordx4 v[88:91], v6, s[56:57] offset:0 sc1\n"
"v_mfma_f32_16x16x32_bf16 v[100:103], a[248:251], a[120:123], v[100:103]\n"
"ds_read_b128 v[104:107], v3 offset:512\n"
"ds_read_b128 v[120:123], v4 offset:1536\n"
"v_mfma_f32_16x16x32_bf16 v[100:103], a[252:255], a[124:127], v[100:103]\n"
"global_load_dwordx4 v[92:95], v6, s[56:57] offset:1024 sc1\n"
"ds_read_b128 v[124:127], v2 offset:4352\n"
"v_mfma_f32_16x16x32_bf16 v[132:135], a[128:131], a[224:227], 0\n"
"ds_read_b128 v[152:155], v3 offset:768\n"
"ds_read_b128 v[156:159], v4 offset:1792\n"
"v_mfma_f32_16x16x32_bf16 v[132:135], a[132:135], a[228:231], v[132:135]\n"
"ds_write_addtid_b32 v100 offset:3328\n"
"v_mfma_f32_16x16x32_bf16 v[132:135], a[136:139], a[232:235], v[132:135]\n"
"ds_write_addtid_b32 v101 offset:7968\n"
"v_mfma_f32_16x16x32_bf16 v[132:135], a[140:143], a[236:239], v[132:135]\n"
"ds_write_addtid_b32 v102 offset:12672\n"
"v_mfma_f32_16x16x32_bf16 v[132:135], a[144:147], a[240:243], v[132:135]\n"
"s_waitcnt lgkmcnt(6)\n"
"ds_write_addtid_b32 v103 offset:17312\n"
"global_load_dwordx4 v[96:99], v7, s[56:57] offset:0 sc1\n"
"v_mfma_f32_16x16x32_bf16 v[132:135], a[148:151], a[244:247], v[132:135]\n"
"v_add_f32 v112, v112, v104\n"
"v_add_f32 v113, v113, v105\n"
"v_mfma_f32_16x16x32_bf16 v[132:135], a[152:155], a[248:251], v[132:135]\n"
"v_add_f32 v114, v114, v106\n"
"v_add_f32 v115, v115, v107\n"
"v_mfma_f32_16x16x32_bf16 v[132:135], a[156:159], a[252:255], v[132:135]\n"
"v_add_f32 v112, v112, v120\n"
"v_add_f32 v113, v113, v121\n"
"v_mfma_f32_16x16x32_bf16 v[160:163], a[160:163], a[224:227], 0\n"
"v_add_f32 v114, v114, v122\n"
"v_add_f32 v115, v115, v123\n"
"v_mfma_f32_16x16x32_bf16 v[160:163], a[164:167], a[228:231], v[160:163]\n"
"ds_write_addtid_b32 v132 offset:3584\n"
"global_load_dwordx4 v[100:103], v7, s[56:57] offset:1024 sc1\n"
"v_mfma_f32_16x16x32_bf16 v[160:163], a[168:171], a[232:235], v[160:163]\n"
"ds_write_addtid_b32 v133 offset:8224\n"
"v_add_f32 v112, v112, v108\n"
"v_mfma_f32_16x16x32_bf16 v[160:163], a[172:175], a[236:239], v[160:163]\n"
"ds_write_addtid_b32 v134 offset:12928\n"
"v_add_f32 v113, v113, v109\n"
"v_mfma_f32_16x16x32_bf16 v[160:163], a[176:179], a[240:243], v[160:163]\n"
"ds_write_addtid_b32 v135 offset:17568\n"
"v_add_f32 v114, v114, v110\n"
"v_mfma_f32_16x16x32_bf16 v[160:163], a[180:183], a[244:247], v[160:163]\n"
"v_add_f32 v115, v115, v111\n"
"v_cvt_pk_bf16_f32 v164, v112, v113\n"
"v_mfma_f32_16x16x32_bf16 v[160:163], a[184:187], a[248:251], v[160:163]\n"
"global_load_dwordx4 v[104:107], v8, s[56:57] offset:0 sc1\n"
"v_cvt_pk_bf16_f32 v165, v114, v115\n"
"v_mfma_f32_16x16x32_bf16 v[160:163], a[188:191], a[252:255], v[160:163]\n"
"ds_read2st64_b32 v[120:121], v11 offset0:0 offset1:1\n"
"ds_read2st64_b32 v[122:123], v11 offset0:2 offset1:3\n"
"v_mfma_f32_16x16x32_bf16 v[112:115], a[224:227], a[0:3], 0\n"
"s_waitcnt lgkmcnt(10)\n"
"v_add_f32 v124, v124, v152\n"
"v_add_f32 v125, v125, v153\n"
"v_mfma_f32_16x16x32_bf16 v[112:115], a[228:231], a[4:7], v[112:115]\n"
"ds_write_addtid_b32 v160 offset:3840\n"
"v_add_f32 v126, v126, v154\n"
"v_mfma_f32_16x16x32_bf16 v[112:115], a[232:235], a[8:11], v[112:115]\n"
"ds_write_addtid_b32 v161 offset:8480\n"
"v_add_f32 v127, v127, v155\n"
"v_mfma_f32_16x16x32_bf16 v[112:115], a[236:239], a[12:15], v[112:115]\n"
"ds_write_addtid_b32 v162 offset:13184\n"
"global_load_dwordx4 v[108:111], v8, s[56:57] offset:1024 sc1\n"
"v_mfma_f32_16x16x32_bf16 v[112:115], a[240:243], a[16:19], v[112:115]\n"
"ds_write_addtid_b32 v163 offset:17824\n"
"v_add_f32 v124, v124, v156\n"
"v_mfma_f32_16x16x32_bf16 v[112:115], a[244:247], a[20:23], v[112:115]\n"
"v_add_f32 v125, v125, v157\n"
"v_add_f32 v126, v126, v158\n"
"v_mfma_f32_16x16x32_bf16 v[112:115], a[248:251], a[24:27], v[112:115]\n"
"v_add_f32 v127, v127, v159\n"
"v_add_f32 v124, v124, v116\n"
"v_mfma_f32_16x16x32_bf16 v[112:115], a[252:255], a[28:31], v[112:115]\n"
"v_add_f32 v125, v125, v117\n"
"v_add_f32 v126, v126, v118\n"
"v_mfma_f32_16x16x32_bf16 v[132:135], a[224:227], a[32:35], 0\n"
"s_add_u32 s58, s50, 0x100000\n"
"buffer_load_dwordx4 v[152:155], v9, s[40:43], s50 offen sc1\n"
"v_mfma_f32_16x16x32_bf16 v[132:135], a[228:231], a[36:39], v[132:135]\n"
"s_and_b32 s58, s58, 0x3ffffff\n"
"ds_write_addtid_b32 v112 offset:4096\n"
"v_add_f32 v127, v127, v119\n"
"v_mfma_f32_16x16x32_bf16 v[132:135], a[232:235], a[40:43], v[132:135]\n"
"s_add_u32 s56, s40, s58\n"
"ds_write_addtid_b32 v113 offset:8736\n"
"v_cvt_pk_bf16_f32 v166, v124, v125\n"
"v_mfma_f32_16x16x32_bf16 v[132:135], a[236:239], a[44:47], v[132:135]\n"
"s_addc_u32 s57, s41, 0\n"
"ds_write_addtid_b32 v114 offset:13440\n"
"v_cvt_pk_bf16_f32 v167, v126, v127\n"
"v_mfma_f32_16x16x32_bf16 v[132:135], a[240:243], a[48:51], v[132:135]\n"
"s_waitcnt vmcnt(9)\n"
"ds_write_addtid_b32 v115 offset:18080\n"
"v_cmp_ne_u64 vcc, s[48:49], v[146:147]\n"
"v_mfma_f32_16x16x32_bf16 v[132:135], a[244:247], a[52:55], v[132:135]\n"
"s_waitcnt lgkmcnt(8)\n"
"v_mfma_f32_16x16x32_bf16 v[156:159], v[120:123], v[164:167], v[20:23]\n"
"v_mfma_f32_16x16x32_bf16 v[132:135], a[248:251], a[56:59], v[132:135]\n"
"s_cbranch_vccnz .LQ_3s\n"
".LQ_3r:\n"
"v_mfma_f32_16x16x32_bf16 v[132:135], a[252:255], a[60:63], v[132:135]\n"
"s_branch .LQ_top\n"
".LQ_1s:\n"
"s_cmp_ge_u32 s50, s53\n"
"s_cbranch_scc1 .LQ_end\n"
"s_cmp_ge_u32 s50, s52\n"
"s_cbranch_scc1 .LQ_1r\n"
"s_sub_u32 s60, s50, 0x100000\n"
"s_mov_b32 s61, 200000\n"
"s_nop 7\n"
".LQ_1p:\n"
"buffer_load_dwordx4 v[128:131], v9, s[40:43], s60 offen sc1\n"
"s_waitcnt vmcnt(0)\n"
"v_cmp_ne_u64 vcc, s[48:49], v[130:131]\n"
"s_cbranch_vccz .LQ_1px\n"
"s_sleep 1\n"
"s_sub_u32 s61, s61, 1\n"
"s_cbranch_scc0 .LQ_1p\n"
"s_branch .LQ_end\n"
".LQ_1px:\n"
"s_branch .LQ_1r\n"
".LQ_2s:\n"
"s_cmp_ge_u32 s50, s53\n"
"s_cbranch_scc1 .LQ_end\n"
"s_cmp_ge_u32 s50, s52\n"
"s_cbranch_scc1 .LQ_2r\n"
"s_sub_u32 s60, s50, 0x100000\n"
"s_mov_b32 s61, 200000\n"
"s_nop 7\n"
".LQ_2p:\n"
"buffer_load_dwordx4 v[136:139], v10, s[40:43], s60 offen sc1\n"
"s_waitcnt vmcnt(0)\n"
"v_cmp_ne_u64 vcc, s[48:49], v[138:139]\n"
"s_cbranch_vccz .LQ_2px\n"
"s_sleep 1\n"
"s_sub_u32 s61, s61, 1\n"
"s_cbranch_scc0 .LQ_2p\n"
"s_branch .LQ_end\n"
".LQ_2px:\n"
"s_branch .LQ_2r\n"
".LQ_3s:\n"
"s_cmp_ge_u32 s50, s53\n"
"s_cbranch_scc1 .LQ_end\n"
"s_cmp_ge_u32 s50, s52\n"
"s_cbranch_scc1 .LQ_3r\n"
"s_sub_u32 s60, s50, 0x100000\n"
"s_mov_b32 s61, 200000\n"
"s_nop 7\n"
".LQ_3p:\n"
"buffer_load_dwordx4 v[144:147], v9, s[40:43], s60 offen sc1\n"
"s_waitcnt vmcnt(0)\n"
"v_cmp_ne_u64 vcc, s[48:49], v[146:147]\n"
"s_cbranch_vccz .LQ_3px\n"
"s_sleep 1\n"
"s_sub_u32 s61, s61, 1\n"
"s_cbranch_scc0 .LQ_3p\n"
"s_branch .LQ_end\n"
".LQ_3px:\n"
"s_branch .LQ_3r\n"
".LQ_4s:\n"
"s_cmp_ge_u32 s50, s53\n"
"s_cbranch_scc1 .LQ_end\n"
"s_cmp_ge_u32 s50, s52\n"
"s_cbranch_scc1 .LQ_4r\n"
"s_sub_u32 s60, s50, 0x100000\n"
"s_mov_b32 s61, 200000\n"
"s_nop 7\n"
".LQ_4p:\n"
"buffer_load_dwordx4 v[152:155], v9, s[40:43], s60 offen sc1\n"
"s_waitcnt vmcnt(0)\n"
"v_cmp_ne_u64 vcc, s[48:49], v[154:155]\n"
"s_cbranch_vccz .LQ_4px\n"
"s_sleep 1\n"
"s_sub_u32 s61, s61, 1\n"
"s_cbranch_scc0 .LQ_4p\n"
"s_branch .LQ_end\n"
".LQ_4px:\n"
"s_branch .LQ_4r\n"
".LQ_fs:\n"
"s_nop 7\n"
"v_mov_b32 v156, 0xff800000\n"
"v_mov_b32 v157, 0xff800000\n"
"v_mov_b32 v158, 0xff800000\n"
"v_mov_b32 v159, 0xff800000\n"
"v_mov_b32 v142, 0\n"
"v_mov_b32 v143, 0\n"
"v_mov_b32 v150, 0\n"
"v_mov_b32 v151, 0\n"
"s_add_u32 s62, s62, 0x4000000\n"
"s_branch .LQ_fr\n"
".LQ_end:\n"
"s_waitcnt vmcnt(0) lgkmcnt(0)\n"
"s_endpgm\n"
      :: "{v2}"((unsigned)(lds0 + ((wid + 3) & 3) * 20640 + trp)), "{v3}"((unsigned)(lds0 + ((wid + 2) & 3) * 20640 + trp)), "{v4}"((unsigned)(lds0 + ((wid + 1) & 3) * 20640 + trp)), "{v5}"((unsigned)(lane * 16 + wid * 8192 + ((0 + rot) % 4) * 2048 + 134217728)), "{v6}"((unsigned)(lane * 16 + wid * 8192 + ((1 + rot) % 4) * 2048 + 134217728)), "{v7}"((unsigned)(lane * 16 + wid * 8192 + ((2 + rot) % 4) * 2048 + 134217728)), "{v8}"((unsigned)(lane * 16 + wid * 8192 + ((3 + rot) % 4) * 2048 + 134217728)), "{v9}"((unsigned)(li * 16 + 352256 + Q_GL * 1048576)), "{v10}"((unsigned)(((li + 4 * lg) & 15) * 16 + 352256 + Q_GL * 1048576)), "{v11}"((unsigned)(lds0 + ((wid + 1) & 3) * 20640 + 18592 + lane * 4)), "{v12}"((unsigned)(lds0 + ((wid + 2) & 3) * 20640 + 18592 + lane * 4)), "{v13}"((unsigned)(lane * 16 + idx * 1024 + 32768)), "{v14}"((unsigned)(lane ? DROP : idx * 8 + 352512)), "{v16}"((unsigned)(qmask(0, 0))), "{v17}"((unsigned)(qmask(1, 0))), "{v18}"((unsigned)(qmask(2, 0))), "{v19}"((unsigned)(qmask(3, 0))), "{v20}"((unsigned)(qmask(0, 1))), "{v21}"((unsigned)(qmask(1, 1))), "{v22}"((unsigned)(qmask(2, 1))), "{v23}"((unsigned)(qmask(3, 1))), "{v24}"((unsigned)(pmask(0))), "{v25}"((unsigned)(pmask(1))), "{v26}"((unsigned)(pmask(2))), "{v27}"((unsigned)(pmask(3))), "{v48}"((unsigned)(vT + tsw)), "{v49}"((unsigned)(vT + (tsw ^ 8))), "{v50}"((unsigned)(vD + ((0 + rot) % 4) * 393216)), "{v51}"((unsigned)(vD + ((1 + rot) % 4) * 393216)), "{v52}"((unsigned)(vD + ((2 + rot) % 4) * 393216)), "{v53}"((unsigned)(vD + ((3 + rot) % 4) * 393216)), "{v54}"((unsigned)(lds0 + wid * 20640 + trp)), "{s[40:43]}"(rS), "{s[44:47]}"(rW), "{s[48:49]}"(wsp), "{s50}"((unsigned)(sA)), "{s51}"((unsigned)(sLo)), "{s52}"((unsigned)(sHi)), "{s53}"((unsigned)(sEx)), "{s54}"((unsigned)(sEnt)), "{s55}"((unsigned)(sLd)), "{s[56:57]}"(wsb + (sLo & 0x3ffffff)), "{s59}"((unsigned)(lds0 + wid * 20640)), "{s62}"((unsigned)(sLo)), "{s63}"((unsigned)(sW0 + 0)), "{s64}"((unsigned)(sW0 + 2048)), "{s65}"((unsigned)(sW0 + 4096))
      : "v255", "a255", "s95", "vcc", "scc", "memory");

  }
  while (__builtin_amdgcn_s_memrealtime() - tz < (role == 1 ? DLO : role == 2 ? DLF : DLG)) __builtin_amdgcn_s_sleep(4);
  const unsigned sA = sLo - 3 * RS, sHi = sLo + (set ? 84 : 85) * 3 * RS;
  if (role == 1) {
    const unsigned sEx = sHi + O_EX * 3 * RS, sEnt = OR ? -wid & 3 : 0, tsw = 0, pair = idx * 4 + wid, rot = 0;
    const unsigned sW0 = wid * 256 * 2048 + idx * 256, vD = (lane >> 2) * 2048 + (lane & 3) * 16;
    const u32x4 rW = desc(a.wo, 1024 * 2048);
    asm volatile(
"s_nop 4\n"
"v_mov_b32 v186, 0\n"
"global_load_dwordx2 v[0:1], v186, s[48:49] sc1\n"
"v_mov_b32 v188, 0x3f803f80\n"
"v_mov_b32 v189, 0x3f803f80\n"
"v_mov_b32 v190, 0x3f803f80\n"
"v_mov_b32 v191, 0x3f803f80\n"
"s_add_u32 m0, s55, 0\n"
"s_add_u32 s60, s66, 0\n"
"buffer_load_dwordx4 v22, s[44:47], s60 offen lds\n"
"s_add_u32 m0, s55, 1024\n"
"s_add_u32 s60, s66, 32768\n"
"buffer_load_dwordx4 v22, s[44:47], s60 offen lds\n"
"s_add_u32 m0, s55, 2048\n"
"s_add_u32 s60, s67, 0\n"
"buffer_load_dwordx4 v22, s[44:47], s60 offen lds\n"
"s_add_u32 m0, s55, 3072\n"
"s_add_u32 s60, s67, 32768\n"
"buffer_load_dwordx4 v22, s[44:47], s60 offen lds\n"
"s_add_u32 m0, s55, 4096\n"
"s_add_u32 s60, s68, 0\n"
"buffer_load_dwordx4 v22, s[44:47], s60 offen lds\n"
"s_add_u32 m0, s55, 5120\n"
"s_add_u32 s60, s68, 32768\n"
"buffer_load_dwordx4 v22, s[44:47], s60 offen lds\n"
"s_add_u32 m0, s55, 6144\n"
"s_add_u32 s60, s69, 0\n"
"buffer_load_dwordx4 v22, s[44:47], s60 offen lds\n"
"s_add_u32 m0, s55, 7168\n"
"s_add_u32 s60, s69, 32768\n"
"buffer_load_dwordx4 v22, s[44:47], s60 offen lds\n"
"s_add_u32 m0, s55, 8192\n"
"s_add_u32 s60, s66, 65536\n"
"buffer_load_dwordx4 v22, s[44:47], s60 offen lds\n"
"s_add_u32 m0, s55, 9216\n"
"s_add_u32 s60, s66, 98304\n"
"buffer_load_dwordx4 v22, s[44:47], s60 offen lds\n"
"s_add_u32 m0, s55, 10240\n"
"s_add_u32 s60, s67, 65536\n"
"buffer_load_dwordx4 v22, s[44:47], s60 offen lds\n"
"s_add_u32 m0, s55, 11264\n"
"s_add_u32 s60, s67, 98304\n"
"buffer_load_dwordx4 v22, s[44:47], s60 offen lds\n"
"s_add_u32 m0, s55, 12288\n"
"s_add_u32 s60, s68, 65536\n"
"buffer_load_dwordx4 v22, s[44:47], s60 offen lds\n"
"s_add_u32 m0, s55, 13312\n"
"s_add_u32 s60, s68, 98304\n"
"buffer_load_dwordx4 v22, s[44:47], s60 offen lds\n"
"s_add_u32 m0, s55, 14336\n"
"s_add_u32 s60, s69, 65536\n"
"buffer_load_dwordx4 v22, s[44:47], s60 offen lds\n"
"s_add_u32 m0, s55, 15360\n"
"s_add_u32 s60, s69, 98304\n"
"buffer_load_dwordx4 v22, s[44:47], s60 offen lds\n"
"s_add_u32 m0, s55, 16384\n"
"s_add_u32 s60, s66, 0\n"
"buffer_load_dwordx4 v23, s[44:47], s60 offen lds\n"
"s_add_u32 m0, s55, 17408\n"
"s_add_u32 s60, s66, 32768\n"
"buffer_load_dwordx4 v23, s[44:47], s60 offen lds\n"
"s_add_u32 m0, s55, 18432\n"
"s_add_u32 s60, s67, 0\n"
"buffer_load_dwordx4 v23, s[44:47], s60 offen lds\n"
"s_add_u32 m0, s55, 19456\n"
"s_add_u32 s60, s67, 32768\n"
"buffer_load_dwordx4 v23, s[44:47], s60 offen lds\n"
"s_add_u32 m0, s55, 20480\n"
"s_add_u32 s60, s68, 0\n"
"buffer_load_dwordx4 v23, s[44:47], s60 offen lds\n"
"s_add_u32 m0, s55, 21504\n"
"s_add_u32 s60, s68, 32768\n"
"buffer_load_dwordx4 v23, s[44:47], s60 offen lds\n"
"s_add_u32 m0, s55, 22528\n"
"s_add_u32 s60, s69, 0\n"
"buffer_load_dwordx4 v23, s[44:47], s60 offen lds\n"
"s_add_u32 m0, s55, 23552\n"
"s_add_u32 s60, s69, 32768\n"
"buffer_load_dwordx4 v23, s[44:47], s60 offen lds\n"
"s_add_u32 m0, s55, 24576\n"
"s_add_u32 s60, s66, 65536\n"
"buffer_load_dwordx4 v23, s[44:47], s60 offen lds\n"
"s_add_u32 m0, s55, 25600\n"
"s_add_u32 s60, s66, 98304\n"
"buffer_load_dwordx4 v23, s[44:47], s60 offen lds\n"
"s_add_u32 m0, s55, 26624\n"
"s_add_u32 s60, s67, 65536\n"
"buffer_load_dwordx4 v23, s[44:47], s60 offen lds\n"
"s_add_u32 m0, s55, 27648\n"
"s_add_u32 s60, s67, 98304\n"
"buffer_load_dwordx4 v23, s[44:47], s60 offen lds\n"
"s_add_u32 m0, s55, 28672\n"
"s_add_u32 s60, s68, 65536\n"
"buffer_load_dwordx4 v23, s[44:47], s60 offen lds\n"
"s_add_u32 m0, s55, 29696\n"
"s_add_u32 s60, s68, 98304\n"
"buffer_load_dwordx4 v23, s[44:47], s60 offen lds\n"
"s_add_u32 m0, s55, 30720\n"
"s_add_u32 s60, s69, 65536\n"
"buffer_load_dwordx4 v23, s[44:47], s60 offen lds\n"
"s_add_u32 m0, s55, 31744\n"
"s_add_u32 s60, s69, 98304\n"
"buffer_load_dwordx4 v23, s[44:47], s60 offen lds\n"
"s_add_u32 m0, s55, 32768\n"
"s_add_u32 s60, s66, 0\n"
"buffer_load_dwordx4 v24, s[44:47], s60 offen lds\n"
"s_add_u32 m0, s55, 33792\n"
"s_add_u32 s60, s66, 32768\n"
"buffer_load_dwordx4 v24, s[44:47], s60 offen lds\n"
"s_add_u32 m0, s55, 34816\n"
"s_add_u32 s60, s67, 0\n"
"buffer_load_dwordx4 v24, s[44:47], s60 offen lds\n"
"s_add_u32 m0, s55, 35840\n"
"s_add_u32 s60, s67, 32768\n"
"buffer_load_dwordx4 v24, s[44:47], s60 offen lds\n"
"s_add_u32 m0, s55, 36864\n"
"s_add_u32 s60, s68, 0\n"
"buffer_load_dwordx4 v24, s[44:47], s60 offen lds\n"
"s_add_u32 m0, s55, 37888\n"
"s_add_u32 s60, s68, 32768\n"
"buffer_load_dwordx4 v24, s[44:47], s60 offen lds\n"
"s_add_u32 m0, s55, 38912\n"
"s_add_u32 s60, s69, 0\n"
"buffer_load_dwordx4 v24, s[44:47], s60 offen lds\n"
"s_add_u32 m0, s55, 39936\n"
"s_add_u32 s60, s69, 32768\n"
"buffer_load_dwordx4 v24, s[44:47], s60 offen lds\n"
"s_waitcnt vmcnt(38)\n"
"ds_read_b64_tr_b16 a[0:1], v20 offset:0\n"
"ds_read_b64_tr_b16 a[2:3], v20 offset:256\n"
"ds_read_b64_tr_b16 a[32:33], v21 offset:0\n"
"ds_read_b64_tr_b16 a[34:35], v21 offset:256\n"
"v_add_u32 v0, 2, v0\n"
"v_add_u32 v1, 0x9e3779b9, v1\n"
"v_or_b32 v0, 1, v0\n"
"v_readfirstlane_b32 s49, v1\n"
"v_readfirstlane_b32 s48, v0\n"
"s_waitcnt vmcnt(36)\n"
"ds_read_b64_tr_b16 a[64:65], v20 offset:2048\n"
"ds_read_b64_tr_b16 a[66:67], v20 offset:2304\n"
"ds_read_b64_tr_b16 a[96:97], v21 offset:2048\n"
"ds_read_b64_tr_b16 a[98:99], v21 offset:2304\n"
"s_waitcnt lgkmcnt(4)\n"
"s_add_u32 m0, s55, 0\n"
"s_add_u32 s60, s66, 65536\n"
"buffer_load_dwordx4 v24, s[44:47], s60 offen lds\n"
"s_add_u32 m0, s55, 1024\n"
"s_add_u32 s60, s66, 98304\n"
"buffer_load_dwordx4 v24, s[44:47], s60 offen lds\n"
"s_waitcnt vmcnt(36)\n"
"ds_read_b64_tr_b16 a[128:129], v20 offset:4096\n"
"ds_read_b64_tr_b16 a[130:131], v20 offset:4352\n"
"ds_read_b64_tr_b16 a[160:161], v21 offset:4096\n"
"ds_read_b64_tr_b16 a[162:163], v21 offset:4352\n"
"s_waitcnt lgkmcnt(4)\n"
"s_add_u32 m0, s55, 2048\n"
"s_add_u32 s60, s67, 65536\n"
"buffer_load_dwordx4 v24, s[44:47], s60 offen lds\n"
"s_add_u32 m0, s55, 3072\n"
"s_add_u32 s60, s67, 98304\n"
"buffer_load_dwordx4 v24, s[44:47], s60 offen lds\n"
"s_waitcnt vmcnt(36)\n"
"ds_read_b64_tr_b16 a[192:193], v20 offset:6144\n"
"ds_read_b64_tr_b16 a[194:195], v20 offset:6400\n"
"ds_read_b64_tr_b16 a[224:225], v21 offset:6144\n"
"ds_read_b64_tr_b16 a[226:227], v21 offset:6400\n"
"s_waitcnt lgkmcnt(4)\n"
"s_add_u32 m0, s55, 4096\n"
"s_add_u32 s60, s68, 65536\n"
"buffer_load_dwordx4 v24, s[44:47], s60 offen lds\n"
"s_add_u32 m0, s55, 5120\n"
"s_add_u32 s60, s68, 98304\n"
"buffer_load_dwordx4 v24, s[44:47], s60 offen lds\n"
"s_waitcnt vmcnt(36)\n"
"ds_read_b64_tr_b16 a[4:5], v20 offset:8192\n"
"ds_read_b64_tr_b16 a[6:7], v20 offset:8448\n"
"ds_read_b64_tr_b16 a[36:37], v21 offset:8192\n"
"ds_read_b64_tr_b16 a[38:39], v21 offset:8448\n"
"s_waitcnt lgkmcnt(4)\n"
"s_add_u32 m0, s55, 6144\n"
"s_add_u32 s60, s69, 65536\n"
"buffer_load_dwordx4 v24, s[44:47], s60 offen lds\n"
"s_add_u32 m0, s55, 7168\n"
"s_add_u32 s60, s69, 98304\n"
"buffer_load_dwordx4 v24, s[44:47], s60 offen lds\n"
"s_waitcnt vmcnt(36)\n"
"ds_read_b64_tr_b16 a[68:69], v20 offset:10240\n"
"ds_read_b64_tr_b16 a[70:71], v20 offset:10496\n"
"ds_read_b64_tr_b16 a[100:101], v21 offset:10240\n"
"ds_read_b64_tr_b16 a[102:103], v21 offset:10496\n"
"s_waitcnt lgkmcnt(4)\n"
"s_add_u32 m0, s55, 8192\n"
"s_add_u32 s60, s66, 0\n"
"buffer_load_dwordx4 v25, s[44:47], s60 offen lds\n"
"s_add_u32 m0, s55, 9216\n"
"s_add_u32 s60, s66, 32768\n"
"buffer_load_dwordx4 v25, s[44:47], s60 offen lds\n"
"s_waitcnt vmcnt(36)\n"
"ds_read_b64_tr_b16 a[132:133], v20 offset:12288\n"
"ds_read_b64_tr_b16 a[134:135], v20 offset:12544\n"
"ds_read_b64_tr_b16 a[164:165], v21 offset:12288\n"
"ds_read_b64_tr_b16 a[166:167], v21 offset:12544\n"
"s_waitcnt lgkmcnt(4)\n"
"s_add_u32 m0, s55, 10240\n"
"s_add_u32 s60, s67, 0\n"
"buffer_load_dwordx4 v25, s[44:47], s60 offen lds\n"
"s_add_u32 m0, s55, 11264\n"
"s_add_u32 s60, s67, 32768\n"
"buffer_load_dwordx4 v25, s[44:47], s60 offen lds\n"
"s_waitcnt vmcnt(36)\n"
"ds_read_b64_tr_b16 a[196:197], v20 offset:14336\n"
"ds_read_b64_tr_b16 a[198:199], v20 offset:14592\n"
"ds_read_b64_tr_b16 a[228:229], v21 offset:14336\n"
"ds_read_b64_tr_b16 a[230:231], v21 offset:14592\n"
"s_waitcnt lgkmcnt(4)\n"
"s_add_u32 m0, s55, 12288\n"
"s_add_u32 s60, s68, 0\n"
"buffer_load_dwordx4 v25, s[44:47], s60 offen lds\n"
"s_add_u32 m0, s55, 13312\n"
"s_add_u32 s60, s68, 32768\n"
"buffer_load_dwordx4 v25, s[44:47], s60 offen lds\n"
"s_waitcnt vmcnt(36)\n"
"ds_read_b64_tr_b16 a[8:9], v20 offset:16384\n"
"ds_read_b64_tr_b16 a[10:11], v20 offset:16640\n"
"ds_read_b64_tr_b16 a[40:41], v21 offset:16384\n"
"ds_read_b64_tr_b16 a[42:43], v21 offset:16640\n"
"s_waitcnt lgkmcnt(4)\n"
"s_add_u32 m0, s55, 14336\n"
"s_add_u32 s60, s69, 0\n"
"buffer_load_dwordx4 v25, s[44:47], s60 offen lds\n"
"s_add_u32 m0, s55, 15360\n"
"s_add_u32 s60, s69, 32768\n"
"buffer_load_dwordx4 v25, s[44:47], s60 offen lds\n"
"s_waitcnt vmcnt(36)\n"
"ds_read_b64_tr_b16 a[72:73], v20 offset:18432\n"
"ds_read_b64_tr_b16 a[74:75], v20 offset:18688\n"
"ds_read_b64_tr_b16 a[104:105], v21 offset:18432\n"
"ds_read_b64_tr_b16 a[106:107], v21 offset:18688\n"
"s_waitcnt lgkmcnt(4)\n"
"s_add_u32 m0, s55, 16384\n"
"s_add_u32 s60, s66, 65536\n"
"buffer_load_dwordx4 v25, s[44:47], s60 offen lds\n"
"s_add_u32 m0, s55, 17408\n"
"s_add_u32 s60, s66, 98304\n"
"buffer_load_dwordx4 v25, s[44:47], s60 offen lds\n"
"s_waitcnt vmcnt(36)\n"
"ds_read_b64_tr_b16 a[136:137], v20 offset:20480\n"
"ds_read_b64_tr_b16 a[138:139], v20 offset:20736\n"
"ds_read_b64_tr_b16 a[168:169], v21 offset:20480\n"
"ds_read_b64_tr_b16 a[170:171], v21 offset:20736\n"
"s_waitcnt lgkmcnt(4)\n"
"s_add_u32 m0, s55, 18432\n"
"s_add_u32 s60, s67, 65536\n"
"buffer_load_dwordx4 v25, s[44:47], s60 offen lds\n"
"s_add_u32 m0, s55, 19456\n"
"s_add_u32 s60, s67, 98304\n"
"buffer_load_dwordx4 v25, s[44:47], s60 offen lds\n"
"s_waitcnt vmcnt(36)\n"
"ds_read_b64_tr_b16 a[200:201], v20 offset:22528\n"
"ds_read_b64_tr_b16 a[202:203], v20 offset:22784\n"
"ds_read_b64_tr_b16 a[232:233], v21 offset:22528\n"
"ds_read_b64_tr_b16 a[234:235], v21 offset:22784\n"
"s_waitcnt lgkmcnt(4)\n"
"s_add_u32 m0, s55, 20480\n"
"s_add_u32 s60, s68, 65536\n"
"buffer_load_dwordx4 v25, s[44:47], s60 offen lds\n"
"s_add_u32 m0, s55, 21504\n"
"s_add_u32 s60, s68, 98304\n"
"buffer_load_dwordx4 v25, s[44:47], s60 offen lds\n"
"s_waitcnt vmcnt(36)\n"
"ds_read_b64_tr_b16 a[12:13], v20 offset:24576\n"
"ds_read_b64_tr_b16 a[14:15], v20 offset:24832\n"
"ds_read_b64_tr_b16 a[44:45], v21 offset:24576\n"
"ds_read_b64_tr_b16 a[46:47], v21 offset:24832\n"
"s_waitcnt lgkmcnt(4)\n"
"s_add_u32 m0, s55, 22528\n"
"s_add_u32 s60, s69, 65536\n"
"buffer_load_dwordx4 v25, s[44:47], s60 offen lds\n"
"s_add_u32 m0, s55, 23552\n"
"s_add_u32 s60, s69, 98304\n"
"buffer_load_dwordx4 v25, s[44:47], s60 offen lds\n"
"s_waitcnt vmcnt(36)\n"
"ds_read_b64_tr_b16 a[76:77], v20 offset:26624\n"
"ds_read_b64_tr_b16 a[78:79], v20 offset:26880\n"
"ds_read_b64_tr_b16 a[108:109], v21 offset:26624\n"
"ds_read_b64_tr_b16 a[110:111], v21 offset:26880\n"
"s_waitcnt lgkmcnt(4)\n"
"s_waitcnt vmcnt(34)\n"
"ds_read_b64_tr_b16 a[140:141], v20 offset:28672\n"
"ds_read_b64_tr_b16 a[142:143], v20 offset:28928\n"
"ds_read_b64_tr_b16 a[172:173], v21 offset:28672\n"
"ds_read_b64_tr_b16 a[174:175], v21 offset:28928\n"
"s_waitcnt lgkmcnt(4)\n"
"s_waitcnt vmcnt(32)\n"
"ds_read_b64_tr_b16 a[204:205], v20 offset:30720\n"
"ds_read_b64_tr_b16 a[206:207], v20 offset:30976\n"
"ds_read_b64_tr_b16 a[236:237], v21 offset:30720\n"
"ds_read_b64_tr_b16 a[238:239], v21 offset:30976\n"
"s_waitcnt lgkmcnt(4)\n"
"s_waitcnt vmcnt(30)\n"
"ds_read_b64_tr_b16 a[16:17], v20 offset:32768\n"
"ds_read_b64_tr_b16 a[18:19], v20 offset:33024\n"
"ds_read_b64_tr_b16 a[48:49], v21 offset:32768\n"
"ds_read_b64_tr_b16 a[50:51], v21 offset:33024\n"
"s_waitcnt lgkmcnt(4)\n"
"s_waitcnt vmcnt(28)\n"
"ds_read_b64_tr_b16 a[80:81], v20 offset:34816\n"
"ds_read_b64_tr_b16 a[82:83], v20 offset:35072\n"
"ds_read_b64_tr_b16 a[112:113], v21 offset:34816\n"
"ds_read_b64_tr_b16 a[114:115], v21 offset:35072\n"
"s_waitcnt lgkmcnt(4)\n"
"s_waitcnt vmcnt(26)\n"
"ds_read_b64_tr_b16 a[144:145], v20 offset:36864\n"
"ds_read_b64_tr_b16 a[146:147], v20 offset:37120\n"
"ds_read_b64_tr_b16 a[176:177], v21 offset:36864\n"
"ds_read_b64_tr_b16 a[178:179], v21 offset:37120\n"
"s_waitcnt lgkmcnt(4)\n"
"s_waitcnt vmcnt(24)\n"
"ds_read_b64_tr_b16 a[208:209], v20 offset:38912\n"
"ds_read_b64_tr_b16 a[210:211], v20 offset:39168\n"
"ds_read_b64_tr_b16 a[240:241], v21 offset:38912\n"
"ds_read_b64_tr_b16 a[242:243], v21 offset:39168\n"
"s_waitcnt lgkmcnt(4)\n"
"s_waitcnt vmcnt(22)\n"
"ds_read_b64_tr_b16 a[20:21], v20 offset:0\n"
"ds_read_b64_tr_b16 a[22:23], v20 offset:256\n"
"ds_read_b64_tr_b16 a[52:53], v21 offset:0\n"
"ds_read_b64_tr_b16 a[54:55], v21 offset:256\n"
"s_waitcnt lgkmcnt(4)\n"
"s_waitcnt vmcnt(20)\n"
"ds_read_b64_tr_b16 a[84:85], v20 offset:2048\n"
"ds_read_b64_tr_b16 a[86:87], v20 offset:2304\n"
"ds_read_b64_tr_b16 a[116:117], v21 offset:2048\n"
"ds_read_b64_tr_b16 a[118:119], v21 offset:2304\n"
"s_waitcnt lgkmcnt(4)\n"
"s_waitcnt vmcnt(18)\n"
"ds_read_b64_tr_b16 a[148:149], v20 offset:4096\n"
"ds_read_b64_tr_b16 a[150:151], v20 offset:4352\n"
"ds_read_b64_tr_b16 a[180:181], v21 offset:4096\n"
"ds_read_b64_tr_b16 a[182:183], v21 offset:4352\n"
"s_waitcnt lgkmcnt(4)\n"
"s_waitcnt vmcnt(16)\n"
"ds_read_b64_tr_b16 a[212:213], v20 offset:6144\n"
"ds_read_b64_tr_b16 a[214:215], v20 offset:6400\n"
"ds_read_b64_tr_b16 a[244:245], v21 offset:6144\n"
"ds_read_b64_tr_b16 a[246:247], v21 offset:6400\n"
"s_waitcnt lgkmcnt(4)\n"
"s_waitcnt vmcnt(14)\n"
"ds_read_b64_tr_b16 a[24:25], v20 offset:8192\n"
"ds_read_b64_tr_b16 a[26:27], v20 offset:8448\n"
"ds_read_b64_tr_b16 a[56:57], v21 offset:8192\n"
"ds_read_b64_tr_b16 a[58:59], v21 offset:8448\n"
"s_waitcnt lgkmcnt(4)\n"
"s_waitcnt vmcnt(12)\n"
"ds_read_b64_tr_b16 a[88:89], v20 offset:10240\n"
"ds_read_b64_tr_b16 a[90:91], v20 offset:10496\n"
"ds_read_b64_tr_b16 a[120:121], v21 offset:10240\n"
"ds_read_b64_tr_b16 a[122:123], v21 offset:10496\n"
"s_waitcnt lgkmcnt(4)\n"
"s_waitcnt vmcnt(10)\n"
"ds_read_b64_tr_b16 a[152:153], v20 offset:12288\n"
"ds_read_b64_tr_b16 a[154:155], v20 offset:12544\n"
"ds_read_b64_tr_b16 a[184:185], v21 offset:12288\n"
"ds_read_b64_tr_b16 a[186:187], v21 offset:12544\n"
"s_waitcnt lgkmcnt(4)\n"
"s_waitcnt vmcnt(8)\n"
"ds_read_b64_tr_b16 a[216:217], v20 offset:14336\n"
"ds_read_b64_tr_b16 a[218:219], v20 offset:14592\n"
"ds_read_b64_tr_b16 a[248:249], v21 offset:14336\n"
"ds_read_b64_tr_b16 a[250:251], v21 offset:14592\n"
"s_waitcnt lgkmcnt(4)\n"
"s_waitcnt vmcnt(6)\n"
"ds_read_b64_tr_b16 a[28:29], v20 offset:16384\n"
"ds_read_b64_tr_b16 a[30:31], v20 offset:16640\n"
"ds_read_b64_tr_b16 a[60:61], v21 offset:16384\n"
"ds_read_b64_tr_b16 a[62:63], v21 offset:16640\n"
"s_waitcnt lgkmcnt(4)\n"
"s_waitcnt vmcnt(4)\n"
"ds_read_b64_tr_b16 a[92:93], v20 offset:18432\n"
"ds_read_b64_tr_b16 a[94:95], v20 offset:18688\n"
"ds_read_b64_tr_b16 a[124:125], v21 offset:18432\n"
"ds_read_b64_tr_b16 a[126:127], v21 offset:18688\n"
"s_waitcnt lgkmcnt(4)\n"
"s_waitcnt vmcnt(2)\n"
"ds_read_b64_tr_b16 a[156:157], v20 offset:20480\n"
"ds_read_b64_tr_b16 a[158:159], v20 offset:20736\n"
"ds_read_b64_tr_b16 a[188:189], v21 offset:20480\n"
"ds_read_b64_tr_b16 a[190:191], v21 offset:20736\n"
"s_waitcnt lgkmcnt(4)\n"
"s_waitcnt vmcnt(0)\n"
"ds_read_b64_tr_b16 a[220:221], v20 offset:22528\n"
"ds_read_b64_tr_b16 a[222:223], v20 offset:22784\n"
"ds_read_b64_tr_b16 a[252:253], v21 offset:22528\n"
"ds_read_b64_tr_b16 a[254:255], v21 offset:22784\n"
"s_waitcnt lgkmcnt(4)\n"
"s_waitcnt lgkmcnt(0)\n"
"s_mov_b32 m0, s59\n"
"s_barrier\n"
"v_mov_b32 v124, 0\n"
"v_mov_b32 v125, 0\n"
"v_mov_b32 v120, 0\n"
"v_mov_b32 v121, 0\n"
"v_mov_b32 v124, 0\n"
"v_mov_b32 v125, 0\n"
"v_mov_b32 v120, 0\n"
"v_mov_b32 v121, 0\n"
"s_mov_b32 s93, 0x80000000\n"
"s_mov_b32 s95, 0x80000000\n"
"s_mov_b32 s94, 0x80000000\n"
"s_mov_b32 s95, 0x80000000\n"
"s_mov_b32 s93, 0x80000000\n"
"s_mov_b32 s95, 0x80000000\n"
"s_mov_b32 s94, 0x80000000\n"
"s_mov_b32 s95, 0x80000000\n"
"s_cmp_eq_u32 s54, 0\n"
"s_cbranch_scc1 .LO_1s\n"
"s_cmp_eq_u32 s54, 1\n"
"s_cbranch_scc1 .LO_2s\n"
"s_cmp_eq_u32 s54, 2\n"
"s_cbranch_scc1 .LO_3s\n"
"s_cmp_eq_u32 s54, 3\n"
"s_cbranch_scc1 .LO_4s\n"
".p2align 6\n"
".LO_top:\n"
"v_mfma_f32_16x16x32_bf16 v[40:43], v[84:87], a[0:3], 0\n"
"global_load_dwordx4 v[20:23], v6, s[56:57] offset:1024 sc1\n"
"v_mfma_f32_16x16x32_bf16 v[40:43], v[88:91], a[4:7], v[40:43]\n"
"s_waitcnt vmcnt(13)\n"
"v_mfma_f32_16x16x32_bf16 v[40:43], v[92:95], a[8:11], v[40:43]\n"
"s_waitcnt lgkmcnt(3)\n"
"v_add_f32 v72, v72, v44\n"
"v_add_f32 v73, v73, v45\n"
"v_mfma_f32_16x16x32_bf16 v[40:43], v[52:55], a[12:15], v[40:43]\n"
"s_waitcnt vmcnt(11)\n"
"v_add_f32 v74, v74, v46\n"
"v_add_f32 v75, v75, v47\n"
"v_mfma_f32_16x16x32_bf16 v[40:43], v[56:59], a[16:19], v[40:43]\n"
"v_add_f32 v72, v72, v48\n"
"v_add_f32 v73, v73, v49\n"
"v_mfma_f32_16x16x32_bf16 v[40:43], v[60:63], a[20:23], v[40:43]\n"
"s_waitcnt vmcnt(8)\n"
"global_load_dwordx4 v[24:27], v7, s[56:57] offset:0 sc1\n"
"v_add_f32 v74, v74, v50\n"
"v_mfma_f32_16x16x32_bf16 v[40:43], v[64:67], a[24:27], v[40:43]\n"
"v_add_f32 v75, v75, v51\n"
"v_add_f32 v72, v72, v112\n"
"v_mfma_f32_16x16x32_bf16 v[40:43], v[68:71], a[28:31], v[40:43]\n"
"v_add_f32 v73, v73, v113\n"
"v_add_f32 v74, v74, v114\n"
"v_mfma_f32_16x16x32_bf16 v[44:47], v[84:87], a[32:35], 0\n"
"global_load_dwordx4 v[28:31], v7, s[56:57] offset:1024 sc1\n"
"v_add_f32 v75, v75, v115\n"
"v_mfma_f32_16x16x32_bf16 v[44:47], v[88:91], a[36:39], v[44:47]\n"
"s_add_u32 s94, s50, 0xffd00000\n"
"s_and_b32 s95, s94, 0xfbffffff\n"
"ds_write_addtid_b32 v40 offset:0\n"
"v_mfma_f32_16x16x32_bf16 v[44:47], v[92:95], a[40:43], v[44:47]\n"
"ds_write_addtid_b32 v41 offset:4640\n"
"v_cvt_pk_bf16_f32 v48, v72, v73\n"
"v_mfma_f32_16x16x32_bf16 v[44:47], v[52:55], a[44:47], v[44:47]\n"
"ds_write_addtid_b32 v42 offset:9344\n"
"v_cvt_pk_bf16_f32 v49, v74, v75\n"
"v_mfma_f32_16x16x32_bf16 v[44:47], v[56:59], a[48:51], v[44:47]\n"
"s_waitcnt lgkmcnt(3)\n"
"ds_write_addtid_b32 v43 offset:13984\n"
"v_add_f32 v76, v76, v80\n"
"v_mfma_f32_16x16x32_bf16 v[44:47], v[60:63], a[52:55], v[44:47]\n"
"global_load_dwordx4 v[32:35], v8, s[56:57] offset:0 sc1\n"
"v_add_f32 v77, v77, v81\n"
"v_mfma_f32_16x16x32_bf16 v[44:47], v[64:67], a[56:59], v[44:47]\n"
"v_add_f32 v78, v78, v82\n"
"v_add_f32 v79, v79, v83\n"
"v_mfma_f32_16x16x32_bf16 v[44:47], v[68:71], a[60:63], v[44:47]\n"
"v_add_f32 v76, v76, v120\n"
"v_add_f32 v77, v77, v121\n"
"v_mfma_f32_16x16x32_bf16 v[40:43], v[84:87], a[64:67], 0\n"
"v_add_f32 v78, v78, v122\n"
"v_add_f32 v79, v79, v123\n"
"v_mfma_f32_16x16x32_bf16 v[40:43], v[88:91], a[68:71], v[40:43]\n"
"ds_write_addtid_b32 v44 offset:256\n"
"v_add_f32 v76, v76, v108\n"
"v_mfma_f32_16x16x32_bf16 v[40:43], v[92:95], a[72:75], v[40:43]\n"
"s_waitcnt vmcnt(9)\n"
"buffer_store_dwordx2 v[0:1], v12, s[40:43], s93 offen offset:0 sc1\n"
"ds_write_addtid_b32 v45 offset:4896\n"
"v_mfma_f32_16x16x32_bf16 v[40:43], v[52:55], a[76:79], v[40:43]\n"
"ds_write_addtid_b32 v46 offset:9600\n"
"v_add_f32 v77, v77, v109\n"
"v_mfma_f32_16x16x32_bf16 v[40:43], v[56:59], a[80:83], v[40:43]\n"
"s_waitcnt vmcnt(9)\n"
"ds_write_addtid_b32 v47 offset:14240\n"
"global_load_dwordx4 v[36:39], v8, s[56:57] offset:1024 sc1\n"
"v_mfma_f32_16x16x32_bf16 v[40:43], v[60:63], a[84:87], v[40:43]\n"
"v_add_f32 v78, v78, v110\n"
"v_add_f32 v79, v79, v111\n"
"v_mfma_f32_16x16x32_bf16 v[40:43], v[64:67], a[88:91], v[40:43]\n"
"v_cvt_pk_bf16_f32 v50, v76, v77\n"
"v_cvt_pk_bf16_f32 v51, v78, v79\n"
"v_mfma_f32_16x16x32_bf16 v[40:43], v[68:71], a[92:95], v[40:43]\n"
"v_lshlrev_b32 v72, 16, v116\n"
"v_and_b32 v73, 0xffff0000, v116\n"
"v_mfma_f32_16x16x32_bf16 v[44:47], v[84:87], a[96:99], 0\n"
"v_lshlrev_b32 v74, 16, v117\n"
"v_and_b32 v75, 0xffff0000, v117\n"
"v_mfma_f32_16x16x32_bf16 v[44:47], v[88:91], a[100:103], v[44:47]\n"
"ds_write_addtid_b32 v40 offset:512\n"
"v_lshlrev_b32 v76, 16, v118\n"
"v_mfma_f32_16x16x32_bf16 v[44:47], v[92:95], a[104:107], v[44:47]\n"
"ds_write_addtid_b32 v41 offset:5152\n"
"v_and_b32 v77, 0xffff0000, v118\n"
"v_mfma_f32_16x16x32_bf16 v[44:47], v[52:55], a[108:111], v[44:47]\n"
"ds_write_addtid_b32 v42 offset:9856\n"
"v_lshlrev_b32 v78, 16, v119\n"
"v_mfma_f32_16x16x32_bf16 v[44:47], v[56:59], a[112:115], v[44:47]\n"
"ds_write_addtid_b32 v43 offset:14496\n"
"v_and_b32 v79, 0xffff0000, v119\n"
"v_mfma_f32_16x16x32_bf16 v[44:47], v[60:63], a[116:119], v[44:47]\n"
"v_mfma_f32_16x16x32_bf16 v[44:47], v[64:67], a[120:123], v[44:47]\n"
"v_mfma_f32_16x16x32_bf16 v[44:47], v[68:71], a[124:127], v[44:47]\n"
"v_mfma_f32_16x16x32_bf16 v[40:43], v[84:87], a[128:131], 0\n"
"v_mfma_f32_16x16x32_bf16 v[40:43], v[88:91], a[132:135], v[40:43]\n"
"ds_write_addtid_b32 v44 offset:768\n"
"v_mfma_f32_16x16x32_bf16 v[40:43], v[92:95], a[136:139], v[40:43]\n"
"buffer_store_dwordx4 v[48:51], v11, s[40:43], s95 offen sc1\n"
"ds_write_addtid_b32 v45 offset:5408\n"
"v_mfma_f32_16x16x32_bf16 v[80:83], v[48:51], v[48:51], 0\n"
"v_mfma_f32_16x16x32_bf16 v[108:111], v[14:17], v[48:51], 0\n"
"v_mfma_f32_16x16x32_bf16 v[40:43], v[52:55], a[140:143], v[40:43]\n"
"ds_write_addtid_b32 v46 offset:10112\n"
"v_cndmask_b32_e64 v80, v80, v81, s[62:63]\n"
"v_mfma_f32_16x16x32_bf16 v[40:43], v[56:59], a[144:147], v[40:43]\n"
"ds_write_addtid_b32 v47 offset:14752\n"
"v_cndmask_b32_e64 v82, v82, v83, s[62:63]\n"
"v_mfma_f32_16x16x32_bf16 v[40:43], v[60:63], a[148:151], v[40:43]\n"
"global_load_dwordx4 v[112:115], v10, s[56:57] sc1\n"
"v_cndmask_b32_e64 v109, v80, v82, s[64:65]\n"
"v_mfma_f32_16x16x32_bf16 v[40:43], v[64:67], a[152:155], v[40:43]\n"
"buffer_store_dwordx2 v[108:109], v13, s[40:43], s95 offen offset:0 sc1\n"
"v_mfma_f32_16x16x32_bf16 v[40:43], v[68:71], a[156:159], v[40:43]\n"
"v_mfma_f32_16x16x32_bf16 v[48:51], v[84:87], a[160:163], 0\n"
"s_add_u32 s58, s50, 0x180000\n"
"v_mfma_f32_16x16x32_bf16 v[48:51], v[88:91], a[164:167], v[48:51]\n"
"s_and_b32 s58, s58, 0x3ffffff\n"
"ds_write_addtid_b32 v40 offset:1024\n"
"v_mfma_f32_16x16x32_bf16 v[48:51], v[92:95], a[168:171], v[48:51]\n"
"s_add_u32 s56, s40, s58\n"
"ds_write_addtid_b32 v41 offset:5664\n"
"v_mfma_f32_16x16x32_bf16 v[48:51], v[52:55], a[172:175], v[48:51]\n"
"s_addc_u32 s57, s41, 0\n"
"ds_write_addtid_b32 v42 offset:10368\n"
"v_mfma_f32_16x16x32_bf16 v[48:51], v[56:59], a[176:179], v[48:51]\n"
"s_waitcnt vmcnt(10)\n"
"ds_write_addtid_b32 v43 offset:15008\n"
"v_cmp_ne_u64 vcc, s[48:49], v[124:125]\n"
"v_mfma_f32_16x16x32_bf16 v[48:51], v[60:63], a[180:183], v[48:51]\n"
"v_mfma_f32_16x16x32_bf16 v[48:51], v[64:67], a[184:187], v[48:51]\n"
"s_cbranch_vccnz .LO_3s\n"
".LO_3r:\n"
"v_mfma_f32_16x16x32_bf16 v[48:51], v[68:71], a[188:191], v[48:51]\n"
"s_add_u32 s50, s50, 0x180000\n"
"global_load_dwordx4 v[40:43], v5, s[56:57] offset:0 sc1\n"
"v_mfma_f32_16x16x32_bf16 v[72:75], a[192:195], v[84:87], v[72:75]\n"
"v_mfma_f32_16x16x32_bf16 v[72:75], a[196:199], v[88:91], v[72:75]\n"
"ds_write_addtid_b32 v48 offset:1280\n"
"v_mfma_f32_16x16x32_bf16 v[72:75], a[200:203], v[92:95], v[72:75]\n"
"ds_write_addtid_b32 v49 offset:5920\n"
"v_mfma_f32_16x16x32_bf16 v[72:75], a[204:207], v[52:55], v[72:75]\n"
"ds_write_addtid_b32 v50 offset:10624\n"
"v_mfma_f32_16x16x32_bf16 v[72:75], a[208:211], v[56:59], v[72:75]\n"
"ds_write_addtid_b32 v51 offset:15264\n"
"global_load_dwordx4 v[44:47], v5, s[56:57] offset:1024 sc1\n"
"v_mfma_f32_16x16x32_bf16 v[72:75], a[212:215], v[60:63], v[72:75]\n"
"v_mfma_f32_16x16x32_bf16 v[72:75], a[216:219], v[64:67], v[72:75]\n"
"v_mfma_f32_16x16x32_bf16 v[72:75], a[220:223], v[68:71], v[72:75]\n"
"v_mfma_f32_16x16x32_bf16 v[76:79], a[224:227], v[84:87], v[76:79]\n"
"s_waitcnt lgkmcnt(0)\n"
"v_mfma_f32_16x16x32_bf16 v[76:79], a[228:231], v[88:91], v[76:79]\n"
"s_barrier\n"
"v_mfma_f32_16x16x32_bf16 v[76:79], a[232:235], v[92:95], v[76:79]\n"
"ds_read_b128 v[80:83], v2 offset:1536\n"
"ds_read_b128 v[84:87], v3 offset:512\n"
"v_mfma_f32_16x16x32_bf16 v[76:79], a[236:239], v[52:55], v[76:79]\n"
"buffer_load_dwordx2 v[120:121], v9, s[40:43], s50 offen sc1\n"
"v_mfma_f32_16x16x32_bf16 v[76:79], a[240:243], v[56:59], v[76:79]\n"
"global_load_dwordx4 v[48:51], v6, s[56:57] offset:0 sc1\n"
"ds_read_b128 v[88:91], v4 offset:2560\n"
"v_mfma_f32_16x16x32_bf16 v[76:79], a[244:247], v[60:63], v[76:79]\n"
"ds_read_b128 v[92:95], v2 offset:1792\n"
"ds_read_b128 v[108:111], v3 offset:768\n"
"v_mfma_f32_16x16x32_bf16 v[76:79], a[248:251], v[64:67], v[76:79]\n"
"global_load_dwordx4 v[52:55], v6, s[56:57] offset:1024 sc1\n"
"ds_read_b128 v[116:119], v4 offset:2816\n"
"v_mfma_f32_16x16x32_bf16 v[76:79], a[252:255], v[68:71], v[76:79]\n"
"v_mfma_f32_16x16x32_bf16 v[64:67], v[96:99], a[0:3], 0\n"
"v_mfma_f32_16x16x32_bf16 v[64:67], v[100:103], a[4:7], v[64:67]\n"
"s_waitcnt vmcnt(13)\n"
"v_mfma_f32_16x16x32_bf16 v[64:67], v[104:107], a[8:11], v[64:67]\n"
"s_waitcnt lgkmcnt(3)\n"
"v_add_f32 v80, v80, v84\n"
"v_add_f32 v81, v81, v85\n"
"v_mfma_f32_16x16x32_bf16 v[64:67], v[20:23], a[12:15], v[64:67]\n"
"s_waitcnt vmcnt(11)\n"
"global_load_dwordx4 v[56:59], v7, s[56:57] offset:0 sc1\n"
"v_add_f32 v82, v82, v86\n"
"v_mfma_f32_16x16x32_bf16 v[64:67], v[24:27], a[16:19], v[64:67]\n"
"v_add_f32 v83, v83, v87\n"
"v_add_f32 v80, v80, v88\n"
"v_mfma_f32_16x16x32_bf16 v[64:67], v[28:31], a[20:23], v[64:67]\n"
"s_waitcnt vmcnt(9)\n"
"v_add_f32 v81, v81, v89\n"
"v_add_f32 v82, v82, v90\n"
"v_mfma_f32_16x16x32_bf16 v[64:67], v[32:35], a[24:27], v[64:67]\n"
"v_add_f32 v83, v83, v91\n"
"v_add_f32 v80, v80, v72\n"
"v_mfma_f32_16x16x32_bf16 v[64:67], v[36:39], a[28:31], v[64:67]\n"
"v_add_f32 v81, v81, v73\n"
"v_add_f32 v82, v82, v74\n"
"v_mfma_f32_16x16x32_bf16 v[84:87], v[96:99], a[32:35], 0\n"
"v_add_f32 v83, v83, v75\n"
"v_mfma_f32_16x16x32_bf16 v[84:87], v[100:103], a[36:39], v[84:87]\n"
"s_add_u32 s93, s50, 0xffd00000\n"
"s_and_b32 s95, s93, 0xfbffffff\n"
"ds_write_addtid_b32 v64 offset:1536\n"
"v_mfma_f32_16x16x32_bf16 v[84:87], v[104:107], a[40:43], v[84:87]\n"
"ds_write_addtid_b32 v65 offset:6176\n"
"v_cvt_pk_bf16_f32 v72, v80, v81\n"
"v_mfma_f32_16x16x32_bf16 v[84:87], v[20:23], a[44:47], v[84:87]\n"
"ds_write_addtid_b32 v66 offset:10880\n"
"global_load_dwordx4 v[60:63], v7, s[56:57] offset:1024 sc1\n"
"v_mfma_f32_16x16x32_bf16 v[84:87], v[24:27], a[48:51], v[84:87]\n"
"ds_write_addtid_b32 v67 offset:15520\n"
"v_cvt_pk_bf16_f32 v73, v82, v83\n"
"v_mfma_f32_16x16x32_bf16 v[84:87], v[28:31], a[52:55], v[84:87]\n"
"s_waitcnt lgkmcnt(4)\n"
"v_add_f32 v92, v92, v108\n"
"v_add_f32 v93, v93, v109\n"
"v_mfma_f32_16x16x32_bf16 v[84:87], v[32:35], a[56:59], v[84:87]\n"
"v_add_f32 v94, v94, v110\n"
"v_add_f32 v95, v95, v111\n"
"v_mfma_f32_16x16x32_bf16 v[84:87], v[36:39], a[60:63], v[84:87]\n"
"v_add_f32 v92, v92, v116\n"
"v_add_f32 v93, v93, v117\n"
"v_mfma_f32_16x16x32_bf16 v[80:83], v[96:99], a[64:67], 0\n"
"global_load_dwordx4 v[64:67], v8, s[56:57] offset:0 sc1\n"
"v_add_f32 v94, v94, v118\n"
"v_mfma_f32_16x16x32_bf16 v[80:83], v[100:103], a[68:71], v[80:83]\n"
"ds_write_addtid_b32 v84 offset:1792\n"
"global_load_dwordx4 v[68:71], v8, s[56:57] offset:1024 sc1\n"
"v_mfma_f32_16x16x32_bf16 v[80:83], v[104:107], a[72:75], v[80:83]\n"
"ds_write_addtid_b32 v85 offset:6432\n"
"v_add_f32 v95, v95, v119\n"
"v_mfma_f32_16x16x32_bf16 v[80:83], v[20:23], a[76:79], v[80:83]\n"
"ds_write_addtid_b32 v86 offset:11136\n"
"v_add_f32 v92, v92, v76\n"
"v_mfma_f32_16x16x32_bf16 v[80:83], v[24:27], a[80:83], v[80:83]\n"
"ds_write_addtid_b32 v87 offset:15776\n"
"v_add_f32 v93, v93, v77\n"
"v_mfma_f32_16x16x32_bf16 v[80:83], v[28:31], a[84:87], v[80:83]\n"
"s_waitcnt vmcnt(10)\n"
"v_add_f32 v94, v94, v78\n"
"v_add_f32 v95, v95, v79\n"
"v_mfma_f32_16x16x32_bf16 v[80:83], v[32:35], a[88:91], v[80:83]\n"
"s_waitcnt vmcnt(9)\n"
"v_cvt_pk_bf16_f32 v74, v92, v93\n"
"v_cvt_pk_bf16_f32 v75, v94, v95\n"
"v_mfma_f32_16x16x32_bf16 v[80:83], v[36:39], a[92:95], v[80:83]\n"
"buffer_store_dwordx4 v[72:75], v11, s[40:43], s95 offen sc1\n"
"v_lshlrev_b32 v84, 16, v112\n"
"v_and_b32 v85, 0xffff0000, v112\n"
"v_lshlrev_b32 v86, 16, v113\n"
"v_mfma_f32_16x16x32_bf16 v[76:79], v[72:75], v[72:75], 0\n"
"buffer_store_dwordx2 v[0:1], v12, s[40:43], s94 offen offset:0 sc1\n"
"v_and_b32 v87, 0xffff0000, v113\n"
"v_mfma_f32_16x16x32_bf16 v[88:91], v[14:17], v[72:75], 0\n"
"v_lshlrev_b32 v92, 16, v114\n"
"v_and_b32 v93, 0xffff0000, v114\n"
"v_mfma_f32_16x16x32_bf16 v[108:111], v[96:99], a[96:99], 0\n"
"v_cndmask_b32_e64 v76, v76, v77, s[62:63]\n"
"v_cndmask_b32_e64 v78, v78, v79, s[62:63]\n"
"v_mfma_f32_16x16x32_bf16 v[108:111], v[100:103], a[100:103], v[108:111]\n"
"ds_write_addtid_b32 v80 offset:2048\n"
"v_cndmask_b32_e64 v89, v76, v78, s[64:65]\n"
"v_mfma_f32_16x16x32_bf16 v[108:111], v[104:107], a[104:107], v[108:111]\n"
"buffer_store_dwordx2 v[88:89], v13, s[40:43], s95 offen offset:0 sc1\n"
"ds_write_addtid_b32 v81 offset:6688\n"
"v_mfma_f32_16x16x32_bf16 v[108:111], v[20:23], a[108:111], v[108:111]\n"
"ds_write_addtid_b32 v82 offset:11392\n"
"v_lshlrev_b32 v94, 16, v115\n"
"v_mfma_f32_16x16x32_bf16 v[108:111], v[24:27], a[112:115], v[108:111]\n"
"ds_write_addtid_b32 v83 offset:16032\n"
"v_and_b32 v95, 0xffff0000, v115\n"
"v_mfma_f32_16x16x32_bf16 v[108:111], v[28:31], a[116:119], v[108:111]\n"
"v_mfma_f32_16x16x32_bf16 v[108:111], v[32:35], a[120:123], v[108:111]\n"
"v_mfma_f32_16x16x32_bf16 v[108:111], v[36:39], a[124:127], v[108:111]\n"
"v_mfma_f32_16x16x32_bf16 v[72:75], v[96:99], a[128:131], 0\n"
"v_mfma_f32_16x16x32_bf16 v[72:75], v[100:103], a[132:135], v[72:75]\n"
"ds_write_addtid_b32 v108 offset:2304\n"
"v_mfma_f32_16x16x32_bf16 v[72:75], v[104:107], a[136:139], v[72:75]\n"
"ds_write_addtid_b32 v109 offset:6944\n"
"v_mfma_f32_16x16x32_bf16 v[72:75], v[20:23], a[140:143], v[72:75]\n"
"ds_write_addtid_b32 v110 offset:11648\n"
"v_mfma_f32_16x16x32_bf16 v[72:75], v[24:27], a[144:147], v[72:75]\n"
"ds_write_addtid_b32 v111 offset:16288\n"
"v_mfma_f32_16x16x32_bf16 v[72:75], v[28:31], a[148:151], v[72:75]\n"
"v_mfma_f32_16x16x32_bf16 v[72:75], v[32:35], a[152:155], v[72:75]\n"
"global_load_dwordx4 v[88:91], v10, s[56:57] sc1\n"
"v_mfma_f32_16x16x32_bf16 v[72:75], v[36:39], a[156:159], v[72:75]\n"
"v_mfma_f32_16x16x32_bf16 v[76:79], v[96:99], a[160:163], 0\n"
"v_mfma_f32_16x16x32_bf16 v[76:79], v[100:103], a[164:167], v[76:79]\n"
"ds_write_addtid_b32 v72 offset:2560\n"
"v_mfma_f32_16x16x32_bf16 v[76:79], v[104:107], a[168:171], v[76:79]\n"
"s_add_u32 s58, s50, 0x180000\n"
"ds_write_addtid_b32 v73 offset:7200\n"
"v_mfma_f32_16x16x32_bf16 v[76:79], v[20:23], a[172:175], v[76:79]\n"
"s_and_b32 s58, s58, 0x3ffffff\n"
"ds_write_addtid_b32 v74 offset:11904\n"
"v_mfma_f32_16x16x32_bf16 v[76:79], v[24:27], a[176:179], v[76:79]\n"
"s_add_u32 s56, s40, s58\n"
"ds_write_addtid_b32 v75 offset:16544\n"
"v_mfma_f32_16x16x32_bf16 v[76:79], v[28:31], a[180:183], v[76:79]\n"
"s_addc_u32 s57, s41, 0\n"
"v_mfma_f32_16x16x32_bf16 v[76:79], v[32:35], a[184:187], v[76:79]\n"
"s_waitcnt vmcnt(10)\n"
"v_cmp_ne_u64 vcc, s[48:49], v[120:121]\n"
"v_mfma_f32_16x16x32_bf16 v[76:79], v[36:39], a[188:191], v[76:79]\n"
"v_mfma_f32_16x16x32_bf16 v[84:87], a[192:195], v[96:99], v[84:87]\n"
"s_cbranch_vccnz .LO_4s\n"
".LO_4r:\n"
"v_mfma_f32_16x16x32_bf16 v[84:87], a[196:199], v[100:103], v[84:87]\n"
"s_add_u32 s50, s50, 0x180000\n"
"ds_write_addtid_b32 v76 offset:2816\n"
"v_mfma_f32_16x16x32_bf16 v[84:87], a[200:203], v[104:107], v[84:87]\n"
"ds_write_addtid_b32 v77 offset:7456\n"
"global_load_dwordx4 v[72:75], v5, s[56:57] offset:0 sc1\n"
"v_mfma_f32_16x16x32_bf16 v[84:87], a[204:207], v[20:23], v[84:87]\n"
"ds_write_addtid_b32 v78 offset:12160\n"
"v_mfma_f32_16x16x32_bf16 v[84:87], a[208:211], v[24:27], v[84:87]\n"
"ds_write_addtid_b32 v79 offset:16800\n"
"v_mfma_f32_16x16x32_bf16 v[84:87], a[212:215], v[28:31], v[84:87]\n"
"v_mfma_f32_16x16x32_bf16 v[84:87], a[216:219], v[32:35], v[84:87]\n"
"v_mfma_f32_16x16x32_bf16 v[84:87], a[220:223], v[36:39], v[84:87]\n"
"global_load_dwordx4 v[76:79], v5, s[56:57] offset:1024 sc1\n"
"v_mfma_f32_16x16x32_bf16 v[92:95], a[224:227], v[96:99], v[92:95]\n"
"s_waitcnt lgkmcnt(0)\n"
"global_load_dwordx4 v[80:83], v6, s[56:57] offset:0 sc1\n"
"v_mfma_f32_16x16x32_bf16 v[92:95], a[228:231], v[100:103], v[92:95]\n"
"s_barrier\n"
"v_mfma_f32_16x16x32_bf16 v[92:95], a[232:235], v[104:107], v[92:95]\n"
"ds_read_b128 v[96:99], v2 offset:0\n"
"ds_read_b128 v[100:103], v3 offset:2048\n"
"v_mfma_f32_16x16x32_bf16 v[92:95], a[236:239], v[20:23], v[92:95]\n"
"ds_read_b128 v[104:107], v4 offset:1024\n"
"ds_read_b128 v[108:111], v2 offset:256\n"
"v_mfma_f32_16x16x32_bf16 v[92:95], a[240:243], v[24:27], v[92:95]\n"
"ds_read_b128 v[112:115], v3 offset:2304\n"
"ds_read_b128 v[116:119], v4 offset:1280\n"
"v_mfma_f32_16x16x32_bf16 v[92:95], a[244:247], v[28:31], v[92:95]\n"
"buffer_load_dwordx2 v[124:125], v9, s[40:43], s50 offen sc1\n"
"v_mfma_f32_16x16x32_bf16 v[92:95], a[248:251], v[32:35], v[92:95]\n"
"v_mfma_f32_16x16x32_bf16 v[92:95], a[252:255], v[36:39], v[92:95]\n"
"v_mfma_f32_16x16x32_bf16 v[120:123], v[40:43], a[0:3], 0\n"
"v_mfma_f32_16x16x32_bf16 v[120:123], v[44:47], a[4:7], v[120:123]\n"
"s_waitcnt vmcnt(12)\n"
"global_load_dwordx4 v[20:23], v6, s[56:57] offset:1024 sc1\n"
"v_mfma_f32_16x16x32_bf16 v[120:123], v[48:51], a[8:11], v[120:123]\n"
"s_waitcnt lgkmcnt(3)\n"
"v_add_f32 v96, v96, v100\n"
"v_add_f32 v97, v97, v101\n"
"v_mfma_f32_16x16x32_bf16 v[120:123], v[52:55], a[12:15], v[120:123]\n"
"s_waitcnt vmcnt(11)\n"
"v_add_f32 v98, v98, v102\n"
"v_add_f32 v99, v99, v103\n"
"v_mfma_f32_16x16x32_bf16 v[120:123], v[56:59], a[16:19], v[120:123]\n"
"v_add_f32 v96, v96, v104\n"
"v_add_f32 v97, v97, v105\n"
"v_mfma_f32_16x16x32_bf16 v[120:123], v[60:63], a[20:23], v[120:123]\n"
"s_waitcnt vmcnt(9)\n"
"v_add_f32 v98, v98, v106\n"
"v_add_f32 v99, v99, v107\n"
"v_mfma_f32_16x16x32_bf16 v[120:123], v[64:67], a[24:27], v[120:123]\n"
"global_load_dwordx4 v[24:27], v7, s[56:57] offset:0 sc1\n"
"v_add_f32 v96, v96, v84\n"
"v_mfma_f32_16x16x32_bf16 v[120:123], v[68:71], a[28:31], v[120:123]\n"
"global_load_dwordx4 v[28:31], v7, s[56:57] offset:1024 sc1\n"
"v_add_f32 v97, v97, v85\n"
"v_mfma_f32_16x16x32_bf16 v[100:103], v[40:43], a[32:35], 0\n"
"v_add_f32 v98, v98, v86\n"
"v_add_f32 v99, v99, v87\n"
"v_mfma_f32_16x16x32_bf16 v[100:103], v[44:47], a[36:39], v[100:103]\n"
"s_add_u32 s94, s50, 0xffd00000\n"
"s_and_b32 s95, s94, 0xfbffffff\n"
"ds_write_addtid_b32 v120 offset:0\n"
"v_mfma_f32_16x16x32_bf16 v[100:103], v[48:51], a[40:43], v[100:103]\n"
"s_waitcnt vmcnt(8)\n"
"buffer_store_dwordx2 v[0:1], v12, s[40:43], s93 offen offset:0 sc1\n"
"ds_write_addtid_b32 v121 offset:4640\n"
"v_mfma_f32_16x16x32_bf16 v[100:103], v[52:55], a[44:47], v[100:103]\n"
"ds_write_addtid_b32 v122 offset:9344\n"
"v_cvt_pk_bf16_f32 v84, v96, v97\n"
"v_mfma_f32_16x16x32_bf16 v[100:103], v[56:59], a[48:51], v[100:103]\n"
"ds_write_addtid_b32 v123 offset:13984\n"
"global_load_dwordx4 v[32:35], v8, s[56:57] offset:0 sc1\n"
"v_mfma_f32_16x16x32_bf16 v[100:103], v[60:63], a[52:55], v[100:103]\n"
"v_cvt_pk_bf16_f32 v85, v98, v99\n"
"v_mfma_f32_16x16x32_bf16 v[100:103], v[64:67], a[56:59], v[100:103]\n"
"s_waitcnt lgkmcnt(4)\n"
"v_add_f32 v108, v108, v112\n"
"v_add_f32 v109, v109, v113\n"
"v_mfma_f32_16x16x32_bf16 v[100:103], v[68:71], a[60:63], v[100:103]\n"
"v_add_f32 v110, v110, v114\n"
"v_add_f32 v111, v111, v115\n"
"v_mfma_f32_16x16x32_bf16 v[96:99], v[40:43], a[64:67], 0\n"
"v_add_f32 v108, v108, v116\n"
"v_add_f32 v109, v109, v117\n"
"v_mfma_f32_16x16x32_bf16 v[96:99], v[44:47], a[68:71], v[96:99]\n"
"ds_write_addtid_b32 v100 offset:256\n"
"v_add_f32 v110, v110, v118\n"
"v_mfma_f32_16x16x32_bf16 v[96:99], v[48:51], a[72:75], v[96:99]\n"
"ds_write_addtid_b32 v101 offset:4896\n"
"v_add_f32 v111, v111, v119\n"
"v_mfma_f32_16x16x32_bf16 v[96:99], v[52:55], a[76:79], v[96:99]\n"
"ds_write_addtid_b32 v102 offset:9600\n"
"v_add_f32 v108, v108, v92\n"
"v_mfma_f32_16x16x32_bf16 v[96:99], v[56:59], a[80:83], v[96:99]\n"
"ds_write_addtid_b32 v103 offset:14240\n"
"v_add_f32 v109, v109, v93\n"
"v_mfma_f32_16x16x32_bf16 v[96:99], v[60:63], a[84:87], v[96:99]\n"
"global_load_dwordx4 v[36:39], v8, s[56:57] offset:1024 sc1\n"
"v_add_f32 v110, v110, v94\n"
"v_mfma_f32_16x16x32_bf16 v[96:99], v[64:67], a[88:91], v[96:99]\n"
"s_waitcnt vmcnt(10)\n"
"v_add_f32 v111, v111, v95\n"
"v_cvt_pk_bf16_f32 v86, v108, v109\n"
"v_mfma_f32_16x16x32_bf16 v[96:99], v[68:71], a[92:95], v[96:99]\n"
"v_cvt_pk_bf16_f32 v87, v110, v111\n"
"v_lshlrev_b32 v100, 16, v88\n"
"v_mfma_f32_16x16x32_bf16 v[92:95], v[40:43], a[96:99], 0\n"
"v_and_b32 v101, 0xffff0000, v88\n"
"v_lshlrev_b32 v102, 16, v89\n"
"v_mfma_f32_16x16x32_bf16 v[92:95], v[44:47], a[100:103], v[92:95]\n"
"ds_write_addtid_b32 v96 offset:512\n"
"v_and_b32 v103, 0xffff0000, v89\n"
"v_mfma_f32_16x16x32_bf16 v[92:95], v[48:51], a[104:107], v[92:95]\n"
"ds_write_addtid_b32 v97 offset:5152\n"
"v_lshlrev_b32 v104, 16, v90\n"
"v_mfma_f32_16x16x32_bf16 v[92:95], v[52:55], a[108:111], v[92:95]\n"
"buffer_store_dwordx4 v[84:87], v11, s[40:43], s95 offen sc1\n"
"ds_write_addtid_b32 v98 offset:9856\n"
"v_and_b32 v105, 0xffff0000, v90\n"
"v_lshlrev_b32 v106, 16, v91\n"
"v_mfma_f32_16x16x32_bf16 v[108:111], v[84:87], v[84:87], 0\n"
"v_and_b32 v107, 0xffff0000, v91\n"
"v_mfma_f32_16x16x32_bf16 v[112:115], v[14:17], v[84:87], 0\n"
"v_mfma_f32_16x16x32_bf16 v[92:95], v[56:59], a[112:115], v[92:95]\n"
"ds_write_addtid_b32 v99 offset:14496\n"
"v_cndmask_b32_e64 v108, v108, v109, s[62:63]\n"
"v_mfma_f32_16x16x32_bf16 v[92:95], v[60:63], a[116:119], v[92:95]\n"
"v_cndmask_b32_e64 v110, v110, v111, s[62:63]\n"
"v_cndmask_b32_e64 v113, v108, v110, s[64:65]\n"
"v_mfma_f32_16x16x32_bf16 v[92:95], v[64:67], a[120:123], v[92:95]\n"
"buffer_store_dwordx2 v[112:113], v13, s[40:43], s95 offen offset:0 sc1\n"
"v_mfma_f32_16x16x32_bf16 v[92:95], v[68:71], a[124:127], v[92:95]\n"
"v_mfma_f32_16x16x32_bf16 v[84:87], v[40:43], a[128:131], 0\n"
"v_mfma_f32_16x16x32_bf16 v[84:87], v[44:47], a[132:135], v[84:87]\n"
"ds_write_addtid_b32 v92 offset:768\n"
"v_mfma_f32_16x16x32_bf16 v[84:87], v[48:51], a[136:139], v[84:87]\n"
"ds_write_addtid_b32 v93 offset:5408\n"
"v_mfma_f32_16x16x32_bf16 v[84:87], v[52:55], a[140:143], v[84:87]\n"
"ds_write_addtid_b32 v94 offset:10112\n"
"v_mfma_f32_16x16x32_bf16 v[84:87], v[56:59], a[144:147], v[84:87]\n"
"ds_write_addtid_b32 v95 offset:14752\n"
"v_mfma_f32_16x16x32_bf16 v[84:87], v[60:63], a[148:151], v[84:87]\n"
"v_mfma_f32_16x16x32_bf16 v[84:87], v[64:67], a[152:155], v[84:87]\n"
"v_mfma_f32_16x16x32_bf16 v[84:87], v[68:71], a[156:159], v[84:87]\n"
"global_load_dwordx4 v[96:99], v10, s[56:57] sc1\n"
"v_mfma_f32_16x16x32_bf16 v[92:95], v[40:43], a[160:163], 0\n"
"v_mfma_f32_16x16x32_bf16 v[92:95], v[44:47], a[164:167], v[92:95]\n"
"s_add_u32 s58, s50, 0x180000\n"
"ds_write_addtid_b32 v84 offset:1024\n"
"v_mfma_f32_16x16x32_bf16 v[92:95], v[48:51], a[168:171], v[92:95]\n"
"s_and_b32 s58, s58, 0x3ffffff\n"
"ds_write_addtid_b32 v85 offset:5664\n"
"v_mfma_f32_16x16x32_bf16 v[92:95], v[52:55], a[172:175], v[92:95]\n"
"s_add_u32 s56, s40, s58\n"
"ds_write_addtid_b32 v86 offset:10368\n"
"v_mfma_f32_16x16x32_bf16 v[92:95], v[56:59], a[176:179], v[92:95]\n"
"s_addc_u32 s57, s41, 0\n"
"ds_write_addtid_b32 v87 offset:15008\n"
"v_mfma_f32_16x16x32_bf16 v[92:95], v[60:63], a[180:183], v[92:95]\n"
"s_waitcnt vmcnt(9)\n"
"v_cmp_ne_u64 vcc, s[48:49], v[124:125]\n"
"v_mfma_f32_16x16x32_bf16 v[92:95], v[64:67], a[184:187], v[92:95]\n"
"v_mfma_f32_16x16x32_bf16 v[92:95], v[68:71], a[188:191], v[92:95]\n"
"s_cbranch_vccnz .LO_1s\n"
".LO_1r:\n"
"v_mfma_f32_16x16x32_bf16 v[100:103], a[192:195], v[40:43], v[100:103]\n"
"s_add_u32 s50, s50, 0x180000\n"
"global_load_dwordx4 v[84:87], v5, s[56:57] offset:0 sc1\n"
"v_mfma_f32_16x16x32_bf16 v[100:103], a[196:199], v[44:47], v[100:103]\n"
"ds_write_addtid_b32 v92 offset:1280\n"
"v_mfma_f32_16x16x32_bf16 v[100:103], a[200:203], v[48:51], v[100:103]\n"
"ds_write_addtid_b32 v93 offset:5920\n"
"v_mfma_f32_16x16x32_bf16 v[100:103], a[204:207], v[52:55], v[100:103]\n"
"ds_write_addtid_b32 v94 offset:10624\n"
"v_mfma_f32_16x16x32_bf16 v[100:103], a[208:211], v[56:59], v[100:103]\n"
"ds_write_addtid_b32 v95 offset:15264\n"
"v_mfma_f32_16x16x32_bf16 v[100:103], a[212:215], v[60:63], v[100:103]\n"
"global_load_dwordx4 v[88:91], v5, s[56:57] offset:1024 sc1\n"
"v_mfma_f32_16x16x32_bf16 v[100:103], a[216:219], v[64:67], v[100:103]\n"
"v_mfma_f32_16x16x32_bf16 v[100:103], a[220:223], v[68:71], v[100:103]\n"
"v_mfma_f32_16x16x32_bf16 v[104:107], a[224:227], v[40:43], v[104:107]\n"
"s_waitcnt lgkmcnt(0)\n"
"v_mfma_f32_16x16x32_bf16 v[104:107], a[228:231], v[44:47], v[104:107]\n"
"s_barrier\n"
"v_mfma_f32_16x16x32_bf16 v[104:107], a[232:235], v[48:51], v[104:107]\n"
"global_load_dwordx4 v[92:95], v6, s[56:57] offset:0 sc1\n"
"ds_read_b128 v[40:43], v2 offset:1536\n"
"v_mfma_f32_16x16x32_bf16 v[104:107], a[236:239], v[52:55], v[104:107]\n"
"ds_read_b128 v[44:47], v3 offset:512\n"
"ds_read_b128 v[48:51], v4 offset:2560\n"
"v_mfma_f32_16x16x32_bf16 v[104:107], a[240:243], v[56:59], v[104:107]\n"
"ds_read_b128 v[108:111], v2 offset:1792\n"
"ds_read_b128 v[112:115], v3 offset:768\n"
"v_mfma_f32_16x16x32_bf16 v[104:107], a[244:247], v[60:63], v[104:107]\n"
"ds_read_b128 v[116:119], v4 offset:2816\n"
"v_mfma_f32_16x16x32_bf16 v[104:107], a[248:251], v[64:67], v[104:107]\n"
"buffer_load_dwordx2 v[120:121], v9, s[40:43], s50 offen sc1\n"
"v_mfma_f32_16x16x32_bf16 v[104:107], a[252:255], v[68:71], v[104:107]\n"
"global_load_dwordx4 v[52:55], v6, s[56:57] offset:1024 sc1\n"
"v_mfma_f32_16x16x32_bf16 v[64:67], v[72:75], a[0:3], 0\n"
"v_mfma_f32_16x16x32_bf16 v[64:67], v[76:79], a[4:7], v[64:67]\n"
"s_waitcnt vmcnt(13)\n"
"v_mfma_f32_16x16x32_bf16 v[64:67], v[80:83], a[8:11], v[64:67]\n"
"s_waitcnt lgkmcnt(3)\n"
"v_add_f32 v40, v40, v44\n"
"v_add_f32 v41, v41, v45\n"
"v_mfma_f32_16x16x32_bf16 v[64:67], v[20:23], a[12:15], v[64:67]\n"
"s_waitcnt vmcnt(11)\n"
"v_add_f32 v42, v42, v46\n"
"v_add_f32 v43, v43, v47\n"
"v_mfma_f32_16x16x32_bf16 v[64:67], v[24:27], a[16:19], v[64:67]\n"
"global_load_dwordx4 v[56:59], v7, s[56:57] offset:0 sc1\n"
"v_add_f32 v40, v40, v48\n"
"v_mfma_f32_16x16x32_bf16 v[64:67], v[28:31], a[20:23], v[64:67]\n"
"s_waitcnt vmcnt(9)\n"
"v_add_f32 v41, v41, v49\n"
"v_add_f32 v42, v42, v50\n"
"v_mfma_f32_16x16x32_bf16 v[64:67], v[32:35], a[24:27], v[64:67]\n"
"v_add_f32 v43, v43, v51\n"
"v_add_f32 v40, v40, v100\n"
"v_mfma_f32_16x16x32_bf16 v[64:67], v[36:39], a[28:31], v[64:67]\n"
"v_add_f32 v41, v41, v101\n"
"v_add_f32 v42, v42, v102\n"
"v_mfma_f32_16x16x32_bf16 v[44:47], v[72:75], a[32:35], 0\n"
"v_add_f32 v43, v43, v103\n"
"v_mfma_f32_16x16x32_bf16 v[44:47], v[76:79], a[36:39], v[44:47]\n"
"ds_write_addtid_b32 v64 offset:1536\n"
"global_load_dwordx4 v[60:63], v7, s[56:57] offset:1024 sc1\n"
"v_mfma_f32_16x16x32_bf16 v[44:47], v[80:83], a[40:43], v[44:47]\n"
"s_add_u32 s93, s50, 0xffd00000\n"
"s_and_b32 s95, s93, 0xfbffffff\n"
"ds_write_addtid_b32 v65 offset:6176\n"
"v_mfma_f32_16x16x32_bf16 v[44:47], v[20:23], a[44:47], v[44:47]\n"
"ds_write_addtid_b32 v66 offset:10880\n"
"v_cvt_pk_bf16_f32 v48, v40, v41\n"
"v_mfma_f32_16x16x32_bf16 v[44:47], v[24:27], a[48:51], v[44:47]\n"
"ds_write_addtid_b32 v67 offset:15520\n"
"v_cvt_pk_bf16_f32 v49, v42, v43\n"
"v_mfma_f32_16x16x32_bf16 v[44:47], v[28:31], a[52:55], v[44:47]\n"
"s_waitcnt lgkmcnt(4)\n"
"v_add_f32 v108, v108, v112\n"
"v_add_f32 v109, v109, v113\n"
"v_mfma_f32_16x16x32_bf16 v[44:47], v[32:35], a[56:59], v[44:47]\n"
"s_waitcnt vmcnt(8)\n"
"global_load_dwordx4 v[64:67], v8, s[56:57] offset:0 sc1\n"
"v_add_f32 v110, v110, v114\n"
"v_mfma_f32_16x16x32_bf16 v[44:47], v[36:39], a[60:63], v[44:47]\n"
"buffer_store_dwordx2 v[0:1], v12, s[40:43], s94 offen offset:0 sc1\n"
"v_add_f32 v111, v111, v115\n"
"v_mfma_f32_16x16x32_bf16 v[40:43], v[72:75], a[64:67], 0\n"
"v_add_f32 v108, v108, v116\n"
"v_add_f32 v109, v109, v117\n"
"v_mfma_f32_16x16x32_bf16 v[40:43], v[76:79], a[68:71], v[40:43]\n"
"ds_write_addtid_b32 v44 offset:1792\n"
"v_add_f32 v110, v110, v118\n"
"v_mfma_f32_16x16x32_bf16 v[40:43], v[80:83], a[72:75], v[40:43]\n"
"ds_write_addtid_b32 v45 offset:6432\n"
"v_add_f32 v111, v111, v119\n"
"v_mfma_f32_16x16x32_bf16 v[40:43], v[20:23], a[76:79], v[40:43]\n"
"ds_write_addtid_b32 v46 offset:11136\n"
"global_load_dwordx4 v[68:71], v8, s[56:57] offset:1024 sc1\n"
"v_mfma_f32_16x16x32_bf16 v[40:43], v[24:27], a[80:83], v[40:43]\n"
"ds_write_addtid_b32 v47 offset:15776\n"
"v_add_f32 v108, v108, v104\n"
"v_mfma_f32_16x16x32_bf16 v[40:43], v[28:31], a[84:87], v[40:43]\n"
"v_add_f32 v109, v109, v105\n"
"v_add_f32 v110, v110, v106\n"
"v_mfma_f32_16x16x32_bf16 v[40:43], v[32:35], a[88:91], v[40:43]\n"
"v_add_f32 v111, v111, v107\n"
"v_cvt_pk_bf16_f32 v50, v108, v109\n"
"v_mfma_f32_16x16x32_bf16 v[40:43], v[36:39], a[92:95], v[40:43]\n"
"s_waitcnt vmcnt(10)\n"
"v_cvt_pk_bf16_f32 v51, v110, v111\n"
"v_lshlrev_b32 v112, 16, v96\n"
"v_mfma_f32_16x16x32_bf16 v[44:47], v[72:75], a[96:99], 0\n"
"v_and_b32 v113, 0xffff0000, v96\n"
"v_lshlrev_b32 v114, 16, v97\n"
"v_mfma_f32_16x16x32_bf16 v[44:47], v[76:79], a[100:103], v[44:47]\n"
"ds_write_addtid_b32 v40 offset:2048\n"
"v_and_b32 v115, 0xffff0000, v97\n"
"v_mfma_f32_16x16x32_bf16 v[44:47], v[80:83], a[104:107], v[44:47]\n"
"ds_write_addtid_b32 v41 offset:6688\n"
"v_lshlrev_b32 v108, 16, v98\n"
"v_mfma_f32_16x16x32_bf16 v[44:47], v[20:23], a[108:111], v[44:47]\n"
"ds_write_addtid_b32 v42 offset:11392\n"
"v_and_b32 v109, 0xffff0000, v98\n"
"v_mfma_f32_16x16x32_bf16 v[44:47], v[24:27], a[112:115], v[44:47]\n"
"ds_write_addtid_b32 v43 offset:16032\n"
"v_lshlrev_b32 v110, 16, v99\n"
"v_mfma_f32_16x16x32_bf16 v[44:47], v[28:31], a[116:119], v[44:47]\n"
"v_and_b32 v111, 0xffff0000, v99\n"
"v_mfma_f32_16x16x32_bf16 v[44:47], v[32:35], a[120:123], v[44:47]\n"
"v_mfma_f32_16x16x32_bf16 v[44:47], v[36:39], a[124:127], v[44:47]\n"
"buffer_store_dwordx4 v[48:51], v11, s[40:43], s95 offen sc1\n"
"v_mfma_f32_16x16x32_bf16 v[40:43], v[48:51], v[48:51], 0\n"
"v_mfma_f32_16x16x32_bf16 v[96:99], v[14:17], v[48:51], 0\n"
"v_mfma_f32_16x16x32_bf16 v[100:103], v[72:75], a[128:131], 0\n"
"v_cndmask_b32_e64 v40, v40, v41, s[62:63]\n"
"v_cndmask_b32_e64 v42, v42, v43, s[62:63]\n"
"v_mfma_f32_16x16x32_bf16 v[100:103], v[76:79], a[132:135], v[100:103]\n"
"ds_write_addtid_b32 v44 offset:2304\n"
"v_cndmask_b32_e64 v97, v40, v42, s[64:65]\n"
"v_mfma_f32_16x16x32_bf16 v[100:103], v[80:83], a[136:139], v[100:103]\n"
"buffer_store_dwordx2 v[96:97], v13, s[40:43], s95 offen offset:0 sc1\n"
"ds_write_addtid_b32 v45 offset:6944\n"
"v_mfma_f32_16x16x32_bf16 v[100:103], v[20:23], a[140:143], v[100:103]\n"
"ds_write_addtid_b32 v46 offset:11648\n"
"v_mfma_f32_16x16x32_bf16 v[100:103], v[24:27], a[144:147], v[100:103]\n"
"ds_write_addtid_b32 v47 offset:16288\n"
"global_load_dwordx4 v[116:119], v10, s[56:57] sc1\n"
"v_mfma_f32_16x16x32_bf16 v[100:103], v[28:31], a[148:151], v[100:103]\n"
"v_mfma_f32_16x16x32_bf16 v[100:103], v[32:35], a[152:155], v[100:103]\n"
"v_mfma_f32_16x16x32_bf16 v[100:103], v[36:39], a[156:159], v[100:103]\n"
"v_mfma_f32_16x16x32_bf16 v[40:43], v[72:75], a[160:163], 0\n"
"v_mfma_f32_16x16x32_bf16 v[40:43], v[76:79], a[164:167], v[40:43]\n"
"ds_write_addtid_b32 v100 offset:2560\n"
"v_mfma_f32_16x16x32_bf16 v[40:43], v[80:83], a[168:171], v[40:43]\n"
"s_add_u32 s58, s50, 0x180000\n"
"ds_write_addtid_b32 v101 offset:7200\n"
"v_mfma_f32_16x16x32_bf16 v[40:43], v[20:23], a[172:175], v[40:43]\n"
"s_and_b32 s58, s58, 0x3ffffff\n"
"ds_write_addtid_b32 v102 offset:11904\n"
"v_mfma_f32_16x16x32_bf16 v[40:43], v[24:27], a[176:179], v[40:43]\n"
"s_add_u32 s56, s40, s58\n"
"ds_write_addtid_b32 v103 offset:16544\n"
"v_mfma_f32_16x16x32_bf16 v[40:43], v[28:31], a[180:183], v[40:43]\n"
"s_addc_u32 s57, s41, 0\n"
"v_mfma_f32_16x16x32_bf16 v[40:43], v[32:35], a[184:187], v[40:43]\n"
"s_waitcnt vmcnt(9)\n"
"v_cmp_ne_u64 vcc, s[48:49], v[120:121]\n"
"v_mfma_f32_16x16x32_bf16 v[40:43], v[36:39], a[188:191], v[40:43]\n"
"v_mfma_f32_16x16x32_bf16 v[112:115], a[192:195], v[72:75], v[112:115]\n"
"s_cbranch_vccnz .LO_2s\n"
".LO_2r:\n"
"v_mfma_f32_16x16x32_bf16 v[112:115], a[196:199], v[76:79], v[112:115]\n"
"s_add_u32 s50, s50, 0x180000\n"
"ds_write_addtid_b32 v40 offset:2816\n"
"global_load_dwordx4 v[96:99], v5, s[56:57] offset:0 sc1\n"
"v_mfma_f32_16x16x32_bf16 v[112:115], a[200:203], v[80:83], v[112:115]\n"
"ds_write_addtid_b32 v41 offset:7456\n"
"v_mfma_f32_16x16x32_bf16 v[112:115], a[204:207], v[20:23], v[112:115]\n"
"ds_write_addtid_b32 v42 offset:12160\n"
"v_mfma_f32_16x16x32_bf16 v[112:115], a[208:211], v[24:27], v[112:115]\n"
"ds_write_addtid_b32 v43 offset:16800\n"
"v_mfma_f32_16x16x32_bf16 v[112:115], a[212:215], v[28:31], v[112:115]\n"
"v_mfma_f32_16x16x32_bf16 v[112:115], a[216:219], v[32:35], v[112:115]\n"
"global_load_dwordx4 v[100:103], v5, s[56:57] offset:1024 sc1\n"
"v_mfma_f32_16x16x32_bf16 v[112:115], a[220:223], v[36:39], v[112:115]\n"
"v_mfma_f32_16x16x32_bf16 v[108:111], a[224:227], v[72:75], v[108:111]\n"
"s_waitcnt lgkmcnt(0)\n"
"v_mfma_f32_16x16x32_bf16 v[108:111], a[228:231], v[76:79], v[108:111]\n"
"s_barrier\n"
"v_mfma_f32_16x16x32_bf16 v[108:111], a[232:235], v[80:83], v[108:111]\n"
"buffer_load_dwordx2 v[124:125], v9, s[40:43], s50 offen sc1\n"
"v_mfma_f32_16x16x32_bf16 v[108:111], a[236:239], v[20:23], v[108:111]\n"
"global_load_dwordx4 v[104:107], v6, s[56:57] offset:0 sc1\n"
"ds_read_b128 v[72:75], v2 offset:0\n"
"v_mfma_f32_16x16x32_bf16 v[108:111], a[240:243], v[24:27], v[108:111]\n"
"ds_read_b128 v[44:47], v3 offset:2048\n"
"ds_read_b128 v[48:51], v4 offset:1024\n"
"v_mfma_f32_16x16x32_bf16 v[108:111], a[244:247], v[28:31], v[108:111]\n"
"ds_read_b128 v[76:79], v2 offset:256\n"
"ds_read_b128 v[80:83], v3 offset:2304\n"
"v_mfma_f32_16x16x32_bf16 v[108:111], a[248:251], v[32:35], v[108:111]\n"
"ds_read_b128 v[120:123], v4 offset:1280\n"
"v_mfma_f32_16x16x32_bf16 v[108:111], a[252:255], v[36:39], v[108:111]\n"
"s_branch .LO_top\n"
".LO_1s:\n"
"s_cmp_ge_u32 s50, s53\n"
"s_cbranch_scc1 .LO_end\n"
"s_cmp_ge_u32 s50, s52\n"
"s_cbranch_scc1 .LO_1r\n"
"s_sub_u32 s60, s50, 0x0\n"
"s_mov_b32 s61, 200000\n"
"s_nop 7\n"
".LO_1p:\n"
"buffer_load_dwordx2 v[124:125], v9, s[40:43], s60 offen sc1\n"
"s_waitcnt vmcnt(0)\n"
"v_cmp_ne_u64 vcc, s[48:49], v[124:125]\n"
"s_cbranch_vccz .LO_1px\n"
"s_sleep 1\n"
"s_sub_u32 s61, s61, 1\n"
"s_cbranch_scc0 .LO_1p\n"
"s_branch .LO_end\n"
".LO_1px:\n"
"s_branch .LO_1r\n"
".LO_2s:\n"
"s_cmp_ge_u32 s50, s53\n"
"s_cbranch_scc1 .LO_end\n"
"s_cmp_ge_u32 s50, s52\n"
"s_cbranch_scc1 .LO_2r\n"
"s_sub_u32 s60, s50, 0x0\n"
"s_mov_b32 s61, 200000\n"
"s_nop 7\n"
".LO_2p:\n"
"buffer_load_dwordx2 v[120:121], v9, s[40:43], s60 offen sc1\n"
"s_waitcnt vmcnt(0)\n"
"v_cmp_ne_u64 vcc, s[48:49], v[120:121]\n"
"s_cbranch_vccz .LO_2px\n"
"s_sleep 1\n"
"s_sub_u32 s61, s61, 1\n"
"s_cbranch_scc0 .LO_2p\n"
"s_branch .LO_end\n"
".LO_2px:\n"
"s_branch .LO_2r\n"
".LO_3s:\n"
"s_cmp_ge_u32 s50, s53\n"
"s_cbranch_scc1 .LO_end\n"
"s_cmp_ge_u32 s50, s52\n"
"s_cbranch_scc1 .LO_3r\n"
"s_sub_u32 s60, s50, 0x0\n"
"s_mov_b32 s61, 200000\n"
"s_nop 7\n"
".LO_3p:\n"
"buffer_load_dwordx2 v[124:125], v9, s[40:43], s60 offen sc1\n"
"s_waitcnt vmcnt(0)\n"
"v_cmp_ne_u64 vcc, s[48:49], v[124:125]\n"
"s_cbranch_vccz .LO_3px\n"
"s_sleep 1\n"
"s_sub_u32 s61, s61, 1\n"
"s_cbranch_scc0 .LO_3p\n"
"s_branch .LO_end\n"
".LO_3px:\n"
"s_branch .LO_3r\n"
".LO_4s:\n"
"s_cmp_ge_u32 s50, s53\n"
"s_cbranch_scc1 .LO_end\n"
"s_cmp_ge_u32 s50, s52\n"
"s_cbranch_scc1 .LO_4r\n"
"s_sub_u32 s60, s50, 0x0\n"
"s_mov_b32 s61, 200000\n"
"s_nop 7\n"
".LO_4p:\n"
"buffer_load_dwordx2 v[120:121], v9, s[40:43], s60 offen sc1\n"
"s_waitcnt vmcnt(0)\n"
"v_cmp_ne_u64 vcc, s[48:49], v[120:121]\n"
"s_cbranch_vccz .LO_4px\n"
"s_sleep 1\n"
"s_sub_u32 s61, s61, 1\n"
"s_cbranch_scc0 .LO_4p\n"
"s_branch .LO_end\n"
".LO_4px:\n"
"s_branch .LO_4r\n"
".LO_end:\n"
"s_waitcnt vmcnt(0) lgkmcnt(0)\n"
"s_endpgm\n"
      :: "{v2}"((unsigned)(lds0 + ((wid + 3) & 3) * 18592 + trp)), "{v3}"((unsigned)(lds0 + ((wid + 2) & 3) * 18592 + trp)), "{v4}"((unsigned)(lds0 + ((wid + 1) & 3) * 18592 + trp)), "{v5}"((unsigned)(lane * 16 + wid * 8192 + ((0 + rot) % 4) * 2048 + 134250496)), "{v6}"((unsigned)(lane * 16 + wid * 8192 + ((1 + rot) % 4) * 2048 + 134250496)), "{v7}"((unsigned)(lane * 16 + wid * 8192 + ((2 + rot) % 4) * 2048 + 134250496)), "{v8}"((unsigned)(lane * 16 + wid * 8192 + ((3 + rot) % 4) * 2048 + 134250496)), "{v9}"((unsigned)((wid * 8 + (lane & 7)) * 8 + 352512 + O_GL * 1572864)), "{v10}"((unsigned)(lane * 16 + pair * 1024 + 134217728)), "{v11}"((unsigned)(lane * 16 + pair * 1024 + 65536)), "{v12}"((unsigned)(lane ? DROP : pair * 8 + 352768)), "{v13}"((unsigned)(lg == (li >> 2) ? pair * 128 + li * 8 + 344064 : DROP)), "{v14}"((unsigned)(0x3f803f80)), "{v15}"((unsigned)(0x3f803f80)), "{v16}"((unsigned)(0x3f803f80)), "{v17}"((unsigned)(0x3f803f80)), "{v20}"((unsigned)(vT + tsw)), "{v21}"((unsigned)(vT + (tsw ^ 8))), "{v22}"((unsigned)(vD + ((0 + rot) % 4) * 131072)), "{v23}"((unsigned)(vD + ((1 + rot) % 4) * 131072)), "{v24}"((unsigned)(vD + ((2 + rot) % 4) * 131072)), "{v25}"((unsigned)(vD + ((3 + rot) % 4) * 131072)), "{s[40:43]}"(rS), "{s[44:47]}"(rW), "{s[48:49]}"(wsp), "{s50}"((unsigned)(sA)), "{s51}"((unsigned)(sLo)), "{s52}"((unsigned)(sHi)), "{s53}"((unsigned)(sEx)), "{s54}"((unsigned)(sEnt)), "{s55}"((unsigned)(sLd)), "{s[56:57]}"(wsb + (sLo & 0x3ffffff)), "{s59}"((unsigned)(lds0 + wid * 18592)), "{s[62:63]}"(0xAAAAAAAAAAAAAAAAull), "{s[64:65]}"(0xCCCCCCCCCCCCCCCCull), "{s66}"((unsigned)(sW0 + 64 * ((wid + 1) & 3))), "{s67}"((unsigned)(sW0 + 64 * ((wid + 2) & 3))), "{s68}"((unsigned)(sW0 + 64 * ((wid + 3) & 3))), "{s69}"((unsigned)(sW0 + 64 * ((wid + 4) & 3)))
      : "v255", "a255", "s95", "vcc", "scc", "memory");

  } else if (role == 2) {
    const unsigned sEx = sHi + F_EX * 3 * RS, sEnt = -wid & 3, tsw = 0, pair = idx * 4 + wid, rot = KR * (set ? set & 2 : 1);
    const unsigned sW0 = wid * 256 * 7168 + idx * 256, vD = (lane >> 2) * 7168 + (lane & 3) * 16;
    const u32x4 rW = desc(a.w1, 1024 * 7168), bp = *(const u32x4*)(a.b1 + pair * 32 + lg * 8);
    const unsigned bia[8] = {bp[0] << 16, bp[0] & 0xffff0000u, bp[1] << 16, bp[1] & 0xffff0000u, bp[2] << 16, bp[2] & 0xffff0000u, bp[3] << 16, bp[3] & 0xffff0000u};
    asm volatile(
"s_nop 4\n"
"v_mov_b32 v186, 0\n"
"global_load_dwordx2 v[0:1], v186, s[48:49] sc1\n"
"v_mov_b32 v188, 0x3f803f80\n"
"v_mov_b32 v189, 0x3f803f80\n"
"v_mov_b32 v190, 0x3f803f80\n"
"v_mov_b32 v191, 0x3f803f80\n"
"s_add_u32 m0, s55, 0\n"
"s_add_u32 s60, s63, 0\n"
"buffer_load_dwordx4 v38, s[44:47], s60 offen lds\n"
"s_add_u32 m0, s55, 1024\n"
"s_add_u32 s60, s63, 114688\n"
"buffer_load_dwordx4 v38, s[44:47], s60 offen lds\n"
"s_add_u32 m0, s55, 2048\n"
"s_add_u32 s60, s64, 0\n"
"buffer_load_dwordx4 v38, s[44:47], s60 offen lds\n"
"s_add_u32 m0, s55, 3072\n"
"s_add_u32 s60, s64, 114688\n"
"buffer_load_dwordx4 v38, s[44:47], s60 offen lds\n"
"s_add_u32 m0, s55, 4096\n"
"s_add_u32 s60, s65, 0\n"
"buffer_load_dwordx4 v38, s[44:47], s60 offen lds\n"
"s_add_u32 m0, s55, 5120\n"
"s_add_u32 s60, s65, 114688\n"
"buffer_load_dwordx4 v38, s[44:47], s60 offen lds\n"
"s_add_u32 m0, s55, 6144\n"
"s_add_u32 s60, s66, 0\n"
"buffer_load_dwordx4 v38, s[44:47], s60 offen lds\n"
"s_add_u32 m0, s55, 7168\n"
"s_add_u32 s60, s66, 114688\n"
"buffer_load_dwordx4 v38, s[44:47], s60 offen lds\n"
"s_add_u32 m0, s55, 8192\n"
"s_add_u32 s60, s63, 229376\n"
"buffer_load_dwordx4 v38, s[44:47], s60 offen lds\n"
"s_add_u32 m0, s55, 9216\n"
"s_add_u32 s60, s63, 344064\n"
"buffer_load_dwordx4 v38, s[44:47], s60 offen lds\n"
"s_add_u32 m0, s55, 10240\n"
"s_add_u32 s60, s64, 229376\n"
"buffer_load_dwordx4 v38, s[44:47], s60 offen lds\n"
"s_add_u32 m0, s55, 11264\n"
"s_add_u32 s60, s64, 344064\n"
"buffer_load_dwordx4 v38, s[44:47], s60 offen lds\n"
"s_add_u32 m0, s55, 12288\n"
"s_add_u32 s60, s65, 229376\n"
"buffer_load_dwordx4 v38, s[44:47], s60 offen lds\n"
"s_add_u32 m0, s55, 13312\n"
"s_add_u32 s60, s65, 344064\n"
"buffer_load_dwordx4 v38, s[44:47], s60 offen lds\n"
"s_add_u32 m0, s55, 14336\n"
"s_add_u32 s60, s66, 229376\n"
"buffer_load_dwordx4 v38, s[44:47], s60 offen lds\n"
"s_add_u32 m0, s55, 15360\n"
"s_add_u32 s60, s66, 344064\n"
"buffer_load_dwordx4 v38, s[44:47], s60 offen lds\n"
"s_add_u32 m0, s55, 16384\n"
"s_add_u32 s60, s63, 0\n"
"buffer_load_dwordx4 v39, s[44:47], s60 offen lds\n"
"s_add_u32 m0, s55, 17408\n"
"s_add_u32 s60, s63, 114688\n"
"buffer_load_dwordx4 v39, s[44:47], s60 offen lds\n"
"s_add_u32 m0, s55, 18432\n"
"s_add_u32 s60, s64, 0\n"
"buffer_load_dwordx4 v39, s[44:47], s60 offen lds\n"
"s_add_u32 m0, s55, 19456\n"
"s_add_u32 s60, s64, 114688\n"
"buffer_load_dwordx4 v39, s[44:47], s60 offen lds\n"
"s_add_u32 m0, s55, 20480\n"
"s_add_u32 s60, s65, 0\n"
"buffer_load_dwordx4 v39, s[44:47], s60 offen lds\n"
"s_add_u32 m0, s55, 21504\n"
"s_add_u32 s60, s65, 114688\n"
"buffer_load_dwordx4 v39, s[44:47], s60 offen lds\n"
"s_add_u32 m0, s55, 22528\n"
"s_add_u32 s60, s66, 0\n"
"buffer_load_dwordx4 v39, s[44:47], s60 offen lds\n"
"s_add_u32 m0, s55, 23552\n"
"s_add_u32 s60, s66, 114688\n"
"buffer_load_dwordx4 v39, s[44:47], s60 offen lds\n"
"s_add_u32 m0, s55, 24576\n"
"s_add_u32 s60, s63, 229376\n"
"buffer_load_dwordx4 v39, s[44:47], s60 offen lds\n"
"s_add_u32 m0, s55, 25600\n"
"s_add_u32 s60, s63, 344064\n"
"buffer_load_dwordx4 v39, s[44:47], s60 offen lds\n"
"s_add_u32 m0, s55, 26624\n"
"s_add_u32 s60, s64, 229376\n"
"buffer_load_dwordx4 v39, s[44:47], s60 offen lds\n"
"s_add_u32 m0, s55, 27648\n"
"s_add_u32 s60, s64, 344064\n"
"buffer_load_dwordx4 v39, s[44:47], s60 offen lds\n"
"s_add_u32 m0, s55, 28672\n"
"s_add_u32 s60, s65, 229376\n"
"buffer_load_dwordx4 v39, s[44:47], s60 offen lds\n"
"s_add_u32 m0, s55, 29696\n"
"s_add_u32 s60, s65, 344064\n"
"buffer_load_dwordx4 v39, s[44:47], s60 offen lds\n"
"s_add_u32 m0, s55, 30720\n"
"s_add_u32 s60, s66, 229376\n"
"buffer_load_dwordx4 v39, s[44:47], s60 offen lds\n"
"s_add_u32 m0, s55, 31744\n"
"s_add_u32 s60, s66, 344064\n"
"buffer_load_dwordx4 v39, s[44:47], s60 offen lds\n"
"s_add_u32 m0, s55, 32768\n"
"s_add_u32 s60, s63, 0\n"
"buffer_load_dwordx4 v40, s[44:47], s60 offen lds\n"
"s_add_u32 m0, s55, 33792\n"
"s_add_u32 s60, s63, 114688\n"
"buffer_load_dwordx4 v40, s[44:47], s60 offen lds\n"
"s_add_u32 m0, s55, 34816\n"
"s_add_u32 s60, s64, 0\n"
"buffer_load_dwordx4 v40, s[44:47], s60 offen lds\n"
"s_add_u32 m0, s55, 35840\n"
"s_add_u32 s60, s64, 114688\n"
"buffer_load_dwordx4 v40, s[44:47], s60 offen lds\n"
"s_add_u32 m0, s55, 36864\n"
"s_add_u32 s60, s65, 0\n"
"buffer_load_dwordx4 v40, s[44:47], s60 offen lds\n"
"s_add_u32 m0, s55, 37888\n"
"s_add_u32 s60, s65, 114688\n"
"buffer_load_dwordx4 v40, s[44:47], s60 offen lds\n"
"s_add_u32 m0, s55, 38912\n"
"s_add_u32 s60, s66, 0\n"
"buffer_load_dwordx4 v40, s[44:47], s60 offen lds\n"
"s_add_u32 m0, s55, 39936\n"
"s_add_u32 s60, s66, 114688\n"
"buffer_load_dwordx4 v40, s[44:47], s60 offen lds\n"
"s_waitcnt vmcnt(38)\n"
"ds_read_b64_tr_b16 a[0:1], v36 offset:0\n"
"ds_read_b64_tr_b16 a[2:3], v36 offset:256\n"
"ds_read_b64_tr_b16 a[32:33], v37 offset:0\n"
"ds_read_b64_tr_b16 a[34:35], v37 offset:256\n"
"v_add_u32 v0, 2, v0\n"
"v_add_u32 v1, 0x9e3779b9, v1\n"
"v_or_b32 v0, 1, v0\n"
"v_readfirstlane_b32 s49, v1\n"
"v_readfirstlane_b32 s48, v0\n"
"s_waitcnt vmcnt(36)\n"
"ds_read_b64_tr_b16 a[64:65], v36 offset:2048\n"
"ds_read_b64_tr_b16 a[66:67], v36 offset:2304\n"
"ds_read_b64_tr_b16 a[96:97], v37 offset:2048\n"
"ds_read_b64_tr_b16 a[98:99], v37 offset:2304\n"
"s_waitcnt lgkmcnt(4)\n"
"s_add_u32 m0, s55, 0\n"
"s_add_u32 s60, s63, 229376\n"
"buffer_load_dwordx4 v40, s[44:47], s60 offen lds\n"
"s_add_u32 m0, s55, 1024\n"
"s_add_u32 s60, s63, 344064\n"
"buffer_load_dwordx4 v40, s[44:47], s60 offen lds\n"
"v_mfma_f32_16x16x32_bf16 v[192:195], v[188:191], a[0:3], 0\n"
"v_mfma_f32_16x16x32_bf16 v[196:199], v[188:191], a[32:35], 0\n"
"s_waitcnt vmcnt(36)\n"
"ds_read_b64_tr_b16 a[128:129], v36 offset:4096\n"
"ds_read_b64_tr_b16 a[130:131], v36 offset:4352\n"
"ds_read_b64_tr_b16 a[160:161], v37 offset:4096\n"
"ds_read_b64_tr_b16 a[162:163], v37 offset:4352\n"
"s_waitcnt lgkmcnt(4)\n"
"s_add_u32 m0, s55, 2048\n"
"s_add_u32 s60, s64, 229376\n"
"buffer_load_dwordx4 v40, s[44:47], s60 offen lds\n"
"s_add_u32 m0, s55, 3072\n"
"s_add_u32 s60, s64, 344064\n"
"buffer_load_dwordx4 v40, s[44:47], s60 offen lds\n"
"v_mfma_f32_16x16x32_bf16 v[200:203], v[188:191], a[64:67], 0\n"
"v_mfma_f32_16x16x32_bf16 v[204:207], v[188:191], a[96:99], 0\n"
"s_waitcnt vmcnt(36)\n"
"ds_read_b64_tr_b16 a[192:193], v36 offset:6144\n"
"ds_read_b64_tr_b16 a[194:195], v36 offset:6400\n"
"ds_read_b64_tr_b16 a[224:225], v37 offset:6144\n"
"ds_read_b64_tr_b16 a[226:227], v37 offset:6400\n"
"s_waitcnt lgkmcnt(4)\n"
"s_add_u32 m0, s55, 4096\n"
"s_add_u32 s60, s65, 229376\n"
"buffer_load_dwordx4 v40, s[44:47], s60 offen lds\n"
"s_add_u32 m0, s55, 5120\n"
"s_add_u32 s60, s65, 344064\n"
"buffer_load_dwordx4 v40, s[44:47], s60 offen lds\n"
"v_mfma_f32_16x16x32_bf16 v[208:211], v[188:191], a[128:131], 0\n"
"v_mfma_f32_16x16x32_bf16 v[212:215], v[188:191], a[160:163], 0\n"
"s_waitcnt vmcnt(36)\n"
"ds_read_b64_tr_b16 a[4:5], v36 offset:8192\n"
"ds_read_b64_tr_b16 a[6:7], v36 offset:8448\n"
"ds_read_b64_tr_b16 a[36:37], v37 offset:8192\n"
"ds_read_b64_tr_b16 a[38:39], v37 offset:8448\n"
"s_waitcnt lgkmcnt(4)\n"
"s_add_u32 m0, s55, 6144\n"
"s_add_u32 s60, s66, 229376\n"
"buffer_load_dwordx4 v40, s[44:47], s60 offen lds\n"
"s_add_u32 m0, s55, 7168\n"
"s_add_u32 s60, s66, 344064\n"
"buffer_load_dwordx4 v40, s[44:47], s60 offen lds\n"
"v_mfma_f32_16x16x32_bf16 v[216:219], v[188:191], a[192:195], 0\n"
"v_mfma_f32_16x16x32_bf16 v[220:223], v[188:191], a[224:227], 0\n"
"s_waitcnt vmcnt(36)\n"
"ds_read_b64_tr_b16 a[68:69], v36 offset:10240\n"
"ds_read_b64_tr_b16 a[70:71], v36 offset:10496\n"
"ds_read_b64_tr_b16 a[100:101], v37 offset:10240\n"
"ds_read_b64_tr_b16 a[102:103], v37 offset:10496\n"
"s_waitcnt lgkmcnt(4)\n"
"s_add_u32 m0, s55, 8192\n"
"s_add_u32 s60, s63, 0\n"
"buffer_load_dwordx4 v41, s[44:47], s60 offen lds\n"
"s_add_u32 m0, s55, 9216\n"
"s_add_u32 s60, s63, 114688\n"
"buffer_load_dwordx4 v41, s[44:47], s60 offen lds\n"
"v_mfma_f32_16x16x32_bf16 v[192:195], v[188:191], a[4:7], v[192:195]\n"
"v_mfma_f32_16x16x32_bf16 v[196:199], v[188:191], a[36:39], v[196:199]\n"
"s_waitcnt vmcnt(36)\n"
"ds_read_b64_tr_b16 a[132:133], v36 offset:12288\n"
"ds_read_b64_tr_b16 a[134:135], v36 offset:12544\n"
"ds_read_b64_tr_b16 a[164:165], v37 offset:12288\n"
"ds_read_b64_tr_b16 a[166:167], v37 offset:12544\n"
"s_waitcnt lgkmcnt(4)\n"
"s_add_u32 m0, s55, 10240\n"
"s_add_u32 s60, s64, 0\n"
"buffer_load_dwordx4 v41, s[44:47], s60 offen lds\n"
"s_add_u32 m0, s55, 11264\n"
"s_add_u32 s60, s64, 114688\n"
"buffer_load_dwordx4 v41, s[44:47], s60 offen lds\n"
"v_mfma_f32_16x16x32_bf16 v[200:203], v[188:191], a[68:71], v[200:203]\n"
"v_mfma_f32_16x16x32_bf16 v[204:207], v[188:191], a[100:103], v[204:207]\n"
"s_waitcnt vmcnt(36)\n"
"ds_read_b64_tr_b16 a[196:197], v36 offset:14336\n"
"ds_read_b64_tr_b16 a[198:199], v36 offset:14592\n"
"ds_read_b64_tr_b16 a[228:229], v37 offset:14336\n"
"ds_read_b64_tr_b16 a[230:231], v37 offset:14592\n"
"s_waitcnt lgkmcnt(4)\n"
"s_add_u32 m0, s55, 12288\n"
"s_add_u32 s60, s65, 0\n"
"buffer_load_dwordx4 v41, s[44:47], s60 offen lds\n"
"s_add_u32 m0, s55, 13312\n"
"s_add_u32 s60, s65, 114688\n"
"buffer_load_dwordx4 v41, s[44:47], s60 offen lds\n"
"v_mfma_f32_16x16x32_bf16 v[208:211], v[188:191], a[132:135], v[208:211]\n"
"v_mfma_f32_16x16x32_bf16 v[212:215], v[188:191], a[164:167], v[212:215]\n"
"s_waitcnt vmcnt(36)\n"
"ds_read_b64_tr_b16 a[8:9], v36 offset:16384\n"
"ds_read_b64_tr_b16 a[10:11], v36 offset:16640\n"
"ds_read_b64_tr_b16 a[40:41], v37 offset:16384\n"
"ds_read_b64_tr_b16 a[42:43], v37 offset:16640\n"
"s_waitcnt lgkmcnt(4)\n"
"s_add_u32 m0, s55, 14336\n"
"s_add_u32 s60, s66, 0\n"
"buffer_load_dwordx4 v41, s[44:47], s60 offen lds\n"
"s_add_u32 m0, s55, 15360\n"
"s_add_u32 s60, s66, 114688\n"
"buffer_load_dwordx4 v41, s[44:47], s60 offen lds\n"
"v_mfma_f32_16x16x32_bf16 v[216:219], v[188:191], a[196:199], v[216:219]\n"
"v_mfma_f32_16x16x32_bf16 v[220:223], v[188:191], a[228:231], v[220:223]\n"
"s_waitcnt vmcnt(36)\n"
"ds_read_b64_tr_b16 a[72:73], v36 offset:18432\n"
"ds_read_b64_tr_b16 a[74:75], v36 offset:18688\n"
"ds_read_b64_tr_b16 a[104:105], v37 offset:18432\n"
"ds_read_b64_tr_b16 a[106:107], v37 offset:18688\n"
"s_waitcnt lgkmcnt(4)\n"
"s_add_u32 m0, s55, 16384\n"
"s_add_u32 s60, s63, 229376\n"
"buffer_load_dwordx4 v41, s[44:47], s60 offen lds\n"
"s_add_u32 m0, s55, 17408\n"
"s_add_u32 s60, s63, 344064\n"
"buffer_load_dwordx4 v41, s[44:47], s60 offen lds\n"
"v_mfma_f32_16x16x32_bf16 v[192:195], v[188:191], a[8:11], v[192:195]\n"
"v_mfma_f32_16x16x32_bf16 v[196:199], v[188:191], a[40:43], v[196:199]\n"
"s_waitcnt vmcnt(36)\n"
"ds_read_b64_tr_b16 a[136:137], v36 offset:20480\n"
"ds_read_b64_tr_b16 a[138:139], v36 offset:20736\n"
"ds_read_b64_tr_b16 a[168:169], v37 offset:20480\n"
"ds_read_b64_tr_b16 a[170:171], v37 offset:20736\n"
"s_waitcnt lgkmcnt(4)\n"
"s_add_u32 m0, s55, 18432\n"
"s_add_u32 s60, s64, 229376\n"
"buffer_load_dwordx4 v41, s[44:47], s60 offen lds\n"
"s_add_u32 m0, s55, 19456\n"
"s_add_u32 s60, s64, 344064\n"
"buffer_load_dwordx4 v41, s[44:47], s60 offen lds\n"
"v_mfma_f32_16x16x32_bf16 v[200:203], v[188:191], a[72:75], v[200:203]\n"
"v_mfma_f32_16x16x32_bf16 v[204:207], v[188:191], a[104:107], v[204:207]\n"
"s_waitcnt vmcnt(36)\n"
"ds_read_b64_tr_b16 a[200:201], v36 offset:22528\n"
"ds_read_b64_tr_b16 a[202:203], v36 offset:22784\n"
"ds_read_b64_tr_b16 a[232:233], v37 offset:22528\n"
"ds_read_b64_tr_b16 a[234:235], v37 offset:22784\n"
"s_waitcnt lgkmcnt(4)\n"
"s_add_u32 m0, s55, 20480\n"
"s_add_u32 s60, s65, 229376\n"
"buffer_load_dwordx4 v41, s[44:47], s60 offen lds\n"
"s_add_u32 m0, s55, 21504\n"
"s_add_u32 s60, s65, 344064\n"
"buffer_load_dwordx4 v41, s[44:47], s60 offen lds\n"
"v_mfma_f32_16x16x32_bf16 v[208:211], v[188:191], a[136:139], v[208:211]\n"
"v_mfma_f32_16x16x32_bf16 v[212:215], v[188:191], a[168:171], v[212:215]\n"
"s_waitcnt vmcnt(36)\n"
"ds_read_b64_tr_b16 a[12:13], v36 offset:24576\n"
"ds_read_b64_tr_b16 a[14:15], v36 offset:24832\n"
"ds_read_b64_tr_b16 a[44:45], v37 offset:24576\n"
"ds_read_b64_tr_b16 a[46:47], v37 offset:24832\n"
"s_waitcnt lgkmcnt(4)\n"
"s_add_u32 m0, s55, 22528\n"
"s_add_u32 s60, s66, 229376\n"
"buffer_load_dwordx4 v41, s[44:47], s60 offen lds\n"
"s_add_u32 m0, s55, 23552\n"
"s_add_u32 s60, s66, 344064\n"
"buffer_load_dwordx4 v41, s[44:47], s60 offen lds\n"
"v_mfma_f32_16x16x32_bf16 v[216:219], v[188:191], a[200:203], v[216:219]\n"
"v_mfma_f32_16x16x32_bf16 v[220:223], v[188:191], a[232:235], v[220:223]\n"
"s_waitcnt vmcnt(36)\n"
"ds_read_b64_tr_b16 a[76:77], v36 offset:26624\n"
"ds_read_b64_tr_b16 a[78:79], v36 offset:26880\n"
"ds_read_b64_tr_b16 a[108:109], v37 offset:26624\n"
"ds_read_b64_tr_b16 a[110:111], v37 offset:26880\n"
"s_waitcnt lgkmcnt(4)\n"
"v_mfma_f32_16x16x32_bf16 v[192:195], v[188:191], a[12:15], v[192:195]\n"
"v_mfma_f32_16x16x32_bf16 v[196:199], v[188:191], a[44:47], v[196:199]\n"
"s_waitcnt vmcnt(34)\n"
"ds_read_b64_tr_b16 a[140:141], v36 offset:28672\n"
"ds_read_b64_tr_b16 a[142:143], v36 offset:28928\n"
"ds_read_b64_tr_b16 a[172:173], v37 offset:28672\n"
"ds_read_b64_tr_b16 a[174:175], v37 offset:28928\n"
"s_waitcnt lgkmcnt(4)\n"
"v_mfma_f32_16x16x32_bf16 v[200:203], v[188:191], a[76:79], v[200:203]\n"
"v_mfma_f32_16x16x32_bf16 v[204:207], v[188:191], a[108:111], v[204:207]\n"
"s_waitcnt vmcnt(32)\n"
"ds_read_b64_tr_b16 a[204:205], v36 offset:30720\n"
"ds_read_b64_tr_b16 a[206:207], v36 offset:30976\n"
"ds_read_b64_tr_b16 a[236:237], v37 offset:30720\n"
"ds_read_b64_tr_b16 a[238:239], v37 offset:30976\n"
"s_waitcnt lgkmcnt(4)\n"
"v_mfma_f32_16x16x32_bf16 v[208:211], v[188:191], a[140:143], v[208:211]\n"
"v_mfma_f32_16x16x32_bf16 v[212:215], v[188:191], a[172:175], v[212:215]\n"
"s_waitcnt vmcnt(30)\n"
"ds_read_b64_tr_b16 a[16:17], v36 offset:32768\n"
"ds_read_b64_tr_b16 a[18:19], v36 offset:33024\n"
"ds_read_b64_tr_b16 a[48:49], v37 offset:32768\n"
"ds_read_b64_tr_b16 a[50:51], v37 offset:33024\n"
"s_waitcnt lgkmcnt(4)\n"
"v_mfma_f32_16x16x32_bf16 v[216:219], v[188:191], a[204:207], v[216:219]\n"
"v_mfma_f32_16x16x32_bf16 v[220:223], v[188:191], a[236:239], v[220:223]\n"
"s_waitcnt vmcnt(28)\n"
"ds_read_b64_tr_b16 a[80:81], v36 offset:34816\n"
"ds_read_b64_tr_b16 a[82:83], v36 offset:35072\n"
"ds_read_b64_tr_b16 a[112:113], v37 offset:34816\n"
"ds_read_b64_tr_b16 a[114:115], v37 offset:35072\n"
"s_waitcnt lgkmcnt(4)\n"
"v_mfma_f32_16x16x32_bf16 v[192:195], v[188:191], a[16:19], v[192:195]\n"
"v_mfma_f32_16x16x32_bf16 v[196:199], v[188:191], a[48:51], v[196:199]\n"
"s_waitcnt vmcnt(26)\n"
"ds_read_b64_tr_b16 a[144:145], v36 offset:36864\n"
"ds_read_b64_tr_b16 a[146:147], v36 offset:37120\n"
"ds_read_b64_tr_b16 a[176:177], v37 offset:36864\n"
"ds_read_b64_tr_b16 a[178:179], v37 offset:37120\n"
"s_waitcnt lgkmcnt(4)\n"
"v_mfma_f32_16x16x32_bf16 v[200:203], v[188:191], a[80:83], v[200:203]\n"
"v_mfma_f32_16x16x32_bf16 v[204:207], v[188:191], a[112:115], v[204:207]\n"
"s_waitcnt vmcnt(24)\n"
"ds_read_b64_tr_b16 a[208:209], v36 offset:38912\n"
"ds_read_b64_tr_b16 a[210:211], v36 offset:39168\n"
"ds_read_b64_tr_b16 a[240:241], v37 offset:38912\n"
"ds_read_b64_tr_b16 a[242:243], v37 offset:39168\n"
"s_waitcnt lgkmcnt(4)\n"
"v_mfma_f32_16x16x32_bf16 v[208:211], v[188:191], a[144:147], v[208:211]\n"
"v_mfma_f32_16x16x32_bf16 v[212:215], v[188:191], a[176:179], v[212:215]\n"
"s_waitcnt vmcnt(22)\n"
"ds_read_b64_tr_b16 a[20:21], v36 offset:0\n"
"ds_read_b64_tr_b16 a[22:23], v36 offset:256\n"
"ds_read_b64_tr_b16 a[52:53], v37 offset:0\n"
"ds_read_b64_tr_b16 a[54:55], v37 offset:256\n"
"s_waitcnt lgkmcnt(4)\n"
"v_mfma_f32_16x16x32_bf16 v[216:219], v[188:191], a[208:211], v[216:219]\n"
"v_mfma_f32_16x16x32_bf16 v[220:223], v[188:191], a[240:243], v[220:223]\n"
"s_waitcnt vmcnt(20)\n"
"ds_read_b64_tr_b16 a[84:85], v36 offset:2048\n"
"ds_read_b64_tr_b16 a[86:87], v36 offset:2304\n"
"ds_read_b64_tr_b16 a[116:117], v37 offset:2048\n"
"ds_read_b64_tr_b16 a[118:119], v37 offset:2304\n"
"s_waitcnt lgkmcnt(4)\n"
"v_mfma_f32_16x16x32_bf16 v[192:195], v[188:191], a[20:23], v[192:195]\n"
"v_mfma_f32_16x16x32_bf16 v[196:199], v[188:191], a[52:55], v[196:199]\n"
"s_waitcnt vmcnt(18)\n"
"ds_read_b64_tr_b16 a[148:149], v36 offset:4096\n"
"ds_read_b64_tr_b16 a[150:151], v36 offset:4352\n"
"ds_read_b64_tr_b16 a[180:181], v37 offset:4096\n"
"ds_read_b64_tr_b16 a[182:183], v37 offset:4352\n"
"s_waitcnt lgkmcnt(4)\n"
"v_mfma_f32_16x16x32_bf16 v[200:203], v[188:191], a[84:87], v[200:203]\n"
"v_mfma_f32_16x16x32_bf16 v[204:207], v[188:191], a[116:119], v[204:207]\n"
"s_waitcnt vmcnt(16)\n"
"ds_read_b64_tr_b16 a[212:213], v36 offset:6144\n"
"ds_read_b64_tr_b16 a[214:215], v36 offset:6400\n"
"ds_read_b64_tr_b16 a[244:245], v37 offset:6144\n"
"ds_read_b64_tr_b16 a[246:247], v37 offset:6400\n"
"s_waitcnt lgkmcnt(4)\n"
"v_mfma_f32_16x16x32_bf16 v[208:211], v[188:191], a[148:151], v[208:211]\n"
"v_mfma_f32_16x16x32_bf16 v[212:215], v[188:191], a[180:183], v[212:215]\n"
"s_waitcnt vmcnt(14)\n"
"ds_read_b64_tr_b16 a[24:25], v36 offset:8192\n"
"ds_read_b64_tr_b16 a[26:27], v36 offset:8448\n"
"ds_read_b64_tr_b16 a[56:57], v37 offset:8192\n"
"ds_read_b64_tr_b16 a[58:59], v37 offset:8448\n"
"s_waitcnt lgkmcnt(4)\n"
"v_mfma_f32_16x16x32_bf16 v[216:219], v[188:191], a[212:215], v[216:219]\n"
"v_mfma_f32_16x16x32_bf16 v[220:223], v[188:191], a[244:247], v[220:223]\n"
"s_waitcnt vmcnt(12)\n"
"ds_read_b64_tr_b16 a[88:89], v36 offset:10240\n"
"ds_read_b64_tr_b16 a[90:91], v36 offset:10496\n"
"ds_read_b64_tr_b16 a[120:121], v37 offset:10240\n"
"ds_read_b64_tr_b16 a[122:123], v37 offset:10496\n"
"s_waitcnt lgkmcnt(4)\n"
"v_mfma_f32_16x16x32_bf16 v[192:195], v[188:191], a[24:27], v[192:195]\n"
"v_mfma_f32_16x16x32_bf16 v[196:199], v[188:191], a[56:59], v[196:199]\n"
"s_waitcnt vmcnt(10)\n"
"ds_read_b64_tr_b16 a[152:153], v36 offset:12288\n"
"ds_read_b64_tr_b16 a[154:155], v36 offset:12544\n"
"ds_read_b64_tr_b16 a[184:185], v37 offset:12288\n"
"ds_read_b64_tr_b16 a[186:187], v37 offset:12544\n"
"s_waitcnt lgkmcnt(4)\n"
"v_mfma_f32_16x16x32_bf16 v[200:203], v[188:191], a[88:91], v[200:203]\n"
"v_mfma_f32_16x16x32_bf16 v[204:207], v[188:191], a[120:123], v[204:207]\n"
"s_waitcnt vmcnt(8)\n"
"ds_read_b64_tr_b16 a[216:217], v36 offset:14336\n"
"ds_read_b64_tr_b16 a[218:219], v36 offset:14592\n"
"ds_read_b64_tr_b16 a[248:249], v37 offset:14336\n"
"ds_read_b64_tr_b16 a[250:251], v37 offset:14592\n"
"s_waitcnt lgkmcnt(4)\n"
"v_mfma_f32_16x16x32_bf16 v[208:211], v[188:191], a[152:155], v[208:211]\n"
"v_mfma_f32_16x16x32_bf16 v[212:215], v[188:191], a[184:187], v[212:215]\n"
"s_waitcnt vmcnt(6)\n"
"ds_read_b64_tr_b16 a[28:29], v36 offset:16384\n"
"ds_read_b64_tr_b16 a[30:31], v36 offset:16640\n"
"ds_read_b64_tr_b16 a[60:61], v37 offset:16384\n"
"ds_read_b64_tr_b16 a[62:63], v37 offset:16640\n"
"s_waitcnt lgkmcnt(4)\n"
"v_mfma_f32_16x16x32_bf16 v[216:219], v[188:191], a[216:219], v[216:219]\n"
"v_mfma_f32_16x16x32_bf16 v[220:223], v[188:191], a[248:251], v[220:223]\n"
"s_waitcnt vmcnt(4)\n"
"ds_read_b64_tr_b16 a[92:93], v36 offset:18432\n"
"ds_read_b64_tr_b16 a[94:95], v36 offset:18688\n"
"ds_read_b64_tr_b16 a[124:125], v37 offset:18432\n"
"ds_read_b64_tr_b16 a[126:127], v37 offset:18688\n"
"s_waitcnt lgkmcnt(4)\n"
"v_mfma_f32_16x16x32_bf16 v[192:195], v[188:191], a[28:31], v[192:195]\n"
"v_mfma_f32_16x16x32_bf16 v[196:199], v[188:191], a[60:63], v[196:199]\n"
"s_waitcnt vmcnt(2)\n"
"ds_read_b64_tr_b16 a[156:157], v36 offset:20480\n"
"ds_read_b64_tr_b16 a[158:159], v36 offset:20736\n"
"ds_read_b64_tr_b16 a[188:189], v37 offset:20480\n"
"ds_read_b64_tr_b16 a[190:191], v37 offset:20736\n"
"s_waitcnt lgkmcnt(4)\n"
"v_mfma_f32_16x16x32_bf16 v[200:203], v[188:191], a[92:95], v[200:203]\n"
"v_mfma_f32_16x16x32_bf16 v[204:207], v[188:191], a[124:127], v[204:207]\n"
"s_waitcnt vmcnt(0)\n"
"ds_read_b64_tr_b16 a[220:221], v36 offset:22528\n"
"ds_read_b64_tr_b16 a[222:223], v36 offset:22784\n"
"ds_read_b64_tr_b16 a[252:253], v37 offset:22528\n"
"ds_read_b64_tr_b16 a[254:255], v37 offset:22784\n"
"s_waitcnt lgkmcnt(4)\n"
"v_mfma_f32_16x16x32_bf16 v[208:211], v[188:191], a[156:159], v[208:211]\n"
"v_mfma_f32_16x16x32_bf16 v[212:215], v[188:191], a[188:191], v[212:215]\n"
"s_waitcnt lgkmcnt(0)\n"
"v_mfma_f32_16x16x32_bf16 v[216:219], v[188:191], a[220:223], v[216:219]\n"
"v_mfma_f32_16x16x32_bf16 v[220:223], v[188:191], a[252:255], v[220:223]\n"
"s_mov_b32 m0, s59\n"
"s_barrier\n"
"ds_write_addtid_b32 v192 offset:0\n"
"ds_write_addtid_b32 v193 offset:4640\n"
"ds_write_addtid_b32 v194 offset:9344\n"
"ds_write_addtid_b32 v195 offset:13984\n"
"ds_write_addtid_b32 v196 offset:256\n"
"ds_write_addtid_b32 v197 offset:4896\n"
"ds_write_addtid_b32 v198 offset:9600\n"
"ds_write_addtid_b32 v199 offset:14240\n"
"ds_write_addtid_b32 v200 offset:512\n"
"ds_write_addtid_b32 v201 offset:5152\n"
"ds_write_addtid_b32 v202 offset:9856\n"
"ds_write_addtid_b32 v203 offset:14496\n"
"ds_write_addtid_b32 v204 offset:768\n"
"ds_write_addtid_b32 v205 offset:5408\n"
"ds_write_addtid_b32 v206 offset:10112\n"
"ds_write_addtid_b32 v207 offset:14752\n"
"ds_write_addtid_b32 v208 offset:1024\n"
"ds_write_addtid_b32 v209 offset:5664\n"
"ds_write_addtid_b32 v210 offset:10368\n"
"ds_write_addtid_b32 v211 offset:15008\n"
"ds_write_addtid_b32 v212 offset:1280\n"
"ds_write_addtid_b32 v213 offset:5920\n"
"ds_write_addtid_b32 v214 offset:10624\n"
"ds_write_addtid_b32 v215 offset:15264\n"
"ds_write_addtid_b32 v216 offset:1536\n"
"ds_write_addtid_b32 v217 offset:6176\n"
"ds_write_addtid_b32 v218 offset:10880\n"
"ds_write_addtid_b32 v219 offset:15520\n"
"ds_write_addtid_b32 v220 offset:1792\n"
"ds_write_addtid_b32 v221 offset:6432\n"
"ds_write_addtid_b32 v222 offset:11136\n"
"ds_write_addtid_b32 v223 offset:15776\n"
"s_waitcnt lgkmcnt(0)\n"
"s_barrier\n"
"ds_read_b128 v[96:99], v2 offset:0\n"
"ds_read_b128 v[100:103], v3 offset:512\n"
"ds_read_b128 v[104:107], v4 offset:1024\n"
"ds_read_b128 v[108:111], v42 offset:1536\n"
"ds_read_b128 v[112:115], v2 offset:256\n"
"ds_read_b128 v[116:119], v3 offset:768\n"
"ds_read_b128 v[120:123], v4 offset:1280\n"
"ds_read_b128 v[124:127], v42 offset:1792\n"
"s_waitcnt lgkmcnt(0)\n"
"s_barrier\n"
"v_add_f32 v96, v96, v100\n"
"v_add_f32 v104, v104, v108\n"
"v_add_f32 v18, v96, v104\n"
"v_add_f32 v97, v97, v101\n"
"v_add_f32 v105, v105, v109\n"
"v_add_f32 v19, v97, v105\n"
"v_add_f32 v98, v98, v102\n"
"v_add_f32 v106, v106, v110\n"
"v_add_f32 v20, v98, v106\n"
"v_add_f32 v99, v99, v103\n"
"v_add_f32 v107, v107, v111\n"
"v_add_f32 v21, v99, v107\n"
"v_add_f32 v112, v112, v116\n"
"v_add_f32 v120, v120, v124\n"
"v_add_f32 v22, v112, v120\n"
"v_add_f32 v113, v113, v117\n"
"v_add_f32 v121, v121, v125\n"
"v_add_f32 v23, v113, v121\n"
"v_add_f32 v114, v114, v118\n"
"v_add_f32 v122, v122, v126\n"
"v_add_f32 v24, v114, v122\n"
"v_add_f32 v115, v115, v119\n"
"v_add_f32 v123, v123, v127\n"
"v_add_f32 v25, v115, v123\n"
"v_mov_b32 v134, 0\n"
"v_mov_b32 v135, 0\n"
"v_mov_b32 v154, 0\n"
"v_mov_b32 v155, 0\n"
"v_mov_b32 v144, 0\n"
"v_mov_b32 v145, 0\n"
"v_mov_b32 v138, 0\n"
"v_mov_b32 v139, 0\n"
"s_mov_b32 s93, 0x80000000\n"
"s_mov_b32 s95, 0x80000000\n"
"s_mov_b32 s94, 0x80000000\n"
"s_mov_b32 s95, 0x80000000\n"
"s_mov_b32 s93, 0x80000000\n"
"s_mov_b32 s95, 0x80000000\n"
"s_mov_b32 s94, 0x80000000\n"
"s_mov_b32 s95, 0x80000000\n"
"s_cmp_eq_u32 s54, 0\n"
"s_cbranch_scc1 .LF_1s\n"
"s_cmp_eq_u32 s54, 1\n"
"s_cbranch_scc1 .LF_2s\n"
"s_cmp_eq_u32 s54, 2\n"
"s_cbranch_scc1 .LF_3s\n"
"s_cmp_eq_u32 s54, 3\n"
"s_cbranch_scc1 .LF_4s\n"
".p2align 6\n"
".LF_top:\n"
"v_mfma_f32_16x16x32_bf16 v[48:51], v[100:103], a[0:3], 0\n"
"global_load_dwordx4 v[36:39], v6, s[56:57] offset:1024 sc1\n"
"v_mul_f32 v125, v125, v124\n"
"v_mfma_f32_16x16x32_bf16 v[48:51], v[104:107], a[4:7], v[48:51]\n"
"s_waitcnt vmcnt(10)\n"
"ds_write_addtid_b32 v124 offset:18592\n"
"ds_write_addtid_b32 v125 offset:18848\n"
"v_mfma_f32_16x16x32_bf16 v[48:51], v[108:111], a[8:11], v[48:51]\n"
"v_cvt_pk_bf16_f32 v139, v142, v143\n"
"v_pk_max_i16 v138, v138, 0\n"
"v_mfma_f32_16x16x32_bf16 v[48:51], v[64:67], a[12:15], v[48:51]\n"
"s_waitcnt vmcnt(8)\n"
"ds_read2st64_b32 v[140:141], v14 offset0:0 offset1:1\n"
"v_pk_max_i16 v139, v139, 0\n"
"v_mfma_f32_16x16x32_bf16 v[48:51], v[68:71], a[16:19], v[48:51]\n"
"ds_read_b128 v[56:59], v2 offset:0\n"
"ds_read_b128 v[52:55], v3 offset:2048\n"
"v_mfma_f32_16x16x32_bf16 v[48:51], v[72:75], a[20:23], v[48:51]\n"
"s_waitcnt vmcnt(6)\n"
"global_load_dwordx4 v[40:43], v7, s[56:57] offset:0 sc1\n"
"ds_read_b128 v[60:63], v4 offset:1024\n"
"v_mfma_f32_16x16x32_bf16 v[48:51], v[76:79], a[24:27], v[48:51]\n"
"ds_read_b128 v[84:87], v2 offset:256\n"
"ds_read_b128 v[88:91], v3 offset:2304\n"
"v_mfma_f32_16x16x32_bf16 v[48:51], v[80:83], a[28:31], v[48:51]\n"
"ds_read_b128 v[92:95], v4 offset:1280\n"
"v_mfma_f32_16x16x32_bf16 v[96:99], v[100:103], a[32:35], 0\n"
"v_mfma_f32_16x16x32_bf16 v[96:99], v[104:107], a[36:39], v[96:99]\n"
"ds_write_addtid_b32 v48 offset:0\n"
"v_mfma_f32_16x16x32_bf16 v[96:99], v[108:111], a[40:43], v[96:99]\n"
"ds_write_addtid_b32 v49 offset:4640\n"
"global_load_dwordx4 v[44:47], v7, s[56:57] offset:1024 sc1\n"
"v_mfma_f32_16x16x32_bf16 v[96:99], v[64:67], a[44:47], v[96:99]\n"
"s_waitcnt lgkmcnt(5)\n"
"buffer_store_dwordx4 v[136:139], v15, s[40:43], s95 offen sc1\n"
"ds_write_addtid_b32 v50 offset:9344\n"
"v_add_f32 v56, v56, v52\n"
"v_add_f32 v57, v57, v53\n"
"v_mfma_f32_16x16x32_bf16 v[96:99], v[68:71], a[48:51], v[96:99]\n"
"ds_write_addtid_b32 v51 offset:13984\n"
"v_add_f32 v58, v58, v54\n"
"v_mfma_f32_16x16x32_bf16 v[96:99], v[72:75], a[52:55], v[96:99]\n"
"v_add_f32 v59, v59, v55\n"
"v_add_f32 v56, v56, v60\n"
"v_mfma_f32_16x16x32_bf16 v[96:99], v[76:79], a[56:59], v[96:99]\n"
"v_add_f32 v57, v57, v61\n"
"v_add_f32 v58, v58, v62\n"
"v_mfma_f32_16x16x32_bf16 v[96:99], v[80:83], a[60:63], v[96:99]\n"
"v_add_f32 v59, v59, v63\n"
"v_add_f32 v56, v56, v128\n"
"v_mfma_f32_16x16x32_bf16 v[124:127], v[100:103], a[64:67], 0\n"
"global_load_dwordx4 v[48:51], v8, s[56:57] offset:0 sc1\n"
"v_add_f32 v57, v57, v129\n"
"v_mfma_f32_16x16x32_bf16 v[124:127], v[104:107], a[68:71], v[124:127]\n"
"ds_write_addtid_b32 v96 offset:256\n"
"v_add_f32 v58, v58, v130\n"
"v_mfma_f32_16x16x32_bf16 v[124:127], v[108:111], a[72:75], v[124:127]\n"
"ds_write_addtid_b32 v97 offset:4896\n"
"v_add_f32 v59, v59, v131\n"
"v_mfma_f32_16x16x32_bf16 v[124:127], v[64:67], a[76:79], v[124:127]\n"
"s_add_u32 s94, s50, 0xffd00000\n"
"s_and_b32 s95, s94, 0xfbffffff\n"
"ds_write_addtid_b32 v98 offset:9600\n"
"v_mfma_f32_16x16x32_bf16 v[124:127], v[68:71], a[80:83], v[124:127]\n"
"ds_write_addtid_b32 v99 offset:14240\n"
"global_load_dwordx4 v[52:55], v8, s[56:57] offset:1024 sc1\n"
"v_mfma_f32_16x16x32_bf16 v[124:127], v[72:75], a[84:87], v[124:127]\n"
"v_fma_f32 v60, v18, v141, v26\n"
"v_fma_f32 v61, v19, v141, v27\n"
"v_mfma_f32_16x16x32_bf16 v[124:127], v[76:79], a[88:91], v[124:127]\n"
"v_fma_f32 v62, v20, v141, v28\n"
"v_fma_f32 v63, v21, v141, v29\n"
"v_mfma_f32_16x16x32_bf16 v[124:127], v[80:83], a[92:95], v[124:127]\n"
"v_fma_f32 v56, v56, v140, v60\n"
"v_fma_f32 v57, v57, v140, v61\n"
"v_mfma_f32_16x16x32_bf16 v[96:99], v[100:103], a[96:99], 0\n"
"v_fma_f32 v58, v58, v140, v62\n"
"v_fma_f32 v59, v59, v140, v63\n"
"v_mfma_f32_16x16x32_bf16 v[96:99], v[104:107], a[100:103], v[96:99]\n"
"ds_write_addtid_b32 v124 offset:512\n"
"v_mul_f32_e64 v56, v56, |v56|\n"
"v_mfma_f32_16x16x32_bf16 v[96:99], v[108:111], a[104:107], v[96:99]\n"
"ds_write_addtid_b32 v125 offset:5152\n"
"v_mul_f32_e64 v57, v57, |v57|\n"
"v_mfma_f32_16x16x32_bf16 v[96:99], v[64:67], a[108:111], v[96:99]\n"
"ds_write_addtid_b32 v126 offset:9856\n"
"v_mul_f32_e64 v58, v58, |v58|\n"
"v_mfma_f32_16x16x32_bf16 v[96:99], v[68:71], a[112:115], v[96:99]\n"
"ds_write_addtid_b32 v127 offset:14496\n"
"v_mul_f32_e64 v59, v59, |v59|\n"
"v_mfma_f32_16x16x32_bf16 v[96:99], v[72:75], a[116:119], v[96:99]\n"
"v_cvt_pk_bf16_f32 v128, v56, v57\n"
"v_cvt_pk_bf16_f32 v129, v58, v59\n"
"v_mfma_f32_16x16x32_bf16 v[96:99], v[76:79], a[120:123], v[96:99]\n"
"v_pk_max_i16 v128, v128, 0\n"
"v_pk_max_i16 v129, v129, 0\n"
"v_mfma_f32_16x16x32_bf16 v[96:99], v[80:83], a[124:127], v[96:99]\n"
"s_waitcnt lgkmcnt(12)\n"
"v_add_f32 v84, v84, v88\n"
"v_add_f32 v85, v85, v89\n"
"v_mfma_f32_16x16x32_bf16 v[56:59], v[100:103], a[128:131], 0\n"
"v_add_f32 v86, v86, v90\n"
"v_add_f32 v87, v87, v91\n"
"v_mfma_f32_16x16x32_bf16 v[56:59], v[104:107], a[132:135], v[56:59]\n"
"ds_write_addtid_b32 v96 offset:768\n"
"v_add_f32 v84, v84, v92\n"
"v_mfma_f32_16x16x32_bf16 v[56:59], v[108:111], a[136:139], v[56:59]\n"
"ds_write_addtid_b32 v97 offset:5408\n"
"v_add_f32 v85, v85, v93\n"
"v_mfma_f32_16x16x32_bf16 v[56:59], v[64:67], a[140:143], v[56:59]\n"
"ds_write_addtid_b32 v98 offset:10112\n"
"v_add_f32 v86, v86, v94\n"
"v_mfma_f32_16x16x32_bf16 v[56:59], v[68:71], a[144:147], v[56:59]\n"
"ds_write_addtid_b32 v99 offset:14752\n"
"v_add_f32 v87, v87, v95\n"
"v_mfma_f32_16x16x32_bf16 v[56:59], v[72:75], a[148:151], v[56:59]\n"
"v_add_f32 v84, v84, v132\n"
"v_add_f32 v85, v85, v133\n"
"v_mfma_f32_16x16x32_bf16 v[56:59], v[76:79], a[152:155], v[56:59]\n"
"v_add_f32 v86, v86, v134\n"
"v_add_f32 v87, v87, v135\n"
"v_mfma_f32_16x16x32_bf16 v[56:59], v[80:83], a[156:159], v[56:59]\n"
"v_fma_f32 v60, v22, v141, v30\n"
"v_fma_f32 v61, v23, v141, v31\n"
"v_mfma_f32_16x16x32_bf16 v[88:91], v[100:103], a[160:163], 0\n"
"v_fma_f32 v62, v24, v141, v32\n"
"v_fma_f32 v63, v25, v141, v33\n"
"v_mfma_f32_16x16x32_bf16 v[88:91], v[104:107], a[164:167], v[88:91]\n"
"ds_write_addtid_b32 v56 offset:1024\n"
"v_fma_f32 v84, v84, v140, v60\n"
"v_mfma_f32_16x16x32_bf16 v[88:91], v[108:111], a[168:171], v[88:91]\n"
"s_add_u32 s58, s50, 0x180000\n"
"ds_write_addtid_b32 v57 offset:5664\n"
"v_fma_f32 v85, v85, v140, v61\n"
"v_mfma_f32_16x16x32_bf16 v[88:91], v[64:67], a[172:175], v[88:91]\n"
"s_and_b32 s58, s58, 0x3ffffff\n"
"ds_write_addtid_b32 v58 offset:10368\n"
"v_fma_f32 v86, v86, v140, v62\n"
"v_mfma_f32_16x16x32_bf16 v[88:91], v[68:71], a[176:179], v[88:91]\n"
"s_add_u32 s56, s40, s58\n"
"ds_write_addtid_b32 v59 offset:15008\n"
"v_fma_f32 v87, v87, v140, v63\n"
"v_mfma_f32_16x16x32_bf16 v[88:91], v[72:75], a[180:183], v[88:91]\n"
"s_addc_u32 s57, s41, 0\n"
"v_mul_f32_e64 v84, v84, |v84|\n"
"v_mul_f32_e64 v85, v85, |v85|\n"
"v_mfma_f32_16x16x32_bf16 v[88:91], v[76:79], a[184:187], v[88:91]\n"
"s_waitcnt vmcnt(6)\n"
"v_cmp_ne_u64 vcc, s[48:49], v[144:145]\n"
"v_mul_f32_e64 v86, v86, |v86|\n"
"v_mfma_f32_16x16x32_bf16 v[88:91], v[80:83], a[188:191], v[88:91]\n"
"v_mul_f32_e64 v87, v87, |v87|\n"
"v_cvt_pk_bf16_f32 v130, v84, v85\n"
"v_mfma_f32_16x16x32_bf16 v[92:95], a[192:195], v[100:103], 0\n"
"s_cbranch_vccnz .LF_3s\n"
".LF_3r:\n"
"v_cvt_pk_bf16_f32 v131, v86, v87\n"
"v_mfma_f32_16x16x32_bf16 v[92:95], a[196:199], v[104:107], v[92:95]\n"
"s_add_u32 s50, s50, 0x180000\n"
"ds_write_addtid_b32 v88 offset:1280\n"
"global_load_dwordx4 v[56:59], v5, s[56:57] offset:0 sc1\n"
"v_mfma_f32_16x16x32_bf16 v[92:95], a[200:203], v[108:111], v[92:95]\n"
"ds_write_addtid_b32 v89 offset:5920\n"
"v_pk_max_i16 v130, v130, 0\n"
"v_mfma_f32_16x16x32_bf16 v[92:95], a[204:207], v[64:67], v[92:95]\n"
"ds_write_addtid_b32 v90 offset:10624\n"
"v_pk_max_i16 v131, v131, 0\n"
"v_mfma_f32_16x16x32_bf16 v[92:95], a[208:211], v[68:71], v[92:95]\n"
"ds_write_addtid_b32 v91 offset:15264\n"
"v_mfma_f32_16x16x32_bf16 v[92:95], a[212:215], v[72:75], v[92:95]\n"
"v_mfma_f32_16x16x32_bf16 v[92:95], a[216:219], v[76:79], v[92:95]\n"
"v_mfma_f32_16x16x32_bf16 v[92:95], a[220:223], v[80:83], v[92:95]\n"
"v_mfma_f32_16x16x32_bf16 v[88:91], a[224:227], v[100:103], 0\n"
"s_waitcnt lgkmcnt(0)\n"
"global_load_dwordx4 v[60:63], v5, s[56:57] offset:1024 sc1\n"
"v_mfma_f32_16x16x32_bf16 v[88:91], a[228:231], v[104:107], v[88:91]\n"
"s_barrier\n"
"v_mfma_f32_16x16x32_bf16 v[88:91], a[232:235], v[108:111], v[88:91]\n"
"ds_read2st64_b32 v[136:137], v11 offset0:0 offset1:1\n"
"ds_read_b128 v[96:99], v2 offset:1536\n"
"v_mfma_f32_16x16x32_bf16 v[88:91], a[236:239], v[64:67], v[88:91]\n"
"s_waitcnt vmcnt(4)\n"
"buffer_load_dwordx2 v[138:139], v9, s[40:43], s50 offen sc1\n"
"v_mfma_f32_16x16x32_bf16 v[88:91], a[240:243], v[68:71], v[88:91]\n"
"global_load_dwordx4 v[64:67], v6, s[56:57] offset:0 sc1\n"
"ds_read_b128 v[84:87], v3 offset:512\n"
"v_mfma_f32_16x16x32_bf16 v[88:91], a[244:247], v[72:75], v[88:91]\n"
"ds_read_b128 v[100:103], v4 offset:2560\n"
"ds_read_b128 v[104:107], v2 offset:1792\n"
"v_mfma_f32_16x16x32_bf16 v[88:91], a[248:251], v[76:79], v[88:91]\n"
"ds_read_b128 v[108:111], v3 offset:768\n"
"ds_read_b128 v[124:127], v4 offset:2816\n"
"v_mfma_f32_16x16x32_bf16 v[88:91], a[252:255], v[80:83], v[88:91]\n"
"buffer_store_dwordx2 v[0:1], v16, s[40:43], s93 offen offset:0 sc1\n"
"v_mfma_f32_16x16x32_bf16 v[132:135], v[112:115], a[0:3], 0\n"
"buffer_store_dwordx4 v[128:131], v15, s[40:43], s95 offen sc1\n"
"v_mfma_f32_16x16x32_bf16 v[132:135], v[116:119], a[4:7], v[132:135]\n"
"v_mfma_f32_16x16x32_bf16 v[132:135], v[120:123], a[8:11], v[132:135]\n"
"global_load_dwordx4 v[68:71], v6, s[56:57] offset:1024 sc1\n"
"v_mfma_f32_16x16x32_bf16 v[132:135], v[36:39], a[12:15], v[132:135]\n"
"v_mfma_f32_16x16x32_bf16 v[132:135], v[40:43], a[16:19], v[132:135]\n"
"s_waitcnt lgkmcnt(3)\n"
"v_add_f32 v96, v96, v84\n"
"v_add_f32 v97, v97, v85\n"
"v_mfma_f32_16x16x32_bf16 v[132:135], v[44:47], a[20:23], v[132:135]\n"
"s_waitcnt vmcnt(7)\n"
"global_load_dwordx4 v[72:75], v7, s[56:57] offset:0 sc1\n"
"v_add_f32 v98, v98, v86\n"
"v_mfma_f32_16x16x32_bf16 v[132:135], v[48:51], a[24:27], v[132:135]\n"
"v_add_f32 v99, v99, v87\n"
"v_add_f32 v96, v96, v100\n"
"v_mfma_f32_16x16x32_bf16 v[132:135], v[52:55], a[28:31], v[132:135]\n"
"global_load_dwordx4 v[76:79], v7, s[56:57] offset:1024 sc1\n"
"v_add_f32 v97, v97, v101\n"
"v_mfma_f32_16x16x32_bf16 v[128:131], v[112:115], a[32:35], 0\n"
"v_add_f32 v98, v98, v102\n"
"v_add_f32 v99, v99, v103\n"
"v_mfma_f32_16x16x32_bf16 v[128:131], v[116:119], a[36:39], v[128:131]\n"
"ds_write_addtid_b32 v132 offset:1536\n"
"v_add_f32 v96, v96, v92\n"
"v_mfma_f32_16x16x32_bf16 v[128:131], v[120:123], a[40:43], v[128:131]\n"
"ds_write_addtid_b32 v133 offset:6176\n"
"v_add_f32 v97, v97, v93\n"
"v_mfma_f32_16x16x32_bf16 v[128:131], v[36:39], a[44:47], v[128:131]\n"
"ds_write_addtid_b32 v134 offset:10880\n"
"v_add_f32 v98, v98, v94\n"
"v_mfma_f32_16x16x32_bf16 v[128:131], v[40:43], a[48:51], v[128:131]\n"
"ds_write_addtid_b32 v135 offset:15520\n"
"v_add_f32 v99, v99, v95\n"
"v_mfma_f32_16x16x32_bf16 v[128:131], v[44:47], a[52:55], v[128:131]\n"
"s_add_u32 s93, s50, 0xffd00000\n"
"s_and_b32 s95, s93, 0xfbffffff\n"
"v_fma_f32 v84, v18, v137, v26\n"
"v_mfma_f32_16x16x32_bf16 v[128:131], v[48:51], a[56:59], v[128:131]\n"
"v_fma_f32 v85, v19, v137, v27\n"
"v_fma_f32 v86, v20, v137, v28\n"
"v_mfma_f32_16x16x32_bf16 v[128:131], v[52:55], a[60:63], v[128:131]\n"
"v_fma_f32 v87, v21, v137, v29\n"
"v_fma_f32 v96, v96, v136, v84\n"
"v_mfma_f32_16x16x32_bf16 v[92:95], v[112:115], a[64:67], 0\n"
"v_fma_f32 v97, v97, v136, v85\n"
"v_fma_f32 v98, v98, v136, v86\n"
"v_mfma_f32_16x16x32_bf16 v[92:95], v[116:119], a[68:71], v[92:95]\n"
"ds_write_addtid_b32 v128 offset:1792\n"
"global_load_dwordx4 v[80:83], v8, s[56:57] offset:0 sc1\n"
"v_mfma_f32_16x16x32_bf16 v[92:95], v[120:123], a[72:75], v[92:95]\n"
"ds_write_addtid_b32 v129 offset:6432\n"
"v_fma_f32 v99, v99, v136, v87\n"
"v_mfma_f32_16x16x32_bf16 v[92:95], v[36:39], a[76:79], v[92:95]\n"
"ds_write_addtid_b32 v130 offset:11136\n"
"v_mul_f32_e64 v96, v96, |v96|\n"
"v_mfma_f32_16x16x32_bf16 v[92:95], v[40:43], a[80:83], v[92:95]\n"
"ds_write_addtid_b32 v131 offset:15776\n"
"v_mul_f32_e64 v97, v97, |v97|\n"
"v_mfma_f32_16x16x32_bf16 v[92:95], v[44:47], a[84:87], v[92:95]\n"
"global_load_dwordx4 v[84:87], v8, s[56:57] offset:1024 sc1\n"
"v_mul_f32_e64 v98, v98, |v98|\n"
"v_mfma_f32_16x16x32_bf16 v[92:95], v[48:51], a[88:91], v[92:95]\n"
"v_mul_f32_e64 v99, v99, |v99|\n"
"v_cvt_pk_bf16_f32 v100, v96, v97\n"
"v_mfma_f32_16x16x32_bf16 v[92:95], v[52:55], a[92:95], v[92:95]\n"
"v_cvt_pk_bf16_f32 v101, v98, v99\n"
"v_pk_max_i16 v100, v100, 0\n"
"v_mfma_f32_16x16x32_bf16 v[128:131], v[112:115], a[96:99], 0\n"
"v_pk_max_i16 v101, v101, 0\n"
"v_mfma_f32_16x16x32_bf16 v[128:131], v[116:119], a[100:103], v[128:131]\n"
"s_waitcnt lgkmcnt(8)\n"
"ds_write_addtid_b32 v92 offset:2048\n"
"v_add_f32 v104, v104, v108\n"
"v_mfma_f32_16x16x32_bf16 v[128:131], v[120:123], a[104:107], v[128:131]\n"
"ds_write_addtid_b32 v93 offset:6688\n"
"v_add_f32 v105, v105, v109\n"
"v_mfma_f32_16x16x32_bf16 v[128:131], v[36:39], a[108:111], v[128:131]\n"
"ds_write_addtid_b32 v94 offset:11392\n"
"v_add_f32 v106, v106, v110\n"
"v_mfma_f32_16x16x32_bf16 v[128:131], v[40:43], a[112:115], v[128:131]\n"
"ds_write_addtid_b32 v95 offset:16032\n"
"v_add_f32 v107, v107, v111\n"
"v_mfma_f32_16x16x32_bf16 v[128:131], v[44:47], a[116:119], v[128:131]\n"
"v_add_f32 v104, v104, v124\n"
"v_add_f32 v105, v105, v125\n"
"v_mfma_f32_16x16x32_bf16 v[128:131], v[48:51], a[120:123], v[128:131]\n"
"v_add_f32 v106, v106, v126\n"
"v_add_f32 v107, v107, v127\n"
"v_mfma_f32_16x16x32_bf16 v[128:131], v[52:55], a[124:127], v[128:131]\n"
"v_add_f32 v104, v104, v88\n"
"v_add_f32 v105, v105, v89\n"
"v_mfma_f32_16x16x32_bf16 v[92:95], v[112:115], a[128:131], 0\n"
"v_add_f32 v106, v106, v90\n"
"v_add_f32 v107, v107, v91\n"
"v_mfma_f32_16x16x32_bf16 v[92:95], v[116:119], a[132:135], v[92:95]\n"
"ds_write_addtid_b32 v128 offset:2304\n"
"v_fma_f32 v96, v22, v137, v30\n"
"v_mfma_f32_16x16x32_bf16 v[92:95], v[120:123], a[136:139], v[92:95]\n"
"ds_write_addtid_b32 v129 offset:6944\n"
"v_fma_f32 v97, v23, v137, v31\n"
"v_mfma_f32_16x16x32_bf16 v[92:95], v[36:39], a[140:143], v[92:95]\n"
"ds_write_addtid_b32 v130 offset:11648\n"
"v_fma_f32 v98, v24, v137, v32\n"
"v_mfma_f32_16x16x32_bf16 v[92:95], v[40:43], a[144:147], v[92:95]\n"
"ds_write_addtid_b32 v131 offset:16288\n"
"v_fma_f32 v99, v25, v137, v33\n"
"v_mfma_f32_16x16x32_bf16 v[92:95], v[44:47], a[148:151], v[92:95]\n"
"v_fma_f32 v104, v104, v136, v96\n"
"v_fma_f32 v105, v105, v136, v97\n"
"v_mfma_f32_16x16x32_bf16 v[92:95], v[48:51], a[152:155], v[92:95]\n"
"v_fma_f32 v106, v106, v136, v98\n"
"v_fma_f32 v107, v107, v136, v99\n"
"v_mfma_f32_16x16x32_bf16 v[92:95], v[52:55], a[156:159], v[92:95]\n"
"v_mul_f32_e64 v104, v104, |v104|\n"
"v_mul_f32_e64 v105, v105, |v105|\n"
"v_mfma_f32_16x16x32_bf16 v[96:99], v[112:115], a[160:163], 0\n"
"v_mul_f32_e64 v106, v106, |v106|\n"
"v_mul_f32_e64 v107, v107, |v107|\n"
"v_mfma_f32_16x16x32_bf16 v[96:99], v[116:119], a[164:167], v[96:99]\n"
"ds_write_addtid_b32 v92 offset:2560\n"
"v_cvt_pk_bf16_f32 v102, v104, v105\n"
"v_mfma_f32_16x16x32_bf16 v[96:99], v[120:123], a[168:171], v[96:99]\n"
"s_add_u32 s58, s50, 0x180000\n"
"ds_write_addtid_b32 v93 offset:7200\n"
"v_cvt_pk_bf16_f32 v103, v106, v107\n"
"v_mfma_f32_16x16x32_bf16 v[96:99], v[36:39], a[172:175], v[96:99]\n"
"s_and_b32 s58, s58, 0x3ffffff\n"
"ds_write_addtid_b32 v94 offset:11904\n"
"v_pk_max_i16 v102, v102, 0\n"
"v_mfma_f32_16x16x32_bf16 v[96:99], v[40:43], a[176:179], v[96:99]\n"
"s_add_u32 s56, s40, s58\n"
"ds_write_addtid_b32 v95 offset:16544\n"
"v_pk_max_i16 v103, v103, 0\n"
"v_mfma_f32_16x16x32_bf16 v[96:99], v[44:47], a[180:183], v[96:99]\n"
"s_addc_u32 s57, s41, 0\n"
"buffer_store_dwordx4 v[100:103], v15, s[40:43], s95 offen sc1\n"
"v_mfma_f32_16x16x32_bf16 v[96:99], v[48:51], a[184:187], v[96:99]\n"
"s_waitcnt vmcnt(9)\n"
"v_cmp_ne_u64 vcc, s[48:49], v[138:139]\n"
"v_mfma_f32_16x16x32_bf16 v[96:99], v[52:55], a[188:191], v[96:99]\n"
"v_mfma_f32_16x16x32_bf16 v[100:103], a[192:195], v[112:115], 0\n"
"s_cbranch_vccnz .LF_4s\n"
".LF_4r:\n"
"v_mfma_f32_16x16x32_bf16 v[100:103], a[196:199], v[116:119], v[100:103]\n"
"s_add_u32 s50, s50, 0x180000\n"
"ds_write_addtid_b32 v96 offset:2816\n"
"v_mfma_f32_16x16x32_bf16 v[100:103], a[200:203], v[120:123], v[100:103]\n"
"s_waitcnt vmcnt(6)\n"
"buffer_store_dwordx2 v[0:1], v16, s[40:43], s94 offen offset:0 sc1\n"
"ds_write_addtid_b32 v97 offset:7456\n"
"v_mfma_f32_16x16x32_bf16 v[100:103], a[204:207], v[36:39], v[100:103]\n"
"ds_write_addtid_b32 v98 offset:12160\n"
"global_load_dwordx4 v[88:91], v5, s[56:57] offset:0 sc1\n"
"v_mfma_f32_16x16x32_bf16 v[100:103], a[208:211], v[40:43], v[100:103]\n"
"ds_write_addtid_b32 v99 offset:16800\n"
"v_mfma_f32_16x16x32_bf16 v[100:103], a[212:215], v[44:47], v[100:103]\n"
"v_mfma_f32_16x16x32_bf16 v[100:103], a[216:219], v[48:51], v[100:103]\n"
"v_mfma_f32_16x16x32_bf16 v[100:103], a[220:223], v[52:55], v[100:103]\n"
"v_mfma_f32_16x16x32_bf16 v[104:107], a[224:227], v[112:115], 0\n"
"s_waitcnt lgkmcnt(0)\n"
"global_load_dwordx4 v[92:95], v5, s[56:57] offset:1024 sc1\n"
"v_mfma_f32_16x16x32_bf16 v[104:107], a[228:231], v[116:119], v[104:107]\n"
"s_barrier\n"
"v_mfma_f32_16x16x32_bf16 v[104:107], a[232:235], v[120:123], v[104:107]\n"
"ds_read2st64_b32 v[132:133], v12 offset0:0 offset1:1\n"
"ds_read_b128 v[108:111], v2 offset:0\n"
"v_mfma_f32_16x16x32_bf16 v[104:107], a[236:239], v[36:39], v[104:107]\n"
"ds_read_b128 v[96:99], v3 offset:2048\n"
"ds_read_b128 v[112:115], v4 offset:1024\n"
"v_mfma_f32_16x16x32_bf16 v[104:107], a[240:243], v[40:43], v[104:107]\n"
"buffer_load_dwordx2 v[134:135], v9, s[40:43], s50 offen sc1\n"
"v_mfma_f32_16x16x32_bf16 v[104:107], a[244:247], v[44:47], v[104:107]\n"
"global_load_dwordx4 v[36:39], v6, s[56:57] offset:0 sc1\n"
"ds_read_b128 v[116:119], v2 offset:256\n"
"v_mfma_f32_16x16x32_bf16 v[104:107], a[248:251], v[48:51], v[104:107]\n"
"ds_read_b128 v[120:123], v3 offset:2304\n"
"ds_read_b128 v[124:127], v4 offset:1280\n"
"v_mfma_f32_16x16x32_bf16 v[104:107], a[252:255], v[52:55], v[104:107]\n"
"global_load_dwordx4 v[40:43], v6, s[56:57] offset:1024 sc1\n"
"v_mfma_f32_16x16x32_bf16 v[128:131], v[56:59], a[0:3], 0\n"
"v_mfma_f32_16x16x32_bf16 v[128:131], v[60:63], a[4:7], v[128:131]\n"
"s_waitcnt vmcnt(11)\n"
"v_mfma_f32_16x16x32_bf16 v[128:131], v[64:67], a[8:11], v[128:131]\n"
"s_waitcnt lgkmcnt(3)\n"
"v_add_f32 v108, v108, v96\n"
"v_add_f32 v109, v109, v97\n"
"v_mfma_f32_16x16x32_bf16 v[128:131], v[68:71], a[12:15], v[128:131]\n"
"s_waitcnt vmcnt(9)\n"
"v_add_f32 v110, v110, v98\n"
"v_add_f32 v111, v111, v99\n"
"v_mfma_f32_16x16x32_bf16 v[128:131], v[72:75], a[16:19], v[128:131]\n"
"v_add_f32 v108, v108, v112\n"
"v_add_f32 v109, v109, v113\n"
"v_mfma_f32_16x16x32_bf16 v[128:131], v[76:79], a[20:23], v[128:131]\n"
"s_waitcnt vmcnt(7)\n"
"v_add_f32 v110, v110, v114\n"
"v_add_f32 v111, v111, v115\n"
"v_mfma_f32_16x16x32_bf16 v[128:131], v[80:83], a[24:27], v[128:131]\n"
"v_add_f32 v108, v108, v100\n"
"v_add_f32 v109, v109, v101\n"
"v_mfma_f32_16x16x32_bf16 v[128:131], v[84:87], a[28:31], v[128:131]\n"
"global_load_dwordx4 v[44:47], v7, s[56:57] offset:0 sc1\n"
"v_add_f32 v110, v110, v102\n"
"v_mfma_f32_16x16x32_bf16 v[112:115], v[56:59], a[32:35], 0\n"
"v_add_f32 v111, v111, v103\n"
"v_mfma_f32_16x16x32_bf16 v[112:115], v[60:63], a[36:39], v[112:115]\n"
"ds_write_addtid_b32 v128 offset:0\n"
"global_load_dwordx4 v[48:51], v7, s[56:57] offset:1024 sc1\n"
"v_mfma_f32_16x16x32_bf16 v[112:115], v[64:67], a[40:43], v[112:115]\n"
"s_add_u32 s94, s50, 0xffd00000\n"
"s_and_b32 s95, s94, 0xfbffffff\n"
"ds_write_addtid_b32 v129 offset:4640\n"
"v_mfma_f32_16x16x32_bf16 v[112:115], v[68:71], a[44:47], v[112:115]\n"
"ds_write_addtid_b32 v130 offset:9344\n"
"v_fma_f32 v96, v18, v133, v26\n"
"v_mfma_f32_16x16x32_bf16 v[112:115], v[72:75], a[48:51], v[112:115]\n"
"ds_write_addtid_b32 v131 offset:13984\n"
"v_fma_f32 v97, v19, v133, v27\n"
"v_mfma_f32_16x16x32_bf16 v[112:115], v[76:79], a[52:55], v[112:115]\n"
"v_fma_f32 v98, v20, v133, v28\n"
"v_fma_f32 v99, v21, v133, v29\n"
"v_mfma_f32_16x16x32_bf16 v[112:115], v[80:83], a[56:59], v[112:115]\n"
"v_fma_f32 v108, v108, v132, v96\n"
"v_fma_f32 v109, v109, v132, v97\n"
"v_mfma_f32_16x16x32_bf16 v[112:115], v[84:87], a[60:63], v[112:115]\n"
"v_fma_f32 v110, v110, v132, v98\n"
"v_fma_f32 v111, v111, v132, v99\n"
"v_mfma_f32_16x16x32_bf16 v[100:103], v[56:59], a[64:67], 0\n"
"global_load_dwordx4 v[52:55], v8, s[56:57] offset:0 sc1\n"
"v_mul_f32_e64 v108, v108, |v108|\n"
"v_mfma_f32_16x16x32_bf16 v[100:103], v[60:63], a[68:71], v[100:103]\n"
"ds_write_addtid_b32 v112 offset:256\n"
"v_mul_f32_e64 v109, v109, |v109|\n"
"v_mfma_f32_16x16x32_bf16 v[100:103], v[64:67], a[72:75], v[100:103]\n"
"ds_write_addtid_b32 v113 offset:4896\n"
"v_mul_f32_e64 v110, v110, |v110|\n"
"v_mfma_f32_16x16x32_bf16 v[100:103], v[68:71], a[76:79], v[100:103]\n"
"ds_write_addtid_b32 v114 offset:9600\n"
"v_mul_f32_e64 v111, v111, |v111|\n"
"v_mfma_f32_16x16x32_bf16 v[100:103], v[72:75], a[80:83], v[100:103]\n"
"ds_write_addtid_b32 v115 offset:14240\n"
"global_load_dwordx4 v[96:99], v8, s[56:57] offset:1024 sc1\n"
"v_mfma_f32_16x16x32_bf16 v[100:103], v[76:79], a[84:87], v[100:103]\n"
"v_cvt_pk_bf16_f32 v128, v108, v109\n"
"v_cvt_pk_bf16_f32 v129, v110, v111\n"
"v_mfma_f32_16x16x32_bf16 v[100:103], v[80:83], a[88:91], v[100:103]\n"
"v_pk_max_i16 v128, v128, 0\n"
"v_pk_max_i16 v129, v129, 0\n"
"v_mfma_f32_16x16x32_bf16 v[100:103], v[84:87], a[92:95], v[100:103]\n"
"s_waitcnt lgkmcnt(8)\n"
"v_add_f32 v116, v116, v120\n"
"v_add_f32 v117, v117, v121\n"
"v_mfma_f32_16x16x32_bf16 v[108:111], v[56:59], a[96:99], 0\n"
"v_add_f32 v118, v118, v122\n"
"v_add_f32 v119, v119, v123\n"
"v_mfma_f32_16x16x32_bf16 v[108:111], v[60:63], a[100:103], v[108:111]\n"
"ds_write_addtid_b32 v100 offset:512\n"
"v_add_f32 v116, v116, v124\n"
"v_mfma_f32_16x16x32_bf16 v[108:111], v[64:67], a[104:107], v[108:111]\n"
"ds_write_addtid_b32 v101 offset:5152\n"
"v_add_f32 v117, v117, v125\n"
"v_mfma_f32_16x16x32_bf16 v[108:111], v[68:71], a[108:111], v[108:111]\n"
"ds_write_addtid_b32 v102 offset:9856\n"
"v_add_f32 v118, v118, v126\n"
"v_mfma_f32_16x16x32_bf16 v[108:111], v[72:75], a[112:115], v[108:111]\n"
"ds_write_addtid_b32 v103 offset:14496\n"
"v_add_f32 v119, v119, v127\n"
"v_mfma_f32_16x16x32_bf16 v[108:111], v[76:79], a[116:119], v[108:111]\n"
"s_waitcnt vmcnt(10)\n"
"buffer_store_dwordx2 v[0:1], v16, s[40:43], s93 offen offset:0 sc1\n"
"v_add_f32 v116, v116, v104\n"
"v_mfma_f32_16x16x32_bf16 v[108:111], v[80:83], a[120:123], v[108:111]\n"
"v_add_f32 v117, v117, v105\n"
"v_add_f32 v118, v118, v106\n"
"v_mfma_f32_16x16x32_bf16 v[108:111], v[84:87], a[124:127], v[108:111]\n"
"v_add_f32 v119, v119, v107\n"
"v_fma_f32 v100, v22, v133, v30\n"
"v_mfma_f32_16x16x32_bf16 v[112:115], v[56:59], a[128:131], 0\n"
"v_fma_f32 v101, v23, v133, v31\n"
"v_fma_f32 v102, v24, v133, v32\n"
"v_mfma_f32_16x16x32_bf16 v[112:115], v[60:63], a[132:135], v[112:115]\n"
"ds_write_addtid_b32 v108 offset:768\n"
"v_fma_f32 v103, v25, v133, v33\n"
"v_mfma_f32_16x16x32_bf16 v[112:115], v[64:67], a[136:139], v[112:115]\n"
"ds_write_addtid_b32 v109 offset:5408\n"
"v_fma_f32 v116, v116, v132, v100\n"
"v_mfma_f32_16x16x32_bf16 v[112:115], v[68:71], a[140:143], v[112:115]\n"
"ds_write_addtid_b32 v110 offset:10112\n"
"v_fma_f32 v117, v117, v132, v101\n"
"v_mfma_f32_16x16x32_bf16 v[112:115], v[72:75], a[144:147], v[112:115]\n"
"ds_write_addtid_b32 v111 offset:14752\n"
"v_fma_f32 v118, v118, v132, v102\n"
"v_mfma_f32_16x16x32_bf16 v[112:115], v[76:79], a[148:151], v[112:115]\n"
"v_fma_f32 v119, v119, v132, v103\n"
"v_mul_f32_e64 v116, v116, |v116|\n"
"v_mfma_f32_16x16x32_bf16 v[112:115], v[80:83], a[152:155], v[112:115]\n"
"v_mul_f32_e64 v117, v117, |v117|\n"
"v_mul_f32_e64 v118, v118, |v118|\n"
"v_mfma_f32_16x16x32_bf16 v[112:115], v[84:87], a[156:159], v[112:115]\n"
"v_mul_f32_e64 v119, v119, |v119|\n"
"v_cvt_pk_bf16_f32 v130, v116, v117\n"
"v_mfma_f32_16x16x32_bf16 v[108:111], v[56:59], a[160:163], 0\n"
"v_cvt_pk_bf16_f32 v131, v118, v119\n"
"v_pk_max_i16 v130, v130, 0\n"
"v_mfma_f32_16x16x32_bf16 v[108:111], v[60:63], a[164:167], v[108:111]\n"
"s_add_u32 s58, s50, 0x180000\n"
"ds_write_addtid_b32 v112 offset:1024\n"
"v_pk_max_i16 v131, v131, 0\n"
"v_mfma_f32_16x16x32_bf16 v[108:111], v[64:67], a[168:171], v[108:111]\n"
"s_and_b32 s58, s58, 0x3ffffff\n"
"ds_write_addtid_b32 v113 offset:5664\n"
"v_mfma_f32_16x16x32_bf16 v[108:111], v[68:71], a[172:175], v[108:111]\n"
"s_add_u32 s56, s40, s58\n"
"ds_write_addtid_b32 v114 offset:10368\n"
"v_mfma_f32_16x16x32_bf16 v[108:111], v[72:75], a[176:179], v[108:111]\n"
"s_addc_u32 s57, s41, 0\n"
"ds_write_addtid_b32 v115 offset:15008\n"
"v_mfma_f32_16x16x32_bf16 v[108:111], v[76:79], a[180:183], v[108:111]\n"
"s_waitcnt vmcnt(7)\n"
"buffer_store_dwordx4 v[128:131], v15, s[40:43], s95 offen sc1\n"
"v_cmp_ne_u64 vcc, s[48:49], v[134:135]\n"
"v_mfma_f32_16x16x32_bf16 v[108:111], v[80:83], a[184:187], v[108:111]\n"
"v_mfma_f32_16x16x32_bf16 v[108:111], v[84:87], a[188:191], v[108:111]\n"
"s_cbranch_vccnz .LF_1s\n"
".LF_1r:\n"
"v_mfma_f32_16x16x32_bf16 v[112:115], a[192:195], v[56:59], 0\n"
"s_add_u32 s50, s50, 0x180000\n"
"global_load_dwordx4 v[100:103], v5, s[56:57] offset:0 sc1\n"
"v_mfma_f32_16x16x32_bf16 v[112:115], a[196:199], v[60:63], v[112:115]\n"
"ds_write_addtid_b32 v108 offset:1280\n"
"global_load_dwordx4 v[124:127], v10, s[56:57] offset:0 sc1\n"
"v_mfma_f32_16x16x32_bf16 v[112:115], a[200:203], v[64:67], v[112:115]\n"
"ds_write_addtid_b32 v109 offset:5920\n"
"global_load_dwordx4 v[116:119], v10, s[56:57] offset:1024 sc1\n"
"v_mfma_f32_16x16x32_bf16 v[112:115], a[204:207], v[68:71], v[112:115]\n"
"ds_write_addtid_b32 v110 offset:10624\n"
"global_load_dwordx4 v[120:123], v10, s[56:57] offset:2048 sc1\n"
"v_mfma_f32_16x16x32_bf16 v[112:115], a[208:211], v[72:75], v[112:115]\n"
"ds_write_addtid_b32 v111 offset:15264\n"
"global_load_dwordx4 v[128:131], v10, s[56:57] offset:3072 sc1\n"
"v_mfma_f32_16x16x32_bf16 v[112:115], a[212:215], v[76:79], v[112:115]\n"
"global_load_dwordx4 v[104:107], v5, s[56:57] offset:1024 sc1\n"
"v_mfma_f32_16x16x32_bf16 v[112:115], a[216:219], v[80:83], v[112:115]\n"
"v_mfma_f32_16x16x32_bf16 v[112:115], a[220:223], v[84:87], v[112:115]\n"
"v_mfma_f32_16x16x32_bf16 v[132:135], a[224:227], v[56:59], 0\n"
"s_waitcnt lgkmcnt(0)\n"
"v_mfma_f32_16x16x32_bf16 v[132:135], a[228:231], v[60:63], v[132:135]\n"
"s_barrier\n"
"v_mfma_f32_16x16x32_bf16 v[132:135], a[232:235], v[64:67], v[132:135]\n"
"global_load_dwordx4 v[108:111], v6, s[56:57] offset:0 sc1\n"
"ds_read2st64_b32 v[152:153], v13 offset0:0 offset1:1\n"
"v_mfma_f32_16x16x32_bf16 v[132:135], a[236:239], v[68:71], v[132:135]\n"
"ds_read_b128 v[56:59], v2 offset:1536\n"
"ds_read_b128 v[60:63], v3 offset:512\n"
"v_mfma_f32_16x16x32_bf16 v[132:135], a[240:243], v[72:75], v[132:135]\n"
"ds_read_b128 v[136:139], v4 offset:2560\n"
"ds_read_b128 v[140:143], v2 offset:1792\n"
"v_mfma_f32_16x16x32_bf16 v[132:135], a[244:247], v[76:79], v[132:135]\n"
"buffer_load_dwordx2 v[154:155], v9, s[40:43], s50 offen sc1\n"
"v_mfma_f32_16x16x32_bf16 v[132:135], a[248:251], v[80:83], v[132:135]\n"
"global_load_dwordx4 v[64:67], v6, s[56:57] offset:1024 sc1\n"
"ds_read_b128 v[144:147], v3 offset:768\n"
"v_mfma_f32_16x16x32_bf16 v[132:135], a[252:255], v[84:87], v[132:135]\n"
"ds_read_b128 v[148:151], v4 offset:2816\n"
"v_mfma_f32_16x16x32_bf16 v[76:79], v[88:91], a[0:3], 0\n"
"v_mfma_f32_16x16x32_bf16 v[76:79], v[92:95], a[4:7], v[76:79]\n"
"s_waitcnt vmcnt(15)\n"
"v_mfma_f32_16x16x32_bf16 v[76:79], v[36:39], a[8:11], v[76:79]\n"
"s_waitcnt lgkmcnt(3)\n"
"v_add_f32 v56, v56, v60\n"
"v_add_f32 v57, v57, v61\n"
"v_mfma_f32_16x16x32_bf16 v[76:79], v[40:43], a[12:15], v[76:79]\n"
"s_waitcnt vmcnt(13)\n"
"v_add_f32 v58, v58, v62\n"
"v_add_f32 v59, v59, v63\n"
"v_mfma_f32_16x16x32_bf16 v[76:79], v[44:47], a[16:19], v[76:79]\n"
"global_load_dwordx4 v[68:71], v7, s[56:57] offset:0 sc1\n"
"v_add_f32 v56, v56, v136\n"
"v_mfma_f32_16x16x32_bf16 v[76:79], v[48:51], a[20:23], v[76:79]\n"
"s_waitcnt vmcnt(12)\n"
"v_add_f32 v57, v57, v137\n"
"v_add_f32 v58, v58, v138\n"
"v_mfma_f32_16x16x32_bf16 v[76:79], v[52:55], a[24:27], v[76:79]\n"
"v_add_f32 v59, v59, v139\n"
"v_add_f32 v56, v56, v112\n"
"v_mfma_f32_16x16x32_bf16 v[76:79], v[96:99], a[28:31], v[76:79]\n"
"v_add_f32 v57, v57, v113\n"
"v_add_f32 v58, v58, v114\n"
"v_mfma_f32_16x16x32_bf16 v[60:63], v[88:91], a[32:35], 0\n"
"v_add_f32 v59, v59, v115\n"
"v_mfma_f32_16x16x32_bf16 v[60:63], v[92:95], a[36:39], v[60:63]\n"
"ds_write_addtid_b32 v76 offset:1536\n"
"global_load_dwordx4 v[72:75], v7, s[56:57] offset:1024 sc1\n"
"v_mfma_f32_16x16x32_bf16 v[60:63], v[36:39], a[40:43], v[60:63]\n"
"s_add_u32 s93, s50, 0xffd00000\n"
"s_and_b32 s95, s93, 0xfbffffff\n"
"ds_write_addtid_b32 v77 offset:6176\n"
"v_mfma_f32_16x16x32_bf16 v[60:63], v[40:43], a[44:47], v[60:63]\n"
"ds_write_addtid_b32 v78 offset:10880\n"
"v_fma_f32 v80, v18, v153, v26\n"
"v_mfma_f32_16x16x32_bf16 v[60:63], v[44:47], a[48:51], v[60:63]\n"
"ds_write_addtid_b32 v79 offset:15520\n"
"v_fma_f32 v81, v19, v153, v27\n"
"v_mfma_f32_16x16x32_bf16 v[60:63], v[48:51], a[52:55], v[60:63]\n"
"v_fma_f32 v82, v20, v153, v28\n"
"v_fma_f32 v83, v21, v153, v29\n"
"v_mfma_f32_16x16x32_bf16 v[60:63], v[52:55], a[56:59], v[60:63]\n"
"v_fma_f32 v56, v56, v152, v80\n"
"v_fma_f32 v57, v57, v152, v81\n"
"v_mfma_f32_16x16x32_bf16 v[60:63], v[96:99], a[60:63], v[60:63]\n"
"global_load_dwordx4 v[76:79], v8, s[56:57] offset:0 sc1\n"
"v_fma_f32 v58, v58, v152, v82\n"
"v_mfma_f32_16x16x32_bf16 v[84:87], v[88:91], a[64:67], 0\n"
"v_fma_f32 v59, v59, v152, v83\n"
"v_mul_f32_e64 v56, v56, |v56|\n"
"v_mfma_f32_16x16x32_bf16 v[84:87], v[92:95], a[68:71], v[84:87]\n"
"ds_write_addtid_b32 v60 offset:1792\n"
"v_mul_f32_e64 v57, v57, |v57|\n"
"v_mfma_f32_16x16x32_bf16 v[84:87], v[36:39], a[72:75], v[84:87]\n"
"ds_write_addtid_b32 v61 offset:6432\n"
"v_mul_f32_e64 v58, v58, |v58|\n"
"v_mfma_f32_16x16x32_bf16 v[84:87], v[40:43], a[76:79], v[84:87]\n"
"ds_write_addtid_b32 v62 offset:11136\n"
"global_load_dwordx4 v[80:83], v8, s[56:57] offset:1024 sc1\n"
"v_mfma_f32_16x16x32_bf16 v[84:87], v[44:47], a[80:83], v[84:87]\n"
"ds_write_addtid_b32 v63 offset:15776\n"
"v_mul_f32_e64 v59, v59, |v59|\n"
"v_mfma_f32_16x16x32_bf16 v[84:87], v[48:51], a[84:87], v[84:87]\n"
"v_cvt_pk_bf16_f32 v136, v56, v57\n"
"v_cvt_pk_bf16_f32 v137, v58, v59\n"
"v_mfma_f32_16x16x32_bf16 v[84:87], v[52:55], a[88:91], v[84:87]\n"
"v_pk_max_i16 v136, v136, 0\n"
"v_pk_max_i16 v137, v137, 0\n"
"v_mfma_f32_16x16x32_bf16 v[84:87], v[96:99], a[92:95], v[84:87]\n"
"s_waitcnt lgkmcnt(8)\n"
"v_add_f32 v140, v140, v144\n"
"v_add_f32 v141, v141, v145\n"
"v_mfma_f32_16x16x32_bf16 v[56:59], v[88:91], a[96:99], 0\n"
"v_add_f32 v142, v142, v146\n"
"v_add_f32 v143, v143, v147\n"
"v_mfma_f32_16x16x32_bf16 v[56:59], v[92:95], a[100:103], v[56:59]\n"
"ds_write_addtid_b32 v84 offset:2048\n"
"v_add_f32 v140, v140, v148\n"
"v_mfma_f32_16x16x32_bf16 v[56:59], v[36:39], a[104:107], v[56:59]\n"
"ds_write_addtid_b32 v85 offset:6688\n"
"v_add_f32 v141, v141, v149\n"
"v_mfma_f32_16x16x32_bf16 v[56:59], v[40:43], a[108:111], v[56:59]\n"
"ds_write_addtid_b32 v86 offset:11392\n"
"v_add_f32 v142, v142, v150\n"
"v_mfma_f32_16x16x32_bf16 v[56:59], v[44:47], a[112:115], v[56:59]\n"
"ds_write_addtid_b32 v87 offset:16032\n"
"v_add_f32 v143, v143, v151\n"
"v_mfma_f32_16x16x32_bf16 v[56:59], v[48:51], a[116:119], v[56:59]\n"
"s_waitcnt vmcnt(13)\n"
"buffer_store_dwordx2 v[0:1], v16, s[40:43], s94 offen offset:0 sc1\n"
"v_add_f32 v140, v140, v132\n"
"v_mfma_f32_16x16x32_bf16 v[56:59], v[52:55], a[120:123], v[56:59]\n"
"v_add_f32 v141, v141, v133\n"
"v_add_f32 v142, v142, v134\n"
"v_mfma_f32_16x16x32_bf16 v[56:59], v[96:99], a[124:127], v[56:59]\n"
"v_add_f32 v143, v143, v135\n"
"v_fma_f32 v60, v22, v153, v30\n"
"v_mfma_f32_16x16x32_bf16 v[84:87], v[88:91], a[128:131], 0\n"
"v_fma_f32 v61, v23, v153, v31\n"
"v_fma_f32 v62, v24, v153, v32\n"
"v_mfma_f32_16x16x32_bf16 v[84:87], v[92:95], a[132:135], v[84:87]\n"
"ds_write_addtid_b32 v56 offset:2304\n"
"v_fma_f32 v63, v25, v153, v33\n"
"v_mfma_f32_16x16x32_bf16 v[84:87], v[36:39], a[136:139], v[84:87]\n"
"ds_write_addtid_b32 v57 offset:6944\n"
"v_fma_f32 v140, v140, v152, v60\n"
"v_mfma_f32_16x16x32_bf16 v[84:87], v[40:43], a[140:143], v[84:87]\n"
"ds_write_addtid_b32 v58 offset:11648\n"
"v_fma_f32 v141, v141, v152, v61\n"
"v_mfma_f32_16x16x32_bf16 v[84:87], v[44:47], a[144:147], v[84:87]\n"
"s_waitcnt vmcnt(9)\n"
"ds_write_addtid_b32 v59 offset:16288\n"
"v_add_f32 v124, v124, v116\n"
"v_mfma_f32_16x16x32_bf16 v[84:87], v[48:51], a[148:151], v[84:87]\n"
"v_add_f32 v125, v125, v117\n"
"v_add_f32 v126, v126, v118\n"
"v_mfma_f32_16x16x32_bf16 v[84:87], v[52:55], a[152:155], v[84:87]\n"
"v_add_f32 v127, v127, v119\n"
"v_add_f32 v120, v120, v128\n"
"v_mfma_f32_16x16x32_bf16 v[84:87], v[96:99], a[156:159], v[84:87]\n"
"v_add_f32 v121, v121, v129\n"
"v_add_f32 v122, v122, v130\n"
"v_mfma_f32_16x16x32_bf16 v[56:59], v[88:91], a[160:163], 0\n"
"v_add_f32 v123, v123, v131\n"
"v_add_f32 v124, v124, v120\n"
"v_mfma_f32_16x16x32_bf16 v[56:59], v[92:95], a[164:167], v[56:59]\n"
"ds_write_addtid_b32 v84 offset:2560\n"
"v_add_f32 v125, v125, v121\n"
"v_mfma_f32_16x16x32_bf16 v[56:59], v[36:39], a[168:171], v[56:59]\n"
"s_add_u32 s58, s50, 0x180000\n"
"ds_write_addtid_b32 v85 offset:7200\n"
"v_add_f32 v126, v126, v122\n"
"v_mfma_f32_16x16x32_bf16 v[56:59], v[40:43], a[172:175], v[56:59]\n"
"s_and_b32 s58, s58, 0x3ffffff\n"
"ds_write_addtid_b32 v86 offset:11904\n"
"v_add_f32 v127, v127, v123\n"
"v_mfma_f32_16x16x32_bf16 v[56:59], v[44:47], a[176:179], v[56:59]\n"
"s_add_u32 s56, s40, s58\n"
"ds_write_addtid_b32 v87 offset:16544\n"
"v_fma_f32 v142, v142, v152, v62\n"
"v_mfma_f32_16x16x32_bf16 v[56:59], v[48:51], a[180:183], v[56:59]\n"
"s_addc_u32 s57, s41, 0\n"
"v_permlane32_swap_b32 v124, v126\n"
"v_mfma_f32_16x16x32_bf16 v[56:59], v[52:55], a[184:187], v[56:59]\n"
"s_waitcnt vmcnt(6)\n"
"v_cmp_ne_u64 vcc, s[48:49], v[154:155]\n"
"v_fma_f32 v143, v143, v152, v63\n"
"v_mfma_f32_16x16x32_bf16 v[56:59], v[96:99], a[188:191], v[56:59]\n"
"v_permlane32_swap_b32 v125, v127\n"
"v_mfma_f32_16x16x32_bf16 v[128:131], a[192:195], v[88:91], 0\n"
"s_cbranch_vccnz .LF_2s\n"
".LF_2r:\n"
"v_add_f32 v124, v124, v126\n"
"v_mfma_f32_16x16x32_bf16 v[128:131], a[196:199], v[92:95], v[128:131]\n"
"s_add_u32 s50, s50, 0x180000\n"
"ds_write_addtid_b32 v56 offset:2816\n"
"v_add_f32 v125, v125, v127\n"
"v_mfma_f32_16x16x32_bf16 v[128:131], a[200:203], v[36:39], v[128:131]\n"
"ds_write_addtid_b32 v57 offset:7456\n"
"global_load_dwordx4 v[112:115], v5, s[56:57] offset:0 sc1\n"
"v_mfma_f32_16x16x32_bf16 v[128:131], a[204:207], v[40:43], v[128:131]\n"
"ds_write_addtid_b32 v58 offset:12160\n"
"v_mul_f32_e64 v140, v140, |v140|\n"
"v_mfma_f32_16x16x32_bf16 v[128:131], a[208:211], v[44:47], v[128:131]\n"
"ds_write_addtid_b32 v59 offset:16800\n"
"v_mul_f32_e64 v141, v141, |v141|\n"
"v_mfma_f32_16x16x32_bf16 v[128:131], a[212:215], v[48:51], v[128:131]\n"
"v_permlane16_swap_b32 v124, v125\n"
"v_mfma_f32_16x16x32_bf16 v[128:131], a[216:219], v[52:55], v[128:131]\n"
"v_add_f32 v124, v124, v125\n"
"v_mul_f32_e64 v142, v142, |v142|\n"
"v_mfma_f32_16x16x32_bf16 v[128:131], a[220:223], v[96:99], v[128:131]\n"
"global_load_dwordx4 v[116:119], v5, s[56:57] offset:1024 sc1\n"
"v_add_f32_dpp v125, v124, v124 row_ror:8 row_mask:0xf bank_mask:0xf\n"
"v_mfma_f32_16x16x32_bf16 v[132:135], a[224:227], v[88:91], 0\n"
"s_waitcnt lgkmcnt(0)\n"
"v_mov_b32 v124, v125\n"
"v_mul_f32_e64 v143, v143, |v143|\n"
"v_mfma_f32_16x16x32_bf16 v[132:135], a[228:231], v[92:95], v[132:135]\n"
"s_barrier\n"
"v_mfma_f32_16x16x32_bf16 v[132:135], a[232:235], v[36:39], v[132:135]\n"
"v_permlane16_swap_b32 v125, v124\n"
"v_mfma_f32_16x16x32_bf16 v[132:135], a[236:239], v[40:43], v[132:135]\n"
"global_load_dwordx4 v[120:123], v6, s[56:57] offset:0 sc1\n"
"v_mul_f32 v125, 0xba800000, v125\n"
"v_mfma_f32_16x16x32_bf16 v[132:135], a[240:243], v[44:47], v[132:135]\n"
"buffer_load_dwordx2 v[144:145], v9, s[40:43], s50 offen sc1\n"
"v_mfma_f32_16x16x32_bf16 v[132:135], a[244:247], v[48:51], v[132:135]\n"
"v_fma_f32 v124, v124, s62, v17\n"
"v_fma_f32 v124, -v125, v125, v124\n"
"v_mfma_f32_16x16x32_bf16 v[132:135], a[248:251], v[52:55], v[132:135]\n"
"v_max_f32 v124, v17, v124\n"
"v_cvt_pk_bf16_f32 v138, v140, v141\n"
"v_mfma_f32_16x16x32_bf16 v[132:135], a[252:255], v[96:99], v[132:135]\n"
"v_rsq_f32 v124, v124\n"
"s_branch .LF_top\n"
".LF_1s:\n"
"s_cmp_ge_u32 s50, s53\n"
"s_cbranch_scc1 .LF_end\n"
"s_cmp_ge_u32 s50, s52\n"
"s_cbranch_scc1 .LF_1r\n"
"s_sub_u32 s60, s50, 0x0\n"
"s_mov_b32 s61, 200000\n"
"s_nop 7\n"
".LF_1p:\n"
"buffer_load_dwordx2 v[134:135], v9, s[40:43], s60 offen sc1\n"
"s_waitcnt vmcnt(0)\n"
"v_cmp_ne_u64 vcc, s[48:49], v[134:135]\n"
"s_cbranch_vccz .LF_1px\n"
"s_sleep 1\n"
"s_sub_u32 s61, s61, 1\n"
"s_cbranch_scc0 .LF_1p\n"
"s_branch .LF_end\n"
".LF_1px:\n"
"s_branch .LF_1r\n"
".LF_2s:\n"
"s_cmp_ge_u32 s50, s53\n"
"s_cbranch_scc1 .LF_end\n"
"s_cmp_ge_u32 s50, s52\n"
"s_cbranch_scc1 .LF_2r\n"
"s_sub_u32 s60, s50, 0x0\n"
"s_mov_b32 s61, 200000\n"
"s_nop 7\n"
".LF_2p:\n"
"buffer_load_dwordx2 v[154:155], v9, s[40:43], s60 offen sc1\n"
"s_waitcnt vmcnt(0)\n"
"v_cmp_ne_u64 vcc, s[48:49], v[154:155]\n"
"s_cbranch_vccz .LF_2px\n"
"s_sleep 1\n"
"s_sub_u32 s61, s61, 1\n"
"s_cbranch_scc0 .LF_2p\n"
"s_branch .LF_end\n"
".LF_2px:\n"
"s_branch .LF_2r\n"
".LF_3s:\n"
"s_cmp_ge_u32 s50, s53\n"
"s_cbranch_scc1 .LF_end\n"
"s_cmp_ge_u32 s50, s52\n"
"s_cbranch_scc1 .LF_3r\n"
"s_sub_u32 s60, s50, 0x0\n"
"s_mov_b32 s61, 200000\n"
"s_nop 7\n"
".LF_3p:\n"
"buffer_load_dwordx2 v[144:145], v9, s[40:43], s60 offen sc1\n"
"s_waitcnt vmcnt(0)\n"
"v_cmp_ne_u64 vcc, s[48:49], v[144:145]\n"
"s_cbranch_vccz .LF_3px\n"
"s_sleep 1\n"
"s_sub_u32 s61, s61, 1\n"
"s_cbranch_scc0 .LF_3p\n"
"s_branch .LF_end\n"
".LF_3px:\n"
"s_branch .LF_3r\n"
".LF_4s:\n"
"s_cmp_ge_u32 s50, s53\n"
"s_cbranch_scc1 .LF_end\n"
"s_cmp_ge_u32 s50, s52\n"
"s_cbranch_scc1 .LF_4r\n"
"s_sub_u32 s60, s50, 0x0\n"
"s_mov_b32 s61, 200000\n"
"s_nop 7\n"
".LF_4p:\n"
"buffer_load_dwordx2 v[138:139], v9, s[40:43], s60 offen sc1\n"
"s_waitcnt vmcnt(0)\n"
"v_cmp_ne_u64 vcc, s[48:49], v[138:139]\n"
"s_cbranch_vccz .LF_4px\n"
"s_sleep 1\n"
"s_sub_u32 s61, s61, 1\n"
"s_cbranch_scc0 .LF_4p\n"
"s_branch .LF_end\n"
".LF_4px:\n"
"s_branch .LF_4r\n"
".LF_end:\n"
"s_waitcnt vmcnt(0) lgkmcnt(0)\n"
"s_endpgm\n"
      :: "{v2}"((unsigned)(lds0 + ((wid + 3) & 3) * 19104 + trp)), "{v3}"((unsigned)(lds0 + ((wid + 2) & 3) * 19104 + trp)), "{v4}"((unsigned)(lds0 + ((wid + 1) & 3) * 19104 + trp)), "{v5}"((unsigned)(lane * 16 + wid * 8192 + ((0 + rot) % 4) * 2048 + 134283264)), "{v6}"((unsigned)(lane * 16 + wid * 8192 + ((1 + rot) % 4) * 2048 + 134283264)), "{v7}"((unsigned)(lane * 16 + wid * 8192 + ((2 + rot) % 4) * 2048 + 134283264)), "{v8}"((unsigned)(lane * 16 + wid * 8192 + ((3 + rot) % 4) * 2048 + 134283264)), "{v9}"((unsigned)((lane & 31) * 8 + 352768 + F_GL * 1572864)), "{v10}"((unsigned)(lane * 16 + 134561792)), "{v11}"((unsigned)(lds0 + ((wid + 0) & 3) * 19104 + 18592 + ((li >> 1) + 32 * (li & 1)) * 4)), "{v12}"((unsigned)(lds0 + ((wid + 1) & 3) * 19104 + 18592 + ((li >> 1) + 32 * (li & 1)) * 4)), "{v13}"((unsigned)(lds0 + ((wid + 2) & 3) * 19104 + 18592 + ((li >> 1) + 32 * (li & 1)) * 4)), "{v14}"((unsigned)(lds0 + ((wid + 3) & 3) * 19104 + 18592 + ((li >> 1) + 32 * (li & 1)) * 4)), "{v15}"((unsigned)(lane * 16 + pair * 1024 + 98304)), "{v16}"((unsigned)(lane ? DROP : pair * 8 + 353024)), "{v17}"((unsigned)(0x3727c5ac)), "{v26}"((unsigned)(bia[0])), "{v27}"((unsigned)(bia[1])), "{v28}"((unsigned)(bia[2])), "{v29}"((unsigned)(bia[3])), "{v30}"((unsigned)(bia[4])), "{v31}"((unsigned)(bia[5])), "{v32}"((unsigned)(bia[6])), "{v33}"((unsigned)(bia[7])), "{v36}"((unsigned)(vT + tsw)), "{v37}"((unsigned)(vT + (tsw ^ 8))), "{v38}"((unsigned)(vD + ((0 + rot) % 4) * 458752)), "{v39}"((unsigned)(vD + ((1 + rot) % 4) * 458752)), "{v40}"((unsigned)(vD + ((2 + rot) % 4) * 458752)), "{v41}"((unsigned)(vD + ((3 + rot) % 4) * 458752)), "{v42}"((unsigned)(lds0 + wid * 19104 + trp)), "{s[40:43]}"(rS), "{s[44:47]}"(rW), "{s[48:49]}"(wsp), "{s50}"((unsigned)(sA)), "{s51}"((unsigned)(sLo)), "{s52}"((unsigned)(sHi)), "{s53}"((unsigned)(sEx)), "{s54}"((unsigned)(sEnt)), "{s55}"((unsigned)(sLd)), "{s[56:57]}"(wsb + (sLo & 0x3ffffff)), "{s59}"((unsigned)(lds0 + wid * 19104)), "{s62}"((unsigned)(0x3a800000)), "{s63}"((unsigned)(sW0 + 64 * ((wid + 1) & 3))), "{s64}"((unsigned)(sW0 + 64 * ((wid + 2) & 3))), "{s65}"((unsigned)(sW0 + 64 * ((wid + 3) & 3))), "{s66}"((unsigned)(sW0 + 64 * ((wid + 4) & 3)))
      : "v255", "a255", "s95", "vcc", "scc", "memory");

  } else {
    const int kq = idx / 7, grp = idx % 7, f0 = kq * 28 + wid * 7, pg0 = grp < 4 ? grp * 5 : 4 + grp * 4, pair = pg0 + wid;
    const unsigned rot = (set * 5) >> 1, tsw = (kq & 1) * 8, sW0 = f0 * 32 * 2048 + pg0 * 64, vD = (lane >> 2) * 2048 + (lane & 3) * 16;
    const u32x4 rW = desc(a.w2, 3584 * 2048);
    if (idx == 0) {
      const unsigned sEx = sHi + H_EX * 3 * RS, sEnt = -wid & 3;
    asm volatile(
"s_nop 4\n"
"v_mov_b32 v186, 0\n"
"global_load_dwordx2 v[0:1], v186, s[48:49] sc1\n"
"v_mov_b32 v188, 0x3f803f80\n"
"v_mov_b32 v189, 0x3f803f80\n"
"v_mov_b32 v190, 0x3f803f80\n"
"v_mov_b32 v191, 0x3f803f80\n"
"s_add_u32 m0, s55, 0\n"
"s_add_u32 s60, s66, 0\n"
"buffer_load_dwordx4 v42, s[44:47], s60 offen lds\n"
"s_add_u32 m0, s55, 1024\n"
"s_add_u32 s60, s66, 32768\n"
"buffer_load_dwordx4 v42, s[44:47], s60 offen lds\n"
"s_add_u32 m0, s55, 2048\n"
"s_add_u32 s60, s67, 0\n"
"buffer_load_dwordx4 v42, s[44:47], s60 offen lds\n"
"s_add_u32 m0, s55, 3072\n"
"s_add_u32 s60, s67, 32768\n"
"buffer_load_dwordx4 v42, s[44:47], s60 offen lds\n"
"s_add_u32 m0, s55, 4096\n"
"s_add_u32 s60, s68, 0\n"
"buffer_load_dwordx4 v42, s[44:47], s60 offen lds\n"
"s_add_u32 m0, s55, 5120\n"
"s_add_u32 s60, s68, 32768\n"
"buffer_load_dwordx4 v42, s[44:47], s60 offen lds\n"
"s_add_u32 m0, s55, 6144\n"
"s_add_u32 s60, s69, 0\n"
"buffer_load_dwordx4 v42, s[44:47], s60 offen lds\n"
"s_add_u32 m0, s55, 7168\n"
"s_add_u32 s60, s69, 32768\n"
"buffer_load_dwordx4 v42, s[44:47], s60 offen lds\n"
"s_add_u32 m0, s55, 8192\n"
"s_add_u32 s60, s70, 0\n"
"buffer_load_dwordx4 v42, s[44:47], s60 offen lds\n"
"s_add_u32 m0, s55, 9216\n"
"s_add_u32 s60, s70, 32768\n"
"buffer_load_dwordx4 v42, s[44:47], s60 offen lds\n"
"s_add_u32 m0, s55, 10240\n"
"s_add_u32 s60, s66, 65536\n"
"buffer_load_dwordx4 v42, s[44:47], s60 offen lds\n"
"s_add_u32 m0, s55, 11264\n"
"s_add_u32 s60, s66, 98304\n"
"buffer_load_dwordx4 v42, s[44:47], s60 offen lds\n"
"s_add_u32 m0, s55, 12288\n"
"s_add_u32 s60, s67, 65536\n"
"buffer_load_dwordx4 v42, s[44:47], s60 offen lds\n"
"s_add_u32 m0, s55, 13312\n"
"s_add_u32 s60, s67, 98304\n"
"buffer_load_dwordx4 v42, s[44:47], s60 offen lds\n"
"s_add_u32 m0, s55, 14336\n"
"s_add_u32 s60, s68, 65536\n"
"buffer_load_dwordx4 v42, s[44:47], s60 offen lds\n"
"s_add_u32 m0, s55, 15360\n"
"s_add_u32 s60, s68, 98304\n"
"buffer_load_dwordx4 v42, s[44:47], s60 offen lds\n"
"s_add_u32 m0, s55, 16384\n"
"s_add_u32 s60, s69, 65536\n"
"buffer_load_dwordx4 v42, s[44:47], s60 offen lds\n"
"s_add_u32 m0, s55, 17408\n"
"s_add_u32 s60, s69, 98304\n"
"buffer_load_dwordx4 v42, s[44:47], s60 offen lds\n"
"s_add_u32 m0, s55, 18432\n"
"s_add_u32 s60, s70, 65536\n"
"buffer_load_dwordx4 v42, s[44:47], s60 offen lds\n"
"s_add_u32 m0, s55, 19456\n"
"s_add_u32 s60, s70, 98304\n"
"buffer_load_dwordx4 v42, s[44:47], s60 offen lds\n"
"s_add_u32 m0, s55, 20480\n"
"s_add_u32 s60, s66, 131072\n"
"buffer_load_dwordx4 v42, s[44:47], s60 offen lds\n"
"s_add_u32 m0, s55, 21504\n"
"s_add_u32 s60, s66, 163840\n"
"buffer_load_dwordx4 v42, s[44:47], s60 offen lds\n"
"s_add_u32 m0, s55, 22528\n"
"s_add_u32 s60, s67, 131072\n"
"buffer_load_dwordx4 v42, s[44:47], s60 offen lds\n"
"s_add_u32 m0, s55, 23552\n"
"s_add_u32 s60, s67, 163840\n"
"buffer_load_dwordx4 v42, s[44:47], s60 offen lds\n"
"s_add_u32 m0, s55, 24576\n"
"s_add_u32 s60, s68, 131072\n"
"buffer_load_dwordx4 v42, s[44:47], s60 offen lds\n"
"s_add_u32 m0, s55, 25600\n"
"s_add_u32 s60, s68, 163840\n"
"buffer_load_dwordx4 v42, s[44:47], s60 offen lds\n"
"s_add_u32 m0, s55, 26624\n"
"s_add_u32 s60, s69, 131072\n"
"buffer_load_dwordx4 v42, s[44:47], s60 offen lds\n"
"s_add_u32 m0, s55, 27648\n"
"s_add_u32 s60, s69, 163840\n"
"buffer_load_dwordx4 v42, s[44:47], s60 offen lds\n"
"s_add_u32 m0, s55, 28672\n"
"s_add_u32 s60, s70, 131072\n"
"buffer_load_dwordx4 v42, s[44:47], s60 offen lds\n"
"s_add_u32 m0, s55, 29696\n"
"s_add_u32 s60, s70, 163840\n"
"buffer_load_dwordx4 v42, s[44:47], s60 offen lds\n"
"s_add_u32 m0, s55, 30720\n"
"s_add_u32 s60, s66, 196608\n"
"buffer_load_dwordx4 v42, s[44:47], s60 offen lds\n"
"s_add_u32 m0, s55, 31744\n"
"s_add_u32 s60, s66, 229376\n"
"buffer_load_dwordx4 v42, s[44:47], s60 offen lds\n"
"s_add_u32 m0, s55, 32768\n"
"s_add_u32 s60, s67, 196608\n"
"buffer_load_dwordx4 v42, s[44:47], s60 offen lds\n"
"s_add_u32 m0, s55, 33792\n"
"s_add_u32 s60, s67, 229376\n"
"buffer_load_dwordx4 v42, s[44:47], s60 offen lds\n"
"s_add_u32 m0, s55, 34816\n"
"s_add_u32 s60, s68, 196608\n"
"buffer_load_dwordx4 v42, s[44:47], s60 offen lds\n"
"s_add_u32 m0, s55, 35840\n"
"s_add_u32 s60, s68, 229376\n"
"buffer_load_dwordx4 v42, s[44:47], s60 offen lds\n"
"s_add_u32 m0, s55, 36864\n"
"s_add_u32 s60, s69, 196608\n"
"buffer_load_dwordx4 v42, s[44:47], s60 offen lds\n"
"s_add_u32 m0, s55, 37888\n"
"s_add_u32 s60, s69, 229376\n"
"buffer_load_dwordx4 v42, s[44:47], s60 offen lds\n"
"s_add_u32 m0, s55, 38912\n"
"s_add_u32 s60, s70, 196608\n"
"buffer_load_dwordx4 v42, s[44:47], s60 offen lds\n"
"s_add_u32 m0, s55, 39936\n"
"s_add_u32 s60, s70, 229376\n"
"buffer_load_dwordx4 v42, s[44:47], s60 offen lds\n"
"s_waitcnt vmcnt(38)\n"
"ds_read_b64_tr_b16 a[0:1], v40 offset:0\n"
"ds_read_b64_tr_b16 a[2:3], v40 offset:256\n"
"ds_read_b64_tr_b16 a[28:29], v41 offset:0\n"
"ds_read_b64_tr_b16 a[30:31], v41 offset:256\n"
"v_add_u32 v0, 2, v0\n"
"v_add_u32 v1, 0x9e3779b9, v1\n"
"v_or_b32 v0, 1, v0\n"
"v_readfirstlane_b32 s49, v1\n"
"v_readfirstlane_b32 s48, v0\n"
"s_waitcnt vmcnt(36)\n"
"ds_read_b64_tr_b16 a[56:57], v40 offset:2048\n"
"ds_read_b64_tr_b16 a[58:59], v40 offset:2304\n"
"ds_read_b64_tr_b16 a[84:85], v41 offset:2048\n"
"ds_read_b64_tr_b16 a[86:87], v41 offset:2304\n"
"s_waitcnt lgkmcnt(4)\n"
"s_add_u32 m0, s55, 0\n"
"s_add_u32 s60, s66, 262144\n"
"buffer_load_dwordx4 v42, s[44:47], s60 offen lds\n"
"s_add_u32 m0, s55, 1024\n"
"s_add_u32 s60, s66, 294912\n"
"buffer_load_dwordx4 v42, s[44:47], s60 offen lds\n"
"s_waitcnt vmcnt(36)\n"
"ds_read_b64_tr_b16 a[112:113], v40 offset:4096\n"
"ds_read_b64_tr_b16 a[114:115], v40 offset:4352\n"
"ds_read_b64_tr_b16 a[140:141], v41 offset:4096\n"
"ds_read_b64_tr_b16 a[142:143], v41 offset:4352\n"
"s_waitcnt lgkmcnt(4)\n"
"s_add_u32 m0, s55, 2048\n"
"s_add_u32 s60, s67, 262144\n"
"buffer_load_dwordx4 v42, s[44:47], s60 offen lds\n"
"s_add_u32 m0, s55, 3072\n"
"s_add_u32 s60, s67, 294912\n"
"buffer_load_dwordx4 v42, s[44:47], s60 offen lds\n"
"s_waitcnt vmcnt(36)\n"
"ds_read_b64_tr_b16 a[168:169], v40 offset:6144\n"
"ds_read_b64_tr_b16 a[170:171], v40 offset:6400\n"
"ds_read_b64_tr_b16 a[196:197], v41 offset:6144\n"
"ds_read_b64_tr_b16 a[198:199], v41 offset:6400\n"
"s_waitcnt lgkmcnt(4)\n"
"s_add_u32 m0, s55, 4096\n"
"s_add_u32 s60, s68, 262144\n"
"buffer_load_dwordx4 v42, s[44:47], s60 offen lds\n"
"s_add_u32 m0, s55, 5120\n"
"s_add_u32 s60, s68, 294912\n"
"buffer_load_dwordx4 v42, s[44:47], s60 offen lds\n"
"s_waitcnt vmcnt(36)\n"
"ds_read_b64_tr_b16 a[224:225], v40 offset:8192\n"
"ds_read_b64_tr_b16 a[226:227], v40 offset:8448\n"
"ds_read_b64_tr_b16 a[252:253], v41 offset:8192\n"
"ds_read_b64_tr_b16 a[254:255], v41 offset:8448\n"
"s_waitcnt lgkmcnt(4)\n"
"s_add_u32 m0, s55, 6144\n"
"s_add_u32 s60, s69, 262144\n"
"buffer_load_dwordx4 v42, s[44:47], s60 offen lds\n"
"s_add_u32 m0, s55, 7168\n"
"s_add_u32 s60, s69, 294912\n"
"buffer_load_dwordx4 v42, s[44:47], s60 offen lds\n"
"s_waitcnt vmcnt(36)\n"
"ds_read_b64_tr_b16 a[4:5], v40 offset:10240\n"
"ds_read_b64_tr_b16 a[6:7], v40 offset:10496\n"
"ds_read_b64_tr_b16 a[32:33], v41 offset:10240\n"
"ds_read_b64_tr_b16 a[34:35], v41 offset:10496\n"
"s_waitcnt lgkmcnt(4)\n"
"s_add_u32 m0, s55, 8192\n"
"s_add_u32 s60, s70, 262144\n"
"buffer_load_dwordx4 v42, s[44:47], s60 offen lds\n"
"s_add_u32 m0, s55, 9216\n"
"s_add_u32 s60, s70, 294912\n"
"buffer_load_dwordx4 v42, s[44:47], s60 offen lds\n"
"s_waitcnt vmcnt(36)\n"
"ds_read_b64_tr_b16 a[60:61], v40 offset:12288\n"
"ds_read_b64_tr_b16 a[62:63], v40 offset:12544\n"
"ds_read_b64_tr_b16 a[88:89], v41 offset:12288\n"
"ds_read_b64_tr_b16 a[90:91], v41 offset:12544\n"
"s_waitcnt lgkmcnt(4)\n"
"s_add_u32 m0, s55, 10240\n"
"s_add_u32 s60, s66, 327680\n"
"buffer_load_dwordx4 v42, s[44:47], s60 offen lds\n"
"s_add_u32 m0, s55, 11264\n"
"s_add_u32 s60, s66, 360448\n"
"buffer_load_dwordx4 v42, s[44:47], s60 offen lds\n"
"s_waitcnt vmcnt(36)\n"
"ds_read_b64_tr_b16 a[116:117], v40 offset:14336\n"
"ds_read_b64_tr_b16 a[118:119], v40 offset:14592\n"
"ds_read_b64_tr_b16 a[144:145], v41 offset:14336\n"
"ds_read_b64_tr_b16 a[146:147], v41 offset:14592\n"
"s_waitcnt lgkmcnt(4)\n"
"s_add_u32 m0, s55, 12288\n"
"s_add_u32 s60, s67, 327680\n"
"buffer_load_dwordx4 v42, s[44:47], s60 offen lds\n"
"s_add_u32 m0, s55, 13312\n"
"s_add_u32 s60, s67, 360448\n"
"buffer_load_dwordx4 v42, s[44:47], s60 offen lds\n"
"s_waitcnt vmcnt(36)\n"
"ds_read_b64_tr_b16 a[172:173], v40 offset:16384\n"
"ds_read_b64_tr_b16 a[174:175], v40 offset:16640\n"
"ds_read_b64_tr_b16 a[200:201], v41 offset:16384\n"
"ds_read_b64_tr_b16 a[202:203], v41 offset:16640\n"
"s_waitcnt lgkmcnt(4)\n"
"s_add_u32 m0, s55, 14336\n"
"s_add_u32 s60, s68, 327680\n"
"buffer_load_dwordx4 v42, s[44:47], s60 offen lds\n"
"s_add_u32 m0, s55, 15360\n"
"s_add_u32 s60, s68, 360448\n"
"buffer_load_dwordx4 v42, s[44:47], s60 offen lds\n"
"s_waitcnt vmcnt(36)\n"
"ds_read_b64_tr_b16 a[228:229], v40 offset:18432\n"
"ds_read_b64_tr_b16 a[230:231], v40 offset:18688\n"
"ds_read_b64_tr_b16 v[16:17], v41 offset:18432\n"
"ds_read_b64_tr_b16 v[18:19], v41 offset:18688\n"
"s_waitcnt lgkmcnt(4)\n"
"s_add_u32 m0, s55, 16384\n"
"s_add_u32 s60, s69, 327680\n"
"buffer_load_dwordx4 v42, s[44:47], s60 offen lds\n"
"s_add_u32 m0, s55, 17408\n"
"s_add_u32 s60, s69, 360448\n"
"buffer_load_dwordx4 v42, s[44:47], s60 offen lds\n"
"s_waitcnt vmcnt(36)\n"
"ds_read_b64_tr_b16 a[8:9], v40 offset:20480\n"
"ds_read_b64_tr_b16 a[10:11], v40 offset:20736\n"
"ds_read_b64_tr_b16 a[36:37], v41 offset:20480\n"
"ds_read_b64_tr_b16 a[38:39], v41 offset:20736\n"
"s_waitcnt lgkmcnt(4)\n"
"s_add_u32 m0, s55, 18432\n"
"s_add_u32 s60, s70, 327680\n"
"buffer_load_dwordx4 v42, s[44:47], s60 offen lds\n"
"s_add_u32 m0, s55, 19456\n"
"s_add_u32 s60, s70, 360448\n"
"buffer_load_dwordx4 v42, s[44:47], s60 offen lds\n"
"s_waitcnt vmcnt(36)\n"
"ds_read_b64_tr_b16 a[64:65], v40 offset:22528\n"
"ds_read_b64_tr_b16 a[66:67], v40 offset:22784\n"
"ds_read_b64_tr_b16 a[92:93], v41 offset:22528\n"
"ds_read_b64_tr_b16 a[94:95], v41 offset:22784\n"
"s_waitcnt lgkmcnt(4)\n"
"s_add_u32 m0, s55, 20480\n"
"s_add_u32 s60, s66, 393216\n"
"buffer_load_dwordx4 v42, s[44:47], s60 offen lds\n"
"s_add_u32 m0, s55, 21504\n"
"s_add_u32 s60, s66, 425984\n"
"buffer_load_dwordx4 v42, s[44:47], s60 offen lds\n"
"s_waitcnt vmcnt(36)\n"
"ds_read_b64_tr_b16 a[120:121], v40 offset:24576\n"
"ds_read_b64_tr_b16 a[122:123], v40 offset:24832\n"
"ds_read_b64_tr_b16 a[148:149], v41 offset:24576\n"
"ds_read_b64_tr_b16 a[150:151], v41 offset:24832\n"
"s_waitcnt lgkmcnt(4)\n"
"s_add_u32 m0, s55, 22528\n"
"s_add_u32 s60, s67, 393216\n"
"buffer_load_dwordx4 v42, s[44:47], s60 offen lds\n"
"s_add_u32 m0, s55, 23552\n"
"s_add_u32 s60, s67, 425984\n"
"buffer_load_dwordx4 v42, s[44:47], s60 offen lds\n"
"s_waitcnt vmcnt(36)\n"
"ds_read_b64_tr_b16 a[176:177], v40 offset:26624\n"
"ds_read_b64_tr_b16 a[178:179], v40 offset:26880\n"
"ds_read_b64_tr_b16 a[204:205], v41 offset:26624\n"
"ds_read_b64_tr_b16 a[206:207], v41 offset:26880\n"
"s_waitcnt lgkmcnt(4)\n"
"s_add_u32 m0, s55, 24576\n"
"s_add_u32 s60, s68, 393216\n"
"buffer_load_dwordx4 v42, s[44:47], s60 offen lds\n"
"s_add_u32 m0, s55, 25600\n"
"s_add_u32 s60, s68, 425984\n"
"buffer_load_dwordx4 v42, s[44:47], s60 offen lds\n"
"s_waitcnt vmcnt(36)\n"
"ds_read_b64_tr_b16 a[232:233], v40 offset:28672\n"
"ds_read_b64_tr_b16 a[234:235], v40 offset:28928\n"
"ds_read_b64_tr_b16 v[20:21], v41 offset:28672\n"
"ds_read_b64_tr_b16 v[22:23], v41 offset:28928\n"
"s_waitcnt lgkmcnt(4)\n"
"s_add_u32 m0, s55, 26624\n"
"s_add_u32 s60, s69, 393216\n"
"buffer_load_dwordx4 v42, s[44:47], s60 offen lds\n"
"s_add_u32 m0, s55, 27648\n"
"s_add_u32 s60, s69, 425984\n"
"buffer_load_dwordx4 v42, s[44:47], s60 offen lds\n"
"s_waitcnt vmcnt(36)\n"
"ds_read_b64_tr_b16 a[12:13], v40 offset:30720\n"
"ds_read_b64_tr_b16 a[14:15], v40 offset:30976\n"
"ds_read_b64_tr_b16 a[40:41], v41 offset:30720\n"
"ds_read_b64_tr_b16 a[42:43], v41 offset:30976\n"
"s_waitcnt lgkmcnt(4)\n"
"s_add_u32 m0, s55, 28672\n"
"s_add_u32 s60, s70, 393216\n"
"buffer_load_dwordx4 v42, s[44:47], s60 offen lds\n"
"s_add_u32 m0, s55, 29696\n"
"s_add_u32 s60, s70, 425984\n"
"buffer_load_dwordx4 v42, s[44:47], s60 offen lds\n"
"s_waitcnt vmcnt(36)\n"
"ds_read_b64_tr_b16 a[68:69], v40 offset:32768\n"
"ds_read_b64_tr_b16 a[70:71], v40 offset:33024\n"
"ds_read_b64_tr_b16 a[96:97], v41 offset:32768\n"
"ds_read_b64_tr_b16 a[98:99], v41 offset:33024\n"
"s_waitcnt lgkmcnt(4)\n"
"s_waitcnt vmcnt(34)\n"
"ds_read_b64_tr_b16 a[124:125], v40 offset:34816\n"
"ds_read_b64_tr_b16 a[126:127], v40 offset:35072\n"
"ds_read_b64_tr_b16 a[152:153], v41 offset:34816\n"
"ds_read_b64_tr_b16 a[154:155], v41 offset:35072\n"
"s_waitcnt lgkmcnt(4)\n"
"s_waitcnt vmcnt(32)\n"
"ds_read_b64_tr_b16 a[180:181], v40 offset:36864\n"
"ds_read_b64_tr_b16 a[182:183], v40 offset:37120\n"
"ds_read_b64_tr_b16 a[208:209], v41 offset:36864\n"
"ds_read_b64_tr_b16 a[210:211], v41 offset:37120\n"
"s_waitcnt lgkmcnt(4)\n"
"s_waitcnt vmcnt(30)\n"
"ds_read_b64_tr_b16 a[236:237], v40 offset:38912\n"
"ds_read_b64_tr_b16 a[238:239], v40 offset:39168\n"
"ds_read_b64_tr_b16 v[24:25], v41 offset:38912\n"
"ds_read_b64_tr_b16 v[26:27], v41 offset:39168\n"
"s_waitcnt lgkmcnt(4)\n"
"s_waitcnt vmcnt(28)\n"
"ds_read_b64_tr_b16 a[16:17], v40 offset:0\n"
"ds_read_b64_tr_b16 a[18:19], v40 offset:256\n"
"ds_read_b64_tr_b16 a[44:45], v41 offset:0\n"
"ds_read_b64_tr_b16 a[46:47], v41 offset:256\n"
"s_waitcnt lgkmcnt(4)\n"
"s_waitcnt vmcnt(26)\n"
"ds_read_b64_tr_b16 a[72:73], v40 offset:2048\n"
"ds_read_b64_tr_b16 a[74:75], v40 offset:2304\n"
"ds_read_b64_tr_b16 a[100:101], v41 offset:2048\n"
"ds_read_b64_tr_b16 a[102:103], v41 offset:2304\n"
"s_waitcnt lgkmcnt(4)\n"
"s_waitcnt vmcnt(24)\n"
"ds_read_b64_tr_b16 a[128:129], v40 offset:4096\n"
"ds_read_b64_tr_b16 a[130:131], v40 offset:4352\n"
"ds_read_b64_tr_b16 a[156:157], v41 offset:4096\n"
"ds_read_b64_tr_b16 a[158:159], v41 offset:4352\n"
"s_waitcnt lgkmcnt(4)\n"
"s_waitcnt vmcnt(22)\n"
"ds_read_b64_tr_b16 a[184:185], v40 offset:6144\n"
"ds_read_b64_tr_b16 a[186:187], v40 offset:6400\n"
"ds_read_b64_tr_b16 a[212:213], v41 offset:6144\n"
"ds_read_b64_tr_b16 a[214:215], v41 offset:6400\n"
"s_waitcnt lgkmcnt(4)\n"
"s_waitcnt vmcnt(20)\n"
"ds_read_b64_tr_b16 a[240:241], v40 offset:8192\n"
"ds_read_b64_tr_b16 a[242:243], v40 offset:8448\n"
"ds_read_b64_tr_b16 v[28:29], v41 offset:8192\n"
"ds_read_b64_tr_b16 v[30:31], v41 offset:8448\n"
"s_waitcnt lgkmcnt(4)\n"
"s_waitcnt vmcnt(18)\n"
"ds_read_b64_tr_b16 a[20:21], v40 offset:10240\n"
"ds_read_b64_tr_b16 a[22:23], v40 offset:10496\n"
"ds_read_b64_tr_b16 a[48:49], v41 offset:10240\n"
"ds_read_b64_tr_b16 a[50:51], v41 offset:10496\n"
"s_waitcnt lgkmcnt(4)\n"
"s_waitcnt vmcnt(16)\n"
"ds_read_b64_tr_b16 a[76:77], v40 offset:12288\n"
"ds_read_b64_tr_b16 a[78:79], v40 offset:12544\n"
"ds_read_b64_tr_b16 a[104:105], v41 offset:12288\n"
"ds_read_b64_tr_b16 a[106:107], v41 offset:12544\n"
"s_waitcnt lgkmcnt(4)\n"
"s_waitcnt vmcnt(14)\n"
"ds_read_b64_tr_b16 a[132:133], v40 offset:14336\n"
"ds_read_b64_tr_b16 a[134:135], v40 offset:14592\n"
"ds_read_b64_tr_b16 a[160:161], v41 offset:14336\n"
"ds_read_b64_tr_b16 a[162:163], v41 offset:14592\n"
"s_waitcnt lgkmcnt(4)\n"
"s_waitcnt vmcnt(12)\n"
"ds_read_b64_tr_b16 a[188:189], v40 offset:16384\n"
"ds_read_b64_tr_b16 a[190:191], v40 offset:16640\n"
"ds_read_b64_tr_b16 a[216:217], v41 offset:16384\n"
"ds_read_b64_tr_b16 a[218:219], v41 offset:16640\n"
"s_waitcnt lgkmcnt(4)\n"
"s_waitcnt vmcnt(10)\n"
"ds_read_b64_tr_b16 a[244:245], v40 offset:18432\n"
"ds_read_b64_tr_b16 a[246:247], v40 offset:18688\n"
"ds_read_b64_tr_b16 v[32:33], v41 offset:18432\n"
"ds_read_b64_tr_b16 v[34:35], v41 offset:18688\n"
"s_waitcnt lgkmcnt(4)\n"
"s_waitcnt vmcnt(8)\n"
"ds_read_b64_tr_b16 a[24:25], v40 offset:20480\n"
"ds_read_b64_tr_b16 a[26:27], v40 offset:20736\n"
"ds_read_b64_tr_b16 a[52:53], v41 offset:20480\n"
"ds_read_b64_tr_b16 a[54:55], v41 offset:20736\n"
"s_waitcnt lgkmcnt(4)\n"
"s_waitcnt vmcnt(6)\n"
"ds_read_b64_tr_b16 a[80:81], v40 offset:22528\n"
"ds_read_b64_tr_b16 a[82:83], v40 offset:22784\n"
"ds_read_b64_tr_b16 a[108:109], v41 offset:22528\n"
"ds_read_b64_tr_b16 a[110:111], v41 offset:22784\n"
"s_waitcnt lgkmcnt(4)\n"
"s_waitcnt vmcnt(4)\n"
"ds_read_b64_tr_b16 a[136:137], v40 offset:24576\n"
"ds_read_b64_tr_b16 a[138:139], v40 offset:24832\n"
"ds_read_b64_tr_b16 a[164:165], v41 offset:24576\n"
"ds_read_b64_tr_b16 a[166:167], v41 offset:24832\n"
"s_waitcnt lgkmcnt(4)\n"
"s_waitcnt vmcnt(2)\n"
"ds_read_b64_tr_b16 a[192:193], v40 offset:26624\n"
"ds_read_b64_tr_b16 a[194:195], v40 offset:26880\n"
"ds_read_b64_tr_b16 a[220:221], v41 offset:26624\n"
"ds_read_b64_tr_b16 a[222:223], v41 offset:26880\n"
"s_waitcnt lgkmcnt(4)\n"
"s_waitcnt vmcnt(0)\n"
"ds_read_b64_tr_b16 a[248:249], v40 offset:28672\n"
"ds_read_b64_tr_b16 a[250:251], v40 offset:28928\n"
"ds_read_b64_tr_b16 v[36:37], v41 offset:28672\n"
"ds_read_b64_tr_b16 v[38:39], v41 offset:28928\n"
"s_waitcnt lgkmcnt(4)\n"
"s_waitcnt vmcnt(0)\n"
"s_waitcnt lgkmcnt(0)\n"
"s_mov_b32 m0, s59\n"
"s_barrier\n"
"s_mov_b64 s[64:65], s[48:49]\n"
"v_mov_b32 v130, 0\n"
"v_mov_b32 v131, 0\n"
"v_mov_b32 v136, 0\n"
"v_mov_b32 v137, 0\n"
"v_mov_b32 v128, 0\n"
"v_mov_b32 v129, 0\n"
"v_mov_b32 v164, 0\n"
"v_mov_b32 v165, 0\n"
"s_mov_b32 s92, 0x80000000\n"
"s_mov_b32 s93, 0x80000000\n"
"s_mov_b32 s93, 0x80000000\n"
"s_mov_b32 s94, 0x80000000\n"
"s_mov_b32 s93, 0x80000000\n"
"s_mov_b32 s94, 0x80000000\n"
"s_mov_b32 s94, 0x80000000\n"
"s_mov_b32 s95, 0x80000000\n"
"s_mov_b32 s95, 0x80000000\n"
"v_mov_b32 v128, 0\n"
"v_mov_b32 v129, 0\n"
"s_cmp_eq_u32 s54, 0\n"
"s_cbranch_scc1 .LH_1s\n"
"s_cmp_eq_u32 s54, 1\n"
"s_cbranch_scc1 .LH_2s\n"
"s_cmp_eq_u32 s54, 2\n"
"s_cbranch_scc1 .LH_3s\n"
"s_cmp_eq_u32 s54, 3\n"
"s_cbranch_scc1 .LH_4s\n"
".p2align 6\n"
".LH_top:\n"
"v_mfma_f32_16x16x32_bf16 v[52:55], v[96:99], a[0:3], 0\n"
"v_mfma_f32_16x16x32_bf16 v[52:55], v[64:67], a[4:7], v[52:55]\n"
"global_load_dwordx4 v[40:43], v5, s[56:57] offset:2048 sc1\n"
"v_mfma_f32_16x16x32_bf16 v[52:55], v[68:71], a[8:11], v[52:55]\n"
"v_mfma_f32_16x16x32_bf16 v[52:55], v[72:75], a[12:15], v[52:55]\n"
"s_waitcnt vmcnt(6)\n"
"v_mfma_f32_16x16x32_bf16 v[52:55], v[76:79], a[16:19], v[52:55]\n"
"s_waitcnt lgkmcnt(3)\n"
"v_add_f32 v92, v92, v60\n"
"v_add_f32 v93, v93, v61\n"
"v_mfma_f32_16x16x32_bf16 v[52:55], v[80:83], a[20:23], v[52:55]\n"
"s_waitcnt vmcnt(5)\n"
"v_add_f32 v94, v94, v62\n"
"v_add_f32 v95, v95, v63\n"
"v_mfma_f32_16x16x32_bf16 v[52:55], v[84:87], a[24:27], v[52:55]\n"
"global_load_dwordx4 v[44:47], v5, s[56:57] offset:3072 sc1\n"
"v_add_f32 v92, v92, v88\n"
"v_mfma_f32_16x16x32_bf16 v[56:59], v[96:99], a[28:31], 0\n"
"buffer_load_dwordx2 v[128:129], v7, s[40:43], s50 offen sc1\n"
"v_mfma_f32_16x16x32_bf16 v[56:59], v[64:67], a[32:35], v[56:59]\n"
"ds_write_addtid_b32 v52 offset:0\n"
"v_add_f32 v93, v93, v89\n"
"v_mfma_f32_16x16x32_bf16 v[56:59], v[68:71], a[36:39], v[56:59]\n"
"ds_write_addtid_b32 v53 offset:4640\n"
"global_load_dwordx4 v[48:51], v6, s[56:57] offset:0 sc1\n"
"v_mfma_f32_16x16x32_bf16 v[56:59], v[72:75], a[40:43], v[56:59]\n"
"ds_write_addtid_b32 v54 offset:9344\n"
"v_add_f32 v94, v94, v90\n"
"v_mfma_f32_16x16x32_bf16 v[56:59], v[76:79], a[44:47], v[56:59]\n"
"ds_write_addtid_b32 v55 offset:13984\n"
"v_add_f32 v95, v95, v91\n"
"v_mfma_f32_16x16x32_bf16 v[56:59], v[80:83], a[48:51], v[56:59]\n"
"v_add_f32 v92, v92, v108\n"
"v_add_f32 v93, v93, v109\n"
"v_mfma_f32_16x16x32_bf16 v[56:59], v[84:87], a[52:55], v[56:59]\n"
"v_add_f32 v94, v94, v110\n"
"v_add_f32 v95, v95, v111\n"
"v_mfma_f32_16x16x32_bf16 v[60:63], v[96:99], a[56:59], 0\n"
"s_waitcnt vmcnt(7)\n"
"buffer_store_dwordx2 v[0:1], v9, s[40:43], s93 offen offset:0 sc1\n"
"v_mfma_f32_16x16x32_bf16 v[60:63], v[64:67], a[60:63], v[60:63]\n"
"s_add_u32 s94, s50, 0xffd00000\n"
"s_and_b32 s95, s94, 0xfbffffff\n"
"ds_write_addtid_b32 v56 offset:256\n"
"v_mfma_f32_16x16x32_bf16 v[60:63], v[68:71], a[64:67], v[60:63]\n"
"ds_write_addtid_b32 v57 offset:4896\n"
"global_load_dwordx4 v[52:55], v6, s[56:57] offset:1024 sc1\n"
"v_mfma_f32_16x16x32_bf16 v[60:63], v[72:75], a[68:71], v[60:63]\n"
"ds_write_addtid_b32 v58 offset:9600\n"
"v_cvt_pk_bf16_f32 v88, v92, v93\n"
"v_mfma_f32_16x16x32_bf16 v[60:63], v[76:79], a[72:75], v[60:63]\n"
"ds_write_addtid_b32 v59 offset:14240\n"
"v_cvt_pk_bf16_f32 v89, v94, v95\n"
"v_mfma_f32_16x16x32_bf16 v[60:63], v[80:83], a[76:79], v[60:63]\n"
"s_waitcnt lgkmcnt(8)\n"
"v_add_f32 v116, v116, v120\n"
"v_add_f32 v117, v117, v121\n"
"v_mfma_f32_16x16x32_bf16 v[60:63], v[84:87], a[80:83], v[60:63]\n"
"v_add_f32 v118, v118, v122\n"
"v_add_f32 v119, v119, v123\n"
"v_mfma_f32_16x16x32_bf16 v[92:95], v[96:99], a[84:87], 0\n"
"global_load_dwordx4 v[56:59], v6, s[56:57] offset:2048 sc1\n"
"v_add_f32 v116, v116, v124\n"
"v_mfma_f32_16x16x32_bf16 v[92:95], v[64:67], a[88:91], v[92:95]\n"
"ds_write_addtid_b32 v60 offset:512\n"
"v_add_f32 v117, v117, v125\n"
"v_mfma_f32_16x16x32_bf16 v[92:95], v[68:71], a[92:95], v[92:95]\n"
"ds_write_addtid_b32 v61 offset:5152\n"
"v_add_f32 v118, v118, v126\n"
"v_mfma_f32_16x16x32_bf16 v[92:95], v[72:75], a[96:99], v[92:95]\n"
"ds_write_addtid_b32 v62 offset:9856\n"
"v_add_f32 v119, v119, v127\n"
"v_mfma_f32_16x16x32_bf16 v[92:95], v[76:79], a[100:103], v[92:95]\n"
"ds_write_addtid_b32 v63 offset:14496\n"
"v_add_f32 v116, v116, v112\n"
"v_mfma_f32_16x16x32_bf16 v[92:95], v[80:83], a[104:107], v[92:95]\n"
"v_add_f32 v117, v117, v113\n"
"v_add_f32 v118, v118, v114\n"
"v_mfma_f32_16x16x32_bf16 v[92:95], v[84:87], a[108:111], v[92:95]\n"
"v_add_f32 v119, v119, v115\n"
"v_cvt_pk_bf16_f32 v90, v116, v117\n"
"v_mfma_f32_16x16x32_bf16 v[60:63], v[96:99], a[112:115], 0\n"
"v_cvt_pk_bf16_f32 v91, v118, v119\n"
"v_mfma_f32_16x16x32_bf16 v[60:63], v[64:67], a[116:119], v[60:63]\n"
"ds_write_addtid_b32 v92 offset:768\n"
"v_mfma_f32_16x16x32_bf16 v[60:63], v[68:71], a[120:123], v[60:63]\n"
"ds_write_addtid_b32 v93 offset:5408\n"
"v_mfma_f32_16x16x32_bf16 v[60:63], v[72:75], a[124:127], v[60:63]\n"
"ds_write_addtid_b32 v94 offset:10112\n"
"v_mfma_f32_16x16x32_bf16 v[60:63], v[76:79], a[128:131], v[60:63]\n"
"ds_write_addtid_b32 v95 offset:14752\n"
"v_mfma_f32_16x16x32_bf16 v[60:63], v[80:83], a[132:135], v[60:63]\n"
"v_mfma_f32_16x16x32_bf16 v[60:63], v[84:87], a[136:139], v[60:63]\n"
"v_mfma_f32_16x16x32_bf16 v[108:111], v[96:99], a[140:143], 0\n"
"v_mfma_f32_16x16x32_bf16 v[108:111], v[64:67], a[144:147], v[108:111]\n"
"ds_write_addtid_b32 v60 offset:1024\n"
"v_mfma_f32_16x16x32_bf16 v[108:111], v[68:71], a[148:151], v[108:111]\n"
"ds_write_addtid_b32 v61 offset:5664\n"
"v_mfma_f32_16x16x32_bf16 v[108:111], v[72:75], a[152:155], v[108:111]\n"
"ds_write_addtid_b32 v62 offset:10368\n"
"v_mfma_f32_16x16x32_bf16 v[108:111], v[76:79], a[156:159], v[108:111]\n"
"ds_write_addtid_b32 v63 offset:15008\n"
"v_mfma_f32_16x16x32_bf16 v[108:111], v[80:83], a[160:163], v[108:111]\n"
"buffer_store_dwordx4 v[88:91], v8, s[40:43], s95 offen sc1\n"
"v_mfma_f32_16x16x32_bf16 v[108:111], v[84:87], a[164:167], v[108:111]\n"
"v_mfma_f32_16x16x32_bf16 v[92:95], a[168:171], v[96:99], 0\n"
"v_mfma_f32_16x16x32_bf16 v[92:95], a[172:175], v[64:67], v[92:95]\n"
"ds_write_addtid_b32 v108 offset:1280\n"
"v_mfma_f32_16x16x32_bf16 v[92:95], a[176:179], v[68:71], v[92:95]\n"
"ds_write_addtid_b32 v109 offset:5920\n"
"v_mfma_f32_16x16x32_bf16 v[92:95], a[180:183], v[72:75], v[92:95]\n"
"ds_write_addtid_b32 v110 offset:10624\n"
"v_mfma_f32_16x16x32_bf16 v[92:95], a[184:187], v[76:79], v[92:95]\n"
"ds_write_addtid_b32 v111 offset:15264\n"
"v_mfma_f32_16x16x32_bf16 v[92:95], a[188:191], v[80:83], v[92:95]\n"
"v_mfma_f32_16x16x32_bf16 v[92:95], a[192:195], v[84:87], v[92:95]\n"
"v_mfma_f32_16x16x32_bf16 v[88:91], a[196:199], v[96:99], 0\n"
"v_mfma_f32_16x16x32_bf16 v[88:91], a[200:203], v[64:67], v[88:91]\n"
"v_mfma_f32_16x16x32_bf16 v[88:91], a[204:207], v[68:71], v[88:91]\n"
"v_mfma_f32_16x16x32_bf16 v[88:91], a[208:211], v[72:75], v[88:91]\n"
"s_add_u32 s58, s50, 0x180000\n"
"v_mfma_f32_16x16x32_bf16 v[88:91], a[212:215], v[76:79], v[88:91]\n"
"s_and_b32 s58, s58, 0x3ffffff\n"
"v_mfma_f32_16x16x32_bf16 v[88:91], a[216:219], v[80:83], v[88:91]\n"
"s_add_u32 s56, s40, s58\n"
"v_mfma_f32_16x16x32_bf16 v[88:91], a[220:223], v[84:87], v[88:91]\n"
"s_addc_u32 s57, s41, 0\n"
"v_mfma_f32_16x16x32_bf16 v[108:111], a[224:227], v[96:99], 0\n"
"s_waitcnt vmcnt(5)\n"
"v_cmp_ne_u64 vcc, s[48:49], v[128:129]\n"
"v_mfma_f32_16x16x32_bf16 v[108:111], a[228:231], v[64:67], v[108:111]\n"
"v_mfma_f32_16x16x32_bf16 v[108:111], a[232:235], v[68:71], v[108:111]\n"
"s_cbranch_vccnz .LH_3s\n"
".LH_3r:\n"
"v_mfma_f32_16x16x32_bf16 v[108:111], a[236:239], v[72:75], v[108:111]\n"
"s_add_u32 s50, s50, 0x180000\n"
"global_load_dwordx4 v[60:63], v5, s[56:57] offset:0 sc1\n"
"v_mfma_f32_16x16x32_bf16 v[108:111], a[240:243], v[76:79], v[108:111]\n"
"v_mfma_f32_16x16x32_bf16 v[108:111], a[244:247], v[80:83], v[108:111]\n"
"v_mfma_f32_16x16x32_bf16 v[108:111], a[248:251], v[84:87], v[108:111]\n"
"v_mfma_f32_16x16x32_bf16 v[112:115], a[252:255], v[96:99], 0\n"
"v_mfma_f32_16x16x32_bf16 v[112:115], v[16:19], v[64:67], v[112:115]\n"
"s_waitcnt lgkmcnt(0)\n"
"v_mfma_f32_16x16x32_bf16 v[112:115], v[20:23], v[68:71], v[112:115]\n"
"s_barrier\n"
"v_mfma_f32_16x16x32_bf16 v[112:115], v[24:27], v[72:75], v[112:115]\n"
"ds_read_b128 v[96:99], v2 offset:2048\n"
"ds_read_b128 v[116:119], v3 offset:512\n"
"v_mfma_f32_16x16x32_bf16 v[112:115], v[28:31], v[76:79], v[112:115]\n"
"global_load_dwordx4 v[64:67], v5, s[56:57] offset:1024 sc1\n"
"ds_read_b128 v[120:123], v4 offset:3072\n"
"v_mfma_f32_16x16x32_bf16 v[112:115], v[32:35], v[80:83], v[112:115]\n"
"ds_read_b128 v[124:127], v2 offset:2304\n"
"ds_read_b128 v[128:131], v3 offset:768\n"
"v_mfma_f32_16x16x32_bf16 v[112:115], v[36:39], v[84:87], v[112:115]\n"
"global_load_dwordx4 v[68:71], v5, s[56:57] offset:2048 sc1\n"
"ds_read_b128 v[132:135], v4 offset:3328\n"
"v_mfma_f32_16x16x32_bf16 v[80:83], v[100:103], a[0:3], 0\n"
"ds_read_b128 v[136:139], v2 offset:3584\n"
"ds_read_b128 v[140:143], v3 offset:1536\n"
"v_mfma_f32_16x16x32_bf16 v[80:83], v[104:107], a[4:7], v[80:83]\n"
"ds_read_b128 v[144:147], v4 offset:3584\n"
"ds_read_b128 v[148:151], v2 offset:3840\n"
"v_mfma_f32_16x16x32_bf16 v[80:83], v[40:43], a[8:11], v[80:83]\n"
"ds_read_b128 v[152:155], v3 offset:1792\n"
"ds_read_b128 v[156:159], v4 offset:3840\n"
"v_mfma_f32_16x16x32_bf16 v[80:83], v[44:47], a[12:15], v[80:83]\n"
"s_waitcnt vmcnt(5)\n"
"v_mfma_f32_16x16x32_bf16 v[80:83], v[48:51], a[16:19], v[80:83]\n"
"s_waitcnt lgkmcnt(9)\n"
"global_load_dwordx4 v[72:75], v5, s[56:57] offset:3072 sc1\n"
"v_add_f32 v96, v96, v116\n"
"v_mfma_f32_16x16x32_bf16 v[80:83], v[52:55], a[20:23], v[80:83]\n"
"s_waitcnt vmcnt(5)\n"
"v_add_f32 v97, v97, v117\n"
"v_add_f32 v98, v98, v118\n"
"v_mfma_f32_16x16x32_bf16 v[80:83], v[56:59], a[24:27], v[80:83]\n"
"s_add_u32 s95, s50, 0xff000000\n"
"v_add_f32 v99, v99, v119\n"
"v_add_f32 v96, v96, v120\n"
"v_mfma_f32_16x16x32_bf16 v[160:163], v[100:103], a[28:31], 0\n"
"buffer_load_dwordx2 v[164:165], v7, s[40:43], s50 offen sc1\n"
"v_mfma_f32_16x16x32_bf16 v[160:163], v[104:107], a[32:35], v[160:163]\n"
"ds_write_addtid_b32 v80 offset:2048\n"
"v_add_f32 v97, v97, v121\n"
"v_mfma_f32_16x16x32_bf16 v[160:163], v[40:43], a[36:39], v[160:163]\n"
"ds_write_addtid_b32 v81 offset:6688\n"
"v_add_f32 v98, v98, v122\n"
"v_mfma_f32_16x16x32_bf16 v[160:163], v[44:47], a[40:43], v[160:163]\n"
"ds_write_addtid_b32 v82 offset:11392\n"
"v_add_f32 v99, v99, v123\n"
"v_mfma_f32_16x16x32_bf16 v[160:163], v[48:51], a[44:47], v[160:163]\n"
"ds_write_addtid_b32 v83 offset:16032\n"
"v_add_f32 v96, v96, v92\n"
"v_mfma_f32_16x16x32_bf16 v[160:163], v[52:55], a[48:51], v[160:163]\n"
"global_load_dwordx4 v[76:79], v6, s[56:57] offset:0 sc1\n"
"v_add_f32 v97, v97, v93\n"
"v_mfma_f32_16x16x32_bf16 v[160:163], v[56:59], a[52:55], v[160:163]\n"
"v_add_f32 v98, v98, v94\n"
"v_add_f32 v99, v99, v95\n"
"v_mfma_f32_16x16x32_bf16 v[116:119], v[100:103], a[56:59], 0\n"
"s_add_u32 s92, s50, 0xffd00000\n"
"s_and_b32 s93, s92, 0xfbffffff\n"
"v_cvt_pk_bf16_f32 v120, v96, v97\n"
"v_mfma_f32_16x16x32_bf16 v[116:119], v[104:107], a[60:63], v[116:119]\n"
"ds_write_addtid_b32 v160 offset:2304\n"
"v_cvt_pk_bf16_f32 v121, v98, v99\n"
"v_mfma_f32_16x16x32_bf16 v[116:119], v[40:43], a[64:67], v[116:119]\n"
"s_waitcnt lgkmcnt(11)\n"
"ds_write_addtid_b32 v161 offset:6944\n"
"v_add_f32 v124, v124, v128\n"
"v_mfma_f32_16x16x32_bf16 v[116:119], v[44:47], a[68:71], v[116:119]\n"
"ds_write_addtid_b32 v162 offset:11648\n"
"v_add_f32 v125, v125, v129\n"
"v_mfma_f32_16x16x32_bf16 v[116:119], v[48:51], a[72:75], v[116:119]\n"
"s_waitcnt vmcnt(6)\n"
"ds_write_addtid_b32 v163 offset:16288\n"
"global_load_dwordx4 v[80:83], v6, s[56:57] offset:1024 sc1\n"
"v_mfma_f32_16x16x32_bf16 v[116:119], v[52:55], a[76:79], v[116:119]\n"
"global_load_dwordx4 v[84:87], v6, s[56:57] offset:2048 sc1\n"
"v_add_f32 v126, v126, v130\n"
"v_mfma_f32_16x16x32_bf16 v[116:119], v[56:59], a[80:83], v[116:119]\n"
"v_add_f32 v127, v127, v131\n"
"v_add_f32 v124, v124, v132\n"
"v_mfma_f32_16x16x32_bf16 v[92:95], v[100:103], a[84:87], 0\n"
"v_add_f32 v125, v125, v133\n"
"v_add_f32 v126, v126, v134\n"
"v_mfma_f32_16x16x32_bf16 v[92:95], v[104:107], a[88:91], v[92:95]\n"
"ds_write_addtid_b32 v116 offset:2560\n"
"v_add_f32 v127, v127, v135\n"
"v_mfma_f32_16x16x32_bf16 v[92:95], v[40:43], a[92:95], v[92:95]\n"
"ds_write_addtid_b32 v117 offset:7200\n"
"v_add_f32 v124, v124, v88\n"
"v_mfma_f32_16x16x32_bf16 v[92:95], v[44:47], a[96:99], v[92:95]\n"
"ds_write_addtid_b32 v118 offset:11904\n"
"v_add_f32 v125, v125, v89\n"
"v_mfma_f32_16x16x32_bf16 v[92:95], v[48:51], a[100:103], v[92:95]\n"
"ds_write_addtid_b32 v119 offset:16544\n"
"v_add_f32 v126, v126, v90\n"
"v_mfma_f32_16x16x32_bf16 v[92:95], v[52:55], a[104:107], v[92:95]\n"
"v_add_f32 v127, v127, v91\n"
"v_cvt_pk_bf16_f32 v122, v124, v125\n"
"v_mfma_f32_16x16x32_bf16 v[92:95], v[56:59], a[108:111], v[92:95]\n"
"buffer_store_dwordx2 v[0:1], v9, s[40:43], s94 offen offset:0 sc1\n"
"v_cvt_pk_bf16_f32 v123, v126, v127\n"
"v_mfma_f32_16x16x32_bf16 v[88:91], v[100:103], a[112:115], 0\n"
"buffer_store_dwordx4 v[120:123], v8, s[40:43], s93 offen sc1\n"
"v_mfma_f32_16x16x32_bf16 v[88:91], v[104:107], a[116:119], v[88:91]\n"
"s_waitcnt lgkmcnt(15)\n"
"ds_write_addtid_b32 v92 offset:2816\n"
"v_add_f32 v136, v136, v140\n"
"v_mfma_f32_16x16x32_bf16 v[88:91], v[40:43], a[120:123], v[88:91]\n"
"ds_write_addtid_b32 v93 offset:7456\n"
"v_add_f32 v137, v137, v141\n"
"v_mfma_f32_16x16x32_bf16 v[88:91], v[44:47], a[124:127], v[88:91]\n"
"ds_write_addtid_b32 v94 offset:12160\n"
"v_add_f32 v138, v138, v142\n"
"v_mfma_f32_16x16x32_bf16 v[88:91], v[48:51], a[128:131], v[88:91]\n"
"ds_write_addtid_b32 v95 offset:16800\n"
"v_add_f32 v139, v139, v143\n"
"v_mfma_f32_16x16x32_bf16 v[88:91], v[52:55], a[132:135], v[88:91]\n"
"v_add_f32 v136, v136, v144\n"
"v_add_f32 v137, v137, v145\n"
"v_mfma_f32_16x16x32_bf16 v[88:91], v[56:59], a[136:139], v[88:91]\n"
"v_add_f32 v138, v138, v146\n"
"v_add_f32 v139, v139, v147\n"
"v_mfma_f32_16x16x32_bf16 v[92:95], v[100:103], a[140:143], 0\n"
"v_add_f32 v136, v136, v108\n"
"v_add_f32 v137, v137, v109\n"
"v_mfma_f32_16x16x32_bf16 v[92:95], v[104:107], a[144:147], v[92:95]\n"
"ds_write_addtid_b32 v88 offset:3072\n"
"v_add_f32 v138, v138, v110\n"
"v_mfma_f32_16x16x32_bf16 v[92:95], v[40:43], a[148:151], v[92:95]\n"
"ds_write_addtid_b32 v89 offset:7712\n"
"v_add_f32 v139, v139, v111\n"
"v_mfma_f32_16x16x32_bf16 v[92:95], v[44:47], a[152:155], v[92:95]\n"
"ds_write_addtid_b32 v90 offset:12416\n"
"v_cvt_pk_bf16_f32 v96, v136, v137\n"
"v_mfma_f32_16x16x32_bf16 v[92:95], v[48:51], a[156:159], v[92:95]\n"
"ds_write_addtid_b32 v91 offset:17056\n"
"v_cvt_pk_bf16_f32 v97, v138, v139\n"
"v_mfma_f32_16x16x32_bf16 v[92:95], v[52:55], a[160:163], v[92:95]\n"
"s_waitcnt lgkmcnt(15)\n"
"v_add_f32 v148, v148, v152\n"
"v_add_f32 v149, v149, v153\n"
"v_mfma_f32_16x16x32_bf16 v[92:95], v[56:59], a[164:167], v[92:95]\n"
"v_add_f32 v150, v150, v154\n"
"v_add_f32 v151, v151, v155\n"
"v_mfma_f32_16x16x32_bf16 v[88:91], v[100:103], a[224:227], 0\n"
"v_add_f32 v148, v148, v156\n"
"v_add_f32 v149, v149, v157\n"
"v_mfma_f32_16x16x32_bf16 v[88:91], v[104:107], a[228:231], v[88:91]\n"
"ds_write_addtid_b32 v92 offset:3328\n"
"v_add_f32 v150, v150, v158\n"
"v_mfma_f32_16x16x32_bf16 v[88:91], v[40:43], a[232:235], v[88:91]\n"
"ds_write_addtid_b32 v93 offset:7968\n"
"v_add_f32 v151, v151, v159\n"
"v_mfma_f32_16x16x32_bf16 v[88:91], v[44:47], a[236:239], v[88:91]\n"
"ds_write_addtid_b32 v94 offset:12672\n"
"v_add_f32 v148, v148, v112\n"
"v_mfma_f32_16x16x32_bf16 v[88:91], v[48:51], a[240:243], v[88:91]\n"
"ds_write_addtid_b32 v95 offset:17312\n"
"v_add_f32 v149, v149, v113\n"
"v_mfma_f32_16x16x32_bf16 v[88:91], v[52:55], a[244:247], v[88:91]\n"
"v_add_f32 v150, v150, v114\n"
"v_add_f32 v151, v151, v115\n"
"v_mfma_f32_16x16x32_bf16 v[88:91], v[56:59], a[248:251], v[88:91]\n"
"v_cvt_pk_bf16_f32 v98, v148, v149\n"
"v_cvt_pk_bf16_f32 v99, v150, v151\n"
"v_mfma_f32_16x16x32_bf16 v[92:95], v[100:103], a[252:255], 0\n"
"buffer_store_dwordx4 v[96:99], v10, s[40:43], s93 offen sc1\n"
"v_mfma_f32_16x16x32_bf16 v[92:95], v[104:107], v[16:19], v[92:95]\n"
"s_cmp_lt_u32 s95, 0x8000000\n"
"s_cselect_b32 s95, 0x80000000, s95\n"
"ds_write_addtid_b32 v88 offset:3584\n"
"v_mfma_f32_16x16x32_bf16 v[92:95], v[40:43], v[20:23], v[92:95]\n"
"ds_write_addtid_b32 v89 offset:8224\n"
"v_mfma_f32_16x16x32_bf16 v[92:95], v[44:47], v[24:27], v[92:95]\n"
"ds_write_addtid_b32 v90 offset:12928\n"
"v_mfma_f32_16x16x32_bf16 v[92:95], v[48:51], v[28:31], v[92:95]\n"
"ds_write_addtid_b32 v91 offset:17568\n"
"v_mfma_f32_16x16x32_bf16 v[92:95], v[52:55], v[32:35], v[92:95]\n"
"buffer_load_dwordx2 v[128:129], v12, s[40:43], s95 offen offset:0 sc1\n"
"v_mfma_f32_16x16x32_bf16 v[92:95], v[56:59], v[36:39], v[92:95]\n"
"s_add_u32 s58, s50, 0x180000\n"
"v_mfma_f32_16x16x32_bf16 v[96:99], a[168:171], v[100:103], 0\n"
"s_and_b32 s58, s58, 0x3ffffff\n"
"v_mfma_f32_16x16x32_bf16 v[96:99], a[172:175], v[104:107], v[96:99]\n"
"s_add_u32 s56, s40, s58\n"
"ds_write_addtid_b32 v92 offset:3840\n"
"v_mfma_f32_16x16x32_bf16 v[96:99], a[176:179], v[40:43], v[96:99]\n"
"s_addc_u32 s57, s41, 0\n"
"ds_write_addtid_b32 v93 offset:8480\n"
"v_mfma_f32_16x16x32_bf16 v[96:99], a[180:183], v[44:47], v[96:99]\n"
"s_waitcnt vmcnt(7)\n"
"ds_write_addtid_b32 v94 offset:13184\n"
"v_cmp_ne_u64 vcc, s[48:49], v[164:165]\n"
"v_mfma_f32_16x16x32_bf16 v[96:99], a[184:187], v[48:51], v[96:99]\n"
"ds_write_addtid_b32 v95 offset:17824\n"
"v_mfma_f32_16x16x32_bf16 v[96:99], a[188:191], v[52:55], v[96:99]\n"
"s_cbranch_vccnz .LH_4s\n"
".LH_4r:\n"
"v_mfma_f32_16x16x32_bf16 v[96:99], a[192:195], v[56:59], v[96:99]\n"
"s_add_u32 s50, s50, 0x180000\n"
"global_load_dwordx4 v[88:91], v5, s[56:57] offset:0 sc1\n"
"v_mfma_f32_16x16x32_bf16 v[108:111], a[196:199], v[100:103], 0\n"
"global_load_dwordx4 v[92:95], v5, s[56:57] offset:1024 sc1\n"
"v_mfma_f32_16x16x32_bf16 v[108:111], a[200:203], v[104:107], v[108:111]\n"
"s_waitcnt lgkmcnt(0)\n"
"v_mfma_f32_16x16x32_bf16 v[108:111], a[204:207], v[40:43], v[108:111]\n"
"s_barrier\n"
"v_mfma_f32_16x16x32_bf16 v[108:111], a[208:211], v[44:47], v[108:111]\n"
"ds_read_b128 v[100:103], v2 offset:0\n"
"ds_read_b128 v[104:107], v3 offset:2560\n"
"v_mfma_f32_16x16x32_bf16 v[108:111], a[212:215], v[48:51], v[108:111]\n"
"ds_read_b128 v[112:115], v4 offset:1024\n"
"ds_read_b128 v[116:119], v2 offset:256\n"
"v_mfma_f32_16x16x32_bf16 v[108:111], a[216:219], v[52:55], v[108:111]\n"
"ds_read_b128 v[120:123], v3 offset:2816\n"
"ds_read_b128 v[124:127], v4 offset:1280\n"
"v_mfma_f32_16x16x32_bf16 v[108:111], a[220:223], v[56:59], v[108:111]\n"
"v_mfma_f32_16x16x32_bf16 v[48:51], v[60:63], a[0:3], 0\n"
"v_mfma_f32_16x16x32_bf16 v[48:51], v[64:67], a[4:7], v[48:51]\n"
"v_mfma_f32_16x16x32_bf16 v[48:51], v[68:71], a[8:11], v[48:51]\n"
"global_load_dwordx4 v[40:43], v5, s[56:57] offset:2048 sc1\n"
"v_mfma_f32_16x16x32_bf16 v[48:51], v[72:75], a[12:15], v[48:51]\n"
"s_waitcnt vmcnt(8)\n"
"buffer_load_dwordx2 v[130:131], v7, s[40:43], s50 offen sc1\n"
"v_mfma_f32_16x16x32_bf16 v[48:51], v[76:79], a[16:19], v[48:51]\n"
"s_waitcnt lgkmcnt(3)\n"
"v_add_f32 v100, v100, v104\n"
"v_add_f32 v101, v101, v105\n"
"v_mfma_f32_16x16x32_bf16 v[48:51], v[80:83], a[20:23], v[48:51]\n"
"s_waitcnt vmcnt(8)\n"
"v_add_f32 v102, v102, v106\n"
"v_add_f32 v103, v103, v107\n"
"v_mfma_f32_16x16x32_bf16 v[48:51], v[84:87], a[24:27], v[48:51]\n"
"v_add_f32 v100, v100, v112\n"
"v_add_f32 v101, v101, v113\n"
"v_mfma_f32_16x16x32_bf16 v[56:59], v[60:63], a[28:31], 0\n"
"global_load_dwordx4 v[44:47], v5, s[56:57] offset:3072 sc1\n"
"v_add_f32 v102, v102, v114\n"
"v_mfma_f32_16x16x32_bf16 v[56:59], v[64:67], a[32:35], v[56:59]\n"
"ds_write_addtid_b32 v48 offset:0\n"
"v_add_f32 v103, v103, v115\n"
"v_mfma_f32_16x16x32_bf16 v[56:59], v[68:71], a[36:39], v[56:59]\n"
"ds_write_addtid_b32 v49 offset:4640\n"
"v_add_f32 v100, v100, v96\n"
"v_mfma_f32_16x16x32_bf16 v[56:59], v[72:75], a[40:43], v[56:59]\n"
"ds_write_addtid_b32 v50 offset:9344\n"
"v_add_f32 v101, v101, v97\n"
"v_mfma_f32_16x16x32_bf16 v[56:59], v[76:79], a[44:47], v[56:59]\n"
"ds_write_addtid_b32 v51 offset:13984\n"
"v_add_f32 v102, v102, v98\n"
"v_mfma_f32_16x16x32_bf16 v[56:59], v[80:83], a[48:51], v[56:59]\n"
"v_add_f32 v103, v103, v99\n"
"v_mfma_f32_16x16x32_bf16 v[56:59], v[84:87], a[52:55], v[56:59]\n"
"s_add_u32 s93, s50, 0xffd00000\n"
"s_and_b32 s94, s93, 0xfbffffff\n"
"global_load_dwordx4 v[48:51], v6, s[56:57] offset:0 sc1\n"
"v_mfma_f32_16x16x32_bf16 v[96:99], v[60:63], a[56:59], 0\n"
"v_cvt_pk_bf16_f32 v104, v100, v101\n"
"v_cvt_pk_bf16_f32 v105, v102, v103\n"
"v_mfma_f32_16x16x32_bf16 v[96:99], v[64:67], a[60:63], v[96:99]\n"
"s_waitcnt lgkmcnt(4)\n"
"ds_write_addtid_b32 v56 offset:256\n"
"v_add_f32 v116, v116, v120\n"
"v_mfma_f32_16x16x32_bf16 v[96:99], v[68:71], a[64:67], v[96:99]\n"
"ds_write_addtid_b32 v57 offset:4896\n"
"v_add_f32 v117, v117, v121\n"
"v_mfma_f32_16x16x32_bf16 v[96:99], v[72:75], a[68:71], v[96:99]\n"
"ds_write_addtid_b32 v58 offset:9600\n"
"global_load_dwordx4 v[52:55], v6, s[56:57] offset:1024 sc1\n"
"v_mfma_f32_16x16x32_bf16 v[96:99], v[76:79], a[72:75], v[96:99]\n"
"ds_write_addtid_b32 v59 offset:14240\n"
"v_add_f32 v118, v118, v122\n"
"v_mfma_f32_16x16x32_bf16 v[96:99], v[80:83], a[76:79], v[96:99]\n"
"v_add_f32 v119, v119, v123\n"
"v_add_f32 v116, v116, v124\n"
"v_mfma_f32_16x16x32_bf16 v[96:99], v[84:87], a[80:83], v[96:99]\n"
"v_add_f32 v117, v117, v125\n"
"v_add_f32 v118, v118, v126\n"
"v_mfma_f32_16x16x32_bf16 v[100:103], v[60:63], a[84:87], 0\n"
"v_add_f32 v119, v119, v127\n"
"v_add_f32 v116, v116, v108\n"
"v_mfma_f32_16x16x32_bf16 v[100:103], v[64:67], a[88:91], v[100:103]\n"
"ds_write_addtid_b32 v96 offset:512\n"
"v_add_f32 v117, v117, v109\n"
"v_mfma_f32_16x16x32_bf16 v[100:103], v[68:71], a[92:95], v[100:103]\n"
"ds_write_addtid_b32 v97 offset:5152\n"
"global_load_dwordx4 v[56:59], v6, s[56:57] offset:2048 sc1\n"
"v_mfma_f32_16x16x32_bf16 v[100:103], v[72:75], a[96:99], v[100:103]\n"
"ds_write_addtid_b32 v98 offset:9856\n"
"v_add_f32 v118, v118, v110\n"
"v_mfma_f32_16x16x32_bf16 v[100:103], v[76:79], a[100:103], v[100:103]\n"
"ds_write_addtid_b32 v99 offset:14496\n"
"v_add_f32 v119, v119, v111\n"
"v_mfma_f32_16x16x32_bf16 v[100:103], v[80:83], a[104:107], v[100:103]\n"
"v_cvt_pk_bf16_f32 v106, v116, v117\n"
"v_cvt_pk_bf16_f32 v107, v118, v119\n"
"v_mfma_f32_16x16x32_bf16 v[100:103], v[84:87], a[108:111], v[100:103]\n"
"s_waitcnt vmcnt(9)\n"
"buffer_store_dwordx2 v[0:1], v11, s[40:43], s92 offen offset:0 sc1\n"
"v_mfma_f32_16x16x32_bf16 v[96:99], v[60:63], a[112:115], 0\n"
"v_mfma_f32_16x16x32_bf16 v[96:99], v[64:67], a[116:119], v[96:99]\n"
"ds_write_addtid_b32 v100 offset:768\n"
"v_mfma_f32_16x16x32_bf16 v[96:99], v[68:71], a[120:123], v[96:99]\n"
"buffer_store_dwordx4 v[104:107], v8, s[40:43], s94 offen sc1\n"
"ds_write_addtid_b32 v101 offset:5408\n"
"v_mfma_f32_16x16x32_bf16 v[96:99], v[72:75], a[124:127], v[96:99]\n"
"ds_write_addtid_b32 v102 offset:10112\n"
"v_mfma_f32_16x16x32_bf16 v[96:99], v[76:79], a[128:131], v[96:99]\n"
"s_waitcnt vmcnt(10)\n"
"ds_write_addtid_b32 v103 offset:14752\n"
"v_cmp_ne_u64 vcc, s[64:65], v[128:129]\n"
"v_mfma_f32_16x16x32_bf16 v[96:99], v[80:83], a[132:135], v[96:99]\n"
"v_mfma_f32_16x16x32_bf16 v[96:99], v[84:87], a[136:139], v[96:99]\n"
"s_cbranch_vccnz .LH_5s\n"
".LH_5r:\n"
"v_mfma_f32_16x16x32_bf16 v[100:103], v[60:63], a[140:143], 0\n"
"buffer_load_dwordx4 v[104:107], v13, s[40:43], s95 offen offset:0 sc1\n"
"v_mfma_f32_16x16x32_bf16 v[100:103], v[64:67], a[144:147], v[100:103]\n"
"ds_write_addtid_b32 v96 offset:1024\n"
"v_mfma_f32_16x16x32_bf16 v[100:103], v[68:71], a[148:151], v[100:103]\n"
"ds_write_addtid_b32 v97 offset:5664\n"
"v_mfma_f32_16x16x32_bf16 v[100:103], v[72:75], a[152:155], v[100:103]\n"
"ds_write_addtid_b32 v98 offset:10368\n"
"v_mfma_f32_16x16x32_bf16 v[100:103], v[76:79], a[156:159], v[100:103]\n"
"ds_write_addtid_b32 v99 offset:15008\n"
"v_mfma_f32_16x16x32_bf16 v[100:103], v[80:83], a[160:163], v[100:103]\n"
"buffer_load_dwordx4 v[108:111], v13, s[40:43], s95 offen offset:1024 sc1\n"
"v_mfma_f32_16x16x32_bf16 v[100:103], v[84:87], a[164:167], v[100:103]\n"
"v_mfma_f32_16x16x32_bf16 v[96:99], v[60:63], a[224:227], 0\n"
"v_mfma_f32_16x16x32_bf16 v[96:99], v[64:67], a[228:231], v[96:99]\n"
"ds_write_addtid_b32 v100 offset:1280\n"
"v_mfma_f32_16x16x32_bf16 v[96:99], v[68:71], a[232:235], v[96:99]\n"
"ds_write_addtid_b32 v101 offset:5920\n"
"v_mfma_f32_16x16x32_bf16 v[96:99], v[72:75], a[236:239], v[96:99]\n"
"ds_write_addtid_b32 v102 offset:10624\n"
"v_mfma_f32_16x16x32_bf16 v[96:99], v[76:79], a[240:243], v[96:99]\n"
"ds_write_addtid_b32 v103 offset:15264\n"
"v_mfma_f32_16x16x32_bf16 v[96:99], v[80:83], a[244:247], v[96:99]\n"
"v_mfma_f32_16x16x32_bf16 v[96:99], v[84:87], a[248:251], v[96:99]\n"
"v_mfma_f32_16x16x32_bf16 v[112:115], v[60:63], a[252:255], 0\n"
"v_mfma_f32_16x16x32_bf16 v[112:115], v[64:67], v[16:19], v[112:115]\n"
"s_add_u32 s58, s50, 0x180000\n"
"ds_write_addtid_b32 v96 offset:1536\n"
"v_mfma_f32_16x16x32_bf16 v[112:115], v[68:71], v[20:23], v[112:115]\n"
"s_and_b32 s58, s58, 0x3ffffff\n"
"ds_write_addtid_b32 v97 offset:6176\n"
"v_mfma_f32_16x16x32_bf16 v[112:115], v[72:75], v[24:27], v[112:115]\n"
"s_add_u32 s56, s40, s58\n"
"ds_write_addtid_b32 v98 offset:10880\n"
"v_mfma_f32_16x16x32_bf16 v[112:115], v[76:79], v[28:31], v[112:115]\n"
"s_addc_u32 s57, s41, 0\n"
"ds_write_addtid_b32 v99 offset:15520\n"
"v_mfma_f32_16x16x32_bf16 v[112:115], v[80:83], v[32:35], v[112:115]\n"
"s_waitcnt vmcnt(8)\n"
"v_cmp_ne_u64 vcc, s[48:49], v[130:131]\n"
"v_mfma_f32_16x16x32_bf16 v[112:115], v[84:87], v[36:39], v[112:115]\n"
"v_mfma_f32_16x16x32_bf16 v[100:103], a[168:171], v[60:63], 0\n"
"s_cbranch_vccnz .LH_1s\n"
".LH_1r:\n"
"v_mfma_f32_16x16x32_bf16 v[100:103], a[172:175], v[64:67], v[100:103]\n"
"s_add_u32 s50, s50, 0x180000\n"
"ds_write_addtid_b32 v112 offset:1792\n"
"v_mfma_f32_16x16x32_bf16 v[100:103], a[176:179], v[68:71], v[100:103]\n"
"ds_write_addtid_b32 v113 offset:6432\n"
"v_mfma_f32_16x16x32_bf16 v[100:103], a[180:183], v[72:75], v[100:103]\n"
"ds_write_addtid_b32 v114 offset:11136\n"
"v_mfma_f32_16x16x32_bf16 v[100:103], a[184:187], v[76:79], v[100:103]\n"
"ds_write_addtid_b32 v115 offset:15776\n"
"global_load_dwordx4 v[96:99], v5, s[56:57] offset:0 sc1\n"
"v_mfma_f32_16x16x32_bf16 v[100:103], a[188:191], v[80:83], v[100:103]\n"
"v_mfma_f32_16x16x32_bf16 v[100:103], a[192:195], v[84:87], v[100:103]\n"
"v_mfma_f32_16x16x32_bf16 v[112:115], a[196:199], v[60:63], 0\n"
"v_mfma_f32_16x16x32_bf16 v[112:115], a[200:203], v[64:67], v[112:115]\n"
"s_waitcnt lgkmcnt(0)\n"
"v_mfma_f32_16x16x32_bf16 v[112:115], a[204:207], v[68:71], v[112:115]\n"
"s_barrier\n"
"v_mfma_f32_16x16x32_bf16 v[112:115], a[208:211], v[72:75], v[112:115]\n"
"global_load_dwordx4 v[64:67], v5, s[56:57] offset:1024 sc1\n"
"ds_read_b128 v[60:63], v2 offset:2048\n"
"v_mfma_f32_16x16x32_bf16 v[112:115], a[212:215], v[76:79], v[112:115]\n"
"ds_read_b128 v[116:119], v3 offset:512\n"
"ds_read_b128 v[120:123], v4 offset:3072\n"
"v_mfma_f32_16x16x32_bf16 v[112:115], a[216:219], v[80:83], v[112:115]\n"
"ds_read_b128 v[124:127], v2 offset:2304\n"
"ds_read_b128 v[128:131], v3 offset:768\n"
"v_mfma_f32_16x16x32_bf16 v[112:115], a[220:223], v[84:87], v[112:115]\n"
"ds_read_b128 v[132:135], v4 offset:3328\n"
"v_mfma_f32_16x16x32_bf16 v[80:83], v[88:91], a[0:3], 0\n"
"global_load_dwordx4 v[68:71], v5, s[56:57] offset:2048 sc1\n"
"v_mfma_f32_16x16x32_bf16 v[80:83], v[92:95], a[4:7], v[80:83]\n"
"s_waitcnt vmcnt(10)\n"
"v_mfma_f32_16x16x32_bf16 v[80:83], v[40:43], a[8:11], v[80:83]\n"
"v_mfma_f32_16x16x32_bf16 v[80:83], v[44:47], a[12:15], v[80:83]\n"
"s_waitcnt vmcnt(8)\n"
"v_mfma_f32_16x16x32_bf16 v[80:83], v[48:51], a[16:19], v[80:83]\n"
"s_waitcnt lgkmcnt(3)\n"
"v_add_f32 v60, v60, v116\n"
"v_add_f32 v61, v61, v117\n"
"v_mfma_f32_16x16x32_bf16 v[80:83], v[52:55], a[20:23], v[80:83]\n"
"s_waitcnt vmcnt(7)\n"
"global_load_dwordx4 v[72:75], v5, s[56:57] offset:3072 sc1\n"
"v_add_f32 v62, v62, v118\n"
"v_mfma_f32_16x16x32_bf16 v[80:83], v[56:59], a[24:27], v[80:83]\n"
"buffer_load_dwordx2 v[136:137], v7, s[40:43], s50 offen sc1\n"
"v_mfma_f32_16x16x32_bf16 v[84:87], v[88:91], a[28:31], 0\n"
"v_add_f32 v63, v63, v119\n"
"v_add_f32 v60, v60, v120\n"
"v_mfma_f32_16x16x32_bf16 v[84:87], v[92:95], a[32:35], v[84:87]\n"
"s_waitcnt vmcnt(7)\n"
"buffer_store_dwordx2 v[0:1], v9, s[40:43], s93 offen offset:0 sc1\n"
"ds_write_addtid_b32 v80 offset:2048\n"
"v_mfma_f32_16x16x32_bf16 v[84:87], v[40:43], a[36:39], v[84:87]\n"
"ds_write_addtid_b32 v81 offset:6688\n"
"v_add_f32 v61, v61, v121\n"
"v_mfma_f32_16x16x32_bf16 v[84:87], v[44:47], a[40:43], v[84:87]\n"
"ds_write_addtid_b32 v82 offset:11392\n"
"global_load_dwordx4 v[76:79], v6, s[56:57] offset:0 sc1\n"
"v_mfma_f32_16x16x32_bf16 v[84:87], v[48:51], a[44:47], v[84:87]\n"
"ds_write_addtid_b32 v83 offset:16032\n"
"v_add_f32 v62, v62, v122\n"
"v_mfma_f32_16x16x32_bf16 v[84:87], v[52:55], a[48:51], v[84:87]\n"
"v_add_f32 v63, v63, v123\n"
"v_add_f32 v60, v60, v100\n"
"v_mfma_f32_16x16x32_bf16 v[84:87], v[56:59], a[52:55], v[84:87]\n"
"v_add_f32 v61, v61, v101\n"
"v_add_f32 v62, v62, v102\n"
"v_mfma_f32_16x16x32_bf16 v[116:119], v[88:91], a[56:59], 0\n"
"v_add_f32 v63, v63, v103\n"
"v_mfma_f32_16x16x32_bf16 v[116:119], v[92:95], a[60:63], v[116:119]\n"
"ds_write_addtid_b32 v84 offset:2304\n"
"global_load_dwordx4 v[80:83], v6, s[56:57] offset:1024 sc1\n"
"v_mfma_f32_16x16x32_bf16 v[116:119], v[40:43], a[64:67], v[116:119]\n"
"s_add_u32 s93, s50, 0xffd00000\n"
"s_and_b32 s94, s93, 0xfbffffff\n"
"ds_write_addtid_b32 v85 offset:6944\n"
"v_mfma_f32_16x16x32_bf16 v[116:119], v[44:47], a[68:71], v[116:119]\n"
"ds_write_addtid_b32 v86 offset:11648\n"
"v_cvt_pk_bf16_f32 v100, v60, v61\n"
"v_mfma_f32_16x16x32_bf16 v[116:119], v[48:51], a[72:75], v[116:119]\n"
"s_waitcnt vmcnt(8)\n"
"ds_write_addtid_b32 v87 offset:16288\n"
"v_cvt_pk_bf16_f32 v101, v62, v63\n"
"v_mfma_f32_16x16x32_bf16 v[116:119], v[52:55], a[76:79], v[116:119]\n"
"s_waitcnt lgkmcnt(8)\n"
"v_add_f32 v124, v124, v128\n"
"v_add_f32 v125, v125, v129\n"
"v_mfma_f32_16x16x32_bf16 v[116:119], v[56:59], a[80:83], v[116:119]\n"
"global_load_dwordx4 v[84:87], v6, s[56:57] offset:2048 sc1\n"
"v_add_f32 v126, v126, v130\n"
"v_mfma_f32_16x16x32_bf16 v[60:63], v[88:91], a[84:87], 0\n"
"v_add_f32 v127, v127, v131\n"
"v_add_f32 v124, v124, v132\n"
"v_mfma_f32_16x16x32_bf16 v[60:63], v[92:95], a[88:91], v[60:63]\n"
"ds_write_addtid_b32 v116 offset:2560\n"
"v_add_f32 v125, v125, v133\n"
"v_mfma_f32_16x16x32_bf16 v[60:63], v[40:43], a[92:95], v[60:63]\n"
"ds_write_addtid_b32 v117 offset:7200\n"
"v_add_f32 v126, v126, v134\n"
"v_mfma_f32_16x16x32_bf16 v[60:63], v[44:47], a[96:99], v[60:63]\n"
"ds_write_addtid_b32 v118 offset:11904\n"
"v_add_f32 v127, v127, v135\n"
"v_mfma_f32_16x16x32_bf16 v[60:63], v[48:51], a[100:103], v[60:63]\n"
"ds_write_addtid_b32 v119 offset:16544\n"
"v_add_f32 v124, v124, v112\n"
"v_mfma_f32_16x16x32_bf16 v[60:63], v[52:55], a[104:107], v[60:63]\n"
"v_add_f32 v125, v125, v113\n"
"v_add_f32 v126, v126, v114\n"
"v_mfma_f32_16x16x32_bf16 v[60:63], v[56:59], a[108:111], v[60:63]\n"
"v_add_f32 v127, v127, v115\n"
"v_cvt_pk_bf16_f32 v102, v124, v125\n"
"v_mfma_f32_16x16x32_bf16 v[116:119], v[88:91], a[112:115], 0\n"
"v_cvt_pk_bf16_f32 v103, v126, v127\n"
"v_add_f32 v104, v104, v108\n"
"v_mfma_f32_16x16x32_bf16 v[116:119], v[92:95], a[116:119], v[116:119]\n"
"ds_write_addtid_b32 v60 offset:2816\n"
"v_add_f32 v105, v105, v109\n"
"v_mfma_f32_16x16x32_bf16 v[116:119], v[40:43], a[120:123], v[116:119]\n"
"ds_write_addtid_b32 v61 offset:7456\n"
"v_add_f32 v106, v106, v110\n"
"v_mfma_f32_16x16x32_bf16 v[116:119], v[44:47], a[124:127], v[116:119]\n"
"ds_write_addtid_b32 v62 offset:12160\n"
"v_add_f32 v107, v107, v111\n"
"v_mfma_f32_16x16x32_bf16 v[116:119], v[48:51], a[128:131], v[116:119]\n"
"ds_write_addtid_b32 v63 offset:16800\n"
"v_mfma_f32_16x16x32_bf16 v[116:119], v[52:55], a[132:135], v[116:119]\n"
"v_permlane32_swap_b32 v104, v106\n"
"v_mfma_f32_16x16x32_bf16 v[116:119], v[56:59], a[136:139], v[116:119]\n"
"v_permlane32_swap_b32 v105, v107\n"
"v_mfma_f32_16x16x32_bf16 v[60:63], v[88:91], a[140:143], 0\n"
"v_add_f32 v104, v104, v106\n"
"v_add_f32 v105, v105, v107\n"
"v_mfma_f32_16x16x32_bf16 v[60:63], v[92:95], a[144:147], v[60:63]\n"
"buffer_store_dwordx4 v[100:103], v8, s[40:43], s94 offen sc1\n"
"ds_write_addtid_b32 v116 offset:3072\n"
"v_permlane16_swap_b32 v104, v105\n"
"v_mfma_f32_16x16x32_bf16 v[60:63], v[40:43], a[148:151], v[60:63]\n"
"ds_write_addtid_b32 v117 offset:7712\n"
"v_add_f32 v104, v104, v105\n"
"v_mfma_f32_16x16x32_bf16 v[60:63], v[44:47], a[152:155], v[60:63]\n"
"ds_write_addtid_b32 v118 offset:12416\n"
"v_add_f32_dpp v105, v104, v104 row_ror:8 row_mask:0xf bank_mask:0xf\n"
"v_mfma_f32_16x16x32_bf16 v[60:63], v[48:51], a[156:159], v[60:63]\n"
"ds_write_addtid_b32 v119 offset:17056\n"
"v_mov_b32 v104, v105\n"
"v_mfma_f32_16x16x32_bf16 v[60:63], v[52:55], a[160:163], v[60:63]\n"
"v_permlane16_swap_b32 v105, v104\n"
"v_mfma_f32_16x16x32_bf16 v[60:63], v[56:59], a[164:167], v[60:63]\n"
"v_mul_f32 v105, 0xba800000, v105\n"
"v_fma_f32 v104, v104, s62, v15\n"
"v_mfma_f32_16x16x32_bf16 v[100:103], v[88:91], a[224:227], 0\n"
"v_fma_f32 v104, -v105, v105, v104\n"
"v_max_f32 v104, v15, v104\n"
"v_mfma_f32_16x16x32_bf16 v[100:103], v[92:95], a[228:231], v[100:103]\n"
"ds_write_addtid_b32 v60 offset:3328\n"
"v_mfma_f32_16x16x32_bf16 v[100:103], v[40:43], a[232:235], v[100:103]\n"
"ds_write_addtid_b32 v61 offset:7968\n"
"v_mfma_f32_16x16x32_bf16 v[100:103], v[44:47], a[236:239], v[100:103]\n"
"ds_write_addtid_b32 v62 offset:12672\n"
"v_mfma_f32_16x16x32_bf16 v[100:103], v[48:51], a[240:243], v[100:103]\n"
"ds_write_addtid_b32 v63 offset:17312\n"
"v_mfma_f32_16x16x32_bf16 v[100:103], v[52:55], a[244:247], v[100:103]\n"
"v_rsq_f32 v104, v104\n"
"v_mfma_f32_16x16x32_bf16 v[100:103], v[56:59], a[248:251], v[100:103]\n"
"v_mov_b32 v106, v0\n"
"v_mov_b32 v107, v1\n"
"v_mfma_f32_16x16x32_bf16 v[60:63], v[88:91], a[252:255], 0\n"
"buffer_store_dwordx4 v[104:107], v14, s[40:43], s95 offen sc1\n"
"v_mfma_f32_16x16x32_bf16 v[60:63], v[92:95], v[16:19], v[60:63]\n"
"s_add_u32 s58, s50, 0x180000\n"
"ds_write_addtid_b32 v100 offset:3584\n"
"v_mfma_f32_16x16x32_bf16 v[60:63], v[40:43], v[20:23], v[60:63]\n"
"s_and_b32 s58, s58, 0x3ffffff\n"
"ds_write_addtid_b32 v101 offset:8224\n"
"v_mfma_f32_16x16x32_bf16 v[60:63], v[44:47], v[24:27], v[60:63]\n"
"s_add_u32 s56, s40, s58\n"
"ds_write_addtid_b32 v102 offset:12928\n"
"v_mfma_f32_16x16x32_bf16 v[60:63], v[48:51], v[28:31], v[60:63]\n"
"s_addc_u32 s57, s41, 0\n"
"ds_write_addtid_b32 v103 offset:17568\n"
"v_mfma_f32_16x16x32_bf16 v[60:63], v[52:55], v[32:35], v[60:63]\n"
"s_waitcnt vmcnt(6)\n"
"v_cmp_ne_u64 vcc, s[48:49], v[136:137]\n"
"v_mfma_f32_16x16x32_bf16 v[60:63], v[56:59], v[36:39], v[60:63]\n"
"v_mfma_f32_16x16x32_bf16 v[108:111], a[168:171], v[88:91], 0\n"
"s_cbranch_vccnz .LH_2s\n"
".LH_2r:\n"
"v_mfma_f32_16x16x32_bf16 v[108:111], a[172:175], v[92:95], v[108:111]\n"
"s_add_u32 s50, s50, 0x180000\n"
"ds_write_addtid_b32 v60 offset:3840\n"
"v_mfma_f32_16x16x32_bf16 v[108:111], a[176:179], v[40:43], v[108:111]\n"
"ds_write_addtid_b32 v61 offset:8480\n"
"v_mfma_f32_16x16x32_bf16 v[108:111], a[180:183], v[44:47], v[108:111]\n"
"ds_write_addtid_b32 v62 offset:13184\n"
"v_mfma_f32_16x16x32_bf16 v[108:111], a[184:187], v[48:51], v[108:111]\n"
"ds_write_addtid_b32 v63 offset:17824\n"
"v_mfma_f32_16x16x32_bf16 v[108:111], a[188:191], v[52:55], v[108:111]\n"
"global_load_dwordx4 v[100:103], v5, s[56:57] offset:0 sc1\n"
"v_mfma_f32_16x16x32_bf16 v[108:111], a[192:195], v[56:59], v[108:111]\n"
"v_mfma_f32_16x16x32_bf16 v[112:115], a[196:199], v[88:91], 0\n"
"v_mfma_f32_16x16x32_bf16 v[112:115], a[200:203], v[92:95], v[112:115]\n"
"s_waitcnt lgkmcnt(0)\n"
"global_load_dwordx4 v[104:107], v5, s[56:57] offset:1024 sc1\n"
"v_mfma_f32_16x16x32_bf16 v[112:115], a[204:207], v[40:43], v[112:115]\n"
"s_barrier\n"
"v_mfma_f32_16x16x32_bf16 v[112:115], a[208:211], v[44:47], v[112:115]\n"
"ds_read_b128 v[92:95], v2 offset:0\n"
"ds_read_b128 v[60:63], v3 offset:2560\n"
"v_mfma_f32_16x16x32_bf16 v[112:115], a[212:215], v[48:51], v[112:115]\n"
"ds_read_b128 v[88:91], v4 offset:1024\n"
"ds_read_b128 v[116:119], v2 offset:256\n"
"v_mfma_f32_16x16x32_bf16 v[112:115], a[216:219], v[52:55], v[112:115]\n"
"ds_read_b128 v[120:123], v3 offset:2816\n"
"ds_read_b128 v[124:127], v4 offset:1280\n"
"v_mfma_f32_16x16x32_bf16 v[112:115], a[220:223], v[56:59], v[112:115]\n"
"s_branch .LH_top\n"
".LH_1s:\n"
"s_cmp_ge_u32 s50, s53\n"
"s_cbranch_scc1 .LH_end\n"
"s_cmp_ge_u32 s50, s52\n"
"s_cbranch_scc1 .LH_1r\n"
"s_sub_u32 s60, s50, 0x0\n"
"s_mov_b32 s61, 200000\n"
"s_nop 7\n"
".LH_1p:\n"
"buffer_load_dwordx2 v[130:131], v7, s[40:43], s60 offen sc1\n"
"s_waitcnt vmcnt(0)\n"
"v_cmp_ne_u64 vcc, s[48:49], v[130:131]\n"
"s_cbranch_vccz .LH_1px\n"
"s_sleep 1\n"
"s_sub_u32 s61, s61, 1\n"
"s_cbranch_scc0 .LH_1p\n"
"s_branch .LH_end\n"
".LH_1px:\n"
"s_branch .LH_1r\n"
".LH_2s:\n"
"s_cmp_ge_u32 s50, s53\n"
"s_cbranch_scc1 .LH_end\n"
"s_cmp_ge_u32 s50, s52\n"
"s_cbranch_scc1 .LH_2r\n"
"s_sub_u32 s60, s50, 0x0\n"
"s_mov_b32 s61, 200000\n"
"s_nop 7\n"
".LH_2p:\n"
"buffer_load_dwordx2 v[136:137], v7, s[40:43], s60 offen sc1\n"
"s_waitcnt vmcnt(0)\n"
"v_cmp_ne_u64 vcc, s[48:49], v[136:137]\n"
"s_cbranch_vccz .LH_2px\n"
"s_sleep 1\n"
"s_sub_u32 s61, s61, 1\n"
"s_cbranch_scc0 .LH_2p\n"
"s_branch .LH_end\n"
".LH_2px:\n"
"s_branch .LH_2r\n"
".LH_3s:\n"
"s_cmp_ge_u32 s50, s53\n"
"s_cbranch_scc1 .LH_end\n"
"s_cmp_ge_u32 s50, s52\n"
"s_cbranch_scc1 .LH_3r\n"
"s_sub_u32 s60, s50, 0x0\n"
"s_mov_b32 s61, 200000\n"
"s_nop 7\n"
".LH_3p:\n"
"buffer_load_dwordx2 v[128:129], v7, s[40:43], s60 offen sc1\n"
"s_waitcnt vmcnt(0)\n"
"v_cmp_ne_u64 vcc, s[48:49], v[128:129]\n"
"s_cbranch_vccz .LH_3px\n"
"s_sleep 1\n"
"s_sub_u32 s61, s61, 1\n"
"s_cbranch_scc0 .LH_3p\n"
"s_branch .LH_end\n"
".LH_3px:\n"
"s_branch .LH_3r\n"
".LH_4s:\n"
"s_cmp_ge_u32 s50, s53\n"
"s_cbranch_scc1 .LH_end\n"
"s_cmp_ge_u32 s50, s52\n"
"s_cbranch_scc1 .LH_4r\n"
"s_sub_u32 s60, s50, 0x0\n"
"s_mov_b32 s61, 200000\n"
"s_nop 7\n"
".LH_4p:\n"
"buffer_load_dwordx2 v[164:165], v7, s[40:43], s60 offen sc1\n"
"s_waitcnt vmcnt(0)\n"
"v_cmp_ne_u64 vcc, s[48:49], v[164:165]\n"
"s_cbranch_vccz .LH_4px\n"
"s_sleep 1\n"
"s_sub_u32 s61, s61, 1\n"
"s_cbranch_scc0 .LH_4p\n"
"s_branch .LH_end\n"
".LH_4px:\n"
"s_branch .LH_4r\n"
".LH_5s:\n"
"s_cmp_ge_i32 s95, 0xc000000\n"
"s_cselect_b64 s[64:65], 0, s[64:65]\n"
"s_cmp_ge_u32 s95, 0xc000000\n"
"s_cbranch_scc1 .LH_5r\n"
"s_mov_b32 s61, 200000\n"
"s_nop 7\n"
".LH_5p:\n"
"buffer_load_dwordx2 v[128:129], v12, s[40:43], s95 offen offset:0 sc1\n"
"s_waitcnt vmcnt(0)\n"
"v_cmp_ne_u64 vcc, s[64:65], v[128:129]\n"
"s_cbranch_vccz .LH_5px\n"
"s_sleep 1\n"
"s_sub_u32 s61, s61, 1\n"
"s_cbranch_scc0 .LH_5p\n"
"s_branch .LH_end\n"
".LH_5px:\n"
"s_branch .LH_5r\n"
".LH_end:\n"
"s_waitcnt vmcnt(0) lgkmcnt(0)\n"
"s_endpgm\n"
      :: "{v2}"((unsigned)(lds0 + ((wid + 3) & 3) * 18592 + trp)), "{v3}"((unsigned)(lds0 + ((wid + 2) & 3) * 18592 + trp)), "{v4}"((unsigned)(lds0 + ((wid + 1) & 3) * 18592 + trp)), "{v5}"((unsigned)(lane * 16 + f0 * 1024 + 134316032)), "{v6}"((unsigned)(lane * 16 + f0 * 1024 + 134320128)), "{v7}"((unsigned)((f0 + lane % 7) * 8 + 353024 + H_GL * 1572864)), "{v8}"((unsigned)(lane * 16 + pair * 4096 + kq * 1024 + 212992)), "{v9}"((unsigned)(lane ? DROP : pair * 32 + kq * 8 + 354048)), "{v10}"((unsigned)(lane * 16 + (pg0 + 4) * 4096 + kq * 1024 + 212992)), "{v11}"((unsigned)(lane > 1 ? DROP : (lane ? pg0 + 4 : pair) * 32 + kq * 8 + 354048)), "{v12}"((unsigned)((lane & 15) * 8 + 67463936)), "{v13}"((unsigned)(lane * 16 + 67457024)), "{v14}"((unsigned)((lane & 24) ? DROP : (2 * (lane & 7) + (lane >> 5)) * 16 + 67461120)), "{v15}"((unsigned)(0x3727c5ac)), "{v40}"((unsigned)(vT + tsw)), "{v41}"((unsigned)(vT + (tsw ^ 8))), "{v42}"((unsigned)(vD)), "{s[40:43]}"(rS), "{s[44:47]}"(rW), "{s[48:49]}"(wsp), "{s50}"((unsigned)(sA)), "{s51}"((unsigned)(sLo)), "{s52}"((unsigned)(sHi)), "{s53}"((unsigned)(sEx)), "{s54}"((unsigned)(sEnt)), "{s55}"((unsigned)(sLd)), "{s[56:57]}"(wsb + (sLo & 0x3ffffff)), "{s59}"((unsigned)(lds0 + wid * 18592)), "{s62}"((unsigned)(0x3a800000)), "{s66}"((unsigned)(sW0 + 64 * ((wid + 1) & 3))), "{s67}"((unsigned)(sW0 + 64 * ((wid + 2) & 3))), "{s68}"((unsigned)(sW0 + 64 * ((wid + 3) & 3))), "{s69}"((unsigned)(sW0 + 64 * ((wid + 4) & 3))), "{s70}"((unsigned)(sW0 + 256))
      : "v255", "a255", "s95", "vcc", "scc", "memory");

    } else if (grp < 4) {
      const unsigned sEx = sHi + G_EX * 3 * RS, sEnt = -wid & 3;
    asm volatile(
"s_nop 4\n"
"v_mov_b32 v186, 0\n"
"global_load_dwordx2 v[0:1], v186, s[48:49] sc1\n"
"v_mov_b32 v188, 0x3f803f80\n"
"v_mov_b32 v189, 0x3f803f80\n"
"v_mov_b32 v190, 0x3f803f80\n"
"v_mov_b32 v191, 0x3f803f80\n"
"s_add_u32 m0, s55, 0\n"
"s_add_u32 s60, s62, 0\n"
"buffer_load_dwordx4 v38, s[44:47], s60 offen lds\n"
"s_add_u32 m0, s55, 1024\n"
"s_add_u32 s60, s62, 32768\n"
"buffer_load_dwordx4 v38, s[44:47], s60 offen lds\n"
"s_add_u32 m0, s55, 2048\n"
"s_add_u32 s60, s63, 0\n"
"buffer_load_dwordx4 v38, s[44:47], s60 offen lds\n"
"s_add_u32 m0, s55, 3072\n"
"s_add_u32 s60, s63, 32768\n"
"buffer_load_dwordx4 v38, s[44:47], s60 offen lds\n"
"s_add_u32 m0, s55, 4096\n"
"s_add_u32 s60, s64, 0\n"
"buffer_load_dwordx4 v38, s[44:47], s60 offen lds\n"
"s_add_u32 m0, s55, 5120\n"
"s_add_u32 s60, s64, 32768\n"
"buffer_load_dwordx4 v38, s[44:47], s60 offen lds\n"
"s_add_u32 m0, s55, 6144\n"
"s_add_u32 s60, s65, 0\n"
"buffer_load_dwordx4 v38, s[44:47], s60 offen lds\n"
"s_add_u32 m0, s55, 7168\n"
"s_add_u32 s60, s65, 32768\n"
"buffer_load_dwordx4 v38, s[44:47], s60 offen lds\n"
"s_add_u32 m0, s55, 8192\n"
"s_add_u32 s60, s66, 0\n"
"buffer_load_dwordx4 v38, s[44:47], s60 offen lds\n"
"s_add_u32 m0, s55, 9216\n"
"s_add_u32 s60, s66, 32768\n"
"buffer_load_dwordx4 v38, s[44:47], s60 offen lds\n"
"s_add_u32 m0, s55, 10240\n"
"s_add_u32 s60, s62, 65536\n"
"buffer_load_dwordx4 v38, s[44:47], s60 offen lds\n"
"s_add_u32 m0, s55, 11264\n"
"s_add_u32 s60, s62, 98304\n"
"buffer_load_dwordx4 v38, s[44:47], s60 offen lds\n"
"s_add_u32 m0, s55, 12288\n"
"s_add_u32 s60, s63, 65536\n"
"buffer_load_dwordx4 v38, s[44:47], s60 offen lds\n"
"s_add_u32 m0, s55, 13312\n"
"s_add_u32 s60, s63, 98304\n"
"buffer_load_dwordx4 v38, s[44:47], s60 offen lds\n"
"s_add_u32 m0, s55, 14336\n"
"s_add_u32 s60, s64, 65536\n"
"buffer_load_dwordx4 v38, s[44:47], s60 offen lds\n"
"s_add_u32 m0, s55, 15360\n"
"s_add_u32 s60, s64, 98304\n"
"buffer_load_dwordx4 v38, s[44:47], s60 offen lds\n"
"s_add_u32 m0, s55, 16384\n"
"s_add_u32 s60, s65, 65536\n"
"buffer_load_dwordx4 v38, s[44:47], s60 offen lds\n"
"s_add_u32 m0, s55, 17408\n"
"s_add_u32 s60, s65, 98304\n"
"buffer_load_dwordx4 v38, s[44:47], s60 offen lds\n"
"s_add_u32 m0, s55, 18432\n"
"s_add_u32 s60, s66, 65536\n"
"buffer_load_dwordx4 v38, s[44:47], s60 offen lds\n"
"s_add_u32 m0, s55, 19456\n"
"s_add_u32 s60, s66, 98304\n"
"buffer_load_dwordx4 v38, s[44:47], s60 offen lds\n"
"s_add_u32 m0, s55, 20480\n"
"s_add_u32 s60, s62, 131072\n"
"buffer_load_dwordx4 v38, s[44:47], s60 offen lds\n"
"s_add_u32 m0, s55, 21504\n"
"s_add_u32 s60, s62, 163840\n"
"buffer_load_dwordx4 v38, s[44:47], s60 offen lds\n"
"s_add_u32 m0, s55, 22528\n"
"s_add_u32 s60, s63, 131072\n"
"buffer_load_dwordx4 v38, s[44:47], s60 offen lds\n"
"s_add_u32 m0, s55, 23552\n"
"s_add_u32 s60, s63, 163840\n"
"buffer_load_dwordx4 v38, s[44:47], s60 offen lds\n"
"s_add_u32 m0, s55, 24576\n"
"s_add_u32 s60, s64, 131072\n"
"buffer_load_dwordx4 v38, s[44:47], s60 offen lds\n"
"s_add_u32 m0, s55, 25600\n"
"s_add_u32 s60, s64, 163840\n"
"buffer_load_dwordx4 v38, s[44:47], s60 offen lds\n"
"s_add_u32 m0, s55, 26624\n"
"s_add_u32 s60, s65, 131072\n"
"buffer_load_dwordx4 v38, s[44:47], s60 offen lds\n"
"s_add_u32 m0, s55, 27648\n"
"s_add_u32 s60, s65, 163840\n"
"buffer_load_dwordx4 v38, s[44:47], s60 offen lds\n"
"s_add_u32 m0, s55, 28672\n"
"s_add_u32 s60, s66, 131072\n"
"buffer_load_dwordx4 v38, s[44:47], s60 offen lds\n"
"s_add_u32 m0, s55, 29696\n"
"s_add_u32 s60, s66, 163840\n"
"buffer_load_dwordx4 v38, s[44:47], s60 offen lds\n"
"s_add_u32 m0, s55, 30720\n"
"s_add_u32 s60, s62, 196608\n"
"buffer_load_dwordx4 v38, s[44:47], s60 offen lds\n"
"s_add_u32 m0, s55, 31744\n"
"s_add_u32 s60, s62, 229376\n"
"buffer_load_dwordx4 v38, s[44:47], s60 offen lds\n"
"s_add_u32 m0, s55, 32768\n"
"s_add_u32 s60, s63, 196608\n"
"buffer_load_dwordx4 v38, s[44:47], s60 offen lds\n"
"s_add_u32 m0, s55, 33792\n"
"s_add_u32 s60, s63, 229376\n"
"buffer_load_dwordx4 v38, s[44:47], s60 offen lds\n"
"s_add_u32 m0, s55, 34816\n"
"s_add_u32 s60, s64, 196608\n"
"buffer_load_dwordx4 v38, s[44:47], s60 offen lds\n"
"s_add_u32 m0, s55, 35840\n"
"s_add_u32 s60, s64, 229376\n"
"buffer_load_dwordx4 v38, s[44:47], s60 offen lds\n"
"s_add_u32 m0, s55, 36864\n"
"s_add_u32 s60, s65, 196608\n"
"buffer_load_dwordx4 v38, s[44:47], s60 offen lds\n"
"s_add_u32 m0, s55, 37888\n"
"s_add_u32 s60, s65, 229376\n"
"buffer_load_dwordx4 v38, s[44:47], s60 offen lds\n"
"s_add_u32 m0, s55, 38912\n"
"s_add_u32 s60, s66, 196608\n"
"buffer_load_dwordx4 v38, s[44:47], s60 offen lds\n"
"s_add_u32 m0, s55, 39936\n"
"s_add_u32 s60, s66, 229376\n"
"buffer_load_dwordx4 v38, s[44:47], s60 offen lds\n"
"s_waitcnt vmcnt(38)\n"
"ds_read_b64_tr_b16 a[0:1], v36 offset:0\n"
"ds_read_b64_tr_b16 a[2:3], v36 offset:256\n"
"ds_read_b64_tr_b16 a[28:29], v37 offset:0\n"
"ds_read_b64_tr_b16 a[30:31], v37 offset:256\n"
"v_add_u32 v0, 2, v0\n"
"v_add_u32 v1, 0x9e3779b9, v1\n"
"v_or_b32 v0, 1, v0\n"
"v_readfirstlane_b32 s49, v1\n"
"v_readfirstlane_b32 s48, v0\n"
"s_waitcnt vmcnt(36)\n"
"ds_read_b64_tr_b16 a[56:57], v36 offset:2048\n"
"ds_read_b64_tr_b16 a[58:59], v36 offset:2304\n"
"ds_read_b64_tr_b16 a[84:85], v37 offset:2048\n"
"ds_read_b64_tr_b16 a[86:87], v37 offset:2304\n"
"s_waitcnt lgkmcnt(4)\n"
"s_add_u32 m0, s55, 0\n"
"s_add_u32 s60, s62, 262144\n"
"buffer_load_dwordx4 v38, s[44:47], s60 offen lds\n"
"s_add_u32 m0, s55, 1024\n"
"s_add_u32 s60, s62, 294912\n"
"buffer_load_dwordx4 v38, s[44:47], s60 offen lds\n"
"s_waitcnt vmcnt(36)\n"
"ds_read_b64_tr_b16 a[112:113], v36 offset:4096\n"
"ds_read_b64_tr_b16 a[114:115], v36 offset:4352\n"
"ds_read_b64_tr_b16 a[140:141], v37 offset:4096\n"
"ds_read_b64_tr_b16 a[142:143], v37 offset:4352\n"
"s_waitcnt lgkmcnt(4)\n"
"s_add_u32 m0, s55, 2048\n"
"s_add_u32 s60, s63, 262144\n"
"buffer_load_dwordx4 v38, s[44:47], s60 offen lds\n"
"s_add_u32 m0, s55, 3072\n"
"s_add_u32 s60, s63, 294912\n"
"buffer_load_dwordx4 v38, s[44:47], s60 offen lds\n"
"s_waitcnt vmcnt(36)\n"
"ds_read_b64_tr_b16 a[168:169], v36 offset:6144\n"
"ds_read_b64_tr_b16 a[170:171], v36 offset:6400\n"
"ds_read_b64_tr_b16 a[196:197], v37 offset:6144\n"
"ds_read_b64_tr_b16 a[198:199], v37 offset:6400\n"
"s_waitcnt lgkmcnt(4)\n"
"s_add_u32 m0, s55, 4096\n"
"s_add_u32 s60, s64, 262144\n"
"buffer_load_dwordx4 v38, s[44:47], s60 offen lds\n"
"s_add_u32 m0, s55, 5120\n"
"s_add_u32 s60, s64, 294912\n"
"buffer_load_dwordx4 v38, s[44:47], s60 offen lds\n"
"s_waitcnt vmcnt(36)\n"
"ds_read_b64_tr_b16 a[224:225], v36 offset:8192\n"
"ds_read_b64_tr_b16 a[226:227], v36 offset:8448\n"
"ds_read_b64_tr_b16 a[252:253], v37 offset:8192\n"
"ds_read_b64_tr_b16 a[254:255], v37 offset:8448\n"
"s_waitcnt lgkmcnt(4)\n"
"s_add_u32 m0, s55, 6144\n"
"s_add_u32 s60, s65, 262144\n"
"buffer_load_dwordx4 v38, s[44:47], s60 offen lds\n"
"s_add_u32 m0, s55, 7168\n"
"s_add_u32 s60, s65, 294912\n"
"buffer_load_dwordx4 v38, s[44:47], s60 offen lds\n"
"s_waitcnt vmcnt(36)\n"
"ds_read_b64_tr_b16 a[4:5], v36 offset:10240\n"
"ds_read_b64_tr_b16 a[6:7], v36 offset:10496\n"
"ds_read_b64_tr_b16 a[32:33], v37 offset:10240\n"
"ds_read_b64_tr_b16 a[34:35], v37 offset:10496\n"
"s_waitcnt lgkmcnt(4)\n"
"s_add_u32 m0, s55, 8192\n"
"s_add_u32 s60, s66, 262144\n"
"buffer_load_dwordx4 v38, s[44:47], s60 offen lds\n"
"s_add_u32 m0, s55, 9216\n"
"s_add_u32 s60, s66, 294912\n"
"buffer_load_dwordx4 v38, s[44:47], s60 offen lds\n"
"s_waitcnt vmcnt(36)\n"
"ds_read_b64_tr_b16 a[60:61], v36 offset:12288\n"
"ds_read_b64_tr_b16 a[62:63], v36 offset:12544\n"
"ds_read_b64_tr_b16 a[88:89], v37 offset:12288\n"
"ds_read_b64_tr_b16 a[90:91], v37 offset:12544\n"
"s_waitcnt lgkmcnt(4)\n"
"s_add_u32 m0, s55, 10240\n"
"s_add_u32 s60, s62, 327680\n"
"buffer_load_dwordx4 v38, s[44:47], s60 offen lds\n"
"s_add_u32 m0, s55, 11264\n"
"s_add_u32 s60, s62, 360448\n"
"buffer_load_dwordx4 v38, s[44:47], s60 offen lds\n"
"s_waitcnt vmcnt(36)\n"
"ds_read_b64_tr_b16 a[116:117], v36 offset:14336\n"
"ds_read_b64_tr_b16 a[118:119], v36 offset:14592\n"
"ds_read_b64_tr_b16 a[144:145], v37 offset:14336\n"
"ds_read_b64_tr_b16 a[146:147], v37 offset:14592\n"
"s_waitcnt lgkmcnt(4)\n"
"s_add_u32 m0, s55, 12288\n"
"s_add_u32 s60, s63, 327680\n"
"buffer_load_dwordx4 v38, s[44:47], s60 offen lds\n"
"s_add_u32 m0, s55, 13312\n"
"s_add_u32 s60, s63, 360448\n"
"buffer_load_dwordx4 v38, s[44:47], s60 offen lds\n"
"s_waitcnt vmcnt(36)\n"
"ds_read_b64_tr_b16 a[172:173], v36 offset:16384\n"
"ds_read_b64_tr_b16 a[174:175], v36 offset:16640\n"
"ds_read_b64_tr_b16 a[200:201], v37 offset:16384\n"
"ds_read_b64_tr_b16 a[202:203], v37 offset:16640\n"
"s_waitcnt lgkmcnt(4)\n"
"s_add_u32 m0, s55, 14336\n"
"s_add_u32 s60, s64, 327680\n"
"buffer_load_dwordx4 v38, s[44:47], s60 offen lds\n"
"s_add_u32 m0, s55, 15360\n"
"s_add_u32 s60, s64, 360448\n"
"buffer_load_dwordx4 v38, s[44:47], s60 offen lds\n"
"s_waitcnt vmcnt(36)\n"
"ds_read_b64_tr_b16 a[228:229], v36 offset:18432\n"
"ds_read_b64_tr_b16 a[230:231], v36 offset:18688\n"
"ds_read_b64_tr_b16 v[12:13], v37 offset:18432\n"
"ds_read_b64_tr_b16 v[14:15], v37 offset:18688\n"
"s_waitcnt lgkmcnt(4)\n"
"s_add_u32 m0, s55, 16384\n"
"s_add_u32 s60, s65, 327680\n"
"buffer_load_dwordx4 v38, s[44:47], s60 offen lds\n"
"s_add_u32 m0, s55, 17408\n"
"s_add_u32 s60, s65, 360448\n"
"buffer_load_dwordx4 v38, s[44:47], s60 offen lds\n"
"s_waitcnt vmcnt(36)\n"
"ds_read_b64_tr_b16 a[8:9], v36 offset:20480\n"
"ds_read_b64_tr_b16 a[10:11], v36 offset:20736\n"
"ds_read_b64_tr_b16 a[36:37], v37 offset:20480\n"
"ds_read_b64_tr_b16 a[38:39], v37 offset:20736\n"
"s_waitcnt lgkmcnt(4)\n"
"s_add_u32 m0, s55, 18432\n"
"s_add_u32 s60, s66, 327680\n"
"buffer_load_dwordx4 v38, s[44:47], s60 offen lds\n"
"s_add_u32 m0, s55, 19456\n"
"s_add_u32 s60, s66, 360448\n"
"buffer_load_dwordx4 v38, s[44:47], s60 offen lds\n"
"s_waitcnt vmcnt(36)\n"
"ds_read_b64_tr_b16 a[64:65], v36 offset:22528\n"
"ds_read_b64_tr_b16 a[66:67], v36 offset:22784\n"
"ds_read_b64_tr_b16 a[92:93], v37 offset:22528\n"
"ds_read_b64_tr_b16 a[94:95], v37 offset:22784\n"
"s_waitcnt lgkmcnt(4)\n"
"s_add_u32 m0, s55, 20480\n"
"s_add_u32 s60, s62, 393216\n"
"buffer_load_dwordx4 v38, s[44:47], s60 offen lds\n"
"s_add_u32 m0, s55, 21504\n"
"s_add_u32 s60, s62, 425984\n"
"buffer_load_dwordx4 v38, s[44:47], s60 offen lds\n"
"s_waitcnt vmcnt(36)\n"
"ds_read_b64_tr_b16 a[120:121], v36 offset:24576\n"
"ds_read_b64_tr_b16 a[122:123], v36 offset:24832\n"
"ds_read_b64_tr_b16 a[148:149], v37 offset:24576\n"
"ds_read_b64_tr_b16 a[150:151], v37 offset:24832\n"
"s_waitcnt lgkmcnt(4)\n"
"s_add_u32 m0, s55, 22528\n"
"s_add_u32 s60, s63, 393216\n"
"buffer_load_dwordx4 v38, s[44:47], s60 offen lds\n"
"s_add_u32 m0, s55, 23552\n"
"s_add_u32 s60, s63, 425984\n"
"buffer_load_dwordx4 v38, s[44:47], s60 offen lds\n"
"s_waitcnt vmcnt(36)\n"
"ds_read_b64_tr_b16 a[176:177], v36 offset:26624\n"
"ds_read_b64_tr_b16 a[178:179], v36 offset:26880\n"
"ds_read_b64_tr_b16 a[204:205], v37 offset:26624\n"
"ds_read_b64_tr_b16 a[206:207], v37 offset:26880\n"
"s_waitcnt lgkmcnt(4)\n"
"s_add_u32 m0, s55, 24576\n"
"s_add_u32 s60, s64, 393216\n"
"buffer_load_dwordx4 v38, s[44:47], s60 offen lds\n"
"s_add_u32 m0, s55, 25600\n"
"s_add_u32 s60, s64, 425984\n"
"buffer_load_dwordx4 v38, s[44:47], s60 offen lds\n"
"s_waitcnt vmcnt(36)\n"
"ds_read_b64_tr_b16 a[232:233], v36 offset:28672\n"
"ds_read_b64_tr_b16 a[234:235], v36 offset:28928\n"
"ds_read_b64_tr_b16 v[16:17], v37 offset:28672\n"
"ds_read_b64_tr_b16 v[18:19], v37 offset:28928\n"
"s_waitcnt lgkmcnt(4)\n"
"s_add_u32 m0, s55, 26624\n"
"s_add_u32 s60, s65, 393216\n"
"buffer_load_dwordx4 v38, s[44:47], s60 offen lds\n"
"s_add_u32 m0, s55, 27648\n"
"s_add_u32 s60, s65, 425984\n"
"buffer_load_dwordx4 v38, s[44:47], s60 offen lds\n"
"s_waitcnt vmcnt(36)\n"
"ds_read_b64_tr_b16 a[12:13], v36 offset:30720\n"
"ds_read_b64_tr_b16 a[14:15], v36 offset:30976\n"
"ds_read_b64_tr_b16 a[40:41], v37 offset:30720\n"
"ds_read_b64_tr_b16 a[42:43], v37 offset:30976\n"
"s_waitcnt lgkmcnt(4)\n"
"s_add_u32 m0, s55, 28672\n"
"s_add_u32 s60, s66, 393216\n"
"buffer_load_dwordx4 v38, s[44:47], s60 offen lds\n"
"s_add_u32 m0, s55, 29696\n"
"s_add_u32 s60, s66, 425984\n"
"buffer_load_dwordx4 v38, s[44:47], s60 offen lds\n"
"s_waitcnt vmcnt(36)\n"
"ds_read_b64_tr_b16 a[68:69], v36 offset:32768\n"
"ds_read_b64_tr_b16 a[70:71], v36 offset:33024\n"
"ds_read_b64_tr_b16 a[96:97], v37 offset:32768\n"
"ds_read_b64_tr_b16 a[98:99], v37 offset:33024\n"
"s_waitcnt lgkmcnt(4)\n"
"s_waitcnt vmcnt(34)\n"
"ds_read_b64_tr_b16 a[124:125], v36 offset:34816\n"
"ds_read_b64_tr_b16 a[126:127], v36 offset:35072\n"
"ds_read_b64_tr_b16 a[152:153], v37 offset:34816\n"
"ds_read_b64_tr_b16 a[154:155], v37 offset:35072\n"
"s_waitcnt lgkmcnt(4)\n"
"s_waitcnt vmcnt(32)\n"
"ds_read_b64_tr_b16 a[180:181], v36 offset:36864\n"
"ds_read_b64_tr_b16 a[182:183], v36 offset:37120\n"
"ds_read_b64_tr_b16 a[208:209], v37 offset:36864\n"
"ds_read_b64_tr_b16 a[210:211], v37 offset:37120\n"
"s_waitcnt lgkmcnt(4)\n"
"s_waitcnt vmcnt(30)\n"
"ds_read_b64_tr_b16 a[236:237], v36 offset:38912\n"
"ds_read_b64_tr_b16 a[238:239], v36 offset:39168\n"
"ds_read_b64_tr_b16 v[20:21], v37 offset:38912\n"
"ds_read_b64_tr_b16 v[22:23], v37 offset:39168\n"
"s_waitcnt lgkmcnt(4)\n"
"s_waitcnt vmcnt(28)\n"
"ds_read_b64_tr_b16 a[16:17], v36 offset:0\n"
"ds_read_b64_tr_b16 a[18:19], v36 offset:256\n"
"ds_read_b64_tr_b16 a[44:45], v37 offset:0\n"
"ds_read_b64_tr_b16 a[46:47], v37 offset:256\n"
"s_waitcnt lgkmcnt(4)\n"
"s_waitcnt vmcnt(26)\n"
"ds_read_b64_tr_b16 a[72:73], v36 offset:2048\n"
"ds_read_b64_tr_b16 a[74:75], v36 offset:2304\n"
"ds_read_b64_tr_b16 a[100:101], v37 offset:2048\n"
"ds_read_b64_tr_b16 a[102:103], v37 offset:2304\n"
"s_waitcnt lgkmcnt(4)\n"
"s_waitcnt vmcnt(24)\n"
"ds_read_b64_tr_b16 a[128:129], v36 offset:4096\n"
"ds_read_b64_tr_b16 a[130:131], v36 offset:4352\n"
"ds_read_b64_tr_b16 a[156:157], v37 offset:4096\n"
"ds_read_b64_tr_b16 a[158:159], v37 offset:4352\n"
"s_waitcnt lgkmcnt(4)\n"
"s_waitcnt vmcnt(22)\n"
"ds_read_b64_tr_b16 a[184:185], v36 offset:6144\n"
"ds_read_b64_tr_b16 a[186:187], v36 offset:6400\n"
"ds_read_b64_tr_b16 a[212:213], v37 offset:6144\n"
"ds_read_b64_tr_b16 a[214:215], v37 offset:6400\n"
"s_waitcnt lgkmcnt(4)\n"
"s_waitcnt vmcnt(20)\n"
"ds_read_b64_tr_b16 a[240:241], v36 offset:8192\n"
"ds_read_b64_tr_b16 a[242:243], v36 offset:8448\n"
"ds_read_b64_tr_b16 v[24:25], v37 offset:8192\n"
"ds_read_b64_tr_b16 v[26:27], v37 offset:8448\n"
"s_waitcnt lgkmcnt(4)\n"
"s_waitcnt vmcnt(18)\n"
"ds_read_b64_tr_b16 a[20:21], v36 offset:10240\n"
"ds_read_b64_tr_b16 a[22:23], v36 offset:10496\n"
"ds_read_b64_tr_b16 a[48:49], v37 offset:10240\n"
"ds_read_b64_tr_b16 a[50:51], v37 offset:10496\n"
"s_waitcnt lgkmcnt(4)\n"
"s_waitcnt vmcnt(16)\n"
"ds_read_b64_tr_b16 a[76:77], v36 offset:12288\n"
"ds_read_b64_tr_b16 a[78:79], v36 offset:12544\n"
"ds_read_b64_tr_b16 a[104:105], v37 offset:12288\n"
"ds_read_b64_tr_b16 a[106:107], v37 offset:12544\n"
"s_waitcnt lgkmcnt(4)\n"
"s_waitcnt vmcnt(14)\n"
"ds_read_b64_tr_b16 a[132:133], v36 offset:14336\n"
"ds_read_b64_tr_b16 a[134:135], v36 offset:14592\n"
"ds_read_b64_tr_b16 a[160:161], v37 offset:14336\n"
"ds_read_b64_tr_b16 a[162:163], v37 offset:14592\n"
"s_waitcnt lgkmcnt(4)\n"
"s_waitcnt vmcnt(12)\n"
"ds_read_b64_tr_b16 a[188:189], v36 offset:16384\n"
"ds_read_b64_tr_b16 a[190:191], v36 offset:16640\n"
"ds_read_b64_tr_b16 a[216:217], v37 offset:16384\n"
"ds_read_b64_tr_b16 a[218:219], v37 offset:16640\n"
"s_waitcnt lgkmcnt(4)\n"
"s_waitcnt vmcnt(10)\n"
"ds_read_b64_tr_b16 a[244:245], v36 offset:18432\n"
"ds_read_b64_tr_b16 a[246:247], v36 offset:18688\n"
"ds_read_b64_tr_b16 v[28:29], v37 offset:18432\n"
"ds_read_b64_tr_b16 v[30:31], v37 offset:18688\n"
"s_waitcnt lgkmcnt(4)\n"
"s_waitcnt vmcnt(8)\n"
"ds_read_b64_tr_b16 a[24:25], v36 offset:20480\n"
"ds_read_b64_tr_b16 a[26:27], v36 offset:20736\n"
"ds_read_b64_tr_b16 a[52:53], v37 offset:20480\n"
"ds_read_b64_tr_b16 a[54:55], v37 offset:20736\n"
"s_waitcnt lgkmcnt(4)\n"
"s_waitcnt vmcnt(6)\n"
"ds_read_b64_tr_b16 a[80:81], v36 offset:22528\n"
"ds_read_b64_tr_b16 a[82:83], v36 offset:22784\n"
"ds_read_b64_tr_b16 a[108:109], v37 offset:22528\n"
"ds_read_b64_tr_b16 a[110:111], v37 offset:22784\n"
"s_waitcnt lgkmcnt(4)\n"
"s_waitcnt vmcnt(4)\n"
"ds_read_b64_tr_b16 a[136:137], v36 offset:24576\n"
"ds_read_b64_tr_b16 a[138:139], v36 offset:24832\n"
"ds_read_b64_tr_b16 a[164:165], v37 offset:24576\n"
"ds_read_b64_tr_b16 a[166:167], v37 offset:24832\n"
"s_waitcnt lgkmcnt(4)\n"
"s_waitcnt vmcnt(2)\n"
"ds_read_b64_tr_b16 a[192:193], v36 offset:26624\n"
"ds_read_b64_tr_b16 a[194:195], v36 offset:26880\n"
"ds_read_b64_tr_b16 a[220:221], v37 offset:26624\n"
"ds_read_b64_tr_b16 a[222:223], v37 offset:26880\n"
"s_waitcnt lgkmcnt(4)\n"
"s_waitcnt vmcnt(0)\n"
"ds_read_b64_tr_b16 a[248:249], v36 offset:28672\n"
"ds_read_b64_tr_b16 a[250:251], v36 offset:28928\n"
"ds_read_b64_tr_b16 v[32:33], v37 offset:28672\n"
"ds_read_b64_tr_b16 v[34:35], v37 offset:28928\n"
"s_waitcnt lgkmcnt(4)\n"
"s_waitcnt vmcnt(0)\n"
"s_waitcnt lgkmcnt(0)\n"
"s_mov_b32 m0, s59\n"
"s_barrier\n"
"v_mov_b32 v120, 0\n"
"v_mov_b32 v121, 0\n"
"v_mov_b32 v124, 0\n"
"v_mov_b32 v125, 0\n"
"v_mov_b32 v128, 0\n"
"v_mov_b32 v129, 0\n"
"v_mov_b32 v160, 0\n"
"v_mov_b32 v161, 0\n"
"s_mov_b32 s93, 0x80000000\n"
"s_mov_b32 s95, 0x80000000\n"
"s_mov_b32 s94, 0x80000000\n"
"s_mov_b32 s95, 0x80000000\n"
"s_mov_b32 s93, 0x80000000\n"
"s_mov_b32 s95, 0x80000000\n"
"s_mov_b32 s94, 0x80000000\n"
"s_mov_b32 s95, 0x80000000\n"
"s_cmp_eq_u32 s54, 0\n"
"s_cbranch_scc1 .LG_1s\n"
"s_cmp_eq_u32 s54, 1\n"
"s_cbranch_scc1 .LG_2s\n"
"s_cmp_eq_u32 s54, 2\n"
"s_cbranch_scc1 .LG_3s\n"
"s_cmp_eq_u32 s54, 3\n"
"s_cbranch_scc1 .LG_4s\n"
".p2align 6\n"
".LG_top:\n"
"v_mfma_f32_16x16x32_bf16 v[48:51], v[92:95], a[0:3], 0\n"
"v_mfma_f32_16x16x32_bf16 v[48:51], v[60:63], a[4:7], v[48:51]\n"
"global_load_dwordx4 v[36:39], v5, s[56:57] offset:2048 sc1\n"
"v_mfma_f32_16x16x32_bf16 v[48:51], v[64:67], a[8:11], v[48:51]\n"
"v_mfma_f32_16x16x32_bf16 v[48:51], v[68:71], a[12:15], v[48:51]\n"
"s_waitcnt vmcnt(5)\n"
"v_mfma_f32_16x16x32_bf16 v[48:51], v[72:75], a[16:19], v[48:51]\n"
"s_waitcnt lgkmcnt(3)\n"
"v_add_f32 v112, v112, v56\n"
"v_add_f32 v113, v113, v57\n"
"v_mfma_f32_16x16x32_bf16 v[48:51], v[76:79], a[20:23], v[48:51]\n"
"s_waitcnt vmcnt(4)\n"
"v_add_f32 v114, v114, v58\n"
"v_add_f32 v115, v115, v59\n"
"v_mfma_f32_16x16x32_bf16 v[48:51], v[80:83], a[24:27], v[48:51]\n"
"global_load_dwordx4 v[40:43], v5, s[56:57] offset:3072 sc1\n"
"v_add_f32 v112, v112, v84\n"
"v_mfma_f32_16x16x32_bf16 v[52:55], v[92:95], a[28:31], 0\n"
"buffer_load_dwordx2 v[128:129], v7, s[40:43], s50 offen sc1\n"
"v_mfma_f32_16x16x32_bf16 v[52:55], v[60:63], a[32:35], v[52:55]\n"
"ds_write_addtid_b32 v48 offset:0\n"
"v_add_f32 v113, v113, v85\n"
"v_mfma_f32_16x16x32_bf16 v[52:55], v[64:67], a[36:39], v[52:55]\n"
"ds_write_addtid_b32 v49 offset:4640\n"
"global_load_dwordx4 v[44:47], v6, s[56:57] offset:0 sc1\n"
"v_mfma_f32_16x16x32_bf16 v[52:55], v[68:71], a[40:43], v[52:55]\n"
"ds_write_addtid_b32 v50 offset:9344\n"
"v_add_f32 v114, v114, v86\n"
"v_mfma_f32_16x16x32_bf16 v[52:55], v[72:75], a[44:47], v[52:55]\n"
"ds_write_addtid_b32 v51 offset:13984\n"
"v_add_f32 v115, v115, v87\n"
"v_mfma_f32_16x16x32_bf16 v[52:55], v[76:79], a[48:51], v[52:55]\n"
"v_add_f32 v112, v112, v108\n"
"v_add_f32 v113, v113, v109\n"
"v_mfma_f32_16x16x32_bf16 v[52:55], v[80:83], a[52:55], v[52:55]\n"
"v_add_f32 v114, v114, v110\n"
"v_add_f32 v115, v115, v111\n"
"v_mfma_f32_16x16x32_bf16 v[56:59], v[92:95], a[56:59], 0\n"
"s_waitcnt vmcnt(6)\n"
"buffer_store_dwordx2 v[0:1], v9, s[40:43], s93 offen offset:0 sc1\n"
"v_mfma_f32_16x16x32_bf16 v[56:59], v[60:63], a[60:63], v[56:59]\n"
"s_add_u32 s94, s50, 0xffd00000\n"
"s_and_b32 s95, s94, 0xfbffffff\n"
"ds_write_addtid_b32 v52 offset:256\n"
"v_mfma_f32_16x16x32_bf16 v[56:59], v[64:67], a[64:67], v[56:59]\n"
"ds_write_addtid_b32 v53 offset:4896\n"
"global_load_dwordx4 v[48:51], v6, s[56:57] offset:1024 sc1\n"
"v_mfma_f32_16x16x32_bf16 v[56:59], v[68:71], a[68:71], v[56:59]\n"
"ds_write_addtid_b32 v54 offset:9600\n"
"v_cvt_pk_bf16_f32 v84, v112, v113\n"
"v_mfma_f32_16x16x32_bf16 v[56:59], v[72:75], a[72:75], v[56:59]\n"
"ds_write_addtid_b32 v55 offset:14240\n"
"v_cvt_pk_bf16_f32 v85, v114, v115\n"
"v_mfma_f32_16x16x32_bf16 v[56:59], v[76:79], a[76:79], v[56:59]\n"
"s_waitcnt lgkmcnt(8)\n"
"v_add_f32 v116, v116, v120\n"
"v_add_f32 v117, v117, v121\n"
"v_mfma_f32_16x16x32_bf16 v[56:59], v[80:83], a[80:83], v[56:59]\n"
"v_add_f32 v118, v118, v122\n"
"v_add_f32 v119, v119, v123\n"
"v_mfma_f32_16x16x32_bf16 v[88:91], v[92:95], a[84:87], 0\n"
"global_load_dwordx4 v[52:55], v6, s[56:57] offset:2048 sc1\n"
"v_add_f32 v116, v116, v124\n"
"v_mfma_f32_16x16x32_bf16 v[88:91], v[60:63], a[88:91], v[88:91]\n"
"ds_write_addtid_b32 v56 offset:512\n"
"v_add_f32 v117, v117, v125\n"
"v_mfma_f32_16x16x32_bf16 v[88:91], v[64:67], a[92:95], v[88:91]\n"
"ds_write_addtid_b32 v57 offset:5152\n"
"v_add_f32 v118, v118, v126\n"
"v_mfma_f32_16x16x32_bf16 v[88:91], v[68:71], a[96:99], v[88:91]\n"
"ds_write_addtid_b32 v58 offset:9856\n"
"v_add_f32 v119, v119, v127\n"
"v_mfma_f32_16x16x32_bf16 v[88:91], v[72:75], a[100:103], v[88:91]\n"
"ds_write_addtid_b32 v59 offset:14496\n"
"v_add_f32 v116, v116, v104\n"
"v_mfma_f32_16x16x32_bf16 v[88:91], v[76:79], a[104:107], v[88:91]\n"
"v_add_f32 v117, v117, v105\n"
"v_add_f32 v118, v118, v106\n"
"v_mfma_f32_16x16x32_bf16 v[88:91], v[80:83], a[108:111], v[88:91]\n"
"v_add_f32 v119, v119, v107\n"
"v_cvt_pk_bf16_f32 v86, v116, v117\n"
"v_mfma_f32_16x16x32_bf16 v[56:59], v[92:95], a[112:115], 0\n"
"v_cvt_pk_bf16_f32 v87, v118, v119\n"
"v_mfma_f32_16x16x32_bf16 v[56:59], v[60:63], a[116:119], v[56:59]\n"
"ds_write_addtid_b32 v88 offset:768\n"
"v_mfma_f32_16x16x32_bf16 v[56:59], v[64:67], a[120:123], v[56:59]\n"
"ds_write_addtid_b32 v89 offset:5408\n"
"v_mfma_f32_16x16x32_bf16 v[56:59], v[68:71], a[124:127], v[56:59]\n"
"ds_write_addtid_b32 v90 offset:10112\n"
"v_mfma_f32_16x16x32_bf16 v[56:59], v[72:75], a[128:131], v[56:59]\n"
"ds_write_addtid_b32 v91 offset:14752\n"
"v_mfma_f32_16x16x32_bf16 v[56:59], v[76:79], a[132:135], v[56:59]\n"
"v_mfma_f32_16x16x32_bf16 v[56:59], v[80:83], a[136:139], v[56:59]\n"
"v_mfma_f32_16x16x32_bf16 v[104:107], v[92:95], a[140:143], 0\n"
"v_mfma_f32_16x16x32_bf16 v[104:107], v[60:63], a[144:147], v[104:107]\n"
"ds_write_addtid_b32 v56 offset:1024\n"
"v_mfma_f32_16x16x32_bf16 v[104:107], v[64:67], a[148:151], v[104:107]\n"
"ds_write_addtid_b32 v57 offset:5664\n"
"v_mfma_f32_16x16x32_bf16 v[104:107], v[68:71], a[152:155], v[104:107]\n"
"ds_write_addtid_b32 v58 offset:10368\n"
"v_mfma_f32_16x16x32_bf16 v[104:107], v[72:75], a[156:159], v[104:107]\n"
"ds_write_addtid_b32 v59 offset:15008\n"
"v_mfma_f32_16x16x32_bf16 v[104:107], v[76:79], a[160:163], v[104:107]\n"
"buffer_store_dwordx4 v[84:87], v8, s[40:43], s95 offen sc1\n"
"v_mfma_f32_16x16x32_bf16 v[104:107], v[80:83], a[164:167], v[104:107]\n"
"v_mfma_f32_16x16x32_bf16 v[88:91], a[168:171], v[92:95], 0\n"
"v_mfma_f32_16x16x32_bf16 v[88:91], a[172:175], v[60:63], v[88:91]\n"
"ds_write_addtid_b32 v104 offset:1280\n"
"v_mfma_f32_16x16x32_bf16 v[88:91], a[176:179], v[64:67], v[88:91]\n"
"ds_write_addtid_b32 v105 offset:5920\n"
"v_mfma_f32_16x16x32_bf16 v[88:91], a[180:183], v[68:71], v[88:91]\n"
"ds_write_addtid_b32 v106 offset:10624\n"
"v_mfma_f32_16x16x32_bf16 v[88:91], a[184:187], v[72:75], v[88:91]\n"
"ds_write_addtid_b32 v107 offset:15264\n"
"v_mfma_f32_16x16x32_bf16 v[88:91], a[188:191], v[76:79], v[88:91]\n"
"v_mfma_f32_16x16x32_bf16 v[88:91], a[192:195], v[80:83], v[88:91]\n"
"v_mfma_f32_16x16x32_bf16 v[84:87], a[196:199], v[92:95], 0\n"
"v_mfma_f32_16x16x32_bf16 v[84:87], a[200:203], v[60:63], v[84:87]\n"
"v_mfma_f32_16x16x32_bf16 v[84:87], a[204:207], v[64:67], v[84:87]\n"
"v_mfma_f32_16x16x32_bf16 v[84:87], a[208:211], v[68:71], v[84:87]\n"
"s_add_u32 s58, s50, 0x180000\n"
"v_mfma_f32_16x16x32_bf16 v[84:87], a[212:215], v[72:75], v[84:87]\n"
"s_and_b32 s58, s58, 0x3ffffff\n"
"v_mfma_f32_16x16x32_bf16 v[84:87], a[216:219], v[76:79], v[84:87]\n"
"s_add_u32 s56, s40, s58\n"
"v_mfma_f32_16x16x32_bf16 v[84:87], a[220:223], v[80:83], v[84:87]\n"
"s_addc_u32 s57, s41, 0\n"
"v_mfma_f32_16x16x32_bf16 v[104:107], a[224:227], v[92:95], 0\n"
"s_waitcnt vmcnt(5)\n"
"v_cmp_ne_u64 vcc, s[48:49], v[128:129]\n"
"v_mfma_f32_16x16x32_bf16 v[104:107], a[228:231], v[60:63], v[104:107]\n"
"v_mfma_f32_16x16x32_bf16 v[104:107], a[232:235], v[64:67], v[104:107]\n"
"s_cbranch_vccnz .LG_3s\n"
".LG_3r:\n"
"v_mfma_f32_16x16x32_bf16 v[104:107], a[236:239], v[68:71], v[104:107]\n"
"s_add_u32 s50, s50, 0x180000\n"
"global_load_dwordx4 v[56:59], v5, s[56:57] offset:0 sc1\n"
"v_mfma_f32_16x16x32_bf16 v[104:107], a[240:243], v[72:75], v[104:107]\n"
"v_mfma_f32_16x16x32_bf16 v[104:107], a[244:247], v[76:79], v[104:107]\n"
"v_mfma_f32_16x16x32_bf16 v[104:107], a[248:251], v[80:83], v[104:107]\n"
"v_mfma_f32_16x16x32_bf16 v[108:111], a[252:255], v[92:95], 0\n"
"v_mfma_f32_16x16x32_bf16 v[108:111], v[12:15], v[60:63], v[108:111]\n"
"s_waitcnt lgkmcnt(0)\n"
"v_mfma_f32_16x16x32_bf16 v[108:111], v[16:19], v[64:67], v[108:111]\n"
"s_barrier\n"
"v_mfma_f32_16x16x32_bf16 v[108:111], v[20:23], v[68:71], v[108:111]\n"
"ds_read_b128 v[92:95], v2 offset:2048\n"
"ds_read_b128 v[112:115], v3 offset:512\n"
"v_mfma_f32_16x16x32_bf16 v[108:111], v[24:27], v[72:75], v[108:111]\n"
"global_load_dwordx4 v[60:63], v5, s[56:57] offset:1024 sc1\n"
"ds_read_b128 v[116:119], v4 offset:3072\n"
"v_mfma_f32_16x16x32_bf16 v[108:111], v[28:31], v[76:79], v[108:111]\n"
"ds_read_b128 v[120:123], v2 offset:2304\n"
"ds_read_b128 v[124:127], v3 offset:768\n"
"v_mfma_f32_16x16x32_bf16 v[108:111], v[32:35], v[80:83], v[108:111]\n"
"global_load_dwordx4 v[64:67], v5, s[56:57] offset:2048 sc1\n"
"ds_read_b128 v[128:131], v4 offset:3328\n"
"v_mfma_f32_16x16x32_bf16 v[76:79], v[96:99], a[0:3], 0\n"
"ds_read_b128 v[132:135], v2 offset:3584\n"
"ds_read_b128 v[136:139], v3 offset:1536\n"
"v_mfma_f32_16x16x32_bf16 v[76:79], v[100:103], a[4:7], v[76:79]\n"
"ds_read_b128 v[140:143], v4 offset:3584\n"
"ds_read_b128 v[144:147], v2 offset:3840\n"
"v_mfma_f32_16x16x32_bf16 v[76:79], v[36:39], a[8:11], v[76:79]\n"
"ds_read_b128 v[148:151], v3 offset:1792\n"
"ds_read_b128 v[152:155], v4 offset:3840\n"
"v_mfma_f32_16x16x32_bf16 v[76:79], v[40:43], a[12:15], v[76:79]\n"
"s_waitcnt vmcnt(5)\n"
"v_mfma_f32_16x16x32_bf16 v[76:79], v[44:47], a[16:19], v[76:79]\n"
"s_waitcnt lgkmcnt(9)\n"
"global_load_dwordx4 v[68:71], v5, s[56:57] offset:3072 sc1\n"
"v_add_f32 v92, v92, v112\n"
"v_mfma_f32_16x16x32_bf16 v[76:79], v[48:51], a[20:23], v[76:79]\n"
"s_waitcnt vmcnt(5)\n"
"v_add_f32 v93, v93, v113\n"
"v_add_f32 v94, v94, v114\n"
"v_mfma_f32_16x16x32_bf16 v[76:79], v[52:55], a[24:27], v[76:79]\n"
"v_add_f32 v95, v95, v115\n"
"v_add_f32 v92, v92, v116\n"
"v_mfma_f32_16x16x32_bf16 v[156:159], v[96:99], a[28:31], 0\n"
"buffer_load_dwordx2 v[160:161], v7, s[40:43], s50 offen sc1\n"
"v_mfma_f32_16x16x32_bf16 v[156:159], v[100:103], a[32:35], v[156:159]\n"
"ds_write_addtid_b32 v76 offset:2048\n"
"v_add_f32 v93, v93, v117\n"
"v_mfma_f32_16x16x32_bf16 v[156:159], v[36:39], a[36:39], v[156:159]\n"
"ds_write_addtid_b32 v77 offset:6688\n"
"v_add_f32 v94, v94, v118\n"
"v_mfma_f32_16x16x32_bf16 v[156:159], v[40:43], a[40:43], v[156:159]\n"
"ds_write_addtid_b32 v78 offset:11392\n"
"v_add_f32 v95, v95, v119\n"
"v_mfma_f32_16x16x32_bf16 v[156:159], v[44:47], a[44:47], v[156:159]\n"
"ds_write_addtid_b32 v79 offset:16032\n"
"v_add_f32 v92, v92, v88\n"
"v_mfma_f32_16x16x32_bf16 v[156:159], v[48:51], a[48:51], v[156:159]\n"
"global_load_dwordx4 v[72:75], v6, s[56:57] offset:0 sc1\n"
"v_add_f32 v93, v93, v89\n"
"v_mfma_f32_16x16x32_bf16 v[156:159], v[52:55], a[52:55], v[156:159]\n"
"v_add_f32 v94, v94, v90\n"
"v_add_f32 v95, v95, v91\n"
"v_mfma_f32_16x16x32_bf16 v[112:115], v[96:99], a[56:59], 0\n"
"s_add_u32 s93, s50, 0xffd00000\n"
"s_and_b32 s95, s93, 0xfbffffff\n"
"v_cvt_pk_bf16_f32 v116, v92, v93\n"
"v_mfma_f32_16x16x32_bf16 v[112:115], v[100:103], a[60:63], v[112:115]\n"
"ds_write_addtid_b32 v156 offset:2304\n"
"v_cvt_pk_bf16_f32 v117, v94, v95\n"
"v_mfma_f32_16x16x32_bf16 v[112:115], v[36:39], a[64:67], v[112:115]\n"
"s_waitcnt lgkmcnt(11)\n"
"ds_write_addtid_b32 v157 offset:6944\n"
"v_add_f32 v120, v120, v124\n"
"v_mfma_f32_16x16x32_bf16 v[112:115], v[40:43], a[68:71], v[112:115]\n"
"ds_write_addtid_b32 v158 offset:11648\n"
"v_add_f32 v121, v121, v125\n"
"v_mfma_f32_16x16x32_bf16 v[112:115], v[44:47], a[72:75], v[112:115]\n"
"s_waitcnt vmcnt(6)\n"
"ds_write_addtid_b32 v159 offset:16288\n"
"global_load_dwordx4 v[76:79], v6, s[56:57] offset:1024 sc1\n"
"v_mfma_f32_16x16x32_bf16 v[112:115], v[48:51], a[76:79], v[112:115]\n"
"global_load_dwordx4 v[80:83], v6, s[56:57] offset:2048 sc1\n"
"v_add_f32 v122, v122, v126\n"
"v_mfma_f32_16x16x32_bf16 v[112:115], v[52:55], a[80:83], v[112:115]\n"
"v_add_f32 v123, v123, v127\n"
"v_add_f32 v120, v120, v128\n"
"v_mfma_f32_16x16x32_bf16 v[88:91], v[96:99], a[84:87], 0\n"
"v_add_f32 v121, v121, v129\n"
"v_add_f32 v122, v122, v130\n"
"v_mfma_f32_16x16x32_bf16 v[88:91], v[100:103], a[88:91], v[88:91]\n"
"ds_write_addtid_b32 v112 offset:2560\n"
"v_add_f32 v123, v123, v131\n"
"v_mfma_f32_16x16x32_bf16 v[88:91], v[36:39], a[92:95], v[88:91]\n"
"ds_write_addtid_b32 v113 offset:7200\n"
"v_add_f32 v120, v120, v84\n"
"v_mfma_f32_16x16x32_bf16 v[88:91], v[40:43], a[96:99], v[88:91]\n"
"ds_write_addtid_b32 v114 offset:11904\n"
"v_add_f32 v121, v121, v85\n"
"v_mfma_f32_16x16x32_bf16 v[88:91], v[44:47], a[100:103], v[88:91]\n"
"ds_write_addtid_b32 v115 offset:16544\n"
"v_add_f32 v122, v122, v86\n"
"v_mfma_f32_16x16x32_bf16 v[88:91], v[48:51], a[104:107], v[88:91]\n"
"v_add_f32 v123, v123, v87\n"
"v_cvt_pk_bf16_f32 v118, v120, v121\n"
"v_mfma_f32_16x16x32_bf16 v[88:91], v[52:55], a[108:111], v[88:91]\n"
"buffer_store_dwordx2 v[0:1], v9, s[40:43], s94 offen offset:0 sc1\n"
"v_cvt_pk_bf16_f32 v119, v122, v123\n"
"v_mfma_f32_16x16x32_bf16 v[84:87], v[96:99], a[112:115], 0\n"
"buffer_store_dwordx4 v[116:119], v8, s[40:43], s95 offen sc1\n"
"v_mfma_f32_16x16x32_bf16 v[84:87], v[100:103], a[116:119], v[84:87]\n"
"s_waitcnt lgkmcnt(15)\n"
"ds_write_addtid_b32 v88 offset:2816\n"
"v_add_f32 v132, v132, v136\n"
"v_mfma_f32_16x16x32_bf16 v[84:87], v[36:39], a[120:123], v[84:87]\n"
"ds_write_addtid_b32 v89 offset:7456\n"
"v_add_f32 v133, v133, v137\n"
"v_mfma_f32_16x16x32_bf16 v[84:87], v[40:43], a[124:127], v[84:87]\n"
"ds_write_addtid_b32 v90 offset:12160\n"
"v_add_f32 v134, v134, v138\n"
"v_mfma_f32_16x16x32_bf16 v[84:87], v[44:47], a[128:131], v[84:87]\n"
"ds_write_addtid_b32 v91 offset:16800\n"
"v_add_f32 v135, v135, v139\n"
"v_mfma_f32_16x16x32_bf16 v[84:87], v[48:51], a[132:135], v[84:87]\n"
"v_add_f32 v132, v132, v140\n"
"v_add_f32 v133, v133, v141\n"
"v_mfma_f32_16x16x32_bf16 v[84:87], v[52:55], a[136:139], v[84:87]\n"
"v_add_f32 v134, v134, v142\n"
"v_add_f32 v135, v135, v143\n"
"v_mfma_f32_16x16x32_bf16 v[88:91], v[96:99], a[140:143], 0\n"
"v_add_f32 v132, v132, v104\n"
"v_add_f32 v133, v133, v105\n"
"v_mfma_f32_16x16x32_bf16 v[88:91], v[100:103], a[144:147], v[88:91]\n"
"ds_write_addtid_b32 v84 offset:3072\n"
"v_add_f32 v134, v134, v106\n"
"v_mfma_f32_16x16x32_bf16 v[88:91], v[36:39], a[148:151], v[88:91]\n"
"ds_write_addtid_b32 v85 offset:7712\n"
"v_add_f32 v135, v135, v107\n"
"v_mfma_f32_16x16x32_bf16 v[88:91], v[40:43], a[152:155], v[88:91]\n"
"ds_write_addtid_b32 v86 offset:12416\n"
"v_cvt_pk_bf16_f32 v92, v132, v133\n"
"v_mfma_f32_16x16x32_bf16 v[88:91], v[44:47], a[156:159], v[88:91]\n"
"ds_write_addtid_b32 v87 offset:17056\n"
"v_cvt_pk_bf16_f32 v93, v134, v135\n"
"v_mfma_f32_16x16x32_bf16 v[88:91], v[48:51], a[160:163], v[88:91]\n"
"s_waitcnt lgkmcnt(15)\n"
"v_add_f32 v144, v144, v148\n"
"v_add_f32 v145, v145, v149\n"
"v_mfma_f32_16x16x32_bf16 v[88:91], v[52:55], a[164:167], v[88:91]\n"
"v_add_f32 v146, v146, v150\n"
"v_add_f32 v147, v147, v151\n"
"v_mfma_f32_16x16x32_bf16 v[84:87], v[96:99], a[224:227], 0\n"
"v_add_f32 v144, v144, v152\n"
"v_add_f32 v145, v145, v153\n"
"v_mfma_f32_16x16x32_bf16 v[84:87], v[100:103], a[228:231], v[84:87]\n"
"ds_write_addtid_b32 v88 offset:3328\n"
"v_add_f32 v146, v146, v154\n"
"v_mfma_f32_16x16x32_bf16 v[84:87], v[36:39], a[232:235], v[84:87]\n"
"ds_write_addtid_b32 v89 offset:7968\n"
"v_add_f32 v147, v147, v155\n"
"v_mfma_f32_16x16x32_bf16 v[84:87], v[40:43], a[236:239], v[84:87]\n"
"ds_write_addtid_b32 v90 offset:12672\n"
"v_add_f32 v144, v144, v108\n"
"v_mfma_f32_16x16x32_bf16 v[84:87], v[44:47], a[240:243], v[84:87]\n"
"ds_write_addtid_b32 v91 offset:17312\n"
"v_add_f32 v145, v145, v109\n"
"v_mfma_f32_16x16x32_bf16 v[84:87], v[48:51], a[244:247], v[84:87]\n"
"v_add_f32 v146, v146, v110\n"
"v_add_f32 v147, v147, v111\n"
"v_mfma_f32_16x16x32_bf16 v[84:87], v[52:55], a[248:251], v[84:87]\n"
"v_cvt_pk_bf16_f32 v94, v144, v145\n"
"v_cvt_pk_bf16_f32 v95, v146, v147\n"
"v_mfma_f32_16x16x32_bf16 v[88:91], v[96:99], a[252:255], 0\n"
"buffer_store_dwordx4 v[92:95], v10, s[40:43], s95 offen sc1\n"
"v_mfma_f32_16x16x32_bf16 v[88:91], v[100:103], v[12:15], v[88:91]\n"
"ds_write_addtid_b32 v84 offset:3584\n"
"v_mfma_f32_16x16x32_bf16 v[88:91], v[36:39], v[16:19], v[88:91]\n"
"ds_write_addtid_b32 v85 offset:8224\n"
"v_mfma_f32_16x16x32_bf16 v[88:91], v[40:43], v[20:23], v[88:91]\n"
"ds_write_addtid_b32 v86 offset:12928\n"
"v_mfma_f32_16x16x32_bf16 v[88:91], v[44:47], v[24:27], v[88:91]\n"
"ds_write_addtid_b32 v87 offset:17568\n"
"v_mfma_f32_16x16x32_bf16 v[88:91], v[48:51], v[28:31], v[88:91]\n"
"v_mfma_f32_16x16x32_bf16 v[88:91], v[52:55], v[32:35], v[88:91]\n"
"s_add_u32 s58, s50, 0x180000\n"
"v_mfma_f32_16x16x32_bf16 v[92:95], a[168:171], v[96:99], 0\n"
"s_and_b32 s58, s58, 0x3ffffff\n"
"v_mfma_f32_16x16x32_bf16 v[92:95], a[172:175], v[100:103], v[92:95]\n"
"s_add_u32 s56, s40, s58\n"
"ds_write_addtid_b32 v88 offset:3840\n"
"v_mfma_f32_16x16x32_bf16 v[92:95], a[176:179], v[36:39], v[92:95]\n"
"s_addc_u32 s57, s41, 0\n"
"ds_write_addtid_b32 v89 offset:8480\n"
"v_mfma_f32_16x16x32_bf16 v[92:95], a[180:183], v[40:43], v[92:95]\n"
"s_waitcnt vmcnt(6)\n"
"ds_write_addtid_b32 v90 offset:13184\n"
"v_cmp_ne_u64 vcc, s[48:49], v[160:161]\n"
"v_mfma_f32_16x16x32_bf16 v[92:95], a[184:187], v[44:47], v[92:95]\n"
"ds_write_addtid_b32 v91 offset:17824\n"
"v_mfma_f32_16x16x32_bf16 v[92:95], a[188:191], v[48:51], v[92:95]\n"
"s_cbranch_vccnz .LG_4s\n"
".LG_4r:\n"
"v_mfma_f32_16x16x32_bf16 v[92:95], a[192:195], v[52:55], v[92:95]\n"
"s_add_u32 s50, s50, 0x180000\n"
"global_load_dwordx4 v[84:87], v5, s[56:57] offset:0 sc1\n"
"v_mfma_f32_16x16x32_bf16 v[104:107], a[196:199], v[96:99], 0\n"
"v_mfma_f32_16x16x32_bf16 v[104:107], a[200:203], v[100:103], v[104:107]\n"
"s_waitcnt lgkmcnt(0)\n"
"v_mfma_f32_16x16x32_bf16 v[104:107], a[204:207], v[36:39], v[104:107]\n"
"s_barrier\n"
"v_mfma_f32_16x16x32_bf16 v[104:107], a[208:211], v[40:43], v[104:107]\n"
"ds_read_b128 v[88:91], v2 offset:0\n"
"ds_read_b128 v[96:99], v3 offset:2560\n"
"v_mfma_f32_16x16x32_bf16 v[104:107], a[212:215], v[44:47], v[104:107]\n"
"ds_read_b128 v[100:103], v4 offset:1024\n"
"ds_read_b128 v[108:111], v2 offset:256\n"
"v_mfma_f32_16x16x32_bf16 v[104:107], a[216:219], v[48:51], v[104:107]\n"
"global_load_dwordx4 v[36:39], v5, s[56:57] offset:1024 sc1\n"
"ds_read_b128 v[112:115], v3 offset:2816\n"
"v_mfma_f32_16x16x32_bf16 v[104:107], a[220:223], v[52:55], v[104:107]\n"
"ds_read_b128 v[116:119], v4 offset:1280\n"
"v_mfma_f32_16x16x32_bf16 v[48:51], v[56:59], a[0:3], 0\n"
"v_mfma_f32_16x16x32_bf16 v[48:51], v[60:63], a[4:7], v[48:51]\n"
"v_mfma_f32_16x16x32_bf16 v[48:51], v[64:67], a[8:11], v[48:51]\n"
"global_load_dwordx4 v[40:43], v5, s[56:57] offset:2048 sc1\n"
"v_mfma_f32_16x16x32_bf16 v[48:51], v[68:71], a[12:15], v[48:51]\n"
"s_waitcnt vmcnt(7)\n"
"buffer_load_dwordx2 v[120:121], v7, s[40:43], s50 offen sc1\n"
"v_mfma_f32_16x16x32_bf16 v[48:51], v[72:75], a[16:19], v[48:51]\n"
"s_waitcnt lgkmcnt(3)\n"
"v_add_f32 v88, v88, v96\n"
"v_add_f32 v89, v89, v97\n"
"v_mfma_f32_16x16x32_bf16 v[48:51], v[76:79], a[20:23], v[48:51]\n"
"s_waitcnt vmcnt(7)\n"
"v_add_f32 v90, v90, v98\n"
"v_add_f32 v91, v91, v99\n"
"v_mfma_f32_16x16x32_bf16 v[48:51], v[80:83], a[24:27], v[48:51]\n"
"v_add_f32 v88, v88, v100\n"
"v_add_f32 v89, v89, v101\n"
"v_mfma_f32_16x16x32_bf16 v[96:99], v[56:59], a[28:31], 0\n"
"global_load_dwordx4 v[44:47], v5, s[56:57] offset:3072 sc1\n"
"v_add_f32 v90, v90, v102\n"
"v_mfma_f32_16x16x32_bf16 v[96:99], v[60:63], a[32:35], v[96:99]\n"
"ds_write_addtid_b32 v48 offset:0\n"
"v_add_f32 v91, v91, v103\n"
"v_mfma_f32_16x16x32_bf16 v[96:99], v[64:67], a[36:39], v[96:99]\n"
"ds_write_addtid_b32 v49 offset:4640\n"
"v_add_f32 v88, v88, v92\n"
"v_mfma_f32_16x16x32_bf16 v[96:99], v[68:71], a[40:43], v[96:99]\n"
"ds_write_addtid_b32 v50 offset:9344\n"
"v_add_f32 v89, v89, v93\n"
"v_mfma_f32_16x16x32_bf16 v[96:99], v[72:75], a[44:47], v[96:99]\n"
"ds_write_addtid_b32 v51 offset:13984\n"
"v_add_f32 v90, v90, v94\n"
"v_mfma_f32_16x16x32_bf16 v[96:99], v[76:79], a[48:51], v[96:99]\n"
"v_add_f32 v91, v91, v95\n"
"v_mfma_f32_16x16x32_bf16 v[96:99], v[80:83], a[52:55], v[96:99]\n"
"s_add_u32 s94, s50, 0xffd00000\n"
"s_and_b32 s95, s94, 0xfbffffff\n"
"global_load_dwordx4 v[48:51], v6, s[56:57] offset:0 sc1\n"
"v_mfma_f32_16x16x32_bf16 v[92:95], v[56:59], a[56:59], 0\n"
"v_cvt_pk_bf16_f32 v100, v88, v89\n"
"v_cvt_pk_bf16_f32 v101, v90, v91\n"
"v_mfma_f32_16x16x32_bf16 v[92:95], v[60:63], a[60:63], v[92:95]\n"
"s_waitcnt lgkmcnt(4)\n"
"ds_write_addtid_b32 v96 offset:256\n"
"v_add_f32 v108, v108, v112\n"
"v_mfma_f32_16x16x32_bf16 v[92:95], v[64:67], a[64:67], v[92:95]\n"
"ds_write_addtid_b32 v97 offset:4896\n"
"v_add_f32 v109, v109, v113\n"
"v_mfma_f32_16x16x32_bf16 v[92:95], v[68:71], a[68:71], v[92:95]\n"
"ds_write_addtid_b32 v98 offset:9600\n"
"global_load_dwordx4 v[52:55], v6, s[56:57] offset:1024 sc1\n"
"v_mfma_f32_16x16x32_bf16 v[92:95], v[72:75], a[72:75], v[92:95]\n"
"ds_write_addtid_b32 v99 offset:14240\n"
"v_add_f32 v110, v110, v114\n"
"v_mfma_f32_16x16x32_bf16 v[92:95], v[76:79], a[76:79], v[92:95]\n"
"v_add_f32 v111, v111, v115\n"
"v_add_f32 v108, v108, v116\n"
"v_mfma_f32_16x16x32_bf16 v[92:95], v[80:83], a[80:83], v[92:95]\n"
"v_add_f32 v109, v109, v117\n"
"v_add_f32 v110, v110, v118\n"
"v_mfma_f32_16x16x32_bf16 v[96:99], v[56:59], a[84:87], 0\n"
"v_add_f32 v111, v111, v119\n"
"v_add_f32 v108, v108, v104\n"
"v_mfma_f32_16x16x32_bf16 v[96:99], v[60:63], a[88:91], v[96:99]\n"
"ds_write_addtid_b32 v92 offset:512\n"
"v_add_f32 v109, v109, v105\n"
"v_mfma_f32_16x16x32_bf16 v[96:99], v[64:67], a[92:95], v[96:99]\n"
"ds_write_addtid_b32 v93 offset:5152\n"
"global_load_dwordx4 v[88:91], v6, s[56:57] offset:2048 sc1\n"
"v_mfma_f32_16x16x32_bf16 v[96:99], v[68:71], a[96:99], v[96:99]\n"
"ds_write_addtid_b32 v94 offset:9856\n"
"v_add_f32 v110, v110, v106\n"
"v_mfma_f32_16x16x32_bf16 v[96:99], v[72:75], a[100:103], v[96:99]\n"
"ds_write_addtid_b32 v95 offset:14496\n"
"v_add_f32 v111, v111, v107\n"
"v_mfma_f32_16x16x32_bf16 v[96:99], v[76:79], a[104:107], v[96:99]\n"
"v_cvt_pk_bf16_f32 v102, v108, v109\n"
"v_cvt_pk_bf16_f32 v103, v110, v111\n"
"v_mfma_f32_16x16x32_bf16 v[96:99], v[80:83], a[108:111], v[96:99]\n"
"s_waitcnt vmcnt(8)\n"
"buffer_store_dwordx2 v[0:1], v11, s[40:43], s93 offen offset:0 sc1\n"
"v_mfma_f32_16x16x32_bf16 v[92:95], v[56:59], a[112:115], 0\n"
"v_mfma_f32_16x16x32_bf16 v[92:95], v[60:63], a[116:119], v[92:95]\n"
"ds_write_addtid_b32 v96 offset:768\n"
"v_mfma_f32_16x16x32_bf16 v[92:95], v[64:67], a[120:123], v[92:95]\n"
"buffer_store_dwordx4 v[100:103], v8, s[40:43], s95 offen sc1\n"
"ds_write_addtid_b32 v97 offset:5408\n"
"v_mfma_f32_16x16x32_bf16 v[92:95], v[68:71], a[124:127], v[92:95]\n"
"ds_write_addtid_b32 v98 offset:10112\n"
"v_mfma_f32_16x16x32_bf16 v[92:95], v[72:75], a[128:131], v[92:95]\n"
"ds_write_addtid_b32 v99 offset:14752\n"
"v_mfma_f32_16x16x32_bf16 v[92:95], v[76:79], a[132:135], v[92:95]\n"
"v_mfma_f32_16x16x32_bf16 v[92:95], v[80:83], a[136:139], v[92:95]\n"
"v_mfma_f32_16x16x32_bf16 v[100:103], v[56:59], a[140:143], 0\n"
"v_mfma_f32_16x16x32_bf16 v[100:103], v[60:63], a[144:147], v[100:103]\n"
"ds_write_addtid_b32 v92 offset:1024\n"
"v_mfma_f32_16x16x32_bf16 v[100:103], v[64:67], a[148:151], v[100:103]\n"
"ds_write_addtid_b32 v93 offset:5664\n"
"v_mfma_f32_16x16x32_bf16 v[100:103], v[68:71], a[152:155], v[100:103]\n"
"ds_write_addtid_b32 v94 offset:10368\n"
"v_mfma_f32_16x16x32_bf16 v[100:103], v[72:75], a[156:159], v[100:103]\n"
"ds_write_addtid_b32 v95 offset:15008\n"
"v_mfma_f32_16x16x32_bf16 v[100:103], v[76:79], a[160:163], v[100:103]\n"
"v_mfma_f32_16x16x32_bf16 v[100:103], v[80:83], a[164:167], v[100:103]\n"
"v_mfma_f32_16x16x32_bf16 v[96:99], v[56:59], a[224:227], 0\n"
"v_mfma_f32_16x16x32_bf16 v[96:99], v[60:63], a[228:231], v[96:99]\n"
"ds_write_addtid_b32 v100 offset:1280\n"
"v_mfma_f32_16x16x32_bf16 v[96:99], v[64:67], a[232:235], v[96:99]\n"
"ds_write_addtid_b32 v101 offset:5920\n"
"v_mfma_f32_16x16x32_bf16 v[96:99], v[68:71], a[236:239], v[96:99]\n"
"ds_write_addtid_b32 v102 offset:10624\n"
"v_mfma_f32_16x16x32_bf16 v[96:99], v[72:75], a[240:243], v[96:99]\n"
"ds_write_addtid_b32 v103 offset:15264\n"
"v_mfma_f32_16x16x32_bf16 v[96:99], v[76:79], a[244:247], v[96:99]\n"
"v_mfma_f32_16x16x32_bf16 v[96:99], v[80:83], a[248:251], v[96:99]\n"
"v_mfma_f32_16x16x32_bf16 v[104:107], v[56:59], a[252:255], 0\n"
"v_mfma_f32_16x16x32_bf16 v[104:107], v[60:63], v[12:15], v[104:107]\n"
"s_add_u32 s58, s50, 0x180000\n"
"ds_write_addtid_b32 v96 offset:1536\n"
"v_mfma_f32_16x16x32_bf16 v[104:107], v[64:67], v[16:19], v[104:107]\n"
"s_and_b32 s58, s58, 0x3ffffff\n"
"ds_write_addtid_b32 v97 offset:6176\n"
"v_mfma_f32_16x16x32_bf16 v[104:107], v[68:71], v[20:23], v[104:107]\n"
"s_add_u32 s56, s40, s58\n"
"ds_write_addtid_b32 v98 offset:10880\n"
"v_mfma_f32_16x16x32_bf16 v[104:107], v[72:75], v[24:27], v[104:107]\n"
"s_addc_u32 s57, s41, 0\n"
"ds_write_addtid_b32 v99 offset:15520\n"
"v_mfma_f32_16x16x32_bf16 v[104:107], v[76:79], v[28:31], v[104:107]\n"
"s_waitcnt vmcnt(6)\n"
"v_cmp_ne_u64 vcc, s[48:49], v[120:121]\n"
"v_mfma_f32_16x16x32_bf16 v[104:107], v[80:83], v[32:35], v[104:107]\n"
"v_mfma_f32_16x16x32_bf16 v[96:99], a[168:171], v[56:59], 0\n"
"s_cbranch_vccnz .LG_1s\n"
".LG_1r:\n"
"v_mfma_f32_16x16x32_bf16 v[96:99], a[172:175], v[60:63], v[96:99]\n"
"s_add_u32 s50, s50, 0x180000\n"
"ds_write_addtid_b32 v104 offset:1792\n"
"v_mfma_f32_16x16x32_bf16 v[96:99], a[176:179], v[64:67], v[96:99]\n"
"ds_write_addtid_b32 v105 offset:6432\n"
"v_mfma_f32_16x16x32_bf16 v[96:99], a[180:183], v[68:71], v[96:99]\n"
"ds_write_addtid_b32 v106 offset:11136\n"
"v_mfma_f32_16x16x32_bf16 v[96:99], a[184:187], v[72:75], v[96:99]\n"
"ds_write_addtid_b32 v107 offset:15776\n"
"global_load_dwordx4 v[92:95], v5, s[56:57] offset:0 sc1\n"
"v_mfma_f32_16x16x32_bf16 v[96:99], a[188:191], v[76:79], v[96:99]\n"
"v_mfma_f32_16x16x32_bf16 v[96:99], a[192:195], v[80:83], v[96:99]\n"
"v_mfma_f32_16x16x32_bf16 v[100:103], a[196:199], v[56:59], 0\n"
"v_mfma_f32_16x16x32_bf16 v[100:103], a[200:203], v[60:63], v[100:103]\n"
"s_waitcnt lgkmcnt(0)\n"
"v_mfma_f32_16x16x32_bf16 v[100:103], a[204:207], v[64:67], v[100:103]\n"
"s_barrier\n"
"v_mfma_f32_16x16x32_bf16 v[100:103], a[208:211], v[68:71], v[100:103]\n"
"global_load_dwordx4 v[60:63], v5, s[56:57] offset:1024 sc1\n"
"ds_read_b128 v[56:59], v2 offset:2048\n"
"v_mfma_f32_16x16x32_bf16 v[100:103], a[212:215], v[72:75], v[100:103]\n"
"ds_read_b128 v[104:107], v3 offset:512\n"
"ds_read_b128 v[108:111], v4 offset:3072\n"
"v_mfma_f32_16x16x32_bf16 v[100:103], a[216:219], v[76:79], v[100:103]\n"
"ds_read_b128 v[112:115], v2 offset:2304\n"
"ds_read_b128 v[116:119], v3 offset:768\n"
"v_mfma_f32_16x16x32_bf16 v[100:103], a[220:223], v[80:83], v[100:103]\n"
"ds_read_b128 v[120:123], v4 offset:3328\n"
"v_mfma_f32_16x16x32_bf16 v[76:79], v[84:87], a[0:3], 0\n"
"global_load_dwordx4 v[64:67], v5, s[56:57] offset:2048 sc1\n"
"v_mfma_f32_16x16x32_bf16 v[76:79], v[36:39], a[4:7], v[76:79]\n"
"s_waitcnt vmcnt(8)\n"
"v_mfma_f32_16x16x32_bf16 v[76:79], v[40:43], a[8:11], v[76:79]\n"
"v_mfma_f32_16x16x32_bf16 v[76:79], v[44:47], a[12:15], v[76:79]\n"
"s_waitcnt vmcnt(6)\n"
"v_mfma_f32_16x16x32_bf16 v[76:79], v[48:51], a[16:19], v[76:79]\n"
"s_waitcnt lgkmcnt(3)\n"
"v_add_f32 v56, v56, v104\n"
"v_add_f32 v57, v57, v105\n"
"v_mfma_f32_16x16x32_bf16 v[76:79], v[52:55], a[20:23], v[76:79]\n"
"s_waitcnt vmcnt(5)\n"
"global_load_dwordx4 v[68:71], v5, s[56:57] offset:3072 sc1\n"
"v_add_f32 v58, v58, v106\n"
"v_mfma_f32_16x16x32_bf16 v[76:79], v[88:91], a[24:27], v[76:79]\n"
"buffer_load_dwordx2 v[124:125], v7, s[40:43], s50 offen sc1\n"
"v_mfma_f32_16x16x32_bf16 v[80:83], v[84:87], a[28:31], 0\n"
"v_add_f32 v59, v59, v107\n"
"v_add_f32 v56, v56, v108\n"
"v_mfma_f32_16x16x32_bf16 v[80:83], v[36:39], a[32:35], v[80:83]\n"
"s_waitcnt vmcnt(5)\n"
"buffer_store_dwordx2 v[0:1], v9, s[40:43], s94 offen offset:0 sc1\n"
"ds_write_addtid_b32 v76 offset:2048\n"
"v_mfma_f32_16x16x32_bf16 v[80:83], v[40:43], a[36:39], v[80:83]\n"
"ds_write_addtid_b32 v77 offset:6688\n"
"v_add_f32 v57, v57, v109\n"
"v_mfma_f32_16x16x32_bf16 v[80:83], v[44:47], a[40:43], v[80:83]\n"
"ds_write_addtid_b32 v78 offset:11392\n"
"global_load_dwordx4 v[72:75], v6, s[56:57] offset:0 sc1\n"
"v_mfma_f32_16x16x32_bf16 v[80:83], v[48:51], a[44:47], v[80:83]\n"
"ds_write_addtid_b32 v79 offset:16032\n"
"v_add_f32 v58, v58, v110\n"
"v_mfma_f32_16x16x32_bf16 v[80:83], v[52:55], a[48:51], v[80:83]\n"
"v_add_f32 v59, v59, v111\n"
"v_add_f32 v56, v56, v96\n"
"v_mfma_f32_16x16x32_bf16 v[80:83], v[88:91], a[52:55], v[80:83]\n"
"v_add_f32 v57, v57, v97\n"
"v_add_f32 v58, v58, v98\n"
"v_mfma_f32_16x16x32_bf16 v[104:107], v[84:87], a[56:59], 0\n"
"v_add_f32 v59, v59, v99\n"
"v_mfma_f32_16x16x32_bf16 v[104:107], v[36:39], a[60:63], v[104:107]\n"
"ds_write_addtid_b32 v80 offset:2304\n"
"global_load_dwordx4 v[76:79], v6, s[56:57] offset:1024 sc1\n"
"v_mfma_f32_16x16x32_bf16 v[104:107], v[40:43], a[64:67], v[104:107]\n"
"s_add_u32 s93, s50, 0xffd00000\n"
"s_and_b32 s95, s93, 0xfbffffff\n"
"ds_write_addtid_b32 v81 offset:6944\n"
"v_mfma_f32_16x16x32_bf16 v[104:107], v[44:47], a[68:71], v[104:107]\n"
"ds_write_addtid_b32 v82 offset:11648\n"
"v_cvt_pk_bf16_f32 v96, v56, v57\n"
"v_mfma_f32_16x16x32_bf16 v[104:107], v[48:51], a[72:75], v[104:107]\n"
"ds_write_addtid_b32 v83 offset:16288\n"
"v_cvt_pk_bf16_f32 v97, v58, v59\n"
"v_mfma_f32_16x16x32_bf16 v[104:107], v[52:55], a[76:79], v[104:107]\n"
"s_waitcnt lgkmcnt(8)\n"
"v_add_f32 v112, v112, v116\n"
"v_add_f32 v113, v113, v117\n"
"v_mfma_f32_16x16x32_bf16 v[104:107], v[88:91], a[80:83], v[104:107]\n"
"global_load_dwordx4 v[80:83], v6, s[56:57] offset:2048 sc1\n"
"v_add_f32 v114, v114, v118\n"
"v_mfma_f32_16x16x32_bf16 v[56:59], v[84:87], a[84:87], 0\n"
"v_add_f32 v115, v115, v119\n"
"v_add_f32 v112, v112, v120\n"
"v_mfma_f32_16x16x32_bf16 v[56:59], v[36:39], a[88:91], v[56:59]\n"
"ds_write_addtid_b32 v104 offset:2560\n"
"v_add_f32 v113, v113, v121\n"
"v_mfma_f32_16x16x32_bf16 v[56:59], v[40:43], a[92:95], v[56:59]\n"
"ds_write_addtid_b32 v105 offset:7200\n"
"v_add_f32 v114, v114, v122\n"
"v_mfma_f32_16x16x32_bf16 v[56:59], v[44:47], a[96:99], v[56:59]\n"
"ds_write_addtid_b32 v106 offset:11904\n"
"v_add_f32 v115, v115, v123\n"
"v_mfma_f32_16x16x32_bf16 v[56:59], v[48:51], a[100:103], v[56:59]\n"
"ds_write_addtid_b32 v107 offset:16544\n"
"v_add_f32 v112, v112, v100\n"
"v_mfma_f32_16x16x32_bf16 v[56:59], v[52:55], a[104:107], v[56:59]\n"
"v_add_f32 v113, v113, v101\n"
"v_add_f32 v114, v114, v102\n"
"v_mfma_f32_16x16x32_bf16 v[56:59], v[88:91], a[108:111], v[56:59]\n"
"v_add_f32 v115, v115, v103\n"
"v_cvt_pk_bf16_f32 v98, v112, v113\n"
"v_mfma_f32_16x16x32_bf16 v[104:107], v[84:87], a[112:115], 0\n"
"v_cvt_pk_bf16_f32 v99, v114, v115\n"
"v_mfma_f32_16x16x32_bf16 v[104:107], v[36:39], a[116:119], v[104:107]\n"
"ds_write_addtid_b32 v56 offset:2816\n"
"v_mfma_f32_16x16x32_bf16 v[104:107], v[40:43], a[120:123], v[104:107]\n"
"ds_write_addtid_b32 v57 offset:7456\n"
"v_mfma_f32_16x16x32_bf16 v[104:107], v[44:47], a[124:127], v[104:107]\n"
"ds_write_addtid_b32 v58 offset:12160\n"
"v_mfma_f32_16x16x32_bf16 v[104:107], v[48:51], a[128:131], v[104:107]\n"
"ds_write_addtid_b32 v59 offset:16800\n"
"v_mfma_f32_16x16x32_bf16 v[104:107], v[52:55], a[132:135], v[104:107]\n"
"v_mfma_f32_16x16x32_bf16 v[104:107], v[88:91], a[136:139], v[104:107]\n"
"v_mfma_f32_16x16x32_bf16 v[100:103], v[84:87], a[140:143], 0\n"
"v_mfma_f32_16x16x32_bf16 v[100:103], v[36:39], a[144:147], v[100:103]\n"
"buffer_store_dwordx4 v[96:99], v8, s[40:43], s95 offen sc1\n"
"ds_write_addtid_b32 v104 offset:3072\n"
"v_mfma_f32_16x16x32_bf16 v[100:103], v[40:43], a[148:151], v[100:103]\n"
"ds_write_addtid_b32 v105 offset:7712\n"
"v_mfma_f32_16x16x32_bf16 v[100:103], v[44:47], a[152:155], v[100:103]\n"
"ds_write_addtid_b32 v106 offset:12416\n"
"v_mfma_f32_16x16x32_bf16 v[100:103], v[48:51], a[156:159], v[100:103]\n"
"ds_write_addtid_b32 v107 offset:17056\n"
"v_mfma_f32_16x16x32_bf16 v[100:103], v[52:55], a[160:163], v[100:103]\n"
"v_mfma_f32_16x16x32_bf16 v[100:103], v[88:91], a[164:167], v[100:103]\n"
"v_mfma_f32_16x16x32_bf16 v[56:59], v[84:87], a[224:227], 0\n"
"v_mfma_f32_16x16x32_bf16 v[56:59], v[36:39], a[228:231], v[56:59]\n"
"ds_write_addtid_b32 v100 offset:3328\n"
"v_mfma_f32_16x16x32_bf16 v[56:59], v[40:43], a[232:235], v[56:59]\n"
"ds_write_addtid_b32 v101 offset:7968\n"
"v_mfma_f32_16x16x32_bf16 v[56:59], v[44:47], a[236:239], v[56:59]\n"
"ds_write_addtid_b32 v102 offset:12672\n"
"v_mfma_f32_16x16x32_bf16 v[56:59], v[48:51], a[240:243], v[56:59]\n"
"ds_write_addtid_b32 v103 offset:17312\n"
"v_mfma_f32_16x16x32_bf16 v[56:59], v[52:55], a[244:247], v[56:59]\n"
"v_mfma_f32_16x16x32_bf16 v[56:59], v[88:91], a[248:251], v[56:59]\n"
"v_mfma_f32_16x16x32_bf16 v[104:107], v[84:87], a[252:255], 0\n"
"v_mfma_f32_16x16x32_bf16 v[104:107], v[36:39], v[12:15], v[104:107]\n"
"s_add_u32 s58, s50, 0x180000\n"
"ds_write_addtid_b32 v56 offset:3584\n"
"v_mfma_f32_16x16x32_bf16 v[104:107], v[40:43], v[16:19], v[104:107]\n"
"s_and_b32 s58, s58, 0x3ffffff\n"
"ds_write_addtid_b32 v57 offset:8224\n"
"v_mfma_f32_16x16x32_bf16 v[104:107], v[44:47], v[20:23], v[104:107]\n"
"s_add_u32 s56, s40, s58\n"
"ds_write_addtid_b32 v58 offset:12928\n"
"v_mfma_f32_16x16x32_bf16 v[104:107], v[48:51], v[24:27], v[104:107]\n"
"s_addc_u32 s57, s41, 0\n"
"ds_write_addtid_b32 v59 offset:17568\n"
"v_mfma_f32_16x16x32_bf16 v[104:107], v[52:55], v[28:31], v[104:107]\n"
"s_waitcnt vmcnt(5)\n"
"v_cmp_ne_u64 vcc, s[48:49], v[124:125]\n"
"v_mfma_f32_16x16x32_bf16 v[104:107], v[88:91], v[32:35], v[104:107]\n"
"v_mfma_f32_16x16x32_bf16 v[108:111], a[168:171], v[84:87], 0\n"
"s_cbranch_vccnz .LG_2s\n"
".LG_2r:\n"
"v_mfma_f32_16x16x32_bf16 v[108:111], a[172:175], v[36:39], v[108:111]\n"
"s_add_u32 s50, s50, 0x180000\n"
"ds_write_addtid_b32 v104 offset:3840\n"
"v_mfma_f32_16x16x32_bf16 v[108:111], a[176:179], v[40:43], v[108:111]\n"
"ds_write_addtid_b32 v105 offset:8480\n"
"v_mfma_f32_16x16x32_bf16 v[108:111], a[180:183], v[44:47], v[108:111]\n"
"ds_write_addtid_b32 v106 offset:13184\n"
"v_mfma_f32_16x16x32_bf16 v[108:111], a[184:187], v[48:51], v[108:111]\n"
"ds_write_addtid_b32 v107 offset:17824\n"
"v_mfma_f32_16x16x32_bf16 v[108:111], a[188:191], v[52:55], v[108:111]\n"
"global_load_dwordx4 v[96:99], v5, s[56:57] offset:0 sc1\n"
"v_mfma_f32_16x16x32_bf16 v[108:111], a[192:195], v[88:91], v[108:111]\n"
"v_mfma_f32_16x16x32_bf16 v[104:107], a[196:199], v[84:87], 0\n"
"v_mfma_f32_16x16x32_bf16 v[104:107], a[200:203], v[36:39], v[104:107]\n"
"s_waitcnt lgkmcnt(0)\n"
"global_load_dwordx4 v[100:103], v5, s[56:57] offset:1024 sc1\n"
"v_mfma_f32_16x16x32_bf16 v[104:107], a[204:207], v[40:43], v[104:107]\n"
"s_barrier\n"
"v_mfma_f32_16x16x32_bf16 v[104:107], a[208:211], v[44:47], v[104:107]\n"
"ds_read_b128 v[112:115], v2 offset:0\n"
"ds_read_b128 v[56:59], v3 offset:2560\n"
"v_mfma_f32_16x16x32_bf16 v[104:107], a[212:215], v[48:51], v[104:107]\n"
"ds_read_b128 v[84:87], v4 offset:1024\n"
"ds_read_b128 v[116:119], v2 offset:256\n"
"v_mfma_f32_16x16x32_bf16 v[104:107], a[216:219], v[52:55], v[104:107]\n"
"ds_read_b128 v[120:123], v3 offset:2816\n"
"ds_read_b128 v[124:127], v4 offset:1280\n"
"v_mfma_f32_16x16x32_bf16 v[104:107], a[220:223], v[88:91], v[104:107]\n"
"s_branch .LG_top\n"
".LG_1s:\n"
"s_cmp_ge_u32 s50, s53\n"
"s_cbranch_scc1 .LG_end\n"
"s_cmp_ge_u32 s50, s52\n"
"s_cbranch_scc1 .LG_1r\n"
"s_sub_u32 s60, s50, 0x0\n"
"s_mov_b32 s61, 200000\n"
"s_nop 7\n"
".LG_1p:\n"
"buffer_load_dwordx2 v[120:121], v7, s[40:43], s60 offen sc1\n"
"s_waitcnt vmcnt(0)\n"
"v_cmp_ne_u64 vcc, s[48:49], v[120:121]\n"
"s_cbranch_vccz .LG_1px\n"
"s_sleep 1\n"
"s_sub_u32 s61, s61, 1\n"
"s_cbranch_scc0 .LG_1p\n"
"s_branch .LG_end\n"
".LG_1px:\n"
"s_branch .LG_1r\n"
".LG_2s:\n"
"s_cmp_ge_u32 s50, s53\n"
"s_cbranch_scc1 .LG_end\n"
"s_cmp_ge_u32 s50, s52\n"
"s_cbranch_scc1 .LG_2r\n"
"s_sub_u32 s60, s50, 0x0\n"
"s_mov_b32 s61, 200000\n"
"s_nop 7\n"
".LG_2p:\n"
"buffer_load_dwordx2 v[124:125], v7, s[40:43], s60 offen sc1\n"
"s_waitcnt vmcnt(0)\n"
"v_cmp_ne_u64 vcc, s[48:49], v[124:125]\n"
"s_cbranch_vccz .LG_2px\n"
"s_sleep 1\n"
"s_sub_u32 s61, s61, 1\n"
"s_cbranch_scc0 .LG_2p\n"
"s_branch .LG_end\n"
".LG_2px:\n"
"s_branch .LG_2r\n"
".LG_3s:\n"
"s_cmp_ge_u32 s50, s53\n"
"s_cbranch_scc1 .LG_end\n"
"s_cmp_ge_u32 s50, s52\n"
"s_cbranch_scc1 .LG_3r\n"
"s_sub_u32 s60, s50, 0x0\n"
"s_mov_b32 s61, 200000\n"
"s_nop 7\n"
".LG_3p:\n"
"buffer_load_dwordx2 v[128:129], v7, s[40:43], s60 offen sc1\n"
"s_waitcnt vmcnt(0)\n"
"v_cmp_ne_u64 vcc, s[48:49], v[128:129]\n"
"s_cbranch_vccz .LG_3px\n"
"s_sleep 1\n"
"s_sub_u32 s61, s61, 1\n"
"s_cbranch_scc0 .LG_3p\n"
"s_branch .LG_end\n"
".LG_3px:\n"
"s_branch .LG_3r\n"
".LG_4s:\n"
"s_cmp_ge_u32 s50, s53\n"
"s_cbranch_scc1 .LG_end\n"
"s_cmp_ge_u32 s50, s52\n"
"s_cbranch_scc1 .LG_4r\n"
"s_sub_u32 s60, s50, 0x0\n"
"s_mov_b32 s61, 200000\n"
"s_nop 7\n"
".LG_4p:\n"
"buffer_load_dwordx2 v[160:161], v7, s[40:43], s60 offen sc1\n"
"s_waitcnt vmcnt(0)\n"
"v_cmp_ne_u64 vcc, s[48:49], v[160:161]\n"
"s_cbranch_vccz .LG_4px\n"
"s_sleep 1\n"
"s_sub_u32 s61, s61, 1\n"
"s_cbranch_scc0 .LG_4p\n"
"s_branch .LG_end\n"
".LG_4px:\n"
"s_branch .LG_4r\n"
".LG_end:\n"
"s_waitcnt vmcnt(0) lgkmcnt(0)\n"
"s_endpgm\n"
      :: "{v2}"((unsigned)(lds0 + ((wid + 3) & 3) * 18592 + trp)), "{v3}"((unsigned)(lds0 + ((wid + 2) & 3) * 18592 + trp)), "{v4}"((unsigned)(lds0 + ((wid + 1) & 3) * 18592 + trp)), "{v5}"((unsigned)(lane * 16 + f0 * 1024 + 134316032)), "{v6}"((unsigned)(lane * 16 + f0 * 1024 + 134320128)), "{v7}"((unsigned)((f0 + lane % 7) * 8 + 353024 + G_GL * 1572864)), "{v8}"((unsigned)(lane * 16 + pair * 4096 + kq * 1024 + 212992)), "{v9}"((unsigned)(lane ? DROP : pair * 32 + kq * 8 + 354048)), "{v10}"((unsigned)(lane * 16 + (pg0 + 4) * 4096 + kq * 1024 + 212992)), "{v11}"((unsigned)(lane > 1 ? DROP : (lane ? pg0 + 4 : pair) * 32 + kq * 8 + 354048)), "{v36}"((unsigned)(vT + tsw)), "{v37}"((unsigned)(vT + (tsw ^ 8))), "{v38}"((unsigned)(vD)), "{s[40:43]}"(rS), "{s[44:47]}"(rW), "{s[48:49]}"(wsp), "{s50}"((unsigned)(sA)), "{s51}"((unsigned)(sLo)), "{s52}"((unsigned)(sHi)), "{s53}"((unsigned)(sEx)), "{s54}"((unsigned)(sEnt)), "{s55}"((unsigned)(sLd)), "{s[56:57]}"(wsb + (sLo & 0x3ffffff)), "{s59}"((unsigned)(lds0 + wid * 18592)), "{s62}"((unsigned)(sW0 + 64 * ((wid + 1) & 3))), "{s63}"((unsigned)(sW0 + 64 * ((wid + 2) & 3))), "{s64}"((unsigned)(sW0 + 64 * ((wid + 3) & 3))), "{s65}"((unsigned)(sW0 + 64 * ((wid + 4) & 3))), "{s66}"((unsigned)(sW0 + 256))
      : "v255", "a255", "s95", "vcc", "scc", "memory");

    } else {
      const int v16 = kq * 4 + wid;
      const unsigned sEx = sHi + (J_EX + (set == 1)) * 3 * RS, sEnt = grp - 4;
      const u32x4 rO = desc(a.out, 2048 * 2048), bia0 = *(const u32x4*)(a.b2 + v16 * (32 << JJ) + lg * 8), bia1 = *(const u32x4*)(a.b2 + (JJ ? 32 : 512) + v16 * (32 << JJ) + lg * 8);
      const unsigned se = lg == (li >> 2) ? 0x3f80u << (16 * (li & 1)) : 0u, sel0 = (li & 2) ? 0u : se, sel1 = (li & 2) ? se : 0u;
      const u64 ep = (set == 2 && idx == 27 && wid == 0) ? (u64)a.ws : 0ull;
    asm volatile(
"s_nop 4\n"
"v_mov_b32 v186, 0\n"
"global_load_dwordx2 v[0:1], v186, s[48:49] sc1\n"
"v_mov_b32 v188, 0x3f803f80\n"
"v_mov_b32 v189, 0x3f803f80\n"
"v_mov_b32 v190, 0x3f803f80\n"
"v_mov_b32 v191, 0x3f803f80\n"
"s_add_u32 m0, s55, 0\n"
"s_add_u32 s60, s86, 0\n"
"buffer_load_dwordx4 v38, s[44:47], s60 offen lds\n"
"s_add_u32 m0, s55, 1024\n"
"s_add_u32 s60, s86, 32768\n"
"buffer_load_dwordx4 v38, s[44:47], s60 offen lds\n"
"s_add_u32 m0, s55, 2048\n"
"s_add_u32 s60, s87, 0\n"
"buffer_load_dwordx4 v38, s[44:47], s60 offen lds\n"
"s_add_u32 m0, s55, 3072\n"
"s_add_u32 s60, s87, 32768\n"
"buffer_load_dwordx4 v38, s[44:47], s60 offen lds\n"
"s_add_u32 m0, s55, 4096\n"
"s_add_u32 s60, s88, 0\n"
"buffer_load_dwordx4 v38, s[44:47], s60 offen lds\n"
"s_add_u32 m0, s55, 5120\n"
"s_add_u32 s60, s88, 32768\n"
"buffer_load_dwordx4 v38, s[44:47], s60 offen lds\n"
"s_add_u32 m0, s55, 6144\n"
"s_add_u32 s60, s89, 0\n"
"buffer_load_dwordx4 v38, s[44:47], s60 offen lds\n"
"s_add_u32 m0, s55, 7168\n"
"s_add_u32 s60, s89, 32768\n"
"buffer_load_dwordx4 v38, s[44:47], s60 offen lds\n"
"s_add_u32 m0, s55, 8192\n"
"s_add_u32 s60, s86, 65536\n"
"buffer_load_dwordx4 v38, s[44:47], s60 offen lds\n"
"s_add_u32 m0, s55, 9216\n"
"s_add_u32 s60, s86, 98304\n"
"buffer_load_dwordx4 v38, s[44:47], s60 offen lds\n"
"s_add_u32 m0, s55, 10240\n"
"s_add_u32 s60, s87, 65536\n"
"buffer_load_dwordx4 v38, s[44:47], s60 offen lds\n"
"s_add_u32 m0, s55, 11264\n"
"s_add_u32 s60, s87, 98304\n"
"buffer_load_dwordx4 v38, s[44:47], s60 offen lds\n"
"s_add_u32 m0, s55, 12288\n"
"s_add_u32 s60, s88, 65536\n"
"buffer_load_dwordx4 v38, s[44:47], s60 offen lds\n"
"s_add_u32 m0, s55, 13312\n"
"s_add_u32 s60, s88, 98304\n"
"buffer_load_dwordx4 v38, s[44:47], s60 offen lds\n"
"s_add_u32 m0, s55, 14336\n"
"s_add_u32 s60, s89, 65536\n"
"buffer_load_dwordx4 v38, s[44:47], s60 offen lds\n"
"s_add_u32 m0, s55, 15360\n"
"s_add_u32 s60, s89, 98304\n"
"buffer_load_dwordx4 v38, s[44:47], s60 offen lds\n"
"s_add_u32 m0, s55, 16384\n"
"s_add_u32 s60, s86, 131072\n"
"buffer_load_dwordx4 v38, s[44:47], s60 offen lds\n"
"s_add_u32 m0, s55, 17408\n"
"s_add_u32 s60, s86, 163840\n"
"buffer_load_dwordx4 v38, s[44:47], s60 offen lds\n"
"s_add_u32 m0, s55, 18432\n"
"s_add_u32 s60, s87, 131072\n"
"buffer_load_dwordx4 v38, s[44:47], s60 offen lds\n"
"s_add_u32 m0, s55, 19456\n"
"s_add_u32 s60, s87, 163840\n"
"buffer_load_dwordx4 v38, s[44:47], s60 offen lds\n"
"s_add_u32 m0, s55, 20480\n"
"s_add_u32 s60, s88, 131072\n"
"buffer_load_dwordx4 v38, s[44:47], s60 offen lds\n"
"s_add_u32 m0, s55, 21504\n"
"s_add_u32 s60, s88, 163840\n"
"buffer_load_dwordx4 v38, s[44:47], s60 offen lds\n"
"s_add_u32 m0, s55, 22528\n"
"s_add_u32 s60, s89, 131072\n"
"buffer_load_dwordx4 v38, s[44:47], s60 offen lds\n"
"s_add_u32 m0, s55, 23552\n"
"s_add_u32 s60, s89, 163840\n"
"buffer_load_dwordx4 v38, s[44:47], s60 offen lds\n"
"s_add_u32 m0, s55, 24576\n"
"s_add_u32 s60, s86, 196608\n"
"buffer_load_dwordx4 v38, s[44:47], s60 offen lds\n"
"s_add_u32 m0, s55, 25600\n"
"s_add_u32 s60, s86, 229376\n"
"buffer_load_dwordx4 v38, s[44:47], s60 offen lds\n"
"s_add_u32 m0, s55, 26624\n"
"s_add_u32 s60, s87, 196608\n"
"buffer_load_dwordx4 v38, s[44:47], s60 offen lds\n"
"s_add_u32 m0, s55, 27648\n"
"s_add_u32 s60, s87, 229376\n"
"buffer_load_dwordx4 v38, s[44:47], s60 offen lds\n"
"s_add_u32 m0, s55, 28672\n"
"s_add_u32 s60, s88, 196608\n"
"buffer_load_dwordx4 v38, s[44:47], s60 offen lds\n"
"s_add_u32 m0, s55, 29696\n"
"s_add_u32 s60, s88, 229376\n"
"buffer_load_dwordx4 v38, s[44:47], s60 offen lds\n"
"s_add_u32 m0, s55, 30720\n"
"s_add_u32 s60, s89, 196608\n"
"buffer_load_dwordx4 v38, s[44:47], s60 offen lds\n"
"s_add_u32 m0, s55, 31744\n"
"s_add_u32 s60, s89, 229376\n"
"buffer_load_dwordx4 v38, s[44:47], s60 offen lds\n"
"s_add_u32 m0, s55, 32768\n"
"s_add_u32 s60, s86, 262144\n"
"buffer_load_dwordx4 v38, s[44:47], s60 offen lds\n"
"s_add_u32 m0, s55, 33792\n"
"s_add_u32 s60, s86, 294912\n"
"buffer_load_dwordx4 v38, s[44:47], s60 offen lds\n"
"s_add_u32 m0, s55, 34816\n"
"s_add_u32 s60, s87, 262144\n"
"buffer_load_dwordx4 v38, s[44:47], s60 offen lds\n"
"s_add_u32 m0, s55, 35840\n"
"s_add_u32 s60, s87, 294912\n"
"buffer_load_dwordx4 v38, s[44:47], s60 offen lds\n"
"s_add_u32 m0, s55, 36864\n"
"s_add_u32 s60, s88, 262144\n"
"buffer_load_dwordx4 v38, s[44:47], s60 offen lds\n"
"s_add_u32 m0, s55, 37888\n"
"s_add_u32 s60, s88, 294912\n"
"buffer_load_dwordx4 v38, s[44:47], s60 offen lds\n"
"s_add_u32 m0, s55, 38912\n"
"s_add_u32 s60, s89, 262144\n"
"buffer_load_dwordx4 v38, s[44:47], s60 offen lds\n"
"s_add_u32 m0, s55, 39936\n"
"s_add_u32 s60, s89, 294912\n"
"buffer_load_dwordx4 v38, s[44:47], s60 offen lds\n"
"s_waitcnt vmcnt(38)\n"
"ds_read_b64_tr_b16 a[0:1], v36 offset:0\n"
"ds_read_b64_tr_b16 a[2:3], v36 offset:256\n"
"ds_read_b64_tr_b16 a[28:29], v37 offset:0\n"
"ds_read_b64_tr_b16 a[30:31], v37 offset:256\n"
"v_add_u32 v0, 2, v0\n"
"v_add_u32 v1, 0x9e3779b9, v1\n"
"v_or_b32 v0, 1, v0\n"
"v_readfirstlane_b32 s49, v1\n"
"v_readfirstlane_b32 s48, v0\n"
"s_waitcnt vmcnt(36)\n"
"ds_read_b64_tr_b16 a[56:57], v36 offset:2048\n"
"ds_read_b64_tr_b16 a[58:59], v36 offset:2304\n"
"ds_read_b64_tr_b16 a[84:85], v37 offset:2048\n"
"ds_read_b64_tr_b16 a[86:87], v37 offset:2304\n"
"s_waitcnt lgkmcnt(4)\n"
"s_add_u32 m0, s55, 0\n"
"s_add_u32 s60, s86, 327680\n"
"buffer_load_dwordx4 v38, s[44:47], s60 offen lds\n"
"s_add_u32 m0, s55, 1024\n"
"s_add_u32 s60, s86, 360448\n"
"buffer_load_dwordx4 v38, s[44:47], s60 offen lds\n"
"s_waitcnt vmcnt(36)\n"
"ds_read_b64_tr_b16 a[112:113], v36 offset:4096\n"
"ds_read_b64_tr_b16 a[114:115], v36 offset:4352\n"
"ds_read_b64_tr_b16 a[140:141], v37 offset:4096\n"
"ds_read_b64_tr_b16 a[142:143], v37 offset:4352\n"
"s_waitcnt lgkmcnt(4)\n"
"s_add_u32 m0, s55, 2048\n"
"s_add_u32 s60, s87, 327680\n"
"buffer_load_dwordx4 v38, s[44:47], s60 offen lds\n"
"s_add_u32 m0, s55, 3072\n"
"s_add_u32 s60, s87, 360448\n"
"buffer_load_dwordx4 v38, s[44:47], s60 offen lds\n"
"s_waitcnt vmcnt(36)\n"
"ds_read_b64_tr_b16 a[168:169], v36 offset:6144\n"
"ds_read_b64_tr_b16 a[170:171], v36 offset:6400\n"
"ds_read_b64_tr_b16 a[196:197], v37 offset:6144\n"
"ds_read_b64_tr_b16 a[198:199], v37 offset:6400\n"
"s_waitcnt lgkmcnt(4)\n"
"s_add_u32 m0, s55, 4096\n"
"s_add_u32 s60, s88, 327680\n"
"buffer_load_dwordx4 v38, s[44:47], s60 offen lds\n"
"s_add_u32 m0, s55, 5120\n"
"s_add_u32 s60, s88, 360448\n"
"buffer_load_dwordx4 v38, s[44:47], s60 offen lds\n"
"s_waitcnt vmcnt(36)\n"
"ds_read_b64_tr_b16 a[4:5], v36 offset:8192\n"
"ds_read_b64_tr_b16 a[6:7], v36 offset:8448\n"
"ds_read_b64_tr_b16 a[32:33], v37 offset:8192\n"
"ds_read_b64_tr_b16 a[34:35], v37 offset:8448\n"
"s_waitcnt lgkmcnt(4)\n"
"s_add_u32 m0, s55, 6144\n"
"s_add_u32 s60, s89, 327680\n"
"buffer_load_dwordx4 v38, s[44:47], s60 offen lds\n"
"s_add_u32 m0, s55, 7168\n"
"s_add_u32 s60, s89, 360448\n"
"buffer_load_dwordx4 v38, s[44:47], s60 offen lds\n"
"s_waitcnt vmcnt(36)\n"
"ds_read_b64_tr_b16 a[60:61], v36 offset:10240\n"
"ds_read_b64_tr_b16 a[62:63], v36 offset:10496\n"
"ds_read_b64_tr_b16 a[88:89], v37 offset:10240\n"
"ds_read_b64_tr_b16 a[90:91], v37 offset:10496\n"
"s_waitcnt lgkmcnt(4)\n"
"s_add_u32 m0, s55, 8192\n"
"s_add_u32 s60, s86, 393216\n"
"buffer_load_dwordx4 v38, s[44:47], s60 offen lds\n"
"s_add_u32 m0, s55, 9216\n"
"s_add_u32 s60, s86, 425984\n"
"buffer_load_dwordx4 v38, s[44:47], s60 offen lds\n"
"s_waitcnt vmcnt(36)\n"
"ds_read_b64_tr_b16 a[116:117], v36 offset:12288\n"
"ds_read_b64_tr_b16 a[118:119], v36 offset:12544\n"
"ds_read_b64_tr_b16 a[144:145], v37 offset:12288\n"
"ds_read_b64_tr_b16 a[146:147], v37 offset:12544\n"
"s_waitcnt lgkmcnt(4)\n"
"s_add_u32 m0, s55, 10240\n"
"s_add_u32 s60, s87, 393216\n"
"buffer_load_dwordx4 v38, s[44:47], s60 offen lds\n"
"s_add_u32 m0, s55, 11264\n"
"s_add_u32 s60, s87, 425984\n"
"buffer_load_dwordx4 v38, s[44:47], s60 offen lds\n"
"s_waitcnt vmcnt(36)\n"
"ds_read_b64_tr_b16 a[172:173], v36 offset:14336\n"
"ds_read_b64_tr_b16 a[174:175], v36 offset:14592\n"
"ds_read_b64_tr_b16 a[200:201], v37 offset:14336\n"
"ds_read_b64_tr_b16 a[202:203], v37 offset:14592\n"
"s_waitcnt lgkmcnt(4)\n"
"s_add_u32 m0, s55, 12288\n"
"s_add_u32 s60, s88, 393216\n"
"buffer_load_dwordx4 v38, s[44:47], s60 offen lds\n"
"s_add_u32 m0, s55, 13312\n"
"s_add_u32 s60, s88, 425984\n"
"buffer_load_dwordx4 v38, s[44:47], s60 offen lds\n"
"s_waitcnt vmcnt(36)\n"
"ds_read_b64_tr_b16 a[8:9], v36 offset:16384\n"
"ds_read_b64_tr_b16 a[10:11], v36 offset:16640\n"
"ds_read_b64_tr_b16 a[36:37], v37 offset:16384\n"
"ds_read_b64_tr_b16 a[38:39], v37 offset:16640\n"
"s_waitcnt lgkmcnt(4)\n"
"s_add_u32 m0, s55, 14336\n"
"s_add_u32 s60, s89, 393216\n"
"buffer_load_dwordx4 v38, s[44:47], s60 offen lds\n"
"s_add_u32 m0, s55, 15360\n"
"s_add_u32 s60, s89, 425984\n"
"buffer_load_dwordx4 v38, s[44:47], s60 offen lds\n"
"s_waitcnt vmcnt(36)\n"
"ds_read_b64_tr_b16 a[64:65], v36 offset:18432\n"
"ds_read_b64_tr_b16 a[66:67], v36 offset:18688\n"
"ds_read_b64_tr_b16 a[92:93], v37 offset:18432\n"
"ds_read_b64_tr_b16 a[94:95], v37 offset:18688\n"
"s_waitcnt lgkmcnt(4)\n"
"s_waitcnt vmcnt(34)\n"
"ds_read_b64_tr_b16 a[120:121], v36 offset:20480\n"
"ds_read_b64_tr_b16 a[122:123], v36 offset:20736\n"
"ds_read_b64_tr_b16 a[148:149], v37 offset:20480\n"
"ds_read_b64_tr_b16 a[150:151], v37 offset:20736\n"
"s_waitcnt lgkmcnt(4)\n"
"s_waitcnt vmcnt(32)\n"
"ds_read_b64_tr_b16 a[176:177], v36 offset:22528\n"
"ds_read_b64_tr_b16 a[178:179], v36 offset:22784\n"
"ds_read_b64_tr_b16 a[204:205], v37 offset:22528\n"
"ds_read_b64_tr_b16 a[206:207], v37 offset:22784\n"
"s_waitcnt lgkmcnt(4)\n"
"s_waitcnt vmcnt(30)\n"
"ds_read_b64_tr_b16 a[12:13], v36 offset:24576\n"
"ds_read_b64_tr_b16 a[14:15], v36 offset:24832\n"
"ds_read_b64_tr_b16 a[40:41], v37 offset:24576\n"
"ds_read_b64_tr_b16 a[42:43], v37 offset:24832\n"
"s_waitcnt lgkmcnt(4)\n"
"s_waitcnt vmcnt(28)\n"
"ds_read_b64_tr_b16 a[68:69], v36 offset:26624\n"
"ds_read_b64_tr_b16 a[70:71], v36 offset:26880\n"
"ds_read_b64_tr_b16 a[96:97], v37 offset:26624\n"
"ds_read_b64_tr_b16 a[98:99], v37 offset:26880\n"
"s_waitcnt lgkmcnt(4)\n"
"s_waitcnt vmcnt(26)\n"
"ds_read_b64_tr_b16 a[124:125], v36 offset:28672\n"
"ds_read_b64_tr_b16 a[126:127], v36 offset:28928\n"
"ds_read_b64_tr_b16 a[152:153], v37 offset:28672\n"
"ds_read_b64_tr_b16 a[154:155], v37 offset:28928\n"
"s_waitcnt lgkmcnt(4)\n"
"s_waitcnt vmcnt(24)\n"
"ds_read_b64_tr_b16 a[180:181], v36 offset:30720\n"
"ds_read_b64_tr_b16 a[182:183], v36 offset:30976\n"
"ds_read_b64_tr_b16 a[208:209], v37 offset:30720\n"
"ds_read_b64_tr_b16 a[210:211], v37 offset:30976\n"
"s_waitcnt lgkmcnt(4)\n"
"s_waitcnt vmcnt(22)\n"
"ds_read_b64_tr_b16 a[16:17], v36 offset:32768\n"
"ds_read_b64_tr_b16 a[18:19], v36 offset:33024\n"
"ds_read_b64_tr_b16 a[44:45], v37 offset:32768\n"
"ds_read_b64_tr_b16 a[46:47], v37 offset:33024\n"
"s_waitcnt lgkmcnt(4)\n"
"s_waitcnt vmcnt(20)\n"
"ds_read_b64_tr_b16 a[72:73], v36 offset:34816\n"
"ds_read_b64_tr_b16 a[74:75], v36 offset:35072\n"
"ds_read_b64_tr_b16 a[100:101], v37 offset:34816\n"
"ds_read_b64_tr_b16 a[102:103], v37 offset:35072\n"
"s_waitcnt lgkmcnt(4)\n"
"s_waitcnt vmcnt(18)\n"
"ds_read_b64_tr_b16 a[128:129], v36 offset:36864\n"
"ds_read_b64_tr_b16 a[130:131], v36 offset:37120\n"
"ds_read_b64_tr_b16 a[156:157], v37 offset:36864\n"
"ds_read_b64_tr_b16 a[158:159], v37 offset:37120\n"
"s_waitcnt lgkmcnt(4)\n"
"s_waitcnt vmcnt(16)\n"
"ds_read_b64_tr_b16 a[184:185], v36 offset:38912\n"
"ds_read_b64_tr_b16 a[186:187], v36 offset:39168\n"
"ds_read_b64_tr_b16 a[212:213], v37 offset:38912\n"
"ds_read_b64_tr_b16 a[214:215], v37 offset:39168\n"
"s_waitcnt lgkmcnt(4)\n"
"s_waitcnt vmcnt(14)\n"
"ds_read_b64_tr_b16 a[20:21], v36 offset:0\n"
"ds_read_b64_tr_b16 a[22:23], v36 offset:256\n"
"ds_read_b64_tr_b16 a[48:49], v37 offset:0\n"
"ds_read_b64_tr_b16 a[50:51], v37 offset:256\n"
"s_waitcnt lgkmcnt(4)\n"
"s_waitcnt vmcnt(12)\n"
"ds_read_b64_tr_b16 a[76:77], v36 offset:2048\n"
"ds_read_b64_tr_b16 a[78:79], v36 offset:2304\n"
"ds_read_b64_tr_b16 a[104:105], v37 offset:2048\n"
"ds_read_b64_tr_b16 a[106:107], v37 offset:2304\n"
"s_waitcnt lgkmcnt(4)\n"
"s_waitcnt vmcnt(10)\n"
"ds_read_b64_tr_b16 a[132:133], v36 offset:4096\n"
"ds_read_b64_tr_b16 a[134:135], v36 offset:4352\n"
"ds_read_b64_tr_b16 a[160:161], v37 offset:4096\n"
"ds_read_b64_tr_b16 a[162:163], v37 offset:4352\n"
"s_waitcnt lgkmcnt(4)\n"
"s_waitcnt vmcnt(8)\n"
"ds_read_b64_tr_b16 a[188:189], v36 offset:6144\n"
"ds_read_b64_tr_b16 a[190:191], v36 offset:6400\n"
"ds_read_b64_tr_b16 a[216:217], v37 offset:6144\n"
"ds_read_b64_tr_b16 a[218:219], v37 offset:6400\n"
"s_waitcnt lgkmcnt(4)\n"
"s_waitcnt vmcnt(6)\n"
"ds_read_b64_tr_b16 a[24:25], v36 offset:8192\n"
"ds_read_b64_tr_b16 a[26:27], v36 offset:8448\n"
"ds_read_b64_tr_b16 a[52:53], v37 offset:8192\n"
"ds_read_b64_tr_b16 a[54:55], v37 offset:8448\n"
"s_waitcnt lgkmcnt(4)\n"
"s_waitcnt vmcnt(4)\n"
"ds_read_b64_tr_b16 a[80:81], v36 offset:10240\n"
"ds_read_b64_tr_b16 a[82:83], v36 offset:10496\n"
"ds_read_b64_tr_b16 a[108:109], v37 offset:10240\n"
"ds_read_b64_tr_b16 a[110:111], v37 offset:10496\n"
"s_waitcnt lgkmcnt(4)\n"
"s_waitcnt vmcnt(2)\n"
"ds_read_b64_tr_b16 a[136:137], v36 offset:12288\n"
"ds_read_b64_tr_b16 a[138:139], v36 offset:12544\n"
"ds_read_b64_tr_b16 a[164:165], v37 offset:12288\n"
"ds_read_b64_tr_b16 a[166:167], v37 offset:12544\n"
"s_waitcnt lgkmcnt(4)\n"
"s_waitcnt vmcnt(0)\n"
"ds_read_b64_tr_b16 a[192:193], v36 offset:14336\n"
"ds_read_b64_tr_b16 a[194:195], v36 offset:14592\n"
"ds_read_b64_tr_b16 a[220:221], v37 offset:14336\n"
"ds_read_b64_tr_b16 a[222:223], v37 offset:14592\n"
"s_waitcnt lgkmcnt(4)\n"
"s_waitcnt vmcnt(0)\n"
"s_waitcnt lgkmcnt(0)\n"
"s_mov_b32 m0, s59\n"
"s_barrier\n"
"s_mov_b64 s[84:85], s[48:49]\n"
"v_mov_b32 v136, 0\n"
"v_mov_b32 v137, 0\n"
"v_mov_b32 v152, 0\n"
"v_mov_b32 v153, 0\n"
"v_mov_b32 v156, 0\n"
"v_mov_b32 v157, 0\n"
"s_mov_b32 s29, 0x80000000\n"
"s_mov_b32 s30, 0x80000000\n"
"s_mov_b32 s28, 0x80000000\n"
"s_mov_b32 s31, 0x80000000\n"
"s_mov_b32 s31, 0x80000000\n"
"s_mov_b32 s30, 0x80000000\n"
"s_mov_b32 s90, 0x80000000\n"
"v_mov_b32 v84, 0\n"
"v_mov_b32 v85, 0\n"
"s_mov_b32 s29, 0x80000000\n"
"s_mov_b32 s31, 0x80000000\n"
"s_mov_b32 s30, 0x80000000\n"
"s_mov_b32 s91, 0x80000000\n"
"s_cmp_eq_u32 s54, 0\n"
"s_cbranch_scc1 .LJ_1s\n"
"s_cmp_eq_u32 s54, 1\n"
"s_cbranch_scc1 .LJ_2s\n"
"s_cmp_eq_u32 s54, 2\n"
"s_cbranch_scc1 .LJ_3s\n"
".p2align 6\n"
".LJ_top:\n"
"v_mfma_f32_16x16x32_bf16 v[36:39], v[88:91], a[0:3], 0\n"
"s_cmp_lt_u32 s90, 0x8000000\n"
"s_cselect_b32 s90, 0x80000000, s90\n"
"v_mfma_f32_16x16x32_bf16 v[36:39], v[92:95], a[4:7], v[36:39]\n"
"s_waitcnt vmcnt(15)\n"
"global_load_dwordx4 a[224:227], v6, s[56:57] offset:1024 sc1\n"
"v_mfma_f32_16x16x32_bf16 v[36:39], v[96:99], a[8:11], v[36:39]\n"
"s_waitcnt lgkmcnt(6)\n"
"buffer_load_dwordx2 v[84:85], v10, s[40:43], s90 offen offset:0 sc1\n"
"v_mfma_f32_16x16x32_bf16 v[36:39], v[100:103], a[12:15], v[36:39]\n"
"s_waitcnt vmcnt(13)\n"
"buffer_store_dwordx4 v[40:43], v13, s[76:79], s29 offen sc1\n"
"v_mfma_f32_16x16x32_bf16 v[36:39], v[104:107], a[16:19], v[36:39]\n"
"s_waitcnt lgkmcnt(3)\n"
"v_add_f32 v52, v52, v48\n"
"v_add_f32 v53, v53, v49\n"
"v_mfma_f32_16x16x32_bf16 v[36:39], v[108:111], a[20:23], v[36:39]\n"
"s_waitcnt vmcnt(13)\n"
"v_add_f32 v54, v54, v50\n"
"v_add_f32 v55, v55, v51\n"
"v_mfma_f32_16x16x32_bf16 v[36:39], v[112:115], a[24:27], v[36:39]\n"
"global_load_dwordx4 a[228:231], v6, s[56:57] offset:2048 sc1\n"
"v_add_f32 v52, v52, v56\n"
"v_mfma_f32_16x16x32_bf16 v[40:43], v[88:91], a[28:31], 0\n"
"v_add_f32 v53, v53, v57\n"
"v_add_f32 v54, v54, v58\n"
"v_mfma_f32_16x16x32_bf16 v[40:43], v[92:95], a[32:35], v[40:43]\n"
"ds_write_addtid_b32 v36 offset:0\n"
"v_add_f32 v55, v55, v59\n"
"v_mfma_f32_16x16x32_bf16 v[40:43], v[96:99], a[36:39], v[40:43]\n"
"ds_write_addtid_b32 v37 offset:4640\n"
"v_add_f32 v52, v52, v44\n"
"v_mfma_f32_16x16x32_bf16 v[40:43], v[100:103], a[40:43], v[40:43]\n"
"ds_write_addtid_b32 v38 offset:9344\n"
"v_add_f32 v53, v53, v45\n"
"v_mfma_f32_16x16x32_bf16 v[40:43], v[104:107], a[44:47], v[40:43]\n"
"ds_write_addtid_b32 v39 offset:13984\n"
"v_add_f32 v54, v54, v46\n"
"v_mfma_f32_16x16x32_bf16 v[40:43], v[108:111], a[48:51], v[40:43]\n"
"v_add_f32 v55, v55, v47\n"
"v_mfma_f32_16x16x32_bf16 v[40:43], v[112:115], a[52:55], v[40:43]\n"
"s_add_u32 s31, s50, 0xffd00000\n"
"s_and_b32 s30, s31, 0xfbffffff\n"
"v_cvt_pk_bf16_f32 v36, v52, v53\n"
"v_mfma_f32_16x16x32_bf16 v[44:47], v[88:91], a[56:59], 0\n"
"s_waitcnt vmcnt(10)\n"
"v_cvt_pk_bf16_f32 v37, v54, v55\n"
"v_mfma_f32_16x16x32_bf16 v[48:51], v[32:35], v[60:63], 0\n"
"v_mfma_f32_16x16x32_bf16 v[48:51], v[32:35], v[68:71], v[48:51]\n"
"v_mfma_f32_16x16x32_bf16 v[48:51], v[32:35], v[76:79], v[48:51]\n"
"v_mfma_f32_16x16x32_bf16 v[52:55], v[32:35], v[64:67], 0\n"
"v_mfma_f32_16x16x32_bf16 v[52:55], v[32:35], v[72:75], v[52:55]\n"
"v_mfma_f32_16x16x32_bf16 v[52:55], v[32:35], v[80:83], v[52:55]\n"
"v_cvt_pk_bf16_f32 v56, v48, v49\n"
"v_cvt_pk_bf16_f32 v57, v50, v51\n"
"v_mfma_f32_16x16x32_bf16 v[44:47], v[92:95], a[60:63], v[44:47]\n"
"s_waitcnt lgkmcnt(4)\n"
"ds_write_addtid_b32 v40 offset:256\n"
"v_add_f32 v144, v144, v148\n"
"v_mfma_f32_16x16x32_bf16 v[44:47], v[96:99], a[64:67], v[44:47]\n"
"ds_write_addtid_b32 v41 offset:4896\n"
"v_add_f32 v145, v145, v149\n"
"v_mfma_f32_16x16x32_bf16 v[44:47], v[100:103], a[68:71], v[44:47]\n"
"ds_write_addtid_b32 v42 offset:9600\n"
"v_add_f32 v146, v146, v150\n"
"v_mfma_f32_16x16x32_bf16 v[44:47], v[104:107], a[72:75], v[44:47]\n"
"ds_write_addtid_b32 v43 offset:14240\n"
"v_add_f32 v147, v147, v151\n"
"v_mfma_f32_16x16x32_bf16 v[44:47], v[108:111], a[76:79], v[44:47]\n"
"buffer_store_dwordx2 v[0:1], v9, s[40:43], s28 offen offset:0 sc1\n"
"v_add_f32 v144, v144, v152\n"
"v_mfma_f32_16x16x32_bf16 v[44:47], v[112:115], a[80:83], v[44:47]\n"
"v_add_f32 v145, v145, v153\n"
"v_add_f32 v146, v146, v154\n"
"v_mfma_f32_16x16x32_bf16 v[40:43], v[88:91], a[84:87], 0\n"
"v_add_f32 v147, v147, v155\n"
"v_add_f32 v144, v144, v140\n"
"v_mfma_f32_16x16x32_bf16 v[40:43], v[92:95], a[88:91], v[40:43]\n"
"s_add_u32 s58, s50, 0x180000\n"
"ds_write_addtid_b32 v44 offset:512\n"
"v_add_f32 v145, v145, v141\n"
"v_mfma_f32_16x16x32_bf16 v[40:43], v[96:99], a[92:95], v[40:43]\n"
"s_and_b32 s58, s58, 0x3ffffff\n"
"ds_write_addtid_b32 v45 offset:5152\n"
"v_add_f32 v146, v146, v142\n"
"v_mfma_f32_16x16x32_bf16 v[40:43], v[100:103], a[96:99], v[40:43]\n"
"s_add_u32 s56, s40, s58\n"
"ds_write_addtid_b32 v46 offset:9856\n"
"v_add_f32 v147, v147, v143\n"
"v_mfma_f32_16x16x32_bf16 v[40:43], v[104:107], a[100:103], v[40:43]\n"
"s_addc_u32 s57, s41, 0\n"
"ds_write_addtid_b32 v47 offset:14496\n"
"v_cvt_pk_bf16_f32 v38, v144, v145\n"
"v_mfma_f32_16x16x32_bf16 v[40:43], v[108:111], a[104:107], v[40:43]\n"
"s_waitcnt vmcnt(9)\n"
"v_cmp_ne_u64 vcc, s[48:49], v[156:157]\n"
"v_cvt_pk_bf16_f32 v39, v146, v147\n"
"v_mfma_f32_16x16x32_bf16 v[40:43], v[112:115], a[108:111], v[40:43]\n"
"buffer_store_dwordx4 v[36:39], v8, s[40:43], s30 offen sc1\n"
"v_cvt_pk_bf16_f32 v58, v52, v53\n"
"v_cvt_pk_bf16_f32 v59, v54, v55\n"
"v_mfma_f32_16x16x32_bf16 v[44:47], v[136:139], v[136:139], 0\n"
"v_mfma_f32_16x16x32_bf16 v[36:39], v[28:31], v[136:139], 0\n"
"v_mfma_f32_16x16x32_bf16 v[44:47], v[56:59], v[56:59], v[44:47]\n"
"v_mfma_f32_16x16x32_bf16 v[36:39], v[28:31], v[56:59], v[36:39]\n"
"v_mfma_f32_16x16x32_bf16 v[48:51], v[88:91], a[112:115], 0\n"
"s_cbranch_vccnz .LJ_3s\n"
".LJ_3r:\n"
"v_cndmask_b32_e64 v44, v44, v45, s[64:65]\n"
"v_mfma_f32_16x16x32_bf16 v[48:51], v[92:95], a[116:119], v[48:51]\n"
"s_add_u32 s50, s50, 0x180000\n"
"ds_write_addtid_b32 v40 offset:768\n"
"v_cndmask_b32_e64 v46, v46, v47, s[64:65]\n"
"v_mfma_f32_16x16x32_bf16 v[48:51], v[96:99], a[120:123], v[48:51]\n"
"ds_write_addtid_b32 v41 offset:5408\n"
"v_cndmask_b32_e64 v37, v44, v46, s[66:67]\n"
"v_mfma_f32_16x16x32_bf16 v[48:51], v[100:103], a[124:127], v[48:51]\n"
"buffer_store_dwordx2 v[36:37], v18, s[40:43], s91 offen offset:0 sc1\n"
"ds_write_addtid_b32 v42 offset:10112\n"
"v_mfma_f32_16x16x32_bf16 v[48:51], v[104:107], a[128:131], v[48:51]\n"
"ds_write_addtid_b32 v43 offset:14752\n"
"global_load_dwordx4 a[232:235], v5, s[56:57] offset:0 sc1\n"
"v_mfma_f32_16x16x32_bf16 v[48:51], v[108:111], a[132:135], v[48:51]\n"
"ds_bpermute_b32 v36, v16, v56\n"
"v_mfma_f32_16x16x32_bf16 v[48:51], v[112:115], a[136:139], v[48:51]\n"
"buffer_load_dwordx2 v[136:137], v7, s[40:43], s50 offen sc1\n"
"v_mfma_f32_16x16x32_bf16 v[40:43], v[88:91], a[140:143], 0\n"
"ds_bpermute_b32 v37, v16, v57\n"
"v_mfma_f32_16x16x32_bf16 v[40:43], v[92:95], a[144:147], v[40:43]\n"
"ds_write_addtid_b32 v48 offset:1024\n"
"v_mfma_f32_16x16x32_bf16 v[40:43], v[96:99], a[148:151], v[40:43]\n"
"ds_write_addtid_b32 v49 offset:5664\n"
"global_load_dwordx4 a[236:239], v5, s[56:57] offset:1024 sc1\n"
"v_mfma_f32_16x16x32_bf16 v[40:43], v[100:103], a[152:155], v[40:43]\n"
"ds_write_addtid_b32 v50 offset:10368\n"
"v_mfma_f32_16x16x32_bf16 v[40:43], v[104:107], a[156:159], v[40:43]\n"
"ds_write_addtid_b32 v51 offset:15008\n"
"v_mfma_f32_16x16x32_bf16 v[40:43], v[108:111], a[160:163], v[40:43]\n"
"ds_bpermute_b32 v38, v16, v58\n"
"v_mfma_f32_16x16x32_bf16 v[40:43], v[112:115], a[164:167], v[40:43]\n"
"ds_bpermute_b32 v39, v16, v59\n"
"v_mfma_f32_16x16x32_bf16 v[60:63], a[168:171], v[88:91], 0\n"
"global_load_dwordx4 a[240:243], v5, s[56:57] offset:2048 sc1\n"
"v_mfma_f32_16x16x32_bf16 v[60:63], a[172:175], v[92:95], v[60:63]\n"
"ds_write_addtid_b32 v40 offset:1280\n"
"v_mfma_f32_16x16x32_bf16 v[60:63], a[176:179], v[96:99], v[60:63]\n"
"ds_write_addtid_b32 v41 offset:5920\n"
"v_mfma_f32_16x16x32_bf16 v[60:63], a[180:183], v[100:103], v[60:63]\n"
"ds_write_addtid_b32 v42 offset:10624\n"
"v_mfma_f32_16x16x32_bf16 v[60:63], a[184:187], v[104:107], v[60:63]\n"
"ds_write_addtid_b32 v43 offset:15264\n"
"v_mfma_f32_16x16x32_bf16 v[60:63], a[188:191], v[108:111], v[60:63]\n"
"s_waitcnt lgkmcnt(4)\n"
"buffer_store_dwordx4 v[36:39], v14, s[76:79], s29 offen sc1\n"
"global_load_dwordx4 a[244:247], v5, s[56:57] offset:3072 sc1\n"
"v_mfma_f32_16x16x32_bf16 v[60:63], a[192:195], v[112:115], v[60:63]\n"
"v_mfma_f32_16x16x32_bf16 v[64:67], a[196:199], v[88:91], 0\n"
"s_and_b32 s30, s90, 0x3ffffff\n"
"v_mfma_f32_16x16x32_bf16 v[64:67], a[200:203], v[92:95], v[64:67]\n"
"s_waitcnt lgkmcnt(0)\n"
"v_mfma_f32_16x16x32_bf16 v[64:67], a[204:207], v[96:99], v[64:67]\n"
"s_barrier\n"
"v_mfma_f32_16x16x32_bf16 v[64:67], a[208:211], v[100:103], v[64:67]\n"
"s_waitcnt vmcnt(11)\n"
"global_load_dwordx4 a[248:251], v6, s[56:57] offset:0 sc1\n"
"ds_read_b128 v[68:71], v2 offset:0\n"
"v_mfma_f32_16x16x32_bf16 v[64:67], a[212:215], v[104:107], v[64:67]\n"
"s_add_u32 s62, s40, s30\n"
"ds_read_b128 v[72:75], v3 offset:512\n"
"ds_read_b128 v[76:79], v4 offset:1024\n"
"v_mfma_f32_16x16x32_bf16 v[64:67], a[216:219], v[108:111], v[64:67]\n"
"s_addc_u32 s63, s41, 0\n"
"ds_read_b128 v[80:83], v2 offset:256\n"
"ds_read_b128 v[88:91], v3 offset:768\n"
"v_mfma_f32_16x16x32_bf16 v[64:67], a[220:223], v[112:115], v[64:67]\n"
"ds_read_b128 v[92:95], v4 offset:1280\n"
"v_mfma_f32_16x16x32_bf16 v[96:99], v[116:119], a[0:3], 0\n"
"v_cmp_ne_u64 vcc, s[84:85], v[84:85]\n"
"v_mfma_f32_16x16x32_bf16 v[96:99], v[120:123], a[4:7], v[96:99]\n"
"global_load_dwordx4 a[252:255], v6, s[56:57] offset:1024 sc1\n"
"v_mfma_f32_16x16x32_bf16 v[96:99], v[124:127], a[8:11], v[96:99]\n"
"s_cbranch_vccnz .LJ_4s\n"
".LJ_4r:\n"
"v_mfma_f32_16x16x32_bf16 v[96:99], v[128:131], a[12:15], v[96:99]\n"
"global_load_dwordx4 v[38:41], v11, s[62:63] offset:-4096 sc1\n"
"v_mfma_f32_16x16x32_bf16 v[96:99], v[132:135], a[16:19], v[96:99]\n"
"s_waitcnt lgkmcnt(3)\n"
"v_add_f32 v68, v68, v72\n"
"v_add_f32 v69, v69, v73\n"
"v_mfma_f32_16x16x32_bf16 v[96:99], a[224:227], a[20:23], v[96:99]\n"
"s_waitcnt vmcnt(12)\n"
"v_add_f32 v70, v70, v74\n"
"v_add_f32 v71, v71, v75\n"
"v_mfma_f32_16x16x32_bf16 v[96:99], a[228:231], a[24:27], v[96:99]\n"
"global_load_dwordx4 v[84:87], v6, s[56:57] offset:2048 sc1\n"
"v_add_f32 v68, v68, v76\n"
"v_mfma_f32_16x16x32_bf16 v[72:75], v[116:119], a[28:31], 0\n"
"v_add_f32 v69, v69, v77\n"
"v_add_f32 v70, v70, v78\n"
"v_mfma_f32_16x16x32_bf16 v[72:75], v[120:123], a[32:35], v[72:75]\n"
"ds_write_addtid_b32 v96 offset:1536\n"
"v_add_f32 v71, v71, v79\n"
"v_mfma_f32_16x16x32_bf16 v[72:75], v[124:127], a[36:39], v[72:75]\n"
"ds_write_addtid_b32 v97 offset:6176\n"
"v_add_f32 v68, v68, v60\n"
"v_mfma_f32_16x16x32_bf16 v[72:75], v[128:131], a[40:43], v[72:75]\n"
"ds_write_addtid_b32 v98 offset:10880\n"
"v_add_f32 v69, v69, v61\n"
"v_mfma_f32_16x16x32_bf16 v[72:75], v[132:135], a[44:47], v[72:75]\n"
"ds_write_addtid_b32 v99 offset:15520\n"
"v_add_f32 v70, v70, v62\n"
"v_mfma_f32_16x16x32_bf16 v[72:75], a[224:227], a[48:51], v[72:75]\n"
"global_load_dwordx4 v[42:45], v11, s[62:63] offset:-3072 sc1\n"
"v_add_f32 v71, v71, v63\n"
"v_mfma_f32_16x16x32_bf16 v[72:75], a[228:231], a[52:55], v[72:75]\n"
"s_add_u32 s29, s50, 0xffd00000\n"
"s_and_b32 s30, s29, 0xfbffffff\n"
"v_cvt_pk_bf16_f32 v76, v68, v69\n"
"v_mfma_f32_16x16x32_bf16 v[60:63], v[116:119], a[56:59], 0\n"
"v_cvt_pk_bf16_f32 v77, v70, v71\n"
"v_mfma_f32_16x16x32_bf16 v[60:63], v[120:123], a[60:63], v[60:63]\n"
"s_waitcnt lgkmcnt(4)\n"
"ds_write_addtid_b32 v72 offset:1792\n"
"v_add_f32 v80, v80, v88\n"
"v_mfma_f32_16x16x32_bf16 v[60:63], v[124:127], a[64:67], v[60:63]\n"
"ds_write_addtid_b32 v73 offset:6432\n"
"v_add_f32 v81, v81, v89\n"
"v_mfma_f32_16x16x32_bf16 v[60:63], v[128:131], a[68:71], v[60:63]\n"
"ds_write_addtid_b32 v74 offset:11136\n"
"v_add_f32 v82, v82, v90\n"
"v_mfma_f32_16x16x32_bf16 v[60:63], v[132:135], a[72:75], v[60:63]\n"
"ds_write_addtid_b32 v75 offset:15776\n"
"v_add_f32 v83, v83, v91\n"
"v_mfma_f32_16x16x32_bf16 v[60:63], a[224:227], a[76:79], v[60:63]\n"
"s_waitcnt vmcnt(12)\n"
"v_add_f32 v80, v80, v92\n"
"v_add_f32 v81, v81, v93\n"
"v_mfma_f32_16x16x32_bf16 v[60:63], a[228:231], a[80:83], v[60:63]\n"
"v_add_f32 v82, v82, v94\n"
"v_add_f32 v83, v83, v95\n"
"v_mfma_f32_16x16x32_bf16 v[68:71], v[116:119], a[84:87], 0\n"
"v_add_f32 v80, v80, v64\n"
"v_add_f32 v81, v81, v65\n"
"v_mfma_f32_16x16x32_bf16 v[68:71], v[120:123], a[88:91], v[68:71]\n"
"s_add_u32 s58, s50, 0x180000\n"
"ds_write_addtid_b32 v60 offset:2048\n"
"v_add_f32 v82, v82, v66\n"
"v_mfma_f32_16x16x32_bf16 v[68:71], v[124:127], a[92:95], v[68:71]\n"
"s_and_b32 s58, s58, 0x3ffffff\n"
"ds_write_addtid_b32 v61 offset:6688\n"
"v_add_f32 v83, v83, v67\n"
"v_mfma_f32_16x16x32_bf16 v[68:71], v[128:131], a[96:99], v[68:71]\n"
"s_add_u32 s56, s40, s58\n"
"ds_write_addtid_b32 v62 offset:11392\n"
"v_cvt_pk_bf16_f32 v78, v80, v81\n"
"v_mfma_f32_16x16x32_bf16 v[68:71], v[132:135], a[100:103], v[68:71]\n"
"s_addc_u32 s57, s41, 0\n"
"ds_write_addtid_b32 v63 offset:16032\n"
"v_cvt_pk_bf16_f32 v79, v82, v83\n"
"v_mfma_f32_16x16x32_bf16 v[68:71], a[224:227], a[104:107], v[68:71]\n"
"s_waitcnt vmcnt(9)\n"
"buffer_store_dwordx4 v[76:79], v8, s[40:43], s30 offen sc1\n"
"v_cmp_ne_u64 vcc, s[48:49], v[136:137]\n"
"v_mfma_f32_16x16x32_bf16 v[68:71], a[228:231], a[108:111], v[68:71]\n"
"buffer_store_dwordx2 v[0:1], v9, s[40:43], s31 offen offset:0 sc1\n"
"v_mfma_f32_16x16x32_bf16 v[60:63], v[116:119], a[112:115], 0\n"
"s_cbranch_vccnz .LJ_1s\n"
".LJ_1r:\n"
"v_mfma_f32_16x16x32_bf16 v[60:63], v[120:123], a[116:119], v[60:63]\n"
"s_add_u32 s50, s50, 0x180000\n"
"ds_write_addtid_b32 v68 offset:2304\n"
"global_load_dwordx4 v[46:49], v11, s[62:63] offset:-2048 sc1\n"
"v_mfma_f32_16x16x32_bf16 v[60:63], v[124:127], a[120:123], v[60:63]\n"
"ds_write_addtid_b32 v69 offset:6944\n"
"v_mfma_f32_16x16x32_bf16 v[60:63], v[128:131], a[124:127], v[60:63]\n"
"ds_write_addtid_b32 v70 offset:11648\n"
"v_mfma_f32_16x16x32_bf16 v[60:63], v[132:135], a[128:131], v[60:63]\n"
"ds_write_addtid_b32 v71 offset:16288\n"
"global_load_dwordx4 v[88:91], v5, s[56:57] offset:0 sc1\n"
"v_mfma_f32_16x16x32_bf16 v[60:63], a[224:227], a[132:135], v[60:63]\n"
"v_mfma_f32_16x16x32_bf16 v[60:63], a[228:231], a[136:139], v[60:63]\n"
"buffer_load_dwordx2 v[152:153], v7, s[40:43], s50 offen sc1\n"
"v_mfma_f32_16x16x32_bf16 v[104:107], v[116:119], a[140:143], 0\n"
"global_load_dwordx4 v[50:53], v11, s[62:63] offset:-1024 sc1\n"
"v_mfma_f32_16x16x32_bf16 v[104:107], v[120:123], a[144:147], v[104:107]\n"
"ds_write_addtid_b32 v60 offset:2560\n"
"v_mfma_f32_16x16x32_bf16 v[104:107], v[124:127], a[148:151], v[104:107]\n"
"ds_write_addtid_b32 v61 offset:7200\n"
"global_load_dwordx4 v[92:95], v5, s[56:57] offset:1024 sc1\n"
"v_mfma_f32_16x16x32_bf16 v[104:107], v[128:131], a[152:155], v[104:107]\n"
"ds_write_addtid_b32 v62 offset:11904\n"
"v_mfma_f32_16x16x32_bf16 v[104:107], v[132:135], a[156:159], v[104:107]\n"
"s_waitcnt vmcnt(13)\n"
"buffer_store_dwordx2 v[0:1], v19, s[40:43], s91 offen offset:0 sc1\n"
"ds_write_addtid_b32 v63 offset:16544\n"
"v_mfma_f32_16x16x32_bf16 v[104:107], a[224:227], a[160:163], v[104:107]\n"
"global_load_dwordx4 v[54:57], v12, s[62:63] offset:0 sc1\n"
"v_mfma_f32_16x16x32_bf16 v[104:107], a[228:231], a[164:167], v[104:107]\n"
"v_mfma_f32_16x16x32_bf16 v[136:139], a[168:171], v[116:119], 0\n"
"global_load_dwordx4 v[96:99], v5, s[56:57] offset:2048 sc1\n"
"v_mfma_f32_16x16x32_bf16 v[136:139], a[172:175], v[120:123], v[136:139]\n"
"ds_write_addtid_b32 v104 offset:2816\n"
"v_mfma_f32_16x16x32_bf16 v[136:139], a[176:179], v[124:127], v[136:139]\n"
"ds_write_addtid_b32 v105 offset:7456\n"
"v_mfma_f32_16x16x32_bf16 v[136:139], a[180:183], v[128:131], v[136:139]\n"
"ds_write_addtid_b32 v106 offset:12160\n"
"global_load_dwordx4 v[62:65], v11, s[62:63] offset:0 sc1\n"
"v_mfma_f32_16x16x32_bf16 v[136:139], a[184:187], v[132:135], v[136:139]\n"
"ds_write_addtid_b32 v107 offset:16800\n"
"v_mfma_f32_16x16x32_bf16 v[136:139], a[188:191], a[224:227], v[136:139]\n"
"global_load_dwordx4 v[100:103], v5, s[56:57] offset:3072 sc1\n"
"v_mfma_f32_16x16x32_bf16 v[136:139], a[192:195], a[228:231], v[136:139]\n"
"v_mfma_f32_16x16x32_bf16 v[140:143], a[196:199], v[116:119], 0\n"
"v_mfma_f32_16x16x32_bf16 v[140:143], a[200:203], v[120:123], v[140:143]\n"
"s_waitcnt lgkmcnt(0)\n"
"global_load_dwordx4 v[66:69], v11, s[62:63] offset:1024 sc1\n"
"v_mfma_f32_16x16x32_bf16 v[140:143], a[204:207], v[124:127], v[140:143]\n"
"s_barrier\n"
"v_mfma_f32_16x16x32_bf16 v[140:143], a[208:211], v[128:131], v[140:143]\n"
"global_load_dwordx4 v[104:107], v6, s[56:57] offset:0 sc1\n"
"ds_read_b128 v[116:119], v2 offset:1536\n"
"v_mfma_f32_16x16x32_bf16 v[140:143], a[212:215], v[132:135], v[140:143]\n"
"ds_read_b128 v[120:123], v3 offset:2048\n"
"ds_read_b128 v[124:127], v4 offset:2560\n"
"v_mfma_f32_16x16x32_bf16 v[140:143], a[216:219], a[224:227], v[140:143]\n"
"ds_read_b128 v[128:131], v2 offset:1792\n"
"ds_read_b128 v[132:135], v3 offset:2304\n"
"v_mfma_f32_16x16x32_bf16 v[140:143], a[220:223], a[228:231], v[140:143]\n"
"ds_read_b128 v[144:147], v4 offset:2816\n"
"global_load_dwordx4 v[70:73], v11, s[62:63] offset:2048 sc1\n"
"v_mfma_f32_16x16x32_bf16 v[148:151], a[232:235], a[0:3], 0\n"
"v_mfma_f32_16x16x32_bf16 v[148:151], a[236:239], a[4:7], v[148:151]\n"
"s_waitcnt vmcnt(20)\n"
"global_load_dwordx4 v[108:111], v6, s[56:57] offset:1024 sc1\n"
"v_mfma_f32_16x16x32_bf16 v[148:151], a[240:243], a[8:11], v[148:151]\n"
"v_mfma_f32_16x16x32_bf16 v[148:151], a[244:247], a[12:15], v[148:151]\n"
"s_waitcnt vmcnt(19)\n"
"v_mfma_f32_16x16x32_bf16 v[148:151], a[248:251], a[16:19], v[148:151]\n"
"s_waitcnt lgkmcnt(3)\n"
"v_add_f32 v116, v116, v120\n"
"v_add_f32 v117, v117, v121\n"
"v_mfma_f32_16x16x32_bf16 v[148:151], a[252:255], a[20:23], v[148:151]\n"
"s_waitcnt vmcnt(17)\n"
"v_add_f32 v118, v118, v122\n"
"v_add_f32 v119, v119, v123\n"
"v_mfma_f32_16x16x32_bf16 v[148:151], v[84:87], a[24:27], v[148:151]\n"
"global_load_dwordx4 v[112:115], v6, s[56:57] offset:2048 sc1\n"
"v_add_f32 v116, v116, v124\n"
"v_mfma_f32_16x16x32_bf16 v[120:123], a[232:235], a[28:31], 0\n"
"v_add_f32 v117, v117, v125\n"
"v_add_f32 v118, v118, v126\n"
"v_mfma_f32_16x16x32_bf16 v[120:123], a[236:239], a[32:35], v[120:123]\n"
"ds_write_addtid_b32 v148 offset:3072\n"
"v_add_f32 v119, v119, v127\n"
"v_mfma_f32_16x16x32_bf16 v[120:123], a[240:243], a[36:39], v[120:123]\n"
"ds_write_addtid_b32 v149 offset:7712\n"
"v_add_f32 v116, v116, v136\n"
"v_mfma_f32_16x16x32_bf16 v[120:123], a[244:247], a[40:43], v[120:123]\n"
"ds_write_addtid_b32 v150 offset:12416\n"
"v_add_f32 v117, v117, v137\n"
"v_mfma_f32_16x16x32_bf16 v[120:123], a[248:251], a[44:47], v[120:123]\n"
"ds_write_addtid_b32 v151 offset:17056\n"
"v_add_f32 v118, v118, v138\n"
"v_mfma_f32_16x16x32_bf16 v[120:123], a[252:255], a[48:51], v[120:123]\n"
"global_load_dwordx4 v[74:77], v11, s[62:63] offset:3072 sc1\n"
"v_add_f32 v119, v119, v139\n"
"v_mfma_f32_16x16x32_bf16 v[120:123], v[84:87], a[52:55], v[120:123]\n"
"s_add_u32 s28, s50, 0xffd00000\n"
"s_and_b32 s31, s28, 0xfbffffff\n"
"v_cvt_pk_bf16_f32 v124, v116, v117\n"
"v_mfma_f32_16x16x32_bf16 v[136:139], a[232:235], a[56:59], 0\n"
"v_cvt_pk_bf16_f32 v125, v118, v119\n"
"v_mfma_f32_16x16x32_bf16 v[136:139], a[236:239], a[60:63], v[136:139]\n"
"s_waitcnt lgkmcnt(4)\n"
"ds_write_addtid_b32 v120 offset:3328\n"
"v_add_f32 v128, v128, v132\n"
"v_mfma_f32_16x16x32_bf16 v[136:139], a[240:243], a[64:67], v[136:139]\n"
"s_mov_b32 s91, s90\n"
"ds_write_addtid_b32 v121 offset:7968\n"
"v_add_f32 v129, v129, v133\n"
"v_mfma_f32_16x16x32_bf16 v[136:139], a[244:247], a[68:71], v[136:139]\n"
"ds_write_addtid_b32 v122 offset:12672\n"
"v_add_f32 v130, v130, v134\n"
"v_mfma_f32_16x16x32_bf16 v[136:139], a[248:251], a[72:75], v[136:139]\n"
"s_waitcnt vmcnt(17)\n"
"buffer_store_dwordx2 v[0:1], v9, s[40:43], s29 offen offset:0 sc1\n"
"ds_write_addtid_b32 v123 offset:17312\n"
"v_mfma_f32_16x16x32_bf16 v[136:139], a[252:255], a[76:79], v[136:139]\n"
"v_add_f32 v131, v131, v135\n"
"v_add_f32 v128, v128, v144\n"
"v_mfma_f32_16x16x32_bf16 v[136:139], v[84:87], a[80:83], v[136:139]\n"
"v_add_f32 v129, v129, v145\n"
"v_add_f32 v130, v130, v146\n"
"v_mfma_f32_16x16x32_bf16 v[120:123], a[232:235], a[84:87], 0\n"
"v_add_f32 v131, v131, v147\n"
"v_add_f32 v128, v128, v140\n"
"v_mfma_f32_16x16x32_bf16 v[120:123], a[236:239], a[88:91], v[120:123]\n"
"s_add_u32 s58, s50, 0x180000\n"
"ds_write_addtid_b32 v136 offset:3584\n"
"v_add_f32 v129, v129, v141\n"
"v_mfma_f32_16x16x32_bf16 v[120:123], a[240:243], a[92:95], v[120:123]\n"
"s_and_b32 s58, s58, 0x3ffffff\n"
"ds_write_addtid_b32 v137 offset:8224\n"
"v_add_f32 v130, v130, v142\n"
"v_mfma_f32_16x16x32_bf16 v[120:123], a[244:247], a[96:99], v[120:123]\n"
"s_add_u32 s56, s40, s58\n"
"ds_write_addtid_b32 v138 offset:12928\n"
"v_add_f32 v131, v131, v143\n"
"v_mfma_f32_16x16x32_bf16 v[120:123], a[248:251], a[100:103], v[120:123]\n"
"s_addc_u32 s57, s41, 0\n"
"ds_write_addtid_b32 v139 offset:17568\n"
"v_cvt_pk_bf16_f32 v126, v128, v129\n"
"v_mfma_f32_16x16x32_bf16 v[120:123], a[252:255], a[104:107], v[120:123]\n"
"s_waitcnt vmcnt(14)\n"
"v_cmp_ne_u64 vcc, s[48:49], v[152:153]\n"
"v_cvt_pk_bf16_f32 v127, v130, v131\n"
"v_mfma_f32_16x16x32_bf16 v[120:123], v[84:87], a[108:111], v[120:123]\n"
"buffer_store_dwordx4 v[124:127], v8, s[40:43], s31 offen sc1\n"
"global_load_dwordx4 v[78:81], v12, s[62:63] offset:1024 sc1\n"
"v_mfma_f32_16x16x32_bf16 v[128:131], a[232:235], a[112:115], 0\n"
"s_cbranch_vccnz .LJ_2s\n"
".LJ_2r:\n"
"v_mov_b32 v36, v20\n"
"v_mfma_f32_16x16x32_bf16 v[128:131], a[236:239], a[116:119], v[128:131]\n"
"s_add_u32 s50, s50, 0x180000\n"
"ds_write_addtid_b32 v120 offset:3840\n"
"v_mov_b32 v37, v21\n"
"v_mfma_f32_16x16x32_bf16 v[128:131], a[240:243], a[120:123], v[128:131]\n"
"ds_write_addtid_b32 v121 offset:8480\n"
"v_mov_b32 v58, v22\n"
"v_mfma_f32_16x16x32_bf16 v[128:131], a[244:247], a[124:127], v[128:131]\n"
"ds_write_addtid_b32 v122 offset:13184\n"
"v_mov_b32 v59, v23\n"
"v_mfma_f32_16x16x32_bf16 v[128:131], a[248:251], a[128:131], v[128:131]\n"
"s_waitcnt vmcnt(12)\n"
"ds_write_addtid_b32 v123 offset:17824\n"
"global_load_dwordx4 v[116:119], v5, s[56:57] offset:0 sc1\n"
"v_mfma_f32_16x16x32_bf16 v[124:127], v[32:35], v[36:39], 0\n"
"s_cmp_ge_i32 s91, s80\n"
"s_cbranch_scc1 .LJ_ws\n"
".LJ_wr:\n"
"v_mfma_f32_16x16x32_bf16 v[124:127], v[32:35], v[44:47], v[124:127]\n"
"s_lshr_b32 s29, s91, s81\n"
"v_mfma_f32_16x16x32_bf16 v[124:127], v[32:35], v[52:55], v[124:127]\n"
"s_bitcmp1_b32 s29, 15\n"
"s_cselect_b32 s31, s83, s82\n"
"v_mfma_f32_16x16x32_bf16 v[120:123], v[32:35], v[40:43], 0\n"
"s_add_u32 s29, s29, s31\n"
"v_mfma_f32_16x16x32_bf16 v[120:123], v[32:35], v[48:51], v[120:123]\n"
"v_mfma_f32_16x16x32_bf16 v[120:123], v[32:35], v[56:59], v[120:123]\n"
"v_cvt_pk_bf16_f32 v136, v124, v125\n"
"v_cvt_pk_bf16_f32 v137, v126, v127\n"
"v_mfma_f32_16x16x32_bf16 v[128:131], a[252:255], a[132:135], v[128:131]\n"
"v_mfma_f32_16x16x32_bf16 v[128:131], v[84:87], a[136:139], v[128:131]\n"
"buffer_load_dwordx2 v[156:157], v7, s[40:43], s50 offen sc1\n"
"v_mfma_f32_16x16x32_bf16 v[36:39], a[232:235], a[140:143], 0\n"
"v_cvt_pk_bf16_f32 v138, v120, v121\n"
"v_cvt_pk_bf16_f32 v139, v122, v123\n"
"v_mfma_f32_16x16x32_bf16 v[36:39], a[236:239], a[144:147], v[36:39]\n"
"ds_write_addtid_b32 v128 offset:4096\n"
"v_mov_b32 v60, v24\n"
"v_mfma_f32_16x16x32_bf16 v[36:39], a[240:243], a[148:151], v[36:39]\n"
"ds_write_addtid_b32 v129 offset:8736\n"
"global_load_dwordx4 v[120:123], v5, s[56:57] offset:1024 sc1\n"
"v_mfma_f32_16x16x32_bf16 v[36:39], a[244:247], a[152:155], v[36:39]\n"
"ds_write_addtid_b32 v130 offset:13440\n"
"v_mov_b32 v61, v25\n"
"v_mfma_f32_16x16x32_bf16 v[36:39], a[248:251], a[156:159], v[36:39]\n"
"ds_write_addtid_b32 v131 offset:18080\n"
"v_mov_b32 v82, v26\n"
"v_mfma_f32_16x16x32_bf16 v[36:39], a[252:255], a[160:163], v[36:39]\n"
"ds_bpermute_b32 v40, v16, v136\n"
"v_mfma_f32_16x16x32_bf16 v[36:39], v[84:87], a[164:167], v[36:39]\n"
"ds_bpermute_b32 v41, v16, v137\n"
"v_mfma_f32_16x16x32_bf16 v[44:47], a[168:171], a[232:235], 0\n"
"global_load_dwordx4 v[124:127], v5, s[56:57] offset:2048 sc1\n"
"v_mov_b32 v83, v27\n"
"v_mfma_f32_16x16x32_bf16 v[44:47], a[172:175], a[236:239], v[44:47]\n"
"ds_write_addtid_b32 v36 offset:4352\n"
"v_mfma_f32_16x16x32_bf16 v[44:47], a[176:179], a[240:243], v[44:47]\n"
"ds_write_addtid_b32 v37 offset:8992\n"
"v_mfma_f32_16x16x32_bf16 v[44:47], a[180:183], a[244:247], v[44:47]\n"
"ds_write_addtid_b32 v38 offset:13696\n"
"v_mfma_f32_16x16x32_bf16 v[44:47], a[184:187], a[248:251], v[44:47]\n"
"ds_write_addtid_b32 v39 offset:18336\n"
"v_mfma_f32_16x16x32_bf16 v[44:47], a[188:191], a[252:255], v[44:47]\n"
"global_load_dwordx4 v[128:131], v5, s[56:57] offset:3072 sc1\n"
"v_mfma_f32_16x16x32_bf16 v[44:47], a[192:195], v[84:87], v[44:47]\n"
"ds_bpermute_b32 v42, v16, v138\n"
"v_mfma_f32_16x16x32_bf16 v[140:143], a[196:199], a[232:235], 0\n"
"ds_bpermute_b32 v43, v16, v139\n"
"v_mfma_f32_16x16x32_bf16 v[140:143], a[200:203], a[236:239], v[140:143]\n"
"s_waitcnt lgkmcnt(2)\n"
"v_mfma_f32_16x16x32_bf16 v[140:143], a[204:207], a[240:243], v[140:143]\n"
"s_barrier\n"
"v_mfma_f32_16x16x32_bf16 v[140:143], a[208:211], a[244:247], v[140:143]\n"
"global_load_dwordx4 v[132:135], v6, s[56:57] offset:0 sc1\n"
"ds_read_b128 v[52:55], v2 offset:3072\n"
"v_mfma_f32_16x16x32_bf16 v[140:143], a[212:215], a[248:251], v[140:143]\n"
"ds_read_b128 v[48:51], v3 offset:3584\n"
"ds_read_b128 v[56:59], v4 offset:4096\n"
"v_mfma_f32_16x16x32_bf16 v[140:143], a[216:219], a[252:255], v[140:143]\n"
"s_add_u32 s90, s50, 0xff980000\n"
"ds_read_b128 v[144:147], v2 offset:3328\n"
"ds_read_b128 v[148:151], v3 offset:3840\n"
"v_mfma_f32_16x16x32_bf16 v[140:143], a[220:223], v[84:87], v[140:143]\n"
"ds_read_b128 v[152:155], v4 offset:4352\n"
"s_branch .LJ_top\n"
".LJ_1s:\n"
"s_cmp_ge_u32 s50, s53\n"
"s_cbranch_scc1 .LJ_end\n"
"s_cmp_ge_u32 s50, s52\n"
"s_cbranch_scc1 .LJ_1r\n"
"s_sub_u32 s60, s50, 0x0\n"
"s_mov_b32 s61, 200000\n"
"s_nop 7\n"
".LJ_1p:\n"
"buffer_load_dwordx2 v[136:137], v7, s[40:43], s60 offen sc1\n"
"s_waitcnt vmcnt(0)\n"
"v_cmp_ne_u64 vcc, s[48:49], v[136:137]\n"
"s_cbranch_vccz .LJ_1px\n"
"s_sleep 1\n"
"s_sub_u32 s61, s61, 1\n"
"s_cbranch_scc0 .LJ_1p\n"
"s_branch .LJ_end\n"
".LJ_1px:\n"
"s_branch .LJ_1r\n"
".LJ_2s:\n"
"s_cmp_ge_u32 s50, s53\n"
"s_cbranch_scc1 .LJ_end\n"
"s_cmp_ge_u32 s50, s52\n"
"s_cbranch_scc1 .LJ_2r\n"
"s_sub_u32 s60, s50, 0x0\n"
"s_mov_b32 s61, 200000\n"
"s_nop 7\n"
".LJ_2p:\n"
"buffer_load_dwordx2 v[152:153], v7, s[40:43], s60 offen sc1\n"
"s_waitcnt vmcnt(0)\n"
"v_cmp_ne_u64 vcc, s[48:49], v[152:153]\n"
"s_cbranch_vccz .LJ_2px\n"
"s_sleep 1\n"
"s_sub_u32 s61, s61, 1\n"
"s_cbranch_scc0 .LJ_2p\n"
"s_branch .LJ_end\n"
".LJ_2px:\n"
"s_branch .LJ_2r\n"
".LJ_3s:\n"
"s_cmp_ge_u32 s50, s53\n"
"s_cbranch_scc1 .LJ_end\n"
"s_cmp_ge_u32 s50, s52\n"
"s_cbranch_scc1 .LJ_3r\n"
"s_sub_u32 s60, s50, 0x0\n"
"s_mov_b32 s61, 200000\n"
"s_nop 7\n"
".LJ_3p:\n"
"buffer_load_dwordx2 v[156:157], v7, s[40:43], s60 offen sc1\n"
"s_waitcnt vmcnt(0)\n"
"v_cmp_ne_u64 vcc, s[48:49], v[156:157]\n"
"s_cbranch_vccz .LJ_3px\n"
"s_sleep 1\n"
"s_sub_u32 s61, s61, 1\n"
"s_cbranch_scc0 .LJ_3p\n"
"s_branch .LJ_end\n"
".LJ_3px:\n"
"s_branch .LJ_3r\n"
".LJ_4s:\n"
"s_cmp_ge_i32 s90, 0x10000000\n"
"s_cselect_b64 s[84:85], 0, s[84:85]\n"
"s_cmp_ge_u32 s90, 0x10000000\n"
"s_cbranch_scc1 .LJ_4r\n"
"s_mov_b32 s61, 200000\n"
"s_nop 7\n"
".LJ_4p:\n"
"buffer_load_dwordx2 v[84:85], v10, s[40:43], s90 offen offset:0 sc1\n"
"s_waitcnt vmcnt(0)\n"
"v_cmp_ne_u64 vcc, s[84:85], v[84:85]\n"
"s_cbranch_vccz .LJ_4px\n"
"s_sleep 1\n"
"s_sub_u32 s61, s61, 1\n"
"s_cbranch_scc0 .LJ_4p\n"
"s_branch .LJ_end\n"
".LJ_4px:\n"
"s_branch .LJ_4r\n"
".LJ_ws:\n"
"s_mov_b64 s[76:77], s[68:69]\n"
"s_mov_b32 s78, s70\n"
"s_nop 7\n"
"v_mov_b32 v13, v15\n"
"v_add_u32 v14, 64, v15\n"
"v_mov_b32 v16, v17\n"
"s_mov_b32 s80, 0x7fffffff\n"
"s_mov_b32 s81, 4\n"
"s_mov_b32 s82, 0xff400000\n"
"s_mov_b32 s83, 0xff3f8800\n"
"s_branch .LJ_wr\n"
".LJ_end:\n"
"s_cmp_lt_u32 s50, s53\n"
"s_cbranch_scc1 .LJ_ne\n"
"s_cmp_eq_u64 s[72:73], 0\n"
"s_cbranch_scc1 .LJ_ne\n"
"v_mov_b32 v7, 0\n"
"global_store_dwordx2 v7, v[0:1], s[72:73] sc1\n"
".LJ_ne:\n"
"s_waitcnt vmcnt(0) lgkmcnt(0)\n"
"s_endpgm\n"
      :: "{v2}"((unsigned)(lds0 + ((wid + 3) & 3) * 18592 + trp)), "{v3}"((unsigned)(lds0 + ((wid + 2) & 3) * 18592 + trp)), "{v4}"((unsigned)(lds0 + ((wid + 1) & 3) * 18592 + trp)), "{v5}"((unsigned)(lane * 16 + f0 * 1024 + 134316032)), "{v6}"((unsigned)(lane * 16 + f0 * 1024 + 134320128)), "{v7}"((unsigned)((f0 + lane % 7) * 8 + 353024 + J_GL * 1572864)), "{v8}"((unsigned)(lane * 16 + pair * 4096 + kq * 1024 + 212992)), "{v9}"((unsigned)(lane ? DROP : pair * 32 + kq * 8 + 354048)), "{v10}"((unsigned)(v16 * 64 + (lane & 7) * 8 + 354048)), "{v11}"((unsigned)(lane * 16 + v16 * 8192 + 134434816)), "{v12}"((unsigned)(lane * 16 + v16 * 2048 + 134283264)), "{v13}"((unsigned)(lane * 16 + v16 * 2048 + 0)), "{v14}"((unsigned)(lane * 16 + v16 * 2048 + 1024)), "{v15}"((unsigned)(((lane >> 4) + (lane & 12)) * 4096 + (lane & 3) * 16 + v16 * 128)), "{v16}"((unsigned)(lane * 4)), "{v17}"((unsigned)((lane & 3) * 64 + (lane & 60))), "{v18}"((unsigned)(lg == (li >> 2) ? v16 * 128 + li * 8 + 67457024 : DROP)), "{v19}"((unsigned)(lane ? DROP : v16 * 8 + 67463936)), "{v20}"((unsigned)(bia0[0])), "{v21}"((unsigned)(bia0[1])), "{v22}"((unsigned)(bia0[2])), "{v23}"((unsigned)(bia0[3])), "{v24}"((unsigned)(bia1[0])), "{v25}"((unsigned)(bia1[1])), "{v26}"((unsigned)(bia1[2])), "{v27}"((unsigned)(bia1[3])), "{v28}"((unsigned)(0x3f803f80)), "{v29}"((unsigned)(0x3f803f80)), "{v30}"((unsigned)(0x3f803f80)), "{v31}"((unsigned)(0x3f803f80)), "{v32}"((unsigned)(sel0)), "{v33}"((unsigned)(sel1)), "{v34}"((unsigned)(sel0)), "{v35}"((unsigned)(sel1)), "{v36}"((unsigned)(vT + tsw)), "{v37}"((unsigned)(vT + (tsw ^ 8))), "{v38}"((unsigned)(vD)), "{s[40:43]}"(rS), "{s[44:47]}"(rW), "{s[48:49]}"(wsp), "{s50}"((unsigned)(sA)), "{s51}"((unsigned)(sLo)), "{s52}"((unsigned)(sHi)), "{s53}"((unsigned)(sEx)), "{s54}"((unsigned)(sEnt)), "{s55}"((unsigned)(sLd)), "{s[56:57]}"(wsb + (sLo & 0x3ffffff)), "{s59}"((unsigned)(lds0 + wid * 18592)), "{s[62:63]}"(wsb), "{s[64:65]}"(0xAAAAAAAAAAAAAAAAull), "{s[66:67]}"(0xCCCCCCCCCCCCCCCCull), "{s[68:71]}"(rO), "{s[72:73]}"(ep), "{s[76:79]}"(rS), "{s80}"((unsigned)(201326592)), "{s81}"((unsigned)(0)), "{s82}"((unsigned)(0)), "{s83}"((unsigned)(0)), "{s86}"((unsigned)(sW0 + 64 * ((wid + 1) & 3))), "{s87}"((unsigned)(sW0 + 64 * ((wid + 2) & 3))), "{s88}"((unsigned)(sW0 + 64 * ((wid + 3) & 3))), "{s89}"((unsigned)(sW0 + 64 * ((wid + 4) & 3)))
      : "v255", "a255", "s95", "vcc", "scc", "memory");

    }
  }
}

extern "C" void kernel_launch(void* const* d_in, const int* in_sizes, int n_in, void* d_out, int out_size,
                              void* d_ws, size_t ws_size, hipStream_t stream) {
  Args m{};
  m.x = (const u16*)d_in[0]; m.wqkv = (const u16*)d_in[1]; m.wo = (const u16*)d_in[2]; m.w1 = (const u16*)d_in[4];
  m.b1 = (const u16*)d_in[5]; m.w2 = (const u16*)d_in[6]; m.b2 = (const u16*)d_in[7];
  m.out = (u16*)d_out; m.ws = (char*)d_ws;
  (void)hipFuncSetAttribute((const void*)stream_kernel, hipFuncAttributeMaxDynamicSharedMemorySize, LDS_BYTES);
  hipLaunchKernelGGL(stream_kernel, dim3(256), dim3(256), LDS_BYTES, stream, m);
}
```

```cpp
#include <hip/hip_runtime.h>
#include <stdint.h>
typedef unsigned short u16;
typedef unsigned long long u64;
typedef __attribute__((ext_vector_type(4))) unsigned u32x4;
constexpr unsigned F_X = 0;
constexpr unsigned F_ATT = 32768;
constexpr unsigned F_X1 = 65536;
constexpr unsigned F_H = 98304;
constexpr unsigned F_P = 212992;
constexpr unsigned F_ST1 = 344064;
constexpr unsigned F_ST2 = 348160;
constexpr unsigned F_G = 352256;
constexpr unsigned F_FA = 352512;
constexpr unsigned F_FX1 = 352768;
constexpr unsigned F_FH = 353024;
constexpr unsigned F_FX = 354048;
constexpr unsigned F_FP = 354304;
constexpr unsigned RS = 524288;
constexpr unsigned PADF = 256;
constexpr unsigned LOWB = 64;
constexpr unsigned L_TAB = 98304;
constexpr unsigned LDS_BYTES = 163840;
constexpr int Q_GL = 2;
constexpr int Q_EX = 6;
constexpr int O_GL = 1;
constexpr int O_EX = 3;
constexpr int F_GL = 1;
constexpr int F_EX = 4;
constexpr int G_GL = 1;
constexpr int G_EX = 3;
constexpr int H_GL = 1;
constexpr int H_EX = 3;
constexpr int J_GL = 1;
constexpr int J_EX = 8;
#define MAPV 4
#define FDLY 0
#define PF 10
#define KR 1
#define XP 2
#define OR 1
#define JJ 1
#define DLO 0
#define DLF 0
#define DLG 0
#define SLP 0
#define PQ 1
#define QW 6
#define NFL 48
#define PSTX 0
#define CP 1
#define FN 4
#define XF 1
#define NE 16
#define DLX 300

constexpr unsigned DROP = 0x40000000u, WS0 = 1u << 20;
static_assert(WS0 + (PADF + 256 - LOWB) * RS <= (256u << 20), "workspace");
struct Args { const u16 *x, *wqkv, *wo, *w1, *b1, *w2, *b2; u16* out; char* ws;
};
__device__ __forceinline__ u32x4 desc(const void* p, unsigned n) { const u64 a = (u64)p; return (u32x4){(unsigned)a, (unsigned)(a >> 32) & 0xffffu, n, 0x00020000u}; }
__device__ __forceinline__ float lo16(unsigned v) { return __builtin_bit_cast(float, v << 16); }
__device__ __forceinline__ float hi16(unsigned v) { return __builtin_bit_cast(float, v & 0xffff0000u); }

__host__ __device__ inline void decode(int bid, int& role, int& set, int& idx) {
  const int xg = bid & 7, xj = bid >> 3;
  if (xg < 4) {
    if (xj < 16) { role = 0; set = xj & 1; idx = MAPV == 1 || MAPV == 4 ? 8 * xg + (xj >> 1) : MAPV == 2 ? 4 * (xj >> 1) + xg : 4 * (xj >> 1) + ((xg + 2 * set) & 3); }
    else if (MAPV == 4 && SLP && xj >= 30) { role = 2; set = 0; idx = (int)(0x1915110e0d090501ull >> (16 * xg + 8 * (xj & 1))) & 31; }
    else { role = 2; set = xj < 23 ? 1 : xj < 30 ? 2 : 0; idx = 7 * xg + (xj - 16) % 7; }
  } else if (xj < 21) { role = 3; set = xj / 7; idx = (xg - 4) * 7 + xj % 7; }
  else {
    const int k = xj - 21, no = xg == 4 ? 11 : xg == 5 ? 5 : 4;
    if (k < no) { role = 1; set = xg == 4 ? k >> 3 : xg == 5 ? 1 : 2; idx = xg == 4 ? k & 7 : xg == 5 ? 3 + k : (xg - 6) * 4 + k; }
    else if (SLP) { role = 2; set = 0; idx = (int)((xg == 5 ? 0x181716100f0000ull : xg == 6 ? 0x1b1a0a08070302ull : 0x14131206040c0bull) >> (8 * (k - 4))) & 31; }
    else { role = 2; set = 0; idx = (int)((xg == 5 ? 0x19181711100200ull : xg == 6 ? 0x1b1a0b0a090403ull : 0x14131206050d0cull) >> (8 * (k - 4))) & 31; }
  }
}

__global__ __launch_bounds__(256, 1) void stream_kernel(Args a) {
  extern __shared__ __attribute__((aligned(16))) char smem[];
  const int tid = threadIdx.x, lane = tid & 63, wid = __builtin_amdgcn_readfirstlane(tid >> 6), li = lane & 15, lg = lane >> 4;
  const unsigned lds0 = (unsigned)(size_t)smem;
  const u64 tz = __builtin_amdgcn_s_memrealtime();
  const int bp = (blockIdx.x + 2 * (XP - 1)) & 7, bid = (blockIdx.x & ~7) | ((bp & 1) << 2) | (bp >> 1);
  {
    u64 pc; unsigned len;
    asm volatile("s_getpc_b64 %0\n.Lc0:\n s_mov_b32 %1, .LO_end-.Lc0\n s_max_u32 %1, %1, .LH_end-.Lc0\n s_max_u32 %1, %1, .LJ_end-.Lc0\n s_max_u32 %1, %1, .LG_end-.Lc0" : "=s"(pc), "=s"(len)::"scc");
    asm volatile("global_load_dword v252, %0, %1" ::"v"(min(((blockIdx.x >> 3) * 256 + tid) * 8, len)), "s"(pc) : "v252");
  }
  if (!(bid & 4)) {
    const unsigned o = (wid * 256 + (bid >> 3) * 2 + (lane >> 5)) * 6144 + (bid & 7) * 512 + (lane & 31) * 16 + 2048;
#pragma unroll
    for (int i = 0; i < (PQ == 1 ? 5 : 4); ++i)
      if (PQ == 2 || i < 3 || bid >= 128)
        asm volatile("global_load_dwordx4 v[248:251], %0, %1 offset:-2048\n global_load_dwordx4 v[248:251], %0, %1\n global_load_dwordx4 v[248:251], %0, %1 offset:2048"
                     ::"v"(o + (PQ == 2 || i < 3 ? i * 64 : i * 32 + 64) * 6144), "s"(a.wqkv) : "v248", "v249", "v250", "v251");
  }
  if (FN == 4) __builtin_amdgcn_s_barrier();
  int role, set, idx;
  decode(bid, role, set, idx);
  const u64 wsp = (u64)a.ws, wsb = wsp + WS0 - LOWB * RS;
  const u32x4 rS = desc(a.ws + WS0 - LOWB * RS, (PADF + 256) * RS);
  const int pi = (li >> 2) + 4 * (li & 3);
  const int xj = bid >> 3, xo = (bid & 7) - 4;
  if (role == 2 && xo < 0 && (PF & 1)) {
    const int g = (xj - 16) * 4 + wid;
    const char* p = (const char*)a.wqkv + (((g >> 2) * 2 + 1) * 32 + (g & 3) * 8 + (lane >> 5)) * 6144 + (bid & 7) * 512 + (lane & 31) * 16;
    u32x4 t[12];
    unsigned fl = 0;
    if (PF & 4) fl = __hip_atomic_load((const unsigned*)(a.ws + WS0 + (size_t)(PADF - LOWB + g * 4 + (bid & 7)) * RS + F_ST2 + (lane < 56 ? lane : 0) * 128), __ATOMIC_RELAXED, __HIP_MEMORY_SCOPE_AGENT);
#pragma unroll
    for (int i = 0; i < 12; ++i) t[i] = *(const u32x4*)(p + (i & 3) * 12288 + (i >> 2) * 2048);
#pragma unroll
    for (int i = 0; i < 12; ++i) asm volatile("" ::"v"(t[i]));
    asm volatile("" ::"v"(fl));
  }
  if (role == 2 && xo < 0)
    asm volatile("global_load_dword v252, %0, %1 sc1" ::"v"(WS0 + (PADF - LOWB + (xj * 4 + wid - 64) * 4 + (bid & 7)) * RS + F_ST2 + (lane < 16 ? lane : lane < NFL ? lane + 16 : 0) * 128), "s"(a.ws) : "v252");
  if (role == 2 && xo < 0 && (SLP & 1)) {
    const unsigned c0 = 14 * (xo + 4), c = c0 + ((3 - c0 - (unsigned)((u64)a.w1 >> 7)) & 7) + (lane & 2) * 4;
    asm volatile("global_load_dword v252, %0, %1" ::"v"(((xj * 4 + wid - 64) * 16 + (lane >> 2)) * 7168 + (c < c0 + 14 ? c : c0) * 128 + (lane & 1) * 64), "s"(a.w1) : "v252");
  }
  if (role == 3 && xj * 4 + wid < 56 && (SLP & 2))
    asm volatile("global_load_dword v252, %0, %1" ::"v"((xo * 896 + (xj * 4 + wid) * 16 + (lane >> 2)) * 2048 + (((3 - (unsigned)((u64)a.w2 >> 7)) & 7) + (lane & 2) * 4) * 128 + (lane & 1) * 64), "s"(a.w2) : "v252");
  if (role == 0 || FN == 2 || (FN == 3 && role == 2)) asm volatile("s_waitcnt vmcnt(%0)" ::"n"(QW));
  if (FN == 3) __builtin_amdgcn_s_barrier();
  if (xo < 0 && (PF & 8) && !FN) {
    const int g = xj * 4 + wid;
    const char* p = (const char*)a.wqkv + (g * 8 + (lane >> 5)) * 6144 + (bid & 7) * 512 + (lane & 31) * 16;
    u32x4 t[12];
#pragma unroll
    for (int i = 0; i < 12; ++i) t[i] = *(const u32x4*)(p + (i & 3) * 12288 + (i >> 2) * 2048);
    unsigned fl = 0;
    if (role == 2) fl = __hip_atomic_load((const unsigned*)(a.ws + WS0 + (size_t)(PADF - LOWB + (g - 64) * 4 + (bid & 7)) * RS + F_ST2 + (lane < 56 ? lane : 0) * 128), __ATOMIC_RELAXED, __HIP_MEMORY_SCOPE_AGENT);
#pragma unroll
    for (int i = 0; i < 12; ++i) asm volatile("" ::"v"(t[i]));
    asm volatile("" ::"v"(fl));
  }
  if (role == 2 && xo < 0) for (int i = 0; i < FDLY; ++i) __builtin_amdgcn_s_sleep(16);
  const int p2 = xj < 4 ? 84 + xj * 4 + xo : xj % 7 > 3 ? 84 + (set * 3 + xj % 7) * 4 + xo : 128;
  for (int pr = role < 3 ? 128 : xj * 4 + xo; pr < 128; pr = pr < 84 ? p2 : 128) {
    if (pr >= NE) while (__builtin_amdgcn_s_memrealtime() - tz < DLX) __builtin_amdgcn_s_sleep(4);
    const __amdgpu_buffer_rsrc_t rb = __builtin_amdgcn_make_buffer_rsrc(a.ws + WS0 - LOWB * RS, 0, (PADF + 256) * RS, 0x00020000);
    const u32x4* src = (const u32x4*)(a.x + ((pr >> 1) * 32 + 2 * pi + (pr & 1)) * 1024 + wid * 256 + lg * 8);
    const u64 ev = __hip_atomic_load((const u64*)a.ws, __ATOMIC_RELAXED, __HIP_MEMORY_SCOPE_AGENT);
    u32x4 v[8];
#pragma unroll
    for (int k = 0; k < 8; ++k) v[k] = src[k * 4];
    u32x4 pf = {};
    if (pr >= NE && pr < (MAPV == 4 ? 84 : 104) && (PF & 2)) {
      const int hf = MAPV == 4 ? xo > 1 : xo, g = xj * 4 + wid - NE, n = MAPV == 4 ? (hf ? 767 / (84 - NE) + 1 : 1535 / (84 - NE) + 1) : hf ? 767 / (112 - NE) + 1 : 1535 / (80 - NE) + 1;
      for (int i = 0; i < n; ++i) {
        const int u = g * n + i, r0 = hf ? u : u >> 1, row = r0 < 768 ? (r0 / 192) * 256 + 64 + r0 % 192 : 0;
        asm volatile("global_load_dwordx4 %0, %1, off" : "+v"(pf) : "v"((const char*)a.wo + row * 2048 + (hf ? xo == (MAPV == 4 ? 3 : 2) : u & 1) * 1024 + lane * 16));
      }
    }
    float s = 0.f, q = 0.f;
#pragma unroll
    for (int k = 0; k < 8; ++k) {
      __builtin_amdgcn_raw_buffer_store_b128(v[k], rb, lane * 16 + k * 1024, (PADF + pr) * RS + F_X + wid * 8192, 16);
#pragma unroll
      for (int d = 0; d < 4; ++d) { const float l = lo16(v[k][d]), h = hi16(v[k][d]); s += l + h; q = fmaf(l, l, fmaf(h, h, q)); }
    }
    s += __shfl_xor(s, 16); q += __shfl_xor(q, 16); s += __shfl_xor(s, 32); q += __shfl_xor(q, 32);
    float2* t = (float2*)smem;
    if (lg == 0) t[wid * 16 + li] = make_float2(s, q);
    asm volatile("s_waitcnt vmcnt(0)" : "+v"(pf)::"memory");
    __syncthreads();
    if (tid < 16) {
      const float2 t0 = t[li], t1 = t[16 + li], t2 = t[32 + li], t3 = t[48 + li];
      const float nm = ((t0.x + t1.x) + (t2.x + t3.x)) * -0x1p-10f;
      const float var = fmaxf(fmaf(-nm, nm, fmaf((t0.y + t1.y) + (t2.y + t3.y), 0x1p-10f, 1e-5f)), 1e-5f);
      const u32x4 g = {__builtin_bit_cast(unsigned, __builtin_amdgcn_rsqf(var)), __builtin_bit_cast(unsigned, nm), ((unsigned)ev + 2u) | 1u, (unsigned)(ev >> 32) + 0x9E3779B9u};
      __builtin_amdgcn_raw_buffer_store_b128(g, rb, li * 16, (PADF + pr) * RS + F_G, 16);
    }
    __syncthreads();
  }
  const unsigned sLo = (PADF + set) * RS, sLd = lds0 + wid * 40960, vT = sLd + (8 * lg + (li >> 2)) * 64 + (li & 3) * 16;
  const unsigned trp = (li & 3) * 4608 + (li & 1) * 32 + (li & 2) * 64 + (li >> 2) * 64 + lg * 16;
  if (role == 0) {
    const unsigned sA = sLo - 2 * RS, sHi = sLo + 127 * 2 * RS, sEx = sHi + Q_EX * 2 * RS, sEnt = -wid & 3, tsw = 0, rot = KR * !PQ * (set + 2 * (idx & 1));
    const unsigned sW0 = wid * 256 * 6144 + idx * 64, vD = (lane >> 2) * 6144 + (lane & 3) * 16;
    const u32x4 rW = desc(a.wqkv, 1024 * 6144);
    auto ok = [&](int r, int prev) { const int k = lg + 4 * r; return prev ? k >= pi + 9 : (k <= pi && k >= pi - 7); };
    auto qmask = [&](int r, int prev) { return ok(r, prev) ? 0u : 0xff800000u; };
    auto pmask = [&](int d) { return (ok(2 * (d & 1), d >> 1) ? 0xffffu : 0u) | (ok(2 * (d & 1) + 1, d >> 1) ? 0xffff0000u : 0u); };
    asm volatile(
"s_nop 4\n"
"v_mov_b32 v186, 0\n"
"global_load_dwordx2 v[0:1], v186, s[48:49] sc1\n"
"v_mov_b32 v188, 0x3f803f80\n"
"v_mov_b32 v189, 0x3f803f80\n"
"v_mov_b32 v190, 0x3f803f80\n"
"v_mov_b32 v191, 0x3f803f80\n"
"s_add_u32 m0, s55, 0\n"
"s_add_u32 s60, s63, 0\n"
"buffer_load_dwordx4 v53, s[44:47], s60 offen lds\n"
"s_add_u32 m0, s55, 1024\n"
"s_add_u32 s60, s63, 98304\n"
"buffer_load_dwordx4 v53, s[44:47], s60 offen lds\n"
"s_add_u32 m0, s55, 2048\n"
"s_add_u32 s60, s64, 0\n"
"buffer_load_dwordx4 v53, s[44:47], s60 offen lds\n"
"s_add_u32 m0, s55, 3072\n"
"s_add_u32 s60, s64, 98304\n"
"buffer_load_dwordx4 v53, s[44:47], s60 offen lds\n"
"s_add_u32 m0, s55, 4096\n"
"s_add_u32 s60, s65, 0\n"
"buffer_load_dwordx4 v53, s[44:47], s60 offen lds\n"
"s_add_u32 m0, s55, 5120\n"
"s_add_u32 s60, s65, 98304\n"
"buffer_load_dwordx4 v53, s[44:47], s60 offen lds\n"
"s_add_u32 m0, s55, 6144\n"
"s_add_u32 s60, s63, 196608\n"
"buffer_load_dwordx4 v53, s[44:47], s60 offen lds\n"
"s_add_u32 m0, s55, 7168\n"
"s_add_u32 s60, s63, 294912\n"
"buffer_load_dwordx4 v53, s[44:47], s60 offen lds\n"
"s_add_u32 m0, s55, 8192\n"
"s_add_u32 s60, s64, 196608\n"
"buffer_load_dwordx4 v53, s[44:47], s60 offen lds\n"
"s_add_u32 m0, s55, 9216\n"
"s_add_u32 s60, s64, 294912\n"
"buffer_load_dwordx4 v53, s[44:47], s60 offen lds\n"
"s_add_u32 m0, s55, 10240\n"
"s_add_u32 s60, s65, 196608\n"
"buffer_load_dwordx4 v53, s[44:47], s60 offen lds\n"
"s_add_u32 m0, s55, 11264\n"
"s_add_u32 s60, s65, 294912\n"
"buffer_load_dwordx4 v53, s[44:47], s60 offen lds\n"
"s_add_u32 m0, s55, 12288\n"
"s_add_u32 s60, s63, 0\n"
"buffer_load_dwordx4 v54, s[44:47], s60 offen lds\n"
"s_add_u32 m0, s55, 13312\n"
"s_add_u32 s60, s63, 98304\n"
"buffer_load_dwordx4 v54, s[44:47], s60 offen lds\n"
"s_add_u32 m0, s55, 14336\n"
"s_add_u32 s60, s64, 0\n"
"buffer_load_dwordx4 v54, s[44:47], s60 offen lds\n"
"s_add_u32 m0, s55, 15360\n"
"s_add_u32 s60, s64, 98304\n"
"buffer_load_dwordx4 v54, s[44:47], s60 offen lds\n"
"s_add_u32 m0, s55, 16384\n"
"s_add_u32 s60, s65, 0\n"
"buffer_load_dwordx4 v54, s[44:47], s60 offen lds\n"
"s_add_u32 m0, s55, 17408\n"
"s_add_u32 s60, s65, 98304\n"
"buffer_load_dwordx4 v54, s[44:47], s60 offen lds\n"
"s_add_u32 m0, s55, 18432\n"
"s_add_u32 s60, s63, 196608\n"
"buffer_load_dwordx4 v54, s[44:47], s60 offen lds\n"
"s_add_u32 m0, s55, 19456\n"
"s_add_u32 s60, s63, 294912\n"
"buffer_load_dwordx4 v54, s[44:47], s60 offen lds\n"
"s_add_u32 m0, s55, 20480\n"
"s_add_u32 s60, s64, 196608\n"
"buffer_load_dwordx4 v54, s[44:47], s60 offen lds\n"
"s_add_u32 m0, s55, 21504\n"
"s_add_u32 s60, s64, 294912\n"
"buffer_load_dwordx4 v54, s[44:47], s60 offen lds\n"
"s_add_u32 m0, s55, 22528\n"
"s_add_u32 s60, s65, 196608\n"
"buffer_load_dwordx4 v54, s[44:47], s60 offen lds\n"
"s_add_u32 m0, s55, 23552\n"
"s_add_u32 s60, s65, 294912\n"
"buffer_load_dwordx4 v54, s[44:47], s60 offen lds\n"
"s_add_u32 m0, s55, 24576\n"
"s_add_u32 s60, s63, 0\n"
"buffer_load_dwordx4 v55, s[44:47], s60 offen lds\n"
"s_add_u32 m0, s55, 25600\n"
"s_add_u32 s60, s63, 98304\n"
"buffer_load_dwordx4 v55, s[44:47], s60 offen lds\n"
"s_add_u32 m0, s55, 26624\n"
"s_add_u32 s60, s64, 0\n"
"buffer_load_dwordx4 v55, s[44:47], s60 offen lds\n"
"s_add_u32 m0, s55, 27648\n"
"s_add_u32 s60, s64, 98304\n"
"buffer_load_dwordx4 v55, s[44:47], s60 offen lds\n"
"s_add_u32 m0, s55, 28672\n"
"s_add_u32 s60, s65, 0\n"
"buffer_load_dwordx4 v55, s[44:47], s60 offen lds\n"
"s_add_u32 m0, s55, 29696\n"
"s_add_u32 s60, s65, 98304\n"
"buffer_load_dwordx4 v55, s[44:47], s60 offen lds\n"
"s_add_u32 m0, s55, 30720\n"
"s_add_u32 s60, s63, 196608\n"
"buffer_load_dwordx4 v55, s[44:47], s60 offen lds\n"
"s_add_u32 m0, s55, 31744\n"
"s_add_u32 s60, s63, 294912\n"
"buffer_load_dwordx4 v55, s[44:47], s60 offen lds\n"
"s_add_u32 m0, s55, 32768\n"
"s_add_u32 s60, s64, 196608\n"
"buffer_load_dwordx4 v55, s[44:47], s60 offen lds\n"
"s_add_u32 m0, s55, 33792\n"
"s_add_u32 s60, s64, 294912\n"
"buffer_load_dwordx4 v55, s[44:47], s60 offen lds\n"
"s_add_u32 m0, s55, 34816\n"
"s_add_u32 s60, s65, 196608\n"
"buffer_load_dwordx4 v55, s[44:47], s60 offen lds\n"
"s_add_u32 m0, s55, 35840\n"
"s_add_u32 s60, s65, 294912\n"
"buffer_load_dwordx4 v55, s[44:47], s60 offen lds\n"
"s_add_u32 m0, s55, 36864\n"
"s_add_u32 s60, s63, 0\n"
"buffer_load_dwordx4 v56, s[44:47], s60 offen lds\n"
"s_add_u32 m0, s55, 37888\n"
"s_add_u32 s60, s63, 98304\n"
"buffer_load_dwordx4 v56, s[44:47], s60 offen lds\n"
"s_add_u32 m0, s55, 38912\n"
"s_add_u32 s60, s64, 0\n"
"buffer_load_dwordx4 v56, s[44:47], s60 offen lds\n"
"s_add_u32 m0, s55, 39936\n"
"s_add_u32 s60, s64, 98304\n"
"buffer_load_dwordx4 v56, s[44:47], s60 offen lds\n"
"s_waitcnt vmcnt(38)\n"
"ds_read_b64_tr_b16 a[0:1], v48 offset:0\n"
"ds_read_b64_tr_b16 a[2:3], v48 offset:256\n"
"ds_read_b64_tr_b16 a[32:33], v49 offset:0\n"
"ds_read_b64_tr_b16 a[34:35], v49 offset:256\n"
"v_add_u32 v0, 2, v0\n"
"v_add_u32 v1, 0x9e3779b9, v1\n"
"v_or_b32 v0, 1, v0\n"
"v_readfirstlane_b32 s49, v1\n"
"v_readfirstlane_b32 s48, v0\n"
"s_waitcnt vmcnt(36)\n"
"ds_read_b64_tr_b16 a[64:65], v48 offset:2048\n"
"ds_read_b64_tr_b16 a[66:67], v48 offset:2304\n"
"ds_read_b64_tr_b16 a[96:97], v49 offset:2048\n"
"ds_read_b64_tr_b16 a[98:99], v49 offset:2304\n"
"s_waitcnt lgkmcnt(4)\n"
"s_add_u32 m0, s55, 0\n"
"s_add_u32 s60, s65, 0\n"
"buffer_load_dwordx4 v56, s[44:47], s60 offen lds\n"
"s_add_u32 m0, s55, 1024\n"
"s_add_u32 s60, s65, 98304\n"
"buffer_load_dwordx4 v56, s[44:47], s60 offen lds\n"
"v_mfma_f32_16x16x32_bf16 v[192:195], v[188:191], a[0:3], 0\n"
"v_mfma_f32_16x16x32_bf16 v[196:199], v[188:191], a[32:35], 0\n"
"s_waitcnt vmcnt(36)\n"
"ds_read_b64_tr_b16 a[128:129], v48 offset:4096\n"
"ds_read_b64_tr_b16 a[130:131], v48 offset:4352\n"
"ds_read_b64_tr_b16 a[160:161], v49 offset:4096\n"
"ds_read_b64_tr_b16 a[162:163], v49 offset:4352\n"
"s_waitcnt lgkmcnt(4)\n"
"s_add_u32 m0, s55, 2048\n"
"s_add_u32 s60, s63, 196608\n"
"buffer_load_dwordx4 v56, s[44:47], s60 offen lds\n"
"s_add_u32 m0, s55, 3072\n"
"s_add_u32 s60, s63, 294912\n"
"buffer_load_dwordx4 v56, s[44:47], s60 offen lds\n"
"v_mfma_f32_16x16x32_bf16 v[200:203], v[188:191], a[64:67], 0\n"
"v_mfma_f32_16x16x32_bf16 v[204:207], v[188:191], a[96:99], 0\n"
"s_waitcnt vmcnt(36)\n"
"ds_read_b64_tr_b16 a[4:5], v48 offset:6144\n"
"ds_read_b64_tr_b16 a[6:7], v48 offset:6400\n"
"ds_read_b64_tr_b16 a[36:37], v49 offset:6144\n"
"ds_read_b64_tr_b16 a[38:39], v49 offset:6400\n"
"s_waitcnt lgkmcnt(4)\n"
"s_add_u32 m0, s55, 4096\n"
"s_add_u32 s60, s64, 196608\n"
"buffer_load_dwordx4 v56, s[44:47], s60 offen lds\n"
"s_add_u32 m0, s55, 5120\n"
"s_add_u32 s60, s64, 294912\n"
"buffer_load_dwordx4 v56, s[44:47], s60 offen lds\n"
"v_mfma_f32_16x16x32_bf16 v[208:211], a[128:131], v[188:191], 0\n"
"v_mfma_f32_16x16x32_bf16 v[212:215], a[160:163], v[188:191], 0\n"
"s_waitcnt vmcnt(36)\n"
"ds_read_b64_tr_b16 a[68:69], v48 offset:8192\n"
"ds_read_b64_tr_b16 a[70:71], v48 offset:8448\n"
"ds_read_b64_tr_b16 a[100:101], v49 offset:8192\n"
"ds_read_b64_tr_b16 a[102:103], v49 offset:8448\n"
"s_waitcnt lgkmcnt(4)\n"
"s_add_u32 m0, s55, 6144\n"
"s_add_u32 s60, s65, 196608\n"
"buffer_load_dwordx4 v56, s[44:47], s60 offen lds\n"
"s_add_u32 m0, s55, 7168\n"
"s_add_u32 s60, s65, 294912\n"
"buffer_load_dwordx4 v56, s[44:47], s60 offen lds\n"
"v_mfma_f32_16x16x32_bf16 v[192:195], v[188:191], a[4:7], v[192:195]\n"
"v_mfma_f32_16x16x32_bf16 v[196:199], v[188:191], a[36:39], v[196:199]\n"
"s_waitcnt vmcnt(36)\n"
"ds_read_b64_tr_b16 a[132:133], v48 offset:10240\n"
"ds_read_b64_tr_b16 a[134:135], v48 offset:10496\n"
"ds_read_b64_tr_b16 a[164:165], v49 offset:10240\n"
"ds_read_b64_tr_b16 a[166:167], v49 offset:10496\n"
"s_waitcnt lgkmcnt(4)\n"
"v_mfma_f32_16x16x32_bf16 v[200:203], v[188:191], a[68:71], v[200:203]\n"
"v_mfma_f32_16x16x32_bf16 v[204:207], v[188:191], a[100:103], v[204:207]\n"
"s_waitcnt vmcnt(34)\n"
"ds_read_b64_tr_b16 a[8:9], v48 offset:12288\n"
"ds_read_b64_tr_b16 a[10:11], v48 offset:12544\n"
"ds_read_b64_tr_b16 a[40:41], v49 offset:12288\n"
"ds_read_b64_tr_b16 a[42:43], v49 offset:12544\n"
"s_waitcnt lgkmcnt(4)\n"
"v_mfma_f32_16x16x32_bf16 v[208:211], a[132:135], v[188:191], v[208:211]\n"
"v_mfma_f32_16x16x32_bf16 v[212:215], a[164:167], v[188:191], v[212:215]\n"
"s_waitcnt vmcnt(32)\n"
"ds_read_b64_tr_b16 a[72:73], v48 offset:14336\n"
"ds_read_b64_tr_b16 a[74:75], v48 offset:14592\n"
"ds_read_b64_tr_b16 a[104:105], v49 offset:14336\n"
"ds_read_b64_tr_b16 a[106:107], v49 offset:14592\n"
"s_waitcnt lgkmcnt(4)\n"
"v_mfma_f32_16x16x32_bf16 v[192:195], v[188:191], a[8:11], v[192:195]\n"
"v_mfma_f32_16x16x32_bf16 v[196:199], v[188:191], a[40:43], v[196:199]\n"
"s_waitcnt vmcnt(30)\n"
"ds_read_b64_tr_b16 a[136:137], v48 offset:16384\n"
"ds_read_b64_tr_b16 a[138:139], v48 offset:16640\n"
"ds_read_b64_tr_b16 a[168:169], v49 offset:16384\n"
"ds_read_b64_tr_b16 a[170:171], v49 offset:16640\n"
"s_waitcnt lgkmcnt(4)\n"
"v_mfma_f32_16x16x32_bf16 v[200:203], v[188:191], a[72:75], v[200:203]\n"
"v_mfma_f32_16x16x32_bf16 v[204:207], v[188:191], a[104:107], v[204:207]\n"
"s_waitcnt vmcnt(28)\n"
"ds_read_b64_tr_b16 a[12:13], v48 offset:18432\n"
"ds_read_b64_tr_b16 a[14:15], v48 offset:18688\n"
"ds_read_b64_tr_b16 a[44:45], v49 offset:18432\n"
"ds_read_b64_tr_b16 a[46:47], v49 offset:18688\n"
"s_waitcnt lgkmcnt(4)\n"
"v_mfma_f32_16x16x32_bf16 v[208:211], a[136:139], v[188:191], v[208:211]\n"
"v_mfma_f32_16x16x32_bf16 v[212:215], a[168:171], v[188:191], v[212:215]\n"
"s_waitcnt vmcnt(26)\n"
"ds_read_b64_tr_b16 a[76:77], v48 offset:20480\n"
"ds_read_b64_tr_b16 a[78:79], v48 offset:20736\n"
"ds_read_b64_tr_b16 a[108:109], v49 offset:20480\n"
"ds_read_b64_tr_b16 a[110:111], v49 offset:20736\n"
"s_waitcnt lgkmcnt(4)\n"
"v_mfma_f32_16x16x32_bf16 v[192:195], v[188:191], a[12:15], v[192:195]\n"
"v_mfma_f32_16x16x32_bf16 v[196:199], v[188:191], a[44:47], v[196:199]\n"
"s_waitcnt vmcnt(24)\n"
"ds_read_b64_tr_b16 a[140:141], v48 offset:22528\n"
"ds_read_b64_tr_b16 a[142:143], v48 offset:22784\n"
"ds_read_b64_tr_b16 a[172:173], v49 offset:22528\n"
"ds_read_b64_tr_b16 a[174:175], v49 offset:22784\n"
"s_waitcnt lgkmcnt(4)\n"
"v_mfma_f32_16x16x32_bf16 v[200:203], v[188:191], a[76:79], v[200:203]\n"
"v_mfma_f32_16x16x32_bf16 v[204:207], v[188:191], a[108:111], v[204:207]\n"
"s_waitcnt vmcnt(22)\n"
"ds_read_b64_tr_b16 a[16:17], v48 offset:24576\n"
"ds_read_b64_tr_b16 a[18:19], v48 offset:24832\n"
"ds_read_b64_tr_b16 a[48:49], v49 offset:24576\n"
"ds_read_b64_tr_b16 a[50:51], v49 offset:24832\n"
"s_waitcnt lgkmcnt(4)\n"
"v_mfma_f32_16x16x32_bf16 v[208:211], a[140:143], v[188:191], v[208:211]\n"
"v_mfma_f32_16x16x32_bf16 v[212:215], a[172:175], v[188:191], v[212:215]\n"
"s_waitcnt vmcnt(20)\n"
"ds_read_b64_tr_b16 a[80:81], v48 offset:26624\n"
"ds_read_b64_tr_b16 a[82:83], v48 offset:26880\n"
"ds_read_b64_tr_b16 a[112:113], v49 offset:26624\n"
"ds_read_b64_tr_b16 a[114:115], v49 offset:26880\n"
"s_waitcnt lgkmcnt(4)\n"
"v_mfma_f32_16x16x32_bf16 v[192:195], v[188:191], a[16:19], v[192:195]\n"
"v_mfma_f32_16x16x32_bf16 v[196:199], v[188:191], a[48:51], v[196:199]\n"
"s_waitcnt vmcnt(18)\n"
"ds_read_b64_tr_b16 a[144:145], v48 offset:28672\n"
"ds_read_b64_tr_b16 a[146:147], v48 offset:28928\n"
"ds_read_b64_tr_b16 a[176:177], v49 offset:28672\n"
"ds_read_b64_tr_b16 a[178:179], v49 offset:28928\n"
"s_waitcnt lgkmcnt(4)\n"
"v_mfma_f32_16x16x32_bf16 v[200:203], v[188:191], a[80:83], v[200:203]\n"
"v_mfma_f32_16x16x32_bf16 v[204:207], v[188:191], a[112:115], v[204:207]\n"
"s_waitcnt vmcnt(16)\n"
"ds_read_b64_tr_b16 a[20:21], v48 offset:30720\n"
"ds_read_b64_tr_b16 a[22:23], v48 offset:30976\n"
"ds_read_b64_tr_b16 a[52:53], v49 offset:30720\n"
"ds_read_b64_tr_b16 a[54:55], v49 offset:30976\n"
"s_waitcnt lgkmcnt(4)\n"
"v_mfma_f32_16x16x32_bf16 v[208:211], a[144:147], v[188:191], v[208:211]\n"
"v_mfma_f32_16x16x32_bf16 v[212:215], a[176:179], v[188:191], v[212:215]\n"
"s_waitcnt vmcnt(14)\n"
"ds_read_b64_tr_b16 a[84:85], v48 offset:32768\n"
"ds_read_b64_tr_b16 a[86:87], v48 offset:33024\n"
"ds_read_b64_tr_b16 a[116:117], v49 offset:32768\n"
"ds_read_b64_tr_b16 a[118:119], v49 offset:33024\n"
"s_waitcnt lgkmcnt(4)\n"
"v_mfma_f32_16x16x32_bf16 v[192:195], v[188:191], a[20:23], v[192:195]\n"
"v_mfma_f32_16x16x32_bf16 v[196:199], v[188:191], a[52:55], v[196:199]\n"
"s_waitcnt vmcnt(12)\n"
"ds_read_b64_tr_b16 a[148:149], v48 offset:34816\n"
"ds_read_b64_tr_b16 a[150:151], v48 offset:35072\n"
"ds_read_b64_tr_b16 a[180:181], v49 offset:34816\n"
"ds_read_b64_tr_b16 a[182:183], v49 offset:35072\n"
"s_waitcnt lgkmcnt(4)\n"
"v_mfma_f32_16x16x32_bf16 v[200:203], v[188:191], a[84:87], v[200:203]\n"
"v_mfma_f32_16x16x32_bf16 v[204:207], v[188:191], a[116:119], v[204:207]\n"
"s_waitcnt vmcnt(10)\n"
"ds_read_b64_tr_b16 a[24:25], v48 offset:36864\n"
"ds_read_b64_tr_b16 a[26:27], v48 offset:37120\n"
"ds_read_b64_tr_b16 a[56:57], v49 offset:36864\n"
"ds_read_b64_tr_b16 a[58:59], v49 offset:37120\n"
"s_waitcnt lgkmcnt(4)\n"
"v_mfma_f32_16x16x32_bf16 v[208:211], a[148:151], v[188:191], v[208:211]\n"
"v_mfma_f32_16x16x32_bf16 v[212:215], a[180:183], v[188:191], v[212:215]\n"
"s_waitcnt vmcnt(8)\n"
"ds_read_b64_tr_b16 a[88:89], v48 offset:38912\n"
"ds_read_b64_tr_b16 a[90:91], v48 offset:39168\n"
"ds_read_b64_tr_b16 a[120:121], v49 offset:38912\n"
"ds_read_b64_tr_b16 a[122:123], v49 offset:39168\n"
"s_waitcnt lgkmcnt(4)\n"
"v_mfma_f32_16x16x32_bf16 v[192:195], v[188:191], a[24:27], v[192:195]\n"
"v_mfma_f32_16x16x32_bf16 v[196:199], v[188:191], a[56:59], v[196:199]\n"
"buffer_load_dwordx4 v[240:243], v50, s[40:43], s50 offen sc1\n"
"buffer_load_dwordx4 v[244:247], v51, s[40:43], s50 offen sc1\n"
"s_waitcnt vmcnt(8)\n"
"ds_read_b64_tr_b16 a[152:153], v48 offset:0\n"
"ds_read_b64_tr_b16 a[154:155], v48 offset:256\n"
"ds_read_b64_tr_b16 a[184:185], v49 offset:0\n"
"ds_read_b64_tr_b16 a[186:187], v49 offset:256\n"
"s_waitcnt lgkmcnt(4)\n"
"v_mfma_f32_16x16x32_bf16 v[200:203], v[188:191], a[88:91], v[200:203]\n"
"v_mfma_f32_16x16x32_bf16 v[204:207], v[188:191], a[120:123], v[204:207]\n"
"s_waitcnt vmcnt(6)\n"
"ds_read_b64_tr_b16 a[28:29], v48 offset:2048\n"
"ds_read_b64_tr_b16 a[30:31], v48 offset:2304\n"
"ds_read_b64_tr_b16 a[60:61], v49 offset:2048\n"
"ds_read_b64_tr_b16 a[62:63], v49 offset:2304\n"
"s_waitcnt lgkmcnt(4)\n"
"v_mfma_f32_16x16x32_bf16 v[208:211], a[152:155], v[188:191], v[208:211]\n"
"v_mfma_f32_16x16x32_bf16 v[212:215], a[184:187], v[188:191], v[212:215]\n"
"s_waitcnt vmcnt(4)\n"
"ds_read_b64_tr_b16 a[92:93], v48 offset:4096\n"
"ds_read_b64_tr_b16 a[94:95], v48 offset:4352\n"
"ds_read_b64_tr_b16 a[124:125], v49 offset:4096\n"
"ds_read_b64_tr_b16 a[126:127], v49 offset:4352\n"
"s_waitcnt lgkmcnt(4)\n"
"v_mfma_f32_16x16x32_bf16 v[192:195], v[188:191], a[28:31], v[192:195]\n"
"v_mfma_f32_16x16x32_bf16 v[196:199], v[188:191], a[60:63], v[196:199]\n"
"s_waitcnt vmcnt(2)\n"
"ds_read_b64_tr_b16 a[156:157], v48 offset:6144\n"
"ds_read_b64_tr_b16 a[158:159], v48 offset:6400\n"
"ds_read_b64_tr_b16 a[188:189], v49 offset:6144\n"
"ds_read_b64_tr_b16 a[190:191], v49 offset:6400\n"
"s_waitcnt lgkmcnt(4)\n"
"v_mfma_f32_16x16x32_bf16 v[200:203], v[188:191], a[92:95], v[200:203]\n"
"v_mfma_f32_16x16x32_bf16 v[204:207], v[188:191], a[124:127], v[204:207]\n"
"global_load_dwordx4 v[228:231], v52, s[56:57] offset:-4096 sc1\n"
"global_load_dwordx4 v[228:231], v52, s[56:57] offset:-3072 sc1\n"
"global_load_dwordx4 v[228:231], v52, s[56:57] offset:-2048 sc1\n"
"global_load_dwordx4 v[228:231], v52, s[56:57] offset:-1024 sc1\n"
"global_load_dwordx4 v[228:231], v52, s[56:57] offset:0 sc1\n"
"global_load_dwordx4 v[228:231], v52, s[56:57] offset:1024 sc1\n"
"global_load_dwordx4 v[228:231], v52, s[56:57] offset:2048 sc1\n"
"global_load_dwordx4 v[228:231], v52, s[56:57] offset:3072 sc1\n"
"s_waitcnt lgkmcnt(0)\n"
"v_mfma_f32_16x16x32_bf16 v[208:211], a[156:159], v[188:191], v[208:211]\n"
"v_mfma_f32_16x16x32_bf16 v[212:215], a[188:191], v[188:191], v[212:215]\n"
"s_mov_b32 m0, s59\n"
"s_barrier\n"
"ds_write_addtid_b32 v192 offset:0\n"
"ds_write_addtid_b32 v193 offset:4640\n"
"ds_write_addtid_b32 v194 offset:9344\n"
"ds_write_addtid_b32 v195 offset:13984\n"
"ds_write_addtid_b32 v196 offset:256\n"
"ds_write_addtid_b32 v197 offset:4896\n"
"ds_write_addtid_b32 v198 offset:9600\n"
"ds_write_addtid_b32 v199 offset:14240\n"
"ds_write_addtid_b32 v200 offset:512\n"
"ds_write_addtid_b32 v201 offset:5152\n"
"ds_write_addtid_b32 v202 offset:9856\n"
"ds_write_addtid_b32 v203 offset:14496\n"
"ds_write_addtid_b32 v204 offset:768\n"
"ds_write_addtid_b32 v205 offset:5408\n"
"ds_write_addtid_b32 v206 offset:10112\n"
"ds_write_addtid_b32 v207 offset:14752\n"
"ds_write_addtid_b32 v208 offset:1024\n"
"ds_write_addtid_b32 v209 offset:5664\n"
"ds_write_addtid_b32 v210 offset:10368\n"
"ds_write_addtid_b32 v211 offset:15008\n"
"ds_write_addtid_b32 v212 offset:1280\n"
"ds_write_addtid_b32 v213 offset:5920\n"
"ds_write_addtid_b32 v214 offset:10624\n"
"ds_write_addtid_b32 v215 offset:15264\n"
"s_waitcnt lgkmcnt(0)\n"
"s_barrier\n"
"ds_read_b128 v[96:99], v2 offset:0\n"
"ds_read_b128 v[100:103], v3 offset:0\n"
"ds_read_b128 v[104:107], v4 offset:0\n"
"ds_read_b128 v[108:111], v57 offset:0\n"
"ds_read_b128 v[112:115], v2 offset:256\n"
"ds_read_b128 v[116:119], v3 offset:256\n"
"ds_read_b128 v[120:123], v4 offset:256\n"
"ds_read_b128 v[124:127], v57 offset:256\n"
"ds_read_b128 v[128:131], v2 offset:512\n"
"ds_read_b128 v[132:135], v3 offset:512\n"
"ds_read_b128 v[136:139], v4 offset:512\n"
"ds_read_b128 v[140:143], v57 offset:512\n"
"ds_read_b128 v[144:147], v2 offset:768\n"
"ds_read_b128 v[148:151], v3 offset:768\n"
"ds_read_b128 v[152:155], v4 offset:768\n"
"ds_read_b128 v[156:159], v57 offset:768\n"
"ds_read_b128 v[160:163], v2 offset:1024\n"
"ds_read_b128 v[164:167], v3 offset:1024\n"
"ds_read_b128 v[168:171], v4 offset:1024\n"
"ds_read_b128 v[172:175], v57 offset:1024\n"
"ds_read_b128 v[176:179], v2 offset:1280\n"
"ds_read_b128 v[180:183], v3 offset:1280\n"
"ds_read_b128 v[184:187], v4 offset:1280\n"
"ds_read_b128 v[188:191], v57 offset:1280\n"
"s_waitcnt lgkmcnt(0)\n"
"s_barrier\n"
"v_add_f32 v96, v96, v100\n"
"v_add_f32 v104, v104, v108\n"
"v_add_f32 v28, v96, v104\n"
"v_add_f32 v97, v97, v101\n"
"v_add_f32 v105, v105, v109\n"
"v_add_f32 v29, v97, v105\n"
"v_add_f32 v98, v98, v102\n"
"v_add_f32 v106, v106, v110\n"
"v_add_f32 v30, v98, v106\n"
"v_add_f32 v99, v99, v103\n"
"v_add_f32 v107, v107, v111\n"
"v_add_f32 v31, v99, v107\n"
"v_add_f32 v112, v112, v116\n"
"v_add_f32 v120, v120, v124\n"
"v_add_f32 v32, v112, v120\n"
"v_add_f32 v113, v113, v117\n"
"v_add_f32 v121, v121, v125\n"
"v_add_f32 v33, v113, v121\n"
"v_add_f32 v114, v114, v118\n"
"v_add_f32 v122, v122, v126\n"
"v_add_f32 v34, v114, v122\n"
"v_add_f32 v115, v115, v119\n"
"v_add_f32 v123, v123, v127\n"
"v_add_f32 v35, v115, v123\n"
"v_add_f32 v128, v128, v132\n"
"v_add_f32 v136, v136, v140\n"
"v_add_f32 v36, v128, v136\n"
"v_add_f32 v129, v129, v133\n"
"v_add_f32 v137, v137, v141\n"
"v_add_f32 v37, v129, v137\n"
"v_add_f32 v130, v130, v134\n"
"v_add_f32 v138, v138, v142\n"
"v_add_f32 v38, v130, v138\n"
"v_add_f32 v131, v131, v135\n"
"v_add_f32 v139, v139, v143\n"
"v_add_f32 v39, v131, v139\n"
"v_add_f32 v144, v144, v148\n"
"v_add_f32 v152, v152, v156\n"
"v_add_f32 v40, v144, v152\n"
"v_add_f32 v145, v145, v149\n"
"v_add_f32 v153, v153, v157\n"
"v_add_f32 v41, v145, v153\n"
"v_add_f32 v146, v146, v150\n"
"v_add_f32 v154, v154, v158\n"
"v_add_f32 v42, v146, v154\n"
"v_add_f32 v147, v147, v151\n"
"v_add_f32 v155, v155, v159\n"
"v_add_f32 v43, v147, v155\n"
"v_add_f32 v160, v160, v164\n"
"v_add_f32 v168, v168, v172\n"
"v_add_f32 v44, v160, v168\n"
"v_add_f32 v176, v176, v180\n"
"v_add_f32 v184, v184, v188\n"
"v_add_f32 v45, v176, v184\n"
"v_mov_b32 v128, 0\n"
"v_mov_b32 v129, 0\n"
"v_mov_b32 v130, 0\n"
"v_mov_b32 v131, 0\n"
"v_mov_b32 v136, 0\n"
"v_mov_b32 v137, 0\n"
"v_mov_b32 v138, 0\n"
"v_mov_b32 v139, 0\n"
"v_mov_b32 v144, 0\n"
"v_mov_b32 v145, 0\n"
"v_mov_b32 v146, 0\n"
"v_mov_b32 v147, 0\n"
"v_mov_b32 v152, 0\n"
"v_mov_b32 v153, 0\n"
"v_mov_b32 v154, 0\n"
"v_mov_b32 v155, 0\n"
"s_mov_b32 s95, 0x80000000\n"
"s_mov_b32 s94, 0x80000000\n"
"s_waitcnt vmcnt(8)\n"
"v_cmp_ne_u64 vcc, s[48:49], v[242:243]\n"
"s_cmp_lg_u32 s54, 0\n"
"s_cbranch_scc1 .LQ_e0\n"
"v_mov_b32 v128, v240\n"
"v_mov_b32 v129, v241\n"
"v_mov_b32 v130, v242\n"
"v_mov_b32 v131, v243\n"
"v_mov_b32 v136, v244\n"
"v_mov_b32 v137, v245\n"
"v_mov_b32 v138, v246\n"
"v_mov_b32 v139, v247\n"
"s_cbranch_vccnz .LQ_1s\n"
"s_branch .LQ_1r\n"
".LQ_e0:\n"
"s_cmp_lg_u32 s54, 1\n"
"s_cbranch_scc1 .LQ_e1\n"
"v_mov_b32 v136, v240\n"
"v_mov_b32 v137, v241\n"
"v_mov_b32 v138, v242\n"
"v_mov_b32 v139, v243\n"
"v_mov_b32 v144, v244\n"
"v_mov_b32 v145, v245\n"
"v_mov_b32 v146, v246\n"
"v_mov_b32 v147, v247\n"
"s_cbranch_vccnz .LQ_2s\n"
"s_branch .LQ_2r\n"
".LQ_e1:\n"
"s_cmp_lg_u32 s54, 2\n"
"s_cbranch_scc1 .LQ_e2\n"
"v_mov_b32 v144, v240\n"
"v_mov_b32 v145, v241\n"
"v_mov_b32 v146, v242\n"
"v_mov_b32 v147, v243\n"
"v_mov_b32 v152, v244\n"
"v_mov_b32 v153, v245\n"
"v_mov_b32 v154, v246\n"
"v_mov_b32 v155, v247\n"
"s_cbranch_vccnz .LQ_3s\n"
"s_branch .LQ_3r\n"
".LQ_e2:\n"
"v_mov_b32 v152, v240\n"
"v_mov_b32 v153, v241\n"
"v_mov_b32 v154, v242\n"
"v_mov_b32 v155, v243\n"
"v_mov_b32 v128, v244\n"
"v_mov_b32 v129, v245\n"
"v_mov_b32 v130, v246\n"
"v_mov_b32 v131, v247\n"
"s_cbranch_vccnz .LQ_4s\n"
"s_branch .LQ_4r\n"
".LQ_e3:\n"
".p2align 6\n"
".LQ_top:\n"
"v_mfma_f32_16x16x32_bf16 v[112:115], a[128:131], v[48:51], 0\n"
"s_add_u32 s50, s50, 0x100000\n"
"global_load_dwordx4 a[192:195], v5, s[56:57] offset:0 sc1\n"
"v_mfma_f32_16x16x32_bf16 v[112:115], a[132:135], v[52:55], v[112:115]\n"
"ds_write_addtid_b32 v132 offset:4352\n"
"v_mfma_f32_16x16x32_bf16 v[112:115], a[136:139], v[56:59], v[112:115]\n"
"ds_write_addtid_b32 v133 offset:8992\n"
"v_mfma_f32_16x16x32_bf16 v[112:115], a[140:143], v[60:63], v[112:115]\n"
"ds_write_addtid_b32 v134 offset:13696\n"
"v_mfma_f32_16x16x32_bf16 v[112:115], a[144:147], v[64:67], v[112:115]\n"
"ds_write_addtid_b32 v135 offset:18336\n"
"v_mov_b32 v255, v128\n"
"v_mfma_f32_16x16x32_bf16 v[112:115], a[148:151], v[68:71], v[112:115]\n"
"global_load_dwordx4 a[196:199], v5, s[56:57] offset:1024 sc1\n"
"v_mul_f32 v119, v129, v39\n"
"v_mfma_f32_16x16x32_bf16 v[112:115], a[152:155], v[72:75], v[112:115]\n"
"v_mul_f32 v117, v129, v37\n"
"v_mul_f32 v118, v129, v38\n"
"v_mfma_f32_16x16x32_bf16 v[112:115], a[156:159], v[76:79], v[112:115]\n"
"v_mul_f32 v123, v129, v43\n"
"v_mul_f32 v116, v129, v36\n"
"v_mfma_f32_16x16x32_bf16 v[124:127], a[160:163], v[48:51], 0\n"
"v_mul_f32 v121, v129, v41\n"
"v_mul_f32 v122, v129, v42\n"
"v_mfma_f32_16x16x32_bf16 v[124:127], a[164:167], v[52:55], v[124:127]\n"
"ds_write_addtid_b32 v112 offset:0\n"
"v_mul_f32 v120, v129, v40\n"
"v_mfma_f32_16x16x32_bf16 v[124:127], a[168:171], v[56:59], v[124:127]\n"
"s_waitcnt lgkmcnt(1)\n"
"ds_write_addtid_b32 v113 offset:4640\n"
"global_load_dwordx4 a[200:203], v6, s[56:57] offset:0 sc1\n"
"v_mfma_f32_16x16x32_bf16 v[124:127], a[172:175], v[60:63], v[124:127]\n"
"s_barrier\n"
"v_mfma_f32_16x16x32_bf16 v[124:127], a[176:179], v[64:67], v[124:127]\n"
"ds_write_addtid_b32 v114 offset:9344\n"
"ds_read2st64_b32 v[128:129], v12 offset0:0 offset1:1\n"
"v_mfma_f32_16x16x32_bf16 v[124:127], a[180:183], v[68:71], v[124:127]\n"
"ds_write_addtid_b32 v115 offset:13984\n"
"ds_read2st64_b32 v[130:131], v12 offset0:2 offset1:3\n"
"v_mfma_f32_16x16x32_bf16 v[124:127], a[184:187], v[72:75], v[124:127]\n"
"ds_read2st64_b32 v[140:141], v11 offset0:4 offset1:5\n"
"ds_read2st64_b32 v[148:149], v11 offset0:6 offset1:7\n"
"v_mfma_f32_16x16x32_bf16 v[124:127], a[188:191], v[76:79], v[124:127]\n"
"s_add_u32 s95, s50, 0xffc00000\n"
"s_and_b32 s94, s95, 0xfbffffff\n"
"v_mfma_f32_16x16x32_bf16 v[112:115], v[48:51], a[0:3], 0\n"
"global_load_dwordx4 a[204:207], v6, s[56:57] offset:1024 sc1\n"
"v_mfma_f32_16x16x32_bf16 v[112:115], v[52:55], a[4:7], v[112:115]\n"
"ds_write_addtid_b32 v124 offset:256\n"
"v_mfma_f32_16x16x32_bf16 v[112:115], v[56:59], a[8:11], v[112:115]\n"
"s_waitcnt lgkmcnt(3)\n"
"ds_write_addtid_b32 v125 offset:4896\n"
"v_mfma_f32_16x16x32_bf16 v[132:135], v[128:131], v[164:167], v[16:19]\n"
"s_cmp_eq_u32 s95, s62\n"
"s_cbranch_scc1 .LQ_fs\n"
".LQ_fr:\n"
"v_mfma_f32_16x16x32_bf16 v[112:115], v[60:63], a[12:15], v[112:115]\n"
"ds_write_addtid_b32 v126 offset:9600\n"
"v_max_f32 v254, v156, v158\n"
"v_mfma_f32_16x16x32_bf16 v[112:115], v[64:67], a[16:19], v[112:115]\n"
"ds_write_addtid_b32 v127 offset:14240\n"
"global_load_dwordx4 a[208:211], v7, s[56:57] offset:0 sc1\n"
"v_mfma_f32_16x16x32_bf16 v[112:115], v[68:71], a[20:23], v[112:115]\n"
"v_max_f32 v253, v157, v159\n"
"v_max3_f32 v254, v132, v134, v254\n"
"v_mfma_f32_16x16x32_bf16 v[112:115], v[72:75], a[24:27], v[112:115]\n"
"v_max3_f32 v253, v133, v135, v253\n"
"v_max_f32 v252, v254, v253\n"
"v_mfma_f32_16x16x32_bf16 v[112:115], v[76:79], a[28:31], v[112:115]\n"
"v_mov_b32 v251, v252\n"
"v_mfma_f32_16x16x32_bf16 v[124:127], v[48:51], a[32:35], 0\n"
"v_permlane32_swap_b32 v251, v252\n"
"v_mfma_f32_16x16x32_bf16 v[124:127], v[52:55], a[36:39], v[124:127]\n"
"ds_write_addtid_b32 v112 offset:512\n"
"global_load_dwordx4 a[212:215], v7, s[56:57] offset:1024 sc1\n"
"v_mfma_f32_16x16x32_bf16 v[124:127], v[56:59], a[40:43], v[124:127]\n"
"ds_write_addtid_b32 v113 offset:5152\n"
"v_max_f32 v252, v251, v252\n"
"v_mfma_f32_16x16x32_bf16 v[124:127], v[60:63], a[44:47], v[124:127]\n"
"ds_write_addtid_b32 v114 offset:9856\n"
"v_mov_b32 v251, v252\n"
"v_mfma_f32_16x16x32_bf16 v[124:127], v[64:67], a[48:51], v[124:127]\n"
"ds_write_addtid_b32 v115 offset:14496\n"
"v_mfma_f32_16x16x32_bf16 v[124:127], v[68:71], a[52:55], v[124:127]\n"
"v_permlane16_swap_b32 v251, v252\n"
"v_mfma_f32_16x16x32_bf16 v[124:127], v[72:75], a[56:59], v[124:127]\n"
"global_load_dwordx4 a[216:219], v8, s[56:57] offset:0 sc1\n"
"v_max_f32 v252, v251, v252\n"
"v_mfma_f32_16x16x32_bf16 v[124:127], v[76:79], a[60:63], v[124:127]\n"
"v_mul_f32 v252, v252, v250\n"
"v_fma_f32 v254, v254, v250, -v252\n"
"v_mfma_f32_16x16x32_bf16 v[116:119], a[64:67], v[48:51], v[116:119]\n"
"v_fma_f32 v253, v253, v250, -v252\n"
"v_mfma_f32_16x16x32_bf16 v[116:119], a[68:71], v[52:55], v[116:119]\n"
"ds_write_addtid_b32 v124 offset:768\n"
"v_mfma_f32_16x16x32_bf16 v[116:119], a[72:75], v[56:59], v[116:119]\n"
"ds_write_addtid_b32 v125 offset:5408\n"
"v_mfma_f32_16x16x32_bf16 v[116:119], a[76:79], v[60:63], v[116:119]\n"
"ds_write_addtid_b32 v126 offset:10112\n"
"global_load_dwordx4 a[220:223], v8, s[56:57] offset:1024 sc1\n"
"v_mfma_f32_16x16x32_bf16 v[116:119], a[80:83], v[64:67], v[116:119]\n"
"ds_write_addtid_b32 v127 offset:14752\n"
"v_mfma_f32_16x16x32_bf16 v[116:119], a[84:87], v[68:71], v[116:119]\n"
"v_exp_f32 v254, v254\n"
"v_mfma_f32_16x16x32_bf16 v[116:119], a[88:91], v[72:75], v[116:119]\n"
"v_exp_f32 v253, v253\n"
"v_mfma_f32_16x16x32_bf16 v[116:119], a[92:95], v[76:79], v[116:119]\n"
"s_add_u32 s58, s50, 0x100000\n"
"v_add_f32 v252, v254, v253\n"
"v_mov_b32 v251, v252\n"
"v_mfma_f32_16x16x32_bf16 v[120:123], a[96:99], v[48:51], v[120:123]\n"
"s_and_b32 s58, s58, 0x3ffffff\n"
"v_permlane32_swap_b32 v251, v252\n"
"v_mfma_f32_16x16x32_bf16 v[120:123], a[100:103], v[52:55], v[120:123]\n"
"s_add_u32 s56, s40, s58\n"
"v_add_f32 v252, v251, v252\n"
"v_mov_b32 v251, v252\n"
"v_mfma_f32_16x16x32_bf16 v[120:123], a[104:107], v[56:59], v[120:123]\n"
"s_addc_u32 s57, s41, 0\n"
"v_permlane16_swap_b32 v251, v252\n"
"v_mfma_f32_16x16x32_bf16 v[120:123], a[108:111], v[60:63], v[120:123]\n"
"s_waitcnt vmcnt(8)\n"
"v_cmp_ne_u64 vcc, s[48:49], v[154:155]\n"
"v_add_f32 v252, v251, v252\n"
"v_mfma_f32_16x16x32_bf16 v[120:123], a[112:115], v[64:67], v[120:123]\n"
"buffer_load_dwordx4 v[128:131], v9, s[40:43], s50 offen sc1\n"
"v_mfma_f32_16x16x32_bf16 v[120:123], a[116:119], v[68:71], v[120:123]\n"
"s_cbranch_vccnz .LQ_4s\n"
".LQ_4r:\n"
"v_mfma_f32_16x16x32_bf16 v[120:123], a[120:123], v[72:75], v[120:123]\n"
"s_add_u32 s50, s50, 0x100000\n"
"v_rcp_f32 v252, v252\n"
"v_mfma_f32_16x16x32_bf16 v[120:123], a[124:127], v[76:79], v[120:123]\n"
"global_load_dwordx4 a[224:227], v5, s[56:57] offset:0 sc1\n"
"v_mul_f32 v254, v254, v252\n"
"v_mfma_f32_16x16x32_bf16 v[48:51], v[80:83], a[64:67], 0\n"
"v_mul_f32 v253, v253, v252\n"
"v_cvt_pk_bf16_f32 v251, v254, v253\n"
"v_mfma_f32_16x16x32_bf16 v[48:51], v[84:87], a[68:71], v[48:51]\n"
"v_mfma_f32_16x16x32_bf16 v[48:51], v[88:91], a[72:75], v[48:51]\n"
"s_waitcnt lgkmcnt(12)\n"
"v_and_b32 v52, v251, v24\n"
"v_and_b32 v53, v251, v25\n"
"v_mfma_f32_16x16x32_bf16 v[48:51], v[92:95], a[76:79], v[48:51]\n"
"v_and_b32 v54, v251, v26\n"
"v_and_b32 v55, v251, v27\n"
"v_mfma_f32_16x16x32_bf16 v[48:51], v[96:99], a[80:83], v[48:51]\n"
"global_load_dwordx4 a[228:231], v5, s[56:57] offset:1024 sc1\n"
"v_mfma_f32_16x16x32_bf16 v[56:59], v[140:143], v[52:55], 0\n"
"v_mul_f32_dpp v75, v137, v44 row_newbcast:3 row_mask:0xf bank_mask:0xf\n"
"v_mov_b32 v254, v136\n"
"v_mfma_f32_16x16x32_bf16 v[60:63], v[148:151], v[52:55], 0\n"
"v_mul_f32_dpp v73, v137, v44 row_newbcast:1 row_mask:0xf bank_mask:0xf\n"
"v_mul_f32_dpp v74, v137, v44 row_newbcast:2 row_mask:0xf bank_mask:0xf\n"
"v_mfma_f32_16x16x32_bf16 v[48:51], v[100:103], a[84:87], v[48:51]\n"
"v_cvt_pk_bf16_f32 v52, v56, v57\n"
"v_cvt_pk_bf16_f32 v53, v58, v59\n"
"v_mfma_f32_16x16x32_bf16 v[48:51], v[104:107], a[88:91], v[48:51]\n"
"v_cvt_pk_bf16_f32 v54, v60, v61\n"
"v_cvt_pk_bf16_f32 v55, v62, v63\n"
"v_mfma_f32_16x16x32_bf16 v[48:51], v[108:111], a[92:95], v[48:51]\n"
"buffer_store_dwordx4 v[52:55], v13, s[40:43], s94 offen sc1\n"
"v_mul_f32_dpp v114, v137, v45 row_newbcast:2 row_mask:0xf bank_mask:0xf\n"
"v_mul_f32_dpp v115, v137, v45 row_newbcast:3 row_mask:0xf bank_mask:0xf\n"
"v_mul_f32_dpp v72, v137, v44 row_newbcast:0 row_mask:0xf bank_mask:0xf\n"
"v_mfma_f32_16x16x32_bf16 v[52:55], v[80:83], a[96:99], 0\n"
"s_waitcnt lgkmcnt(0)\n"
"v_mul_f32_dpp v112, v137, v45 row_newbcast:0 row_mask:0xf bank_mask:0xf\n"
"v_mul_f32_dpp v113, v137, v45 row_newbcast:1 row_mask:0xf bank_mask:0xf\n"
"v_mfma_f32_16x16x32_bf16 v[52:55], v[84:87], a[100:103], v[52:55]\n"
"s_barrier\n"
"v_mfma_f32_16x16x32_bf16 v[52:55], v[88:91], a[104:107], v[52:55]\n"
"ds_write_addtid_b32 v48 offset:1024\n"
"ds_read_b128 v[56:59], v2 offset:1024\n"
"v_mfma_f32_16x16x32_bf16 v[52:55], v[92:95], a[108:111], v[52:55]\n"
"ds_write_addtid_b32 v49 offset:5664\n"
"ds_read_b128 v[60:63], v3 offset:2048\n"
"v_mfma_f32_16x16x32_bf16 v[52:55], v[96:99], a[112:115], v[52:55]\n"
"ds_write_addtid_b32 v50 offset:10368\n"
"global_load_dwordx4 a[232:235], v6, s[56:57] offset:0 sc1\n"
"v_mfma_f32_16x16x32_bf16 v[52:55], v[100:103], a[116:119], v[52:55]\n"
"ds_write_addtid_b32 v51 offset:15008\n"
"ds_read_b128 v[64:67], v4 offset:3072\n"
"v_mfma_f32_16x16x32_bf16 v[52:55], v[104:107], a[120:123], v[52:55]\n"
"ds_read_b128 v[68:71], v2 offset:1280\n"
"ds_read_b128 v[48:51], v3 offset:2304\n"
"v_mfma_f32_16x16x32_bf16 v[52:55], v[108:111], a[124:127], v[52:55]\n"
"ds_read_b128 v[76:79], v4 offset:3328\n"
"v_mfma_f32_16x16x32_bf16 v[124:127], v[80:83], a[0:3], 0\n"
"global_load_dwordx4 a[236:239], v6, s[56:57] offset:1024 sc1\n"
"v_mfma_f32_16x16x32_bf16 v[124:127], v[84:87], a[4:7], v[124:127]\n"
"ds_write_addtid_b32 v52 offset:1280\n"
"v_mfma_f32_16x16x32_bf16 v[124:127], v[88:91], a[8:11], v[124:127]\n"
"ds_write_addtid_b32 v53 offset:5920\n"
"v_mfma_f32_16x16x32_bf16 v[124:127], v[92:95], a[12:15], v[124:127]\n"
"s_waitcnt lgkmcnt(5)\n"
"ds_write_addtid_b32 v54 offset:10624\n"
"v_add_f32 v56, v56, v60\n"
"v_mfma_f32_16x16x32_bf16 v[124:127], v[96:99], a[16:19], v[124:127]\n"
"ds_write_addtid_b32 v55 offset:15264\n"
"v_add_f32 v57, v57, v61\n"
"v_mfma_f32_16x16x32_bf16 v[124:127], v[100:103], a[20:23], v[124:127]\n"
"global_load_dwordx4 a[240:243], v7, s[56:57] offset:0 sc1\n"
"v_add_f32 v58, v58, v62\n"
"v_mfma_f32_16x16x32_bf16 v[124:127], v[104:107], a[24:27], v[124:127]\n"
"v_add_f32 v59, v59, v63\n"
"v_add_f32 v56, v56, v64\n"
"v_mfma_f32_16x16x32_bf16 v[124:127], v[108:111], a[28:31], v[124:127]\n"
"v_add_f32 v57, v57, v65\n"
"v_add_f32 v58, v58, v66\n"
"v_mfma_f32_16x16x32_bf16 v[52:55], v[80:83], a[32:35], 0\n"
"v_add_f32 v59, v59, v67\n"
"v_add_f32 v56, v56, v116\n"
"v_mfma_f32_16x16x32_bf16 v[52:55], v[84:87], a[36:39], v[52:55]\n"
"ds_write_addtid_b32 v124 offset:1536\n"
"v_add_f32 v57, v57, v117\n"
"v_mfma_f32_16x16x32_bf16 v[52:55], v[88:91], a[40:43], v[52:55]\n"
"ds_write_addtid_b32 v125 offset:6176\n"
"global_load_dwordx4 a[244:247], v7, s[56:57] offset:1024 sc1\n"
"v_mfma_f32_16x16x32_bf16 v[52:55], v[92:95], a[44:47], v[52:55]\n"
"ds_write_addtid_b32 v126 offset:10880\n"
"v_add_f32 v58, v58, v118\n"
"v_mfma_f32_16x16x32_bf16 v[52:55], v[96:99], a[48:51], v[52:55]\n"
"ds_write_addtid_b32 v127 offset:15520\n"
"v_add_f32 v59, v59, v119\n"
"v_mfma_f32_16x16x32_bf16 v[52:55], v[100:103], a[52:55], v[52:55]\n"
"v_mul_f32 v56, v255, v56\n"
"v_mul_f32 v57, v255, v57\n"
"v_mfma_f32_16x16x32_bf16 v[52:55], v[104:107], a[56:59], v[52:55]\n"
"v_mul_f32 v58, v255, v58\n"
"v_mul_f32 v59, v255, v59\n"
"v_mfma_f32_16x16x32_bf16 v[52:55], v[108:111], a[60:63], v[52:55]\n"
"global_load_dwordx4 a[248:251], v8, s[56:57] offset:0 sc1\n"
"v_cvt_pk_bf16_f32 v60, v56, v57\n"
"v_mfma_f32_16x16x32_bf16 v[72:75], v[80:83], a[128:131], v[72:75]\n"
"v_cvt_pk_bf16_f32 v61, v58, v59\n"
"v_mfma_f32_16x16x32_bf16 v[72:75], v[84:87], a[132:135], v[72:75]\n"
"ds_write_addtid_b32 v52 offset:1792\n"
"ds_write_addtid_b32 v60 offset:18592\n"
"v_mfma_f32_16x16x32_bf16 v[72:75], v[88:91], a[136:139], v[72:75]\n"
"ds_write_addtid_b32 v53 offset:6432\n"
"ds_write_addtid_b32 v61 offset:18848\n"
"v_mfma_f32_16x16x32_bf16 v[72:75], v[92:95], a[140:143], v[72:75]\n"
"s_waitcnt lgkmcnt(12)\n"
"ds_write_addtid_b32 v54 offset:11136\n"
"v_add_f32 v68, v68, v48\n"
"v_mfma_f32_16x16x32_bf16 v[72:75], v[96:99], a[144:147], v[72:75]\n"
"ds_write_addtid_b32 v55 offset:15776\n"
"global_load_dwordx4 a[252:255], v8, s[56:57] offset:1024 sc1\n"
"v_mfma_f32_16x16x32_bf16 v[72:75], v[100:103], a[148:151], v[72:75]\n"
"v_add_f32 v69, v69, v49\n"
"v_add_f32 v70, v70, v50\n"
"v_mfma_f32_16x16x32_bf16 v[72:75], v[104:107], a[152:155], v[72:75]\n"
"v_add_f32 v71, v71, v51\n"
"v_add_f32 v68, v68, v76\n"
"v_mfma_f32_16x16x32_bf16 v[72:75], v[108:111], a[156:159], v[72:75]\n"
"v_add_f32 v69, v69, v77\n"
"v_add_f32 v70, v70, v78\n"
"v_mfma_f32_16x16x32_bf16 v[112:115], v[80:83], a[160:163], v[112:115]\n"
"s_add_u32 s58, s50, 0x100000\n"
"v_add_f32 v71, v71, v79\n"
"v_add_f32 v68, v68, v120\n"
"v_mfma_f32_16x16x32_bf16 v[112:115], v[84:87], a[164:167], v[112:115]\n"
"s_and_b32 s58, s58, 0x3ffffff\n"
"v_add_f32 v69, v69, v121\n"
"v_add_f32 v70, v70, v122\n"
"v_mfma_f32_16x16x32_bf16 v[112:115], v[88:91], a[168:171], v[112:115]\n"
"s_add_u32 s56, s40, s58\n"
"v_add_f32 v71, v71, v123\n"
"v_mul_f32 v68, v255, v68\n"
"v_mfma_f32_16x16x32_bf16 v[112:115], v[92:95], a[172:175], v[112:115]\n"
"s_addc_u32 s57, s41, 0\n"
"v_mul_f32 v69, v255, v69\n"
"v_mul_f32 v70, v255, v70\n"
"v_mfma_f32_16x16x32_bf16 v[112:115], v[96:99], a[176:179], v[112:115]\n"
"s_waitcnt vmcnt(9)\n"
"v_cmp_ne_u64 vcc, s[48:49], v[130:131]\n"
"v_mul_f32 v71, v255, v71\n"
"v_mfma_f32_16x16x32_bf16 v[112:115], v[100:103], a[180:183], v[112:115]\n"
"buffer_load_dwordx4 v[136:139], v10, s[40:43], s50 offen sc1\n"
"v_mfma_f32_16x16x32_bf16 v[112:115], v[104:107], a[184:187], v[112:115]\n"
"s_cbranch_vccnz .LQ_1s\n"
".LQ_1r:\n"
"v_cvt_pk_bf16_f32 v62, v68, v69\n"
"v_mfma_f32_16x16x32_bf16 v[112:115], v[108:111], a[188:191], v[112:115]\n"
"v_cvt_pk_bf16_f32 v63, v70, v71\n"
"v_mfma_f32_16x16x32_bf16 v[64:67], a[192:195], a[64:67], 0\n"
"s_add_u32 s50, s50, 0x100000\n"
"global_load_dwordx4 v[48:51], v5, s[56:57] offset:0 sc1\n"
"ds_write_addtid_b32 v62 offset:19104\n"
"v_mfma_f32_16x16x32_bf16 v[64:67], a[196:199], a[68:71], v[64:67]\n"
"ds_write_addtid_b32 v63 offset:19360\n"
"v_mfma_f32_16x16x32_bf16 v[64:67], a[200:203], a[72:75], v[64:67]\n"
"v_mfma_f32_16x16x32_bf16 v[64:67], a[204:207], a[76:79], v[64:67]\n"
"v_mul_f32 v250, 0x3e8293ee, v144\n"
"v_mfma_f32_16x16x32_bf16 v[64:67], a[208:211], a[80:83], v[64:67]\n"
"v_mul_f32 v110, v145, v30\n"
"v_mul_f32 v111, v145, v31\n"
"v_mfma_f32_16x16x32_bf16 v[64:67], a[212:215], a[84:87], v[64:67]\n"
"global_load_dwordx4 v[52:55], v5, s[56:57] offset:1024 sc1\n"
"v_mul_f32 v109, v145, v29\n"
"v_mfma_f32_16x16x32_bf16 v[64:67], a[216:219], a[88:91], v[64:67]\n"
"v_mul_f32 v119, v145, v35\n"
"v_mul_f32 v108, v145, v28\n"
"v_mfma_f32_16x16x32_bf16 v[64:67], a[220:223], a[92:95], v[64:67]\n"
"s_waitcnt vmcnt(9)\n"
"buffer_store_dwordx2 v[0:1], v14, s[40:43], s95 offen offset:0 sc1\n"
"v_mul_f32 v118, v145, v34\n"
"v_mfma_f32_16x16x32_bf16 v[68:71], a[192:195], a[96:99], 0\n"
"s_waitcnt lgkmcnt(0)\n"
"v_mul_f32 v116, v145, v32\n"
"v_mul_f32 v117, v145, v33\n"
"v_mfma_f32_16x16x32_bf16 v[68:71], a[196:199], a[100:103], v[68:71]\n"
"s_barrier\n"
"v_mfma_f32_16x16x32_bf16 v[68:71], a[200:203], a[104:107], v[68:71]\n"
"ds_write_addtid_b32 v64 offset:2048\n"
"global_load_dwordx4 v[56:59], v6, s[56:57] offset:0 sc1\n"
"v_mfma_f32_16x16x32_bf16 v[68:71], a[204:207], a[108:111], v[68:71]\n"
"ds_write_addtid_b32 v65 offset:6688\n"
"ds_read_b128 v[76:79], v2 offset:2560\n"
"v_mfma_f32_16x16x32_bf16 v[68:71], a[208:211], a[112:115], v[68:71]\n"
"ds_write_addtid_b32 v66 offset:11392\n"
"ds_read_b128 v[80:83], v3 offset:3584\n"
"v_mfma_f32_16x16x32_bf16 v[68:71], a[212:215], a[116:119], v[68:71]\n"
"ds_write_addtid_b32 v67 offset:16032\n"
"ds_read_b128 v[84:87], v4 offset:0\n"
"v_mfma_f32_16x16x32_bf16 v[68:71], a[216:219], a[120:123], v[68:71]\n"
"ds_read_b128 v[88:91], v2 offset:2816\n"
"ds_read_b128 v[92:95], v3 offset:3840\n"
"v_mfma_f32_16x16x32_bf16 v[68:71], a[220:223], a[124:127], v[68:71]\n"
"global_load_dwordx4 v[60:63], v6, s[56:57] offset:1024 sc1\n"
"ds_read_b128 v[96:99], v4 offset:256\n"
"v_mfma_f32_16x16x32_bf16 v[100:103], a[128:131], a[192:195], 0\n"
"v_mfma_f32_16x16x32_bf16 v[100:103], a[132:135], a[196:199], v[100:103]\n"
"ds_write_addtid_b32 v68 offset:2304\n"
"v_mfma_f32_16x16x32_bf16 v[100:103], a[136:139], a[200:203], v[100:103]\n"
"ds_write_addtid_b32 v69 offset:6944\n"
"v_mfma_f32_16x16x32_bf16 v[100:103], a[140:143], a[204:207], v[100:103]\n"
"s_waitcnt lgkmcnt(5)\n"
"ds_write_addtid_b32 v70 offset:11648\n"
"v_add_f32 v76, v76, v80\n"
"v_mfma_f32_16x16x32_bf16 v[100:103], a[144:147], a[208:211], v[100:103]\n"
"ds_write_addtid_b32 v71 offset:16288\n"
"global_load_dwordx4 v[64:67], v7, s[56:57] offset:0 sc1\n"
"v_mfma_f32_16x16x32_bf16 v[100:103], a[148:151], a[212:215], v[100:103]\n"
"v_add_f32 v77, v77, v81\n"
"v_add_f32 v78, v78, v82\n"
"v_mfma_f32_16x16x32_bf16 v[100:103], a[152:155], a[216:219], v[100:103]\n"
"v_add_f32 v79, v79, v83\n"
"v_add_f32 v76, v76, v84\n"
"v_mfma_f32_16x16x32_bf16 v[100:103], a[156:159], a[220:223], v[100:103]\n"
"v_add_f32 v77, v77, v85\n"
"v_add_f32 v78, v78, v86\n"
"v_mfma_f32_16x16x32_bf16 v[80:83], a[160:163], a[192:195], 0\n"
"v_add_f32 v79, v79, v87\n"
"v_add_f32 v76, v76, v72\n"
"v_mfma_f32_16x16x32_bf16 v[80:83], a[164:167], a[196:199], v[80:83]\n"
"ds_write_addtid_b32 v100 offset:2560\n"
"global_load_dwordx4 v[68:71], v7, s[56:57] offset:1024 sc1\n"
"v_mfma_f32_16x16x32_bf16 v[80:83], a[168:171], a[200:203], v[80:83]\n"
"ds_write_addtid_b32 v101 offset:7200\n"
"v_add_f32 v77, v77, v73\n"
"v_mfma_f32_16x16x32_bf16 v[80:83], a[172:175], a[204:207], v[80:83]\n"
"ds_write_addtid_b32 v102 offset:11904\n"
"v_add_f32 v78, v78, v74\n"
"v_mfma_f32_16x16x32_bf16 v[80:83], a[176:179], a[208:211], v[80:83]\n"
"ds_write_addtid_b32 v103 offset:16544\n"
"v_add_f32 v79, v79, v75\n"
"v_mfma_f32_16x16x32_bf16 v[80:83], a[180:183], a[212:215], v[80:83]\n"
"v_mul_f32_dpp v76, v254, v76 row_newbcast:0 row_mask:0xf bank_mask:0xf\n"
"v_mul_f32_dpp v77, v254, v77 row_newbcast:1 row_mask:0xf bank_mask:0xf\n"
"v_mfma_f32_16x16x32_bf16 v[80:83], a[184:187], a[216:219], v[80:83]\n"
"global_load_dwordx4 v[72:75], v8, s[56:57] offset:0 sc1\n"
"v_mul_f32_dpp v78, v254, v78 row_newbcast:2 row_mask:0xf bank_mask:0xf\n"
"v_mfma_f32_16x16x32_bf16 v[80:83], a[188:191], a[220:223], v[80:83]\n"
"v_mul_f32_dpp v79, v254, v79 row_newbcast:3 row_mask:0xf bank_mask:0xf\n"
"v_cvt_pk_bf16_f32 v142, v76, v77\n"
"v_mfma_f32_16x16x32_bf16 v[108:111], a[0:3], a[192:195], v[108:111]\n"
"v_cvt_pk_bf16_f32 v143, v78, v79\n"
"v_mfma_f32_16x16x32_bf16 v[108:111], a[4:7], a[196:199], v[108:111]\n"
"ds_write_addtid_b32 v80 offset:2816\n"
"ds_write_addtid_b32 v142 offset:19616\n"
"v_mfma_f32_16x16x32_bf16 v[108:111], a[8:11], a[200:203], v[108:111]\n"
"ds_write_addtid_b32 v81 offset:7456\n"
"ds_write_addtid_b32 v143 offset:19872\n"
"v_mfma_f32_16x16x32_bf16 v[108:111], a[12:15], a[204:207], v[108:111]\n"
"s_waitcnt lgkmcnt(12)\n"
"ds_write_addtid_b32 v82 offset:12160\n"
"global_load_dwordx4 v[76:79], v8, s[56:57] offset:1024 sc1\n"
"v_mfma_f32_16x16x32_bf16 v[108:111], a[16:19], a[208:211], v[108:111]\n"
"ds_write_addtid_b32 v83 offset:16800\n"
"v_add_f32 v88, v88, v92\n"
"v_mfma_f32_16x16x32_bf16 v[108:111], a[20:23], a[212:215], v[108:111]\n"
"v_add_f32 v89, v89, v93\n"
"v_add_f32 v90, v90, v94\n"
"v_mfma_f32_16x16x32_bf16 v[108:111], a[24:27], a[216:219], v[108:111]\n"
"v_add_f32 v91, v91, v95\n"
"v_add_f32 v88, v88, v96\n"
"v_mfma_f32_16x16x32_bf16 v[108:111], a[28:31], a[220:223], v[108:111]\n"
"v_add_f32 v89, v89, v97\n"
"v_add_f32 v90, v90, v98\n"
"v_mfma_f32_16x16x32_bf16 v[116:119], a[32:35], a[192:195], v[116:119]\n"
"v_add_f32 v91, v91, v99\n"
"v_add_f32 v88, v88, v112\n"
"v_mfma_f32_16x16x32_bf16 v[116:119], a[36:39], a[196:199], v[116:119]\n"
"v_add_f32 v89, v89, v113\n"
"v_add_f32 v90, v90, v114\n"
"v_mfma_f32_16x16x32_bf16 v[116:119], a[40:43], a[200:203], v[116:119]\n"
"s_add_u32 s58, s50, 0x100000\n"
"buffer_load_dwordx4 v[144:147], v9, s[40:43], s50 offen sc1\n"
"v_mfma_f32_16x16x32_bf16 v[116:119], a[44:47], a[204:207], v[116:119]\n"
"s_and_b32 s58, s58, 0x3ffffff\n"
"v_add_f32 v91, v91, v115\n"
"v_mul_f32_dpp v88, v254, v88 row_newbcast:0 row_mask:0xf bank_mask:0xf\n"
"v_mfma_f32_16x16x32_bf16 v[116:119], a[48:51], a[208:211], v[116:119]\n"
"s_add_u32 s56, s40, s58\n"
"v_mul_f32_dpp v89, v254, v89 row_newbcast:1 row_mask:0xf bank_mask:0xf\n"
"v_mul_f32_dpp v90, v254, v90 row_newbcast:2 row_mask:0xf bank_mask:0xf\n"
"v_mfma_f32_16x16x32_bf16 v[116:119], a[52:55], a[212:215], v[116:119]\n"
"s_addc_u32 s57, s41, 0\n"
"v_mul_f32_dpp v91, v254, v91 row_newbcast:3 row_mask:0xf bank_mask:0xf\n"
"v_cvt_pk_bf16_f32 v150, v88, v89\n"
"v_mfma_f32_16x16x32_bf16 v[116:119], a[56:59], a[216:219], v[116:119]\n"
"s_waitcnt vmcnt(10)\n"
"v_cmp_ne_u64 vcc, s[48:49], v[138:139]\n"
"v_cvt_pk_bf16_f32 v151, v90, v91\n"
"v_mfma_f32_16x16x32_bf16 v[116:119], a[60:63], a[220:223], v[116:119]\n"
"ds_write_addtid_b32 v150 offset:20128\n"
"ds_write_addtid_b32 v151 offset:20384\n"
"v_mfma_f32_16x16x32_bf16 v[92:95], a[224:227], a[64:67], 0\n"
"s_cbranch_vccnz .LQ_2s\n"
".LQ_2r:\n"
"v_mfma_f32_16x16x32_bf16 v[92:95], a[228:231], a[68:71], v[92:95]\n"
"global_load_dwordx4 v[80:83], v5, s[56:57] offset:0 sc1\n"
"v_mfma_f32_16x16x32_bf16 v[92:95], a[232:235], a[72:75], v[92:95]\n"
"s_add_u32 s50, s50, 0x100000\n"
"v_mfma_f32_16x16x32_bf16 v[92:95], a[236:239], a[76:79], v[92:95]\n"
"v_mfma_f32_16x16x32_bf16 v[92:95], a[240:243], a[80:83], v[92:95]\n"
"v_mfma_f32_16x16x32_bf16 v[92:95], a[244:247], a[84:87], v[92:95]\n"
"v_mfma_f32_16x16x32_bf16 v[92:95], a[248:251], a[88:91], v[92:95]\n"
"global_load_dwordx4 v[84:87], v5, s[56:57] offset:1024 sc1\n"
"v_mfma_f32_16x16x32_bf16 v[92:95], a[252:255], a[92:95], v[92:95]\n"
"v_mfma_f32_16x16x32_bf16 v[100:103], a[224:227], a[96:99], 0\n"
"v_mfma_f32_16x16x32_bf16 v[100:103], a[228:231], a[100:103], v[100:103]\n"
"ds_write_addtid_b32 v92 offset:3072\n"
"v_mfma_f32_16x16x32_bf16 v[100:103], a[232:235], a[104:107], v[100:103]\n"
"s_waitcnt lgkmcnt(1)\n"
"ds_write_addtid_b32 v93 offset:7712\n"
"v_mfma_f32_16x16x32_bf16 v[100:103], a[236:239], a[108:111], v[100:103]\n"
"s_barrier\n"
"v_mfma_f32_16x16x32_bf16 v[100:103], a[240:243], a[112:115], v[100:103]\n"
"ds_write_addtid_b32 v94 offset:12416\n"
"ds_read_b128 v[112:115], v2 offset:4096\n"
"v_mfma_f32_16x16x32_bf16 v[100:103], a[244:247], a[116:119], v[100:103]\n"
"ds_write_addtid_b32 v95 offset:17056\n"
"global_load_dwordx4 v[88:91], v6, s[56:57] offset:0 sc1\n"
"v_mfma_f32_16x16x32_bf16 v[100:103], a[248:251], a[120:123], v[100:103]\n"
"ds_read_b128 v[104:107], v3 offset:512\n"
"ds_read_b128 v[120:123], v4 offset:1536\n"
"v_mfma_f32_16x16x32_bf16 v[100:103], a[252:255], a[124:127], v[100:103]\n"
"global_load_dwordx4 v[92:95], v6, s[56:57] offset:1024 sc1\n"
"ds_read_b128 v[124:127], v2 offset:4352\n"
"v_mfma_f32_16x16x32_bf16 v[132:135], a[128:131], a[224:227], 0\n"
"ds_read_b128 v[152:155], v3 offset:768\n"
"ds_read_b128 v[156:159], v4 offset:1792\n"
"v_mfma_f32_16x16x32_bf16 v[132:135], a[132:135], a[228:231], v[132:135]\n"
"ds_write_addtid_b32 v100 offset:3328\n"
"v_mfma_f32_16x16x32_bf16 v[132:135], a[136:139], a[232:235], v[132:135]\n"
"ds_write_addtid_b32 v101 offset:7968\n"
"v_mfma_f32_16x16x32_bf16 v[132:135], a[140:143], a[236:239], v[132:135]\n"
"ds_write_addtid_b32 v102 offset:12672\n"
"v_mfma_f32_16x16x32_bf16 v[132:135], a[144:147], a[240:243], v[132:135]\n"
"s_waitcnt lgkmcnt(6)\n"
"ds_write_addtid_b32 v103 offset:17312\n"
"global_load_dwordx4 v[96:99], v7, s[56:57] offset:0 sc1\n"
"v_mfma_f32_16x16x32_bf16 v[132:135], a[148:151], a[244:247], v[132:135]\n"
"v_add_f32 v112, v112, v104\n"
"v_add_f32 v113, v113, v105\n"
"v_mfma_f32_16x16x32_bf16 v[132:135], a[152:155], a[248:251], v[132:135]\n"
"v_add_f32 v114, v114, v106\n"
"v_add_f32 v115, v115, v107\n"
"v_mfma_f32_16x16x32_bf16 v[132:135], a[156:159], a[252:255], v[132:135]\n"
"v_add_f32 v112, v112, v120\n"
"v_add_f32 v113, v113, v121\n"
"v_mfma_f32_16x16x32_bf16 v[160:163], a[160:163], a[224:227], 0\n"
"v_add_f32 v114, v114, v122\n"
"v_add_f32 v115, v115, v123\n"
"v_mfma_f32_16x16x32_bf16 v[160:163], a[164:167], a[228:231], v[160:163]\n"
"ds_write_addtid_b32 v132 offset:3584\n"
"global_load_dwordx4 v[100:103], v7, s[56:57] offset:1024 sc1\n"
"v_mfma_f32_16x16x32_bf16 v[160:163], a[168:171], a[232:235], v[160:163]\n"
"ds_write_addtid_b32 v133 offset:8224\n"
"v_add_f32 v112, v112, v108\n"
"v_mfma_f32_16x16x32_bf16 v[160:163], a[172:175], a[236:239], v[160:163]\n"
"ds_write_addtid_b32 v134 offset:12928\n"
"v_add_f32 v113, v113, v109\n"
"v_mfma_f32_16x16x32_bf16 v[160:163], a[176:179], a[240:243], v[160:163]\n"
"ds_write_addtid_b32 v135 offset:17568\n"
"v_add_f32 v114, v114, v110\n"
"v_mfma_f32_16x16x32_bf16 v[160:163], a[180:183], a[244:247], v[160:163]\n"
"v_add_f32 v115, v115, v111\n"
"v_cvt_pk_bf16_f32 v164, v112, v113\n"
"v_mfma_f32_16x16x32_bf16 v[160:163], a[184:187], a[248:251], v[160:163]\n"
"global_load_dwordx4 v[104:107], v8, s[56:57] offset:0 sc1\n"
"v_cvt_pk_bf16_f32 v165, v114, v115\n"
"v_mfma_f32_16x16x32_bf16 v[160:163], a[188:191], a[252:255], v[160:163]\n"
"ds_read2st64_b32 v[120:121], v11 offset0:0 offset1:1\n"
"ds_read2st64_b32 v[122:123], v11 offset0:2 offset1:3\n"
"v_mfma_f32_16x16x32_bf16 v[112:115], a[224:227], a[0:3], 0\n"
"s_waitcnt lgkmcnt(10)\n"
"v_add_f32 v124, v124, v152\n"
"v_add_f32 v125, v125, v153\n"
"v_mfma_f32_16x16x32_bf16 v[112:115], a[228:231], a[4:7], v[112:115]\n"
"ds_write_addtid_b32 v160 offset:3840\n"
"v_add_f32 v126, v126, v154\n"
"v_mfma_f32_16x16x32_bf16 v[112:115], a[232:235], a[8:11], v[112:115]\n"
"ds_write_addtid_b32 v161 offset:8480\n"
"v_add_f32 v127, v127, v155\n"
"v_mfma_f32_16x16x32_bf16 v[112:115], a[236:239], a[12:15], v[112:115]\n"
"ds_write_addtid_b32 v162 offset:13184\n"
"global_load_dwordx4 v[108:111], v8, s[56:57] offset:1024 sc1\n"
"v_mfma_f32_16x16x32_bf16 v[112:115], a[240:243], a[16:19], v[112:115]\n"
"ds_write_addtid_b32 v163 offset:17824\n"
"v_add_f32 v124, v124, v156\n"
"v_mfma_f32_16x16x32_bf16 v[112:115], a[244:247], a[20:23], v[112:115]\n"
"v_add_f32 v125, v125, v157\n"
"v_add_f32 v126, v126, v158\n"
"v_mfma_f32_16x16x32_bf16 v[112:115], a[248:251], a[24:27], v[112:115]\n"
"v_add_f32 v127, v127, v159\n"
"v_add_f32 v124, v124, v116\n"
"v_mfma_f32_16x16x32_bf16 v[112:115], a[252:255], a[28:31], v[112:115]\n"
"v_add_f32 v125, v125, v117\n"
"v_add_f32 v126, v126, v118\n"
"v_mfma_f32_16x16x32_bf16 v[132:135], a[224:227], a[32:35], 0\n"
"s_add_u32 s58, s50, 0x100000\n"
"buffer_load_dwordx4 v[152:155], v9, s[40:43], s50 offen sc1\n"
"v_mfma_f32_16x16x32_bf16 v[132:135], a[228:231], a[36:39], v[132:135]\n"
"s_and_b32 s58, s58, 0x3ffffff\n"
"ds_write_addtid_b32 v112 offset:4096\n"
"v_add_f32 v127, v127, v119\n"
"v_mfma_f32_16x16x32_bf16 v[132:135], a[232:235], a[40:43], v[132:135]\n"
"s_add_u32 s56, s40, s58\n"
"ds_write_addtid_b32 v113 offset:8736\n"
"v_cvt_pk_bf16_f32 v166, v124, v125\n"
"v_mfma_f32_16x16x32_bf16 v[132:135], a[236:239], a[44:47], v[132:135]\n"
"s_addc_u32 s57, s41, 0\n"
"ds_write_addtid_b32 v114 offset:13440\n"
"v_cvt_pk_bf16_f32 v167, v126, v127\n"
"v_mfma_f32_16x16x32_bf16 v[132:135], a[240:243], a[48:51], v[132:135]\n"
"s_waitcnt vmcnt(9)\n"
"ds_write_addtid_b32 v115 offset:18080\n"
"v_cmp_ne_u64 vcc, s[48:49], v[146:147]\n"
"v_mfma_f32_16x16x32_bf16 v[132:135], a[244:247], a[52:55], v[132:135]\n"
"s_waitcnt lgkmcnt(8)\n"
"v_mfma_f32_16x16x32_bf16 v[156:159], v[120:123], v[164:167], v[20:23]\n"
"v_mfma_f32_16x16x32_bf16 v[132:135], a[248:251], a[56:59], v[132:135]\n"
"s_cbranch_vccnz .LQ_3s\n"
".LQ_3r:\n"
"v_mfma_f32_16x16x32_bf16 v[132:135], a[252:255], a[60:63], v[132:135]\n"
"s_branch .LQ_top\n"
".LQ_1s:\n"
"s_cmp_ge_u32 s50, s53\n"
"s_cbranch_scc1 .LQ_end\n"
"s_cmp_ge_u32 s50, s52\n"
"s_cbranch_scc1 .LQ_1r\n"
"s_sub_u32 s60, s50, 0x100000\n"
"s_mov_b32 s61, 200000\n"
"s_nop 7\n"
".LQ_1p:\n"
"buffer_load_dwordx4 v[128:131], v9, s[40:43], s60 offen sc1\n"
"s_waitcnt vmcnt(0)\n"
"v_cmp_ne_u64 vcc, s[48:49], v[130:131]\n"
"s_cbranch_vccz .LQ_1px\n"
"s_sleep 1\n"
"s_sub_u32 s61, s61, 1\n"
"s_cbranch_scc0 .LQ_1p\n"
"s_branch .LQ_end\n"
".LQ_1px:\n"
"s_branch .LQ_1r\n"
".LQ_2s:\n"
"s_cmp_ge_u32 s50, s53\n"
"s_cbranch_scc1 .LQ_end\n"
"s_cmp_ge_u32 s50, s52\n"
"s_cbranch_scc1 .LQ_2r\n"
"s_sub_u32 s60, s50, 0x100000\n"
"s_mov_b32 s61, 200000\n"
"s_nop 7\n"
".LQ_2p:\n"
"buffer_load_dwordx4 v[136:139], v10, s[40:43], s60 offen sc1\n"
"s_waitcnt vmcnt(0)\n"
"v_cmp_ne_u64 vcc, s[48:49], v[138:139]\n"
"s_cbranch_vccz .LQ_2px\n"
"s_sleep 1\n"
"s_sub_u32 s61, s61, 1\n"
"s_cbranch_scc0 .LQ_2p\n"
"s_branch .LQ_end\n"
".LQ_2px:\n"
"s_branch .LQ_2r\n"
".LQ_3s:\n"
"s_cmp_ge_u32 s50, s53\n"
"s_cbranch_scc1 .LQ_end\n"
"s_cmp_ge_u32 s50, s52\n"
"s_cbranch_scc1 .LQ_3r\n"
"s_sub_u32 s60, s50, 0x100000\n"
"s_mov_b32 s61, 200000\n"
"s_nop 7\n"
".LQ_3p:\n"
"buffer_load_dwordx4 v[144:147], v9, s[40:43], s60 offen sc1\n"
"s_waitcnt vmcnt(0)\n"
"v_cmp_ne_u64 vcc, s[48:49], v[146:147]\n"
"s_cbranch_vccz .LQ_3px\n"
"s_sleep 1\n"
"s_sub_u32 s61, s61, 1\n"
"s_cbranch_scc0 .LQ_3p\n"
"s_branch .LQ_end\n"
".LQ_3px:\n"
"s_branch .LQ_3r\n"
".LQ_4s:\n"
"s_cmp_ge_u32 s50, s53\n"
"s_cbranch_scc1 .LQ_end\n"
"s_cmp_ge_u32 s50, s52\n"
"s_cbranch_scc1 .LQ_4r\n"
"s_sub_u32 s60, s50, 0x100000\n"
"s_mov_b32 s61, 200000\n"
"s_nop 7\n"
".LQ_4p:\n"
"buffer_load_dwordx4 v[152:155], v9, s[40:43], s60 offen sc1\n"
"s_waitcnt vmcnt(0)\n"
"v_cmp_ne_u64 vcc, s[48:49], v[154:155]\n"
"s_cbranch_vccz .LQ_4px\n"
"s_sleep 1\n"
"s_sub_u32 s61, s61, 1\n"
"s_cbranch_scc0 .LQ_4p\n"
"s_branch .LQ_end\n"
".LQ_4px:\n"
"s_branch .LQ_4r\n"
".LQ_fs:\n"
"s_nop 7\n"
"v_mov_b32 v156, 0xff800000\n"
"v_mov_b32 v157, 0xff800000\n"
"v_mov_b32 v158, 0xff800000\n"
"v_mov_b32 v159, 0xff800000\n"
"v_mov_b32 v142, 0\n"
"v_mov_b32 v143, 0\n"
"v_mov_b32 v150, 0\n"
"v_mov_b32 v151, 0\n"
"s_add_u32 s62, s62, 0x4000000\n"
"s_branch .LQ_fr\n"
".LQ_end:\n"
"s_waitcnt vmcnt(0) lgkmcnt(0)\n"
"s_endpgm\n"
      :: "{v2}"((unsigned)(lds0 + ((wid + 3) & 3) * 20640 + trp)), "{v3}"((unsigned)(lds0 + ((wid + 2) & 3) * 20640 + trp)), "{v4}"((unsigned)(lds0 + ((wid + 1) & 3) * 20640 + trp)), "{v5}"((unsigned)(lane * 16 + wid * 8192 + ((0 + rot) % 4) * 2048 + 134217728)), "{v6}"((unsigned)(lane * 16 + wid * 8192 + ((1 + rot) % 4) * 2048 + 134217728)), "{v7}"((unsigned)(lane * 16 + wid * 8192 + ((2 + rot) % 4) * 2048 + 134217728)), "{v8}"((unsigned)(lane * 16 + wid * 8192 + ((3 + rot) % 4) * 2048 + 134217728)), "{v9}"((unsigned)(li * 16 + 352256 + Q_GL * 1048576)), "{v10}"((unsigned)(((li + 4 * lg) & 15) * 16 + 352256 + Q_GL * 1048576)), "{v11}"((unsigned)(lds0 + ((wid + 1) & 3) * 20640 + 18592 + lane * 4)), "{v12}"((unsigned)(lds0 + ((wid + 2) & 3) * 20640 + 18592 + lane * 4)), "{v13}"((unsigned)(lane * 16 + idx * 1024 + 32768)), "{v14}"((unsigned)(lane ? DROP : idx * 8 + 352512)), "{v16}"((unsigned)(qmask(0, 0))), "{v17}"((unsigned)(qmask(1, 0))), "{v18}"((unsigned)(qmask(2, 0))), "{v19}"((unsigned)(qmask(3, 0))), "{v20}"((unsigned)(qmask(0, 1))), "{v21}"((unsigned)(qmask(1, 1))), "{v22}"((unsigned)(qmask(2, 1))), "{v23}"((unsigned)(qmask(3, 1))), "{v24}"((unsigned)(pmask(0))), "{v25}"((unsigned)(pmask(1))), "{v26}"((unsigned)(pmask(2))), "{v27}"((unsigned)(pmask(3))), "{v48}"((unsigned)(vT + tsw)), "{v49}"((unsigned)(vT + (tsw ^ 8))), "{v50}"((unsigned)((sEnt == 1 ? (li + 4 * lg) & 15 : li) * 16 + 1400832)), "{v51}"((unsigned)((sEnt == 0 ? (li + 4 * lg) & 15 : li) * 16 + 2449408)), "{v52}"((unsigned)(lane * 16 + wid * 8192 + (idx & 7) * 1048576 + 134221824)), "{v53}"((unsigned)(vD + ((0 + rot) % 4) * 393216)), "{v54}"((unsigned)(vD + ((1 + rot) % 4) * 393216)), "{v55}"((unsigned)(vD + ((2 + rot) % 4) * 393216)), "{v56}"((unsigned)(vD + ((3 + rot) % 4) * 393216)), "{v57}"((unsigned)(lds0 + wid * 20640 + trp)), "{s[40:43]}"(rS), "{s[44:47]}"(rW), "{s[48:49]}"(wsp), "{s50}"((unsigned)(sA)), "{s51}"((unsigned)(sLo)), "{s52}"((unsigned)(sHi)), "{s53}"((unsigned)(sEx)), "{s54}"((unsigned)(sEnt)), "{s55}"((unsigned)(sLd)), "{s[56:57]}"(wsb + (sLo & 0x3ffffff)), "{s59}"((unsigned)(lds0 + wid * 20640)), "{s62}"((unsigned)(sLo)), "{s63}"((unsigned)(sW0 + 0)), "{s64}"((unsigned)(sW0 + 2048)), "{s65}"((unsigned)(sW0 + 4096))
      : "v255", "a255", "s95", "vcc", "scc", "memory");

  }
  while (__builtin_amdgcn_s_memrealtime() - tz < (role == 1 ? DLO : role == 2 ? DLF : DLG)) __builtin_amdgcn_s_sleep(4);
  const unsigned sA = sLo - 3 * RS, sHi = sLo + (set ? 84 : 85) * 3 * RS;
  if (role == 1) {
    const unsigned sEx = sHi + O_EX * 3 * RS, sEnt = OR ? -wid & 3 : 0, tsw = 0, pair = idx * 4 + wid, rot = 0;
    const unsigned sW0 = wid * 256 * 2048 + idx * 256, vD = (lane >> 2) * 2048 + (lane & 3) * 16;
    const u32x4 rW = desc(a.wo, 1024 * 2048);
    asm volatile(
"s_nop 4\n"
"v_mov_b32 v186, 0\n"
"global_load_dwordx2 v[0:1], v186, s[48:49] sc1\n"
"v_mov_b32 v188, 0x3f803f80\n"
"v_mov_b32 v189, 0x3f803f80\n"
"v_mov_b32 v190, 0x3f803f80\n"
"v_mov_b32 v191, 0x3f803f80\n"
"s_add_u32 m0, s55, 0\n"
"s_add_u32 s60, s66, 0\n"
"buffer_load_dwordx4 v22, s[44:47], s60 offen lds\n"
"s_add_u32 m0, s55, 1024\n"
"s_add_u32 s60, s66, 32768\n"
"buffer_load_dwordx4 v22, s[44:47], s60 offen lds\n"
"s_add_u32 m0, s55, 2048\n"
"s_add_u32 s60, s67, 0\n"
"buffer_load_dwordx4 v22, s[44:47], s60 offen lds\n"
"s_add_u32 m0, s55, 3072\n"
"s_add_u32 s60, s67, 32768\n"
"buffer_load_dwordx4 v22, s[44:47], s60 offen lds\n"
"s_add_u32 m0, s55, 4096\n"
"s_add_u32 s60, s68, 0\n"
"buffer_load_dwordx4 v22, s[44:47], s60 offen lds\n"
"s_add_u32 m0, s55, 5120\n"
"s_add_u32 s60, s68, 32768\n"
"buffer_load_dwordx4 v22, s[44:47], s60 offen lds\n"
"s_add_u32 m0, s55, 6144\n"
"s_add_u32 s60, s69, 0\n"
"buffer_load_dwordx4 v22, s[44:47], s60 offen lds\n"
"s_add_u32 m0, s55, 7168\n"
"s_add_u32 s60, s69, 32768\n"
"buffer_load_dwordx4 v22, s[44:47], s60 offen lds\n"
"s_add_u32 m0, s55, 8192\n"
"s_add_u32 s60, s66, 65536\n"
"buffer_load_dwordx4 v22, s[44:47], s60 offen lds\n"
"s_add_u32 m0, s55, 9216\n"
"s_add_u32 s60, s66, 98304\n"
"buffer_load_dwordx4 v22, s[44:47], s60 offen lds\n"
"s_add_u32 m0, s55, 10240\n"
"s_add_u32 s60, s67, 65536\n"
"buffer_load_dwordx4 v22, s[44:47], s60 offen lds\n"
"s_add_u32 m0, s55, 11264\n"
"s_add_u32 s60, s67, 98304\n"
"buffer_load_dwordx4 v22, s[44:47], s60 offen lds\n"
"s_add_u32 m0, s55, 12288\n"
"s_add_u32 s60, s68, 65536\n"
"buffer_load_dwordx4 v22, s[44:47], s60 offen lds\n"
"s_add_u32 m0, s55, 13312\n"
"s_add_u32 s60, s68, 98304\n"
"buffer_load_dwordx4 v22, s[44:47], s60 offen lds\n"
"s_add_u32 m0, s55, 14336\n"
"s_add_u32 s60, s69, 65536\n"
"buffer_load_dwordx4 v22, s[44:47], s60 offen lds\n"
"s_add_u32 m0, s55, 15360\n"
"s_add_u32 s60, s69, 98304\n"
"buffer_load_dwordx4 v22, s[44:47], s60 offen lds\n"
"s_add_u32 m0, s55, 16384\n"
"s_add_u32 s60, s66, 0\n"
"buffer_load_dwordx4 v23, s[44:47], s60 offen lds\n"
"s_add_u32 m0, s55, 17408\n"
"s_add_u32 s60, s66, 32768\n"
"buffer_load_dwordx4 v23, s[44:47], s60 offen lds\n"
"s_add_u32 m0, s55, 18432\n"
"s_add_u32 s60, s67, 0\n"
"buffer_load_dwordx4 v23, s[44:47], s60 offen lds\n"
"s_add_u32 m0, s55, 19456\n"
"s_add_u32 s60, s67, 32768\n"
"buffer_load_dwordx4 v23, s[44:47], s60 offen lds\n"
"s_add_u32 m0, s55, 20480\n"
"s_add_u32 s60, s68, 0\n"
"buffer_load_dwordx4 v23, s[44:47], s60 offen lds\n"
"s_add_u32 m0, s55, 21504\n"
"s_add_u32 s60, s68, 32768\n"
"buffer_load_dwordx4 v23, s[44:47], s60 offen lds\n"
"s_add_u32 m0, s55, 22528\n"
"s_add_u32 s60, s69, 0\n"
"buffer_load_dwordx4 v23, s[44:47], s60 offen lds\n"
"s_add_u32 m0, s55, 23552\n"
"s_add_u32 s60, s69, 32768\n"
"buffer_load_dwordx4 v23, s[44:47], s60 offen lds\n"
"s_add_u32 m0, s55, 24576\n"
"s_add_u32 s60, s66, 65536\n"
"buffer_load_dwordx4 v23, s[44:47], s60 offen lds\n"
"s_add_u32 m0, s55, 25600\n"
"s_add_u32 s60, s66, 98304\n"
"buffer_load_dwordx4 v23, s[44:47], s60 offen lds\n"
"s_add_u32 m0, s55, 26624\n"
"s_add_u32 s60, s67, 65536\n"
"buffer_load_dwordx4 v23, s[44:47], s60 offen lds\n"
"s_add_u32 m0, s55, 27648\n"
"s_add_u32 s60, s67, 98304\n"
"buffer_load_dwordx4 v23, s[44:47], s60 offen lds\n"
"s_add_u32 m0, s55, 28672\n"
"s_add_u32 s60, s68, 65536\n"
"buffer_load_dwordx4 v23, s[44:47], s60 offen lds\n"
"s_add_u32 m0, s55, 29696\n"
"s_add_u32 s60, s68, 98304\n"
"buffer_load_dwordx4 v23, s[44:47], s60 offen lds\n"
"s_add_u32 m0, s55, 30720\n"
"s_add_u32 s60, s69, 65536\n"
"buffer_load_dwordx4 v23, s[44:47], s60 offen lds\n"
"s_add_u32 m0, s55, 31744\n"
"s_add_u32 s60, s69, 98304\n"
"buffer_load_dwordx4 v23, s[44:47], s60 offen lds\n"
"s_add_u32 m0, s55, 32768\n"
"s_add_u32 s60, s66, 0\n"
"buffer_load_dwordx4 v24, s[44:47], s60 offen lds\n"
"s_add_u32 m0, s55, 33792\n"
"s_add_u32 s60, s66, 32768\n"
"buffer_load_dwordx4 v24, s[44:47], s60 offen lds\n"
"s_add_u32 m0, s55, 34816\n"
"s_add_u32 s60, s67, 0\n"
"buffer_load_dwordx4 v24, s[44:47], s60 offen lds\n"
"s_add_u32 m0, s55, 35840\n"
"s_add_u32 s60, s67, 32768\n"
"buffer_load_dwordx4 v24, s[44:47], s60 offen lds\n"
"s_add_u32 m0, s55, 36864\n"
"s_add_u32 s60, s68, 0\n"
"buffer_load_dwordx4 v24, s[44:47], s60 offen lds\n"
"s_add_u32 m0, s55, 37888\n"
"s_add_u32 s60, s68, 32768\n"
"buffer_load_dwordx4 v24, s[44:47], s60 offen lds\n"
"s_add_u32 m0, s55, 38912\n"
"s_add_u32 s60, s69, 0\n"
"buffer_load_dwordx4 v24, s[44:47], s60 offen lds\n"
"s_add_u32 m0, s55, 39936\n"
"s_add_u32 s60, s69, 32768\n"
"buffer_load_dwordx4 v24, s[44:47], s60 offen lds\n"
"s_waitcnt vmcnt(38)\n"
"ds_read_b64_tr_b16 a[0:1], v20 offset:0\n"
"ds_read_b64_tr_b16 a[2:3], v20 offset:256\n"
"ds_read_b64_tr_b16 a[32:33], v21 offset:0\n"
"ds_read_b64_tr_b16 a[34:35], v21 offset:256\n"
"v_add_u32 v0, 2, v0\n"
"v_add_u32 v1, 0x9e3779b9, v1\n"
"v_or_b32 v0, 1, v0\n"
"v_readfirstlane_b32 s49, v1\n"
"v_readfirstlane_b32 s48, v0\n"
"s_waitcnt vmcnt(36)\n"
"ds_read_b64_tr_b16 a[64:65], v20 offset:2048\n"
"ds_read_b64_tr_b16 a[66:67], v20 offset:2304\n"
"ds_read_b64_tr_b16 a[96:97], v21 offset:2048\n"
"ds_read_b64_tr_b16 a[98:99], v21 offset:2304\n"
"s_waitcnt lgkmcnt(4)\n"
"s_add_u32 m0, s55, 0\n"
"s_add_u32 s60, s66, 65536\n"
"buffer_load_dwordx4 v24, s[44:47], s60 offen lds\n"
"s_add_u32 m0, s55, 1024\n"
"s_add_u32 s60, s66, 98304\n"
"buffer_load_dwordx4 v24, s[44:47], s60 offen lds\n"
"s_waitcnt vmcnt(36)\n"
"ds_read_b64_tr_b16 a[128:129], v20 offset:4096\n"
"ds_read_b64_tr_b16 a[130:131], v20 offset:4352\n"
"ds_read_b64_tr_b16 a[160:161], v21 offset:4096\n"
"ds_read_b64_tr_b16 a[162:163], v21 offset:4352\n"
"s_waitcnt lgkmcnt(4)\n"
"s_add_u32 m0, s55, 2048\n"
"s_add_u32 s60, s67, 65536\n"
"buffer_load_dwordx4 v24, s[44:47], s60 offen lds\n"
"s_add_u32 m0, s55, 3072\n"
"s_add_u32 s60, s67, 98304\n"
"buffer_load_dwordx4 v24, s[44:47], s60 offen lds\n"
"s_waitcnt vmcnt(36)\n"
"ds_read_b64_tr_b16 a[192:193], v20 offset:6144\n"
"ds_read_b64_tr_b16 a[194:195], v20 offset:6400\n"
"ds_read_b64_tr_b16 a[224:225], v21 offset:6144\n"
"ds_read_b64_tr_b16 a[226:227], v21 offset:6400\n"
"s_waitcnt lgkmcnt(4)\n"
"s_add_u32 m0, s55, 4096\n"
"s_add_u32 s60, s68, 65536\n"
"buffer_load_dwordx4 v24, s[44:47], s60 offen lds\n"
"s_add_u32 m0, s55, 5120\n"
"s_add_u32 s60, s68, 98304\n"
"buffer_load_dwordx4 v24, s[44:47], s60 offen lds\n"
"s_waitcnt vmcnt(36)\n"
"ds_read_b64_tr_b16 a[4:5], v20 offset:8192\n"
"ds_read_b64_tr_b16 a[6:7], v20 offset:8448\n"
"ds_read_b64_tr_b16 a[36:37], v21 offset:8192\n"
"ds_read_b64_tr_b16 a[38:39], v21 offset:8448\n"
"s_waitcnt lgkmcnt(4)\n"
"s_add_u32 m0, s55, 6144\n"
"s_add_u32 s60, s69, 65536\n"
"buffer_load_dwordx4 v24, s[44:47], s60 offen lds\n"
"s_add_u32 m0, s55, 7168\n"
"s_add_u32 s60, s69, 98304\n"
"buffer_load_dwordx4 v24, s[44:47], s60 offen lds\n"
"s_waitcnt vmcnt(36)\n"
"ds_read_b64_tr_b16 a[68:69], v20 offset:10240\n"
"ds_read_b64_tr_b16 a[70:71], v20 offset:10496\n"
"ds_read_b64_tr_b16 a[100:101], v21 offset:10240\n"
"ds_read_b64_tr_b16 a[102:103], v21 offset:10496\n"
"s_waitcnt lgkmcnt(4)\n"
"s_add_u32 m0, s55, 8192\n"
"s_add_u32 s60, s66, 0\n"
"buffer_load_dwordx4 v25, s[44:47], s60 offen lds\n"
"s_add_u32 m0, s55, 9216\n"
"s_add_u32 s60, s66, 32768\n"
"buffer_load_dwordx4 v25, s[44:47], s60 offen lds\n"
"s_waitcnt vmcnt(36)\n"
"ds_read_b64_tr_b16 a[132:133], v20 offset:12288\n"
"ds_read_b64_tr_b16 a[134:135], v20 offset:12544\n"
"ds_read_b64_tr_b16 a[164:165], v21 offset:12288\n"
"ds_read_b64_tr_b16 a[166:167], v21 offset:12544\n"
"s_waitcnt lgkmcnt(4)\n"
"s_add_u32 m0, s55, 10240\n"
"s_add_u32 s60, s67, 0\n"
"buffer_load_dwordx4 v25, s[44:47], s60 offen lds\n"
"s_add_u32 m0, s55, 11264\n"
"s_add_u32 s60, s67, 32768\n"
"buffer_load_dwordx4 v25, s[44:47], s60 offen lds\n"
"s_waitcnt vmcnt(36)\n"
"ds_read_b64_tr_b16 a[196:197], v20 offset:14336\n"
"ds_read_b64_tr_b16 a[198:199], v20 offset:14592\n"
"ds_read_b64_tr_b16 a[228:229], v21 offset:14336\n"
"ds_read_b64_tr_b16 a[230:231], v21 offset:14592\n"
"s_waitcnt lgkmcnt(4)\n"
"s_add_u32 m0, s55, 12288\n"
"s_add_u32 s60, s68, 0\n"
"buffer_load_dwordx4 v25, s[44:47], s60 offen lds\n"
"s_add_u32 m0, s55, 13312\n"
"s_add_u32 s60, s68, 32768\n"
"buffer_load_dwordx4 v25, s[44:47], s60 offen lds\n"
"s_waitcnt vmcnt(36)\n"
"ds_read_b64_tr_b16 a[8:9], v20 offset:16384\n"
"ds_read_b64_tr_b16 a[10:11], v20 offset:16640\n"
"ds_read_b64_tr_b16 a[40:41], v21 offset:16384\n"
"ds_read_b64_tr_b16 a[42:43], v21 offset:16640\n"
"s_waitcnt lgkmcnt(4)\n"
"s_add_u32 m0, s55, 14336\n"
"s_add_u32 s60, s69, 0\n"
"buffer_load_dwordx4 v25, s[44:47], s60 offen lds\n"
"s_add_u32 m0, s55, 15360\n"
"s_add_u32 s60, s69, 32768\n"
"buffer_load_dwordx4 v25, s[44:47], s60 offen lds\n"
"s_waitcnt vmcnt(36)\n"
"ds_read_b64_tr_b16 a[72:73], v20 offset:18432\n"
"ds_read_b64_tr_b16 a[74:75], v20 offset:18688\n"
"ds_read_b64_tr_b16 a[104:105], v21 offset:18432\n"
"ds_read_b64_tr_b16 a[106:107], v21 offset:18688\n"
"s_waitcnt lgkmcnt(4)\n"
"s_add_u32 m0, s55, 16384\n"
"s_add_u32 s60, s66, 65536\n"
"buffer_load_dwordx4 v25, s[44:47], s60 offen lds\n"
"s_add_u32 m0, s55, 17408\n"
"s_add_u32 s60, s66, 98304\n"
"buffer_load_dwordx4 v25, s[44:47], s60 offen lds\n"
"s_waitcnt vmcnt(36)\n"
"ds_read_b64_tr_b16 a[136:137], v20 offset:20480\n"
"ds_read_b64_tr_b16 a[138:139], v20 offset:20736\n"
"ds_read_b64_tr_b16 a[168:169], v21 offset:20480\n"
"ds_read_b64_tr_b16 a[170:171], v21 offset:20736\n"
"s_waitcnt lgkmcnt(4)\n"
"s_add_u32 m0, s55, 18432\n"
"s_add_u32 s60, s67, 65536\n"
"buffer_load_dwordx4 v25, s[44:47], s60 offen lds\n"
"s_add_u32 m0, s55, 19456\n"
"s_add_u32 s60, s67, 98304\n"
"buffer_load_dwordx4 v25, s[44:47], s60 offen lds\n"
"s_waitcnt vmcnt(36)\n"
"ds_read_b64_tr_b16 a[200:201], v20 offset:22528\n"
"ds_read_b64_tr_b16 a[202:203], v20 offset:22784\n"
"ds_read_b64_tr_b16 a[232:233], v21 offset:22528\n"
"ds_read_b64_tr_b16 a[234:235], v21 offset:22784\n"
"s_waitcnt lgkmcnt(4)\n"
"s_add_u32 m0, s55, 20480\n"
"s_add_u32 s60, s68, 65536\n"
"buffer_load_dwordx4 v25, s[44:47], s60 offen lds\n"
"s_add_u32 m0, s55, 21504\n"
"s_add_u32 s60, s68, 98304\n"
"buffer_load_dwordx4 v25, s[44:47], s60 offen lds\n"
"s_waitcnt vmcnt(36)\n"
"ds_read_b64_tr_b16 a[12:13], v20 offset:24576\n"
"ds_read_b64_tr_b16 a[14:15], v20 offset:24832\n"
"ds_read_b64_tr_b16 a[44:45], v21 offset:24576\n"
"ds_read_b64_tr_b16 a[46:47], v21 offset:24832\n"
"s_waitcnt lgkmcnt(4)\n"
"s_add_u32 m0, s55, 22528\n"
"s_add_u32 s60, s69, 65536\n"
"buffer_load_dwordx4 v25, s[44:47], s60 offen lds\n"
"s_add_u32 m0, s55, 23552\n"
"s_add_u32 s60, s69, 98304\n"
"buffer_load_dwordx4 v25, s[44:47], s60 offen lds\n"
"s_waitcnt vmcnt(36)\n"
"ds_read_b64_tr_b16 a[76:77], v20 offset:26624\n"
"ds_read_b64_tr_b16 a[78:79], v20 offset:26880\n"
"ds_read_b64_tr_b16 a[108:109], v21 offset:26624\n"
"ds_read_b64_tr_b16 a[110:111], v21 offset:26880\n"
"s_waitcnt lgkmcnt(4)\n"
"s_waitcnt vmcnt(34)\n"
"ds_read_b64_tr_b16 a[140:141], v20 offset:28672\n"
"ds_read_b64_tr_b16 a[142:143], v20 offset:28928\n"
"ds_read_b64_tr_b16 a[172:173], v21 offset:28672\n"
"ds_read_b64_tr_b16 a[174:175], v21 offset:28928\n"
"s_waitcnt lgkmcnt(4)\n"
"s_waitcnt vmcnt(32)\n"
"ds_read_b64_tr_b16 a[204:205], v20 offset:30720\n"
"ds_read_b64_tr_b16 a[206:207], v20 offset:30976\n"
"ds_read_b64_tr_b16 a[236:237], v21 offset:30720\n"
"ds_read_b64_tr_b16 a[238:239], v21 offset:30976\n"
"s_waitcnt lgkmcnt(4)\n"
"s_waitcnt vmcnt(30)\n"
"ds_read_b64_tr_b16 a[16:17], v20 offset:32768\n"
"ds_read_b64_tr_b16 a[18:19], v20 offset:33024\n"
"ds_read_b64_tr_b16 a[48:49], v21 offset:32768\n"
"ds_read_b64_tr_b16 a[50:51], v21 offset:33024\n"
"s_waitcnt lgkmcnt(4)\n"
"s_waitcnt vmcnt(28)\n"
"ds_read_b64_tr_b16 a[80:81], v20 offset:34816\n"
"ds_read_b64_tr_b16 a[82:83], v20 offset:35072\n"
"ds_read_b64_tr_b16 a[112:113], v21 offset:34816\n"
"ds_read_b64_tr_b16 a[114:115], v21 offset:35072\n"
"s_waitcnt lgkmcnt(4)\n"
"s_waitcnt vmcnt(26)\n"
"ds_read_b64_tr_b16 a[144:145], v20 offset:36864\n"
"ds_read_b64_tr_b16 a[146:147], v20 offset:37120\n"
"ds_read_b64_tr_b16 a[176:177], v21 offset:36864\n"
"ds_read_b64_tr_b16 a[178:179], v21 offset:37120\n"
"s_waitcnt lgkmcnt(4)\n"
"s_waitcnt vmcnt(24)\n"
"ds_read_b64_tr_b16 a[208:209], v20 offset:38912\n"
"ds_read_b64_tr_b16 a[210:211], v20 offset:39168\n"
"ds_read_b64_tr_b16 a[240:241], v21 offset:38912\n"
"ds_read_b64_tr_b16 a[242:243], v21 offset:39168\n"
"s_waitcnt lgkmcnt(4)\n"
"s_waitcnt vmcnt(22)\n"
"ds_read_b64_tr_b16 a[20:21], v20 offset:0\n"
"ds_read_b64_tr_b16 a[22:23], v20 offset:256\n"
"ds_read_b64_tr_b16 a[52:53], v21 offset:0\n"
"ds_read_b64_tr_b16 a[54:55], v21 offset:256\n"
"s_waitcnt lgkmcnt(4)\n"
"s_waitcnt vmcnt(20)\n"
"ds_read_b64_tr_b16 a[84:85], v20 offset:2048\n"
"ds_read_b64_tr_b16 a[86:87], v20 offset:2304\n"
"ds_read_b64_tr_b16 a[116:117], v21 offset:2048\n"
"ds_read_b64_tr_b16 a[118:119], v21 offset:2304\n"
"s_waitcnt lgkmcnt(4)\n"
"s_waitcnt vmcnt(18)\n"
"ds_read_b64_tr_b16 a[148:149], v20 offset:4096\n"
"ds_read_b64_tr_b16 a[150:151], v20 offset:4352\n"
"ds_read_b64_tr_b16 a[180:181], v21 offset:4096\n"
"ds_read_b64_tr_b16 a[182:183], v21 offset:4352\n"
"s_waitcnt lgkmcnt(4)\n"
"s_waitcnt vmcnt(16)\n"
"ds_read_b64_tr_b16 a[212:213], v20 offset:6144\n"
"ds_read_b64_tr_b16 a[214:215], v20 offset:6400\n"
"ds_read_b64_tr_b16 a[244:245], v21 offset:6144\n"
"ds_read_b64_tr_b16 a[246:247], v21 offset:6400\n"
"s_waitcnt lgkmcnt(4)\n"
"s_waitcnt vmcnt(14)\n"
"ds_read_b64_tr_b16 a[24:25], v20 offset:8192\n"
"ds_read_b64_tr_b16 a[26:27], v20 offset:8448\n"
"ds_read_b64_tr_b16 a[56:57], v21 offset:8192\n"
"ds_read_b64_tr_b16 a[58:59], v21 offset:8448\n"
"s_waitcnt lgkmcnt(4)\n"
"s_waitcnt vmcnt(12)\n"
"ds_read_b64_tr_b16 a[88:89], v20 offset:10240\n"
"ds_read_b64_tr_b16 a[90:91], v20 offset:10496\n"
"ds_read_b64_tr_b16 a[120:121], v21 offset:10240\n"
"ds_read_b64_tr_b16 a[122:123], v21 offset:10496\n"
"s_waitcnt lgkmcnt(4)\n"
"s_waitcnt vmcnt(10)\n"
"ds_read_b64_tr_b16 a[152:153], v20 offset:12288\n"
"ds_read_b64_tr_b16 a[154:155], v20 offset:12544\n"
"ds_read_b64_tr_b16 a[184:185], v21 offset:12288\n"
"ds_read_b64_tr_b16 a[186:187], v21 offset:12544\n"
"s_waitcnt lgkmcnt(4)\n"
"s_waitcnt vmcnt(8)\n"
"ds_read_b64_tr_b16 a[216:217], v20 offset:14336\n"
"ds_read_b64_tr_b16 a[218:219], v20 offset:14592\n"
"ds_read_b64_tr_b16 a[248:249], v21 offset:14336\n"
"ds_read_b64_tr_b16 a[250:251], v21 offset:14592\n"
"s_waitcnt lgkmcnt(4)\n"
"s_waitcnt vmcnt(6)\n"
"ds_read_b64_tr_b16 a[28:29], v20 offset:16384\n"
"ds_read_b64_tr_b16 a[30:31], v20 offset:16640\n"
"ds_read_b64_tr_b16 a[60:61], v21 offset:16384\n"
"ds_read_b64_tr_b16 a[62:63], v21 offset:16640\n"
"s_waitcnt lgkmcnt(4)\n"
"s_waitcnt vmcnt(4)\n"
"ds_read_b64_tr_b16 a[92:93], v20 offset:18432\n"
"ds_read_b64_tr_b16 a[94:95], v20 offset:18688\n"
"ds_read_b64_tr_b16 a[124:125], v21 offset:18432\n"
"ds_read_b64_tr_b16 a[126:127], v21 offset:18688\n"
"s_waitcnt lgkmcnt(4)\n"
"s_waitcnt vmcnt(2)\n"
"ds_read_b64_tr_b16 a[156:157], v20 offset:20480\n"
"ds_read_b64_tr_b16 a[158:159], v20 offset:20736\n"
"ds_read_b64_tr_b16 a[188:189], v21 offset:20480\n"
"ds_read_b64_tr_b16 a[190:191], v21 offset:20736\n"
"s_waitcnt lgkmcnt(4)\n"
"s_waitcnt vmcnt(0)\n"
"ds_read_b64_tr_b16 a[220:221], v20 offset:22528\n"
"ds_read_b64_tr_b16 a[222:223], v20 offset:22784\n"
"ds_read_b64_tr_b16 a[252:253], v21 offset:22528\n"
"ds_read_b64_tr_b16 a[254:255], v21 offset:22784\n"
"s_waitcnt lgkmcnt(4)\n"
"s_waitcnt lgkmcnt(0)\n"
"s_mov_b32 m0, s59\n"
"s_barrier\n"
"v_mov_b32 v124, 0\n"
"v_mov_b32 v125, 0\n"
"v_mov_b32 v120, 0\n"
"v_mov_b32 v121, 0\n"
"v_mov_b32 v124, 0\n"
"v_mov_b32 v125, 0\n"
"v_mov_b32 v120, 0\n"
"v_mov_b32 v121, 0\n"
"s_mov_b32 s94, 0x80000000\n"
"s_mov_b32 s93, 0x80000000\n"
"s_mov_b32 s93, 0x80000000\n"
"s_mov_b32 s95, 0x80000000\n"
"s_mov_b32 s92, 0x80000000\n"
"s_mov_b32 s95, 0x80000000\n"
"s_mov_b32 s95, 0x80000000\n"
"s_mov_b32 s94, 0x80000000\n"
"s_cmp_eq_u32 s54, 0\n"
"s_cbranch_scc1 .LO_1s\n"
"s_cmp_eq_u32 s54, 1\n"
"s_cbranch_scc1 .LO_2s\n"
"s_cmp_eq_u32 s54, 2\n"
"s_cbranch_scc1 .LO_3s\n"
"s_cmp_eq_u32 s54, 3\n"
"s_cbranch_scc1 .LO_4s\n"
".p2align 6\n"
".LO_top:\n"
"v_mfma_f32_16x16x32_bf16 v[40:43], v[84:87], a[0:3], 0\n"
"global_load_dwordx4 v[20:23], v6, s[56:57] offset:1024 sc1\n"
"v_mfma_f32_16x16x32_bf16 v[40:43], v[88:91], a[4:7], v[40:43]\n"
"s_waitcnt vmcnt(13)\n"
"v_mfma_f32_16x16x32_bf16 v[40:43], v[92:95], a[8:11], v[40:43]\n"
"s_waitcnt lgkmcnt(3)\n"
"v_add_f32 v72, v72, v44\n"
"v_add_f32 v73, v73, v45\n"
"v_mfma_f32_16x16x32_bf16 v[40:43], v[52:55], a[12:15], v[40:43]\n"
"s_waitcnt vmcnt(11)\n"
"v_add_f32 v74, v74, v46\n"
"v_add_f32 v75, v75, v47\n"
"v_mfma_f32_16x16x32_bf16 v[40:43], v[56:59], a[16:19], v[40:43]\n"
"v_add_f32 v72, v72, v48\n"
"v_add_f32 v73, v73, v49\n"
"v_mfma_f32_16x16x32_bf16 v[40:43], v[60:63], a[20:23], v[40:43]\n"
"s_waitcnt vmcnt(8)\n"
"global_load_dwordx4 v[24:27], v7, s[56:57] offset:0 sc1\n"
"v_add_f32 v74, v74, v50\n"
"v_mfma_f32_16x16x32_bf16 v[40:43], v[64:67], a[24:27], v[40:43]\n"
"v_add_f32 v75, v75, v51\n"
"v_add_f32 v72, v72, v112\n"
"v_mfma_f32_16x16x32_bf16 v[40:43], v[68:71], a[28:31], v[40:43]\n"
"v_add_f32 v73, v73, v113\n"
"v_add_f32 v74, v74, v114\n"
"v_mfma_f32_16x16x32_bf16 v[44:47], v[84:87], a[32:35], 0\n"
"global_load_dwordx4 v[28:31], v7, s[56:57] offset:1024 sc1\n"
"v_add_f32 v75, v75, v115\n"
"v_mfma_f32_16x16x32_bf16 v[44:47], v[88:91], a[36:39], v[44:47]\n"
"s_add_u32 s95, s50, 0xffd00000\n"
"s_and_b32 s94, s95, 0xfbffffff\n"
"ds_write_addtid_b32 v40 offset:0\n"
"v_mfma_f32_16x16x32_bf16 v[44:47], v[92:95], a[40:43], v[44:47]\n"
"ds_write_addtid_b32 v41 offset:4640\n"
"v_cvt_pk_bf16_f32 v48, v72, v73\n"
"v_mfma_f32_16x16x32_bf16 v[44:47], v[52:55], a[44:47], v[44:47]\n"
"ds_write_addtid_b32 v42 offset:9344\n"
"v_cvt_pk_bf16_f32 v49, v74, v75\n"
"v_mfma_f32_16x16x32_bf16 v[44:47], v[56:59], a[48:51], v[44:47]\n"
"s_waitcnt lgkmcnt(3)\n"
"ds_write_addtid_b32 v43 offset:13984\n"
"v_add_f32 v76, v76, v80\n"
"v_mfma_f32_16x16x32_bf16 v[44:47], v[60:63], a[52:55], v[44:47]\n"
"global_load_dwordx4 v[32:35], v8, s[56:57] offset:0 sc1\n"
"v_add_f32 v77, v77, v81\n"
"v_mfma_f32_16x16x32_bf16 v[44:47], v[64:67], a[56:59], v[44:47]\n"
"v_add_f32 v78, v78, v82\n"
"v_add_f32 v79, v79, v83\n"
"v_mfma_f32_16x16x32_bf16 v[44:47], v[68:71], a[60:63], v[44:47]\n"
"v_add_f32 v76, v76, v120\n"
"v_add_f32 v77, v77, v121\n"
"v_mfma_f32_16x16x32_bf16 v[40:43], v[84:87], a[64:67], 0\n"
"v_add_f32 v78, v78, v122\n"
"v_add_f32 v79, v79, v123\n"
"v_mfma_f32_16x16x32_bf16 v[40:43], v[88:91], a[68:71], v[40:43]\n"
"ds_write_addtid_b32 v44 offset:256\n"
"v_add_f32 v76, v76, v108\n"
"v_mfma_f32_16x16x32_bf16 v[40:43], v[92:95], a[72:75], v[40:43]\n"
"s_waitcnt vmcnt(9)\n"
"buffer_store_dwordx2 v[0:1], v12, s[40:43], s92 offen offset:0 sc1\n"
"ds_write_addtid_b32 v45 offset:4896\n"
"v_mfma_f32_16x16x32_bf16 v[40:43], v[52:55], a[76:79], v[40:43]\n"
"ds_write_addtid_b32 v46 offset:9600\n"
"v_add_f32 v77, v77, v109\n"
"v_mfma_f32_16x16x32_bf16 v[40:43], v[56:59], a[80:83], v[40:43]\n"
"s_waitcnt vmcnt(9)\n"
"ds_write_addtid_b32 v47 offset:14240\n"
"global_load_dwordx4 v[36:39], v8, s[56:57] offset:1024 sc1\n"
"v_mfma_f32_16x16x32_bf16 v[40:43], v[60:63], a[84:87], v[40:43]\n"
"v_add_f32 v78, v78, v110\n"
"v_add_f32 v79, v79, v111\n"
"v_mfma_f32_16x16x32_bf16 v[40:43], v[64:67], a[88:91], v[40:43]\n"
"v_cvt_pk_bf16_f32 v50, v76, v77\n"
"v_cvt_pk_bf16_f32 v51, v78, v79\n"
"v_mfma_f32_16x16x32_bf16 v[40:43], v[68:71], a[92:95], v[40:43]\n"
"v_lshlrev_b32 v72, 16, v116\n"
"v_and_b32 v73, 0xffff0000, v116\n"
"v_mfma_f32_16x16x32_bf16 v[44:47], v[84:87], a[96:99], 0\n"
"v_lshlrev_b32 v74, 16, v117\n"
"v_and_b32 v75, 0xffff0000, v117\n"
"v_mfma_f32_16x16x32_bf16 v[44:47], v[88:91], a[100:103], v[44:47]\n"
"ds_write_addtid_b32 v40 offset:512\n"
"v_lshlrev_b32 v76, 16, v118\n"
"v_mfma_f32_16x16x32_bf16 v[44:47], v[92:95], a[104:107], v[44:47]\n"
"ds_write_addtid_b32 v41 offset:5152\n"
"v_and_b32 v77, 0xffff0000, v118\n"
"v_mfma_f32_16x16x32_bf16 v[44:47], v[52:55], a[108:111], v[44:47]\n"
"ds_write_addtid_b32 v42 offset:9856\n"
"v_lshlrev_b32 v78, 16, v119\n"
"v_mfma_f32_16x16x32_bf16 v[44:47], v[56:59], a[112:115], v[44:47]\n"
"ds_write_addtid_b32 v43 offset:14496\n"
"v_and_b32 v79, 0xffff0000, v119\n"
"v_mfma_f32_16x16x32_bf16 v[44:47], v[60:63], a[116:119], v[44:47]\n"
"v_mfma_f32_16x16x32_bf16 v[44:47], v[64:67], a[120:123], v[44:47]\n"
"v_mfma_f32_16x16x32_bf16 v[44:47], v[68:71], a[124:127], v[44:47]\n"
"v_mfma_f32_16x16x32_bf16 v[40:43], v[84:87], a[128:131], 0\n"
"v_mfma_f32_16x16x32_bf16 v[40:43], v[88:91], a[132:135], v[40:43]\n"
"ds_write_addtid_b32 v44 offset:768\n"
"v_mfma_f32_16x16x32_bf16 v[40:43], v[92:95], a[136:139], v[40:43]\n"
"buffer_store_dwordx4 v[48:51], v11, s[40:43], s94 offen sc1\n"
"ds_write_addtid_b32 v45 offset:5408\n"
"v_mfma_f32_16x16x32_bf16 v[80:83], v[48:51], v[48:51], 0\n"
"v_mfma_f32_16x16x32_bf16 v[108:111], v[14:17], v[48:51], 0\n"
"v_mfma_f32_16x16x32_bf16 v[40:43], v[52:55], a[140:143], v[40:43]\n"
"ds_write_addtid_b32 v46 offset:10112\n"
"v_cndmask_b32_e64 v80, v80, v81, s[62:63]\n"
"v_mfma_f32_16x16x32_bf16 v[40:43], v[56:59], a[144:147], v[40:43]\n"
"ds_write_addtid_b32 v47 offset:14752\n"
"v_cndmask_b32_e64 v82, v82, v83, s[62:63]\n"
"v_mfma_f32_16x16x32_bf16 v[40:43], v[60:63], a[148:151], v[40:43]\n"
"global_load_dwordx4 v[112:115], v10, s[56:57] sc1\n"
"v_cndmask_b32_e64 v109, v80, v82, s[64:65]\n"
"v_mfma_f32_16x16x32_bf16 v[40:43], v[64:67], a[152:155], v[40:43]\n"
"buffer_store_dwordx2 v[108:109], v13, s[40:43], s94 offen offset:0 sc1\n"
"v_mfma_f32_16x16x32_bf16 v[40:43], v[68:71], a[156:159], v[40:43]\n"
"v_mfma_f32_16x16x32_bf16 v[48:51], v[84:87], a[160:163], 0\n"
"s_add_u32 s58, s50, 0x180000\n"
"v_mfma_f32_16x16x32_bf16 v[48:51], v[88:91], a[164:167], v[48:51]\n"
"s_and_b32 s58, s58, 0x3ffffff\n"
"ds_write_addtid_b32 v40 offset:1024\n"
"v_mfma_f32_16x16x32_bf16 v[48:51], v[92:95], a[168:171], v[48:51]\n"
"s_add_u32 s56, s40, s58\n"
"ds_write_addtid_b32 v41 offset:5664\n"
"v_mfma_f32_16x16x32_bf16 v[48:51], v[52:55], a[172:175], v[48:51]\n"
"s_addc_u32 s57, s41, 0\n"
"ds_write_addtid_b32 v42 offset:10368\n"
"v_mfma_f32_16x16x32_bf16 v[48:51], v[56:59], a[176:179], v[48:51]\n"
"s_waitcnt vmcnt(10)\n"
"ds_write_addtid_b32 v43 offset:15008\n"
"v_cmp_ne_u64 vcc, s[48:49], v[124:125]\n"
"v_mfma_f32_16x16x32_bf16 v[48:51], v[60:63], a[180:183], v[48:51]\n"
"v_mfma_f32_16x16x32_bf16 v[48:51], v[64:67], a[184:187], v[48:51]\n"
"s_cbranch_vccnz .LO_3s\n"
".LO_3r:\n"
"v_mfma_f32_16x16x32_bf16 v[48:51], v[68:71], a[188:191], v[48:51]\n"
"s_add_u32 s50, s50, 0x180000\n"
"global_load_dwordx4 v[40:43], v5, s[56:57] offset:0 sc1\n"
"v_mfma_f32_16x16x32_bf16 v[72:75], a[192:195], v[84:87], v[72:75]\n"
"v_mfma_f32_16x16x32_bf16 v[72:75], a[196:199], v[88:91], v[72:75]\n"
"ds_write_addtid_b32 v48 offset:1280\n"
"v_mfma_f32_16x16x32_bf16 v[72:75], a[200:203], v[92:95], v[72:75]\n"
"ds_write_addtid_b32 v49 offset:5920\n"
"v_mfma_f32_16x16x32_bf16 v[72:75], a[204:207], v[52:55], v[72:75]\n"
"ds_write_addtid_b32 v50 offset:10624\n"
"v_mfma_f32_16x16x32_bf16 v[72:75], a[208:211], v[56:59], v[72:75]\n"
"ds_write_addtid_b32 v51 offset:15264\n"
"global_load_dwordx4 v[44:47], v5, s[56:57] offset:1024 sc1\n"
"v_mfma_f32_16x16x32_bf16 v[72:75], a[212:215], v[60:63], v[72:75]\n"
"v_mfma_f32_16x16x32_bf16 v[72:75], a[216:219], v[64:67], v[72:75]\n"
"v_mfma_f32_16x16x32_bf16 v[72:75], a[220:223], v[68:71], v[72:75]\n"
"v_mfma_f32_16x16x32_bf16 v[76:79], a[224:227], v[84:87], v[76:79]\n"
"s_waitcnt lgkmcnt(0)\n"
"v_mfma_f32_16x16x32_bf16 v[76:79], a[228:231], v[88:91], v[76:79]\n"
"s_barrier\n"
"v_mfma_f32_16x16x32_bf16 v[76:79], a[232:235], v[92:95], v[76:79]\n"
"ds_read_b128 v[80:83], v2 offset:1536\n"
"ds_read_b128 v[84:87], v3 offset:512\n"
"v_mfma_f32_16x16x32_bf16 v[76:79], a[236:239], v[52:55], v[76:79]\n"
"buffer_load_dwordx2 v[120:121], v9, s[40:43], s50 offen sc1\n"
"v_mfma_f32_16x16x32_bf16 v[76:79], a[240:243], v[56:59], v[76:79]\n"
"global_load_dwordx4 v[48:51], v6, s[56:57] offset:0 sc1\n"
"ds_read_b128 v[88:91], v4 offset:2560\n"
"v_mfma_f32_16x16x32_bf16 v[76:79], a[244:247], v[60:63], v[76:79]\n"
"ds_read_b128 v[92:95], v2 offset:1792\n"
"ds_read_b128 v[108:111], v3 offset:768\n"
"v_mfma_f32_16x16x32_bf16 v[76:79], a[248:251], v[64:67], v[76:79]\n"
"global_load_dwordx4 v[52:55], v6, s[56:57] offset:1024 sc1\n"
"ds_read_b128 v[116:119], v4 offset:2816\n"
"v_mfma_f32_16x16x32_bf16 v[76:79], a[252:255], v[68:71], v[76:79]\n"
"v_mfma_f32_16x16x32_bf16 v[64:67], v[96:99], a[0:3], 0\n"
"v_mfma_f32_16x16x32_bf16 v[64:67], v[100:103], a[4:7], v[64:67]\n"
"s_waitcnt vmcnt(13)\n"
"v_mfma_f32_16x16x32_bf16 v[64:67], v[104:107], a[8:11], v[64:67]\n"
"s_waitcnt lgkmcnt(3)\n"
"v_add_f32 v80, v80, v84\n"
"v_add_f32 v81, v81, v85\n"
"v_mfma_f32_16x16x32_bf16 v[64:67], v[20:23], a[12:15], v[64:67]\n"
"s_waitcnt vmcnt(11)\n"
"global_load_dwordx4 v[56:59], v7, s[56:57] offset:0 sc1\n"
"v_add_f32 v82, v82, v86\n"
"v_mfma_f32_16x16x32_bf16 v[64:67], v[24:27], a[16:19], v[64:67]\n"
"v_add_f32 v83, v83, v87\n"
"v_add_f32 v80, v80, v88\n"
"v_mfma_f32_16x16x32_bf16 v[64:67], v[28:31], a[20:23], v[64:67]\n"
"s_waitcnt vmcnt(9)\n"
"v_add_f32 v81, v81, v89\n"
"v_add_f32 v82, v82, v90\n"
"v_mfma_f32_16x16x32_bf16 v[64:67], v[32:35], a[24:27], v[64:67]\n"
"v_add_f32 v83, v83, v91\n"
"v_add_f32 v80, v80, v72\n"
"v_mfma_f32_16x16x32_bf16 v[64:67], v[36:39], a[28:31], v[64:67]\n"
"v_add_f32 v81, v81, v73\n"
"v_add_f32 v82, v82, v74\n"
"v_mfma_f32_16x16x32_bf16 v[84:87], v[96:99], a[32:35], 0\n"
"v_add_f32 v83, v83, v75\n"
"v_mfma_f32_16x16x32_bf16 v[84:87], v[100:103], a[36:39], v[84:87]\n"
"s_add_u32 s94, s50, 0xffd00000\n"
"s_and_b32 s93, s94, 0xfbffffff\n"
"ds_write_addtid_b32 v64 offset:1536\n"
"v_mfma_f32_16x16x32_bf16 v[84:87], v[104:107], a[40:43], v[84:87]\n"
"ds_write_addtid_b32 v65 offset:6176\n"
"v_cvt_pk_bf16_f32 v72, v80, v81\n"
"v_mfma_f32_16x16x32_bf16 v[84:87], v[20:23], a[44:47], v[84:87]\n"
"ds_write_addtid_b32 v66 offset:10880\n"
"global_load_dwordx4 v[60:63], v7, s[56:57] offset:1024 sc1\n"
"v_mfma_f32_16x16x32_bf16 v[84:87], v[24:27], a[48:51], v[84:87]\n"
"ds_write_addtid_b32 v67 offset:15520\n"
"v_cvt_pk_bf16_f32 v73, v82, v83\n"
"v_mfma_f32_16x16x32_bf16 v[84:87], v[28:31], a[52:55], v[84:87]\n"
"s_waitcnt lgkmcnt(4)\n"
"v_add_f32 v92, v92, v108\n"
"v_add_f32 v93, v93, v109\n"
"v_mfma_f32_16x16x32_bf16 v[84:87], v[32:35], a[56:59], v[84:87]\n"
"v_add_f32 v94, v94, v110\n"
"v_add_f32 v95, v95, v111\n"
"v_mfma_f32_16x16x32_bf16 v[84:87], v[36:39], a[60:63], v[84:87]\n"
"v_add_f32 v92, v92, v116\n"
"v_add_f32 v93, v93, v117\n"
"v_mfma_f32_16x16x32_bf16 v[80:83], v[96:99], a[64:67], 0\n"
"global_load_dwordx4 v[64:67], v8, s[56:57] offset:0 sc1\n"
"v_add_f32 v94, v94, v118\n"
"v_mfma_f32_16x16x32_bf16 v[80:83], v[100:103], a[68:71], v[80:83]\n"
"ds_write_addtid_b32 v84 offset:1792\n"
"global_load_dwordx4 v[68:71], v8, s[56:57] offset:1024 sc1\n"
"v_mfma_f32_16x16x32_bf16 v[80:83], v[104:107], a[72:75], v[80:83]\n"
"ds_write_addtid_b32 v85 offset:6432\n"
"v_add_f32 v95, v95, v119\n"
"v_mfma_f32_16x16x32_bf16 v[80:83], v[20:23], a[76:79], v[80:83]\n"
"ds_write_addtid_b32 v86 offset:11136\n"
"v_add_f32 v92, v92, v76\n"
"v_mfma_f32_16x16x32_bf16 v[80:83], v[24:27], a[80:83], v[80:83]\n"
"ds_write_addtid_b32 v87 offset:15776\n"
"v_add_f32 v93, v93, v77\n"
"v_mfma_f32_16x16x32_bf16 v[80:83], v[28:31], a[84:87], v[80:83]\n"
"s_waitcnt vmcnt(10)\n"
"v_add_f32 v94, v94, v78\n"
"v_add_f32 v95, v95, v79\n"
"v_mfma_f32_16x16x32_bf16 v[80:83], v[32:35], a[88:91], v[80:83]\n"
"s_waitcnt vmcnt(9)\n"
"v_cvt_pk_bf16_f32 v74, v92, v93\n"
"v_cvt_pk_bf16_f32 v75, v94, v95\n"
"v_mfma_f32_16x16x32_bf16 v[80:83], v[36:39], a[92:95], v[80:83]\n"
"buffer_store_dwordx4 v[72:75], v11, s[40:43], s93 offen sc1\n"
"v_lshlrev_b32 v84, 16, v112\n"
"v_and_b32 v85, 0xffff0000, v112\n"
"v_lshlrev_b32 v86, 16, v113\n"
"v_mfma_f32_16x16x32_bf16 v[76:79], v[72:75], v[72:75], 0\n"
"buffer_store_dwordx2 v[0:1], v12, s[40:43], s95 offen offset:0 sc1\n"
"v_and_b32 v87, 0xffff0000, v113\n"
"v_mfma_f32_16x16x32_bf16 v[88:91], v[14:17], v[72:75], 0\n"
"v_lshlrev_b32 v92, 16, v114\n"
"v_and_b32 v93, 0xffff0000, v114\n"
"v_mfma_f32_16x16x32_bf16 v[108:111], v[96:99], a[96:99], 0\n"
"v_cndmask_b32_e64 v76, v76, v77, s[62:63]\n"
"v_cndmask_b32_e64 v78, v78, v79, s[62:63]\n"
"v_mfma_f32_16x16x32_bf16 v[108:111], v[100:103], a[100:103], v[108:111]\n"
"ds_write_addtid_b32 v80 offset:2048\n"
"v_cndmask_b32_e64 v89, v76, v78, s[64:65]\n"
"v_mfma_f32_16x16x32_bf16 v[108:111], v[104:107], a[104:107], v[108:111]\n"
"buffer_store_dwordx2 v[88:89], v13, s[40:43], s93 offen offset:0 sc1\n"
"ds_write_addtid_b32 v81 offset:6688\n"
"v_mfma_f32_16x16x32_bf16 v[108:111], v[20:23], a[108:111], v[108:111]\n"
"ds_write_addtid_b32 v82 offset:11392\n"
"v_lshlrev_b32 v94, 16, v115\n"
"v_mfma_f32_16x16x32_bf16 v[108:111], v[24:27], a[112:115], v[108:111]\n"
"ds_write_addtid_b32 v83 offset:16032\n"
"v_and_b32 v95, 0xffff0000, v115\n"
"v_mfma_f32_16x16x32_bf16 v[108:111], v[28:31], a[116:119], v[108:111]\n"
"v_mfma_f32_16x16x32_bf16 v[108:111], v[32:35], a[120:123], v[108:111]\n"
"v_mfma_f32_16x16x32_bf16 v[108:111], v[36:39], a[124:127], v[108:111]\n"
"v_mfma_f32_16x16x32_bf16 v[72:75], v[96:99], a[128:131], 0\n"
"v_mfma_f32_16x16x32_bf16 v[72:75], v[100:103], a[132:135], v[72:75]\n"
"ds_write_addtid_b32 v108 offset:2304\n"
"v_mfma_f32_16x16x32_bf16 v[72:75], v[104:107], a[136:139], v[72:75]\n"
"ds_write_addtid_b32 v109 offset:6944\n"
"v_mfma_f32_16x16x32_bf16 v[72:75], v[20:23], a[140:143], v[72:75]\n"
"ds_write_addtid_b32 v110 offset:11648\n"
"v_mfma_f32_16x16x32_bf16 v[72:75], v[24:27], a[144:147], v[72:75]\n"
"ds_write_addtid_b32 v111 offset:16288\n"
"v_mfma_f32_16x16x32_bf16 v[72:75], v[28:31], a[148:151], v[72:75]\n"
"v_mfma_f32_16x16x32_bf16 v[72:75], v[32:35], a[152:155], v[72:75]\n"
"global_load_dwordx4 v[88:91], v10, s[56:57] sc1\n"
"v_mfma_f32_16x16x32_bf16 v[72:75], v[36:39], a[156:159], v[72:75]\n"
"v_mfma_f32_16x16x32_bf16 v[76:79], v[96:99], a[160:163], 0\n"
"v_mfma_f32_16x16x32_bf16 v[76:79], v[100:103], a[164:167], v[76:79]\n"
"ds_write_addtid_b32 v72 offset:2560\n"
"v_mfma_f32_16x16x32_bf16 v[76:79], v[104:107], a[168:171], v[76:79]\n"
"s_add_u32 s58, s50, 0x180000\n"
"ds_write_addtid_b32 v73 offset:7200\n"
"v_mfma_f32_16x16x32_bf16 v[76:79], v[20:23], a[172:175], v[76:79]\n"
"s_and_b32 s58, s58, 0x3ffffff\n"
"ds_write_addtid_b32 v74 offset:11904\n"
"v_mfma_f32_16x16x32_bf16 v[76:79], v[24:27], a[176:179], v[76:79]\n"
"s_add_u32 s56, s40, s58\n"
"ds_write_addtid_b32 v75 offset:16544\n"
"v_mfma_f32_16x16x32_bf16 v[76:79], v[28:31], a[180:183], v[76:79]\n"
"s_addc_u32 s57, s41, 0\n"
"v_mfma_f32_16x16x32_bf16 v[76:79], v[32:35], a[184:187], v[76:79]\n"
"s_waitcnt vmcnt(10)\n"
"v_cmp_ne_u64 vcc, s[48:49], v[120:121]\n"
"v_mfma_f32_16x16x32_bf16 v[76:79], v[36:39], a[188:191], v[76:79]\n"
"v_mfma_f32_16x16x32_bf16 v[84:87], a[192:195], v[96:99], v[84:87]\n"
"s_cbranch_vccnz .LO_4s\n"
".LO_4r:\n"
"v_mfma_f32_16x16x32_bf16 v[84:87], a[196:199], v[100:103], v[84:87]\n"
"s_add_u32 s50, s50, 0x180000\n"
"ds_write_addtid_b32 v76 offset:2816\n"
"v_mfma_f32_16x16x32_bf16 v[84:87], a[200:203], v[104:107], v[84:87]\n"
"ds_write_addtid_b32 v77 offset:7456\n"
"global_load_dwordx4 v[72:75], v5, s[56:57] offset:0 sc1\n"
"v_mfma_f32_16x16x32_bf16 v[84:87], a[204:207], v[20:23], v[84:87]\n"
"ds_write_addtid_b32 v78 offset:12160\n"
"v_mfma_f32_16x16x32_bf16 v[84:87], a[208:211], v[24:27], v[84:87]\n"
"ds_write_addtid_b32 v79 offset:16800\n"
"v_mfma_f32_16x16x32_bf16 v[84:87], a[212:215], v[28:31], v[84:87]\n"
"v_mfma_f32_16x16x32_bf16 v[84:87], a[216:219], v[32:35], v[84:87]\n"
"v_mfma_f32_16x16x32_bf16 v[84:87], a[220:223], v[36:39], v[84:87]\n"
"global_load_dwordx4 v[76:79], v5, s[56:57] offset:1024 sc1\n"
"v_mfma_f32_16x16x32_bf16 v[92:95], a[224:227], v[96:99], v[92:95]\n"
"s_waitcnt lgkmcnt(0)\n"
"global_load_dwordx4 v[80:83], v6, s[56:57] offset:0 sc1\n"
"v_mfma_f32_16x16x32_bf16 v[92:95], a[228:231], v[100:103], v[92:95]\n"
"s_barrier\n"
"v_mfma_f32_16x16x32_bf16 v[92:95], a[232:235], v[104:107], v[92:95]\n"
"ds_read_b128 v[96:99], v2 offset:0\n"
"ds_read_b128 v[100:103], v3 offset:2048\n"
"v_mfma_f32_16x16x32_bf16 v[92:95], a[236:239], v[20:23], v[92:95]\n"
"ds_read_b128 v[104:107], v4 offset:1024\n"
"ds_read_b128 v[108:111], v2 offset:256\n"
"v_mfma_f32_16x16x32_bf16 v[92:95], a[240:243], v[24:27], v[92:95]\n"
"ds_read_b128 v[112:115], v3 offset:2304\n"
"ds_read_b128 v[116:119], v4 offset:1280\n"
"v_mfma_f32_16x16x32_bf16 v[92:95], a[244:247], v[28:31], v[92:95]\n"
"buffer_load_dwordx2 v[124:125], v9, s[40:43], s50 offen sc1\n"
"v_mfma_f32_16x16x32_bf16 v[92:95], a[248:251], v[32:35], v[92:95]\n"
"v_mfma_f32_16x16x32_bf16 v[92:95], a[252:255], v[36:39], v[92:95]\n"
"v_mfma_f32_16x16x32_bf16 v[120:123], v[40:43], a[0:3], 0\n"
"v_mfma_f32_16x16x32_bf16 v[120:123], v[44:47], a[4:7], v[120:123]\n"
"s_waitcnt vmcnt(12)\n"
"global_load_dwordx4 v[20:23], v6, s[56:57] offset:1024 sc1\n"
"v_mfma_f32_16x16x32_bf16 v[120:123], v[48:51], a[8:11], v[120:123]\n"
"s_waitcnt lgkmcnt(3)\n"
"v_add_f32 v96, v96, v100\n"
"v_add_f32 v97, v97, v101\n"
"v_mfma_f32_16x16x32_bf16 v[120:123], v[52:55], a[12:15], v[120:123]\n"
"s_waitcnt vmcnt(11)\n"
"v_add_f32 v98, v98, v102\n"
"v_add_f32 v99, v99, v103\n"
"v_mfma_f32_16x16x32_bf16 v[120:123], v[56:59], a[16:19], v[120:123]\n"
"v_add_f32 v96, v96, v104\n"
"v_add_f32 v97, v97, v105\n"
"v_mfma_f32_16x16x32_bf16 v[120:123], v[60:63], a[20:23], v[120:123]\n"
"s_waitcnt vmcnt(9)\n"
"v_add_f32 v98, v98, v106\n"
"v_add_f32 v99, v99, v107\n"
"v_mfma_f32_16x16x32_bf16 v[120:123], v[64:67], a[24:27], v[120:123]\n"
"global_load_dwordx4 v[24:27], v7, s[56:57] offset:0 sc1\n"
"v_add_f32 v96, v96, v84\n"
"v_mfma_f32_16x16x32_bf16 v[120:123], v[68:71], a[28:31], v[120:123]\n"
"global_load_dwordx4 v[28:31], v7, s[56:57] offset:1024 sc1\n"
"v_add_f32 v97, v97, v85\n"
"v_mfma_f32_16x16x32_bf16 v[100:103], v[40:43], a[32:35], 0\n"
"v_add_f32 v98, v98, v86\n"
"v_add_f32 v99, v99, v87\n"
"v_mfma_f32_16x16x32_bf16 v[100:103], v[44:47], a[36:39], v[100:103]\n"
"s_add_u32 s93, s50, 0xffd00000\n"
"s_and_b32 s95, s93, 0xfbffffff\n"
"ds_write_addtid_b32 v120 offset:0\n"
"v_mfma_f32_16x16x32_bf16 v[100:103], v[48:51], a[40:43], v[100:103]\n"
"s_waitcnt vmcnt(8)\n"
"buffer_store_dwordx2 v[0:1], v12, s[40:43], s94 offen offset:0 sc1\n"
"ds_write_addtid_b32 v121 offset:4640\n"
"v_mfma_f32_16x16x32_bf16 v[100:103], v[52:55], a[44:47], v[100:103]\n"
"ds_write_addtid_b32 v122 offset:9344\n"
"v_cvt_pk_bf16_f32 v84, v96, v97\n"
"v_mfma_f32_16x16x32_bf16 v[100:103], v[56:59], a[48:51], v[100:103]\n"
"ds_write_addtid_b32 v123 offset:13984\n"
"global_load_dwordx4 v[32:35], v8, s[56:57] offset:0 sc1\n"
"v_mfma_f32_16x16x32_bf16 v[100:103], v[60:63], a[52:55], v[100:103]\n"
"v_cvt_pk_bf16_f32 v85, v98, v99\n"
"v_mfma_f32_16x16x32_bf16 v[100:103], v[64:67], a[56:59], v[100:103]\n"
"s_waitcnt lgkmcnt(4)\n"
"v_add_f32 v108, v108, v112\n"
"v_add_f32 v109, v109, v113\n"
"v_mfma_f32_16x16x32_bf16 v[100:103], v[68:71], a[60:63], v[100:103]\n"
"v_add_f32 v110, v110, v114\n"
"v_add_f32 v111, v111, v115\n"
"v_mfma_f32_16x16x32_bf16 v[96:99], v[40:43], a[64:67], 0\n"
"v_add_f32 v108, v108, v116\n"
"v_add_f32 v109, v109, v117\n"
"v_mfma_f32_16x16x32_bf16 v[96:99], v[44:47], a[68:71], v[96:99]\n"
"ds_write_addtid_b32 v100 offset:256\n"
"v_add_f32 v110, v110, v118\n"
"v_mfma_f32_16x16x32_bf16 v[96:99], v[48:51], a[72:75], v[96:99]\n"
"ds_write_addtid_b32 v101 offset:4896\n"
"v_add_f32 v111, v111, v119\n"
"v_mfma_f32_16x16x32_bf16 v[96:99], v[52:55], a[76:79], v[96:99]\n"
"ds_write_addtid_b32 v102 offset:9600\n"
"v_add_f32 v108, v108, v92\n"
"v_mfma_f32_16x16x32_bf16 v[96:99], v[56:59], a[80:83], v[96:99]\n"
"ds_write_addtid_b32 v103 offset:14240\n"
"v_add_f32 v109, v109, v93\n"
"v_mfma_f32_16x16x32_bf16 v[96:99], v[60:63], a[84:87], v[96:99]\n"
"global_load_dwordx4 v[36:39], v8, s[56:57] offset:1024 sc1\n"
"v_add_f32 v110, v110, v94\n"
"v_mfma_f32_16x16x32_bf16 v[96:99], v[64:67], a[88:91], v[96:99]\n"
"s_waitcnt vmcnt(10)\n"
"v_add_f32 v111, v111, v95\n"
"v_cvt_pk_bf16_f32 v86, v108, v109\n"
"v_mfma_f32_16x16x32_bf16 v[96:99], v[68:71], a[92:95], v[96:99]\n"
"v_cvt_pk_bf16_f32 v87, v110, v111\n"
"v_lshlrev_b32 v100, 16, v88\n"
"v_mfma_f32_16x16x32_bf16 v[92:95], v[40:43], a[96:99], 0\n"
"v_and_b32 v101, 0xffff0000, v88\n"
"v_lshlrev_b32 v102, 16, v89\n"
"v_mfma_f32_16x16x32_bf16 v[92:95], v[44:47], a[100:103], v[92:95]\n"
"ds_write_addtid_b32 v96 offset:512\n"
"v_and_b32 v103, 0xffff0000, v89\n"
"v_mfma_f32_16x16x32_bf16 v[92:95], v[48:51], a[104:107], v[92:95]\n"
"ds_write_addtid_b32 v97 offset:5152\n"
"v_lshlrev_b32 v104, 16, v90\n"
"v_mfma_f32_16x16x32_bf16 v[92:95], v[52:55], a[108:111], v[92:95]\n"
"buffer_store_dwordx4 v[84:87], v11, s[40:43], s95 offen sc1\n"
"ds_write_addtid_b32 v98 offset:9856\n"
"v_and_b32 v105, 0xffff0000, v90\n"
"v_lshlrev_b32 v106, 16, v91\n"
"v_mfma_f32_16x16x32_bf16 v[108:111], v[84:87], v[84:87], 0\n"
"v_and_b32 v107, 0xffff0000, v91\n"
"v_mfma_f32_16x16x32_bf16 v[112:115], v[14:17], v[84:87], 0\n"
"v_mfma_f32_16x16x32_bf16 v[92:95], v[56:59], a[112:115], v[92:95]\n"
"ds_write_addtid_b32 v99 offset:14496\n"
"v_cndmask_b32_e64 v108, v108, v109, s[62:63]\n"
"v_mfma_f32_16x16x32_bf16 v[92:95], v[60:63], a[116:119], v[92:95]\n"
"v_cndmask_b32_e64 v110, v110, v111, s[62:63]\n"
"v_cndmask_b32_e64 v113, v108, v110, s[64:65]\n"
"v_mfma_f32_16x16x32_bf16 v[92:95], v[64:67], a[120:123], v[92:95]\n"
"buffer_store_dwordx2 v[112:113], v13, s[40:43], s95 offen offset:0 sc1\n"
"v_mfma_f32_16x16x32_bf16 v[92:95], v[68:71], a[124:127], v[92:95]\n"
"v_mfma_f32_16x16x32_bf16 v[84:87], v[40:43], a[128:131], 0\n"
"v_mfma_f32_16x16x32_bf16 v[84:87], v[44:47], a[132:135], v[84:87]\n"
"ds_write_addtid_b32 v92 offset:768\n"
"v_mfma_f32_16x16x32_bf16 v[84:87], v[48:51], a[136:139], v[84:87]\n"
"ds_write_addtid_b32 v93 offset:5408\n"
"v_mfma_f32_16x16x32_bf16 v[84:87], v[52:55], a[140:143], v[84:87]\n"
"ds_write_addtid_b32 v94 offset:10112\n"
"v_mfma_f32_16x16x32_bf16 v[84:87], v[56:59], a[144:147], v[84:87]\n"
"ds_write_addtid_b32 v95 offset:14752\n"
"v_mfma_f32_16x16x32_bf16 v[84:87], v[60:63], a[148:151], v[84:87]\n"
"v_mfma_f32_16x16x32_bf16 v[84:87], v[64:67], a[152:155], v[84:87]\n"
"v_mfma_f32_16x16x32_bf16 v[84:87], v[68:71], a[156:159], v[84:87]\n"
"global_load_dwordx4 v[96:99], v10, s[56:57] sc1\n"
"v_mfma_f32_16x16x32_bf16 v[92:95], v[40:43], a[160:163], 0\n"
"v_mfma_f32_16x16x32_bf16 v[92:95], v[44:47], a[164:167], v[92:95]\n"
"s_add_u32 s58, s50, 0x180000\n"
"ds_write_addtid_b32 v84 offset:1024\n"
"v_mfma_f32_16x16x32_bf16 v[92:95], v[48:51], a[168:171], v[92:95]\n"
"s_and_b32 s58, s58, 0x3ffffff\n"
"ds_write_addtid_b32 v85 offset:5664\n"
"v_mfma_f32_16x16x32_bf16 v[92:95], v[52:55], a[172:175], v[92:95]\n"
"s_add_u32 s56, s40, s58\n"
"ds_write_addtid_b32 v86 offset:10368\n"
"v_mfma_f32_16x16x32_bf16 v[92:95], v[56:59], a[176:179], v[92:95]\n"
"s_addc_u32 s57, s41, 0\n"
"ds_write_addtid_b32 v87 offset:15008\n"
"v_mfma_f32_16x16x32_bf16 v[92:95], v[60:63], a[180:183], v[92:95]\n"
"s_waitcnt vmcnt(9)\n"
"v_cmp_ne_u64 vcc, s[48:49], v[124:125]\n"
"v_mfma_f32_16x16x32_bf16 v[92:95], v[64:67], a[184:187], v[92:95]\n"
"v_mfma_f32_16x16x32_bf16 v[92:95], v[68:71], a[188:191], v[92:95]\n"
"s_cbranch_vccnz .LO_1s\n"
".LO_1r:\n"
"v_mfma_f32_16x16x32_bf16 v[100:103], a[192:195], v[40:43], v[100:103]\n"
"s_add_u32 s50, s50, 0x180000\n"
"global_load_dwordx4 v[84:87], v5, s[56:57] offset:0 sc1\n"
"v_mfma_f32_16x16x32_bf16 v[100:103], a[196:199], v[44:47], v[100:103]\n"
"ds_write_addtid_b32 v92 offset:1280\n"
"v_mfma_f32_16x16x32_bf16 v[100:103], a[200:203], v[48:51], v[100:103]\n"
"ds_write_addtid_b32 v93 offset:5920\n"
"v_mfma_f32_16x16x32_bf16 v[100:103], a[204:207], v[52:55], v[100:103]\n"
"ds_write_addtid_b32 v94 offset:10624\n"
"v_mfma_f32_16x16x32_bf16 v[100:103], a[208:211], v[56:59], v[100:103]\n"
"ds_write_addtid_b32 v95 offset:15264\n"
"v_mfma_f32_16x16x32_bf16 v[100:103], a[212:215], v[60:63], v[100:103]\n"
"global_load_dwordx4 v[88:91], v5, s[56:57] offset:1024 sc1\n"
"v_mfma_f32_16x16x32_bf16 v[100:103], a[216:219], v[64:67], v[100:103]\n"
"v_mfma_f32_16x16x32_bf16 v[100:103], a[220:223], v[68:71], v[100:103]\n"
"v_mfma_f32_16x16x32_bf16 v[104:107], a[224:227], v[40:43], v[104:107]\n"
"s_waitcnt lgkmcnt(0)\n"
"v_mfma_f32_16x16x32_bf16 v[104:107], a[228:231], v[44:47], v[104:107]\n"
"s_barrier\n"
"v_mfma_f32_16x16x32_bf16 v[104:107], a[232:235], v[48:51], v[104:107]\n"
"global_load_dwordx4 v[92:95], v6, s[56:57] offset:0 sc1\n"
"ds_read_b128 v[40:43], v2 offset:1536\n"
"v_mfma_f32_16x16x32_bf16 v[104:107], a[236:239], v[52:55], v[104:107]\n"
"ds_read_b128 v[44:47], v3 offset:512\n"
"ds_read_b128 v[48:51], v4 offset:2560\n"
"v_mfma_f32_16x16x32_bf16 v[104:107], a[240:243], v[56:59], v[104:107]\n"
"ds_read_b128 v[108:111], v2 offset:1792\n"
"ds_read_b128 v[112:115], v3 offset:768\n"
"v_mfma_f32_16x16x32_bf16 v[104:107], a[244:247], v[60:63], v[104:107]\n"
"ds_read_b128 v[116:119], v4 offset:2816\n"
"v_mfma_f32_16x16x32_bf16 v[104:107], a[248:251], v[64:67], v[104:107]\n"
"buffer_load_dwordx2 v[120:121], v9, s[40:43], s50 offen sc1\n"
"v_mfma_f32_16x16x32_bf16 v[104:107], a[252:255], v[68:71], v[104:107]\n"
"global_load_dwordx4 v[52:55], v6, s[56:57] offset:1024 sc1\n"
"v_mfma_f32_16x16x32_bf16 v[64:67], v[72:75], a[0:3], 0\n"
"v_mfma_f32_16x16x32_bf16 v[64:67], v[76:79], a[4:7], v[64:67]\n"
"s_waitcnt vmcnt(13)\n"
"v_mfma_f32_16x16x32_bf16 v[64:67], v[80:83], a[8:11], v[64:67]\n"
"s_waitcnt lgkmcnt(3)\n"
"v_add_f32 v40, v40, v44\n"
"v_add_f32 v41, v41, v45\n"
"v_mfma_f32_16x16x32_bf16 v[64:67], v[20:23], a[12:15], v[64:67]\n"
"s_waitcnt vmcnt(11)\n"
"v_add_f32 v42, v42, v46\n"
"v_add_f32 v43, v43, v47\n"
"v_mfma_f32_16x16x32_bf16 v[64:67], v[24:27], a[16:19], v[64:67]\n"
"global_load_dwordx4 v[56:59], v7, s[56:57] offset:0 sc1\n"
"v_add_f32 v40, v40, v48\n"
"v_mfma_f32_16x16x32_bf16 v[64:67], v[28:31], a[20:23], v[64:67]\n"
"s_waitcnt vmcnt(9)\n"
"v_add_f32 v41, v41, v49\n"
"v_add_f32 v42, v42, v50\n"
"v_mfma_f32_16x16x32_bf16 v[64:67], v[32:35], a[24:27], v[64:67]\n"
"v_add_f32 v43, v43, v51\n"
"v_add_f32 v40, v40, v100\n"
"v_mfma_f32_16x16x32_bf16 v[64:67], v[36:39], a[28:31], v[64:67]\n"
"v_add_f32 v41, v41, v101\n"
"v_add_f32 v42, v42, v102\n"
"v_mfma_f32_16x16x32_bf16 v[44:47], v[72:75], a[32:35], 0\n"
"v_add_f32 v43, v43, v103\n"
"v_mfma_f32_16x16x32_bf16 v[44:47], v[76:79], a[36:39], v[44:47]\n"
"ds_write_addtid_b32 v64 offset:1536\n"
"global_load_dwordx4 v[60:63], v7, s[56:57] offset:1024 sc1\n"
"v_mfma_f32_16x16x32_bf16 v[44:47], v[80:83], a[40:43], v[44:47]\n"
"s_add_u32 s92, s50, 0xffd00000\n"
"s_and_b32 s95, s92, 0xfbffffff\n"
"ds_write_addtid_b32 v65 offset:6176\n"
"v_mfma_f32_16x16x32_bf16 v[44:47], v[20:23], a[44:47], v[44:47]\n"
"ds_write_addtid_b32 v66 offset:10880\n"
"v_cvt_pk_bf16_f32 v48, v40, v41\n"
"v_mfma_f32_16x16x32_bf16 v[44:47], v[24:27], a[48:51], v[44:47]\n"
"ds_write_addtid_b32 v67 offset:15520\n"
"v_cvt_pk_bf16_f32 v49, v42, v43\n"
"v_mfma_f32_16x16x32_bf16 v[44:47], v[28:31], a[52:55], v[44:47]\n"
"s_waitcnt lgkmcnt(4)\n"
"v_add_f32 v108, v108, v112\n"
"v_add_f32 v109, v109, v113\n"
"v_mfma_f32_16x16x32_bf16 v[44:47], v[32:35], a[56:59], v[44:47]\n"
"s_waitcnt vmcnt(8)\n"
"global_load_dwordx4 v[64:67], v8, s[56:57] offset:0 sc1\n"
"v_add_f32 v110, v110, v114\n"
"v_mfma_f32_16x16x32_bf16 v[44:47], v[36:39], a[60:63], v[44:47]\n"
"buffer_store_dwordx2 v[0:1], v12, s[40:43], s93 offen offset:0 sc1\n"
"v_add_f32 v111, v111, v115\n"
"v_mfma_f32_16x16x32_bf16 v[40:43], v[72:75], a[64:67], 0\n"
"v_add_f32 v108, v108, v116\n"
"v_add_f32 v109, v109, v117\n"
"v_mfma_f32_16x16x32_bf16 v[40:43], v[76:79], a[68:71], v[40:43]\n"
"ds_write_addtid_b32 v44 offset:1792\n"
"v_add_f32 v110, v110, v118\n"
"v_mfma_f32_16x16x32_bf16 v[40:43], v[80:83], a[72:75], v[40:43]\n"
"ds_write_addtid_b32 v45 offset:6432\n"
"v_add_f32 v111, v111, v119\n"
"v_mfma_f32_16x16x32_bf16 v[40:43], v[20:23], a[76:79], v[40:43]\n"
"ds_write_addtid_b32 v46 offset:11136\n"
"global_load_dwordx4 v[68:71], v8, s[56:57] offset:1024 sc1\n"
"v_mfma_f32_16x16x32_bf16 v[40:43], v[24:27], a[80:83], v[40:43]\n"
"ds_write_addtid_b32 v47 offset:15776\n"
"v_add_f32 v108, v108, v104\n"
"v_mfma_f32_16x16x32_bf16 v[40:43], v[28:31], a[84:87], v[40:43]\n"
"v_add_f32 v109, v109, v105\n"
"v_add_f32 v110, v110, v106\n"
"v_mfma_f32_16x16x32_bf16 v[40:43], v[32:35], a[88:91], v[40:43]\n"
"v_add_f32 v111, v111, v107\n"
"v_cvt_pk_bf16_f32 v50, v108, v109\n"
"v_mfma_f32_16x16x32_bf16 v[40:43], v[36:39], a[92:95], v[40:43]\n"
"s_waitcnt vmcnt(10)\n"
"v_cvt_pk_bf16_f32 v51, v110, v111\n"
"v_lshlrev_b32 v112, 16, v96\n"
"v_mfma_f32_16x16x32_bf16 v[44:47], v[72:75], a[96:99], 0\n"
"v_and_b32 v113, 0xffff0000, v96\n"
"v_lshlrev_b32 v114, 16, v97\n"
"v_mfma_f32_16x16x32_bf16 v[44:47], v[76:79], a[100:103], v[44:47]\n"
"ds_write_addtid_b32 v40 offset:2048\n"
"v_and_b32 v115, 0xffff0000, v97\n"
"v_mfma_f32_16x16x32_bf16 v[44:47], v[80:83], a[104:107], v[44:47]\n"
"ds_write_addtid_b32 v41 offset:6688\n"
"v_lshlrev_b32 v108, 16, v98\n"
"v_mfma_f32_16x16x32_bf16 v[44:47], v[20:23], a[108:111], v[44:47]\n"
"ds_write_addtid_b32 v42 offset:11392\n"
"v_and_b32 v109, 0xffff0000, v98\n"
"v_mfma_f32_16x16x32_bf16 v[44:47], v[24:27], a[112:115], v[44:47]\n"
"ds_write_addtid_b32 v43 offset:16032\n"
"v_lshlrev_b32 v110, 16, v99\n"
"v_mfma_f32_16x16x32_bf16 v[44:47], v[28:31], a[116:119], v[44:47]\n"
"v_and_b32 v111, 0xffff0000, v99\n"
"v_mfma_f32_16x16x32_bf16 v[44:47], v[32:35], a[120:123], v[44:47]\n"
"v_mfma_f32_16x16x32_bf16 v[44:47], v[36:39], a[124:127], v[44:47]\n"
"buffer_store_dwordx4 v[48:51], v11, s[40:43], s95 offen sc1\n"
"v_mfma_f32_16x16x32_bf16 v[40:43], v[48:51], v[48:51], 0\n"
"v_mfma_f32_16x16x32_bf16 v[96:99], v[14:17], v[48:51], 0\n"
"v_mfma_f32_16x16x32_bf16 v[100:103], v[72:75], a[128:131], 0\n"
"v_cndmask_b32_e64 v40, v40, v41, s[62:63]\n"
"v_cndmask_b32_e64 v42, v42, v43, s[62:63]\n"
"v_mfma_f32_16x16x32_bf16 v[100:103], v[76:79], a[132:135], v[100:103]\n"
"ds_write_addtid_b32 v44 offset:2304\n"
"v_cndmask_b32_e64 v97, v40, v42, s[64:65]\n"
"v_mfma_f32_16x16x32_bf16 v[100:103], v[80:83], a[136:139], v[100:103]\n"
"buffer_store_dwordx2 v[96:97], v13, s[40:43], s95 offen offset:0 sc1\n"
"ds_write_addtid_b32 v45 offset:6944\n"
"v_mfma_f32_16x16x32_bf16 v[100:103], v[20:23], a[140:143], v[100:103]\n"
"ds_write_addtid_b32 v46 offset:11648\n"
"v_mfma_f32_16x16x32_bf16 v[100:103], v[24:27], a[144:147], v[100:103]\n"
"ds_write_addtid_b32 v47 offset:16288\n"
"global_load_dwordx4 v[116:119], v10, s[56:57] sc1\n"
"v_mfma_f32_16x16x32_bf16 v[100:103], v[28:31], a[148:151], v[100:103]\n"
"v_mfma_f32_16x16x32_bf16 v[100:103], v[32:35], a[152:155], v[100:103]\n"
"v_mfma_f32_16x16x32_bf16 v[100:103], v[36:39], a[156:159], v[100:103]\n"
"v_mfma_f32_16x16x32_bf16 v[40:43], v[72:75], a[160:163], 0\n"
"v_mfma_f32_16x16x32_bf16 v[40:43], v[76:79], a[164:167], v[40:43]\n"
"ds_write_addtid_b32 v100 offset:2560\n"
"v_mfma_f32_16x16x32_bf16 v[40:43], v[80:83], a[168:171], v[40:43]\n"
"s_add_u32 s58, s50, 0x180000\n"
"ds_write_addtid_b32 v101 offset:7200\n"
"v_mfma_f32_16x16x32_bf16 v[40:43], v[20:23], a[172:175], v[40:43]\n"
"s_and_b32 s58, s58, 0x3ffffff\n"
"ds_write_addtid_b32 v102 offset:11904\n"
"v_mfma_f32_16x16x32_bf16 v[40:43], v[24:27], a[176:179], v[40:43]\n"
"s_add_u32 s56, s40, s58\n"
"ds_write_addtid_b32 v103 offset:16544\n"
"v_mfma_f32_16x16x32_bf16 v[40:43], v[28:31], a[180:183], v[40:43]\n"
"s_addc_u32 s57, s41, 0\n"
"v_mfma_f32_16x16x32_bf16 v[40:43], v[32:35], a[184:187], v[40:43]\n"
"s_waitcnt vmcnt(9)\n"
"v_cmp_ne_u64 vcc, s[48:49], v[120:121]\n"
"v_mfma_f32_16x16x32_bf16 v[40:43], v[36:39], a[188:191], v[40:43]\n"
"v_mfma_f32_16x16x32_bf16 v[112:115], a[192:195], v[72:75], v[112:115]\n"
"s_cbranch_vccnz .LO_2s\n"
".LO_2r:\n"
"v_mfma_f32_16x16x32_bf16 v[112:115], a[196:199], v[76:79], v[112:115]\n"
"s_add_u32 s50, s50, 0x180000\n"
"ds_write_addtid_b32 v40 offset:2816\n"
"global_load_dwordx4 v[96:99], v5, s[56:57] offset:0 sc1\n"
"v_mfma_f32_16x16x32_bf16 v[112:115], a[200:203], v[80:83], v[112:115]\n"
"ds_write_addtid_b32 v41 offset:7456\n"
"v_mfma_f32_16x16x32_bf16 v[112:115], a[204:207], v[20:23], v[112:115]\n"
"ds_write_addtid_b32 v42 offset:12160\n"
"v_mfma_f32_16x16x32_bf16 v[112:115], a[208:211], v[24:27], v[112:115]\n"
"ds_write_addtid_b32 v43 offset:16800\n"
"v_mfma_f32_16x16x32_bf16 v[112:115], a[212:215], v[28:31], v[112:115]\n"
"v_mfma_f32_16x16x32_bf16 v[112:115], a[216:219], v[32:35], v[112:115]\n"
"global_load_dwordx4 v[100:103], v5, s[56:57] offset:1024 sc1\n"
"v_mfma_f32_16x16x32_bf16 v[112:115], a[220:223], v[36:39], v[112:115]\n"
"v_mfma_f32_16x16x32_bf16 v[108:111], a[224:227], v[72:75], v[108:111]\n"
"s_waitcnt lgkmcnt(0)\n"
"v_mfma_f32_16x16x32_bf16 v[108:111], a[228:231], v[76:79], v[108:111]\n"
"s_barrier\n"
"v_mfma_f32_16x16x32_bf16 v[108:111], a[232:235], v[80:83], v[108:111]\n"
"buffer_load_dwordx2 v[124:125], v9, s[40:43], s50 offen sc1\n"
"v_mfma_f32_16x16x32_bf16 v[108:111], a[236:239], v[20:23], v[108:111]\n"
"global_load_dwordx4 v[104:107], v6, s[56:57] offset:0 sc1\n"
"ds_read_b128 v[72:75], v2 offset:0\n"
"v_mfma_f32_16x16x32_bf16 v[108:111], a[240:243], v[24:27], v[108:111]\n"
"ds_read_b128 v[44:47], v3 offset:2048\n"
"ds_read_b128 v[48:51], v4 offset:1024\n"
"v_mfma_f32_16x16x32_bf16 v[108:111], a[244:247], v[28:31], v[108:111]\n"
"ds_read_b128 v[76:79], v2 offset:256\n"
"ds_read_b128 v[80:83], v3 offset:2304\n"
"v_mfma_f32_16x16x32_bf16 v[108:111], a[248:251], v[32:35], v[108:111]\n"
"ds_read_b128 v[120:123], v4 offset:1280\n"
"v_mfma_f32_16x16x32_bf16 v[108:111], a[252:255], v[36:39], v[108:111]\n"
"s_branch .LO_top\n"
".LO_1s:\n"
"s_cmp_ge_u32 s50, s53\n"
"s_cbranch_scc1 .LO_end\n"
"s_cmp_ge_u32 s50, s52\n"
"s_cbranch_scc1 .LO_1r\n"
"s_sub_u32 s60, s50, 0x0\n"
"s_mov_b32 s61, 200000\n"
"s_nop 7\n"
".LO_1p:\n"
"buffer_load_dwordx2 v[124:125], v9, s[40:43], s60 offen sc1\n"
"s_waitcnt vmcnt(0)\n"
"v_cmp_ne_u64 vcc, s[48:49], v[124:125]\n"
"s_cbranch_vccz .LO_1px\n"
"s_sleep 1\n"
"s_sub_u32 s61, s61, 1\n"
"s_cbranch_scc0 .LO_1p\n"
"s_branch .LO_end\n"
".LO_1px:\n"
"s_branch .LO_1r\n"
".LO_2s:\n"
"s_cmp_ge_u32 s50, s53\n"
"s_cbranch_scc1 .LO_end\n"
"s_cmp_ge_u32 s50, s52\n"
"s_cbranch_scc1 .LO_2r\n"
"s_sub_u32 s60, s50, 0x0\n"
"s_mov_b32 s61, 200000\n"
"s_nop 7\n"
".LO_2p:\n"
"buffer_load_dwordx2 v[120:121], v9, s[40:43], s60 offen sc1\n"
"s_waitcnt vmcnt(0)\n"
"v_cmp_ne_u64 vcc, s[48:49], v[120:121]\n"
"s_cbranch_vccz .LO_2px\n"
"s_sleep 1\n"
"s_sub_u32 s61, s61, 1\n"
"s_cbranch_scc0 .LO_2p\n"
"s_branch .LO_end\n"
".LO_2px:\n"
"s_branch .LO_2r\n"
".LO_3s:\n"
"s_cmp_ge_u32 s50, s53\n"
"s_cbranch_scc1 .LO_end\n"
"s_cmp_ge_u32 s50, s52\n"
"s_cbranch_scc1 .LO_3r\n"
"s_sub_u32 s60, s50, 0x0\n"
"s_mov_b32 s61, 200000\n"
"s_nop 7\n"
".LO_3p:\n"
"buffer_load_dwordx2 v[124:125], v9, s[40:43], s60 offen sc1\n"
"s_waitcnt vmcnt(0)\n"
"v_cmp_ne_u64 vcc, s[48:49], v[124:125]\n"
"s_cbranch_vccz .LO_3px\n"
"s_sleep 1\n"
"s_sub_u32 s61, s61, 1\n"
"s_cbranch_scc0 .LO_3p\n"
"s_branch .LO_end\n"
".LO_3px:\n"
"s_branch .LO_3r\n"
".LO_4s:\n"
"s_cmp_ge_u32 s50, s53\n"
"s_cbranch_scc1 .LO_end\n"
"s_cmp_ge_u32 s50, s52\n"
"s_cbranch_scc1 .LO_4r\n"
"s_sub_u32 s60, s50, 0x0\n"
"s_mov_b32 s61, 200000\n"
"s_nop 7\n"
".LO_4p:\n"
"buffer_load_dwordx2 v[120:121], v9, s[40:43], s60 offen sc1\n"
"s_waitcnt vmcnt(0)\n"
"v_cmp_ne_u64 vcc, s[48:49], v[120:121]\n"
"s_cbranch_vccz .LO_4px\n"
"s_sleep 1\n"
"s_sub_u32 s61, s61, 1\n"
"s_cbranch_scc0 .LO_4p\n"
"s_branch .LO_end\n"
".LO_4px:\n"
"s_branch .LO_4r\n"
".LO_end:\n"
"s_waitcnt vmcnt(0) lgkmcnt(0)\n"
"s_endpgm\n"
      :: "{v2}"((unsigned)(lds0 + ((wid + 3) & 3) * 18592 + trp)), "{v3}"((unsigned)(lds0 + ((wid + 2) & 3) * 18592 + trp)), "{v4}"((unsigned)(lds0 + ((wid + 1) & 3) * 18592 + trp)), "{v5}"((unsigned)(lane * 16 + wid * 8192 + ((0 + rot) % 4) * 2048 + 134250496)), "{v6}"((unsigned)(lane * 16 + wid * 8192 + ((1 + rot) % 4) * 2048 + 134250496)), "{v7}"((unsigned)(lane * 16 + wid * 8192 + ((2 + rot) % 4) * 2048 + 134250496)), "{v8}"((unsigned)(lane * 16 + wid * 8192 + ((3 + rot) % 4) * 2048 + 134250496)), "{v9}"((unsigned)((wid * 8 + (lane & 7)) * 8 + 352512 + O_GL * 1572864)), "{v10}"((unsigned)(lane * 16 + pair * 1024 + 134217728)), "{v11}"((unsigned)(lane * 16 + pair * 1024 + 65536)), "{v12}"((unsigned)(lane ? DROP : pair * 8 + 352768)), "{v13}"((unsigned)(lg == (li >> 2) ? pair * 128 + li * 8 + 344064 : DROP)), "{v14}"((unsigned)(0x3f803f80)), "{v15}"((unsigned)(0x3f803f80)), "{v16}"((unsigned)(0x3f803f80)), "{v17}"((unsigned)(0x3f803f80)), "{v20}"((unsigned)(vT + tsw)), "{v21}"((unsigned)(vT + (tsw ^ 8))), "{v22}"((unsigned)(vD + ((0 + rot) % 4) * 131072)), "{v23}"((unsigned)(vD + ((1 + rot) % 4) * 131072)), "{v24}"((unsigned)(vD + ((2 + rot) % 4) * 131072)), "{v25}"((unsigned)(vD + ((3 + rot) % 4) * 131072)), "{s[40:43]}"(rS), "{s[44:47]}"(rW), "{s[48:49]}"(wsp), "{s50}"((unsigned)(sA)), "{s51}"((unsigned)(sLo)), "{s52}"((unsigned)(sHi)), "{s53}"((unsigned)(sEx)), "{s54}"((unsigned)(sEnt)), "{s55}"((unsigned)(sLd)), "{s[56:57]}"(wsb + (sLo & 0x3ffffff)), "{s59}"((unsigned)(lds0 + wid * 18592)), "{s[62:63]}"(0xAAAAAAAAAAAAAAAAull), "{s[64:65]}"(0xCCCCCCCCCCCCCCCCull), "{s66}"((unsigned)(sW0 + 64 * ((wid + 1) & 3))), "{s67}"((unsigned)(sW0 + 64 * ((wid + 2) & 3))), "{s68}"((unsigned)(sW0 + 64 * ((wid + 3) & 3))), "{s69}"((unsigned)(sW0 + 64 * ((wid + 4) & 3)))
      : "v255", "a255", "s95", "vcc", "scc", "memory");

  } else if (role == 2) {
    const unsigned sEx = sHi + F_EX * 3 * RS, sEnt = -wid & 3, tsw = 0, pair = idx * 4 + wid, rot = KR * (set ? set & 2 : 1);
    const unsigned sW0 = wid * 256 * 7168 + idx * 256, vD = (lane >> 2) * 7168 + (lane & 3) * 16;
    const u32x4 rW = desc(a.w1, 1024 * 7168);
    asm volatile(
"s_nop 4\n"
"v_mov_b32 v186, 0\n"
"global_load_dwordx2 v[0:1], v186, s[48:49] sc1\n"
"global_load_dwordx4 v[30:33], v38, s[64:65]\n"
"v_mov_b32 v188, 0x3f803f80\n"
"v_mov_b32 v189, 0x3f803f80\n"
"v_mov_b32 v190, 0x3f803f80\n"
"v_mov_b32 v191, 0x3f803f80\n"
"s_add_u32 m0, s55, 0\n"
"s_add_u32 s60, s66, 0\n"
"buffer_load_dwordx4 v39, s[44:47], s60 offen lds\n"
"s_add_u32 m0, s55, 1024\n"
"s_add_u32 s60, s66, 114688\n"
"buffer_load_dwordx4 v39, s[44:47], s60 offen lds\n"
"s_add_u32 m0, s55, 2048\n"
"s_add_u32 s60, s67, 0\n"
"buffer_load_dwordx4 v39, s[44:47], s60 offen lds\n"
"s_add_u32 m0, s55, 3072\n"
"s_add_u32 s60, s67, 114688\n"
"buffer_load_dwordx4 v39, s[44:47], s60 offen lds\n"
"s_add_u32 m0, s55, 4096\n"
"s_add_u32 s60, s68, 0\n"
"buffer_load_dwordx4 v39, s[44:47], s60 offen lds\n"
"s_add_u32 m0, s55, 5120\n"
"s_add_u32 s60, s68, 114688\n"
"buffer_load_dwordx4 v39, s[44:47], s60 offen lds\n"
"s_add_u32 m0, s55, 6144\n"
"s_add_u32 s60, s69, 0\n"
"buffer_load_dwordx4 v39, s[44:47], s60 offen lds\n"
"s_add_u32 m0, s55, 7168\n"
"s_add_u32 s60, s69, 114688\n"
"buffer_load_dwordx4 v39, s[44:47], s60 offen lds\n"
"s_add_u32 m0, s55, 8192\n"
"s_add_u32 s60, s66, 229376\n"
"buffer_load_dwordx4 v39, s[44:47], s60 offen lds\n"
"s_add_u32 m0, s55, 9216\n"
"s_add_u32 s60, s66, 344064\n"
"buffer_load_dwordx4 v39, s[44:47], s60 offen lds\n"
"s_add_u32 m0, s55, 10240\n"
"s_add_u32 s60, s67, 229376\n"
"buffer_load_dwordx4 v39, s[44:47], s60 offen lds\n"
"s_add_u32 m0, s55, 11264\n"
"s_add_u32 s60, s67, 344064\n"
"buffer_load_dwordx4 v39, s[44:47], s60 offen lds\n"
"s_add_u32 m0, s55, 12288\n"
"s_add_u32 s60, s68, 229376\n"
"buffer_load_dwordx4 v39, s[44:47], s60 offen lds\n"
"s_add_u32 m0, s55, 13312\n"
"s_add_u32 s60, s68, 344064\n"
"buffer_load_dwordx4 v39, s[44:47], s60 offen lds\n"
"s_add_u32 m0, s55, 14336\n"
"s_add_u32 s60, s69, 229376\n"
"buffer_load_dwordx4 v39, s[44:47], s60 offen lds\n"
"s_add_u32 m0, s55, 15360\n"
"s_add_u32 s60, s69, 344064\n"
"buffer_load_dwordx4 v39, s[44:47], s60 offen lds\n"
"s_add_u32 m0, s55, 16384\n"
"s_add_u32 s60, s66, 0\n"
"buffer_load_dwordx4 v40, s[44:47], s60 offen lds\n"
"s_add_u32 m0, s55, 17408\n"
"s_add_u32 s60, s66, 114688\n"
"buffer_load_dwordx4 v40, s[44:47], s60 offen lds\n"
"s_add_u32 m0, s55, 18432\n"
"s_add_u32 s60, s67, 0\n"
"buffer_load_dwordx4 v40, s[44:47], s60 offen lds\n"
"s_add_u32 m0, s55, 19456\n"
"s_add_u32 s60, s67, 114688\n"
"buffer_load_dwordx4 v40, s[44:47], s60 offen lds\n"
"s_add_u32 m0, s55, 20480\n"
"s_add_u32 s60, s68, 0\n"
"buffer_load_dwordx4 v40, s[44:47], s60 offen lds\n"
"s_add_u32 m0, s55, 21504\n"
"s_add_u32 s60, s68, 114688\n"
"buffer_load_dwordx4 v40, s[44:47], s60 offen lds\n"
"s_add_u32 m0, s55, 22528\n"
"s_add_u32 s60, s69, 0\n"
"buffer_load_dwordx4 v40, s[44:47], s60 offen lds\n"
"s_add_u32 m0, s55, 23552\n"
"s_add_u32 s60, s69, 114688\n"
"buffer_load_dwordx4 v40, s[44:47], s60 offen lds\n"
"s_add_u32 m0, s55, 24576\n"
"s_add_u32 s60, s66, 229376\n"
"buffer_load_dwordx4 v40, s[44:47], s60 offen lds\n"
"s_add_u32 m0, s55, 25600\n"
"s_add_u32 s60, s66, 344064\n"
"buffer_load_dwordx4 v40, s[44:47], s60 offen lds\n"
"s_add_u32 m0, s55, 26624\n"
"s_add_u32 s60, s67, 229376\n"
"buffer_load_dwordx4 v40, s[44:47], s60 offen lds\n"
"s_add_u32 m0, s55, 27648\n"
"s_add_u32 s60, s67, 344064\n"
"buffer_load_dwordx4 v40, s[44:47], s60 offen lds\n"
"s_add_u32 m0, s55, 28672\n"
"s_add_u32 s60, s68, 229376\n"
"buffer_load_dwordx4 v40, s[44:47], s60 offen lds\n"
"s_add_u32 m0, s55, 29696\n"
"s_add_u32 s60, s68, 344064\n"
"buffer_load_dwordx4 v40, s[44:47], s60 offen lds\n"
"s_add_u32 m0, s55, 30720\n"
"s_add_u32 s60, s69, 229376\n"
"buffer_load_dwordx4 v40, s[44:47], s60 offen lds\n"
"s_add_u32 m0, s55, 31744\n"
"s_add_u32 s60, s69, 344064\n"
"buffer_load_dwordx4 v40, s[44:47], s60 offen lds\n"
"s_add_u32 m0, s55, 32768\n"
"s_add_u32 s60, s66, 0\n"
"buffer_load_dwordx4 v41, s[44:47], s60 offen lds\n"
"s_add_u32 m0, s55, 33792\n"
"s_add_u32 s60, s66, 114688\n"
"buffer_load_dwordx4 v41, s[44:47], s60 offen lds\n"
"s_add_u32 m0, s55, 34816\n"
"s_add_u32 s60, s67, 0\n"
"buffer_load_dwordx4 v41, s[44:47], s60 offen lds\n"
"s_add_u32 m0, s55, 35840\n"
"s_add_u32 s60, s67, 114688\n"
"buffer_load_dwordx4 v41, s[44:47], s60 offen lds\n"
"s_add_u32 m0, s55, 36864\n"
"s_add_u32 s60, s68, 0\n"
"buffer_load_dwordx4 v41, s[44:47], s60 offen lds\n"
"s_add_u32 m0, s55, 37888\n"
"s_add_u32 s60, s68, 114688\n"
"buffer_load_dwordx4 v41, s[44:47], s60 offen lds\n"
"s_add_u32 m0, s55, 38912\n"
"s_add_u32 s60, s69, 0\n"
"buffer_load_dwordx4 v41, s[44:47], s60 offen lds\n"
"s_add_u32 m0, s55, 39936\n"
"s_add_u32 s60, s69, 114688\n"
"buffer_load_dwordx4 v41, s[44:47], s60 offen lds\n"
"s_waitcnt vmcnt(38)\n"
"ds_read_b64_tr_b16 a[0:1], v36 offset:0\n"
"ds_read_b64_tr_b16 a[2:3], v36 offset:256\n"
"ds_read_b64_tr_b16 a[32:33], v37 offset:0\n"
"ds_read_b64_tr_b16 a[34:35], v37 offset:256\n"
"v_add_u32 v0, 2, v0\n"
"v_add_u32 v1, 0x9e3779b9, v1\n"
"v_or_b32 v0, 1, v0\n"
"v_readfirstlane_b32 s49, v1\n"
"v_readfirstlane_b32 s48, v0\n"
"s_waitcnt vmcnt(36)\n"
"ds_read_b64_tr_b16 a[64:65], v36 offset:2048\n"
"ds_read_b64_tr_b16 a[66:67], v36 offset:2304\n"
"ds_read_b64_tr_b16 a[96:97], v37 offset:2048\n"
"ds_read_b64_tr_b16 a[98:99], v37 offset:2304\n"
"s_waitcnt lgkmcnt(4)\n"
"s_add_u32 m0, s55, 0\n"
"s_add_u32 s60, s66, 229376\n"
"buffer_load_dwordx4 v41, s[44:47], s60 offen lds\n"
"s_add_u32 m0, s55, 1024\n"
"s_add_u32 s60, s66, 344064\n"
"buffer_load_dwordx4 v41, s[44:47], s60 offen lds\n"
"v_mfma_f32_16x16x32_bf16 v[192:195], v[188:191], a[0:3], 0\n"
"v_mfma_f32_16x16x32_bf16 v[196:199], v[188:191], a[32:35], 0\n"
"s_waitcnt vmcnt(36)\n"
"ds_read_b64_tr_b16 a[128:129], v36 offset:4096\n"
"ds_read_b64_tr_b16 a[130:131], v36 offset:4352\n"
"ds_read_b64_tr_b16 a[160:161], v37 offset:4096\n"
"ds_read_b64_tr_b16 a[162:163], v37 offset:4352\n"
"s_waitcnt lgkmcnt(4)\n"
"s_add_u32 m0, s55, 2048\n"
"s_add_u32 s60, s67, 229376\n"
"buffer_load_dwordx4 v41, s[44:47], s60 offen lds\n"
"s_add_u32 m0, s55, 3072\n"
"s_add_u32 s60, s67, 344064\n"
"buffer_load_dwordx4 v41, s[44:47], s60 offen lds\n"
"v_mfma_f32_16x16x32_bf16 v[200:203], v[188:191], a[64:67], 0\n"
"v_mfma_f32_16x16x32_bf16 v[204:207], v[188:191], a[96:99], 0\n"
"s_waitcnt vmcnt(36)\n"
"ds_read_b64_tr_b16 a[192:193], v36 offset:6144\n"
"ds_read_b64_tr_b16 a[194:195], v36 offset:6400\n"
"ds_read_b64_tr_b16 a[224:225], v37 offset:6144\n"
"ds_read_b64_tr_b16 a[226:227], v37 offset:6400\n"
"s_waitcnt lgkmcnt(4)\n"
"s_add_u32 m0, s55, 4096\n"
"s_add_u32 s60, s68, 229376\n"
"buffer_load_dwordx4 v41, s[44:47], s60 offen lds\n"
"s_add_u32 m0, s55, 5120\n"
"s_add_u32 s60, s68, 344064\n"
"buffer_load_dwordx4 v41, s[44:47], s60 offen lds\n"
"v_mfma_f32_16x16x32_bf16 v[208:211], v[188:191], a[128:131], 0\n"
"v_mfma_f32_16x16x32_bf16 v[212:215], v[188:191], a[160:163], 0\n"
"s_waitcnt vmcnt(36)\n"
"ds_read_b64_tr_b16 a[4:5], v36 offset:8192\n"
"ds_read_b64_tr_b16 a[6:7], v36 offset:8448\n"
"ds_read_b64_tr_b16 a[36:37], v37 offset:8192\n"
"ds_read_b64_tr_b16 a[38:39], v37 offset:8448\n"
"s_waitcnt lgkmcnt(4)\n"
"s_add_u32 m0, s55, 6144\n"
"s_add_u32 s60, s69, 229376\n"
"buffer_load_dwordx4 v41, s[44:47], s60 offen lds\n"
"s_add_u32 m0, s55, 7168\n"
"s_add_u32 s60, s69, 344064\n"
"buffer_load_dwordx4 v41, s[44:47], s60 offen lds\n"
"v_mfma_f32_16x16x32_bf16 v[216:219], v[188:191], a[192:195], 0\n"
"v_mfma_f32_16x16x32_bf16 v[220:223], v[188:191], a[224:227], 0\n"
"s_waitcnt vmcnt(36)\n"
"ds_read_b64_tr_b16 a[68:69], v36 offset:10240\n"
"ds_read_b64_tr_b16 a[70:71], v36 offset:10496\n"
"ds_read_b64_tr_b16 a[100:101], v37 offset:10240\n"
"ds_read_b64_tr_b16 a[102:103], v37 offset:10496\n"
"s_waitcnt lgkmcnt(4)\n"
"s_add_u32 m0, s55, 8192\n"
"s_add_u32 s60, s66, 0\n"
"buffer_load_dwordx4 v42, s[44:47], s60 offen lds\n"
"s_add_u32 m0, s55, 9216\n"
"s_add_u32 s60, s66, 114688\n"
"buffer_load_dwordx4 v42, s[44:47], s60 offen lds\n"
"v_mfma_f32_16x16x32_bf16 v[192:195], v[188:191], a[4:7], v[192:195]\n"
"v_mfma_f32_16x16x32_bf16 v[196:199], v[188:191], a[36:39], v[196:199]\n"
"s_waitcnt vmcnt(36)\n"
"ds_read_b64_tr_b16 a[132:133], v36 offset:12288\n"
"ds_read_b64_tr_b16 a[134:135], v36 offset:12544\n"
"ds_read_b64_tr_b16 a[164:165], v37 offset:12288\n"
"ds_read_b64_tr_b16 a[166:167], v37 offset:12544\n"
"s_waitcnt lgkmcnt(4)\n"
"s_add_u32 m0, s55, 10240\n"
"s_add_u32 s60, s67, 0\n"
"buffer_load_dwordx4 v42, s[44:47], s60 offen lds\n"
"s_add_u32 m0, s55, 11264\n"
"s_add_u32 s60, s67, 114688\n"
"buffer_load_dwordx4 v42, s[44:47], s60 offen lds\n"
"v_mfma_f32_16x16x32_bf16 v[200:203], v[188:191], a[68:71], v[200:203]\n"
"v_mfma_f32_16x16x32_bf16 v[204:207], v[188:191], a[100:103], v[204:207]\n"
"s_waitcnt vmcnt(36)\n"
"ds_read_b64_tr_b16 a[196:197], v36 offset:14336\n"
"ds_read_b64_tr_b16 a[198:199], v36 offset:14592\n"
"ds_read_b64_tr_b16 a[228:229], v37 offset:14336\n"
"ds_read_b64_tr_b16 a[230:231], v37 offset:14592\n"
"s_waitcnt lgkmcnt(4)\n"
"s_add_u32 m0, s55, 12288\n"
"s_add_u32 s60, s68, 0\n"
"buffer_load_dwordx4 v42, s[44:47], s60 offen lds\n"
"s_add_u32 m0, s55, 13312\n"
"s_add_u32 s60, s68, 114688\n"
"buffer_load_dwordx4 v42, s[44:47], s60 offen lds\n"
"v_mfma_f32_16x16x32_bf16 v[208:211], v[188:191], a[132:135], v[208:211]\n"
"v_mfma_f32_16x16x32_bf16 v[212:215], v[188:191], a[164:167], v[212:215]\n"
"s_waitcnt vmcnt(36)\n"
"ds_read_b64_tr_b16 a[8:9], v36 offset:16384\n"
"ds_read_b64_tr_b16 a[10:11], v36 offset:16640\n"
"ds_read_b64_tr_b16 a[40:41], v37 offset:16384\n"
"ds_read_b64_tr_b16 a[42:43], v37 offset:16640\n"
"s_waitcnt lgkmcnt(4)\n"
"s_add_u32 m0, s55, 14336\n"
"s_add_u32 s60, s69, 0\n"
"buffer_load_dwordx4 v42, s[44:47], s60 offen lds\n"
"s_add_u32 m0, s55, 15360\n"
"s_add_u32 s60, s69, 114688\n"
"buffer_load_dwordx4 v42, s[44:47], s60 offen lds\n"
"v_mfma_f32_16x16x32_bf16 v[216:219], v[188:191], a[196:199], v[216:219]\n"
"v_mfma_f32_16x16x32_bf16 v[220:223], v[188:191], a[228:231], v[220:223]\n"
"s_waitcnt vmcnt(36)\n"
"ds_read_b64_tr_b16 a[72:73], v36 offset:18432\n"
"ds_read_b64_tr_b16 a[74:75], v36 offset:18688\n"
"ds_read_b64_tr_b16 a[104:105], v37 offset:18432\n"
"ds_read_b64_tr_b16 a[106:107], v37 offset:18688\n"
"s_waitcnt lgkmcnt(4)\n"
"s_add_u32 m0, s55, 16384\n"
"s_add_u32 s60, s66, 229376\n"
"buffer_load_dwordx4 v42, s[44:47], s60 offen lds\n"
"s_add_u32 m0, s55, 17408\n"
"s_add_u32 s60, s66, 344064\n"
"buffer_load_dwordx4 v42, s[44:47], s60 offen lds\n"
"v_mfma_f32_16x16x32_bf16 v[192:195], v[188:191], a[8:11], v[192:195]\n"
"v_mfma_f32_16x16x32_bf16 v[196:199], v[188:191], a[40:43], v[196:199]\n"
"s_waitcnt vmcnt(36)\n"
"ds_read_b64_tr_b16 a[136:137], v36 offset:20480\n"
"ds_read_b64_tr_b16 a[138:139], v36 offset:20736\n"
"ds_read_b64_tr_b16 a[168:169], v37 offset:20480\n"
"ds_read_b64_tr_b16 a[170:171], v37 offset:20736\n"
"s_waitcnt lgkmcnt(4)\n"
"s_add_u32 m0, s55, 18432\n"
"s_add_u32 s60, s67, 229376\n"
"buffer_load_dwordx4 v42, s[44:47], s60 offen lds\n"
"s_add_u32 m0, s55, 19456\n"
"s_add_u32 s60, s67, 344064\n"
"buffer_load_dwordx4 v42, s[44:47], s60 offen lds\n"
"v_mfma_f32_16x16x32_bf16 v[200:203], v[188:191], a[72:75], v[200:203]\n"
"v_mfma_f32_16x16x32_bf16 v[204:207], v[188:191], a[104:107], v[204:207]\n"
"s_waitcnt vmcnt(36)\n"
"ds_read_b64_tr_b16 a[200:201], v36 offset:22528\n"
"ds_read_b64_tr_b16 a[202:203], v36 offset:22784\n"
"ds_read_b64_tr_b16 a[232:233], v37 offset:22528\n"
"ds_read_b64_tr_b16 a[234:235], v37 offset:22784\n"
"s_waitcnt lgkmcnt(4)\n"
"s_add_u32 m0, s55, 20480\n"
"s_add_u32 s60, s68, 229376\n"
"buffer_load_dwordx4 v42, s[44:47], s60 offen lds\n"
"s_add_u32 m0, s55, 21504\n"
"s_add_u32 s60, s68, 344064\n"
"buffer_load_dwordx4 v42, s[44:47], s60 offen lds\n"
"v_mfma_f32_16x16x32_bf16 v[208:211], v[188:191], a[136:139], v[208:211]\n"
"v_mfma_f32_16x16x32_bf16 v[212:215], v[188:191], a[168:171], v[212:215]\n"
"s_waitcnt vmcnt(36)\n"
"ds_read_b64_tr_b16 a[12:13], v36 offset:24576\n"
"ds_read_b64_tr_b16 a[14:15], v36 offset:24832\n"
"ds_read_b64_tr_b16 a[44:45], v37 offset:24576\n"
"ds_read_b64_tr_b16 a[46:47], v37 offset:24832\n"
"s_waitcnt lgkmcnt(4)\n"
"s_add_u32 m0, s55, 22528\n"
"s_add_u32 s60, s69, 229376\n"
"buffer_load_dwordx4 v42, s[44:47], s60 offen lds\n"
"s_add_u32 m0, s55, 23552\n"
"s_add_u32 s60, s69, 344064\n"
"buffer_load_dwordx4 v42, s[44:47], s60 offen lds\n"
"v_mfma_f32_16x16x32_bf16 v[216:219], v[188:191], a[200:203], v[216:219]\n"
"v_mfma_f32_16x16x32_bf16 v[220:223], v[188:191], a[232:235], v[220:223]\n"
"s_waitcnt vmcnt(36)\n"
"ds_read_b64_tr_b16 a[76:77], v36 offset:26624\n"
"ds_read_b64_tr_b16 a[78:79], v36 offset:26880\n"
"ds_read_b64_tr_b16 a[108:109], v37 offset:26624\n"
"ds_read_b64_tr_b16 a[110:111], v37 offset:26880\n"
"s_waitcnt lgkmcnt(4)\n"
"v_mfma_f32_16x16x32_bf16 v[192:195], v[188:191], a[12:15], v[192:195]\n"
"v_mfma_f32_16x16x32_bf16 v[196:199], v[188:191], a[44:47], v[196:199]\n"
"s_waitcnt vmcnt(34)\n"
"ds_read_b64_tr_b16 a[140:141], v36 offset:28672\n"
"ds_read_b64_tr_b16 a[142:143], v36 offset:28928\n"
"ds_read_b64_tr_b16 a[172:173], v37 offset:28672\n"
"ds_read_b64_tr_b16 a[174:175], v37 offset:28928\n"
"s_waitcnt lgkmcnt(4)\n"
"v_mfma_f32_16x16x32_bf16 v[200:203], v[188:191], a[76:79], v[200:203]\n"
"v_mfma_f32_16x16x32_bf16 v[204:207], v[188:191], a[108:111], v[204:207]\n"
"s_waitcnt vmcnt(32)\n"
"ds_read_b64_tr_b16 a[204:205], v36 offset:30720\n"
"ds_read_b64_tr_b16 a[206:207], v36 offset:30976\n"
"ds_read_b64_tr_b16 a[236:237], v37 offset:30720\n"
"ds_read_b64_tr_b16 a[238:239], v37 offset:30976\n"
"s_waitcnt lgkmcnt(4)\n"
"v_mfma_f32_16x16x32_bf16 v[208:211], v[188:191], a[140:143], v[208:211]\n"
"v_mfma_f32_16x16x32_bf16 v[212:215], v[188:191], a[172:175], v[212:215]\n"
"s_waitcnt vmcnt(30)\n"
"ds_read_b64_tr_b16 a[16:17], v36 offset:32768\n"
"ds_read_b64_tr_b16 a[18:19], v36 offset:33024\n"
"ds_read_b64_tr_b16 a[48:49], v37 offset:32768\n"
"ds_read_b64_tr_b16 a[50:51], v37 offset:33024\n"
"s_waitcnt lgkmcnt(4)\n"
"v_mfma_f32_16x16x32_bf16 v[216:219], v[188:191], a[204:207], v[216:219]\n"
"v_mfma_f32_16x16x32_bf16 v[220:223], v[188:191], a[236:239], v[220:223]\n"
"s_waitcnt vmcnt(28)\n"
"ds_read_b64_tr_b16 a[80:81], v36 offset:34816\n"
"ds_read_b64_tr_b16 a[82:83], v36 offset:35072\n"
"ds_read_b64_tr_b16 a[112:113], v37 offset:34816\n"
"ds_read_b64_tr_b16 a[114:115], v37 offset:35072\n"
"s_waitcnt lgkmcnt(4)\n"
"v_mfma_f32_16x16x32_bf16 v[192:195], v[188:191], a[16:19], v[192:195]\n"
"v_mfma_f32_16x16x32_bf16 v[196:199], v[188:191], a[48:51], v[196:199]\n"
"s_waitcnt vmcnt(26)\n"
"ds_read_b64_tr_b16 a[144:145], v36 offset:36864\n"
"ds_read_b64_tr_b16 a[146:147], v36 offset:37120\n"
"ds_read_b64_tr_b16 a[176:177], v37 offset:36864\n"
"ds_read_b64_tr_b16 a[178:179], v37 offset:37120\n"
"s_waitcnt lgkmcnt(4)\n"
"v_mfma_f32_16x16x32_bf16 v[200:203], v[188:191], a[80:83], v[200:203]\n"
"v_mfma_f32_16x16x32_bf16 v[204:207], v[188:191], a[112:115], v[204:207]\n"
"s_waitcnt vmcnt(24)\n"
"ds_read_b64_tr_b16 a[208:209], v36 offset:38912\n"
"ds_read_b64_tr_b16 a[210:211], v36 offset:39168\n"
"ds_read_b64_tr_b16 a[240:241], v37 offset:38912\n"
"ds_read_b64_tr_b16 a[242:243], v37 offset:39168\n"
"s_waitcnt lgkmcnt(4)\n"
"v_mfma_f32_16x16x32_bf16 v[208:211], v[188:191], a[144:147], v[208:211]\n"
"v_mfma_f32_16x16x32_bf16 v[212:215], v[188:191], a[176:179], v[212:215]\n"
"s_waitcnt vmcnt(22)\n"
"ds_read_b64_tr_b16 a[20:21], v36 offset:0\n"
"ds_read_b64_tr_b16 a[22:23], v36 offset:256\n"
"ds_read_b64_tr_b16 a[52:53], v37 offset:0\n"
"ds_read_b64_tr_b16 a[54:55], v37 offset:256\n"
"s_waitcnt lgkmcnt(4)\n"
"v_mfma_f32_16x16x32_bf16 v[216:219], v[188:191], a[208:211], v[216:219]\n"
"v_mfma_f32_16x16x32_bf16 v[220:223], v[188:191], a[240:243], v[220:223]\n"
"s_waitcnt vmcnt(20)\n"
"ds_read_b64_tr_b16 a[84:85], v36 offset:2048\n"
"ds_read_b64_tr_b16 a[86:87], v36 offset:2304\n"
"ds_read_b64_tr_b16 a[116:117], v37 offset:2048\n"
"ds_read_b64_tr_b16 a[118:119], v37 offset:2304\n"
"s_waitcnt lgkmcnt(4)\n"
"v_mfma_f32_16x16x32_bf16 v[192:195], v[188:191], a[20:23], v[192:195]\n"
"v_mfma_f32_16x16x32_bf16 v[196:199], v[188:191], a[52:55], v[196:199]\n"
"s_waitcnt vmcnt(18)\n"
"ds_read_b64_tr_b16 a[148:149], v36 offset:4096\n"
"ds_read_b64_tr_b16 a[150:151], v36 offset:4352\n"
"ds_read_b64_tr_b16 a[180:181], v37 offset:4096\n"
"ds_read_b64_tr_b16 a[182:183], v37 offset:4352\n"
"s_waitcnt lgkmcnt(4)\n"
"v_mfma_f32_16x16x32_bf16 v[200:203], v[188:191], a[84:87], v[200:203]\n"
"v_mfma_f32_16x16x32_bf16 v[204:207], v[188:191], a[116:119], v[204:207]\n"
"s_waitcnt vmcnt(16)\n"
"ds_read_b64_tr_b16 a[212:213], v36 offset:6144\n"
"ds_read_b64_tr_b16 a[214:215], v36 offset:6400\n"
"ds_read_b64_tr_b16 a[244:245], v37 offset:6144\n"
"ds_read_b64_tr_b16 a[246:247], v37 offset:6400\n"
"s_waitcnt lgkmcnt(4)\n"
"v_mfma_f32_16x16x32_bf16 v[208:211], v[188:191], a[148:151], v[208:211]\n"
"v_mfma_f32_16x16x32_bf16 v[212:215], v[188:191], a[180:183], v[212:215]\n"
"s_waitcnt vmcnt(14)\n"
"ds_read_b64_tr_b16 a[24:25], v36 offset:8192\n"
"ds_read_b64_tr_b16 a[26:27], v36 offset:8448\n"
"ds_read_b64_tr_b16 a[56:57], v37 offset:8192\n"
"ds_read_b64_tr_b16 a[58:59], v37 offset:8448\n"
"s_waitcnt lgkmcnt(4)\n"
"v_mfma_f32_16x16x32_bf16 v[216:219], v[188:191], a[212:215], v[216:219]\n"
"v_mfma_f32_16x16x32_bf16 v[220:223], v[188:191], a[244:247], v[220:223]\n"
"s_waitcnt vmcnt(12)\n"
"ds_read_b64_tr_b16 a[88:89], v36 offset:10240\n"
"ds_read_b64_tr_b16 a[90:91], v36 offset:10496\n"
"ds_read_b64_tr_b16 a[120:121], v37 offset:10240\n"
"ds_read_b64_tr_b16 a[122:123], v37 offset:10496\n"
"s_waitcnt lgkmcnt(4)\n"
"v_mfma_f32_16x16x32_bf16 v[192:195], v[188:191], a[24:27], v[192:195]\n"
"v_mfma_f32_16x16x32_bf16 v[196:199], v[188:191], a[56:59], v[196:199]\n"
"s_waitcnt vmcnt(10)\n"
"ds_read_b64_tr_b16 a[152:153], v36 offset:12288\n"
"ds_read_b64_tr_b16 a[154:155], v36 offset:12544\n"
"ds_read_b64_tr_b16 a[184:185], v37 offset:12288\n"
"ds_read_b64_tr_b16 a[186:187], v37 offset:12544\n"
"s_waitcnt lgkmcnt(4)\n"
"v_mfma_f32_16x16x32_bf16 v[200:203], v[188:191], a[88:91], v[200:203]\n"
"v_mfma_f32_16x16x32_bf16 v[204:207], v[188:191], a[120:123], v[204:207]\n"
"s_waitcnt vmcnt(8)\n"
"ds_read_b64_tr_b16 a[216:217], v36 offset:14336\n"
"ds_read_b64_tr_b16 a[218:219], v36 offset:14592\n"
"ds_read_b64_tr_b16 a[248:249], v37 offset:14336\n"
"ds_read_b64_tr_b16 a[250:251], v37 offset:14592\n"
"s_waitcnt lgkmcnt(4)\n"
"v_mfma_f32_16x16x32_bf16 v[208:211], v[188:191], a[152:155], v[208:211]\n"
"v_mfma_f32_16x16x32_bf16 v[212:215], v[188:191], a[184:187], v[212:215]\n"
"s_waitcnt vmcnt(6)\n"
"ds_read_b64_tr_b16 a[28:29], v36 offset:16384\n"
"ds_read_b64_tr_b16 a[30:31], v36 offset:16640\n"
"ds_read_b64_tr_b16 a[60:61], v37 offset:16384\n"
"ds_read_b64_tr_b16 a[62:63], v37 offset:16640\n"
"s_waitcnt lgkmcnt(4)\n"
"v_mfma_f32_16x16x32_bf16 v[216:219], v[188:191], a[216:219], v[216:219]\n"
"v_mfma_f32_16x16x32_bf16 v[220:223], v[188:191], a[248:251], v[220:223]\n"
"s_waitcnt vmcnt(4)\n"
"ds_read_b64_tr_b16 a[92:93], v36 offset:18432\n"
"ds_read_b64_tr_b16 a[94:95], v36 offset:18688\n"
"ds_read_b64_tr_b16 a[124:125], v37 offset:18432\n"
"ds_read_b64_tr_b16 a[126:127], v37 offset:18688\n"
"s_waitcnt lgkmcnt(4)\n"
"v_mfma_f32_16x16x32_bf16 v[192:195], v[188:191], a[28:31], v[192:195]\n"
"v_mfma_f32_16x16x32_bf16 v[196:199], v[188:191], a[60:63], v[196:199]\n"
"s_waitcnt vmcnt(2)\n"
"ds_read_b64_tr_b16 a[156:157], v36 offset:20480\n"
"ds_read_b64_tr_b16 a[158:159], v36 offset:20736\n"
"ds_read_b64_tr_b16 a[188:189], v37 offset:20480\n"
"ds_read_b64_tr_b16 a[190:191], v37 offset:20736\n"
"s_waitcnt lgkmcnt(4)\n"
"v_mfma_f32_16x16x32_bf16 v[200:203], v[188:191], a[92:95], v[200:203]\n"
"v_mfma_f32_16x16x32_bf16 v[204:207], v[188:191], a[124:127], v[204:207]\n"
"s_waitcnt vmcnt(0)\n"
"ds_read_b64_tr_b16 a[220:221], v36 offset:22528\n"
"ds_read_b64_tr_b16 a[222:223], v36 offset:22784\n"
"ds_read_b64_tr_b16 a[252:253], v37 offset:22528\n"
"ds_read_b64_tr_b16 a[254:255], v37 offset:22784\n"
"s_waitcnt lgkmcnt(4)\n"
"v_mfma_f32_16x16x32_bf16 v[208:211], v[188:191], a[156:159], v[208:211]\n"
"v_mfma_f32_16x16x32_bf16 v[212:215], v[188:191], a[188:191], v[212:215]\n"
"s_waitcnt lgkmcnt(0)\n"
"v_mfma_f32_16x16x32_bf16 v[216:219], v[188:191], a[220:223], v[216:219]\n"
"v_mfma_f32_16x16x32_bf16 v[220:223], v[188:191], a[252:255], v[220:223]\n"
"s_mov_b32 m0, s59\n"
"s_barrier\n"
"ds_write_addtid_b32 v192 offset:0\n"
"ds_write_addtid_b32 v193 offset:4640\n"
"ds_write_addtid_b32 v194 offset:9344\n"
"ds_write_addtid_b32 v195 offset:13984\n"
"ds_write_addtid_b32 v196 offset:256\n"
"ds_write_addtid_b32 v197 offset:4896\n"
"ds_write_addtid_b32 v198 offset:9600\n"
"ds_write_addtid_b32 v199 offset:14240\n"
"ds_write_addtid_b32 v200 offset:512\n"
"ds_write_addtid_b32 v201 offset:5152\n"
"ds_write_addtid_b32 v202 offset:9856\n"
"ds_write_addtid_b32 v203 offset:14496\n"
"ds_write_addtid_b32 v204 offset:768\n"
"ds_write_addtid_b32 v205 offset:5408\n"
"ds_write_addtid_b32 v206 offset:10112\n"
"ds_write_addtid_b32 v207 offset:14752\n"
"ds_write_addtid_b32 v208 offset:1024\n"
"ds_write_addtid_b32 v209 offset:5664\n"
"ds_write_addtid_b32 v210 offset:10368\n"
"ds_write_addtid_b32 v211 offset:15008\n"
"ds_write_addtid_b32 v212 offset:1280\n"
"ds_write_addtid_b32 v213 offset:5920\n"
"ds_write_addtid_b32 v214 offset:10624\n"
"ds_write_addtid_b32 v215 offset:15264\n"
"ds_write_addtid_b32 v216 offset:1536\n"
"ds_write_addtid_b32 v217 offset:6176\n"
"ds_write_addtid_b32 v218 offset:10880\n"
"ds_write_addtid_b32 v219 offset:15520\n"
"ds_write_addtid_b32 v220 offset:1792\n"
"ds_write_addtid_b32 v221 offset:6432\n"
"ds_write_addtid_b32 v222 offset:11136\n"
"ds_write_addtid_b32 v223 offset:15776\n"
"s_waitcnt lgkmcnt(0)\n"
"s_barrier\n"
"ds_read_b128 v[96:99], v2 offset:0\n"
"ds_read_b128 v[100:103], v3 offset:512\n"
"ds_read_b128 v[104:107], v4 offset:1024\n"
"ds_read_b128 v[108:111], v43 offset:1536\n"
"ds_read_b128 v[112:115], v2 offset:256\n"
"ds_read_b128 v[116:119], v3 offset:768\n"
"ds_read_b128 v[120:123], v4 offset:1280\n"
"ds_read_b128 v[124:127], v43 offset:1792\n"
"s_waitcnt lgkmcnt(0)\n"
"s_barrier\n"
"v_add_f32 v96, v96, v100\n"
"v_add_f32 v104, v104, v108\n"
"v_add_f32 v18, v96, v104\n"
"v_add_f32 v97, v97, v101\n"
"v_add_f32 v105, v105, v109\n"
"v_add_f32 v19, v97, v105\n"
"v_add_f32 v98, v98, v102\n"
"v_add_f32 v106, v106, v110\n"
"v_add_f32 v20, v98, v106\n"
"v_add_f32 v99, v99, v103\n"
"v_add_f32 v107, v107, v111\n"
"v_add_f32 v21, v99, v107\n"
"v_add_f32 v112, v112, v116\n"
"v_add_f32 v120, v120, v124\n"
"v_add_f32 v22, v112, v120\n"
"v_add_f32 v113, v113, v117\n"
"v_add_f32 v121, v121, v125\n"
"v_add_f32 v23, v113, v121\n"
"v_add_f32 v114, v114, v118\n"
"v_add_f32 v122, v122, v126\n"
"v_add_f32 v24, v114, v122\n"
"v_add_f32 v115, v115, v119\n"
"v_add_f32 v123, v123, v127\n"
"v_add_f32 v25, v115, v123\n"
"v_lshlrev_b32 v26, 16, v30\n"
"v_and_b32 v27, 0xffff0000, v30\n"
"v_lshlrev_b32 v28, 16, v31\n"
"v_and_b32 v29, 0xffff0000, v31\n"
"v_lshlrev_b32 v30, 16, v32\n"
"v_and_b32 v31, 0xffff0000, v32\n"
"v_lshlrev_b32 v32, 16, v33\n"
"v_and_b32 v33, 0xffff0000, v33\n"
"v_mov_b32 v134, 0\n"
"v_mov_b32 v135, 0\n"
"v_mov_b32 v154, 0\n"
"v_mov_b32 v155, 0\n"
"v_mov_b32 v144, 0\n"
"v_mov_b32 v145, 0\n"
"v_mov_b32 v138, 0\n"
"v_mov_b32 v139, 0\n"
"s_mov_b32 s94, 0x80000000\n"
"s_mov_b32 s93, 0x80000000\n"
"s_mov_b32 s93, 0x80000000\n"
"s_mov_b32 s95, 0x80000000\n"
"s_mov_b32 s92, 0x80000000\n"
"s_mov_b32 s95, 0x80000000\n"
"s_mov_b32 s95, 0x80000000\n"
"s_mov_b32 s94, 0x80000000\n"
"s_cmp_eq_u32 s54, 0\n"
"s_cbranch_scc1 .LF_1s\n"
"s_cmp_eq_u32 s54, 1\n"
"s_cbranch_scc1 .LF_2s\n"
"s_cmp_eq_u32 s54, 2\n"
"s_cbranch_scc1 .LF_3s\n"
"s_cmp_eq_u32 s54, 3\n"
"s_cbranch_scc1 .LF_4s\n"
".p2align 6\n"
".LF_top:\n"
"v_mfma_f32_16x16x32_bf16 v[48:51], v[100:103], a[0:3], 0\n"
"global_load_dwordx4 v[36:39], v6, s[56:57] offset:1024 sc1\n"
"v_mul_f32 v125, v125, v124\n"
"v_mfma_f32_16x16x32_bf16 v[48:51], v[104:107], a[4:7], v[48:51]\n"
"s_waitcnt vmcnt(10)\n"
"ds_write_addtid_b32 v124 offset:18592\n"
"ds_write_addtid_b32 v125 offset:18848\n"
"v_mfma_f32_16x16x32_bf16 v[48:51], v[108:111], a[8:11], v[48:51]\n"
"v_cvt_pk_bf16_f32 v139, v142, v143\n"
"v_pk_max_i16 v138, v138, 0\n"
"v_mfma_f32_16x16x32_bf16 v[48:51], v[64:67], a[12:15], v[48:51]\n"
"s_waitcnt vmcnt(8)\n"
"ds_read2st64_b32 v[140:141], v14 offset0:0 offset1:1\n"
"v_pk_max_i16 v139, v139, 0\n"
"v_mfma_f32_16x16x32_bf16 v[48:51], v[68:71], a[16:19], v[48:51]\n"
"ds_read_b128 v[56:59], v2 offset:0\n"
"ds_read_b128 v[52:55], v3 offset:2048\n"
"v_mfma_f32_16x16x32_bf16 v[48:51], v[72:75], a[20:23], v[48:51]\n"
"s_waitcnt vmcnt(6)\n"
"global_load_dwordx4 v[40:43], v7, s[56:57] offset:0 sc1\n"
"ds_read_b128 v[60:63], v4 offset:1024\n"
"v_mfma_f32_16x16x32_bf16 v[48:51], v[76:79], a[24:27], v[48:51]\n"
"ds_read_b128 v[84:87], v2 offset:256\n"
"ds_read_b128 v[88:91], v3 offset:2304\n"
"v_mfma_f32_16x16x32_bf16 v[48:51], v[80:83], a[28:31], v[48:51]\n"
"ds_read_b128 v[92:95], v4 offset:1280\n"
"v_mfma_f32_16x16x32_bf16 v[96:99], v[100:103], a[32:35], 0\n"
"v_mfma_f32_16x16x32_bf16 v[96:99], v[104:107], a[36:39], v[96:99]\n"
"ds_write_addtid_b32 v48 offset:0\n"
"v_mfma_f32_16x16x32_bf16 v[96:99], v[108:111], a[40:43], v[96:99]\n"
"ds_write_addtid_b32 v49 offset:4640\n"
"global_load_dwordx4 v[44:47], v7, s[56:57] offset:1024 sc1\n"
"v_mfma_f32_16x16x32_bf16 v[96:99], v[64:67], a[44:47], v[96:99]\n"
"s_waitcnt lgkmcnt(5)\n"
"buffer_store_dwordx4 v[136:139], v15, s[40:43], s95 offen sc1\n"
"ds_write_addtid_b32 v50 offset:9344\n"
"v_add_f32 v56, v56, v52\n"
"v_add_f32 v57, v57, v53\n"
"v_mfma_f32_16x16x32_bf16 v[96:99], v[68:71], a[48:51], v[96:99]\n"
"ds_write_addtid_b32 v51 offset:13984\n"
"v_add_f32 v58, v58, v54\n"
"v_mfma_f32_16x16x32_bf16 v[96:99], v[72:75], a[52:55], v[96:99]\n"
"v_add_f32 v59, v59, v55\n"
"v_add_f32 v56, v56, v60\n"
"v_mfma_f32_16x16x32_bf16 v[96:99], v[76:79], a[56:59], v[96:99]\n"
"v_add_f32 v57, v57, v61\n"
"v_add_f32 v58, v58, v62\n"
"v_mfma_f32_16x16x32_bf16 v[96:99], v[80:83], a[60:63], v[96:99]\n"
"v_add_f32 v59, v59, v63\n"
"v_add_f32 v56, v56, v128\n"
"v_mfma_f32_16x16x32_bf16 v[124:127], v[100:103], a[64:67], 0\n"
"global_load_dwordx4 v[48:51], v8, s[56:57] offset:0 sc1\n"
"v_add_f32 v57, v57, v129\n"
"v_mfma_f32_16x16x32_bf16 v[124:127], v[104:107], a[68:71], v[124:127]\n"
"ds_write_addtid_b32 v96 offset:256\n"
"v_add_f32 v58, v58, v130\n"
"v_mfma_f32_16x16x32_bf16 v[124:127], v[108:111], a[72:75], v[124:127]\n"
"ds_write_addtid_b32 v97 offset:4896\n"
"v_add_f32 v59, v59, v131\n"
"v_mfma_f32_16x16x32_bf16 v[124:127], v[64:67], a[76:79], v[124:127]\n"
"s_add_u32 s95, s50, 0xffd00000\n"
"s_and_b32 s94, s95, 0xfbffffff\n"
"ds_write_addtid_b32 v98 offset:9600\n"
"v_mfma_f32_16x16x32_bf16 v[124:127], v[68:71], a[80:83], v[124:127]\n"
"ds_write_addtid_b32 v99 offset:14240\n"
"global_load_dwordx4 v[52:55], v8, s[56:57] offset:1024 sc1\n"
"v_mfma_f32_16x16x32_bf16 v[124:127], v[72:75], a[84:87], v[124:127]\n"
"v_fma_f32 v60, v18, v141, v26\n"
"v_fma_f32 v61, v19, v141, v27\n"
"v_mfma_f32_16x16x32_bf16 v[124:127], v[76:79], a[88:91], v[124:127]\n"
"v_fma_f32 v62, v20, v141, v28\n"
"v_fma_f32 v63, v21, v141, v29\n"
"v_mfma_f32_16x16x32_bf16 v[124:127], v[80:83], a[92:95], v[124:127]\n"
"v_fma_f32 v56, v56, v140, v60\n"
"v_fma_f32 v57, v57, v140, v61\n"
"v_mfma_f32_16x16x32_bf16 v[96:99], v[100:103], a[96:99], 0\n"
"v_fma_f32 v58, v58, v140, v62\n"
"v_fma_f32 v59, v59, v140, v63\n"
"v_mfma_f32_16x16x32_bf16 v[96:99], v[104:107], a[100:103], v[96:99]\n"
"ds_write_addtid_b32 v124 offset:512\n"
"v_mul_f32_e64 v56, v56, |v56|\n"
"v_mfma_f32_16x16x32_bf16 v[96:99], v[108:111], a[104:107], v[96:99]\n"
"ds_write_addtid_b32 v125 offset:5152\n"
"v_mul_f32_e64 v57, v57, |v57|\n"
"v_mfma_f32_16x16x32_bf16 v[96:99], v[64:67], a[108:111], v[96:99]\n"
"ds_write_addtid_b32 v126 offset:9856\n"
"v_mul_f32_e64 v58, v58, |v58|\n"
"v_mfma_f32_16x16x32_bf16 v[96:99], v[68:71], a[112:115], v[96:99]\n"
"ds_write_addtid_b32 v127 offset:14496\n"
"v_mul_f32_e64 v59, v59, |v59|\n"
"v_mfma_f32_16x16x32_bf16 v[96:99], v[72:75], a[116:119], v[96:99]\n"
"v_cvt_pk_bf16_f32 v128, v56, v57\n"
"v_cvt_pk_bf16_f32 v129, v58, v59\n"
"v_mfma_f32_16x16x32_bf16 v[96:99], v[76:79], a[120:123], v[96:99]\n"
"v_pk_max_i16 v128, v128, 0\n"
"v_pk_max_i16 v129, v129, 0\n"
"v_mfma_f32_16x16x32_bf16 v[96:99], v[80:83], a[124:127], v[96:99]\n"
"s_waitcnt lgkmcnt(12)\n"
"v_add_f32 v84, v84, v88\n"
"v_add_f32 v85, v85, v89\n"
"v_mfma_f32_16x16x32_bf16 v[56:59], v[100:103], a[128:131], 0\n"
"v_add_f32 v86, v86, v90\n"
"v_add_f32 v87, v87, v91\n"
"v_mfma_f32_16x16x32_bf16 v[56:59], v[104:107], a[132:135], v[56:59]\n"
"ds_write_addtid_b32 v96 offset:768\n"
"v_add_f32 v84, v84, v92\n"
"v_mfma_f32_16x16x32_bf16 v[56:59], v[108:111], a[136:139], v[56:59]\n"
"ds_write_addtid_b32 v97 offset:5408\n"
"v_add_f32 v85, v85, v93\n"
"v_mfma_f32_16x16x32_bf16 v[56:59], v[64:67], a[140:143], v[56:59]\n"
"ds_write_addtid_b32 v98 offset:10112\n"
"v_add_f32 v86, v86, v94\n"
"v_mfma_f32_16x16x32_bf16 v[56:59], v[68:71], a[144:147], v[56:59]\n"
"ds_write_addtid_b32 v99 offset:14752\n"
"v_add_f32 v87, v87, v95\n"
"v_mfma_f32_16x16x32_bf16 v[56:59], v[72:75], a[148:151], v[56:59]\n"
"v_add_f32 v84, v84, v132\n"
"v_add_f32 v85, v85, v133\n"
"v_mfma_f32_16x16x32_bf16 v[56:59], v[76:79], a[152:155], v[56:59]\n"
"v_add_f32 v86, v86, v134\n"
"v_add_f32 v87, v87, v135\n"
"v_mfma_f32_16x16x32_bf16 v[56:59], v[80:83], a[156:159], v[56:59]\n"
"v_fma_f32 v60, v22, v141, v30\n"
"v_fma_f32 v61, v23, v141, v31\n"
"v_mfma_f32_16x16x32_bf16 v[88:91], v[100:103], a[160:163], 0\n"
"v_fma_f32 v62, v24, v141, v32\n"
"v_fma_f32 v63, v25, v141, v33\n"
"v_mfma_f32_16x16x32_bf16 v[88:91], v[104:107], a[164:167], v[88:91]\n"
"ds_write_addtid_b32 v56 offset:1024\n"
"v_fma_f32 v84, v84, v140, v60\n"
"v_mfma_f32_16x16x32_bf16 v[88:91], v[108:111], a[168:171], v[88:91]\n"
"s_add_u32 s58, s50, 0x180000\n"
"ds_write_addtid_b32 v57 offset:5664\n"
"v_fma_f32 v85, v85, v140, v61\n"
"v_mfma_f32_16x16x32_bf16 v[88:91], v[64:67], a[172:175], v[88:91]\n"
"s_and_b32 s58, s58, 0x3ffffff\n"
"ds_write_addtid_b32 v58 offset:10368\n"
"v_fma_f32 v86, v86, v140, v62\n"
"v_mfma_f32_16x16x32_bf16 v[88:91], v[68:71], a[176:179], v[88:91]\n"
"s_add_u32 s56, s40, s58\n"
"ds_write_addtid_b32 v59 offset:15008\n"
"v_fma_f32 v87, v87, v140, v63\n"
"v_mfma_f32_16x16x32_bf16 v[88:91], v[72:75], a[180:183], v[88:91]\n"
"s_addc_u32 s57, s41, 0\n"
"v_mul_f32_e64 v84, v84, |v84|\n"
"v_mul_f32_e64 v85, v85, |v85|\n"
"v_mfma_f32_16x16x32_bf16 v[88:91], v[76:79], a[184:187], v[88:91]\n"
"s_waitcnt vmcnt(6)\n"
"v_cmp_ne_u64 vcc, s[48:49], v[144:145]\n"
"v_mul_f32_e64 v86, v86, |v86|\n"
"v_mfma_f32_16x16x32_bf16 v[88:91], v[80:83], a[188:191], v[88:91]\n"
"v_mul_f32_e64 v87, v87, |v87|\n"
"v_cvt_pk_bf16_f32 v130, v84, v85\n"
"v_mfma_f32_16x16x32_bf16 v[92:95], a[192:195], v[100:103], 0\n"
"s_cbranch_vccnz .LF_3s\n"
".LF_3r:\n"
"v_cvt_pk_bf16_f32 v131, v86, v87\n"
"v_mfma_f32_16x16x32_bf16 v[92:95], a[196:199], v[104:107], v[92:95]\n"
"s_add_u32 s50, s50, 0x180000\n"
"ds_write_addtid_b32 v88 offset:1280\n"
"global_load_dwordx4 v[56:59], v5, s[56:57] offset:0 sc1\n"
"v_mfma_f32_16x16x32_bf16 v[92:95], a[200:203], v[108:111], v[92:95]\n"
"ds_write_addtid_b32 v89 offset:5920\n"
"v_pk_max_i16 v130, v130, 0\n"
"v_mfma_f32_16x16x32_bf16 v[92:95], a[204:207], v[64:67], v[92:95]\n"
"ds_write_addtid_b32 v90 offset:10624\n"
"v_pk_max_i16 v131, v131, 0\n"
"v_mfma_f32_16x16x32_bf16 v[92:95], a[208:211], v[68:71], v[92:95]\n"
"ds_write_addtid_b32 v91 offset:15264\n"
"v_mfma_f32_16x16x32_bf16 v[92:95], a[212:215], v[72:75], v[92:95]\n"
"v_mfma_f32_16x16x32_bf16 v[92:95], a[216:219], v[76:79], v[92:95]\n"
"v_mfma_f32_16x16x32_bf16 v[92:95], a[220:223], v[80:83], v[92:95]\n"
"v_mfma_f32_16x16x32_bf16 v[88:91], a[224:227], v[100:103], 0\n"
"s_waitcnt lgkmcnt(0)\n"
"global_load_dwordx4 v[60:63], v5, s[56:57] offset:1024 sc1\n"
"v_mfma_f32_16x16x32_bf16 v[88:91], a[228:231], v[104:107], v[88:91]\n"
"s_barrier\n"
"v_mfma_f32_16x16x32_bf16 v[88:91], a[232:235], v[108:111], v[88:91]\n"
"ds_read2st64_b32 v[136:137], v11 offset0:0 offset1:1\n"
"ds_read_b128 v[96:99], v2 offset:1536\n"
"v_mfma_f32_16x16x32_bf16 v[88:91], a[236:239], v[64:67], v[88:91]\n"
"s_waitcnt vmcnt(4)\n"
"buffer_load_dwordx2 v[138:139], v9, s[40:43], s50 offen sc1\n"
"v_mfma_f32_16x16x32_bf16 v[88:91], a[240:243], v[68:71], v[88:91]\n"
"global_load_dwordx4 v[64:67], v6, s[56:57] offset:0 sc1\n"
"ds_read_b128 v[84:87], v3 offset:512\n"
"v_mfma_f32_16x16x32_bf16 v[88:91], a[244:247], v[72:75], v[88:91]\n"
"ds_read_b128 v[100:103], v4 offset:2560\n"
"ds_read_b128 v[104:107], v2 offset:1792\n"
"v_mfma_f32_16x16x32_bf16 v[88:91], a[248:251], v[76:79], v[88:91]\n"
"ds_read_b128 v[108:111], v3 offset:768\n"
"ds_read_b128 v[124:127], v4 offset:2816\n"
"v_mfma_f32_16x16x32_bf16 v[88:91], a[252:255], v[80:83], v[88:91]\n"
"buffer_store_dwordx2 v[0:1], v16, s[40:43], s92 offen offset:0 sc1\n"
"v_mfma_f32_16x16x32_bf16 v[132:135], v[112:115], a[0:3], 0\n"
"buffer_store_dwordx4 v[128:131], v15, s[40:43], s94 offen sc1\n"
"v_mfma_f32_16x16x32_bf16 v[132:135], v[116:119], a[4:7], v[132:135]\n"
"v_mfma_f32_16x16x32_bf16 v[132:135], v[120:123], a[8:11], v[132:135]\n"
"global_load_dwordx4 v[68:71], v6, s[56:57] offset:1024 sc1\n"
"v_mfma_f32_16x16x32_bf16 v[132:135], v[36:39], a[12:15], v[132:135]\n"
"v_mfma_f32_16x16x32_bf16 v[132:135], v[40:43], a[16:19], v[132:135]\n"
"s_waitcnt lgkmcnt(3)\n"
"v_add_f32 v96, v96, v84\n"
"v_add_f32 v97, v97, v85\n"
"v_mfma_f32_16x16x32_bf16 v[132:135], v[44:47], a[20:23], v[132:135]\n"
"s_waitcnt vmcnt(7)\n"
"global_load_dwordx4 v[72:75], v7, s[56:57] offset:0 sc1\n"
"v_add_f32 v98, v98, v86\n"
"v_mfma_f32_16x16x32_bf16 v[132:135], v[48:51], a[24:27], v[132:135]\n"
"v_add_f32 v99, v99, v87\n"
"v_add_f32 v96, v96, v100\n"
"v_mfma_f32_16x16x32_bf16 v[132:135], v[52:55], a[28:31], v[132:135]\n"
"global_load_dwordx4 v[76:79], v7, s[56:57] offset:1024 sc1\n"
"v_add_f32 v97, v97, v101\n"
"v_mfma_f32_16x16x32_bf16 v[128:131], v[112:115], a[32:35], 0\n"
"v_add_f32 v98, v98, v102\n"
"v_add_f32 v99, v99, v103\n"
"v_mfma_f32_16x16x32_bf16 v[128:131], v[116:119], a[36:39], v[128:131]\n"
"ds_write_addtid_b32 v132 offset:1536\n"
"v_add_f32 v96, v96, v92\n"
"v_mfma_f32_16x16x32_bf16 v[128:131], v[120:123], a[40:43], v[128:131]\n"
"ds_write_addtid_b32 v133 offset:6176\n"
"v_add_f32 v97, v97, v93\n"
"v_mfma_f32_16x16x32_bf16 v[128:131], v[36:39], a[44:47], v[128:131]\n"
"ds_write_addtid_b32 v134 offset:10880\n"
"v_add_f32 v98, v98, v94\n"
"v_mfma_f32_16x16x32_bf16 v[128:131], v[40:43], a[48:51], v[128:131]\n"
"ds_write_addtid_b32 v135 offset:15520\n"
"v_add_f32 v99, v99, v95\n"
"v_mfma_f32_16x16x32_bf16 v[128:131], v[44:47], a[52:55], v[128:131]\n"
"s_add_u32 s94, s50, 0xffd00000\n"
"s_and_b32 s93, s94, 0xfbffffff\n"
"v_fma_f32 v84, v18, v137, v26\n"
"v_mfma_f32_16x16x32_bf16 v[128:131], v[48:51], a[56:59], v[128:131]\n"
"v_fma_f32 v85, v19, v137, v27\n"
"v_fma_f32 v86, v20, v137, v28\n"
"v_mfma_f32_16x16x32_bf16 v[128:131], v[52:55], a[60:63], v[128:131]\n"
"v_fma_f32 v87, v21, v137, v29\n"
"v_fma_f32 v96, v96, v136, v84\n"
"v_mfma_f32_16x16x32_bf16 v[92:95], v[112:115], a[64:67], 0\n"
"v_fma_f32 v97, v97, v136, v85\n"
"v_fma_f32 v98, v98, v136, v86\n"
"v_mfma_f32_16x16x32_bf16 v[92:95], v[116:119], a[68:71], v[92:95]\n"
"ds_write_addtid_b32 v128 offset:1792\n"
"global_load_dwordx4 v[80:83], v8, s[56:57] offset:0 sc1\n"
"v_mfma_f32_16x16x32_bf16 v[92:95], v[120:123], a[72:75], v[92:95]\n"
"ds_write_addtid_b32 v129 offset:6432\n"
"v_fma_f32 v99, v99, v136, v87\n"
"v_mfma_f32_16x16x32_bf16 v[92:95], v[36:39], a[76:79], v[92:95]\n"
"ds_write_addtid_b32 v130 offset:11136\n"
"v_mul_f32_e64 v96, v96, |v96|\n"
"v_mfma_f32_16x16x32_bf16 v[92:95], v[40:43], a[80:83], v[92:95]\n"
"ds_write_addtid_b32 v131 offset:15776\n"
"v_mul_f32_e64 v97, v97, |v97|\n"
"v_mfma_f32_16x16x32_bf16 v[92:95], v[44:47], a[84:87], v[92:95]\n"
"global_load_dwordx4 v[84:87], v8, s[56:57] offset:1024 sc1\n"
"v_mul_f32_e64 v98, v98, |v98|\n"
"v_mfma_f32_16x16x32_bf16 v[92:95], v[48:51], a[88:91], v[92:95]\n"
"v_mul_f32_e64 v99, v99, |v99|\n"
"v_cvt_pk_bf16_f32 v100, v96, v97\n"
"v_mfma_f32_16x16x32_bf16 v[92:95], v[52:55], a[92:95], v[92:95]\n"
"v_cvt_pk_bf16_f32 v101, v98, v99\n"
"v_pk_max_i16 v100, v100, 0\n"
"v_mfma_f32_16x16x32_bf16 v[128:131], v[112:115], a[96:99], 0\n"
"v_pk_max_i16 v101, v101, 0\n"
"v_mfma_f32_16x16x32_bf16 v[128:131], v[116:119], a[100:103], v[128:131]\n"
"s_waitcnt lgkmcnt(8)\n"
"ds_write_addtid_b32 v92 offset:2048\n"
"v_add_f32 v104, v104, v108\n"
"v_mfma_f32_16x16x32_bf16 v[128:131], v[120:123], a[104:107], v[128:131]\n"
"ds_write_addtid_b32 v93 offset:6688\n"
"v_add_f32 v105, v105, v109\n"
"v_mfma_f32_16x16x32_bf16 v[128:131], v[36:39], a[108:111], v[128:131]\n"
"ds_write_addtid_b32 v94 offset:11392\n"
"v_add_f32 v106, v106, v110\n"
"v_mfma_f32_16x16x32_bf16 v[128:131], v[40:43], a[112:115], v[128:131]\n"
"ds_write_addtid_b32 v95 offset:16032\n"
"v_add_f32 v107, v107, v111\n"
"v_mfma_f32_16x16x32_bf16 v[128:131], v[44:47], a[116:119], v[128:131]\n"
"v_add_f32 v104, v104, v124\n"
"v_add_f32 v105, v105, v125\n"
"v_mfma_f32_16x16x32_bf16 v[128:131], v[48:51], a[120:123], v[128:131]\n"
"v_add_f32 v106, v106, v126\n"
"v_add_f32 v107, v107, v127\n"
"v_mfma_f32_16x16x32_bf16 v[128:131], v[52:55], a[124:127], v[128:131]\n"
"v_add_f32 v104, v104, v88\n"
"v_add_f32 v105, v105, v89\n"
"v_mfma_f32_16x16x32_bf16 v[92:95], v[112:115], a[128:131], 0\n"
"v_add_f32 v106, v106, v90\n"
"v_add_f32 v107, v107, v91\n"
"v_mfma_f32_16x16x32_bf16 v[92:95], v[116:119], a[132:135], v[92:95]\n"
"ds_write_addtid_b32 v128 offset:2304\n"
"v_fma_f32 v96, v22, v137, v30\n"
"v_mfma_f32_16x16x32_bf16 v[92:95], v[120:123], a[136:139], v[92:95]\n"
"ds_write_addtid_b32 v129 offset:6944\n"
"v_fma_f32 v97, v23, v137, v31\n"
"v_mfma_f32_16x16x32_bf16 v[92:95], v[36:39], a[140:143], v[92:95]\n"
"ds_write_addtid_b32 v130 offset:11648\n"
"v_fma_f32 v98, v24, v137, v32\n"
"v_mfma_f32_16x16x32_bf16 v[92:95], v[40:43], a[144:147], v[92:95]\n"
"ds_write_addtid_b32 v131 offset:16288\n"
"v_fma_f32 v99, v25, v137, v33\n"
"v_mfma_f32_16x16x32_bf16 v[92:95], v[44:47], a[148:151], v[92:95]\n"
"v_fma_f32 v104, v104, v136, v96\n"
"v_fma_f32 v105, v105, v136, v97\n"
"v_mfma_f32_16x16x32_bf16 v[92:95], v[48:51], a[152:155], v[92:95]\n"
"v_fma_f32 v106, v106, v136, v98\n"
"v_fma_f32 v107, v107, v136, v99\n"
"v_mfma_f32_16x16x32_bf16 v[92:95], v[52:55], a[156:159], v[92:95]\n"
"v_mul_f32_e64 v104, v104, |v104|\n"
"v_mul_f32_e64 v105, v105, |v105|\n"
"v_mfma_f32_16x16x32_bf16 v[96:99], v[112:115], a[160:163], 0\n"
"v_mul_f32_e64 v106, v106, |v106|\n"
"v_mul_f32_e64 v107, v107, |v107|\n"
"v_mfma_f32_16x16x32_bf16 v[96:99], v[116:119], a[164:167], v[96:99]\n"
"ds_write_addtid_b32 v92 offset:2560\n"
"v_cvt_pk_bf16_f32 v102, v104, v105\n"
"v_mfma_f32_16x16x32_bf16 v[96:99], v[120:123], a[168:171], v[96:99]\n"
"s_add_u32 s58, s50, 0x180000\n"
"ds_write_addtid_b32 v93 offset:7200\n"
"v_cvt_pk_bf16_f32 v103, v106, v107\n"
"v_mfma_f32_16x16x32_bf16 v[96:99], v[36:39], a[172:175], v[96:99]\n"
"s_and_b32 s58, s58, 0x3ffffff\n"
"ds_write_addtid_b32 v94 offset:11904\n"
"v_pk_max_i16 v102, v102, 0\n"
"v_mfma_f32_16x16x32_bf16 v[96:99], v[40:43], a[176:179], v[96:99]\n"
"s_add_u32 s56, s40, s58\n"
"ds_write_addtid_b32 v95 offset:16544\n"
"v_pk_max_i16 v103, v103, 0\n"
"v_mfma_f32_16x16x32_bf16 v[96:99], v[44:47], a[180:183], v[96:99]\n"
"s_addc_u32 s57, s41, 0\n"
"buffer_store_dwordx4 v[100:103], v15, s[40:43], s93 offen sc1\n"
"v_mfma_f32_16x16x32_bf16 v[96:99], v[48:51], a[184:187], v[96:99]\n"
"s_waitcnt vmcnt(9)\n"
"v_cmp_ne_u64 vcc, s[48:49], v[138:139]\n"
"v_mfma_f32_16x16x32_bf16 v[96:99], v[52:55], a[188:191], v[96:99]\n"
"v_mfma_f32_16x16x32_bf16 v[100:103], a[192:195], v[112:115], 0\n"
"s_cbranch_vccnz .LF_4s\n"
".LF_4r:\n"
"v_mfma_f32_16x16x32_bf16 v[100:103], a[196:199], v[116:119], v[100:103]\n"
"s_add_u32 s50, s50, 0x180000\n"
"ds_write_addtid_b32 v96 offset:2816\n"
"v_mfma_f32_16x16x32_bf16 v[100:103], a[200:203], v[120:123], v[100:103]\n"
"s_waitcnt vmcnt(6)\n"
"buffer_store_dwordx2 v[0:1], v16, s[40:43], s95 offen offset:0 sc1\n"
"ds_write_addtid_b32 v97 offset:7456\n"
"v_mfma_f32_16x16x32_bf16 v[100:103], a[204:207], v[36:39], v[100:103]\n"
"ds_write_addtid_b32 v98 offset:12160\n"
"global_load_dwordx4 v[88:91], v5, s[56:57] offset:0 sc1\n"
"v_mfma_f32_16x16x32_bf16 v[100:103], a[208:211], v[40:43], v[100:103]\n"
"ds_write_addtid_b32 v99 offset:16800\n"
"v_mfma_f32_16x16x32_bf16 v[100:103], a[212:215], v[44:47], v[100:103]\n"
"v_mfma_f32_16x16x32_bf16 v[100:103], a[216:219], v[48:51], v[100:103]\n"
"v_mfma_f32_16x16x32_bf16 v[100:103], a[220:223], v[52:55], v[100:103]\n"
"v_mfma_f32_16x16x32_bf16 v[104:107], a[224:227], v[112:115], 0\n"
"s_waitcnt lgkmcnt(0)\n"
"global_load_dwordx4 v[92:95], v5, s[56:57] offset:1024 sc1\n"
"v_mfma_f32_16x16x32_bf16 v[104:107], a[228:231], v[116:119], v[104:107]\n"
"s_barrier\n"
"v_mfma_f32_16x16x32_bf16 v[104:107], a[232:235], v[120:123], v[104:107]\n"
"ds_read2st64_b32 v[132:133], v12 offset0:0 offset1:1\n"
"ds_read_b128 v[108:111], v2 offset:0\n"
"v_mfma_f32_16x16x32_bf16 v[104:107], a[236:239], v[36:39], v[104:107]\n"
"ds_read_b128 v[96:99], v3 offset:2048\n"
"ds_read_b128 v[112:115], v4 offset:1024\n"
"v_mfma_f32_16x16x32_bf16 v[104:107], a[240:243], v[40:43], v[104:107]\n"
"buffer_load_dwordx2 v[134:135], v9, s[40:43], s50 offen sc1\n"
"v_mfma_f32_16x16x32_bf16 v[104:107], a[244:247], v[44:47], v[104:107]\n"
"global_load_dwordx4 v[36:39], v6, s[56:57] offset:0 sc1\n"
"ds_read_b128 v[116:119], v2 offset:256\n"
"v_mfma_f32_16x16x32_bf16 v[104:107], a[248:251], v[48:51], v[104:107]\n"
"ds_read_b128 v[120:123], v3 offset:2304\n"
"ds_read_b128 v[124:127], v4 offset:1280\n"
"v_mfma_f32_16x16x32_bf16 v[104:107], a[252:255], v[52:55], v[104:107]\n"
"global_load_dwordx4 v[40:43], v6, s[56:57] offset:1024 sc1\n"
"v_mfma_f32_16x16x32_bf16 v[128:131], v[56:59], a[0:3], 0\n"
"v_mfma_f32_16x16x32_bf16 v[128:131], v[60:63], a[4:7], v[128:131]\n"
"s_waitcnt vmcnt(11)\n"
"v_mfma_f32_16x16x32_bf16 v[128:131], v[64:67], a[8:11], v[128:131]\n"
"s_waitcnt lgkmcnt(3)\n"
"v_add_f32 v108, v108, v96\n"
"v_add_f32 v109, v109, v97\n"
"v_mfma_f32_16x16x32_bf16 v[128:131], v[68:71], a[12:15], v[128:131]\n"
"s_waitcnt vmcnt(9)\n"
"v_add_f32 v110, v110, v98\n"
"v_add_f32 v111, v111, v99\n"
"v_mfma_f32_16x16x32_bf16 v[128:131], v[72:75], a[16:19], v[128:131]\n"
"v_add_f32 v108, v108, v112\n"
"v_add_f32 v109, v109, v113\n"
"v_mfma_f32_16x16x32_bf16 v[128:131], v[76:79], a[20:23], v[128:131]\n"
"s_waitcnt vmcnt(7)\n"
"v_add_f32 v110, v110, v114\n"
"v_add_f32 v111, v111, v115\n"
"v_mfma_f32_16x16x32_bf16 v[128:131], v[80:83], a[24:27], v[128:131]\n"
"v_add_f32 v108, v108, v100\n"
"v_add_f32 v109, v109, v101\n"
"v_mfma_f32_16x16x32_bf16 v[128:131], v[84:87], a[28:31], v[128:131]\n"
"global_load_dwordx4 v[44:47], v7, s[56:57] offset:0 sc1\n"
"v_add_f32 v110, v110, v102\n"
"v_mfma_f32_16x16x32_bf16 v[112:115], v[56:59], a[32:35], 0\n"
"v_add_f32 v111, v111, v103\n"
"v_mfma_f32_16x16x32_bf16 v[112:115], v[60:63], a[36:39], v[112:115]\n"
"ds_write_addtid_b32 v128 offset:0\n"
"global_load_dwordx4 v[48:51], v7, s[56:57] offset:1024 sc1\n"
"v_mfma_f32_16x16x32_bf16 v[112:115], v[64:67], a[40:43], v[112:115]\n"
"s_add_u32 s93, s50, 0xffd00000\n"
"s_and_b32 s95, s93, 0xfbffffff\n"
"ds_write_addtid_b32 v129 offset:4640\n"
"v_mfma_f32_16x16x32_bf16 v[112:115], v[68:71], a[44:47], v[112:115]\n"
"ds_write_addtid_b32 v130 offset:9344\n"
"v_fma_f32 v96, v18, v133, v26\n"
"v_mfma_f32_16x16x32_bf16 v[112:115], v[72:75], a[48:51], v[112:115]\n"
"ds_write_addtid_b32 v131 offset:13984\n"
"v_fma_f32 v97, v19, v133, v27\n"
"v_mfma_f32_16x16x32_bf16 v[112:115], v[76:79], a[52:55], v[112:115]\n"
"v_fma_f32 v98, v20, v133, v28\n"
"v_fma_f32 v99, v21, v133, v29\n"
"v_mfma_f32_16x16x32_bf16 v[112:115], v[80:83], a[56:59], v[112:115]\n"
"v_fma_f32 v108, v108, v132, v96\n"
"v_fma_f32 v109, v109, v132, v97\n"
"v_mfma_f32_16x16x32_bf16 v[112:115], v[84:87], a[60:63], v[112:115]\n"
"v_fma_f32 v110, v110, v132, v98\n"
"v_fma_f32 v111, v111, v132, v99\n"
"v_mfma_f32_16x16x32_bf16 v[100:103], v[56:59], a[64:67], 0\n"
"global_load_dwordx4 v[52:55], v8, s[56:57] offset:0 sc1\n"
"v_mul_f32_e64 v108, v108, |v108|\n"
"v_mfma_f32_16x16x32_bf16 v[100:103], v[60:63], a[68:71], v[100:103]\n"
"ds_write_addtid_b32 v112 offset:256\n"
"v_mul_f32_e64 v109, v109, |v109|\n"
"v_mfma_f32_16x16x32_bf16 v[100:103], v[64:67], a[72:75], v[100:103]\n"
"ds_write_addtid_b32 v113 offset:4896\n"
"v_mul_f32_e64 v110, v110, |v110|\n"
"v_mfma_f32_16x16x32_bf16 v[100:103], v[68:71], a[76:79], v[100:103]\n"
"ds_write_addtid_b32 v114 offset:9600\n"
"v_mul_f32_e64 v111, v111, |v111|\n"
"v_mfma_f32_16x16x32_bf16 v[100:103], v[72:75], a[80:83], v[100:103]\n"
"ds_write_addtid_b32 v115 offset:14240\n"
"global_load_dwordx4 v[96:99], v8, s[56:57] offset:1024 sc1\n"
"v_mfma_f32_16x16x32_bf16 v[100:103], v[76:79], a[84:87], v[100:103]\n"
"v_cvt_pk_bf16_f32 v128, v108, v109\n"
"v_cvt_pk_bf16_f32 v129, v110, v111\n"
"v_mfma_f32_16x16x32_bf16 v[100:103], v[80:83], a[88:91], v[100:103]\n"
"v_pk_max_i16 v128, v128, 0\n"
"v_pk_max_i16 v129, v129, 0\n"
"v_mfma_f32_16x16x32_bf16 v[100:103], v[84:87], a[92:95], v[100:103]\n"
"s_waitcnt lgkmcnt(8)\n"
"v_add_f32 v116, v116, v120\n"
"v_add_f32 v117, v117, v121\n"
"v_mfma_f32_16x16x32_bf16 v[108:111], v[56:59], a[96:99], 0\n"
"v_add_f32 v118, v118, v122\n"
"v_add_f32 v119, v119, v123\n"
"v_mfma_f32_16x16x32_bf16 v[108:111], v[60:63], a[100:103], v[108:111]\n"
"ds_write_addtid_b32 v100 offset:512\n"
"v_add_f32 v116, v116, v124\n"
"v_mfma_f32_16x16x32_bf16 v[108:111], v[64:67], a[104:107], v[108:111]\n"
"ds_write_addtid_b32 v101 offset:5152\n"
"v_add_f32 v117, v117, v125\n"
"v_mfma_f32_16x16x32_bf16 v[108:111], v[68:71], a[108:111], v[108:111]\n"
"ds_write_addtid_b32 v102 offset:9856\n"
"v_add_f32 v118, v118, v126\n"
"v_mfma_f32_16x16x32_bf16 v[108:111], v[72:75], a[112:115], v[108:111]\n"
"ds_write_addtid_b32 v103 offset:14496\n"
"v_add_f32 v119, v119, v127\n"
"v_mfma_f32_16x16x32_bf16 v[108:111], v[76:79], a[116:119], v[108:111]\n"
"s_waitcnt vmcnt(10)\n"
"buffer_store_dwordx2 v[0:1], v16, s[40:43], s94 offen offset:0 sc1\n"
"v_add_f32 v116, v116, v104\n"
"v_mfma_f32_16x16x32_bf16 v[108:111], v[80:83], a[120:123], v[108:111]\n"
"v_add_f32 v117, v117, v105\n"
"v_add_f32 v118, v118, v106\n"
"v_mfma_f32_16x16x32_bf16 v[108:111], v[84:87], a[124:127], v[108:111]\n"
"v_add_f32 v119, v119, v107\n"
"v_fma_f32 v100, v22, v133, v30\n"
"v_mfma_f32_16x16x32_bf16 v[112:115], v[56:59], a[128:131], 0\n"
"v_fma_f32 v101, v23, v133, v31\n"
"v_fma_f32 v102, v24, v133, v32\n"
"v_mfma_f32_16x16x32_bf16 v[112:115], v[60:63], a[132:135], v[112:115]\n"
"ds_write_addtid_b32 v108 offset:768\n"
"v_fma_f32 v103, v25, v133, v33\n"
"v_mfma_f32_16x16x32_bf16 v[112:115], v[64:67], a[136:139], v[112:115]\n"
"ds_write_addtid_b32 v109 offset:5408\n"
"v_fma_f32 v116, v116, v132, v100\n"
"v_mfma_f32_16x16x32_bf16 v[112:115], v[68:71], a[140:143], v[112:115]\n"
"ds_write_addtid_b32 v110 offset:10112\n"
"v_fma_f32 v117, v117, v132, v101\n"
"v_mfma_f32_16x16x32_bf16 v[112:115], v[72:75], a[144:147], v[112:115]\n"
"ds_write_addtid_b32 v111 offset:14752\n"
"v_fma_f32 v118, v118, v132, v102\n"
"v_mfma_f32_16x16x32_bf16 v[112:115], v[76:79], a[148:151], v[112:115]\n"
"v_fma_f32 v119, v119, v132, v103\n"
"v_mul_f32_e64 v116, v116, |v116|\n"
"v_mfma_f32_16x16x32_bf16 v[112:115], v[80:83], a[152:155], v[112:115]\n"
"v_mul_f32_e64 v117, v117, |v117|\n"
"v_mul_f32_e64 v118, v118, |v118|\n"
"v_mfma_f32_16x16x32_bf16 v[112:115], v[84:87], a[156:159], v[112:115]\n"
"v_mul_f32_e64 v119, v119, |v119|\n"
"v_cvt_pk_bf16_f32 v130, v116, v117\n"
"v_mfma_f32_16x16x32_bf16 v[108:111], v[56:59], a[160:163], 0\n"
"v_cvt_pk_bf16_f32 v131, v118, v119\n"
"v_pk_max_i16 v130, v130, 0\n"
"v_mfma_f32_16x16x32_bf16 v[108:111], v[60:63], a[164:167], v[108:111]\n"
"s_add_u32 s58, s50, 0x180000\n"
"ds_write_addtid_b32 v112 offset:1024\n"
"v_pk_max_i16 v131, v131, 0\n"
"v_mfma_f32_16x16x32_bf16 v[108:111], v[64:67], a[168:171], v[108:111]\n"
"s_and_b32 s58, s58, 0x3ffffff\n"
"ds_write_addtid_b32 v113 offset:5664\n"
"v_mfma_f32_16x16x32_bf16 v[108:111], v[68:71], a[172:175], v[108:111]\n"
"s_add_u32 s56, s40, s58\n"
"ds_write_addtid_b32 v114 offset:10368\n"
"v_mfma_f32_16x16x32_bf16 v[108:111], v[72:75], a[176:179], v[108:111]\n"
"s_addc_u32 s57, s41, 0\n"
"ds_write_addtid_b32 v115 offset:15008\n"
"v_mfma_f32_16x16x32_bf16 v[108:111], v[76:79], a[180:183], v[108:111]\n"
"s_waitcnt vmcnt(7)\n"
"buffer_store_dwordx4 v[128:131], v15, s[40:43], s95 offen sc1\n"
"v_cmp_ne_u64 vcc, s[48:49], v[134:135]\n"
"v_mfma_f32_16x16x32_bf16 v[108:111], v[80:83], a[184:187], v[108:111]\n"
"v_mfma_f32_16x16x32_bf16 v[108:111], v[84:87], a[188:191], v[108:111]\n"
"s_cbranch_vccnz .LF_1s\n"
".LF_1r:\n"
"v_mfma_f32_16x16x32_bf16 v[112:115], a[192:195], v[56:59], 0\n"
"s_add_u32 s50, s50, 0x180000\n"
"global_load_dwordx4 v[100:103], v5, s[56:57] offset:0 sc1\n"
"v_mfma_f32_16x16x32_bf16 v[112:115], a[196:199], v[60:63], v[112:115]\n"
"ds_write_addtid_b32 v108 offset:1280\n"
"global_load_dwordx4 v[124:127], v10, s[56:57] offset:0 sc1\n"
"v_mfma_f32_16x16x32_bf16 v[112:115], a[200:203], v[64:67], v[112:115]\n"
"ds_write_addtid_b32 v109 offset:5920\n"
"global_load_dwordx4 v[116:119], v10, s[56:57] offset:1024 sc1\n"
"v_mfma_f32_16x16x32_bf16 v[112:115], a[204:207], v[68:71], v[112:115]\n"
"ds_write_addtid_b32 v110 offset:10624\n"
"global_load_dwordx4 v[120:123], v10, s[56:57] offset:2048 sc1\n"
"v_mfma_f32_16x16x32_bf16 v[112:115], a[208:211], v[72:75], v[112:115]\n"
"ds_write_addtid_b32 v111 offset:15264\n"
"global_load_dwordx4 v[128:131], v10, s[56:57] offset:3072 sc1\n"
"v_mfma_f32_16x16x32_bf16 v[112:115], a[212:215], v[76:79], v[112:115]\n"
"global_load_dwordx4 v[104:107], v5, s[56:57] offset:1024 sc1\n"
"v_mfma_f32_16x16x32_bf16 v[112:115], a[216:219], v[80:83], v[112:115]\n"
"v_mfma_f32_16x16x32_bf16 v[112:115], a[220:223], v[84:87], v[112:115]\n"
"v_mfma_f32_16x16x32_bf16 v[132:135], a[224:227], v[56:59], 0\n"
"s_waitcnt lgkmcnt(0)\n"
"v_mfma_f32_16x16x32_bf16 v[132:135], a[228:231], v[60:63], v[132:135]\n"
"s_barrier\n"
"v_mfma_f32_16x16x32_bf16 v[132:135], a[232:235], v[64:67], v[132:135]\n"
"global_load_dwordx4 v[108:111], v6, s[56:57] offset:0 sc1\n"
"ds_read2st64_b32 v[152:153], v13 offset0:0 offset1:1\n"
"v_mfma_f32_16x16x32_bf16 v[132:135], a[236:239], v[68:71], v[132:135]\n"
"ds_read_b128 v[56:59], v2 offset:1536\n"
"ds_read_b128 v[60:63], v3 offset:512\n"
"v_mfma_f32_16x16x32_bf16 v[132:135], a[240:243], v[72:75], v[132:135]\n"
"ds_read_b128 v[136:139], v4 offset:2560\n"
"ds_read_b128 v[140:143], v2 offset:1792\n"
"v_mfma_f32_16x16x32_bf16 v[132:135], a[244:247], v[76:79], v[132:135]\n"
"buffer_load_dwordx2 v[154:155], v9, s[40:43], s50 offen sc1\n"
"v_mfma_f32_16x16x32_bf16 v[132:135], a[248:251], v[80:83], v[132:135]\n"
"global_load_dwordx4 v[64:67], v6, s[56:57] offset:1024 sc1\n"
"ds_read_b128 v[144:147], v3 offset:768\n"
"v_mfma_f32_16x16x32_bf16 v[132:135], a[252:255], v[84:87], v[132:135]\n"
"ds_read_b128 v[148:151], v4 offset:2816\n"
"v_mfma_f32_16x16x32_bf16 v[76:79], v[88:91], a[0:3], 0\n"
"v_mfma_f32_16x16x32_bf16 v[76:79], v[92:95], a[4:7], v[76:79]\n"
"s_waitcnt vmcnt(15)\n"
"v_mfma_f32_16x16x32_bf16 v[76:79], v[36:39], a[8:11], v[76:79]\n"
"s_waitcnt lgkmcnt(3)\n"
"v_add_f32 v56, v56, v60\n"
"v_add_f32 v57, v57, v61\n"
"v_mfma_f32_16x16x32_bf16 v[76:79], v[40:43], a[12:15], v[76:79]\n"
"s_waitcnt vmcnt(13)\n"
"v_add_f32 v58, v58, v62\n"
"v_add_f32 v59, v59, v63\n"
"v_mfma_f32_16x16x32_bf16 v[76:79], v[44:47], a[16:19], v[76:79]\n"
"global_load_dwordx4 v[68:71], v7, s[56:57] offset:0 sc1\n"
"v_add_f32 v56, v56, v136\n"
"v_mfma_f32_16x16x32_bf16 v[76:79], v[48:51], a[20:23], v[76:79]\n"
"s_waitcnt vmcnt(12)\n"
"v_add_f32 v57, v57, v137\n"
"v_add_f32 v58, v58, v138\n"
"v_mfma_f32_16x16x32_bf16 v[76:79], v[52:55], a[24:27], v[76:79]\n"
"v_add_f32 v59, v59, v139\n"
"v_add_f32 v56, v56, v112\n"
"v_mfma_f32_16x16x32_bf16 v[76:79], v[96:99], a[28:31], v[76:79]\n"
"v_add_f32 v57, v57, v113\n"
"v_add_f32 v58, v58, v114\n"
"v_mfma_f32_16x16x32_bf16 v[60:63], v[88:91], a[32:35], 0\n"
"v_add_f32 v59, v59, v115\n"
"v_mfma_f32_16x16x32_bf16 v[60:63], v[92:95], a[36:39], v[60:63]\n"
"ds_write_addtid_b32 v76 offset:1536\n"
"global_load_dwordx4 v[72:75], v7, s[56:57] offset:1024 sc1\n"
"v_mfma_f32_16x16x32_bf16 v[60:63], v[36:39], a[40:43], v[60:63]\n"
"s_add_u32 s92, s50, 0xffd00000\n"
"s_and_b32 s95, s92, 0xfbffffff\n"
"ds_write_addtid_b32 v77 offset:6176\n"
"v_mfma_f32_16x16x32_bf16 v[60:63], v[40:43], a[44:47], v[60:63]\n"
"ds_write_addtid_b32 v78 offset:10880\n"
"v_fma_f32 v80, v18, v153, v26\n"
"v_mfma_f32_16x16x32_bf16 v[60:63], v[44:47], a[48:51], v[60:63]\n"
"ds_write_addtid_b32 v79 offset:15520\n"
"v_fma_f32 v81, v19, v153, v27\n"
"v_mfma_f32_16x16x32_bf16 v[60:63], v[48:51], a[52:55], v[60:63]\n"
"v_fma_f32 v82, v20, v153, v28\n"
"v_fma_f32 v83, v21, v153, v29\n"
"v_mfma_f32_16x16x32_bf16 v[60:63], v[52:55], a[56:59], v[60:63]\n"
"v_fma_f32 v56, v56, v152, v80\n"
"v_fma_f32 v57, v57, v152, v81\n"
"v_mfma_f32_16x16x32_bf16 v[60:63], v[96:99], a[60:63], v[60:63]\n"
"global_load_dwordx4 v[76:79], v8, s[56:57] offset:0 sc1\n"
"v_fma_f32 v58, v58, v152, v82\n"
"v_mfma_f32_16x16x32_bf16 v[84:87], v[88:91], a[64:67], 0\n"
"v_fma_f32 v59, v59, v152, v83\n"
"v_mul_f32_e64 v56, v56, |v56|\n"
"v_mfma_f32_16x16x32_bf16 v[84:87], v[92:95], a[68:71], v[84:87]\n"
"ds_write_addtid_b32 v60 offset:1792\n"
"v_mul_f32_e64 v57, v57, |v57|\n"
"v_mfma_f32_16x16x32_bf16 v[84:87], v[36:39], a[72:75], v[84:87]\n"
"ds_write_addtid_b32 v61 offset:6432\n"
"v_mul_f32_e64 v58, v58, |v58|\n"
"v_mfma_f32_16x16x32_bf16 v[84:87], v[40:43], a[76:79], v[84:87]\n"
"ds_write_addtid_b32 v62 offset:11136\n"
"global_load_dwordx4 v[80:83], v8, s[56:57] offset:1024 sc1\n"
"v_mfma_f32_16x16x32_bf16 v[84:87], v[44:47], a[80:83], v[84:87]\n"
"ds_write_addtid_b32 v63 offset:15776\n"
"v_mul_f32_e64 v59, v59, |v59|\n"
"v_mfma_f32_16x16x32_bf16 v[84:87], v[48:51], a[84:87], v[84:87]\n"
"v_cvt_pk_bf16_f32 v136, v56, v57\n"
"v_cvt_pk_bf16_f32 v137, v58, v59\n"
"v_mfma_f32_16x16x32_bf16 v[84:87], v[52:55], a[88:91], v[84:87]\n"
"v_pk_max_i16 v136, v136, 0\n"
"v_pk_max_i16 v137, v137, 0\n"
"v_mfma_f32_16x16x32_bf16 v[84:87], v[96:99], a[92:95], v[84:87]\n"
"s_waitcnt lgkmcnt(8)\n"
"v_add_f32 v140, v140, v144\n"
"v_add_f32 v141, v141, v145\n"
"v_mfma_f32_16x16x32_bf16 v[56:59], v[88:91], a[96:99], 0\n"
"v_add_f32 v142, v142, v146\n"
"v_add_f32 v143, v143, v147\n"
"v_mfma_f32_16x16x32_bf16 v[56:59], v[92:95], a[100:103], v[56:59]\n"
"ds_write_addtid_b32 v84 offset:2048\n"
"v_add_f32 v140, v140, v148\n"
"v_mfma_f32_16x16x32_bf16 v[56:59], v[36:39], a[104:107], v[56:59]\n"
"ds_write_addtid_b32 v85 offset:6688\n"
"v_add_f32 v141, v141, v149\n"
"v_mfma_f32_16x16x32_bf16 v[56:59], v[40:43], a[108:111], v[56:59]\n"
"ds_write_addtid_b32 v86 offset:11392\n"
"v_add_f32 v142, v142, v150\n"
"v_mfma_f32_16x16x32_bf16 v[56:59], v[44:47], a[112:115], v[56:59]\n"
"ds_write_addtid_b32 v87 offset:16032\n"
"v_add_f32 v143, v143, v151\n"
"v_mfma_f32_16x16x32_bf16 v[56:59], v[48:51], a[116:119], v[56:59]\n"
"s_waitcnt vmcnt(13)\n"
"buffer_store_dwordx2 v[0:1], v16, s[40:43], s93 offen offset:0 sc1\n"
"v_add_f32 v140, v140, v132\n"
"v_mfma_f32_16x16x32_bf16 v[56:59], v[52:55], a[120:123], v[56:59]\n"
"v_add_f32 v141, v141, v133\n"
"v_add_f32 v142, v142, v134\n"
"v_mfma_f32_16x16x32_bf16 v[56:59], v[96:99], a[124:127], v[56:59]\n"
"v_add_f32 v143, v143, v135\n"
"v_fma_f32 v60, v22, v153, v30\n"
"v_mfma_f32_16x16x32_bf16 v[84:87], v[88:91], a[128:131], 0\n"
"v_fma_f32 v61, v23, v153, v31\n"
"v_fma_f32 v62, v24, v153, v32\n"
"v_mfma_f32_16x16x32_bf16 v[84:87], v[92:95], a[132:135], v[84:87]\n"
"ds_write_addtid_b32 v56 offset:2304\n"
"v_fma_f32 v63, v25, v153, v33\n"
"v_mfma_f32_16x16x32_bf16 v[84:87], v[36:39], a[136:139], v[84:87]\n"
"ds_write_addtid_b32 v57 offset:6944\n"
"v_fma_f32 v140, v140, v152, v60\n"
"v_mfma_f32_16x16x32_bf16 v[84:87], v[40:43], a[140:143], v[84:87]\n"
"ds_write_addtid_b32 v58 offset:11648\n"
"v_fma_f32 v141, v141, v152, v61\n"
"v_mfma_f32_16x16x32_bf16 v[84:87], v[44:47], a[144:147], v[84:87]\n"
"s_waitcnt vmcnt(9)\n"
"ds_write_addtid_b32 v59 offset:16288\n"
"v_add_f32 v124, v124, v116\n"
"v_mfma_f32_16x16x32_bf16 v[84:87], v[48:51], a[148:151], v[84:87]\n"
"v_add_f32 v125, v125, v117\n"
"v_add_f32 v126, v126, v118\n"
"v_mfma_f32_16x16x32_bf16 v[84:87], v[52:55], a[152:155], v[84:87]\n"
"v_add_f32 v127, v127, v119\n"
"v_add_f32 v120, v120, v128\n"
"v_mfma_f32_16x16x32_bf16 v[84:87], v[96:99], a[156:159], v[84:87]\n"
"v_add_f32 v121, v121, v129\n"
"v_add_f32 v122, v122, v130\n"
"v_mfma_f32_16x16x32_bf16 v[56:59], v[88:91], a[160:163], 0\n"
"v_add_f32 v123, v123, v131\n"
"v_add_f32 v124, v124, v120\n"
"v_mfma_f32_16x16x32_bf16 v[56:59], v[92:95], a[164:167], v[56:59]\n"
"ds_write_addtid_b32 v84 offset:2560\n"
"v_add_f32 v125, v125, v121\n"
"v_mfma_f32_16x16x32_bf16 v[56:59], v[36:39], a[168:171], v[56:59]\n"
"s_add_u32 s58, s50, 0x180000\n"
"ds_write_addtid_b32 v85 offset:7200\n"
"v_add_f32 v126, v126, v122\n"
"v_mfma_f32_16x16x32_bf16 v[56:59], v[40:43], a[172:175], v[56:59]\n"
"s_and_b32 s58, s58, 0x3ffffff\n"
"ds_write_addtid_b32 v86 offset:11904\n"
"v_add_f32 v127, v127, v123\n"
"v_mfma_f32_16x16x32_bf16 v[56:59], v[44:47], a[176:179], v[56:59]\n"
"s_add_u32 s56, s40, s58\n"
"ds_write_addtid_b32 v87 offset:16544\n"
"v_fma_f32 v142, v142, v152, v62\n"
"v_mfma_f32_16x16x32_bf16 v[56:59], v[48:51], a[180:183], v[56:59]\n"
"s_addc_u32 s57, s41, 0\n"
"v_permlane32_swap_b32 v124, v126\n"
"v_mfma_f32_16x16x32_bf16 v[56:59], v[52:55], a[184:187], v[56:59]\n"
"s_waitcnt vmcnt(6)\n"
"v_cmp_ne_u64 vcc, s[48:49], v[154:155]\n"
"v_fma_f32 v143, v143, v152, v63\n"
"v_mfma_f32_16x16x32_bf16 v[56:59], v[96:99], a[188:191], v[56:59]\n"
"v_permlane32_swap_b32 v125, v127\n"
"v_mfma_f32_16x16x32_bf16 v[128:131], a[192:195], v[88:91], 0\n"
"s_cbranch_vccnz .LF_2s\n"
".LF_2r:\n"
"v_add_f32 v124, v124, v126\n"
"v_mfma_f32_16x16x32_bf16 v[128:131], a[196:199], v[92:95], v[128:131]\n"
"s_add_u32 s50, s50, 0x180000\n"
"ds_write_addtid_b32 v56 offset:2816\n"
"v_add_f32 v125, v125, v127\n"
"v_mfma_f32_16x16x32_bf16 v[128:131], a[200:203], v[36:39], v[128:131]\n"
"ds_write_addtid_b32 v57 offset:7456\n"
"global_load_dwordx4 v[112:115], v5, s[56:57] offset:0 sc1\n"
"v_mfma_f32_16x16x32_bf16 v[128:131], a[204:207], v[40:43], v[128:131]\n"
"ds_write_addtid_b32 v58 offset:12160\n"
"v_mul_f32_e64 v140, v140, |v140|\n"
"v_mfma_f32_16x16x32_bf16 v[128:131], a[208:211], v[44:47], v[128:131]\n"
"ds_write_addtid_b32 v59 offset:16800\n"
"v_mul_f32_e64 v141, v141, |v141|\n"
"v_mfma_f32_16x16x32_bf16 v[128:131], a[212:215], v[48:51], v[128:131]\n"
"v_permlane16_swap_b32 v124, v125\n"
"v_mfma_f32_16x16x32_bf16 v[128:131], a[216:219], v[52:55], v[128:131]\n"
"v_add_f32 v124, v124, v125\n"
"v_mul_f32_e64 v142, v142, |v142|\n"
"v_mfma_f32_16x16x32_bf16 v[128:131], a[220:223], v[96:99], v[128:131]\n"
"global_load_dwordx4 v[116:119], v5, s[56:57] offset:1024 sc1\n"
"v_add_f32_dpp v125, v124, v124 row_ror:8 row_mask:0xf bank_mask:0xf\n"
"v_mfma_f32_16x16x32_bf16 v[132:135], a[224:227], v[88:91], 0\n"
"s_waitcnt lgkmcnt(0)\n"
"v_mov_b32 v124, v125\n"
"v_mul_f32_e64 v143, v143, |v143|\n"
"v_mfma_f32_16x16x32_bf16 v[132:135], a[228:231], v[92:95], v[132:135]\n"
"s_barrier\n"
"v_mfma_f32_16x16x32_bf16 v[132:135], a[232:235], v[36:39], v[132:135]\n"
"v_permlane16_swap_b32 v125, v124\n"
"v_mfma_f32_16x16x32_bf16 v[132:135], a[236:239], v[40:43], v[132:135]\n"
"global_load_dwordx4 v[120:123], v6, s[56:57] offset:0 sc1\n"
"v_mul_f32 v125, 0xba800000, v125\n"
"v_mfma_f32_16x16x32_bf16 v[132:135], a[240:243], v[44:47], v[132:135]\n"
"buffer_load_dwordx2 v[144:145], v9, s[40:43], s50 offen sc1\n"
"v_mfma_f32_16x16x32_bf16 v[132:135], a[244:247], v[48:51], v[132:135]\n"
"v_fma_f32 v124, v124, s62, v17\n"
"v_fma_f32 v124, -v125, v125, v124\n"
"v_mfma_f32_16x16x32_bf16 v[132:135], a[248:251], v[52:55], v[132:135]\n"
"v_max_f32 v124, v17, v124\n"
"v_cvt_pk_bf16_f32 v138, v140, v141\n"
"v_mfma_f32_16x16x32_bf16 v[132:135], a[252:255], v[96:99], v[132:135]\n"
"v_rsq_f32 v124, v124\n"
"s_branch .LF_top\n"
".LF_1s:\n"
"s_cmp_ge_u32 s50, s53\n"
"s_cbranch_scc1 .LF_end\n"
"s_cmp_ge_u32 s50, s52\n"
"s_cbranch_scc1 .LF_1r\n"
"s_sub_u32 s60, s50, 0x0\n"
"s_mov_b32 s61, 200000\n"
"s_nop 7\n"
".LF_1p:\n"
"buffer_load_dwordx2 v[134:135], v9, s[40:43], s60 offen sc1\n"
"s_waitcnt vmcnt(0)\n"
"v_cmp_ne_u64 vcc, s[48:49], v[134:135]\n"
"s_cbranch_vccz .LF_1px\n"
"s_sleep 1\n"
"s_sub_u32 s61, s61, 1\n"
"s_cbranch_scc0 .LF_1p\n"
"s_branch .LF_end\n"
".LF_1px:\n"
"s_branch .LF_1r\n"
".LF_2s:\n"
"s_cmp_ge_u32 s50, s53\n"
"s_cbranch_scc1 .LF_end\n"
"s_cmp_ge_u32 s50, s52\n"
"s_cbranch_scc1 .LF_2r\n"
"s_sub_u32 s60, s50, 0x0\n"
"s_mov_b32 s61, 200000\n"
"s_nop 7\n"
".LF_2p:\n"
"buffer_load_dwordx2 v[154:155], v9, s[40:43], s60 offen sc1\n"
"s_waitcnt vmcnt(0)\n"
"v_cmp_ne_u64 vcc, s[48:49], v[154:155]\n"
"s_cbranch_vccz .LF_2px\n"
"s_sleep 1\n"
"s_sub_u32 s61, s61, 1\n"
"s_cbranch_scc0 .LF_2p\n"
"s_branch .LF_end\n"
".LF_2px:\n"
"s_branch .LF_2r\n"
".LF_3s:\n"
"s_cmp_ge_u32 s50, s53\n"
"s_cbranch_scc1 .LF_end\n"
"s_cmp_ge_u32 s50, s52\n"
"s_cbranch_scc1 .LF_3r\n"
"s_sub_u32 s60, s50, 0x0\n"
"s_mov_b32 s61, 200000\n"
"s_nop 7\n"
".LF_3p:\n"
"buffer_load_dwordx2 v[144:145], v9, s[40:43], s60 offen sc1\n"
"s_waitcnt vmcnt(0)\n"
"v_cmp_ne_u64 vcc, s[48:49], v[144:145]\n"
"s_cbranch_vccz .LF_3px\n"
"s_sleep 1\n"
"s_sub_u32 s61, s61, 1\n"
"s_cbranch_scc0 .LF_3p\n"
"s_branch .LF_end\n"
".LF_3px:\n"
"s_branch .LF_3r\n"
".LF_4s:\n"
"s_cmp_ge_u32 s50, s53\n"
"s_cbranch_scc1 .LF_end\n"
"s_cmp_ge_u32 s50, s52\n"
"s_cbranch_scc1 .LF_4r\n"
"s_sub_u32 s60, s50, 0x0\n"
"s_mov_b32 s61, 200000\n"
"s_nop 7\n"
".LF_4p:\n"
"buffer_load_dwordx2 v[138:139], v9, s[40:43], s60 offen sc1\n"
"s_waitcnt vmcnt(0)\n"
"v_cmp_ne_u64 vcc, s[48:49], v[138:139]\n"
"s_cbranch_vccz .LF_4px\n"
"s_sleep 1\n"
"s_sub_u32 s61, s61, 1\n"
"s_cbranch_scc0 .LF_4p\n"
"s_branch .LF_end\n"
".LF_4px:\n"
"s_branch .LF_4r\n"
".LF_end:\n"
"s_waitcnt vmcnt(0) lgkmcnt(0)\n"
"s_endpgm\n"
      :: "{v2}"((unsigned)(lds0 + ((wid + 3) & 3) * 19104 + trp)), "{v3}"((unsigned)(lds0 + ((wid + 2) & 3) * 19104 + trp)), "{v4}"((unsigned)(lds0 + ((wid + 1) & 3) * 19104 + trp)), "{v5}"((unsigned)(lane * 16 + wid * 8192 + ((0 + rot) % 4) * 2048 + 134283264)), "{v6}"((unsigned)(lane * 16 + wid * 8192 + ((1 + rot) % 4) * 2048 + 134283264)), "{v7}"((unsigned)(lane * 16 + wid * 8192 + ((2 + rot) % 4) * 2048 + 134283264)), "{v8}"((unsigned)(lane * 16 + wid * 8192 + ((3 + rot) % 4) * 2048 + 134283264)), "{v9}"((unsigned)((lane & 31) * 8 + 352768 + F_GL * 1572864)), "{v10}"((unsigned)(lane * 16 + 134561792)), "{v11}"((unsigned)(lds0 + ((wid + 0) & 3) * 19104 + 18592 + ((li >> 1) + 32 * (li & 1)) * 4)), "{v12}"((unsigned)(lds0 + ((wid + 1) & 3) * 19104 + 18592 + ((li >> 1) + 32 * (li & 1)) * 4)), "{v13}"((unsigned)(lds0 + ((wid + 2) & 3) * 19104 + 18592 + ((li >> 1) + 32 * (li & 1)) * 4)), "{v14}"((unsigned)(lds0 + ((wid + 3) & 3) * 19104 + 18592 + ((li >> 1) + 32 * (li & 1)) * 4)), "{v15}"((unsigned)(lane * 16 + pair * 1024 + 98304)), "{v16}"((unsigned)(lane ? DROP : pair * 8 + idx / 7 * 32 + 353024)), "{v17}"((unsigned)(0x3727c5ac)), "{v36}"((unsigned)(vT + tsw)), "{v37}"((unsigned)(vT + (tsw ^ 8))), "{v38}"((unsigned)(pair * 64 + lg * 16)), "{v39}"((unsigned)(vD + ((0 + rot) % 4) * 458752)), "{v40}"((unsigned)(vD + ((1 + rot) % 4) * 458752)), "{v41}"((unsigned)(vD + ((2 + rot) % 4) * 458752)), "{v42}"((unsigned)(vD + ((3 + rot) % 4) * 458752)), "{v43}"((unsigned)(lds0 + wid * 19104 + trp)), "{s[40:43]}"(rS), "{s[44:47]}"(rW), "{s[48:49]}"(wsp), "{s50}"((unsigned)(sA)), "{s51}"((unsigned)(sLo)), "{s52}"((unsigned)(sHi)), "{s53}"((unsigned)(sEx)), "{s54}"((unsigned)(sEnt)), "{s55}"((unsigned)(sLd)), "{s[56:57]}"(wsb + (sLo & 0x3ffffff)), "{s59}"((unsigned)(lds0 + wid * 19104)), "{s62}"((unsigned)(0x3a800000)), "{s[64:65]}"((u64)a.b1), "{s66}"((unsigned)(sW0 + 64 * ((wid + 1) & 3))), "{s67}"((unsigned)(sW0 + 64 * ((wid + 2) & 3))), "{s68}"((unsigned)(sW0 + 64 * ((wid + 3) & 3))), "{s69}"((unsigned)(sW0 + 64 * ((wid + 4) & 3)))
      : "v255", "a255", "s95", "vcc", "scc", "memory");

  } else {
    const int kq = idx / 7, grp = idx % 7, f0 = kq * 28 + wid * 7, pg0 = grp < 4 ? grp * 5 : 4 + grp * 4, pair = pg0 + wid;
    const unsigned rot = (set * 5) >> 1, tsw = (kq & 1) * 8, sW0 = f0 * 32 * 2048 + pg0 * 64, vD = (lane >> 2) * 2048 + (lane & 3) * 16;
    const u32x4 rW = desc(a.w2, 3584 * 2048);
    if (idx == 0) {
      const unsigned sEx = sHi + H_EX * 3 * RS, sEnt = -wid & 3;
    asm volatile(
"s_nop 4\n"
"v_mov_b32 v186, 0\n"
"global_load_dwordx2 v[0:1], v186, s[48:49] sc1\n"
"v_mov_b32 v188, 0x3f803f80\n"
"v_mov_b32 v189, 0x3f803f80\n"
"v_mov_b32 v190, 0x3f803f80\n"
"v_mov_b32 v191, 0x3f803f80\n"
"s_add_u32 m0, s55, 0\n"
"s_add_u32 s60, s66, 0\n"
"buffer_load_dwordx4 v50, s[44:47], s60 offen lds\n"
"s_add_u32 m0, s55, 1024\n"
"s_add_u32 s60, s66, 32768\n"
"buffer_load_dwordx4 v50, s[44:47], s60 offen lds\n"
"s_add_u32 m0, s55, 2048\n"
"s_add_u32 s60, s67, 0\n"
"buffer_load_dwordx4 v50, s[44:47], s60 offen lds\n"
"s_add_u32 m0, s55, 3072\n"
"s_add_u32 s60, s67, 32768\n"
"buffer_load_dwordx4 v50, s[44:47], s60 offen lds\n"
"s_add_u32 m0, s55, 4096\n"
"s_add_u32 s60, s68, 0\n"
"buffer_load_dwordx4 v50, s[44:47], s60 offen lds\n"
"s_add_u32 m0, s55, 5120\n"
"s_add_u32 s60, s68, 32768\n"
"buffer_load_dwordx4 v50, s[44:47], s60 offen lds\n"
"s_add_u32 m0, s55, 6144\n"
"s_add_u32 s60, s69, 0\n"
"buffer_load_dwordx4 v50, s[44:47], s60 offen lds\n"
"s_add_u32 m0, s55, 7168\n"
"s_add_u32 s60, s69, 32768\n"
"buffer_load_dwordx4 v50, s[44:47], s60 offen lds\n"
"s_add_u32 m0, s55, 8192\n"
"s_add_u32 s60, s70, 0\n"
"buffer_load_dwordx4 v50, s[44:47], s60 offen lds\n"
"s_add_u32 m0, s55, 9216\n"
"s_add_u32 s60, s70, 32768\n"
"buffer_load_dwordx4 v50, s[44:47], s60 offen lds\n"
"s_add_u32 m0, s55, 10240\n"
"s_add_u32 s60, s66, 0\n"
"buffer_load_dwordx4 v51, s[44:47], s60 offen lds\n"
"s_add_u32 m0, s55, 11264\n"
"s_add_u32 s60, s66, 32768\n"
"buffer_load_dwordx4 v51, s[44:47], s60 offen lds\n"
"s_add_u32 m0, s55, 12288\n"
"s_add_u32 s60, s67, 0\n"
"buffer_load_dwordx4 v51, s[44:47], s60 offen lds\n"
"s_add_u32 m0, s55, 13312\n"
"s_add_u32 s60, s67, 32768\n"
"buffer_load_dwordx4 v51, s[44:47], s60 offen lds\n"
"s_add_u32 m0, s55, 14336\n"
"s_add_u32 s60, s68, 0\n"
"buffer_load_dwordx4 v51, s[44:47], s60 offen lds\n"
"s_add_u32 m0, s55, 15360\n"
"s_add_u32 s60, s68, 32768\n"
"buffer_load_dwordx4 v51, s[44:47], s60 offen lds\n"
"s_add_u32 m0, s55, 16384\n"
"s_add_u32 s60, s69, 0\n"
"buffer_load_dwordx4 v51, s[44:47], s60 offen lds\n"
"s_add_u32 m0, s55, 17408\n"
"s_add_u32 s60, s69, 32768\n"
"buffer_load_dwordx4 v51, s[44:47], s60 offen lds\n"
"s_add_u32 m0, s55, 18432\n"
"s_add_u32 s60, s70, 0\n"
"buffer_load_dwordx4 v51, s[44:47], s60 offen lds\n"
"s_add_u32 m0, s55, 19456\n"
"s_add_u32 s60, s70, 32768\n"
"buffer_load_dwordx4 v51, s[44:47], s60 offen lds\n"
"s_add_u32 m0, s55, 20480\n"
"s_add_u32 s60, s66, 0\n"
"buffer_load_dwordx4 v52, s[44:47], s60 offen lds\n"
"s_add_u32 m0, s55, 21504\n"
"s_add_u32 s60, s66, 32768\n"
"buffer_load_dwordx4 v52, s[44:47], s60 offen lds\n"
"s_add_u32 m0, s55, 22528\n"
"s_add_u32 s60, s67, 0\n"
"buffer_load_dwordx4 v52, s[44:47], s60 offen lds\n"
"s_add_u32 m0, s55, 23552\n"
"s_add_u32 s60, s67, 32768\n"
"buffer_load_dwordx4 v52, s[44:47], s60 offen lds\n"
"s_add_u32 m0, s55, 24576\n"
"s_add_u32 s60, s68, 0\n"
"buffer_load_dwordx4 v52, s[44:47], s60 offen lds\n"
"s_add_u32 m0, s55, 25600\n"
"s_add_u32 s60, s68, 32768\n"
"buffer_load_dwordx4 v52, s[44:47], s60 offen lds\n"
"s_add_u32 m0, s55, 26624\n"
"s_add_u32 s60, s69, 0\n"
"buffer_load_dwordx4 v52, s[44:47], s60 offen lds\n"
"s_add_u32 m0, s55, 27648\n"
"s_add_u32 s60, s69, 32768\n"
"buffer_load_dwordx4 v52, s[44:47], s60 offen lds\n"
"s_add_u32 m0, s55, 28672\n"
"s_add_u32 s60, s70, 0\n"
"buffer_load_dwordx4 v52, s[44:47], s60 offen lds\n"
"s_add_u32 m0, s55, 29696\n"
"s_add_u32 s60, s70, 32768\n"
"buffer_load_dwordx4 v52, s[44:47], s60 offen lds\n"
"s_add_u32 m0, s55, 30720\n"
"s_add_u32 s60, s66, 0\n"
"buffer_load_dwordx4 v53, s[44:47], s60 offen lds\n"
"s_add_u32 m0, s55, 31744\n"
"s_add_u32 s60, s66, 32768\n"
"buffer_load_dwordx4 v53, s[44:47], s60 offen lds\n"
"s_add_u32 m0, s55, 32768\n"
"s_add_u32 s60, s67, 0\n"
"buffer_load_dwordx4 v53, s[44:47], s60 offen lds\n"
"s_add_u32 m0, s55, 33792\n"
"s_add_u32 s60, s67, 32768\n"
"buffer_load_dwordx4 v53, s[44:47], s60 offen lds\n"
"s_add_u32 m0, s55, 34816\n"
"s_add_u32 s60, s68, 0\n"
"buffer_load_dwordx4 v53, s[44:47], s60 offen lds\n"
"s_add_u32 m0, s55, 35840\n"
"s_add_u32 s60, s68, 32768\n"
"buffer_load_dwordx4 v53, s[44:47], s60 offen lds\n"
"s_add_u32 m0, s55, 36864\n"
"s_add_u32 s60, s69, 0\n"
"buffer_load_dwordx4 v53, s[44:47], s60 offen lds\n"
"s_add_u32 m0, s55, 37888\n"
"s_add_u32 s60, s69, 32768\n"
"buffer_load_dwordx4 v53, s[44:47], s60 offen lds\n"
"s_add_u32 m0, s55, 38912\n"
"s_add_u32 s60, s70, 0\n"
"buffer_load_dwordx4 v53, s[44:47], s60 offen lds\n"
"s_add_u32 m0, s55, 39936\n"
"s_add_u32 s60, s70, 32768\n"
"buffer_load_dwordx4 v53, s[44:47], s60 offen lds\n"
"s_waitcnt vmcnt(38)\n"
"ds_read_b64_tr_b16 a[0:1], v48 offset:0\n"
"ds_read_b64_tr_b16 a[2:3], v48 offset:256\n"
"ds_read_b64_tr_b16 a[28:29], v49 offset:0\n"
"ds_read_b64_tr_b16 a[30:31], v49 offset:256\n"
"v_add_u32 v0, 2, v0\n"
"v_add_u32 v1, 0x9e3779b9, v1\n"
"v_or_b32 v0, 1, v0\n"
"v_readfirstlane_b32 s49, v1\n"
"v_readfirstlane_b32 s48, v0\n"
"s_waitcnt vmcnt(36)\n"
"ds_read_b64_tr_b16 a[56:57], v48 offset:2048\n"
"ds_read_b64_tr_b16 a[58:59], v48 offset:2304\n"
"ds_read_b64_tr_b16 a[84:85], v49 offset:2048\n"
"ds_read_b64_tr_b16 a[86:87], v49 offset:2304\n"
"s_waitcnt lgkmcnt(4)\n"
"s_add_u32 m0, s55, 0\n"
"s_add_u32 s60, s66, 0\n"
"buffer_load_dwordx4 v54, s[44:47], s60 offen lds\n"
"s_add_u32 m0, s55, 1024\n"
"s_add_u32 s60, s66, 32768\n"
"buffer_load_dwordx4 v54, s[44:47], s60 offen lds\n"
"s_waitcnt vmcnt(36)\n"
"ds_read_b64_tr_b16 a[112:113], v48 offset:4096\n"
"ds_read_b64_tr_b16 a[114:115], v48 offset:4352\n"
"ds_read_b64_tr_b16 a[140:141], v49 offset:4096\n"
"ds_read_b64_tr_b16 a[142:143], v49 offset:4352\n"
"s_waitcnt lgkmcnt(4)\n"
"s_add_u32 m0, s55, 2048\n"
"s_add_u32 s60, s67, 0\n"
"buffer_load_dwordx4 v54, s[44:47], s60 offen lds\n"
"s_add_u32 m0, s55, 3072\n"
"s_add_u32 s60, s67, 32768\n"
"buffer_load_dwordx4 v54, s[44:47], s60 offen lds\n"
"s_waitcnt vmcnt(36)\n"
"ds_read_b64_tr_b16 a[168:169], v48 offset:6144\n"
"ds_read_b64_tr_b16 a[170:171], v48 offset:6400\n"
"ds_read_b64_tr_b16 a[196:197], v49 offset:6144\n"
"ds_read_b64_tr_b16 a[198:199], v49 offset:6400\n"
"s_waitcnt lgkmcnt(4)\n"
"s_add_u32 m0, s55, 4096\n"
"s_add_u32 s60, s68, 0\n"
"buffer_load_dwordx4 v54, s[44:47], s60 offen lds\n"
"s_add_u32 m0, s55, 5120\n"
"s_add_u32 s60, s68, 32768\n"
"buffer_load_dwordx4 v54, s[44:47], s60 offen lds\n"
"s_waitcnt vmcnt(36)\n"
"ds_read_b64_tr_b16 a[224:225], v48 offset:8192\n"
"ds_read_b64_tr_b16 a[226:227], v48 offset:8448\n"
"ds_read_b64_tr_b16 a[252:253], v49 offset:8192\n"
"ds_read_b64_tr_b16 a[254:255], v49 offset:8448\n"
"s_waitcnt lgkmcnt(4)\n"
"s_add_u32 m0, s55, 6144\n"
"s_add_u32 s60, s69, 0\n"
"buffer_load_dwordx4 v54, s[44:47], s60 offen lds\n"
"s_add_u32 m0, s55, 7168\n"
"s_add_u32 s60, s69, 32768\n"
"buffer_load_dwordx4 v54, s[44:47], s60 offen lds\n"
"s_waitcnt vmcnt(36)\n"
"ds_read_b64_tr_b16 a[4:5], v48 offset:10240\n"
"ds_read_b64_tr_b16 a[6:7], v48 offset:10496\n"
"ds_read_b64_tr_b16 a[32:33], v49 offset:10240\n"
"ds_read_b64_tr_b16 a[34:35], v49 offset:10496\n"
"s_waitcnt lgkmcnt(4)\n"
"s_add_u32 m0, s55, 8192\n"
"s_add_u32 s60, s70, 0\n"
"buffer_load_dwordx4 v54, s[44:47], s60 offen lds\n"
"s_add_u32 m0, s55, 9216\n"
"s_add_u32 s60, s70, 32768\n"
"buffer_load_dwordx4 v54, s[44:47], s60 offen lds\n"
"s_waitcnt vmcnt(36)\n"
"ds_read_b64_tr_b16 a[60:61], v48 offset:12288\n"
"ds_read_b64_tr_b16 a[62:63], v48 offset:12544\n"
"ds_read_b64_tr_b16 a[88:89], v49 offset:12288\n"
"ds_read_b64_tr_b16 a[90:91], v49 offset:12544\n"
"s_waitcnt lgkmcnt(4)\n"
"s_add_u32 m0, s55, 10240\n"
"s_add_u32 s60, s66, 0\n"
"buffer_load_dwordx4 v55, s[44:47], s60 offen lds\n"
"s_add_u32 m0, s55, 11264\n"
"s_add_u32 s60, s66, 32768\n"
"buffer_load_dwordx4 v55, s[44:47], s60 offen lds\n"
"s_waitcnt vmcnt(36)\n"
"ds_read_b64_tr_b16 a[116:117], v48 offset:14336\n"
"ds_read_b64_tr_b16 a[118:119], v48 offset:14592\n"
"ds_read_b64_tr_b16 a[144:145], v49 offset:14336\n"
"ds_read_b64_tr_b16 a[146:147], v49 offset:14592\n"
"s_waitcnt lgkmcnt(4)\n"
"s_add_u32 m0, s55, 12288\n"
"s_add_u32 s60, s67, 0\n"
"buffer_load_dwordx4 v55, s[44:47], s60 offen lds\n"
"s_add_u32 m0, s55, 13312\n"
"s_add_u32 s60, s67, 32768\n"
"buffer_load_dwordx4 v55, s[44:47], s60 offen lds\n"
"s_waitcnt vmcnt(36)\n"
"ds_read_b64_tr_b16 a[172:173], v48 offset:16384\n"
"ds_read_b64_tr_b16 a[174:175], v48 offset:16640\n"
"ds_read_b64_tr_b16 a[200:201], v49 offset:16384\n"
"ds_read_b64_tr_b16 a[202:203], v49 offset:16640\n"
"s_waitcnt lgkmcnt(4)\n"
"s_add_u32 m0, s55, 14336\n"
"s_add_u32 s60, s68, 0\n"
"buffer_load_dwordx4 v55, s[44:47], s60 offen lds\n"
"s_add_u32 m0, s55, 15360\n"
"s_add_u32 s60, s68, 32768\n"
"buffer_load_dwordx4 v55, s[44:47], s60 offen lds\n"
"s_waitcnt vmcnt(36)\n"
"ds_read_b64_tr_b16 a[228:229], v48 offset:18432\n"
"ds_read_b64_tr_b16 a[230:231], v48 offset:18688\n"
"ds_read_b64_tr_b16 v[24:25], v49 offset:18432\n"
"ds_read_b64_tr_b16 v[26:27], v49 offset:18688\n"
"s_waitcnt lgkmcnt(4)\n"
"s_add_u32 m0, s55, 16384\n"
"s_add_u32 s60, s69, 0\n"
"buffer_load_dwordx4 v55, s[44:47], s60 offen lds\n"
"s_add_u32 m0, s55, 17408\n"
"s_add_u32 s60, s69, 32768\n"
"buffer_load_dwordx4 v55, s[44:47], s60 offen lds\n"
"s_waitcnt vmcnt(36)\n"
"ds_read_b64_tr_b16 a[8:9], v48 offset:20480\n"
"ds_read_b64_tr_b16 a[10:11], v48 offset:20736\n"
"ds_read_b64_tr_b16 a[36:37], v49 offset:20480\n"
"ds_read_b64_tr_b16 a[38:39], v49 offset:20736\n"
"s_waitcnt lgkmcnt(4)\n"
"s_add_u32 m0, s55, 18432\n"
"s_add_u32 s60, s70, 0\n"
"buffer_load_dwordx4 v55, s[44:47], s60 offen lds\n"
"s_add_u32 m0, s55, 19456\n"
"s_add_u32 s60, s70, 32768\n"
"buffer_load_dwordx4 v55, s[44:47], s60 offen lds\n"
"s_waitcnt vmcnt(36)\n"
"ds_read_b64_tr_b16 a[64:65], v48 offset:22528\n"
"ds_read_b64_tr_b16 a[66:67], v48 offset:22784\n"
"ds_read_b64_tr_b16 a[92:93], v49 offset:22528\n"
"ds_read_b64_tr_b16 a[94:95], v49 offset:22784\n"
"s_waitcnt lgkmcnt(4)\n"
"s_add_u32 m0, s55, 20480\n"
"s_add_u32 s60, s66, 0\n"
"buffer_load_dwordx4 v56, s[44:47], s60 offen lds\n"
"s_add_u32 m0, s55, 21504\n"
"s_add_u32 s60, s66, 32768\n"
"buffer_load_dwordx4 v56, s[44:47], s60 offen lds\n"
"s_waitcnt vmcnt(36)\n"
"ds_read_b64_tr_b16 a[120:121], v48 offset:24576\n"
"ds_read_b64_tr_b16 a[122:123], v48 offset:24832\n"
"ds_read_b64_tr_b16 a[148:149], v49 offset:24576\n"
"ds_read_b64_tr_b16 a[150:151], v49 offset:24832\n"
"s_waitcnt lgkmcnt(4)\n"
"s_add_u32 m0, s55, 22528\n"
"s_add_u32 s60, s67, 0\n"
"buffer_load_dwordx4 v56, s[44:47], s60 offen lds\n"
"s_add_u32 m0, s55, 23552\n"
"s_add_u32 s60, s67, 32768\n"
"buffer_load_dwordx4 v56, s[44:47], s60 offen lds\n"
"s_waitcnt vmcnt(36)\n"
"ds_read_b64_tr_b16 a[176:177], v48 offset:26624\n"
"ds_read_b64_tr_b16 a[178:179], v48 offset:26880\n"
"ds_read_b64_tr_b16 a[204:205], v49 offset:26624\n"
"ds_read_b64_tr_b16 a[206:207], v49 offset:26880\n"
"s_waitcnt lgkmcnt(4)\n"
"s_add_u32 m0, s55, 24576\n"
"s_add_u32 s60, s68, 0\n"
"buffer_load_dwordx4 v56, s[44:47], s60 offen lds\n"
"s_add_u32 m0, s55, 25600\n"
"s_add_u32 s60, s68, 32768\n"
"buffer_load_dwordx4 v56, s[44:47], s60 offen lds\n"
"s_waitcnt vmcnt(36)\n"
"ds_read_b64_tr_b16 a[232:233], v48 offset:28672\n"
"ds_read_b64_tr_b16 a[234:235], v48 offset:28928\n"
"ds_read_b64_tr_b16 v[28:29], v49 offset:28672\n"
"ds_read_b64_tr_b16 v[30:31], v49 offset:28928\n"
"s_waitcnt lgkmcnt(4)\n"
"s_add_u32 m0, s55, 26624\n"
"s_add_u32 s60, s69, 0\n"
"buffer_load_dwordx4 v56, s[44:47], s60 offen lds\n"
"s_add_u32 m0, s55, 27648\n"
"s_add_u32 s60, s69, 32768\n"
"buffer_load_dwordx4 v56, s[44:47], s60 offen lds\n"
"s_waitcnt vmcnt(36)\n"
"ds_read_b64_tr_b16 a[12:13], v48 offset:30720\n"
"ds_read_b64_tr_b16 a[14:15], v48 offset:30976\n"
"ds_read_b64_tr_b16 a[40:41], v49 offset:30720\n"
"ds_read_b64_tr_b16 a[42:43], v49 offset:30976\n"
"s_waitcnt lgkmcnt(4)\n"
"s_add_u32 m0, s55, 28672\n"
"s_add_u32 s60, s70, 0\n"
"buffer_load_dwordx4 v56, s[44:47], s60 offen lds\n"
"s_add_u32 m0, s55, 29696\n"
"s_add_u32 s60, s70, 32768\n"
"buffer_load_dwordx4 v56, s[44:47], s60 offen lds\n"
"s_waitcnt vmcnt(36)\n"
"ds_read_b64_tr_b16 a[68:69], v48 offset:32768\n"
"ds_read_b64_tr_b16 a[70:71], v48 offset:33024\n"
"ds_read_b64_tr_b16 a[96:97], v49 offset:32768\n"
"ds_read_b64_tr_b16 a[98:99], v49 offset:33024\n"
"s_waitcnt lgkmcnt(4)\n"
"s_waitcnt vmcnt(34)\n"
"ds_read_b64_tr_b16 a[124:125], v48 offset:34816\n"
"ds_read_b64_tr_b16 a[126:127], v48 offset:35072\n"
"ds_read_b64_tr_b16 a[152:153], v49 offset:34816\n"
"ds_read_b64_tr_b16 a[154:155], v49 offset:35072\n"
"s_waitcnt lgkmcnt(4)\n"
"s_waitcnt vmcnt(32)\n"
"ds_read_b64_tr_b16 a[180:181], v48 offset:36864\n"
"ds_read_b64_tr_b16 a[182:183], v48 offset:37120\n"
"ds_read_b64_tr_b16 a[208:209], v49 offset:36864\n"
"ds_read_b64_tr_b16 a[210:211], v49 offset:37120\n"
"s_waitcnt lgkmcnt(4)\n"
"s_waitcnt vmcnt(30)\n"
"ds_read_b64_tr_b16 a[236:237], v48 offset:38912\n"
"ds_read_b64_tr_b16 a[238:239], v48 offset:39168\n"
"ds_read_b64_tr_b16 v[32:33], v49 offset:38912\n"
"ds_read_b64_tr_b16 v[34:35], v49 offset:39168\n"
"s_waitcnt lgkmcnt(4)\n"
"s_waitcnt vmcnt(28)\n"
"ds_read_b64_tr_b16 a[16:17], v48 offset:0\n"
"ds_read_b64_tr_b16 a[18:19], v48 offset:256\n"
"ds_read_b64_tr_b16 a[44:45], v49 offset:0\n"
"ds_read_b64_tr_b16 a[46:47], v49 offset:256\n"
"s_waitcnt lgkmcnt(4)\n"
"s_waitcnt vmcnt(26)\n"
"ds_read_b64_tr_b16 a[72:73], v48 offset:2048\n"
"ds_read_b64_tr_b16 a[74:75], v48 offset:2304\n"
"ds_read_b64_tr_b16 a[100:101], v49 offset:2048\n"
"ds_read_b64_tr_b16 a[102:103], v49 offset:2304\n"
"s_waitcnt lgkmcnt(4)\n"
"s_waitcnt vmcnt(24)\n"
"ds_read_b64_tr_b16 a[128:129], v48 offset:4096\n"
"ds_read_b64_tr_b16 a[130:131], v48 offset:4352\n"
"ds_read_b64_tr_b16 a[156:157], v49 offset:4096\n"
"ds_read_b64_tr_b16 a[158:159], v49 offset:4352\n"
"s_waitcnt lgkmcnt(4)\n"
"s_waitcnt vmcnt(22)\n"
"ds_read_b64_tr_b16 a[184:185], v48 offset:6144\n"
"ds_read_b64_tr_b16 a[186:187], v48 offset:6400\n"
"ds_read_b64_tr_b16 a[212:213], v49 offset:6144\n"
"ds_read_b64_tr_b16 a[214:215], v49 offset:6400\n"
"s_waitcnt lgkmcnt(4)\n"
"s_waitcnt vmcnt(20)\n"
"ds_read_b64_tr_b16 a[240:241], v48 offset:8192\n"
"ds_read_b64_tr_b16 a[242:243], v48 offset:8448\n"
"ds_read_b64_tr_b16 v[36:37], v49 offset:8192\n"
"ds_read_b64_tr_b16 v[38:39], v49 offset:8448\n"
"s_waitcnt lgkmcnt(4)\n"
"s_waitcnt vmcnt(18)\n"
"ds_read_b64_tr_b16 a[20:21], v48 offset:10240\n"
"ds_read_b64_tr_b16 a[22:23], v48 offset:10496\n"
"ds_read_b64_tr_b16 a[48:49], v49 offset:10240\n"
"ds_read_b64_tr_b16 a[50:51], v49 offset:10496\n"
"s_waitcnt lgkmcnt(4)\n"
"s_waitcnt vmcnt(16)\n"
"ds_read_b64_tr_b16 a[76:77], v48 offset:12288\n"
"ds_read_b64_tr_b16 a[78:79], v48 offset:12544\n"
"ds_read_b64_tr_b16 a[104:105], v49 offset:12288\n"
"ds_read_b64_tr_b16 a[106:107], v49 offset:12544\n"
"s_waitcnt lgkmcnt(4)\n"
"s_waitcnt vmcnt(14)\n"
"ds_read_b64_tr_b16 a[132:133], v48 offset:14336\n"
"ds_read_b64_tr_b16 a[134:135], v48 offset:14592\n"
"ds_read_b64_tr_b16 a[160:161], v49 offset:14336\n"
"ds_read_b64_tr_b16 a[162:163], v49 offset:14592\n"
"s_waitcnt lgkmcnt(4)\n"
"s_waitcnt vmcnt(12)\n"
"ds_read_b64_tr_b16 a[188:189], v48 offset:16384\n"
"ds_read_b64_tr_b16 a[190:191], v48 offset:16640\n"
"ds_read_b64_tr_b16 a[216:217], v49 offset:16384\n"
"ds_read_b64_tr_b16 a[218:219], v49 offset:16640\n"
"s_waitcnt lgkmcnt(4)\n"
"s_waitcnt vmcnt(10)\n"
"ds_read_b64_tr_b16 a[244:245], v48 offset:18432\n"
"ds_read_b64_tr_b16 a[246:247], v48 offset:18688\n"
"ds_read_b64_tr_b16 v[40:41], v49 offset:18432\n"
"ds_read_b64_tr_b16 v[42:43], v49 offset:18688\n"
"s_waitcnt lgkmcnt(4)\n"
"s_waitcnt vmcnt(8)\n"
"ds_read_b64_tr_b16 a[24:25], v48 offset:20480\n"
"ds_read_b64_tr_b16 a[26:27], v48 offset:20736\n"
"ds_read_b64_tr_b16 a[52:53], v49 offset:20480\n"
"ds_read_b64_tr_b16 a[54:55], v49 offset:20736\n"
"s_waitcnt lgkmcnt(4)\n"
"s_waitcnt vmcnt(6)\n"
"ds_read_b64_tr_b16 a[80:81], v48 offset:22528\n"
"ds_read_b64_tr_b16 a[82:83], v48 offset:22784\n"
"ds_read_b64_tr_b16 a[108:109], v49 offset:22528\n"
"ds_read_b64_tr_b16 a[110:111], v49 offset:22784\n"
"s_waitcnt lgkmcnt(4)\n"
"s_waitcnt vmcnt(4)\n"
"ds_read_b64_tr_b16 a[136:137], v48 offset:24576\n"
"ds_read_b64_tr_b16 a[138:139], v48 offset:24832\n"
"ds_read_b64_tr_b16 a[164:165], v49 offset:24576\n"
"ds_read_b64_tr_b16 a[166:167], v49 offset:24832\n"
"s_waitcnt lgkmcnt(4)\n"
"s_waitcnt vmcnt(2)\n"
"ds_read_b64_tr_b16 a[192:193], v48 offset:26624\n"
"ds_read_b64_tr_b16 a[194:195], v48 offset:26880\n"
"ds_read_b64_tr_b16 a[220:221], v49 offset:26624\n"
"ds_read_b64_tr_b16 a[222:223], v49 offset:26880\n"
"s_waitcnt lgkmcnt(4)\n"
"s_waitcnt vmcnt(0)\n"
"ds_read_b64_tr_b16 a[248:249], v48 offset:28672\n"
"ds_read_b64_tr_b16 a[250:251], v48 offset:28928\n"
"ds_read_b64_tr_b16 v[44:45], v49 offset:28672\n"
"ds_read_b64_tr_b16 v[46:47], v49 offset:28928\n"
"s_waitcnt lgkmcnt(4)\n"
"s_waitcnt vmcnt(0)\n"
"s_waitcnt lgkmcnt(0)\n"
"s_mov_b32 m0, s59\n"
"s_barrier\n"
"s_mov_b64 s[64:65], s[48:49]\n"
"v_mov_b32 v138, 0\n"
"v_mov_b32 v139, 0\n"
"v_mov_b32 v144, 0\n"
"v_mov_b32 v145, 0\n"
"v_mov_b32 v136, 0\n"
"v_mov_b32 v137, 0\n"
"v_mov_b32 v172, 0\n"
"v_mov_b32 v173, 0\n"
"s_mov_b32 s93, 0x80000000\n"
"s_mov_b32 s92, 0x80000000\n"
"s_mov_b32 s92, 0x80000000\n"
"s_mov_b32 s95, 0x80000000\n"
"s_mov_b32 s93, 0x80000000\n"
"s_mov_b32 s95, 0x80000000\n"
"s_mov_b32 s95, 0x80000000\n"
"s_mov_b32 s94, 0x80000000\n"
"s_mov_b32 s94, 0x80000000\n"
"v_mov_b32 v136, 0\n"
"v_mov_b32 v137, 0\n"
"s_cmp_eq_u32 s54, 0\n"
"s_cbranch_scc1 .LH_1s\n"
"s_cmp_eq_u32 s54, 1\n"
"s_cbranch_scc1 .LH_2s\n"
"s_cmp_eq_u32 s54, 2\n"
"s_cbranch_scc1 .LH_3s\n"
"s_cmp_eq_u32 s54, 3\n"
"s_cbranch_scc1 .LH_4s\n"
".p2align 6\n"
".LH_top:\n"
"v_mfma_f32_16x16x32_bf16 v[60:63], v[104:107], a[0:3], 0\n"
"v_mfma_f32_16x16x32_bf16 v[60:63], v[72:75], a[4:7], v[60:63]\n"
"global_load_dwordx4 v[48:51], v7, s[56:57] offset:0 sc1\n"
"v_mfma_f32_16x16x32_bf16 v[60:63], v[76:79], a[8:11], v[60:63]\n"
"v_mfma_f32_16x16x32_bf16 v[60:63], v[80:83], a[12:15], v[60:63]\n"
"s_waitcnt vmcnt(6)\n"
"v_mfma_f32_16x16x32_bf16 v[60:63], v[84:87], a[16:19], v[60:63]\n"
"s_waitcnt lgkmcnt(3)\n"
"v_add_f32 v100, v100, v68\n"
"v_add_f32 v101, v101, v69\n"
"v_mfma_f32_16x16x32_bf16 v[60:63], v[88:91], a[20:23], v[60:63]\n"
"s_waitcnt vmcnt(5)\n"
"v_add_f32 v102, v102, v70\n"
"v_add_f32 v103, v103, v71\n"
"v_mfma_f32_16x16x32_bf16 v[60:63], v[92:95], a[24:27], v[60:63]\n"
"global_load_dwordx4 v[52:55], v8, s[56:57] offset:0 sc1\n"
"v_add_f32 v100, v100, v96\n"
"v_mfma_f32_16x16x32_bf16 v[64:67], v[104:107], a[28:31], 0\n"
"buffer_load_dwordx2 v[136:137], v12, s[40:43], s50 offen sc1\n"
"v_mfma_f32_16x16x32_bf16 v[64:67], v[72:75], a[32:35], v[64:67]\n"
"ds_write_addtid_b32 v60 offset:0\n"
"v_add_f32 v101, v101, v97\n"
"v_mfma_f32_16x16x32_bf16 v[64:67], v[76:79], a[36:39], v[64:67]\n"
"ds_write_addtid_b32 v61 offset:4640\n"
"global_load_dwordx4 v[56:59], v9, s[56:57] offset:0 sc1\n"
"v_mfma_f32_16x16x32_bf16 v[64:67], v[80:83], a[40:43], v[64:67]\n"
"ds_write_addtid_b32 v62 offset:9344\n"
"v_add_f32 v102, v102, v98\n"
"v_mfma_f32_16x16x32_bf16 v[64:67], v[84:87], a[44:47], v[64:67]\n"
"ds_write_addtid_b32 v63 offset:13984\n"
"v_add_f32 v103, v103, v99\n"
"v_mfma_f32_16x16x32_bf16 v[64:67], v[88:91], a[48:51], v[64:67]\n"
"v_add_f32 v100, v100, v116\n"
"v_add_f32 v101, v101, v117\n"
"v_mfma_f32_16x16x32_bf16 v[64:67], v[92:95], a[52:55], v[64:67]\n"
"v_add_f32 v102, v102, v118\n"
"v_add_f32 v103, v103, v119\n"
"v_mfma_f32_16x16x32_bf16 v[68:71], v[104:107], a[56:59], 0\n"
"s_waitcnt vmcnt(7)\n"
"buffer_store_dwordx2 v[0:1], v14, s[40:43], s93 offen offset:0 sc1\n"
"v_mfma_f32_16x16x32_bf16 v[68:71], v[72:75], a[60:63], v[68:71]\n"
"s_add_u32 s95, s50, 0xffd00000\n"
"s_and_b32 s94, s95, 0xfbffffff\n"
"ds_write_addtid_b32 v64 offset:256\n"
"v_mfma_f32_16x16x32_bf16 v[68:71], v[76:79], a[64:67], v[68:71]\n"
"ds_write_addtid_b32 v65 offset:4896\n"
"global_load_dwordx4 v[60:63], v10, s[56:57] offset:0 sc1\n"
"v_mfma_f32_16x16x32_bf16 v[68:71], v[80:83], a[68:71], v[68:71]\n"
"ds_write_addtid_b32 v66 offset:9600\n"
"v_cvt_pk_bf16_f32 v96, v100, v101\n"
"v_mfma_f32_16x16x32_bf16 v[68:71], v[84:87], a[72:75], v[68:71]\n"
"ds_write_addtid_b32 v67 offset:14240\n"
"v_cvt_pk_bf16_f32 v97, v102, v103\n"
"v_mfma_f32_16x16x32_bf16 v[68:71], v[88:91], a[76:79], v[68:71]\n"
"s_waitcnt lgkmcnt(8)\n"
"v_add_f32 v124, v124, v128\n"
"v_add_f32 v125, v125, v129\n"
"v_mfma_f32_16x16x32_bf16 v[68:71], v[92:95], a[80:83], v[68:71]\n"
"v_add_f32 v126, v126, v130\n"
"v_add_f32 v127, v127, v131\n"
"v_mfma_f32_16x16x32_bf16 v[100:103], v[104:107], a[84:87], 0\n"
"global_load_dwordx4 v[64:67], v11, s[56:57] offset:0 sc1\n"
"v_add_f32 v124, v124, v132\n"
"v_mfma_f32_16x16x32_bf16 v[100:103], v[72:75], a[88:91], v[100:103]\n"
"ds_write_addtid_b32 v68 offset:512\n"
"v_add_f32 v125, v125, v133\n"
"v_mfma_f32_16x16x32_bf16 v[100:103], v[76:79], a[92:95], v[100:103]\n"
"ds_write_addtid_b32 v69 offset:5152\n"
"v_add_f32 v126, v126, v134\n"
"v_mfma_f32_16x16x32_bf16 v[100:103], v[80:83], a[96:99], v[100:103]\n"
"ds_write_addtid_b32 v70 offset:9856\n"
"v_add_f32 v127, v127, v135\n"
"v_mfma_f32_16x16x32_bf16 v[100:103], v[84:87], a[100:103], v[100:103]\n"
"ds_write_addtid_b32 v71 offset:14496\n"
"v_add_f32 v124, v124, v120\n"
"v_mfma_f32_16x16x32_bf16 v[100:103], v[88:91], a[104:107], v[100:103]\n"
"v_add_f32 v125, v125, v121\n"
"v_add_f32 v126, v126, v122\n"
"v_mfma_f32_16x16x32_bf16 v[100:103], v[92:95], a[108:111], v[100:103]\n"
"v_add_f32 v127, v127, v123\n"
"v_cvt_pk_bf16_f32 v98, v124, v125\n"
"v_mfma_f32_16x16x32_bf16 v[68:71], v[104:107], a[112:115], 0\n"
"v_cvt_pk_bf16_f32 v99, v126, v127\n"
"v_mfma_f32_16x16x32_bf16 v[68:71], v[72:75], a[116:119], v[68:71]\n"
"ds_write_addtid_b32 v100 offset:768\n"
"v_mfma_f32_16x16x32_bf16 v[68:71], v[76:79], a[120:123], v[68:71]\n"
"ds_write_addtid_b32 v101 offset:5408\n"
"v_mfma_f32_16x16x32_bf16 v[68:71], v[80:83], a[124:127], v[68:71]\n"
"ds_write_addtid_b32 v102 offset:10112\n"
"v_mfma_f32_16x16x32_bf16 v[68:71], v[84:87], a[128:131], v[68:71]\n"
"ds_write_addtid_b32 v103 offset:14752\n"
"v_mfma_f32_16x16x32_bf16 v[68:71], v[88:91], a[132:135], v[68:71]\n"
"v_mfma_f32_16x16x32_bf16 v[68:71], v[92:95], a[136:139], v[68:71]\n"
"v_mfma_f32_16x16x32_bf16 v[116:119], v[104:107], a[140:143], 0\n"
"v_mfma_f32_16x16x32_bf16 v[116:119], v[72:75], a[144:147], v[116:119]\n"
"ds_write_addtid_b32 v68 offset:1024\n"
"v_mfma_f32_16x16x32_bf16 v[116:119], v[76:79], a[148:151], v[116:119]\n"
"ds_write_addtid_b32 v69 offset:5664\n"
"v_mfma_f32_16x16x32_bf16 v[116:119], v[80:83], a[152:155], v[116:119]\n"
"ds_write_addtid_b32 v70 offset:10368\n"
"v_mfma_f32_16x16x32_bf16 v[116:119], v[84:87], a[156:159], v[116:119]\n"
"ds_write_addtid_b32 v71 offset:15008\n"
"v_mfma_f32_16x16x32_bf16 v[116:119], v[88:91], a[160:163], v[116:119]\n"
"buffer_store_dwordx4 v[96:99], v13, s[40:43], s94 offen sc1\n"
"v_mfma_f32_16x16x32_bf16 v[116:119], v[92:95], a[164:167], v[116:119]\n"
"v_mfma_f32_16x16x32_bf16 v[100:103], a[168:171], v[104:107], 0\n"
"v_mfma_f32_16x16x32_bf16 v[100:103], a[172:175], v[72:75], v[100:103]\n"
"ds_write_addtid_b32 v116 offset:1280\n"
"v_mfma_f32_16x16x32_bf16 v[100:103], a[176:179], v[76:79], v[100:103]\n"
"ds_write_addtid_b32 v117 offset:5920\n"
"v_mfma_f32_16x16x32_bf16 v[100:103], a[180:183], v[80:83], v[100:103]\n"
"ds_write_addtid_b32 v118 offset:10624\n"
"v_mfma_f32_16x16x32_bf16 v[100:103], a[184:187], v[84:87], v[100:103]\n"
"ds_write_addtid_b32 v119 offset:15264\n"
"v_mfma_f32_16x16x32_bf16 v[100:103], a[188:191], v[88:91], v[100:103]\n"
"v_mfma_f32_16x16x32_bf16 v[100:103], a[192:195], v[92:95], v[100:103]\n"
"v_mfma_f32_16x16x32_bf16 v[96:99], a[196:199], v[104:107], 0\n"
"v_mfma_f32_16x16x32_bf16 v[96:99], a[200:203], v[72:75], v[96:99]\n"
"v_mfma_f32_16x16x32_bf16 v[96:99], a[204:207], v[76:79], v[96:99]\n"
"v_mfma_f32_16x16x32_bf16 v[96:99], a[208:211], v[80:83], v[96:99]\n"
"s_add_u32 s58, s50, 0x180000\n"
"v_mfma_f32_16x16x32_bf16 v[96:99], a[212:215], v[84:87], v[96:99]\n"
"s_and_b32 s58, s58, 0x3ffffff\n"
"v_mfma_f32_16x16x32_bf16 v[96:99], a[216:219], v[88:91], v[96:99]\n"
"s_add_u32 s56, s40, s58\n"
"v_mfma_f32_16x16x32_bf16 v[96:99], a[220:223], v[92:95], v[96:99]\n"
"s_addc_u32 s57, s41, 0\n"
"v_mfma_f32_16x16x32_bf16 v[116:119], a[224:227], v[104:107], 0\n"
"s_waitcnt vmcnt(5)\n"
"v_cmp_ne_u64 vcc, s[48:49], v[136:137]\n"
"v_mfma_f32_16x16x32_bf16 v[116:119], a[228:231], v[72:75], v[116:119]\n"
"v_mfma_f32_16x16x32_bf16 v[116:119], a[232:235], v[76:79], v[116:119]\n"
"s_cbranch_vccnz .LH_3s\n"
".LH_3r:\n"
"v_mfma_f32_16x16x32_bf16 v[116:119], a[236:239], v[80:83], v[116:119]\n"
"s_add_u32 s50, s50, 0x180000\n"
"global_load_dwordx4 v[68:71], v5, s[56:57] offset:0 sc1\n"
"v_mfma_f32_16x16x32_bf16 v[116:119], a[240:243], v[84:87], v[116:119]\n"
"v_mfma_f32_16x16x32_bf16 v[116:119], a[244:247], v[88:91], v[116:119]\n"
"v_mfma_f32_16x16x32_bf16 v[116:119], a[248:251], v[92:95], v[116:119]\n"
"v_mfma_f32_16x16x32_bf16 v[120:123], a[252:255], v[104:107], 0\n"
"v_mfma_f32_16x16x32_bf16 v[120:123], v[24:27], v[72:75], v[120:123]\n"
"s_waitcnt lgkmcnt(0)\n"
"v_mfma_f32_16x16x32_bf16 v[120:123], v[28:31], v[76:79], v[120:123]\n"
"s_barrier\n"
"v_mfma_f32_16x16x32_bf16 v[120:123], v[32:35], v[80:83], v[120:123]\n"
"ds_read_b128 v[104:107], v2 offset:2048\n"
"ds_read_b128 v[124:127], v3 offset:512\n"
"v_mfma_f32_16x16x32_bf16 v[120:123], v[36:39], v[84:87], v[120:123]\n"
"global_load_dwordx4 v[72:75], v6, s[56:57] offset:0 sc1\n"
"ds_read_b128 v[128:131], v4 offset:3072\n"
"v_mfma_f32_16x16x32_bf16 v[120:123], v[40:43], v[88:91], v[120:123]\n"
"ds_read_b128 v[132:135], v2 offset:2304\n"
"ds_read_b128 v[136:139], v3 offset:768\n"
"v_mfma_f32_16x16x32_bf16 v[120:123], v[44:47], v[92:95], v[120:123]\n"
"global_load_dwordx4 v[76:79], v7, s[56:57] offset:0 sc1\n"
"ds_read_b128 v[140:143], v4 offset:3328\n"
"v_mfma_f32_16x16x32_bf16 v[88:91], v[108:111], a[0:3], 0\n"
"ds_read_b128 v[144:147], v2 offset:3584\n"
"ds_read_b128 v[148:151], v3 offset:1536\n"
"v_mfma_f32_16x16x32_bf16 v[88:91], v[112:115], a[4:7], v[88:91]\n"
"ds_read_b128 v[152:155], v4 offset:3584\n"
"ds_read_b128 v[156:159], v2 offset:3840\n"
"v_mfma_f32_16x16x32_bf16 v[88:91], v[48:51], a[8:11], v[88:91]\n"
"ds_read_b128 v[160:163], v3 offset:1792\n"
"ds_read_b128 v[164:167], v4 offset:3840\n"
"v_mfma_f32_16x16x32_bf16 v[88:91], v[52:55], a[12:15], v[88:91]\n"
"s_waitcnt vmcnt(5)\n"
"v_mfma_f32_16x16x32_bf16 v[88:91], v[56:59], a[16:19], v[88:91]\n"
"s_waitcnt lgkmcnt(9)\n"
"global_load_dwordx4 v[80:83], v8, s[56:57] offset:0 sc1\n"
"v_add_f32 v104, v104, v124\n"
"v_mfma_f32_16x16x32_bf16 v[88:91], v[60:63], a[20:23], v[88:91]\n"
"s_waitcnt vmcnt(5)\n"
"v_add_f32 v105, v105, v125\n"
"v_add_f32 v106, v106, v126\n"
"v_mfma_f32_16x16x32_bf16 v[88:91], v[64:67], a[24:27], v[88:91]\n"
"s_add_u32 s94, s50, 0xff000000\n"
"v_add_f32 v107, v107, v127\n"
"v_add_f32 v104, v104, v128\n"
"v_mfma_f32_16x16x32_bf16 v[168:171], v[108:111], a[28:31], 0\n"
"buffer_load_dwordx2 v[172:173], v12, s[40:43], s50 offen sc1\n"
"v_mfma_f32_16x16x32_bf16 v[168:171], v[112:115], a[32:35], v[168:171]\n"
"ds_write_addtid_b32 v88 offset:2048\n"
"v_add_f32 v105, v105, v129\n"
"v_mfma_f32_16x16x32_bf16 v[168:171], v[48:51], a[36:39], v[168:171]\n"
"ds_write_addtid_b32 v89 offset:6688\n"
"v_add_f32 v106, v106, v130\n"
"v_mfma_f32_16x16x32_bf16 v[168:171], v[52:55], a[40:43], v[168:171]\n"
"ds_write_addtid_b32 v90 offset:11392\n"
"v_add_f32 v107, v107, v131\n"
"v_mfma_f32_16x16x32_bf16 v[168:171], v[56:59], a[44:47], v[168:171]\n"
"ds_write_addtid_b32 v91 offset:16032\n"
"v_add_f32 v104, v104, v100\n"
"v_mfma_f32_16x16x32_bf16 v[168:171], v[60:63], a[48:51], v[168:171]\n"
"global_load_dwordx4 v[84:87], v9, s[56:57] offset:0 sc1\n"
"v_add_f32 v105, v105, v101\n"
"v_mfma_f32_16x16x32_bf16 v[168:171], v[64:67], a[52:55], v[168:171]\n"
"v_add_f32 v106, v106, v102\n"
"v_add_f32 v107, v107, v103\n"
"v_mfma_f32_16x16x32_bf16 v[124:127], v[108:111], a[56:59], 0\n"
"s_add_u32 s93, s50, 0xffd00000\n"
"s_and_b32 s92, s93, 0xfbffffff\n"
"v_cvt_pk_bf16_f32 v128, v104, v105\n"
"v_mfma_f32_16x16x32_bf16 v[124:127], v[112:115], a[60:63], v[124:127]\n"
"ds_write_addtid_b32 v168 offset:2304\n"
"v_cvt_pk_bf16_f32 v129, v106, v107\n"
"v_mfma_f32_16x16x32_bf16 v[124:127], v[48:51], a[64:67], v[124:127]\n"
"s_waitcnt lgkmcnt(11)\n"
"ds_write_addtid_b32 v169 offset:6944\n"
"v_add_f32 v132, v132, v136\n"
"v_mfma_f32_16x16x32_bf16 v[124:127], v[52:55], a[68:71], v[124:127]\n"
"ds_write_addtid_b32 v170 offset:11648\n"
"v_add_f32 v133, v133, v137\n"
"v_mfma_f32_16x16x32_bf16 v[124:127], v[56:59], a[72:75], v[124:127]\n"
"s_waitcnt vmcnt(6)\n"
"ds_write_addtid_b32 v171 offset:16288\n"
"global_load_dwordx4 v[88:91], v10, s[56:57] offset:0 sc1\n"
"v_mfma_f32_16x16x32_bf16 v[124:127], v[60:63], a[76:79], v[124:127]\n"
"global_load_dwordx4 v[92:95], v11, s[56:57] offset:0 sc1\n"
"v_add_f32 v134, v134, v138\n"
"v_mfma_f32_16x16x32_bf16 v[124:127], v[64:67], a[80:83], v[124:127]\n"
"v_add_f32 v135, v135, v139\n"
"v_add_f32 v132, v132, v140\n"
"v_mfma_f32_16x16x32_bf16 v[100:103], v[108:111], a[84:87], 0\n"
"v_add_f32 v133, v133, v141\n"
"v_add_f32 v134, v134, v142\n"
"v_mfma_f32_16x16x32_bf16 v[100:103], v[112:115], a[88:91], v[100:103]\n"
"ds_write_addtid_b32 v124 offset:2560\n"
"v_add_f32 v135, v135, v143\n"
"v_mfma_f32_16x16x32_bf16 v[100:103], v[48:51], a[92:95], v[100:103]\n"
"ds_write_addtid_b32 v125 offset:7200\n"
"v_add_f32 v132, v132, v96\n"
"v_mfma_f32_16x16x32_bf16 v[100:103], v[52:55], a[96:99], v[100:103]\n"
"ds_write_addtid_b32 v126 offset:11904\n"
"v_add_f32 v133, v133, v97\n"
"v_mfma_f32_16x16x32_bf16 v[100:103], v[56:59], a[100:103], v[100:103]\n"
"ds_write_addtid_b32 v127 offset:16544\n"
"v_add_f32 v134, v134, v98\n"
"v_mfma_f32_16x16x32_bf16 v[100:103], v[60:63], a[104:107], v[100:103]\n"
"v_add_f32 v135, v135, v99\n"
"v_cvt_pk_bf16_f32 v130, v132, v133\n"
"v_mfma_f32_16x16x32_bf16 v[100:103], v[64:67], a[108:111], v[100:103]\n"
"buffer_store_dwordx2 v[0:1], v14, s[40:43], s95 offen offset:0 sc1\n"
"v_cvt_pk_bf16_f32 v131, v134, v135\n"
"v_mfma_f32_16x16x32_bf16 v[96:99], v[108:111], a[112:115], 0\n"
"buffer_store_dwordx4 v[128:131], v13, s[40:43], s92 offen sc1\n"
"v_mfma_f32_16x16x32_bf16 v[96:99], v[112:115], a[116:119], v[96:99]\n"
"s_waitcnt lgkmcnt(15)\n"
"ds_write_addtid_b32 v100 offset:2816\n"
"v_add_f32 v144, v144, v148\n"
"v_mfma_f32_16x16x32_bf16 v[96:99], v[48:51], a[120:123], v[96:99]\n"
"ds_write_addtid_b32 v101 offset:7456\n"
"v_add_f32 v145, v145, v149\n"
"v_mfma_f32_16x16x32_bf16 v[96:99], v[52:55], a[124:127], v[96:99]\n"
"ds_write_addtid_b32 v102 offset:12160\n"
"v_add_f32 v146, v146, v150\n"
"v_mfma_f32_16x16x32_bf16 v[96:99], v[56:59], a[128:131], v[96:99]\n"
"ds_write_addtid_b32 v103 offset:16800\n"
"v_add_f32 v147, v147, v151\n"
"v_mfma_f32_16x16x32_bf16 v[96:99], v[60:63], a[132:135], v[96:99]\n"
"v_add_f32 v144, v144, v152\n"
"v_add_f32 v145, v145, v153\n"
"v_mfma_f32_16x16x32_bf16 v[96:99], v[64:67], a[136:139], v[96:99]\n"
"v_add_f32 v146, v146, v154\n"
"v_add_f32 v147, v147, v155\n"
"v_mfma_f32_16x16x32_bf16 v[100:103], v[108:111], a[140:143], 0\n"
"v_add_f32 v144, v144, v116\n"
"v_add_f32 v145, v145, v117\n"
"v_mfma_f32_16x16x32_bf16 v[100:103], v[112:115], a[144:147], v[100:103]\n"
"ds_write_addtid_b32 v96 offset:3072\n"
"v_add_f32 v146, v146, v118\n"
"v_mfma_f32_16x16x32_bf16 v[100:103], v[48:51], a[148:151], v[100:103]\n"
"ds_write_addtid_b32 v97 offset:7712\n"
"v_add_f32 v147, v147, v119\n"
"v_mfma_f32_16x16x32_bf16 v[100:103], v[52:55], a[152:155], v[100:103]\n"
"ds_write_addtid_b32 v98 offset:12416\n"
"v_cvt_pk_bf16_f32 v104, v144, v145\n"
"v_mfma_f32_16x16x32_bf16 v[100:103], v[56:59], a[156:159], v[100:103]\n"
"ds_write_addtid_b32 v99 offset:17056\n"
"v_cvt_pk_bf16_f32 v105, v146, v147\n"
"v_mfma_f32_16x16x32_bf16 v[100:103], v[60:63], a[160:163], v[100:103]\n"
"s_waitcnt lgkmcnt(15)\n"
"v_add_f32 v156, v156, v160\n"
"v_add_f32 v157, v157, v161\n"
"v_mfma_f32_16x16x32_bf16 v[100:103], v[64:67], a[164:167], v[100:103]\n"
"v_add_f32 v158, v158, v162\n"
"v_add_f32 v159, v159, v163\n"
"v_mfma_f32_16x16x32_bf16 v[96:99], v[108:111], a[224:227], 0\n"
"v_add_f32 v156, v156, v164\n"
"v_add_f32 v157, v157, v165\n"
"v_mfma_f32_16x16x32_bf16 v[96:99], v[112:115], a[228:231], v[96:99]\n"
"ds_write_addtid_b32 v100 offset:3328\n"
"v_add_f32 v158, v158, v166\n"
"v_mfma_f32_16x16x32_bf16 v[96:99], v[48:51], a[232:235], v[96:99]\n"
"ds_write_addtid_b32 v101 offset:7968\n"
"v_add_f32 v159, v159, v167\n"
"v_mfma_f32_16x16x32_bf16 v[96:99], v[52:55], a[236:239], v[96:99]\n"
"ds_write_addtid_b32 v102 offset:12672\n"
"v_add_f32 v156, v156, v120\n"
"v_mfma_f32_16x16x32_bf16 v[96:99], v[56:59], a[240:243], v[96:99]\n"
"ds_write_addtid_b32 v103 offset:17312\n"
"v_add_f32 v157, v157, v121\n"
"v_mfma_f32_16x16x32_bf16 v[96:99], v[60:63], a[244:247], v[96:99]\n"
"v_add_f32 v158, v158, v122\n"
"v_add_f32 v159, v159, v123\n"
"v_mfma_f32_16x16x32_bf16 v[96:99], v[64:67], a[248:251], v[96:99]\n"
"v_cvt_pk_bf16_f32 v106, v156, v157\n"
"v_cvt_pk_bf16_f32 v107, v158, v159\n"
"v_mfma_f32_16x16x32_bf16 v[100:103], v[108:111], a[252:255], 0\n"
"buffer_store_dwordx4 v[104:107], v15, s[40:43], s92 offen sc1\n"
"v_mfma_f32_16x16x32_bf16 v[100:103], v[112:115], v[24:27], v[100:103]\n"
"s_cmp_lt_u32 s94, 0x8000000\n"
"s_cselect_b32 s94, 0x80000000, s94\n"
"ds_write_addtid_b32 v96 offset:3584\n"
"v_mfma_f32_16x16x32_bf16 v[100:103], v[48:51], v[28:31], v[100:103]\n"
"ds_write_addtid_b32 v97 offset:8224\n"
"v_mfma_f32_16x16x32_bf16 v[100:103], v[52:55], v[32:35], v[100:103]\n"
"ds_write_addtid_b32 v98 offset:12928\n"
"v_mfma_f32_16x16x32_bf16 v[100:103], v[56:59], v[36:39], v[100:103]\n"
"ds_write_addtid_b32 v99 offset:17568\n"
"v_mfma_f32_16x16x32_bf16 v[100:103], v[60:63], v[40:43], v[100:103]\n"
"buffer_load_dwordx2 v[136:137], v17, s[40:43], s94 offen offset:0 sc1\n"
"v_mfma_f32_16x16x32_bf16 v[100:103], v[64:67], v[44:47], v[100:103]\n"
"s_add_u32 s58, s50, 0x180000\n"
"v_mfma_f32_16x16x32_bf16 v[104:107], a[168:171], v[108:111], 0\n"
"s_and_b32 s58, s58, 0x3ffffff\n"
"v_mfma_f32_16x16x32_bf16 v[104:107], a[172:175], v[112:115], v[104:107]\n"
"s_add_u32 s56, s40, s58\n"
"ds_write_addtid_b32 v100 offset:3840\n"
"v_mfma_f32_16x16x32_bf16 v[104:107], a[176:179], v[48:51], v[104:107]\n"
"s_addc_u32 s57, s41, 0\n"
"ds_write_addtid_b32 v101 offset:8480\n"
"v_mfma_f32_16x16x32_bf16 v[104:107], a[180:183], v[52:55], v[104:107]\n"
"s_waitcnt vmcnt(7)\n"
"ds_write_addtid_b32 v102 offset:13184\n"
"v_cmp_ne_u64 vcc, s[48:49], v[172:173]\n"
"v_mfma_f32_16x16x32_bf16 v[104:107], a[184:187], v[56:59], v[104:107]\n"
"ds_write_addtid_b32 v103 offset:17824\n"
"v_mfma_f32_16x16x32_bf16 v[104:107], a[188:191], v[60:63], v[104:107]\n"
"s_cbranch_vccnz .LH_4s\n"
".LH_4r:\n"
"v_mfma_f32_16x16x32_bf16 v[104:107], a[192:195], v[64:67], v[104:107]\n"
"s_add_u32 s50, s50, 0x180000\n"
"global_load_dwordx4 v[96:99], v5, s[56:57] offset:0 sc1\n"
"v_mfma_f32_16x16x32_bf16 v[116:119], a[196:199], v[108:111], 0\n"
"global_load_dwordx4 v[100:103], v6, s[56:57] offset:0 sc1\n"
"v_mfma_f32_16x16x32_bf16 v[116:119], a[200:203], v[112:115], v[116:119]\n"
"s_waitcnt lgkmcnt(0)\n"
"v_mfma_f32_16x16x32_bf16 v[116:119], a[204:207], v[48:51], v[116:119]\n"
"s_barrier\n"
"v_mfma_f32_16x16x32_bf16 v[116:119], a[208:211], v[52:55], v[116:119]\n"
"ds_read_b128 v[108:111], v2 offset:0\n"
"ds_read_b128 v[112:115], v3 offset:2560\n"
"v_mfma_f32_16x16x32_bf16 v[116:119], a[212:215], v[56:59], v[116:119]\n"
"ds_read_b128 v[120:123], v4 offset:1024\n"
"ds_read_b128 v[124:127], v2 offset:256\n"
"v_mfma_f32_16x16x32_bf16 v[116:119], a[216:219], v[60:63], v[116:119]\n"
"ds_read_b128 v[128:131], v3 offset:2816\n"
"ds_read_b128 v[132:135], v4 offset:1280\n"
"v_mfma_f32_16x16x32_bf16 v[116:119], a[220:223], v[64:67], v[116:119]\n"
"v_mfma_f32_16x16x32_bf16 v[56:59], v[68:71], a[0:3], 0\n"
"v_mfma_f32_16x16x32_bf16 v[56:59], v[72:75], a[4:7], v[56:59]\n"
"v_mfma_f32_16x16x32_bf16 v[56:59], v[76:79], a[8:11], v[56:59]\n"
"global_load_dwordx4 v[48:51], v7, s[56:57] offset:0 sc1\n"
"v_mfma_f32_16x16x32_bf16 v[56:59], v[80:83], a[12:15], v[56:59]\n"
"s_waitcnt vmcnt(8)\n"
"buffer_load_dwordx2 v[138:139], v12, s[40:43], s50 offen sc1\n"
"v_mfma_f32_16x16x32_bf16 v[56:59], v[84:87], a[16:19], v[56:59]\n"
"s_waitcnt lgkmcnt(3)\n"
"v_add_f32 v108, v108, v112\n"
"v_add_f32 v109, v109, v113\n"
"v_mfma_f32_16x16x32_bf16 v[56:59], v[88:91], a[20:23], v[56:59]\n"
"s_waitcnt vmcnt(8)\n"
"v_add_f32 v110, v110, v114\n"
"v_add_f32 v111, v111, v115\n"
"v_mfma_f32_16x16x32_bf16 v[56:59], v[92:95], a[24:27], v[56:59]\n"
"v_add_f32 v108, v108, v120\n"
"v_add_f32 v109, v109, v121\n"
"v_mfma_f32_16x16x32_bf16 v[64:67], v[68:71], a[28:31], 0\n"
"global_load_dwordx4 v[52:55], v8, s[56:57] offset:0 sc1\n"
"v_add_f32 v110, v110, v122\n"
"v_mfma_f32_16x16x32_bf16 v[64:67], v[72:75], a[32:35], v[64:67]\n"
"ds_write_addtid_b32 v56 offset:0\n"
"v_add_f32 v111, v111, v123\n"
"v_mfma_f32_16x16x32_bf16 v[64:67], v[76:79], a[36:39], v[64:67]\n"
"ds_write_addtid_b32 v57 offset:4640\n"
"v_add_f32 v108, v108, v104\n"
"v_mfma_f32_16x16x32_bf16 v[64:67], v[80:83], a[40:43], v[64:67]\n"
"ds_write_addtid_b32 v58 offset:9344\n"
"v_add_f32 v109, v109, v105\n"
"v_mfma_f32_16x16x32_bf16 v[64:67], v[84:87], a[44:47], v[64:67]\n"
"ds_write_addtid_b32 v59 offset:13984\n"
"v_add_f32 v110, v110, v106\n"
"v_mfma_f32_16x16x32_bf16 v[64:67], v[88:91], a[48:51], v[64:67]\n"
"v_add_f32 v111, v111, v107\n"
"v_mfma_f32_16x16x32_bf16 v[64:67], v[92:95], a[52:55], v[64:67]\n"
"s_add_u32 s92, s50, 0xffd00000\n"
"s_and_b32 s95, s92, 0xfbffffff\n"
"global_load_dwordx4 v[56:59], v9, s[56:57] offset:0 sc1\n"
"v_mfma_f32_16x16x32_bf16 v[104:107], v[68:71], a[56:59], 0\n"
"v_cvt_pk_bf16_f32 v112, v108, v109\n"
"v_cvt_pk_bf16_f32 v113, v110, v111\n"
"v_mfma_f32_16x16x32_bf16 v[104:107], v[72:75], a[60:63], v[104:107]\n"
"s_waitcnt lgkmcnt(4)\n"
"ds_write_addtid_b32 v64 offset:256\n"
"v_add_f32 v124, v124, v128\n"
"v_mfma_f32_16x16x32_bf16 v[104:107], v[76:79], a[64:67], v[104:107]\n"
"ds_write_addtid_b32 v65 offset:4896\n"
"v_add_f32 v125, v125, v129\n"
"v_mfma_f32_16x16x32_bf16 v[104:107], v[80:83], a[68:71], v[104:107]\n"
"ds_write_addtid_b32 v66 offset:9600\n"
"global_load_dwordx4 v[60:63], v10, s[56:57] offset:0 sc1\n"
"v_mfma_f32_16x16x32_bf16 v[104:107], v[84:87], a[72:75], v[104:107]\n"
"ds_write_addtid_b32 v67 offset:14240\n"
"v_add_f32 v126, v126, v130\n"
"v_mfma_f32_16x16x32_bf16 v[104:107], v[88:91], a[76:79], v[104:107]\n"
"v_add_f32 v127, v127, v131\n"
"v_add_f32 v124, v124, v132\n"
"v_mfma_f32_16x16x32_bf16 v[104:107], v[92:95], a[80:83], v[104:107]\n"
"v_add_f32 v125, v125, v133\n"
"v_add_f32 v126, v126, v134\n"
"v_mfma_f32_16x16x32_bf16 v[108:111], v[68:71], a[84:87], 0\n"
"v_add_f32 v127, v127, v135\n"
"v_add_f32 v124, v124, v116\n"
"v_mfma_f32_16x16x32_bf16 v[108:111], v[72:75], a[88:91], v[108:111]\n"
"ds_write_addtid_b32 v104 offset:512\n"
"v_add_f32 v125, v125, v117\n"
"v_mfma_f32_16x16x32_bf16 v[108:111], v[76:79], a[92:95], v[108:111]\n"
"ds_write_addtid_b32 v105 offset:5152\n"
"global_load_dwordx4 v[64:67], v11, s[56:57] offset:0 sc1\n"
"v_mfma_f32_16x16x32_bf16 v[108:111], v[80:83], a[96:99], v[108:111]\n"
"ds_write_addtid_b32 v106 offset:9856\n"
"v_add_f32 v126, v126, v118\n"
"v_mfma_f32_16x16x32_bf16 v[108:111], v[84:87], a[100:103], v[108:111]\n"
"ds_write_addtid_b32 v107 offset:14496\n"
"v_add_f32 v127, v127, v119\n"
"v_mfma_f32_16x16x32_bf16 v[108:111], v[88:91], a[104:107], v[108:111]\n"
"v_cvt_pk_bf16_f32 v114, v124, v125\n"
"v_cvt_pk_bf16_f32 v115, v126, v127\n"
"v_mfma_f32_16x16x32_bf16 v[108:111], v[92:95], a[108:111], v[108:111]\n"
"s_waitcnt vmcnt(9)\n"
"buffer_store_dwordx2 v[0:1], v16, s[40:43], s93 offen offset:0 sc1\n"
"v_mfma_f32_16x16x32_bf16 v[104:107], v[68:71], a[112:115], 0\n"
"v_mfma_f32_16x16x32_bf16 v[104:107], v[72:75], a[116:119], v[104:107]\n"
"ds_write_addtid_b32 v108 offset:768\n"
"v_mfma_f32_16x16x32_bf16 v[104:107], v[76:79], a[120:123], v[104:107]\n"
"buffer_store_dwordx4 v[112:115], v13, s[40:43], s95 offen sc1\n"
"ds_write_addtid_b32 v109 offset:5408\n"
"v_mfma_f32_16x16x32_bf16 v[104:107], v[80:83], a[124:127], v[104:107]\n"
"ds_write_addtid_b32 v110 offset:10112\n"
"v_mfma_f32_16x16x32_bf16 v[104:107], v[84:87], a[128:131], v[104:107]\n"
"s_waitcnt vmcnt(10)\n"
"ds_write_addtid_b32 v111 offset:14752\n"
"v_cmp_ne_u64 vcc, s[64:65], v[136:137]\n"
"v_mfma_f32_16x16x32_bf16 v[104:107], v[88:91], a[132:135], v[104:107]\n"
"v_mfma_f32_16x16x32_bf16 v[104:107], v[92:95], a[136:139], v[104:107]\n"
"s_cbranch_vccnz .LH_5s\n"
".LH_5r:\n"
"v_mfma_f32_16x16x32_bf16 v[108:111], v[68:71], a[140:143], 0\n"
"buffer_load_dwordx4 v[112:115], v18, s[40:43], s94 offen offset:0 sc1\n"
"v_mfma_f32_16x16x32_bf16 v[108:111], v[72:75], a[144:147], v[108:111]\n"
"ds_write_addtid_b32 v104 offset:1024\n"
"v_mfma_f32_16x16x32_bf16 v[108:111], v[76:79], a[148:151], v[108:111]\n"
"ds_write_addtid_b32 v105 offset:5664\n"
"v_mfma_f32_16x16x32_bf16 v[108:111], v[80:83], a[152:155], v[108:111]\n"
"ds_write_addtid_b32 v106 offset:10368\n"
"v_mfma_f32_16x16x32_bf16 v[108:111], v[84:87], a[156:159], v[108:111]\n"
"ds_write_addtid_b32 v107 offset:15008\n"
"v_mfma_f32_16x16x32_bf16 v[108:111], v[88:91], a[160:163], v[108:111]\n"
"buffer_load_dwordx4 v[116:119], v18, s[40:43], s94 offen offset:1024 sc1\n"
"v_mfma_f32_16x16x32_bf16 v[108:111], v[92:95], a[164:167], v[108:111]\n"
"v_mfma_f32_16x16x32_bf16 v[104:107], v[68:71], a[224:227], 0\n"
"v_mfma_f32_16x16x32_bf16 v[104:107], v[72:75], a[228:231], v[104:107]\n"
"ds_write_addtid_b32 v108 offset:1280\n"
"v_mfma_f32_16x16x32_bf16 v[104:107], v[76:79], a[232:235], v[104:107]\n"
"ds_write_addtid_b32 v109 offset:5920\n"
"v_mfma_f32_16x16x32_bf16 v[104:107], v[80:83], a[236:239], v[104:107]\n"
"ds_write_addtid_b32 v110 offset:10624\n"
"v_mfma_f32_16x16x32_bf16 v[104:107], v[84:87], a[240:243], v[104:107]\n"
"ds_write_addtid_b32 v111 offset:15264\n"
"v_mfma_f32_16x16x32_bf16 v[104:107], v[88:91], a[244:247], v[104:107]\n"
"v_mfma_f32_16x16x32_bf16 v[104:107], v[92:95], a[248:251], v[104:107]\n"
"v_mfma_f32_16x16x32_bf16 v[120:123], v[68:71], a[252:255], 0\n"
"v_mfma_f32_16x16x32_bf16 v[120:123], v[72:75], v[24:27], v[120:123]\n"
"s_add_u32 s58, s50, 0x180000\n"
"ds_write_addtid_b32 v104 offset:1536\n"
"v_mfma_f32_16x16x32_bf16 v[120:123], v[76:79], v[28:31], v[120:123]\n"
"s_and_b32 s58, s58, 0x3ffffff\n"
"ds_write_addtid_b32 v105 offset:6176\n"
"v_mfma_f32_16x16x32_bf16 v[120:123], v[80:83], v[32:35], v[120:123]\n"
"s_add_u32 s56, s40, s58\n"
"ds_write_addtid_b32 v106 offset:10880\n"
"v_mfma_f32_16x16x32_bf16 v[120:123], v[84:87], v[36:39], v[120:123]\n"
"s_addc_u32 s57, s41, 0\n"
"ds_write_addtid_b32 v107 offset:15520\n"
"v_mfma_f32_16x16x32_bf16 v[120:123], v[88:91], v[40:43], v[120:123]\n"
"s_waitcnt vmcnt(8)\n"
"v_cmp_ne_u64 vcc, s[48:49], v[138:139]\n"
"v_mfma_f32_16x16x32_bf16 v[120:123], v[92:95], v[44:47], v[120:123]\n"
"v_mfma_f32_16x16x32_bf16 v[108:111], a[168:171], v[68:71], 0\n"
"s_cbranch_vccnz .LH_1s\n"
".LH_1r:\n"
"v_mfma_f32_16x16x32_bf16 v[108:111], a[172:175], v[72:75], v[108:111]\n"
"s_add_u32 s50, s50, 0x180000\n"
"ds_write_addtid_b32 v120 offset:1792\n"
"v_mfma_f32_16x16x32_bf16 v[108:111], a[176:179], v[76:79], v[108:111]\n"
"ds_write_addtid_b32 v121 offset:6432\n"
"v_mfma_f32_16x16x32_bf16 v[108:111], a[180:183], v[80:83], v[108:111]\n"
"ds_write_addtid_b32 v122 offset:11136\n"
"v_mfma_f32_16x16x32_bf16 v[108:111], a[184:187], v[84:87], v[108:111]\n"
"ds_write_addtid_b32 v123 offset:15776\n"
"global_load_dwordx4 v[104:107], v5, s[56:57] offset:0 sc1\n"
"v_mfma_f32_16x16x32_bf16 v[108:111], a[188:191], v[88:91], v[108:111]\n"
"v_mfma_f32_16x16x32_bf16 v[108:111], a[192:195], v[92:95], v[108:111]\n"
"v_mfma_f32_16x16x32_bf16 v[120:123], a[196:199], v[68:71], 0\n"
"v_mfma_f32_16x16x32_bf16 v[120:123], a[200:203], v[72:75], v[120:123]\n"
"s_waitcnt lgkmcnt(0)\n"
"v_mfma_f32_16x16x32_bf16 v[120:123], a[204:207], v[76:79], v[120:123]\n"
"s_barrier\n"
"v_mfma_f32_16x16x32_bf16 v[120:123], a[208:211], v[80:83], v[120:123]\n"
"global_load_dwordx4 v[72:75], v6, s[56:57] offset:0 sc1\n"
"ds_read_b128 v[68:71], v2 offset:2048\n"
"v_mfma_f32_16x16x32_bf16 v[120:123], a[212:215], v[84:87], v[120:123]\n"
"ds_read_b128 v[124:127], v3 offset:512\n"
"ds_read_b128 v[128:131], v4 offset:3072\n"
"v_mfma_f32_16x16x32_bf16 v[120:123], a[216:219], v[88:91], v[120:123]\n"
"ds_read_b128 v[132:135], v2 offset:2304\n"
"ds_read_b128 v[136:139], v3 offset:768\n"
"v_mfma_f32_16x16x32_bf16 v[120:123], a[220:223], v[92:95], v[120:123]\n"
"ds_read_b128 v[140:143], v4 offset:3328\n"
"v_mfma_f32_16x16x32_bf16 v[88:91], v[96:99], a[0:3], 0\n"
"global_load_dwordx4 v[76:79], v7, s[56:57] offset:0 sc1\n"
"v_mfma_f32_16x16x32_bf16 v[88:91], v[100:103], a[4:7], v[88:91]\n"
"s_waitcnt vmcnt(10)\n"
"v_mfma_f32_16x16x32_bf16 v[88:91], v[48:51], a[8:11], v[88:91]\n"
"v_mfma_f32_16x16x32_bf16 v[88:91], v[52:55], a[12:15], v[88:91]\n"
"s_waitcnt vmcnt(8)\n"
"v_mfma_f32_16x16x32_bf16 v[88:91], v[56:59], a[16:19], v[88:91]\n"
"s_waitcnt lgkmcnt(3)\n"
"v_add_f32 v68, v68, v124\n"
"v_add_f32 v69, v69, v125\n"
"v_mfma_f32_16x16x32_bf16 v[88:91], v[60:63], a[20:23], v[88:91]\n"
"s_waitcnt vmcnt(7)\n"
"global_load_dwordx4 v[80:83], v8, s[56:57] offset:0 sc1\n"
"v_add_f32 v70, v70, v126\n"
"v_mfma_f32_16x16x32_bf16 v[88:91], v[64:67], a[24:27], v[88:91]\n"
"buffer_load_dwordx2 v[144:145], v12, s[40:43], s50 offen sc1\n"
"v_mfma_f32_16x16x32_bf16 v[92:95], v[96:99], a[28:31], 0\n"
"v_add_f32 v71, v71, v127\n"
"v_add_f32 v68, v68, v128\n"
"v_mfma_f32_16x16x32_bf16 v[92:95], v[100:103], a[32:35], v[92:95]\n"
"s_waitcnt vmcnt(7)\n"
"buffer_store_dwordx2 v[0:1], v14, s[40:43], s92 offen offset:0 sc1\n"
"ds_write_addtid_b32 v88 offset:2048\n"
"v_mfma_f32_16x16x32_bf16 v[92:95], v[48:51], a[36:39], v[92:95]\n"
"ds_write_addtid_b32 v89 offset:6688\n"
"v_add_f32 v69, v69, v129\n"
"v_mfma_f32_16x16x32_bf16 v[92:95], v[52:55], a[40:43], v[92:95]\n"
"ds_write_addtid_b32 v90 offset:11392\n"
"global_load_dwordx4 v[84:87], v9, s[56:57] offset:0 sc1\n"
"v_mfma_f32_16x16x32_bf16 v[92:95], v[56:59], a[44:47], v[92:95]\n"
"ds_write_addtid_b32 v91 offset:16032\n"
"v_add_f32 v70, v70, v130\n"
"v_mfma_f32_16x16x32_bf16 v[92:95], v[60:63], a[48:51], v[92:95]\n"
"v_add_f32 v71, v71, v131\n"
"v_add_f32 v68, v68, v108\n"
"v_mfma_f32_16x16x32_bf16 v[92:95], v[64:67], a[52:55], v[92:95]\n"
"v_add_f32 v69, v69, v109\n"
"v_add_f32 v70, v70, v110\n"
"v_mfma_f32_16x16x32_bf16 v[124:127], v[96:99], a[56:59], 0\n"
"v_add_f32 v71, v71, v111\n"
"v_mfma_f32_16x16x32_bf16 v[124:127], v[100:103], a[60:63], v[124:127]\n"
"ds_write_addtid_b32 v92 offset:2304\n"
"global_load_dwordx4 v[88:91], v10, s[56:57] offset:0 sc1\n"
"v_mfma_f32_16x16x32_bf16 v[124:127], v[48:51], a[64:67], v[124:127]\n"
"s_add_u32 s93, s50, 0xffd00000\n"
"s_and_b32 s95, s93, 0xfbffffff\n"
"ds_write_addtid_b32 v93 offset:6944\n"
"v_mfma_f32_16x16x32_bf16 v[124:127], v[52:55], a[68:71], v[124:127]\n"
"ds_write_addtid_b32 v94 offset:11648\n"
"v_cvt_pk_bf16_f32 v108, v68, v69\n"
"v_mfma_f32_16x16x32_bf16 v[124:127], v[56:59], a[72:75], v[124:127]\n"
"s_waitcnt vmcnt(8)\n"
"ds_write_addtid_b32 v95 offset:16288\n"
"v_cvt_pk_bf16_f32 v109, v70, v71\n"
"v_mfma_f32_16x16x32_bf16 v[124:127], v[60:63], a[76:79], v[124:127]\n"
"s_waitcnt lgkmcnt(8)\n"
"v_add_f32 v132, v132, v136\n"
"v_add_f32 v133, v133, v137\n"
"v_mfma_f32_16x16x32_bf16 v[124:127], v[64:67], a[80:83], v[124:127]\n"
"global_load_dwordx4 v[92:95], v11, s[56:57] offset:0 sc1\n"
"v_add_f32 v134, v134, v138\n"
"v_mfma_f32_16x16x32_bf16 v[68:71], v[96:99], a[84:87], 0\n"
"v_add_f32 v135, v135, v139\n"
"v_add_f32 v132, v132, v140\n"
"v_mfma_f32_16x16x32_bf16 v[68:71], v[100:103], a[88:91], v[68:71]\n"
"ds_write_addtid_b32 v124 offset:2560\n"
"v_add_f32 v133, v133, v141\n"
"v_mfma_f32_16x16x32_bf16 v[68:71], v[48:51], a[92:95], v[68:71]\n"
"ds_write_addtid_b32 v125 offset:7200\n"
"v_add_f32 v134, v134, v142\n"
"v_mfma_f32_16x16x32_bf16 v[68:71], v[52:55], a[96:99], v[68:71]\n"
"ds_write_addtid_b32 v126 offset:11904\n"
"v_add_f32 v135, v135, v143\n"
"v_mfma_f32_16x16x32_bf16 v[68:71], v[56:59], a[100:103], v[68:71]\n"
"ds_write_addtid_b32 v127 offset:16544\n"
"v_add_f32 v132, v132, v120\n"
"v_mfma_f32_16x16x32_bf16 v[68:71], v[60:63], a[104:107], v[68:71]\n"
"v_add_f32 v133, v133, v121\n"
"v_add_f32 v134, v134, v122\n"
"v_mfma_f32_16x16x32_bf16 v[68:71], v[64:67], a[108:111], v[68:71]\n"
"v_add_f32 v135, v135, v123\n"
"v_cvt_pk_bf16_f32 v110, v132, v133\n"
"v_mfma_f32_16x16x32_bf16 v[124:127], v[96:99], a[112:115], 0\n"
"v_cvt_pk_bf16_f32 v111, v134, v135\n"
"v_add_f32 v112, v112, v116\n"
"v_mfma_f32_16x16x32_bf16 v[124:127], v[100:103], a[116:119], v[124:127]\n"
"ds_write_addtid_b32 v68 offset:2816\n"
"v_add_f32 v113, v113, v117\n"
"v_mfma_f32_16x16x32_bf16 v[124:127], v[48:51], a[120:123], v[124:127]\n"
"ds_write_addtid_b32 v69 offset:7456\n"
"v_add_f32 v114, v114, v118\n"
"v_mfma_f32_16x16x32_bf16 v[124:127], v[52:55], a[124:127], v[124:127]\n"
"ds_write_addtid_b32 v70 offset:12160\n"
"v_add_f32 v115, v115, v119\n"
"v_mfma_f32_16x16x32_bf16 v[124:127], v[56:59], a[128:131], v[124:127]\n"
"ds_write_addtid_b32 v71 offset:16800\n"
"v_mfma_f32_16x16x32_bf16 v[124:127], v[60:63], a[132:135], v[124:127]\n"
"v_permlane32_swap_b32 v112, v114\n"
"v_mfma_f32_16x16x32_bf16 v[124:127], v[64:67], a[136:139], v[124:127]\n"
"v_permlane32_swap_b32 v113, v115\n"
"v_mfma_f32_16x16x32_bf16 v[68:71], v[96:99], a[140:143], 0\n"
"v_add_f32 v112, v112, v114\n"
"v_add_f32 v113, v113, v115\n"
"v_mfma_f32_16x16x32_bf16 v[68:71], v[100:103], a[144:147], v[68:71]\n"
"buffer_store_dwordx4 v[108:111], v13, s[40:43], s95 offen sc1\n"
"ds_write_addtid_b32 v124 offset:3072\n"
"v_permlane16_swap_b32 v112, v113\n"
"v_mfma_f32_16x16x32_bf16 v[68:71], v[48:51], a[148:151], v[68:71]\n"
"ds_write_addtid_b32 v125 offset:7712\n"
"v_add_f32 v112, v112, v113\n"
"v_mfma_f32_16x16x32_bf16 v[68:71], v[52:55], a[152:155], v[68:71]\n"
"ds_write_addtid_b32 v126 offset:12416\n"
"v_add_f32_dpp v113, v112, v112 row_ror:8 row_mask:0xf bank_mask:0xf\n"
"v_mfma_f32_16x16x32_bf16 v[68:71], v[56:59], a[156:159], v[68:71]\n"
"ds_write_addtid_b32 v127 offset:17056\n"
"v_mov_b32 v112, v113\n"
"v_mfma_f32_16x16x32_bf16 v[68:71], v[60:63], a[160:163], v[68:71]\n"
"v_permlane16_swap_b32 v113, v112\n"
"v_mfma_f32_16x16x32_bf16 v[68:71], v[64:67], a[164:167], v[68:71]\n"
"v_mul_f32 v113, 0xba800000, v113\n"
"v_fma_f32 v112, v112, s62, v20\n"
"v_mfma_f32_16x16x32_bf16 v[108:111], v[96:99], a[224:227], 0\n"
"v_fma_f32 v112, -v113, v113, v112\n"
"v_max_f32 v112, v20, v112\n"
"v_mfma_f32_16x16x32_bf16 v[108:111], v[100:103], a[228:231], v[108:111]\n"
"ds_write_addtid_b32 v68 offset:3328\n"
"v_mfma_f32_16x16x32_bf16 v[108:111], v[48:51], a[232:235], v[108:111]\n"
"ds_write_addtid_b32 v69 offset:7968\n"
"v_mfma_f32_16x16x32_bf16 v[108:111], v[52:55], a[236:239], v[108:111]\n"
"ds_write_addtid_b32 v70 offset:12672\n"
"v_mfma_f32_16x16x32_bf16 v[108:111], v[56:59], a[240:243], v[108:111]\n"
"ds_write_addtid_b32 v71 offset:17312\n"
"v_mfma_f32_16x16x32_bf16 v[108:111], v[60:63], a[244:247], v[108:111]\n"
"v_rsq_f32 v112, v112\n"
"v_mfma_f32_16x16x32_bf16 v[108:111], v[64:67], a[248:251], v[108:111]\n"
"v_mov_b32 v114, v0\n"
"v_mov_b32 v115, v1\n"
"v_mfma_f32_16x16x32_bf16 v[68:71], v[96:99], a[252:255], 0\n"
"buffer_store_dwordx4 v[112:115], v19, s[40:43], s94 offen sc1\n"
"v_mfma_f32_16x16x32_bf16 v[68:71], v[100:103], v[24:27], v[68:71]\n"
"s_add_u32 s58, s50, 0x180000\n"
"ds_write_addtid_b32 v108 offset:3584\n"
"v_mfma_f32_16x16x32_bf16 v[68:71], v[48:51], v[28:31], v[68:71]\n"
"s_and_b32 s58, s58, 0x3ffffff\n"
"ds_write_addtid_b32 v109 offset:8224\n"
"v_mfma_f32_16x16x32_bf16 v[68:71], v[52:55], v[32:35], v[68:71]\n"
"s_add_u32 s56, s40, s58\n"
"ds_write_addtid_b32 v110 offset:12928\n"
"v_mfma_f32_16x16x32_bf16 v[68:71], v[56:59], v[36:39], v[68:71]\n"
"s_addc_u32 s57, s41, 0\n"
"ds_write_addtid_b32 v111 offset:17568\n"
"v_mfma_f32_16x16x32_bf16 v[68:71], v[60:63], v[40:43], v[68:71]\n"
"s_waitcnt vmcnt(6)\n"
"v_cmp_ne_u64 vcc, s[48:49], v[144:145]\n"
"v_mfma_f32_16x16x32_bf16 v[68:71], v[64:67], v[44:47], v[68:71]\n"
"v_mfma_f32_16x16x32_bf16 v[116:119], a[168:171], v[96:99], 0\n"
"s_cbranch_vccnz .LH_2s\n"
".LH_2r:\n"
"v_mfma_f32_16x16x32_bf16 v[116:119], a[172:175], v[100:103], v[116:119]\n"
"s_add_u32 s50, s50, 0x180000\n"
"ds_write_addtid_b32 v68 offset:3840\n"
"v_mfma_f32_16x16x32_bf16 v[116:119], a[176:179], v[48:51], v[116:119]\n"
"ds_write_addtid_b32 v69 offset:8480\n"
"v_mfma_f32_16x16x32_bf16 v[116:119], a[180:183], v[52:55], v[116:119]\n"
"ds_write_addtid_b32 v70 offset:13184\n"
"v_mfma_f32_16x16x32_bf16 v[116:119], a[184:187], v[56:59], v[116:119]\n"
"ds_write_addtid_b32 v71 offset:17824\n"
"v_mfma_f32_16x16x32_bf16 v[116:119], a[188:191], v[60:63], v[116:119]\n"
"global_load_dwordx4 v[108:111], v5, s[56:57] offset:0 sc1\n"
"v_mfma_f32_16x16x32_bf16 v[116:119], a[192:195], v[64:67], v[116:119]\n"
"v_mfma_f32_16x16x32_bf16 v[120:123], a[196:199], v[96:99], 0\n"
"v_mfma_f32_16x16x32_bf16 v[120:123], a[200:203], v[100:103], v[120:123]\n"
"s_waitcnt lgkmcnt(0)\n"
"global_load_dwordx4 v[112:115], v6, s[56:57] offset:0 sc1\n"
"v_mfma_f32_16x16x32_bf16 v[120:123], a[204:207], v[48:51], v[120:123]\n"
"s_barrier\n"
"v_mfma_f32_16x16x32_bf16 v[120:123], a[208:211], v[52:55], v[120:123]\n"
"ds_read_b128 v[100:103], v2 offset:0\n"
"ds_read_b128 v[68:71], v3 offset:2560\n"
"v_mfma_f32_16x16x32_bf16 v[120:123], a[212:215], v[56:59], v[120:123]\n"
"ds_read_b128 v[96:99], v4 offset:1024\n"
"ds_read_b128 v[124:127], v2 offset:256\n"
"v_mfma_f32_16x16x32_bf16 v[120:123], a[216:219], v[60:63], v[120:123]\n"
"ds_read_b128 v[128:131], v3 offset:2816\n"
"ds_read_b128 v[132:135], v4 offset:1280\n"
"v_mfma_f32_16x16x32_bf16 v[120:123], a[220:223], v[64:67], v[120:123]\n"
"s_branch .LH_top\n"
".LH_1s:\n"
"s_cmp_ge_u32 s50, s53\n"
"s_cbranch_scc1 .LH_end\n"
"s_cmp_ge_u32 s50, s52\n"
"s_cbranch_scc1 .LH_1r\n"
"s_sub_u32 s60, s50, 0x0\n"
"s_mov_b32 s61, 200000\n"
"s_nop 7\n"
".LH_1p:\n"
"buffer_load_dwordx2 v[138:139], v12, s[40:43], s60 offen sc1\n"
"s_waitcnt vmcnt(0)\n"
"v_cmp_ne_u64 vcc, s[48:49], v[138:139]\n"
"s_cbranch_vccz .LH_1px\n"
"s_sleep 1\n"
"s_sub_u32 s61, s61, 1\n"
"s_cbranch_scc0 .LH_1p\n"
"s_branch .LH_end\n"
".LH_1px:\n"
"s_branch .LH_1r\n"
".LH_2s:\n"
"s_cmp_ge_u32 s50, s53\n"
"s_cbranch_scc1 .LH_end\n"
"s_cmp_ge_u32 s50, s52\n"
"s_cbranch_scc1 .LH_2r\n"
"s_sub_u32 s60, s50, 0x0\n"
"s_mov_b32 s61, 200000\n"
"s_nop 7\n"
".LH_2p:\n"
"buffer_load_dwordx2 v[144:145], v12, s[40:43], s60 offen sc1\n"
"s_waitcnt vmcnt(0)\n"
"v_cmp_ne_u64 vcc, s[48:49], v[144:145]\n"
"s_cbranch_vccz .LH_2px\n"
"s_sleep 1\n"
"s_sub_u32 s61, s61, 1\n"
"s_cbranch_scc0 .LH_2p\n"
"s_branch .LH_end\n"
".LH_2px:\n"
"s_branch .LH_2r\n"
".LH_3s:\n"
"s_cmp_ge_u32 s50, s53\n"
"s_cbranch_scc1 .LH_end\n"
"s_cmp_ge_u32 s50, s52\n"
"s_cbranch_scc1 .LH_3r\n"
"s_sub_u32 s60, s50, 0x0\n"
"s_mov_b32 s61, 200000\n"
"s_nop 7\n"
".LH_3p:\n"
"buffer_load_dwordx2 v[136:137], v12, s[40:43], s60 offen sc1\n"
"s_waitcnt vmcnt(0)\n"
"v_cmp_ne_u64 vcc, s[48:49], v[136:137]\n"
"s_cbranch_vccz .LH_3px\n"
"s_sleep 1\n"
"s_sub_u32 s61, s61, 1\n"
"s_cbranch_scc0 .LH_3p\n"
"s_branch .LH_end\n"
".LH_3px:\n"
"s_branch .LH_3r\n"
".LH_4s:\n"
"s_cmp_ge_u32 s50, s53\n"
"s_cbranch_scc1 .LH_end\n"
"s_cmp_ge_u32 s50, s52\n"
"s_cbranch_scc1 .LH_4r\n"
"s_sub_u32 s60, s50, 0x0\n"
"s_mov_b32 s61, 200000\n"
"s_nop 7\n"
".LH_4p:\n"
"buffer_load_dwordx2 v[172:173], v12, s[40:43], s60 offen sc1\n"
"s_waitcnt vmcnt(0)\n"
"v_cmp_ne_u64 vcc, s[48:49], v[172:173]\n"
"s_cbranch_vccz .LH_4px\n"
"s_sleep 1\n"
"s_sub_u32 s61, s61, 1\n"
"s_cbranch_scc0 .LH_4p\n"
"s_branch .LH_end\n"
".LH_4px:\n"
"s_branch .LH_4r\n"
".LH_5s:\n"
"s_cmp_ge_i32 s94, 0xc000000\n"
"s_cselect_b64 s[64:65], 0, s[64:65]\n"
"s_cmp_ge_u32 s94, 0xc000000\n"
"s_cbranch_scc1 .LH_5r\n"
"s_mov_b32 s61, 200000\n"
"s_nop 7\n"
".LH_5p:\n"
"buffer_load_dwordx2 v[136:137], v17, s[40:43], s94 offen offset:0 sc1\n"
"s_waitcnt vmcnt(0)\n"
"v_cmp_ne_u64 vcc, s[64:65], v[136:137]\n"
"s_cbranch_vccz .LH_5px\n"
"s_sleep 1\n"
"s_sub_u32 s61, s61, 1\n"
"s_cbranch_scc0 .LH_5p\n"
"s_branch .LH_end\n"
".LH_5px:\n"
"s_branch .LH_5r\n"
".LH_end:\n"
"s_waitcnt vmcnt(0) lgkmcnt(0)\n"
"s_endpgm\n"
      :: "{v2}"((unsigned)(lds0 + ((wid + 3) & 3) * 18592 + trp)), "{v3}"((unsigned)(lds0 + ((wid + 2) & 3) * 18592 + trp)), "{v4}"((unsigned)(lds0 + ((wid + 1) & 3) * 18592 + trp)), "{v5}"((unsigned)(lane * 16 + f0 * 1024 + ((0 + rot) % 7) * 1024 + 134316032)), "{v6}"((unsigned)(lane * 16 + f0 * 1024 + ((1 + rot) % 7) * 1024 + 134316032)), "{v7}"((unsigned)(lane * 16 + f0 * 1024 + ((2 + rot) % 7) * 1024 + 134316032)), "{v8}"((unsigned)(lane * 16 + f0 * 1024 + ((3 + rot) % 7) * 1024 + 134316032)), "{v9}"((unsigned)(lane * 16 + f0 * 1024 + ((4 + rot) % 7) * 1024 + 134316032)), "{v10}"((unsigned)(lane * 16 + f0 * 1024 + ((5 + rot) % 7) * 1024 + 134316032)), "{v11}"((unsigned)(lane * 16 + f0 * 1024 + ((6 + rot) % 7) * 1024 + 134316032)), "{v12}"((unsigned)((f0 + lane % 7) * 8 + kq * 32 + 353024 + H_GL * 1572864)), "{v13}"((unsigned)(lane * 16 + pair * 4096 + kq * 1024 + 212992)), "{v14}"((unsigned)(lane ? DROP : pair * 64 + kq * 8 + 354304)), "{v15}"((unsigned)(lane * 16 + (pg0 + 4) * 4096 + kq * 1024 + 212992)), "{v16}"((unsigned)(lane > 1 ? DROP : (lane ? pg0 + 4 : pair) * 64 + kq * 8 + 354304)), "{v17}"((unsigned)((lane & 15) * 8 + 67462912)), "{v18}"((unsigned)(lane * 16 + 67457024)), "{v19}"((unsigned)((lane & 24) ? DROP : (2 * (lane & 7) + (lane >> 5)) * 16 + 67461120)), "{v20}"((unsigned)(0x3727c5ac)), "{v48}"((unsigned)(vT + tsw)), "{v49}"((unsigned)(vT + (tsw ^ 8))), "{v50}"((unsigned)(vD + ((0 + rot) % 7) * 65536)), "{v51}"((unsigned)(vD + ((1 + rot) % 7) * 65536)), "{v52}"((unsigned)(vD + ((2 + rot) % 7) * 65536)), "{v53}"((unsigned)(vD + ((3 + rot) % 7) * 65536)), "{v54}"((unsigned)(vD + ((4 + rot) % 7) * 65536)), "{v55}"((unsigned)(vD + ((5 + rot) % 7) * 65536)), "{v56}"((unsigned)(vD + ((6 + rot) % 7) * 65536)), "{s[40:43]}"(rS), "{s[44:47]}"(rW), "{s[48:49]}"(wsp), "{s50}"((unsigned)(sA)), "{s51}"((unsigned)(sLo)), "{s52}"((unsigned)(sHi)), "{s53}"((unsigned)(sEx)), "{s54}"((unsigned)(sEnt)), "{s55}"((unsigned)(sLd)), "{s[56:57]}"(wsb + (sLo & 0x3ffffff)), "{s59}"((unsigned)(lds0 + wid * 18592)), "{s62}"((unsigned)(0x3a800000)), "{s66}"((unsigned)(sW0 + 64 * ((wid + 1) & 3))), "{s67}"((unsigned)(sW0 + 64 * ((wid + 2) & 3))), "{s68}"((unsigned)(sW0 + 64 * ((wid + 3) & 3))), "{s69}"((unsigned)(sW0 + 64 * ((wid + 4) & 3))), "{s70}"((unsigned)(sW0 + 256))
      : "v255", "a255", "s95", "vcc", "scc", "memory");

    } else if (grp < 4) {
      const unsigned sEx = sHi + G_EX * 3 * RS, sEnt = -wid & 3;
    asm volatile(
"s_nop 4\n"
"v_mov_b32 v186, 0\n"
"global_load_dwordx2 v[0:1], v186, s[48:49] sc1\n"
"v_mov_b32 v188, 0x3f803f80\n"
"v_mov_b32 v189, 0x3f803f80\n"
"v_mov_b32 v190, 0x3f803f80\n"
"v_mov_b32 v191, 0x3f803f80\n"
"s_add_u32 m0, s55, 0\n"
"s_add_u32 s60, s62, 0\n"
"buffer_load_dwordx4 v46, s[44:47], s60 offen lds\n"
"s_add_u32 m0, s55, 1024\n"
"s_add_u32 s60, s62, 32768\n"
"buffer_load_dwordx4 v46, s[44:47], s60 offen lds\n"
"s_add_u32 m0, s55, 2048\n"
"s_add_u32 s60, s63, 0\n"
"buffer_load_dwordx4 v46, s[44:47], s60 offen lds\n"
"s_add_u32 m0, s55, 3072\n"
"s_add_u32 s60, s63, 32768\n"
"buffer_load_dwordx4 v46, s[44:47], s60 offen lds\n"
"s_add_u32 m0, s55, 4096\n"
"s_add_u32 s60, s64, 0\n"
"buffer_load_dwordx4 v46, s[44:47], s60 offen lds\n"
"s_add_u32 m0, s55, 5120\n"
"s_add_u32 s60, s64, 32768\n"
"buffer_load_dwordx4 v46, s[44:47], s60 offen lds\n"
"s_add_u32 m0, s55, 6144\n"
"s_add_u32 s60, s65, 0\n"
"buffer_load_dwordx4 v46, s[44:47], s60 offen lds\n"
"s_add_u32 m0, s55, 7168\n"
"s_add_u32 s60, s65, 32768\n"
"buffer_load_dwordx4 v46, s[44:47], s60 offen lds\n"
"s_add_u32 m0, s55, 8192\n"
"s_add_u32 s60, s66, 0\n"
"buffer_load_dwordx4 v46, s[44:47], s60 offen lds\n"
"s_add_u32 m0, s55, 9216\n"
"s_add_u32 s60, s66, 32768\n"
"buffer_load_dwordx4 v46, s[44:47], s60 offen lds\n"
"s_add_u32 m0, s55, 10240\n"
"s_add_u32 s60, s62, 0\n"
"buffer_load_dwordx4 v47, s[44:47], s60 offen lds\n"
"s_add_u32 m0, s55, 11264\n"
"s_add_u32 s60, s62, 32768\n"
"buffer_load_dwordx4 v47, s[44:47], s60 offen lds\n"
"s_add_u32 m0, s55, 12288\n"
"s_add_u32 s60, s63, 0\n"
"buffer_load_dwordx4 v47, s[44:47], s60 offen lds\n"
"s_add_u32 m0, s55, 13312\n"
"s_add_u32 s60, s63, 32768\n"
"buffer_load_dwordx4 v47, s[44:47], s60 offen lds\n"
"s_add_u32 m0, s55, 14336\n"
"s_add_u32 s60, s64, 0\n"
"buffer_load_dwordx4 v47, s[44:47], s60 offen lds\n"
"s_add_u32 m0, s55, 15360\n"
"s_add_u32 s60, s64, 32768\n"
"buffer_load_dwordx4 v47, s[44:47], s60 offen lds\n"
"s_add_u32 m0, s55, 16384\n"
"s_add_u32 s60, s65, 0\n"
"buffer_load_dwordx4 v47, s[44:47], s60 offen lds\n"
"s_add_u32 m0, s55, 17408\n"
"s_add_u32 s60, s65, 32768\n"
"buffer_load_dwordx4 v47, s[44:47], s60 offen lds\n"
"s_add_u32 m0, s55, 18432\n"
"s_add_u32 s60, s66, 0\n"
"buffer_load_dwordx4 v47, s[44:47], s60 offen lds\n"
"s_add_u32 m0, s55, 19456\n"
"s_add_u32 s60, s66, 32768\n"
"buffer_load_dwordx4 v47, s[44:47], s60 offen lds\n"
"s_add_u32 m0, s55, 20480\n"
"s_add_u32 s60, s62, 0\n"
"buffer_load_dwordx4 v48, s[44:47], s60 offen lds\n"
"s_add_u32 m0, s55, 21504\n"
"s_add_u32 s60, s62, 32768\n"
"buffer_load_dwordx4 v48, s[44:47], s60 offen lds\n"
"s_add_u32 m0, s55, 22528\n"
"s_add_u32 s60, s63, 0\n"
"buffer_load_dwordx4 v48, s[44:47], s60 offen lds\n"
"s_add_u32 m0, s55, 23552\n"
"s_add_u32 s60, s63, 32768\n"
"buffer_load_dwordx4 v48, s[44:47], s60 offen lds\n"
"s_add_u32 m0, s55, 24576\n"
"s_add_u32 s60, s64, 0\n"
"buffer_load_dwordx4 v48, s[44:47], s60 offen lds\n"
"s_add_u32 m0, s55, 25600\n"
"s_add_u32 s60, s64, 32768\n"
"buffer_load_dwordx4 v48, s[44:47], s60 offen lds\n"
"s_add_u32 m0, s55, 26624\n"
"s_add_u32 s60, s65, 0\n"
"buffer_load_dwordx4 v48, s[44:47], s60 offen lds\n"
"s_add_u32 m0, s55, 27648\n"
"s_add_u32 s60, s65, 32768\n"
"buffer_load_dwordx4 v48, s[44:47], s60 offen lds\n"
"s_add_u32 m0, s55, 28672\n"
"s_add_u32 s60, s66, 0\n"
"buffer_load_dwordx4 v48, s[44:47], s60 offen lds\n"
"s_add_u32 m0, s55, 29696\n"
"s_add_u32 s60, s66, 32768\n"
"buffer_load_dwordx4 v48, s[44:47], s60 offen lds\n"
"s_add_u32 m0, s55, 30720\n"
"s_add_u32 s60, s62, 0\n"
"buffer_load_dwordx4 v49, s[44:47], s60 offen lds\n"
"s_add_u32 m0, s55, 31744\n"
"s_add_u32 s60, s62, 32768\n"
"buffer_load_dwordx4 v49, s[44:47], s60 offen lds\n"
"s_add_u32 m0, s55, 32768\n"
"s_add_u32 s60, s63, 0\n"
"buffer_load_dwordx4 v49, s[44:47], s60 offen lds\n"
"s_add_u32 m0, s55, 33792\n"
"s_add_u32 s60, s63, 32768\n"
"buffer_load_dwordx4 v49, s[44:47], s60 offen lds\n"
"s_add_u32 m0, s55, 34816\n"
"s_add_u32 s60, s64, 0\n"
"buffer_load_dwordx4 v49, s[44:47], s60 offen lds\n"
"s_add_u32 m0, s55, 35840\n"
"s_add_u32 s60, s64, 32768\n"
"buffer_load_dwordx4 v49, s[44:47], s60 offen lds\n"
"s_add_u32 m0, s55, 36864\n"
"s_add_u32 s60, s65, 0\n"
"buffer_load_dwordx4 v49, s[44:47], s60 offen lds\n"
"s_add_u32 m0, s55, 37888\n"
"s_add_u32 s60, s65, 32768\n"
"buffer_load_dwordx4 v49, s[44:47], s60 offen lds\n"
"s_add_u32 m0, s55, 38912\n"
"s_add_u32 s60, s66, 0\n"
"buffer_load_dwordx4 v49, s[44:47], s60 offen lds\n"
"s_add_u32 m0, s55, 39936\n"
"s_add_u32 s60, s66, 32768\n"
"buffer_load_dwordx4 v49, s[44:47], s60 offen lds\n"
"s_waitcnt vmcnt(38)\n"
"ds_read_b64_tr_b16 a[0:1], v44 offset:0\n"
"ds_read_b64_tr_b16 a[2:3], v44 offset:256\n"
"ds_read_b64_tr_b16 a[28:29], v45 offset:0\n"
"ds_read_b64_tr_b16 a[30:31], v45 offset:256\n"
"v_add_u32 v0, 2, v0\n"
"v_add_u32 v1, 0x9e3779b9, v1\n"
"v_or_b32 v0, 1, v0\n"
"v_readfirstlane_b32 s49, v1\n"
"v_readfirstlane_b32 s48, v0\n"
"s_waitcnt vmcnt(36)\n"
"ds_read_b64_tr_b16 a[56:57], v44 offset:2048\n"
"ds_read_b64_tr_b16 a[58:59], v44 offset:2304\n"
"ds_read_b64_tr_b16 a[84:85], v45 offset:2048\n"
"ds_read_b64_tr_b16 a[86:87], v45 offset:2304\n"
"s_waitcnt lgkmcnt(4)\n"
"s_add_u32 m0, s55, 0\n"
"s_add_u32 s60, s62, 0\n"
"buffer_load_dwordx4 v50, s[44:47], s60 offen lds\n"
"s_add_u32 m0, s55, 1024\n"
"s_add_u32 s60, s62, 32768\n"
"buffer_load_dwordx4 v50, s[44:47], s60 offen lds\n"
"s_waitcnt vmcnt(36)\n"
"ds_read_b64_tr_b16 a[112:113], v44 offset:4096\n"
"ds_read_b64_tr_b16 a[114:115], v44 offset:4352\n"
"ds_read_b64_tr_b16 a[140:141], v45 offset:4096\n"
"ds_read_b64_tr_b16 a[142:143], v45 offset:4352\n"
"s_waitcnt lgkmcnt(4)\n"
"s_add_u32 m0, s55, 2048\n"
"s_add_u32 s60, s63, 0\n"
"buffer_load_dwordx4 v50, s[44:47], s60 offen lds\n"
"s_add_u32 m0, s55, 3072\n"
"s_add_u32 s60, s63, 32768\n"
"buffer_load_dwordx4 v50, s[44:47], s60 offen lds\n"
"s_waitcnt vmcnt(36)\n"
"ds_read_b64_tr_b16 a[168:169], v44 offset:6144\n"
"ds_read_b64_tr_b16 a[170:171], v44 offset:6400\n"
"ds_read_b64_tr_b16 a[196:197], v45 offset:6144\n"
"ds_read_b64_tr_b16 a[198:199], v45 offset:6400\n"
"s_waitcnt lgkmcnt(4)\n"
"s_add_u32 m0, s55, 4096\n"
"s_add_u32 s60, s64, 0\n"
"buffer_load_dwordx4 v50, s[44:47], s60 offen lds\n"
"s_add_u32 m0, s55, 5120\n"
"s_add_u32 s60, s64, 32768\n"
"buffer_load_dwordx4 v50, s[44:47], s60 offen lds\n"
"s_waitcnt vmcnt(36)\n"
"ds_read_b64_tr_b16 a[224:225], v44 offset:8192\n"
"ds_read_b64_tr_b16 a[226:227], v44 offset:8448\n"
"ds_read_b64_tr_b16 a[252:253], v45 offset:8192\n"
"ds_read_b64_tr_b16 a[254:255], v45 offset:8448\n"
"s_waitcnt lgkmcnt(4)\n"
"s_add_u32 m0, s55, 6144\n"
"s_add_u32 s60, s65, 0\n"
"buffer_load_dwordx4 v50, s[44:47], s60 offen lds\n"
"s_add_u32 m0, s55, 7168\n"
"s_add_u32 s60, s65, 32768\n"
"buffer_load_dwordx4 v50, s[44:47], s60 offen lds\n"
"s_waitcnt vmcnt(36)\n"
"ds_read_b64_tr_b16 a[4:5], v44 offset:10240\n"
"ds_read_b64_tr_b16 a[6:7], v44 offset:10496\n"
"ds_read_b64_tr_b16 a[32:33], v45 offset:10240\n"
"ds_read_b64_tr_b16 a[34:35], v45 offset:10496\n"
"s_waitcnt lgkmcnt(4)\n"
"s_add_u32 m0, s55, 8192\n"
"s_add_u32 s60, s66, 0\n"
"buffer_load_dwordx4 v50, s[44:47], s60 offen lds\n"
"s_add_u32 m0, s55, 9216\n"
"s_add_u32 s60, s66, 32768\n"
"buffer_load_dwordx4 v50, s[44:47], s60 offen lds\n"
"s_waitcnt vmcnt(36)\n"
"ds_read_b64_tr_b16 a[60:61], v44 offset:12288\n"
"ds_read_b64_tr_b16 a[62:63], v44 offset:12544\n"
"ds_read_b64_tr_b16 a[88:89], v45 offset:12288\n"
"ds_read_b64_tr_b16 a[90:91], v45 offset:12544\n"
"s_waitcnt lgkmcnt(4)\n"
"s_add_u32 m0, s55, 10240\n"
"s_add_u32 s60, s62, 0\n"
"buffer_load_dwordx4 v51, s[44:47], s60 offen lds\n"
"s_add_u32 m0, s55, 11264\n"
"s_add_u32 s60, s62, 32768\n"
"buffer_load_dwordx4 v51, s[44:47], s60 offen lds\n"
"s_waitcnt vmcnt(36)\n"
"ds_read_b64_tr_b16 a[116:117], v44 offset:14336\n"
"ds_read_b64_tr_b16 a[118:119], v44 offset:14592\n"
"ds_read_b64_tr_b16 a[144:145], v45 offset:14336\n"
"ds_read_b64_tr_b16 a[146:147], v45 offset:14592\n"
"s_waitcnt lgkmcnt(4)\n"
"s_add_u32 m0, s55, 12288\n"
"s_add_u32 s60, s63, 0\n"
"buffer_load_dwordx4 v51, s[44:47], s60 offen lds\n"
"s_add_u32 m0, s55, 13312\n"
"s_add_u32 s60, s63, 32768\n"
"buffer_load_dwordx4 v51, s[44:47], s60 offen lds\n"
"s_waitcnt vmcnt(36)\n"
"ds_read_b64_tr_b16 a[172:173], v44 offset:16384\n"
"ds_read_b64_tr_b16 a[174:175], v44 offset:16640\n"
"ds_read_b64_tr_b16 a[200:201], v45 offset:16384\n"
"ds_read_b64_tr_b16 a[202:203], v45 offset:16640\n"
"s_waitcnt lgkmcnt(4)\n"
"s_add_u32 m0, s55, 14336\n"
"s_add_u32 s60, s64, 0\n"
"buffer_load_dwordx4 v51, s[44:47], s60 offen lds\n"
"s_add_u32 m0, s55, 15360\n"
"s_add_u32 s60, s64, 32768\n"
"buffer_load_dwordx4 v51, s[44:47], s60 offen lds\n"
"s_waitcnt vmcnt(36)\n"
"ds_read_b64_tr_b16 a[228:229], v44 offset:18432\n"
"ds_read_b64_tr_b16 a[230:231], v44 offset:18688\n"
"ds_read_b64_tr_b16 v[20:21], v45 offset:18432\n"
"ds_read_b64_tr_b16 v[22:23], v45 offset:18688\n"
"s_waitcnt lgkmcnt(4)\n"
"s_add_u32 m0, s55, 16384\n"
"s_add_u32 s60, s65, 0\n"
"buffer_load_dwordx4 v51, s[44:47], s60 offen lds\n"
"s_add_u32 m0, s55, 17408\n"
"s_add_u32 s60, s65, 32768\n"
"buffer_load_dwordx4 v51, s[44:47], s60 offen lds\n"
"s_waitcnt vmcnt(36)\n"
"ds_read_b64_tr_b16 a[8:9], v44 offset:20480\n"
"ds_read_b64_tr_b16 a[10:11], v44 offset:20736\n"
"ds_read_b64_tr_b16 a[36:37], v45 offset:20480\n"
"ds_read_b64_tr_b16 a[38:39], v45 offset:20736\n"
"s_waitcnt lgkmcnt(4)\n"
"s_add_u32 m0, s55, 18432\n"
"s_add_u32 s60, s66, 0\n"
"buffer_load_dwordx4 v51, s[44:47], s60 offen lds\n"
"s_add_u32 m0, s55, 19456\n"
"s_add_u32 s60, s66, 32768\n"
"buffer_load_dwordx4 v51, s[44:47], s60 offen lds\n"
"s_waitcnt vmcnt(36)\n"
"ds_read_b64_tr_b16 a[64:65], v44 offset:22528\n"
"ds_read_b64_tr_b16 a[66:67], v44 offset:22784\n"
"ds_read_b64_tr_b16 a[92:93], v45 offset:22528\n"
"ds_read_b64_tr_b16 a[94:95], v45 offset:22784\n"
"s_waitcnt lgkmcnt(4)\n"
"s_add_u32 m0, s55, 20480\n"
"s_add_u32 s60, s62, 0\n"
"buffer_load_dwordx4 v52, s[44:47], s60 offen lds\n"
"s_add_u32 m0, s55, 21504\n"
"s_add_u32 s60, s62, 32768\n"
"buffer_load_dwordx4 v52, s[44:47], s60 offen lds\n"
"s_waitcnt vmcnt(36)\n"
"ds_read_b64_tr_b16 a[120:121], v44 offset:24576\n"
"ds_read_b64_tr_b16 a[122:123], v44 offset:24832\n"
"ds_read_b64_tr_b16 a[148:149], v45 offset:24576\n"
"ds_read_b64_tr_b16 a[150:151], v45 offset:24832\n"
"s_waitcnt lgkmcnt(4)\n"
"s_add_u32 m0, s55, 22528\n"
"s_add_u32 s60, s63, 0\n"
"buffer_load_dwordx4 v52, s[44:47], s60 offen lds\n"
"s_add_u32 m0, s55, 23552\n"
"s_add_u32 s60, s63, 32768\n"
"buffer_load_dwordx4 v52, s[44:47], s60 offen lds\n"
"s_waitcnt vmcnt(36)\n"
"ds_read_b64_tr_b16 a[176:177], v44 offset:26624\n"
"ds_read_b64_tr_b16 a[178:179], v44 offset:26880\n"
"ds_read_b64_tr_b16 a[204:205], v45 offset:26624\n"
"ds_read_b64_tr_b16 a[206:207], v45 offset:26880\n"
"s_waitcnt lgkmcnt(4)\n"
"s_add_u32 m0, s55, 24576\n"
"s_add_u32 s60, s64, 0\n"
"buffer_load_dwordx4 v52, s[44:47], s60 offen lds\n"
"s_add_u32 m0, s55, 25600\n"
"s_add_u32 s60, s64, 32768\n"
"buffer_load_dwordx4 v52, s[44:47], s60 offen lds\n"
"s_waitcnt vmcnt(36)\n"
"ds_read_b64_tr_b16 a[232:233], v44 offset:28672\n"
"ds_read_b64_tr_b16 a[234:235], v44 offset:28928\n"
"ds_read_b64_tr_b16 v[24:25], v45 offset:28672\n"
"ds_read_b64_tr_b16 v[26:27], v45 offset:28928\n"
"s_waitcnt lgkmcnt(4)\n"
"s_add_u32 m0, s55, 26624\n"
"s_add_u32 s60, s65, 0\n"
"buffer_load_dwordx4 v52, s[44:47], s60 offen lds\n"
"s_add_u32 m0, s55, 27648\n"
"s_add_u32 s60, s65, 32768\n"
"buffer_load_dwordx4 v52, s[44:47], s60 offen lds\n"
"s_waitcnt vmcnt(36)\n"
"ds_read_b64_tr_b16 a[12:13], v44 offset:30720\n"
"ds_read_b64_tr_b16 a[14:15], v44 offset:30976\n"
"ds_read_b64_tr_b16 a[40:41], v45 offset:30720\n"
"ds_read_b64_tr_b16 a[42:43], v45 offset:30976\n"
"s_waitcnt lgkmcnt(4)\n"
"s_add_u32 m0, s55, 28672\n"
"s_add_u32 s60, s66, 0\n"
"buffer_load_dwordx4 v52, s[44:47], s60 offen lds\n"
"s_add_u32 m0, s55, 29696\n"
"s_add_u32 s60, s66, 32768\n"
"buffer_load_dwordx4 v52, s[44:47], s60 offen lds\n"
"s_waitcnt vmcnt(36)\n"
"ds_read_b64_tr_b16 a[68:69], v44 offset:32768\n"
"ds_read_b64_tr_b16 a[70:71], v44 offset:33024\n"
"ds_read_b64_tr_b16 a[96:97], v45 offset:32768\n"
"ds_read_b64_tr_b16 a[98:99], v45 offset:33024\n"
"s_waitcnt lgkmcnt(4)\n"
"s_waitcnt vmcnt(34)\n"
"ds_read_b64_tr_b16 a[124:125], v44 offset:34816\n"
"ds_read_b64_tr_b16 a[126:127], v44 offset:35072\n"
"ds_read_b64_tr_b16 a[152:153], v45 offset:34816\n"
"ds_read_b64_tr_b16 a[154:155], v45 offset:35072\n"
"s_waitcnt lgkmcnt(4)\n"
"s_waitcnt vmcnt(32)\n"
"ds_read_b64_tr_b16 a[180:181], v44 offset:36864\n"
"ds_read_b64_tr_b16 a[182:183], v44 offset:37120\n"
"ds_read_b64_tr_b16 a[208:209], v45 offset:36864\n"
"ds_read_b64_tr_b16 a[210:211], v45 offset:37120\n"
"s_waitcnt lgkmcnt(4)\n"
"s_waitcnt vmcnt(30)\n"
"ds_read_b64_tr_b16 a[236:237], v44 offset:38912\n"
"ds_read_b64_tr_b16 a[238:239], v44 offset:39168\n"
"ds_read_b64_tr_b16 v[28:29], v45 offset:38912\n"
"ds_read_b64_tr_b16 v[30:31], v45 offset:39168\n"
"s_waitcnt lgkmcnt(4)\n"
"s_waitcnt vmcnt(28)\n"
"ds_read_b64_tr_b16 a[16:17], v44 offset:0\n"
"ds_read_b64_tr_b16 a[18:19], v44 offset:256\n"
"ds_read_b64_tr_b16 a[44:45], v45 offset:0\n"
"ds_read_b64_tr_b16 a[46:47], v45 offset:256\n"
"s_waitcnt lgkmcnt(4)\n"
"s_waitcnt vmcnt(26)\n"
"ds_read_b64_tr_b16 a[72:73], v44 offset:2048\n"
"ds_read_b64_tr_b16 a[74:75], v44 offset:2304\n"
"ds_read_b64_tr_b16 a[100:101], v45 offset:2048\n"
"ds_read_b64_tr_b16 a[102:103], v45 offset:2304\n"
"s_waitcnt lgkmcnt(4)\n"
"s_waitcnt vmcnt(24)\n"
"ds_read_b64_tr_b16 a[128:129], v44 offset:4096\n"
"ds_read_b64_tr_b16 a[130:131], v44 offset:4352\n"
"ds_read_b64_tr_b16 a[156:157], v45 offset:4096\n"
"ds_read_b64_tr_b16 a[158:159], v45 offset:4352\n"
"s_waitcnt lgkmcnt(4)\n"
"s_waitcnt vmcnt(22)\n"
"ds_read_b64_tr_b16 a[184:185], v44 offset:6144\n"
"ds_read_b64_tr_b16 a[186:187], v44 offset:6400\n"
"ds_read_b64_tr_b16 a[212:213], v45 offset:6144\n"
"ds_read_b64_tr_b16 a[214:215], v45 offset:6400\n"
"s_waitcnt lgkmcnt(4)\n"
"s_waitcnt vmcnt(20)\n"
"ds_read_b64_tr_b16 a[240:241], v44 offset:8192\n"
"ds_read_b64_tr_b16 a[242:243], v44 offset:8448\n"
"ds_read_b64_tr_b16 v[32:33], v45 offset:8192\n"
"ds_read_b64_tr_b16 v[34:35], v45 offset:8448\n"
"s_waitcnt lgkmcnt(4)\n"
"s_waitcnt vmcnt(18)\n"
"ds_read_b64_tr_b16 a[20:21], v44 offset:10240\n"
"ds_read_b64_tr_b16 a[22:23], v44 offset:10496\n"
"ds_read_b64_tr_b16 a[48:49], v45 offset:10240\n"
"ds_read_b64_tr_b16 a[50:51], v45 offset:10496\n"
"s_waitcnt lgkmcnt(4)\n"
"s_waitcnt vmcnt(16)\n"
"ds_read_b64_tr_b16 a[76:77], v44 offset:12288\n"
"ds_read_b64_tr_b16 a[78:79], v44 offset:12544\n"
"ds_read_b64_tr_b16 a[104:105], v45 offset:12288\n"
"ds_read_b64_tr_b16 a[106:107], v45 offset:12544\n"
"s_waitcnt lgkmcnt(4)\n"
"s_waitcnt vmcnt(14)\n"
"ds_read_b64_tr_b16 a[132:133], v44 offset:14336\n"
"ds_read_b64_tr_b16 a[134:135], v44 offset:14592\n"
"ds_read_b64_tr_b16 a[160:161], v45 offset:14336\n"
"ds_read_b64_tr_b16 a[162:163], v45 offset:14592\n"
"s_waitcnt lgkmcnt(4)\n"
"s_waitcnt vmcnt(12)\n"
"ds_read_b64_tr_b16 a[188:189], v44 offset:16384\n"
"ds_read_b64_tr_b16 a[190:191], v44 offset:16640\n"
"ds_read_b64_tr_b16 a[216:217], v45 offset:16384\n"
"ds_read_b64_tr_b16 a[218:219], v45 offset:16640\n"
"s_waitcnt lgkmcnt(4)\n"
"s_waitcnt vmcnt(10)\n"
"ds_read_b64_tr_b16 a[244:245], v44 offset:18432\n"
"ds_read_b64_tr_b16 a[246:247], v44 offset:18688\n"
"ds_read_b64_tr_b16 v[36:37], v45 offset:18432\n"
"ds_read_b64_tr_b16 v[38:39], v45 offset:18688\n"
"s_waitcnt lgkmcnt(4)\n"
"s_waitcnt vmcnt(8)\n"
"ds_read_b64_tr_b16 a[24:25], v44 offset:20480\n"
"ds_read_b64_tr_b16 a[26:27], v44 offset:20736\n"
"ds_read_b64_tr_b16 a[52:53], v45 offset:20480\n"
"ds_read_b64_tr_b16 a[54:55], v45 offset:20736\n"
"s_waitcnt lgkmcnt(4)\n"
"s_waitcnt vmcnt(6)\n"
"ds_read_b64_tr_b16 a[80:81], v44 offset:22528\n"
"ds_read_b64_tr_b16 a[82:83], v44 offset:22784\n"
"ds_read_b64_tr_b16 a[108:109], v45 offset:22528\n"
"ds_read_b64_tr_b16 a[110:111], v45 offset:22784\n"
"s_waitcnt lgkmcnt(4)\n"
"s_waitcnt vmcnt(4)\n"
"ds_read_b64_tr_b16 a[136:137], v44 offset:24576\n"
"ds_read_b64_tr_b16 a[138:139], v44 offset:24832\n"
"ds_read_b64_tr_b16 a[164:165], v45 offset:24576\n"
"ds_read_b64_tr_b16 a[166:167], v45 offset:24832\n"
"s_waitcnt lgkmcnt(4)\n"
"s_waitcnt vmcnt(2)\n"
"ds_read_b64_tr_b16 a[192:193], v44 offset:26624\n"
"ds_read_b64_tr_b16 a[194:195], v44 offset:26880\n"
"ds_read_b64_tr_b16 a[220:221], v45 offset:26624\n"
"ds_read_b64_tr_b16 a[222:223], v45 offset:26880\n"
"s_waitcnt lgkmcnt(4)\n"
"s_waitcnt vmcnt(0)\n"
"ds_read_b64_tr_b16 a[248:249], v44 offset:28672\n"
"ds_read_b64_tr_b16 a[250:251], v44 offset:28928\n"
"ds_read_b64_tr_b16 v[40:41], v45 offset:28672\n"
"ds_read_b64_tr_b16 v[42:43], v45 offset:28928\n"
"s_waitcnt lgkmcnt(4)\n"
"s_waitcnt vmcnt(0)\n"
"s_waitcnt lgkmcnt(0)\n"
"s_mov_b32 m0, s59\n"
"s_barrier\n"
"v_mov_b32 v128, 0\n"
"v_mov_b32 v129, 0\n"
"v_mov_b32 v132, 0\n"
"v_mov_b32 v133, 0\n"
"v_mov_b32 v136, 0\n"
"v_mov_b32 v137, 0\n"
"v_mov_b32 v168, 0\n"
"v_mov_b32 v169, 0\n"
"s_mov_b32 s94, 0x80000000\n"
"s_mov_b32 s93, 0x80000000\n"
"s_mov_b32 s93, 0x80000000\n"
"s_mov_b32 s95, 0x80000000\n"
"s_mov_b32 s93, 0x80000000\n"
"s_mov_b32 s95, 0x80000000\n"
"s_mov_b32 s95, 0x80000000\n"
"s_mov_b32 s94, 0x80000000\n"
"s_cmp_eq_u32 s54, 0\n"
"s_cbranch_scc1 .LG_1s\n"
"s_cmp_eq_u32 s54, 1\n"
"s_cbranch_scc1 .LG_2s\n"
"s_cmp_eq_u32 s54, 2\n"
"s_cbranch_scc1 .LG_3s\n"
"s_cmp_eq_u32 s54, 3\n"
"s_cbranch_scc1 .LG_4s\n"
".p2align 6\n"
".LG_top:\n"
"v_mfma_f32_16x16x32_bf16 v[56:59], v[100:103], a[0:3], 0\n"
"v_mfma_f32_16x16x32_bf16 v[56:59], v[68:71], a[4:7], v[56:59]\n"
"global_load_dwordx4 v[44:47], v7, s[56:57] offset:0 sc1\n"
"v_mfma_f32_16x16x32_bf16 v[56:59], v[72:75], a[8:11], v[56:59]\n"
"v_mfma_f32_16x16x32_bf16 v[56:59], v[76:79], a[12:15], v[56:59]\n"
"s_waitcnt vmcnt(5)\n"
"v_mfma_f32_16x16x32_bf16 v[56:59], v[80:83], a[16:19], v[56:59]\n"
"s_waitcnt lgkmcnt(3)\n"
"v_add_f32 v120, v120, v64\n"
"v_add_f32 v121, v121, v65\n"
"v_mfma_f32_16x16x32_bf16 v[56:59], v[84:87], a[20:23], v[56:59]\n"
"s_waitcnt vmcnt(4)\n"
"v_add_f32 v122, v122, v66\n"
"v_add_f32 v123, v123, v67\n"
"v_mfma_f32_16x16x32_bf16 v[56:59], v[88:91], a[24:27], v[56:59]\n"
"global_load_dwordx4 v[48:51], v8, s[56:57] offset:0 sc1\n"
"v_add_f32 v120, v120, v92\n"
"v_mfma_f32_16x16x32_bf16 v[60:63], v[100:103], a[28:31], 0\n"
"buffer_load_dwordx2 v[136:137], v12, s[40:43], s50 offen sc1\n"
"v_mfma_f32_16x16x32_bf16 v[60:63], v[68:71], a[32:35], v[60:63]\n"
"ds_write_addtid_b32 v56 offset:0\n"
"v_add_f32 v121, v121, v93\n"
"v_mfma_f32_16x16x32_bf16 v[60:63], v[72:75], a[36:39], v[60:63]\n"
"ds_write_addtid_b32 v57 offset:4640\n"
"global_load_dwordx4 v[52:55], v9, s[56:57] offset:0 sc1\n"
"v_mfma_f32_16x16x32_bf16 v[60:63], v[76:79], a[40:43], v[60:63]\n"
"ds_write_addtid_b32 v58 offset:9344\n"
"v_add_f32 v122, v122, v94\n"
"v_mfma_f32_16x16x32_bf16 v[60:63], v[80:83], a[44:47], v[60:63]\n"
"ds_write_addtid_b32 v59 offset:13984\n"
"v_add_f32 v123, v123, v95\n"
"v_mfma_f32_16x16x32_bf16 v[60:63], v[84:87], a[48:51], v[60:63]\n"
"v_add_f32 v120, v120, v116\n"
"v_add_f32 v121, v121, v117\n"
"v_mfma_f32_16x16x32_bf16 v[60:63], v[88:91], a[52:55], v[60:63]\n"
"v_add_f32 v122, v122, v118\n"
"v_add_f32 v123, v123, v119\n"
"v_mfma_f32_16x16x32_bf16 v[64:67], v[100:103], a[56:59], 0\n"
"s_waitcnt vmcnt(6)\n"
"buffer_store_dwordx2 v[0:1], v14, s[40:43], s93 offen offset:0 sc1\n"
"v_mfma_f32_16x16x32_bf16 v[64:67], v[68:71], a[60:63], v[64:67]\n"
"s_add_u32 s95, s50, 0xffd00000\n"
"s_and_b32 s94, s95, 0xfbffffff\n"
"ds_write_addtid_b32 v60 offset:256\n"
"v_mfma_f32_16x16x32_bf16 v[64:67], v[72:75], a[64:67], v[64:67]\n"
"ds_write_addtid_b32 v61 offset:4896\n"
"global_load_dwordx4 v[56:59], v10, s[56:57] offset:0 sc1\n"
"v_mfma_f32_16x16x32_bf16 v[64:67], v[76:79], a[68:71], v[64:67]\n"
"ds_write_addtid_b32 v62 offset:9600\n"
"v_cvt_pk_bf16_f32 v92, v120, v121\n"
"v_mfma_f32_16x16x32_bf16 v[64:67], v[80:83], a[72:75], v[64:67]\n"
"ds_write_addtid_b32 v63 offset:14240\n"
"v_cvt_pk_bf16_f32 v93, v122, v123\n"
"v_mfma_f32_16x16x32_bf16 v[64:67], v[84:87], a[76:79], v[64:67]\n"
"s_waitcnt lgkmcnt(8)\n"
"v_add_f32 v124, v124, v128\n"
"v_add_f32 v125, v125, v129\n"
"v_mfma_f32_16x16x32_bf16 v[64:67], v[88:91], a[80:83], v[64:67]\n"
"v_add_f32 v126, v126, v130\n"
"v_add_f32 v127, v127, v131\n"
"v_mfma_f32_16x16x32_bf16 v[96:99], v[100:103], a[84:87], 0\n"
"global_load_dwordx4 v[60:63], v11, s[56:57] offset:0 sc1\n"
"v_add_f32 v124, v124, v132\n"
"v_mfma_f32_16x16x32_bf16 v[96:99], v[68:71], a[88:91], v[96:99]\n"
"ds_write_addtid_b32 v64 offset:512\n"
"v_add_f32 v125, v125, v133\n"
"v_mfma_f32_16x16x32_bf16 v[96:99], v[72:75], a[92:95], v[96:99]\n"
"ds_write_addtid_b32 v65 offset:5152\n"
"v_add_f32 v126, v126, v134\n"
"v_mfma_f32_16x16x32_bf16 v[96:99], v[76:79], a[96:99], v[96:99]\n"
"ds_write_addtid_b32 v66 offset:9856\n"
"v_add_f32 v127, v127, v135\n"
"v_mfma_f32_16x16x32_bf16 v[96:99], v[80:83], a[100:103], v[96:99]\n"
"ds_write_addtid_b32 v67 offset:14496\n"
"v_add_f32 v124, v124, v112\n"
"v_mfma_f32_16x16x32_bf16 v[96:99], v[84:87], a[104:107], v[96:99]\n"
"v_add_f32 v125, v125, v113\n"
"v_add_f32 v126, v126, v114\n"
"v_mfma_f32_16x16x32_bf16 v[96:99], v[88:91], a[108:111], v[96:99]\n"
"v_add_f32 v127, v127, v115\n"
"v_cvt_pk_bf16_f32 v94, v124, v125\n"
"v_mfma_f32_16x16x32_bf16 v[64:67], v[100:103], a[112:115], 0\n"
"v_cvt_pk_bf16_f32 v95, v126, v127\n"
"v_mfma_f32_16x16x32_bf16 v[64:67], v[68:71], a[116:119], v[64:67]\n"
"ds_write_addtid_b32 v96 offset:768\n"
"v_mfma_f32_16x16x32_bf16 v[64:67], v[72:75], a[120:123], v[64:67]\n"
"ds_write_addtid_b32 v97 offset:5408\n"
"v_mfma_f32_16x16x32_bf16 v[64:67], v[76:79], a[124:127], v[64:67]\n"
"ds_write_addtid_b32 v98 offset:10112\n"
"v_mfma_f32_16x16x32_bf16 v[64:67], v[80:83], a[128:131], v[64:67]\n"
"ds_write_addtid_b32 v99 offset:14752\n"
"v_mfma_f32_16x16x32_bf16 v[64:67], v[84:87], a[132:135], v[64:67]\n"
"v_mfma_f32_16x16x32_bf16 v[64:67], v[88:91], a[136:139], v[64:67]\n"
"v_mfma_f32_16x16x32_bf16 v[112:115], v[100:103], a[140:143], 0\n"
"v_mfma_f32_16x16x32_bf16 v[112:115], v[68:71], a[144:147], v[112:115]\n"
"ds_write_addtid_b32 v64 offset:1024\n"
"v_mfma_f32_16x16x32_bf16 v[112:115], v[72:75], a[148:151], v[112:115]\n"
"ds_write_addtid_b32 v65 offset:5664\n"
"v_mfma_f32_16x16x32_bf16 v[112:115], v[76:79], a[152:155], v[112:115]\n"
"ds_write_addtid_b32 v66 offset:10368\n"
"v_mfma_f32_16x16x32_bf16 v[112:115], v[80:83], a[156:159], v[112:115]\n"
"ds_write_addtid_b32 v67 offset:15008\n"
"v_mfma_f32_16x16x32_bf16 v[112:115], v[84:87], a[160:163], v[112:115]\n"
"buffer_store_dwordx4 v[92:95], v13, s[40:43], s94 offen sc1\n"
"v_mfma_f32_16x16x32_bf16 v[112:115], v[88:91], a[164:167], v[112:115]\n"
"v_mfma_f32_16x16x32_bf16 v[96:99], a[168:171], v[100:103], 0\n"
"v_mfma_f32_16x16x32_bf16 v[96:99], a[172:175], v[68:71], v[96:99]\n"
"ds_write_addtid_b32 v112 offset:1280\n"
"v_mfma_f32_16x16x32_bf16 v[96:99], a[176:179], v[72:75], v[96:99]\n"
"ds_write_addtid_b32 v113 offset:5920\n"
"v_mfma_f32_16x16x32_bf16 v[96:99], a[180:183], v[76:79], v[96:99]\n"
"ds_write_addtid_b32 v114 offset:10624\n"
"v_mfma_f32_16x16x32_bf16 v[96:99], a[184:187], v[80:83], v[96:99]\n"
"ds_write_addtid_b32 v115 offset:15264\n"
"v_mfma_f32_16x16x32_bf16 v[96:99], a[188:191], v[84:87], v[96:99]\n"
"v_mfma_f32_16x16x32_bf16 v[96:99], a[192:195], v[88:91], v[96:99]\n"
"v_mfma_f32_16x16x32_bf16 v[92:95], a[196:199], v[100:103], 0\n"
"v_mfma_f32_16x16x32_bf16 v[92:95], a[200:203], v[68:71], v[92:95]\n"
"v_mfma_f32_16x16x32_bf16 v[92:95], a[204:207], v[72:75], v[92:95]\n"
"v_mfma_f32_16x16x32_bf16 v[92:95], a[208:211], v[76:79], v[92:95]\n"
"s_add_u32 s58, s50, 0x180000\n"
"v_mfma_f32_16x16x32_bf16 v[92:95], a[212:215], v[80:83], v[92:95]\n"
"s_and_b32 s58, s58, 0x3ffffff\n"
"v_mfma_f32_16x16x32_bf16 v[92:95], a[216:219], v[84:87], v[92:95]\n"
"s_add_u32 s56, s40, s58\n"
"v_mfma_f32_16x16x32_bf16 v[92:95], a[220:223], v[88:91], v[92:95]\n"
"s_addc_u32 s57, s41, 0\n"
"v_mfma_f32_16x16x32_bf16 v[112:115], a[224:227], v[100:103], 0\n"
"s_waitcnt vmcnt(5)\n"
"v_cmp_ne_u64 vcc, s[48:49], v[136:137]\n"
"v_mfma_f32_16x16x32_bf16 v[112:115], a[228:231], v[68:71], v[112:115]\n"
"v_mfma_f32_16x16x32_bf16 v[112:115], a[232:235], v[72:75], v[112:115]\n"
"s_cbranch_vccnz .LG_3s\n"
".LG_3r:\n"
"v_mfma_f32_16x16x32_bf16 v[112:115], a[236:239], v[76:79], v[112:115]\n"
"s_add_u32 s50, s50, 0x180000\n"
"global_load_dwordx4 v[64:67], v5, s[56:57] offset:0 sc1\n"
"v_mfma_f32_16x16x32_bf16 v[112:115], a[240:243], v[80:83], v[112:115]\n"
"v_mfma_f32_16x16x32_bf16 v[112:115], a[244:247], v[84:87], v[112:115]\n"
"v_mfma_f32_16x16x32_bf16 v[112:115], a[248:251], v[88:91], v[112:115]\n"
"v_mfma_f32_16x16x32_bf16 v[116:119], a[252:255], v[100:103], 0\n"
"v_mfma_f32_16x16x32_bf16 v[116:119], v[20:23], v[68:71], v[116:119]\n"
"s_waitcnt lgkmcnt(0)\n"
"v_mfma_f32_16x16x32_bf16 v[116:119], v[24:27], v[72:75], v[116:119]\n"
"s_barrier\n"
"v_mfma_f32_16x16x32_bf16 v[116:119], v[28:31], v[76:79], v[116:119]\n"
"ds_read_b128 v[100:103], v2 offset:2048\n"
"ds_read_b128 v[120:123], v3 offset:512\n"
"v_mfma_f32_16x16x32_bf16 v[116:119], v[32:35], v[80:83], v[116:119]\n"
"global_load_dwordx4 v[68:71], v6, s[56:57] offset:0 sc1\n"
"ds_read_b128 v[124:127], v4 offset:3072\n"
"v_mfma_f32_16x16x32_bf16 v[116:119], v[36:39], v[84:87], v[116:119]\n"
"ds_read_b128 v[128:131], v2 offset:2304\n"
"ds_read_b128 v[132:135], v3 offset:768\n"
"v_mfma_f32_16x16x32_bf16 v[116:119], v[40:43], v[88:91], v[116:119]\n"
"global_load_dwordx4 v[72:75], v7, s[56:57] offset:0 sc1\n"
"ds_read_b128 v[136:139], v4 offset:3328\n"
"v_mfma_f32_16x16x32_bf16 v[84:87], v[104:107], a[0:3], 0\n"
"ds_read_b128 v[140:143], v2 offset:3584\n"
"ds_read_b128 v[144:147], v3 offset:1536\n"
"v_mfma_f32_16x16x32_bf16 v[84:87], v[108:111], a[4:7], v[84:87]\n"
"ds_read_b128 v[148:151], v4 offset:3584\n"
"ds_read_b128 v[152:155], v2 offset:3840\n"
"v_mfma_f32_16x16x32_bf16 v[84:87], v[44:47], a[8:11], v[84:87]\n"
"ds_read_b128 v[156:159], v3 offset:1792\n"
"ds_read_b128 v[160:163], v4 offset:3840\n"
"v_mfma_f32_16x16x32_bf16 v[84:87], v[48:51], a[12:15], v[84:87]\n"
"s_waitcnt vmcnt(5)\n"
"v_mfma_f32_16x16x32_bf16 v[84:87], v[52:55], a[16:19], v[84:87]\n"
"s_waitcnt lgkmcnt(9)\n"
"global_load_dwordx4 v[76:79], v8, s[56:57] offset:0 sc1\n"
"v_add_f32 v100, v100, v120\n"
"v_mfma_f32_16x16x32_bf16 v[84:87], v[56:59], a[20:23], v[84:87]\n"
"s_waitcnt vmcnt(5)\n"
"v_add_f32 v101, v101, v121\n"
"v_add_f32 v102, v102, v122\n"
"v_mfma_f32_16x16x32_bf16 v[84:87], v[60:63], a[24:27], v[84:87]\n"
"v_add_f32 v103, v103, v123\n"
"v_add_f32 v100, v100, v124\n"
"v_mfma_f32_16x16x32_bf16 v[164:167], v[104:107], a[28:31], 0\n"
"buffer_load_dwordx2 v[168:169], v12, s[40:43], s50 offen sc1\n"
"v_mfma_f32_16x16x32_bf16 v[164:167], v[108:111], a[32:35], v[164:167]\n"
"ds_write_addtid_b32 v84 offset:2048\n"
"v_add_f32 v101, v101, v125\n"
"v_mfma_f32_16x16x32_bf16 v[164:167], v[44:47], a[36:39], v[164:167]\n"
"ds_write_addtid_b32 v85 offset:6688\n"
"v_add_f32 v102, v102, v126\n"
"v_mfma_f32_16x16x32_bf16 v[164:167], v[48:51], a[40:43], v[164:167]\n"
"ds_write_addtid_b32 v86 offset:11392\n"
"v_add_f32 v103, v103, v127\n"
"v_mfma_f32_16x16x32_bf16 v[164:167], v[52:55], a[44:47], v[164:167]\n"
"ds_write_addtid_b32 v87 offset:16032\n"
"v_add_f32 v100, v100, v96\n"
"v_mfma_f32_16x16x32_bf16 v[164:167], v[56:59], a[48:51], v[164:167]\n"
"global_load_dwordx4 v[80:83], v9, s[56:57] offset:0 sc1\n"
"v_add_f32 v101, v101, v97\n"
"v_mfma_f32_16x16x32_bf16 v[164:167], v[60:63], a[52:55], v[164:167]\n"
"v_add_f32 v102, v102, v98\n"
"v_add_f32 v103, v103, v99\n"
"v_mfma_f32_16x16x32_bf16 v[120:123], v[104:107], a[56:59], 0\n"
"s_add_u32 s94, s50, 0xffd00000\n"
"s_and_b32 s93, s94, 0xfbffffff\n"
"v_cvt_pk_bf16_f32 v124, v100, v101\n"
"v_mfma_f32_16x16x32_bf16 v[120:123], v[108:111], a[60:63], v[120:123]\n"
"ds_write_addtid_b32 v164 offset:2304\n"
"v_cvt_pk_bf16_f32 v125, v102, v103\n"
"v_mfma_f32_16x16x32_bf16 v[120:123], v[44:47], a[64:67], v[120:123]\n"
"s_waitcnt lgkmcnt(11)\n"
"ds_write_addtid_b32 v165 offset:6944\n"
"v_add_f32 v128, v128, v132\n"
"v_mfma_f32_16x16x32_bf16 v[120:123], v[48:51], a[68:71], v[120:123]\n"
"ds_write_addtid_b32 v166 offset:11648\n"
"v_add_f32 v129, v129, v133\n"
"v_mfma_f32_16x16x32_bf16 v[120:123], v[52:55], a[72:75], v[120:123]\n"
"s_waitcnt vmcnt(6)\n"
"ds_write_addtid_b32 v167 offset:16288\n"
"global_load_dwordx4 v[84:87], v10, s[56:57] offset:0 sc1\n"
"v_mfma_f32_16x16x32_bf16 v[120:123], v[56:59], a[76:79], v[120:123]\n"
"global_load_dwordx4 v[88:91], v11, s[56:57] offset:0 sc1\n"
"v_add_f32 v130, v130, v134\n"
"v_mfma_f32_16x16x32_bf16 v[120:123], v[60:63], a[80:83], v[120:123]\n"
"v_add_f32 v131, v131, v135\n"
"v_add_f32 v128, v128, v136\n"
"v_mfma_f32_16x16x32_bf16 v[96:99], v[104:107], a[84:87], 0\n"
"v_add_f32 v129, v129, v137\n"
"v_add_f32 v130, v130, v138\n"
"v_mfma_f32_16x16x32_bf16 v[96:99], v[108:111], a[88:91], v[96:99]\n"
"ds_write_addtid_b32 v120 offset:2560\n"
"v_add_f32 v131, v131, v139\n"
"v_mfma_f32_16x16x32_bf16 v[96:99], v[44:47], a[92:95], v[96:99]\n"
"ds_write_addtid_b32 v121 offset:7200\n"
"v_add_f32 v128, v128, v92\n"
"v_mfma_f32_16x16x32_bf16 v[96:99], v[48:51], a[96:99], v[96:99]\n"
"ds_write_addtid_b32 v122 offset:11904\n"
"v_add_f32 v129, v129, v93\n"
"v_mfma_f32_16x16x32_bf16 v[96:99], v[52:55], a[100:103], v[96:99]\n"
"ds_write_addtid_b32 v123 offset:16544\n"
"v_add_f32 v130, v130, v94\n"
"v_mfma_f32_16x16x32_bf16 v[96:99], v[56:59], a[104:107], v[96:99]\n"
"v_add_f32 v131, v131, v95\n"
"v_cvt_pk_bf16_f32 v126, v128, v129\n"
"v_mfma_f32_16x16x32_bf16 v[96:99], v[60:63], a[108:111], v[96:99]\n"
"buffer_store_dwordx2 v[0:1], v14, s[40:43], s95 offen offset:0 sc1\n"
"v_cvt_pk_bf16_f32 v127, v130, v131\n"
"v_mfma_f32_16x16x32_bf16 v[92:95], v[104:107], a[112:115], 0\n"
"buffer_store_dwordx4 v[124:127], v13, s[40:43], s93 offen sc1\n"
"v_mfma_f32_16x16x32_bf16 v[92:95], v[108:111], a[116:119], v[92:95]\n"
"s_waitcnt lgkmcnt(15)\n"
"ds_write_addtid_b32 v96 offset:2816\n"
"v_add_f32 v140, v140, v144\n"
"v_mfma_f32_16x16x32_bf16 v[92:95], v[44:47], a[120:123], v[92:95]\n"
"ds_write_addtid_b32 v97 offset:7456\n"
"v_add_f32 v141, v141, v145\n"
"v_mfma_f32_16x16x32_bf16 v[92:95], v[48:51], a[124:127], v[92:95]\n"
"ds_write_addtid_b32 v98 offset:12160\n"
"v_add_f32 v142, v142, v146\n"
"v_mfma_f32_16x16x32_bf16 v[92:95], v[52:55], a[128:131], v[92:95]\n"
"ds_write_addtid_b32 v99 offset:16800\n"
"v_add_f32 v143, v143, v147\n"
"v_mfma_f32_16x16x32_bf16 v[92:95], v[56:59], a[132:135], v[92:95]\n"
"v_add_f32 v140, v140, v148\n"
"v_add_f32 v141, v141, v149\n"
"v_mfma_f32_16x16x32_bf16 v[92:95], v[60:63], a[136:139], v[92:95]\n"
"v_add_f32 v142, v142, v150\n"
"v_add_f32 v143, v143, v151\n"
"v_mfma_f32_16x16x32_bf16 v[96:99], v[104:107], a[140:143], 0\n"
"v_add_f32 v140, v140, v112\n"
"v_add_f32 v141, v141, v113\n"
"v_mfma_f32_16x16x32_bf16 v[96:99], v[108:111], a[144:147], v[96:99]\n"
"ds_write_addtid_b32 v92 offset:3072\n"
"v_add_f32 v142, v142, v114\n"
"v_mfma_f32_16x16x32_bf16 v[96:99], v[44:47], a[148:151], v[96:99]\n"
"ds_write_addtid_b32 v93 offset:7712\n"
"v_add_f32 v143, v143, v115\n"
"v_mfma_f32_16x16x32_bf16 v[96:99], v[48:51], a[152:155], v[96:99]\n"
"ds_write_addtid_b32 v94 offset:12416\n"
"v_cvt_pk_bf16_f32 v100, v140, v141\n"
"v_mfma_f32_16x16x32_bf16 v[96:99], v[52:55], a[156:159], v[96:99]\n"
"ds_write_addtid_b32 v95 offset:17056\n"
"v_cvt_pk_bf16_f32 v101, v142, v143\n"
"v_mfma_f32_16x16x32_bf16 v[96:99], v[56:59], a[160:163], v[96:99]\n"
"s_waitcnt lgkmcnt(15)\n"
"v_add_f32 v152, v152, v156\n"
"v_add_f32 v153, v153, v157\n"
"v_mfma_f32_16x16x32_bf16 v[96:99], v[60:63], a[164:167], v[96:99]\n"
"v_add_f32 v154, v154, v158\n"
"v_add_f32 v155, v155, v159\n"
"v_mfma_f32_16x16x32_bf16 v[92:95], v[104:107], a[224:227], 0\n"
"v_add_f32 v152, v152, v160\n"
"v_add_f32 v153, v153, v161\n"
"v_mfma_f32_16x16x32_bf16 v[92:95], v[108:111], a[228:231], v[92:95]\n"
"ds_write_addtid_b32 v96 offset:3328\n"
"v_add_f32 v154, v154, v162\n"
"v_mfma_f32_16x16x32_bf16 v[92:95], v[44:47], a[232:235], v[92:95]\n"
"ds_write_addtid_b32 v97 offset:7968\n"
"v_add_f32 v155, v155, v163\n"
"v_mfma_f32_16x16x32_bf16 v[92:95], v[48:51], a[236:239], v[92:95]\n"
"ds_write_addtid_b32 v98 offset:12672\n"
"v_add_f32 v152, v152, v116\n"
"v_mfma_f32_16x16x32_bf16 v[92:95], v[52:55], a[240:243], v[92:95]\n"
"ds_write_addtid_b32 v99 offset:17312\n"
"v_add_f32 v153, v153, v117\n"
"v_mfma_f32_16x16x32_bf16 v[92:95], v[56:59], a[244:247], v[92:95]\n"
"v_add_f32 v154, v154, v118\n"
"v_add_f32 v155, v155, v119\n"
"v_mfma_f32_16x16x32_bf16 v[92:95], v[60:63], a[248:251], v[92:95]\n"
"v_cvt_pk_bf16_f32 v102, v152, v153\n"
"v_cvt_pk_bf16_f32 v103, v154, v155\n"
"v_mfma_f32_16x16x32_bf16 v[96:99], v[104:107], a[252:255], 0\n"
"buffer_store_dwordx4 v[100:103], v15, s[40:43], s93 offen sc1\n"
"v_mfma_f32_16x16x32_bf16 v[96:99], v[108:111], v[20:23], v[96:99]\n"
"ds_write_addtid_b32 v92 offset:3584\n"
"v_mfma_f32_16x16x32_bf16 v[96:99], v[44:47], v[24:27], v[96:99]\n"
"ds_write_addtid_b32 v93 offset:8224\n"
"v_mfma_f32_16x16x32_bf16 v[96:99], v[48:51], v[28:31], v[96:99]\n"
"ds_write_addtid_b32 v94 offset:12928\n"
"v_mfma_f32_16x16x32_bf16 v[96:99], v[52:55], v[32:35], v[96:99]\n"
"ds_write_addtid_b32 v95 offset:17568\n"
"v_mfma_f32_16x16x32_bf16 v[96:99], v[56:59], v[36:39], v[96:99]\n"
"v_mfma_f32_16x16x32_bf16 v[96:99], v[60:63], v[40:43], v[96:99]\n"
"s_add_u32 s58, s50, 0x180000\n"
"v_mfma_f32_16x16x32_bf16 v[100:103], a[168:171], v[104:107], 0\n"
"s_and_b32 s58, s58, 0x3ffffff\n"
"v_mfma_f32_16x16x32_bf16 v[100:103], a[172:175], v[108:111], v[100:103]\n"
"s_add_u32 s56, s40, s58\n"
"ds_write_addtid_b32 v96 offset:3840\n"
"v_mfma_f32_16x16x32_bf16 v[100:103], a[176:179], v[44:47], v[100:103]\n"
"s_addc_u32 s57, s41, 0\n"
"ds_write_addtid_b32 v97 offset:8480\n"
"v_mfma_f32_16x16x32_bf16 v[100:103], a[180:183], v[48:51], v[100:103]\n"
"s_waitcnt vmcnt(6)\n"
"ds_write_addtid_b32 v98 offset:13184\n"
"v_cmp_ne_u64 vcc, s[48:49], v[168:169]\n"
"v_mfma_f32_16x16x32_bf16 v[100:103], a[184:187], v[52:55], v[100:103]\n"
"ds_write_addtid_b32 v99 offset:17824\n"
"v_mfma_f32_16x16x32_bf16 v[100:103], a[188:191], v[56:59], v[100:103]\n"
"s_cbranch_vccnz .LG_4s\n"
".LG_4r:\n"
"v_mfma_f32_16x16x32_bf16 v[100:103], a[192:195], v[60:63], v[100:103]\n"
"s_add_u32 s50, s50, 0x180000\n"
"global_load_dwordx4 v[92:95], v5, s[56:57] offset:0 sc1\n"
"v_mfma_f32_16x16x32_bf16 v[112:115], a[196:199], v[104:107], 0\n"
"v_mfma_f32_16x16x32_bf16 v[112:115], a[200:203], v[108:111], v[112:115]\n"
"s_waitcnt lgkmcnt(0)\n"
"v_mfma_f32_16x16x32_bf16 v[112:115], a[204:207], v[44:47], v[112:115]\n"
"s_barrier\n"
"v_mfma_f32_16x16x32_bf16 v[112:115], a[208:211], v[48:51], v[112:115]\n"
"ds_read_b128 v[96:99], v2 offset:0\n"
"ds_read_b128 v[104:107], v3 offset:2560\n"
"v_mfma_f32_16x16x32_bf16 v[112:115], a[212:215], v[52:55], v[112:115]\n"
"ds_read_b128 v[108:111], v4 offset:1024\n"
"ds_read_b128 v[116:119], v2 offset:256\n"
"v_mfma_f32_16x16x32_bf16 v[112:115], a[216:219], v[56:59], v[112:115]\n"
"global_load_dwordx4 v[44:47], v6, s[56:57] offset:0 sc1\n"
"ds_read_b128 v[120:123], v3 offset:2816\n"
"v_mfma_f32_16x16x32_bf16 v[112:115], a[220:223], v[60:63], v[112:115]\n"
"ds_read_b128 v[124:127], v4 offset:1280\n"
"v_mfma_f32_16x16x32_bf16 v[56:59], v[64:67], a[0:3], 0\n"
"v_mfma_f32_16x16x32_bf16 v[56:59], v[68:71], a[4:7], v[56:59]\n"
"v_mfma_f32_16x16x32_bf16 v[56:59], v[72:75], a[8:11], v[56:59]\n"
"global_load_dwordx4 v[48:51], v7, s[56:57] offset:0 sc1\n"
"v_mfma_f32_16x16x32_bf16 v[56:59], v[76:79], a[12:15], v[56:59]\n"
"s_waitcnt vmcnt(7)\n"
"buffer_load_dwordx2 v[128:129], v12, s[40:43], s50 offen sc1\n"
"v_mfma_f32_16x16x32_bf16 v[56:59], v[80:83], a[16:19], v[56:59]\n"
"s_waitcnt lgkmcnt(3)\n"
"v_add_f32 v96, v96, v104\n"
"v_add_f32 v97, v97, v105\n"
"v_mfma_f32_16x16x32_bf16 v[56:59], v[84:87], a[20:23], v[56:59]\n"
"s_waitcnt vmcnt(7)\n"
"v_add_f32 v98, v98, v106\n"
"v_add_f32 v99, v99, v107\n"
"v_mfma_f32_16x16x32_bf16 v[56:59], v[88:91], a[24:27], v[56:59]\n"
"v_add_f32 v96, v96, v108\n"
"v_add_f32 v97, v97, v109\n"
"v_mfma_f32_16x16x32_bf16 v[104:107], v[64:67], a[28:31], 0\n"
"global_load_dwordx4 v[52:55], v8, s[56:57] offset:0 sc1\n"
"v_add_f32 v98, v98, v110\n"
"v_mfma_f32_16x16x32_bf16 v[104:107], v[68:71], a[32:35], v[104:107]\n"
"ds_write_addtid_b32 v56 offset:0\n"
"v_add_f32 v99, v99, v111\n"
"v_mfma_f32_16x16x32_bf16 v[104:107], v[72:75], a[36:39], v[104:107]\n"
"ds_write_addtid_b32 v57 offset:4640\n"
"v_add_f32 v96, v96, v100\n"
"v_mfma_f32_16x16x32_bf16 v[104:107], v[76:79], a[40:43], v[104:107]\n"
"ds_write_addtid_b32 v58 offset:9344\n"
"v_add_f32 v97, v97, v101\n"
"v_mfma_f32_16x16x32_bf16 v[104:107], v[80:83], a[44:47], v[104:107]\n"
"ds_write_addtid_b32 v59 offset:13984\n"
"v_add_f32 v98, v98, v102\n"
"v_mfma_f32_16x16x32_bf16 v[104:107], v[84:87], a[48:51], v[104:107]\n"
"v_add_f32 v99, v99, v103\n"
"v_mfma_f32_16x16x32_bf16 v[104:107], v[88:91], a[52:55], v[104:107]\n"
"s_add_u32 s93, s50, 0xffd00000\n"
"s_and_b32 s95, s93, 0xfbffffff\n"
"global_load_dwordx4 v[56:59], v9, s[56:57] offset:0 sc1\n"
"v_mfma_f32_16x16x32_bf16 v[100:103], v[64:67], a[56:59], 0\n"
"v_cvt_pk_bf16_f32 v108, v96, v97\n"
"v_cvt_pk_bf16_f32 v109, v98, v99\n"
"v_mfma_f32_16x16x32_bf16 v[100:103], v[68:71], a[60:63], v[100:103]\n"
"s_waitcnt lgkmcnt(4)\n"
"ds_write_addtid_b32 v104 offset:256\n"
"v_add_f32 v116, v116, v120\n"
"v_mfma_f32_16x16x32_bf16 v[100:103], v[72:75], a[64:67], v[100:103]\n"
"ds_write_addtid_b32 v105 offset:4896\n"
"v_add_f32 v117, v117, v121\n"
"v_mfma_f32_16x16x32_bf16 v[100:103], v[76:79], a[68:71], v[100:103]\n"
"ds_write_addtid_b32 v106 offset:9600\n"
"global_load_dwordx4 v[60:63], v10, s[56:57] offset:0 sc1\n"
"v_mfma_f32_16x16x32_bf16 v[100:103], v[80:83], a[72:75], v[100:103]\n"
"ds_write_addtid_b32 v107 offset:14240\n"
"v_add_f32 v118, v118, v122\n"
"v_mfma_f32_16x16x32_bf16 v[100:103], v[84:87], a[76:79], v[100:103]\n"
"v_add_f32 v119, v119, v123\n"
"v_add_f32 v116, v116, v124\n"
"v_mfma_f32_16x16x32_bf16 v[100:103], v[88:91], a[80:83], v[100:103]\n"
"v_add_f32 v117, v117, v125\n"
"v_add_f32 v118, v118, v126\n"
"v_mfma_f32_16x16x32_bf16 v[104:107], v[64:67], a[84:87], 0\n"
"v_add_f32 v119, v119, v127\n"
"v_add_f32 v116, v116, v112\n"
"v_mfma_f32_16x16x32_bf16 v[104:107], v[68:71], a[88:91], v[104:107]\n"
"ds_write_addtid_b32 v100 offset:512\n"
"v_add_f32 v117, v117, v113\n"
"v_mfma_f32_16x16x32_bf16 v[104:107], v[72:75], a[92:95], v[104:107]\n"
"ds_write_addtid_b32 v101 offset:5152\n"
"global_load_dwordx4 v[96:99], v11, s[56:57] offset:0 sc1\n"
"v_mfma_f32_16x16x32_bf16 v[104:107], v[76:79], a[96:99], v[104:107]\n"
"ds_write_addtid_b32 v102 offset:9856\n"
"v_add_f32 v118, v118, v114\n"
"v_mfma_f32_16x16x32_bf16 v[104:107], v[80:83], a[100:103], v[104:107]\n"
"ds_write_addtid_b32 v103 offset:14496\n"
"v_add_f32 v119, v119, v115\n"
"v_mfma_f32_16x16x32_bf16 v[104:107], v[84:87], a[104:107], v[104:107]\n"
"v_cvt_pk_bf16_f32 v110, v116, v117\n"
"v_cvt_pk_bf16_f32 v111, v118, v119\n"
"v_mfma_f32_16x16x32_bf16 v[104:107], v[88:91], a[108:111], v[104:107]\n"
"s_waitcnt vmcnt(8)\n"
"buffer_store_dwordx2 v[0:1], v16, s[40:43], s94 offen offset:0 sc1\n"
"v_mfma_f32_16x16x32_bf16 v[100:103], v[64:67], a[112:115], 0\n"
"v_mfma_f32_16x16x32_bf16 v[100:103], v[68:71], a[116:119], v[100:103]\n"
"ds_write_addtid_b32 v104 offset:768\n"
"v_mfma_f32_16x16x32_bf16 v[100:103], v[72:75], a[120:123], v[100:103]\n"
"buffer_store_dwordx4 v[108:111], v13, s[40:43], s95 offen sc1\n"
"ds_write_addtid_b32 v105 offset:5408\n"
"v_mfma_f32_16x16x32_bf16 v[100:103], v[76:79], a[124:127], v[100:103]\n"
"ds_write_addtid_b32 v106 offset:10112\n"
"v_mfma_f32_16x16x32_bf16 v[100:103], v[80:83], a[128:131], v[100:103]\n"
"ds_write_addtid_b32 v107 offset:14752\n"
"v_mfma_f32_16x16x32_bf16 v[100:103], v[84:87], a[132:135], v[100:103]\n"
"v_mfma_f32_16x16x32_bf16 v[100:103], v[88:91], a[136:139], v[100:103]\n"
"v_mfma_f32_16x16x32_bf16 v[108:111], v[64:67], a[140:143], 0\n"
"v_mfma_f32_16x16x32_bf16 v[108:111], v[68:71], a[144:147], v[108:111]\n"
"ds_write_addtid_b32 v100 offset:1024\n"
"v_mfma_f32_16x16x32_bf16 v[108:111], v[72:75], a[148:151], v[108:111]\n"
"ds_write_addtid_b32 v101 offset:5664\n"
"v_mfma_f32_16x16x32_bf16 v[108:111], v[76:79], a[152:155], v[108:111]\n"
"ds_write_addtid_b32 v102 offset:10368\n"
"v_mfma_f32_16x16x32_bf16 v[108:111], v[80:83], a[156:159], v[108:111]\n"
"ds_write_addtid_b32 v103 offset:15008\n"
"v_mfma_f32_16x16x32_bf16 v[108:111], v[84:87], a[160:163], v[108:111]\n"
"v_mfma_f32_16x16x32_bf16 v[108:111], v[88:91], a[164:167], v[108:111]\n"
"v_mfma_f32_16x16x32_bf16 v[104:107], v[64:67], a[224:227], 0\n"
"v_mfma_f32_16x16x32_bf16 v[104:107], v[68:71], a[228:231], v[104:107]\n"
"ds_write_addtid_b32 v108 offset:1280\n"
"v_mfma_f32_16x16x32_bf16 v[104:107], v[72:75], a[232:235], v[104:107]\n"
"ds_write_addtid_b32 v109 offset:5920\n"
"v_mfma_f32_16x16x32_bf16 v[104:107], v[76:79], a[236:239], v[104:107]\n"
"ds_write_addtid_b32 v110 offset:10624\n"
"v_mfma_f32_16x16x32_bf16 v[104:107], v[80:83], a[240:243], v[104:107]\n"
"ds_write_addtid_b32 v111 offset:15264\n"
"v_mfma_f32_16x16x32_bf16 v[104:107], v[84:87], a[244:247], v[104:107]\n"
"v_mfma_f32_16x16x32_bf16 v[104:107], v[88:91], a[248:251], v[104:107]\n"
"v_mfma_f32_16x16x32_bf16 v[112:115], v[64:67], a[252:255], 0\n"
"v_mfma_f32_16x16x32_bf16 v[112:115], v[68:71], v[20:23], v[112:115]\n"
"s_add_u32 s58, s50, 0x180000\n"
"ds_write_addtid_b32 v104 offset:1536\n"
"v_mfma_f32_16x16x32_bf16 v[112:115], v[72:75], v[24:27], v[112:115]\n"
"s_and_b32 s58, s58, 0x3ffffff\n"
"ds_write_addtid_b32 v105 offset:6176\n"
"v_mfma_f32_16x16x32_bf16 v[112:115], v[76:79], v[28:31], v[112:115]\n"
"s_add_u32 s56, s40, s58\n"
"ds_write_addtid_b32 v106 offset:10880\n"
"v_mfma_f32_16x16x32_bf16 v[112:115], v[80:83], v[32:35], v[112:115]\n"
"s_addc_u32 s57, s41, 0\n"
"ds_write_addtid_b32 v107 offset:15520\n"
"v_mfma_f32_16x16x32_bf16 v[112:115], v[84:87], v[36:39], v[112:115]\n"
"s_waitcnt vmcnt(6)\n"
"v_cmp_ne_u64 vcc, s[48:49], v[128:129]\n"
"v_mfma_f32_16x16x32_bf16 v[112:115], v[88:91], v[40:43], v[112:115]\n"
"v_mfma_f32_16x16x32_bf16 v[104:107], a[168:171], v[64:67], 0\n"
"s_cbranch_vccnz .LG_1s\n"
".LG_1r:\n"
"v_mfma_f32_16x16x32_bf16 v[104:107], a[172:175], v[68:71], v[104:107]\n"
"s_add_u32 s50, s50, 0x180000\n"
"ds_write_addtid_b32 v112 offset:1792\n"
"v_mfma_f32_16x16x32_bf16 v[104:107], a[176:179], v[72:75], v[104:107]\n"
"ds_write_addtid_b32 v113 offset:6432\n"
"v_mfma_f32_16x16x32_bf16 v[104:107], a[180:183], v[76:79], v[104:107]\n"
"ds_write_addtid_b32 v114 offset:11136\n"
"v_mfma_f32_16x16x32_bf16 v[104:107], a[184:187], v[80:83], v[104:107]\n"
"ds_write_addtid_b32 v115 offset:15776\n"
"global_load_dwordx4 v[100:103], v5, s[56:57] offset:0 sc1\n"
"v_mfma_f32_16x16x32_bf16 v[104:107], a[188:191], v[84:87], v[104:107]\n"
"v_mfma_f32_16x16x32_bf16 v[104:107], a[192:195], v[88:91], v[104:107]\n"
"v_mfma_f32_16x16x32_bf16 v[108:111], a[196:199], v[64:67], 0\n"
"v_mfma_f32_16x16x32_bf16 v[108:111], a[200:203], v[68:71], v[108:111]\n"
"s_waitcnt lgkmcnt(0)\n"
"v_mfma_f32_16x16x32_bf16 v[108:111], a[204:207], v[72:75], v[108:111]\n"
"s_barrier\n"
"v_mfma_f32_16x16x32_bf16 v[108:111], a[208:211], v[76:79], v[108:111]\n"
"global_load_dwordx4 v[68:71], v6, s[56:57] offset:0 sc1\n"
"ds_read_b128 v[64:67], v2 offset:2048\n"
"v_mfma_f32_16x16x32_bf16 v[108:111], a[212:215], v[80:83], v[108:111]\n"
"ds_read_b128 v[112:115], v3 offset:512\n"
"ds_read_b128 v[116:119], v4 offset:3072\n"
"v_mfma_f32_16x16x32_bf16 v[108:111], a[216:219], v[84:87], v[108:111]\n"
"ds_read_b128 v[120:123], v2 offset:2304\n"
"ds_read_b128 v[124:127], v3 offset:768\n"
"v_mfma_f32_16x16x32_bf16 v[108:111], a[220:223], v[88:91], v[108:111]\n"
"ds_read_b128 v[128:131], v4 offset:3328\n"
"v_mfma_f32_16x16x32_bf16 v[84:87], v[92:95], a[0:3], 0\n"
"global_load_dwordx4 v[72:75], v7, s[56:57] offset:0 sc1\n"
"v_mfma_f32_16x16x32_bf16 v[84:87], v[44:47], a[4:7], v[84:87]\n"
"s_waitcnt vmcnt(8)\n"
"v_mfma_f32_16x16x32_bf16 v[84:87], v[48:51], a[8:11], v[84:87]\n"
"v_mfma_f32_16x16x32_bf16 v[84:87], v[52:55], a[12:15], v[84:87]\n"
"s_waitcnt vmcnt(6)\n"
"v_mfma_f32_16x16x32_bf16 v[84:87], v[56:59], a[16:19], v[84:87]\n"
"s_waitcnt lgkmcnt(3)\n"
"v_add_f32 v64, v64, v112\n"
"v_add_f32 v65, v65, v113\n"
"v_mfma_f32_16x16x32_bf16 v[84:87], v[60:63], a[20:23], v[84:87]\n"
"s_waitcnt vmcnt(5)\n"
"global_load_dwordx4 v[76:79], v8, s[56:57] offset:0 sc1\n"
"v_add_f32 v66, v66, v114\n"
"v_mfma_f32_16x16x32_bf16 v[84:87], v[96:99], a[24:27], v[84:87]\n"
"buffer_load_dwordx2 v[132:133], v12, s[40:43], s50 offen sc1\n"
"v_mfma_f32_16x16x32_bf16 v[88:91], v[92:95], a[28:31], 0\n"
"v_add_f32 v67, v67, v115\n"
"v_add_f32 v64, v64, v116\n"
"v_mfma_f32_16x16x32_bf16 v[88:91], v[44:47], a[32:35], v[88:91]\n"
"s_waitcnt vmcnt(5)\n"
"buffer_store_dwordx2 v[0:1], v14, s[40:43], s93 offen offset:0 sc1\n"
"ds_write_addtid_b32 v84 offset:2048\n"
"v_mfma_f32_16x16x32_bf16 v[88:91], v[48:51], a[36:39], v[88:91]\n"
"ds_write_addtid_b32 v85 offset:6688\n"
"v_add_f32 v65, v65, v117\n"
"v_mfma_f32_16x16x32_bf16 v[88:91], v[52:55], a[40:43], v[88:91]\n"
"ds_write_addtid_b32 v86 offset:11392\n"
"global_load_dwordx4 v[80:83], v9, s[56:57] offset:0 sc1\n"
"v_mfma_f32_16x16x32_bf16 v[88:91], v[56:59], a[44:47], v[88:91]\n"
"ds_write_addtid_b32 v87 offset:16032\n"
"v_add_f32 v66, v66, v118\n"
"v_mfma_f32_16x16x32_bf16 v[88:91], v[60:63], a[48:51], v[88:91]\n"
"v_add_f32 v67, v67, v119\n"
"v_add_f32 v64, v64, v104\n"
"v_mfma_f32_16x16x32_bf16 v[88:91], v[96:99], a[52:55], v[88:91]\n"
"v_add_f32 v65, v65, v105\n"
"v_add_f32 v66, v66, v106\n"
"v_mfma_f32_16x16x32_bf16 v[112:115], v[92:95], a[56:59], 0\n"
"v_add_f32 v67, v67, v107\n"
"v_mfma_f32_16x16x32_bf16 v[112:115], v[44:47], a[60:63], v[112:115]\n"
"ds_write_addtid_b32 v88 offset:2304\n"
"global_load_dwordx4 v[84:87], v10, s[56:57] offset:0 sc1\n"
"v_mfma_f32_16x16x32_bf16 v[112:115], v[48:51], a[64:67], v[112:115]\n"
"s_add_u32 s93, s50, 0xffd00000\n"
"s_and_b32 s95, s93, 0xfbffffff\n"
"ds_write_addtid_b32 v89 offset:6944\n"
"v_mfma_f32_16x16x32_bf16 v[112:115], v[52:55], a[68:71], v[112:115]\n"
"ds_write_addtid_b32 v90 offset:11648\n"
"v_cvt_pk_bf16_f32 v104, v64, v65\n"
"v_mfma_f32_16x16x32_bf16 v[112:115], v[56:59], a[72:75], v[112:115]\n"
"ds_write_addtid_b32 v91 offset:16288\n"
"v_cvt_pk_bf16_f32 v105, v66, v67\n"
"v_mfma_f32_16x16x32_bf16 v[112:115], v[60:63], a[76:79], v[112:115]\n"
"s_waitcnt lgkmcnt(8)\n"
"v_add_f32 v120, v120, v124\n"
"v_add_f32 v121, v121, v125\n"
"v_mfma_f32_16x16x32_bf16 v[112:115], v[96:99], a[80:83], v[112:115]\n"
"global_load_dwordx4 v[88:91], v11, s[56:57] offset:0 sc1\n"
"v_add_f32 v122, v122, v126\n"
"v_mfma_f32_16x16x32_bf16 v[64:67], v[92:95], a[84:87], 0\n"
"v_add_f32 v123, v123, v127\n"
"v_add_f32 v120, v120, v128\n"
"v_mfma_f32_16x16x32_bf16 v[64:67], v[44:47], a[88:91], v[64:67]\n"
"ds_write_addtid_b32 v112 offset:2560\n"
"v_add_f32 v121, v121, v129\n"
"v_mfma_f32_16x16x32_bf16 v[64:67], v[48:51], a[92:95], v[64:67]\n"
"ds_write_addtid_b32 v113 offset:7200\n"
"v_add_f32 v122, v122, v130\n"
"v_mfma_f32_16x16x32_bf16 v[64:67], v[52:55], a[96:99], v[64:67]\n"
"ds_write_addtid_b32 v114 offset:11904\n"
"v_add_f32 v123, v123, v131\n"
"v_mfma_f32_16x16x32_bf16 v[64:67], v[56:59], a[100:103], v[64:67]\n"
"ds_write_addtid_b32 v115 offset:16544\n"
"v_add_f32 v120, v120, v108\n"
"v_mfma_f32_16x16x32_bf16 v[64:67], v[60:63], a[104:107], v[64:67]\n"
"v_add_f32 v121, v121, v109\n"
"v_add_f32 v122, v122, v110\n"
"v_mfma_f32_16x16x32_bf16 v[64:67], v[96:99], a[108:111], v[64:67]\n"
"v_add_f32 v123, v123, v111\n"
"v_cvt_pk_bf16_f32 v106, v120, v121\n"
"v_mfma_f32_16x16x32_bf16 v[112:115], v[92:95], a[112:115], 0\n"
"v_cvt_pk_bf16_f32 v107, v122, v123\n"
"v_mfma_f32_16x16x32_bf16 v[112:115], v[44:47], a[116:119], v[112:115]\n"
"ds_write_addtid_b32 v64 offset:2816\n"
"v_mfma_f32_16x16x32_bf16 v[112:115], v[48:51], a[120:123], v[112:115]\n"
"ds_write_addtid_b32 v65 offset:7456\n"
"v_mfma_f32_16x16x32_bf16 v[112:115], v[52:55], a[124:127], v[112:115]\n"
"ds_write_addtid_b32 v66 offset:12160\n"
"v_mfma_f32_16x16x32_bf16 v[112:115], v[56:59], a[128:131], v[112:115]\n"
"ds_write_addtid_b32 v67 offset:16800\n"
"v_mfma_f32_16x16x32_bf16 v[112:115], v[60:63], a[132:135], v[112:115]\n"
"v_mfma_f32_16x16x32_bf16 v[112:115], v[96:99], a[136:139], v[112:115]\n"
"v_mfma_f32_16x16x32_bf16 v[108:111], v[92:95], a[140:143], 0\n"
"v_mfma_f32_16x16x32_bf16 v[108:111], v[44:47], a[144:147], v[108:111]\n"
"buffer_store_dwordx4 v[104:107], v13, s[40:43], s95 offen sc1\n"
"ds_write_addtid_b32 v112 offset:3072\n"
"v_mfma_f32_16x16x32_bf16 v[108:111], v[48:51], a[148:151], v[108:111]\n"
"ds_write_addtid_b32 v113 offset:7712\n"
"v_mfma_f32_16x16x32_bf16 v[108:111], v[52:55], a[152:155], v[108:111]\n"
"ds_write_addtid_b32 v114 offset:12416\n"
"v_mfma_f32_16x16x32_bf16 v[108:111], v[56:59], a[156:159], v[108:111]\n"
"ds_write_addtid_b32 v115 offset:17056\n"
"v_mfma_f32_16x16x32_bf16 v[108:111], v[60:63], a[160:163], v[108:111]\n"
"v_mfma_f32_16x16x32_bf16 v[108:111], v[96:99], a[164:167], v[108:111]\n"
"v_mfma_f32_16x16x32_bf16 v[64:67], v[92:95], a[224:227], 0\n"
"v_mfma_f32_16x16x32_bf16 v[64:67], v[44:47], a[228:231], v[64:67]\n"
"ds_write_addtid_b32 v108 offset:3328\n"
"v_mfma_f32_16x16x32_bf16 v[64:67], v[48:51], a[232:235], v[64:67]\n"
"ds_write_addtid_b32 v109 offset:7968\n"
"v_mfma_f32_16x16x32_bf16 v[64:67], v[52:55], a[236:239], v[64:67]\n"
"ds_write_addtid_b32 v110 offset:12672\n"
"v_mfma_f32_16x16x32_bf16 v[64:67], v[56:59], a[240:243], v[64:67]\n"
"ds_write_addtid_b32 v111 offset:17312\n"
"v_mfma_f32_16x16x32_bf16 v[64:67], v[60:63], a[244:247], v[64:67]\n"
"v_mfma_f32_16x16x32_bf16 v[64:67], v[96:99], a[248:251], v[64:67]\n"
"v_mfma_f32_16x16x32_bf16 v[112:115], v[92:95], a[252:255], 0\n"
"v_mfma_f32_16x16x32_bf16 v[112:115], v[44:47], v[20:23], v[112:115]\n"
"s_add_u32 s58, s50, 0x180000\n"
"ds_write_addtid_b32 v64 offset:3584\n"
"v_mfma_f32_16x16x32_bf16 v[112:115], v[48:51], v[24:27], v[112:115]\n"
"s_and_b32 s58, s58, 0x3ffffff\n"
"ds_write_addtid_b32 v65 offset:8224\n"
"v_mfma_f32_16x16x32_bf16 v[112:115], v[52:55], v[28:31], v[112:115]\n"
"s_add_u32 s56, s40, s58\n"
"ds_write_addtid_b32 v66 offset:12928\n"
"v_mfma_f32_16x16x32_bf16 v[112:115], v[56:59], v[32:35], v[112:115]\n"
"s_addc_u32 s57, s41, 0\n"
"ds_write_addtid_b32 v67 offset:17568\n"
"v_mfma_f32_16x16x32_bf16 v[112:115], v[60:63], v[36:39], v[112:115]\n"
"s_waitcnt vmcnt(5)\n"
"v_cmp_ne_u64 vcc, s[48:49], v[132:133]\n"
"v_mfma_f32_16x16x32_bf16 v[112:115], v[96:99], v[40:43], v[112:115]\n"
"v_mfma_f32_16x16x32_bf16 v[116:119], a[168:171], v[92:95], 0\n"
"s_cbranch_vccnz .LG_2s\n"
".LG_2r:\n"
"v_mfma_f32_16x16x32_bf16 v[116:119], a[172:175], v[44:47], v[116:119]\n"
"s_add_u32 s50, s50, 0x180000\n"
"ds_write_addtid_b32 v112 offset:3840\n"
"v_mfma_f32_16x16x32_bf16 v[116:119], a[176:179], v[48:51], v[116:119]\n"
"ds_write_addtid_b32 v113 offset:8480\n"
"v_mfma_f32_16x16x32_bf16 v[116:119], a[180:183], v[52:55], v[116:119]\n"
"ds_write_addtid_b32 v114 offset:13184\n"
"v_mfma_f32_16x16x32_bf16 v[116:119], a[184:187], v[56:59], v[116:119]\n"
"ds_write_addtid_b32 v115 offset:17824\n"
"v_mfma_f32_16x16x32_bf16 v[116:119], a[188:191], v[60:63], v[116:119]\n"
"global_load_dwordx4 v[104:107], v5, s[56:57] offset:0 sc1\n"
"v_mfma_f32_16x16x32_bf16 v[116:119], a[192:195], v[96:99], v[116:119]\n"
"v_mfma_f32_16x16x32_bf16 v[112:115], a[196:199], v[92:95], 0\n"
"v_mfma_f32_16x16x32_bf16 v[112:115], a[200:203], v[44:47], v[112:115]\n"
"s_waitcnt lgkmcnt(0)\n"
"global_load_dwordx4 v[108:111], v6, s[56:57] offset:0 sc1\n"
"v_mfma_f32_16x16x32_bf16 v[112:115], a[204:207], v[48:51], v[112:115]\n"
"s_barrier\n"
"v_mfma_f32_16x16x32_bf16 v[112:115], a[208:211], v[52:55], v[112:115]\n"
"ds_read_b128 v[120:123], v2 offset:0\n"
"ds_read_b128 v[64:67], v3 offset:2560\n"
"v_mfma_f32_16x16x32_bf16 v[112:115], a[212:215], v[56:59], v[112:115]\n"
"ds_read_b128 v[92:95], v4 offset:1024\n"
"ds_read_b128 v[124:127], v2 offset:256\n"
"v_mfma_f32_16x16x32_bf16 v[112:115], a[216:219], v[60:63], v[112:115]\n"
"ds_read_b128 v[128:131], v3 offset:2816\n"
"ds_read_b128 v[132:135], v4 offset:1280\n"
"v_mfma_f32_16x16x32_bf16 v[112:115], a[220:223], v[96:99], v[112:115]\n"
"s_branch .LG_top\n"
".LG_1s:\n"
"s_cmp_ge_u32 s50, s53\n"
"s_cbranch_scc1 .LG_end\n"
"s_cmp_ge_u32 s50, s52\n"
"s_cbranch_scc1 .LG_1r\n"
"s_sub_u32 s60, s50, 0x0\n"
"s_mov_b32 s61, 200000\n"
"s_nop 7\n"
".LG_1p:\n"
"buffer_load_dwordx2 v[128:129], v12, s[40:43], s60 offen sc1\n"
"s_waitcnt vmcnt(0)\n"
"v_cmp_ne_u64 vcc, s[48:49], v[128:129]\n"
"s_cbranch_vccz .LG_1px\n"
"s_sleep 1\n"
"s_sub_u32 s61, s61, 1\n"
"s_cbranch_scc0 .LG_1p\n"
"s_branch .LG_end\n"
".LG_1px:\n"
"s_branch .LG_1r\n"
".LG_2s:\n"
"s_cmp_ge_u32 s50, s53\n"
"s_cbranch_scc1 .LG_end\n"
"s_cmp_ge_u32 s50, s52\n"
"s_cbranch_scc1 .LG_2r\n"
"s_sub_u32 s60, s50, 0x0\n"
"s_mov_b32 s61, 200000\n"
"s_nop 7\n"
".LG_2p:\n"
"buffer_load_dwordx2 v[132:133], v12, s[40:43], s60 offen sc1\n"
"s_waitcnt vmcnt(0)\n"
"v_cmp_ne_u64 vcc, s[48:49], v[132:133]\n"
"s_cbranch_vccz .LG_2px\n"
"s_sleep 1\n"
"s_sub_u32 s61, s61, 1\n"
"s_cbranch_scc0 .LG_2p\n"
"s_branch .LG_end\n"
".LG_2px:\n"
"s_branch .LG_2r\n"
".LG_3s:\n"
"s_cmp_ge_u32 s50, s53\n"
"s_cbranch_scc1 .LG_end\n"
"s_cmp_ge_u32 s50, s52\n"
"s_cbranch_scc1 .LG_3r\n"
"s_sub_u32 s60, s50, 0x0\n"
"s_mov_b32 s61, 200000\n"
"s_nop 7\n"
".LG_3p:\n"
"buffer_load_dwordx2 v[136:137], v12, s[40:43], s60 offen sc1\n"
"s_waitcnt vmcnt(0)\n"
"v_cmp_ne_u64 vcc, s[48:49], v[136:137]\n"
"s_cbranch_vccz .LG_3px\n"
"s_sleep 1\n"
"s_sub_u32 s61, s61, 1\n"
"s_cbranch_scc0 .LG_3p\n"
"s_branch .LG_end\n"
".LG_3px:\n"
"s_branch .LG_3r\n"
".LG_4s:\n"
"s_cmp_ge_u32 s50, s53\n"
"s_cbranch_scc1 .LG_end\n"
"s_cmp_ge_u32 s50, s52\n"
"s_cbranch_scc1 .LG_4r\n"
"s_sub_u32 s60, s50, 0x0\n"
"s_mov_b32 s61, 200000\n"
"s_nop 7\n"
".LG_4p:\n"
"buffer_load_dwordx2 v[168:169], v12, s[40:43], s60 offen sc1\n"
"s_waitcnt vmcnt(0)\n"
"v_cmp_ne_u64 vcc, s[48:49], v[168:169]\n"
"s_cbranch_vccz .LG_4px\n"
"s_sleep 1\n"
"s_sub_u32 s61, s61, 1\n"
"s_cbranch_scc0 .LG_4p\n"
"s_branch .LG_end\n"
".LG_4px:\n"
"s_branch .LG_4r\n"
".LG_end:\n"
"s_waitcnt vmcnt(0) lgkmcnt(0)\n"
"s_endpgm\n"
      :: "{v2}"((unsigned)(lds0 + ((wid + 3) & 3) * 18592 + trp)), "{v3}"((unsigned)(lds0 + ((wid + 2) & 3) * 18592 + trp)), "{v4}"((unsigned)(lds0 + ((wid + 1) & 3) * 18592 + trp)), "{v5}"((unsigned)(lane * 16 + f0 * 1024 + ((0 + rot) % 7) * 1024 + 134316032)), "{v6}"((unsigned)(lane * 16 + f0 * 1024 + ((1 + rot) % 7) * 1024 + 134316032)), "{v7}"((unsigned)(lane * 16 + f0 * 1024 + ((2 + rot) % 7) * 1024 + 134316032)), "{v8}"((unsigned)(lane * 16 + f0 * 1024 + ((3 + rot) % 7) * 1024 + 134316032)), "{v9}"((unsigned)(lane * 16 + f0 * 1024 + ((4 + rot) % 7) * 1024 + 134316032)), "{v10}"((unsigned)(lane * 16 + f0 * 1024 + ((5 + rot) % 7) * 1024 + 134316032)), "{v11}"((unsigned)(lane * 16 + f0 * 1024 + ((6 + rot) % 7) * 1024 + 134316032)), "{v12}"((unsigned)((f0 + lane % 7) * 8 + kq * 32 + 353024 + G_GL * 1572864)), "{v13}"((unsigned)(lane * 16 + pair * 4096 + kq * 1024 + 212992)), "{v14}"((unsigned)(lane ? DROP : pair * 64 + kq * 8 + 354304)), "{v15}"((unsigned)(lane * 16 + (pg0 + 4) * 4096 + kq * 1024 + 212992)), "{v16}"((unsigned)(lane > 1 ? DROP : (lane ? pg0 + 4 : pair) * 64 + kq * 8 + 354304)), "{v44}"((unsigned)(vT + tsw)), "{v45}"((unsigned)(vT + (tsw ^ 8))), "{v46}"((unsigned)(vD + ((0 + rot) % 7) * 65536)), "{v47}"((unsigned)(vD + ((1 + rot) % 7) * 65536)), "{v48}"((unsigned)(vD + ((2 + rot) % 7) * 65536)), "{v49}"((unsigned)(vD + ((3 + rot) % 7) * 65536)), "{v50}"((unsigned)(vD + ((4 + rot) % 7) * 65536)), "{v51}"((unsigned)(vD + ((5 + rot) % 7) * 65536)), "{v52}"((unsigned)(vD + ((6 + rot) % 7) * 65536)), "{s[40:43]}"(rS), "{s[44:47]}"(rW), "{s[48:49]}"(wsp), "{s50}"((unsigned)(sA)), "{s51}"((unsigned)(sLo)), "{s52}"((unsigned)(sHi)), "{s53}"((unsigned)(sEx)), "{s54}"((unsigned)(sEnt)), "{s55}"((unsigned)(sLd)), "{s[56:57]}"(wsb + (sLo & 0x3ffffff)), "{s59}"((unsigned)(lds0 + wid * 18592)), "{s62}"((unsigned)(sW0 + 64 * ((wid + 1) & 3))), "{s63}"((unsigned)(sW0 + 64 * ((wid + 2) & 3))), "{s64}"((unsigned)(sW0 + 64 * ((wid + 3) & 3))), "{s65}"((unsigned)(sW0 + 64 * ((wid + 4) & 3))), "{s66}"((unsigned)(sW0 + 256))
      : "v255", "a255", "s95", "vcc", "scc", "memory");

    } else {
      const int v16 = kq * 4 + wid;
      const unsigned sEx = sHi + (J_EX + (set == 1)) * 3 * RS, sEnt = grp - 4;
      const u32x4 rO = desc(a.out, 2048 * 2048), bia0 = *(const u32x4*)(a.b2 + v16 * (32 << JJ) + lg * 8), bia1 = *(const u32x4*)(a.b2 + (JJ ? 32 : 512) + v16 * (32 << JJ) + lg * 8);
      const unsigned se = lg == (li >> 2) ? 0x3f80u << (16 * (li & 1)) : 0u, sel0 = (li & 2) ? 0u : se, sel1 = (li & 2) ? se : 0u;
      const u64 ep = (set == 2 && idx == 27 && wid == 0) ? (u64)a.ws : 0ull;
    asm volatile(
"s_nop 4\n"
"v_mov_b32 v186, 0\n"
"global_load_dwordx2 v[0:1], v186, s[48:49] sc1\n"
"v_mov_b32 v188, 0x3f803f80\n"
"v_mov_b32 v189, 0x3f803f80\n"
"v_mov_b32 v190, 0x3f803f80\n"
"v_mov_b32 v191, 0x3f803f80\n"
"s_add_u32 m0, s55, 0\n"
"s_add_u32 s60, s86, 0\n"
"buffer_load_dwordx4 v46, s[44:47], s60 offen lds\n"
"s_add_u32 m0, s55, 1024\n"
"s_add_u32 s60, s86, 32768\n"
"buffer_load_dwordx4 v46, s[44:47], s60 offen lds\n"
"s_add_u32 m0, s55, 2048\n"
"s_add_u32 s60, s87, 0\n"
"buffer_load_dwordx4 v46, s[44:47], s60 offen lds\n"
"s_add_u32 m0, s55, 3072\n"
"s_add_u32 s60, s87, 32768\n"
"buffer_load_dwordx4 v46, s[44:47], s60 offen lds\n"
"s_add_u32 m0, s55, 4096\n"
"s_add_u32 s60, s88, 0\n"
"buffer_load_dwordx4 v46, s[44:47], s60 offen lds\n"
"s_add_u32 m0, s55, 5120\n"
"s_add_u32 s60, s88, 32768\n"
"buffer_load_dwordx4 v46, s[44:47], s60 offen lds\n"
"s_add_u32 m0, s55, 6144\n"
"s_add_u32 s60, s89, 0\n"
"buffer_load_dwordx4 v46, s[44:47], s60 offen lds\n"
"s_add_u32 m0, s55, 7168\n"
"s_add_u32 s60, s89, 32768\n"
"buffer_load_dwordx4 v46, s[44:47], s60 offen lds\n"
"s_add_u32 m0, s55, 8192\n"
"s_add_u32 s60, s86, 0\n"
"buffer_load_dwordx4 v47, s[44:47], s60 offen lds\n"
"s_add_u32 m0, s55, 9216\n"
"s_add_u32 s60, s86, 32768\n"
"buffer_load_dwordx4 v47, s[44:47], s60 offen lds\n"
"s_add_u32 m0, s55, 10240\n"
"s_add_u32 s60, s87, 0\n"
"buffer_load_dwordx4 v47, s[44:47], s60 offen lds\n"
"s_add_u32 m0, s55, 11264\n"
"s_add_u32 s60, s87, 32768\n"
"buffer_load_dwordx4 v47, s[44:47], s60 offen lds\n"
"s_add_u32 m0, s55, 12288\n"
"s_add_u32 s60, s88, 0\n"
"buffer_load_dwordx4 v47, s[44:47], s60 offen lds\n"
"s_add_u32 m0, s55, 13312\n"
"s_add_u32 s60, s88, 32768\n"
"buffer_load_dwordx4 v47, s[44:47], s60 offen lds\n"
"s_add_u32 m0, s55, 14336\n"
"s_add_u32 s60, s89, 0\n"
"buffer_load_dwordx4 v47, s[44:47], s60 offen lds\n"
"s_add_u32 m0, s55, 15360\n"
"s_add_u32 s60, s89, 32768\n"
"buffer_load_dwordx4 v47, s[44:47], s60 offen lds\n"
"s_add_u32 m0, s55, 16384\n"
"s_add_u32 s60, s86, 0\n"
"buffer_load_dwordx4 v48, s[44:47], s60 offen lds\n"
"s_add_u32 m0, s55, 17408\n"
"s_add_u32 s60, s86, 32768\n"
"buffer_load_dwordx4 v48, s[44:47], s60 offen lds\n"
"s_add_u32 m0, s55, 18432\n"
"s_add_u32 s60, s87, 0\n"
"buffer_load_dwordx4 v48, s[44:47], s60 offen lds\n"
"s_add_u32 m0, s55, 19456\n"
"s_add_u32 s60, s87, 32768\n"
"buffer_load_dwordx4 v48, s[44:47], s60 offen lds\n"
"s_add_u32 m0, s55, 20480\n"
"s_add_u32 s60, s88, 0\n"
"buffer_load_dwordx4 v48, s[44:47], s60 offen lds\n"
"s_add_u32 m0, s55, 21504\n"
"s_add_u32 s60, s88, 32768\n"
"buffer_load_dwordx4 v48, s[44:47], s60 offen lds\n"
"s_add_u32 m0, s55, 22528\n"
"s_add_u32 s60, s89, 0\n"
"buffer_load_dwordx4 v48, s[44:47], s60 offen lds\n"
"s_add_u32 m0, s55, 23552\n"
"s_add_u32 s60, s89, 32768\n"
"buffer_load_dwordx4 v48, s[44:47], s60 offen lds\n"
"s_add_u32 m0, s55, 24576\n"
"s_add_u32 s60, s86, 0\n"
"buffer_load_dwordx4 v49, s[44:47], s60 offen lds\n"
"s_add_u32 m0, s55, 25600\n"
"s_add_u32 s60, s86, 32768\n"
"buffer_load_dwordx4 v49, s[44:47], s60 offen lds\n"
"s_add_u32 m0, s55, 26624\n"
"s_add_u32 s60, s87, 0\n"
"buffer_load_dwordx4 v49, s[44:47], s60 offen lds\n"
"s_add_u32 m0, s55, 27648\n"
"s_add_u32 s60, s87, 32768\n"
"buffer_load_dwordx4 v49, s[44:47], s60 offen lds\n"
"s_add_u32 m0, s55, 28672\n"
"s_add_u32 s60, s88, 0\n"
"buffer_load_dwordx4 v49, s[44:47], s60 offen lds\n"
"s_add_u32 m0, s55, 29696\n"
"s_add_u32 s60, s88, 32768\n"
"buffer_load_dwordx4 v49, s[44:47], s60 offen lds\n"
"s_add_u32 m0, s55, 30720\n"
"s_add_u32 s60, s89, 0\n"
"buffer_load_dwordx4 v49, s[44:47], s60 offen lds\n"
"s_add_u32 m0, s55, 31744\n"
"s_add_u32 s60, s89, 32768\n"
"buffer_load_dwordx4 v49, s[44:47], s60 offen lds\n"
"s_add_u32 m0, s55, 32768\n"
"s_add_u32 s60, s86, 0\n"
"buffer_load_dwordx4 v50, s[44:47], s60 offen lds\n"
"s_add_u32 m0, s55, 33792\n"
"s_add_u32 s60, s86, 32768\n"
"buffer_load_dwordx4 v50, s[44:47], s60 offen lds\n"
"s_add_u32 m0, s55, 34816\n"
"s_add_u32 s60, s87, 0\n"
"buffer_load_dwordx4 v50, s[44:47], s60 offen lds\n"
"s_add_u32 m0, s55, 35840\n"
"s_add_u32 s60, s87, 32768\n"
"buffer_load_dwordx4 v50, s[44:47], s60 offen lds\n"
"s_add_u32 m0, s55, 36864\n"
"s_add_u32 s60, s88, 0\n"
"buffer_load_dwordx4 v50, s[44:47], s60 offen lds\n"
"s_add_u32 m0, s55, 37888\n"
"s_add_u32 s60, s88, 32768\n"
"buffer_load_dwordx4 v50, s[44:47], s60 offen lds\n"
"s_add_u32 m0, s55, 38912\n"
"s_add_u32 s60, s89, 0\n"
"buffer_load_dwordx4 v50, s[44:47], s60 offen lds\n"
"s_add_u32 m0, s55, 39936\n"
"s_add_u32 s60, s89, 32768\n"
"buffer_load_dwordx4 v50, s[44:47], s60 offen lds\n"
"s_waitcnt vmcnt(38)\n"
"ds_read_b64_tr_b16 a[0:1], v44 offset:0\n"
"ds_read_b64_tr_b16 a[2:3], v44 offset:256\n"
"ds_read_b64_tr_b16 a[28:29], v45 offset:0\n"
"ds_read_b64_tr_b16 a[30:31], v45 offset:256\n"
"v_add_u32 v0, 2, v0\n"
"v_add_u32 v1, 0x9e3779b9, v1\n"
"v_or_b32 v0, 1, v0\n"
"v_readfirstlane_b32 s49, v1\n"
"v_readfirstlane_b32 s48, v0\n"
"s_waitcnt vmcnt(36)\n"
"ds_read_b64_tr_b16 a[56:57], v44 offset:2048\n"
"ds_read_b64_tr_b16 a[58:59], v44 offset:2304\n"
"ds_read_b64_tr_b16 a[84:85], v45 offset:2048\n"
"ds_read_b64_tr_b16 a[86:87], v45 offset:2304\n"
"s_waitcnt lgkmcnt(4)\n"
"s_add_u32 m0, s55, 0\n"
"s_add_u32 s60, s86, 0\n"
"buffer_load_dwordx4 v51, s[44:47], s60 offen lds\n"
"s_add_u32 m0, s55, 1024\n"
"s_add_u32 s60, s86, 32768\n"
"buffer_load_dwordx4 v51, s[44:47], s60 offen lds\n"
"s_waitcnt vmcnt(36)\n"
"ds_read_b64_tr_b16 a[112:113], v44 offset:4096\n"
"ds_read_b64_tr_b16 a[114:115], v44 offset:4352\n"
"ds_read_b64_tr_b16 a[140:141], v45 offset:4096\n"
"ds_read_b64_tr_b16 a[142:143], v45 offset:4352\n"
"s_waitcnt lgkmcnt(4)\n"
"s_add_u32 m0, s55, 2048\n"
"s_add_u32 s60, s87, 0\n"
"buffer_load_dwordx4 v51, s[44:47], s60 offen lds\n"
"s_add_u32 m0, s55, 3072\n"
"s_add_u32 s60, s87, 32768\n"
"buffer_load_dwordx4 v51, s[44:47], s60 offen lds\n"
"s_waitcnt vmcnt(36)\n"
"ds_read_b64_tr_b16 a[168:169], v44 offset:6144\n"
"ds_read_b64_tr_b16 a[170:171], v44 offset:6400\n"
"ds_read_b64_tr_b16 a[196:197], v45 offset:6144\n"
"ds_read_b64_tr_b16 a[198:199], v45 offset:6400\n"
"s_waitcnt lgkmcnt(4)\n"
"s_add_u32 m0, s55, 4096\n"
"s_add_u32 s60, s88, 0\n"
"buffer_load_dwordx4 v51, s[44:47], s60 offen lds\n"
"s_add_u32 m0, s55, 5120\n"
"s_add_u32 s60, s88, 32768\n"
"buffer_load_dwordx4 v51, s[44:47], s60 offen lds\n"
"s_waitcnt vmcnt(36)\n"
"ds_read_b64_tr_b16 a[4:5], v44 offset:8192\n"
"ds_read_b64_tr_b16 a[6:7], v44 offset:8448\n"
"ds_read_b64_tr_b16 a[32:33], v45 offset:8192\n"
"ds_read_b64_tr_b16 a[34:35], v45 offset:8448\n"
"s_waitcnt lgkmcnt(4)\n"
"s_add_u32 m0, s55, 6144\n"
"s_add_u32 s60, s89, 0\n"
"buffer_load_dwordx4 v51, s[44:47], s60 offen lds\n"
"s_add_u32 m0, s55, 7168\n"
"s_add_u32 s60, s89, 32768\n"
"buffer_load_dwordx4 v51, s[44:47], s60 offen lds\n"
"s_waitcnt vmcnt(36)\n"
"ds_read_b64_tr_b16 a[60:61], v44 offset:10240\n"
"ds_read_b64_tr_b16 a[62:63], v44 offset:10496\n"
"ds_read_b64_tr_b16 a[88:89], v45 offset:10240\n"
"ds_read_b64_tr_b16 a[90:91], v45 offset:10496\n"
"s_waitcnt lgkmcnt(4)\n"
"s_add_u32 m0, s55, 8192\n"
"s_add_u32 s60, s86, 0\n"
"buffer_load_dwordx4 v52, s[44:47], s60 offen lds\n"
"s_add_u32 m0, s55, 9216\n"
"s_add_u32 s60, s86, 32768\n"
"buffer_load_dwordx4 v52, s[44:47], s60 offen lds\n"
"s_waitcnt vmcnt(36)\n"
"ds_read_b64_tr_b16 a[116:117], v44 offset:12288\n"
"ds_read_b64_tr_b16 a[118:119], v44 offset:12544\n"
"ds_read_b64_tr_b16 a[144:145], v45 offset:12288\n"
"ds_read_b64_tr_b16 a[146:147], v45 offset:12544\n"
"s_waitcnt lgkmcnt(4)\n"
"s_add_u32 m0, s55, 10240\n"
"s_add_u32 s60, s87, 0\n"
"buffer_load_dwordx4 v52, s[44:47], s60 offen lds\n"
"s_add_u32 m0, s55, 11264\n"
"s_add_u32 s60, s87, 32768\n"
"buffer_load_dwordx4 v52, s[44:47], s60 offen lds\n"
"s_waitcnt vmcnt(36)\n"
"ds_read_b64_tr_b16 a[172:173], v44 offset:14336\n"
"ds_read_b64_tr_b16 a[174:175], v44 offset:14592\n"
"ds_read_b64_tr_b16 a[200:201], v45 offset:14336\n"
"ds_read_b64_tr_b16 a[202:203], v45 offset:14592\n"
"s_waitcnt lgkmcnt(4)\n"
"s_add_u32 m0, s55, 12288\n"
"s_add_u32 s60, s88, 0\n"
"buffer_load_dwordx4 v52, s[44:47], s60 offen lds\n"
"s_add_u32 m0, s55, 13312\n"
"s_add_u32 s60, s88, 32768\n"
"buffer_load_dwordx4 v52, s[44:47], s60 offen lds\n"
"s_waitcnt vmcnt(36)\n"
"ds_read_b64_tr_b16 a[8:9], v44 offset:16384\n"
"ds_read_b64_tr_b16 a[10:11], v44 offset:16640\n"
"ds_read_b64_tr_b16 a[36:37], v45 offset:16384\n"
"ds_read_b64_tr_b16 a[38:39], v45 offset:16640\n"
"s_waitcnt lgkmcnt(4)\n"
"s_add_u32 m0, s55, 14336\n"
"s_add_u32 s60, s89, 0\n"
"buffer_load_dwordx4 v52, s[44:47], s60 offen lds\n"
"s_add_u32 m0, s55, 15360\n"
"s_add_u32 s60, s89, 32768\n"
"buffer_load_dwordx4 v52, s[44:47], s60 offen lds\n"
"s_waitcnt vmcnt(36)\n"
"ds_read_b64_tr_b16 a[64:65], v44 offset:18432\n"
"ds_read_b64_tr_b16 a[66:67], v44 offset:18688\n"
"ds_read_b64_tr_b16 a[92:93], v45 offset:18432\n"
"ds_read_b64_tr_b16 a[94:95], v45 offset:18688\n"
"s_waitcnt lgkmcnt(4)\n"
"s_waitcnt vmcnt(34)\n"
"ds_read_b64_tr_b16 a[120:121], v44 offset:20480\n"
"ds_read_b64_tr_b16 a[122:123], v44 offset:20736\n"
"ds_read_b64_tr_b16 a[148:149], v45 offset:20480\n"
"ds_read_b64_tr_b16 a[150:151], v45 offset:20736\n"
"s_waitcnt lgkmcnt(4)\n"
"s_waitcnt vmcnt(32)\n"
"ds_read_b64_tr_b16 a[176:177], v44 offset:22528\n"
"ds_read_b64_tr_b16 a[178:179], v44 offset:22784\n"
"ds_read_b64_tr_b16 a[204:205], v45 offset:22528\n"
"ds_read_b64_tr_b16 a[206:207], v45 offset:22784\n"
"s_waitcnt lgkmcnt(4)\n"
"s_waitcnt vmcnt(30)\n"
"ds_read_b64_tr_b16 a[12:13], v44 offset:24576\n"
"ds_read_b64_tr_b16 a[14:15], v44 offset:24832\n"
"ds_read_b64_tr_b16 a[40:41], v45 offset:24576\n"
"ds_read_b64_tr_b16 a[42:43], v45 offset:24832\n"
"s_waitcnt lgkmcnt(4)\n"
"s_waitcnt vmcnt(28)\n"
"ds_read_b64_tr_b16 a[68:69], v44 offset:26624\n"
"ds_read_b64_tr_b16 a[70:71], v44 offset:26880\n"
"ds_read_b64_tr_b16 a[96:97], v45 offset:26624\n"
"ds_read_b64_tr_b16 a[98:99], v45 offset:26880\n"
"s_waitcnt lgkmcnt(4)\n"
"s_waitcnt vmcnt(26)\n"
"ds_read_b64_tr_b16 a[124:125], v44 offset:28672\n"
"ds_read_b64_tr_b16 a[126:127], v44 offset:28928\n"
"ds_read_b64_tr_b16 a[152:153], v45 offset:28672\n"
"ds_read_b64_tr_b16 a[154:155], v45 offset:28928\n"
"s_waitcnt lgkmcnt(4)\n"
"s_waitcnt vmcnt(24)\n"
"ds_read_b64_tr_b16 a[180:181], v44 offset:30720\n"
"ds_read_b64_tr_b16 a[182:183], v44 offset:30976\n"
"ds_read_b64_tr_b16 a[208:209], v45 offset:30720\n"
"ds_read_b64_tr_b16 a[210:211], v45 offset:30976\n"
"s_waitcnt lgkmcnt(4)\n"
"s_waitcnt vmcnt(22)\n"
"ds_read_b64_tr_b16 a[16:17], v44 offset:32768\n"
"ds_read_b64_tr_b16 a[18:19], v44 offset:33024\n"
"ds_read_b64_tr_b16 a[44:45], v45 offset:32768\n"
"ds_read_b64_tr_b16 a[46:47], v45 offset:33024\n"
"s_waitcnt lgkmcnt(4)\n"
"s_waitcnt vmcnt(20)\n"
"ds_read_b64_tr_b16 a[72:73], v44 offset:34816\n"
"ds_read_b64_tr_b16 a[74:75], v44 offset:35072\n"
"ds_read_b64_tr_b16 a[100:101], v45 offset:34816\n"
"ds_read_b64_tr_b16 a[102:103], v45 offset:35072\n"
"s_waitcnt lgkmcnt(4)\n"
"s_waitcnt vmcnt(18)\n"
"ds_read_b64_tr_b16 a[128:129], v44 offset:36864\n"
"ds_read_b64_tr_b16 a[130:131], v44 offset:37120\n"
"ds_read_b64_tr_b16 a[156:157], v45 offset:36864\n"
"ds_read_b64_tr_b16 a[158:159], v45 offset:37120\n"
"s_waitcnt lgkmcnt(4)\n"
"s_waitcnt vmcnt(16)\n"
"ds_read_b64_tr_b16 a[184:185], v44 offset:38912\n"
"ds_read_b64_tr_b16 a[186:187], v44 offset:39168\n"
"ds_read_b64_tr_b16 a[212:213], v45 offset:38912\n"
"ds_read_b64_tr_b16 a[214:215], v45 offset:39168\n"
"s_waitcnt lgkmcnt(4)\n"
"s_waitcnt vmcnt(14)\n"
"ds_read_b64_tr_b16 a[20:21], v44 offset:0\n"
"ds_read_b64_tr_b16 a[22:23], v44 offset:256\n"
"ds_read_b64_tr_b16 a[48:49], v45 offset:0\n"
"ds_read_b64_tr_b16 a[50:51], v45 offset:256\n"
"s_waitcnt lgkmcnt(4)\n"
"s_waitcnt vmcnt(12)\n"
"ds_read_b64_tr_b16 a[76:77], v44 offset:2048\n"
"ds_read_b64_tr_b16 a[78:79], v44 offset:2304\n"
"ds_read_b64_tr_b16 a[104:105], v45 offset:2048\n"
"ds_read_b64_tr_b16 a[106:107], v45 offset:2304\n"
"s_waitcnt lgkmcnt(4)\n"
"s_waitcnt vmcnt(10)\n"
"ds_read_b64_tr_b16 a[132:133], v44 offset:4096\n"
"ds_read_b64_tr_b16 a[134:135], v44 offset:4352\n"
"ds_read_b64_tr_b16 a[160:161], v45 offset:4096\n"
"ds_read_b64_tr_b16 a[162:163], v45 offset:4352\n"
"s_waitcnt lgkmcnt(4)\n"
"s_waitcnt vmcnt(8)\n"
"ds_read_b64_tr_b16 a[188:189], v44 offset:6144\n"
"ds_read_b64_tr_b16 a[190:191], v44 offset:6400\n"
"ds_read_b64_tr_b16 a[216:217], v45 offset:6144\n"
"ds_read_b64_tr_b16 a[218:219], v45 offset:6400\n"
"s_waitcnt lgkmcnt(4)\n"
"s_waitcnt vmcnt(6)\n"
"ds_read_b64_tr_b16 a[24:25], v44 offset:8192\n"
"ds_read_b64_tr_b16 a[26:27], v44 offset:8448\n"
"ds_read_b64_tr_b16 a[52:53], v45 offset:8192\n"
"ds_read_b64_tr_b16 a[54:55], v45 offset:8448\n"
"s_waitcnt lgkmcnt(4)\n"
"s_waitcnt vmcnt(4)\n"
"ds_read_b64_tr_b16 a[80:81], v44 offset:10240\n"
"ds_read_b64_tr_b16 a[82:83], v44 offset:10496\n"
"ds_read_b64_tr_b16 a[108:109], v45 offset:10240\n"
"ds_read_b64_tr_b16 a[110:111], v45 offset:10496\n"
"s_waitcnt lgkmcnt(4)\n"
"s_waitcnt vmcnt(2)\n"
"ds_read_b64_tr_b16 a[136:137], v44 offset:12288\n"
"ds_read_b64_tr_b16 a[138:139], v44 offset:12544\n"
"ds_read_b64_tr_b16 a[164:165], v45 offset:12288\n"
"ds_read_b64_tr_b16 a[166:167], v45 offset:12544\n"
"s_waitcnt lgkmcnt(4)\n"
"s_waitcnt vmcnt(0)\n"
"ds_read_b64_tr_b16 a[192:193], v44 offset:14336\n"
"ds_read_b64_tr_b16 a[194:195], v44 offset:14592\n"
"ds_read_b64_tr_b16 a[220:221], v45 offset:14336\n"
"ds_read_b64_tr_b16 a[222:223], v45 offset:14592\n"
"s_waitcnt lgkmcnt(4)\n"
"s_waitcnt vmcnt(0)\n"
"s_waitcnt lgkmcnt(0)\n"
"s_mov_b32 m0, s59\n"
"s_barrier\n"
"s_mov_b64 s[84:85], s[48:49]\n"
"v_mov_b32 v144, 0\n"
"v_mov_b32 v145, 0\n"
"v_mov_b32 v160, 0\n"
"v_mov_b32 v161, 0\n"
"v_mov_b32 v164, 0\n"
"v_mov_b32 v165, 0\n"
"s_mov_b32 s31, 0x80000000\n"
"s_mov_b32 s29, 0x80000000\n"
"s_mov_b32 s29, 0x80000000\n"
"s_mov_b32 s30, 0x80000000\n"
"s_mov_b32 s30, 0x80000000\n"
"s_mov_b32 s31, 0x80000000\n"
"s_mov_b32 s90, 0x80000000\n"
"v_mov_b32 v92, 0\n"
"v_mov_b32 v93, 0\n"
"s_mov_b32 s91, 0x80000000\n"
"s_mov_b32 s31, 0x80000000\n"
"s_mov_b32 s31, 0x80000000\n"
"s_mov_b32 s28, 0x80000000\n"
"s_cmp_eq_u32 s54, 0\n"
"s_cbranch_scc1 .LJ_1s\n"
"s_cmp_eq_u32 s54, 1\n"
"s_cbranch_scc1 .LJ_2s\n"
"s_cmp_eq_u32 s54, 2\n"
"s_cbranch_scc1 .LJ_3s\n"
".p2align 6\n"
".LJ_top:\n"
"v_mfma_f32_16x16x32_bf16 v[44:47], v[96:99], a[0:3], 0\n"
"s_cmp_lt_u32 s90, 0x8000000\n"
"s_cselect_b32 s90, 0x80000000, s90\n"
"v_mfma_f32_16x16x32_bf16 v[44:47], v[100:103], a[4:7], v[44:47]\n"
"s_waitcnt vmcnt(15)\n"
"global_load_dwordx4 a[224:227], v10, s[56:57] offset:0 sc1\n"
"v_mfma_f32_16x16x32_bf16 v[44:47], v[104:107], a[8:11], v[44:47]\n"
"s_waitcnt lgkmcnt(6)\n"
"buffer_load_dwordx2 v[92:93], v15, s[40:43], s90 offen offset:0 sc1\n"
"v_mfma_f32_16x16x32_bf16 v[44:47], v[108:111], a[12:15], v[44:47]\n"
"s_waitcnt vmcnt(13)\n"
"buffer_store_dwordx4 v[48:51], v18, s[76:79], s91 offen sc1\n"
"v_mfma_f32_16x16x32_bf16 v[44:47], v[112:115], a[16:19], v[44:47]\n"
"s_waitcnt lgkmcnt(3)\n"
"v_add_f32 v60, v60, v56\n"
"v_add_f32 v61, v61, v57\n"
"v_mfma_f32_16x16x32_bf16 v[44:47], v[116:119], a[20:23], v[44:47]\n"
"s_waitcnt vmcnt(13)\n"
"v_add_f32 v62, v62, v58\n"
"v_add_f32 v63, v63, v59\n"
"v_mfma_f32_16x16x32_bf16 v[44:47], v[120:123], a[24:27], v[44:47]\n"
"global_load_dwordx4 a[228:231], v11, s[56:57] offset:0 sc1\n"
"v_add_f32 v60, v60, v64\n"
"v_mfma_f32_16x16x32_bf16 v[48:51], v[96:99], a[28:31], 0\n"
"v_add_f32 v61, v61, v65\n"
"v_add_f32 v62, v62, v66\n"
"v_mfma_f32_16x16x32_bf16 v[48:51], v[100:103], a[32:35], v[48:51]\n"
"ds_write_addtid_b32 v44 offset:0\n"
"v_add_f32 v63, v63, v67\n"
"v_mfma_f32_16x16x32_bf16 v[48:51], v[104:107], a[36:39], v[48:51]\n"
"ds_write_addtid_b32 v45 offset:4640\n"
"v_add_f32 v60, v60, v52\n"
"v_mfma_f32_16x16x32_bf16 v[48:51], v[108:111], a[40:43], v[48:51]\n"
"ds_write_addtid_b32 v46 offset:9344\n"
"v_add_f32 v61, v61, v53\n"
"v_mfma_f32_16x16x32_bf16 v[48:51], v[112:115], a[44:47], v[48:51]\n"
"ds_write_addtid_b32 v47 offset:13984\n"
"v_add_f32 v62, v62, v54\n"
"v_mfma_f32_16x16x32_bf16 v[48:51], v[116:119], a[48:51], v[48:51]\n"
"v_add_f32 v63, v63, v55\n"
"v_mfma_f32_16x16x32_bf16 v[48:51], v[120:123], a[52:55], v[48:51]\n"
"s_add_u32 s30, s50, 0xffd00000\n"
"s_and_b32 s31, s30, 0xfbffffff\n"
"v_cvt_pk_bf16_f32 v44, v60, v61\n"
"v_mfma_f32_16x16x32_bf16 v[52:55], v[96:99], a[56:59], 0\n"
"s_waitcnt vmcnt(10)\n"
"v_cvt_pk_bf16_f32 v45, v62, v63\n"
"v_mfma_f32_16x16x32_bf16 v[56:59], v[38:41], v[68:71], 0\n"
"v_mfma_f32_16x16x32_bf16 v[56:59], v[38:41], v[76:79], v[56:59]\n"
"v_mfma_f32_16x16x32_bf16 v[56:59], v[38:41], v[84:87], v[56:59]\n"
"v_mfma_f32_16x16x32_bf16 v[60:63], v[38:41], v[72:75], 0\n"
"v_mfma_f32_16x16x32_bf16 v[60:63], v[38:41], v[80:83], v[60:63]\n"
"v_mfma_f32_16x16x32_bf16 v[60:63], v[38:41], v[88:91], v[60:63]\n"
"v_cvt_pk_bf16_f32 v64, v56, v57\n"
"v_cvt_pk_bf16_f32 v65, v58, v59\n"
"v_mfma_f32_16x16x32_bf16 v[52:55], v[100:103], a[60:63], v[52:55]\n"
"s_waitcnt lgkmcnt(4)\n"
"ds_write_addtid_b32 v48 offset:256\n"
"v_add_f32 v152, v152, v156\n"
"v_mfma_f32_16x16x32_bf16 v[52:55], v[104:107], a[64:67], v[52:55]\n"
"ds_write_addtid_b32 v49 offset:4896\n"
"v_add_f32 v153, v153, v157\n"
"v_mfma_f32_16x16x32_bf16 v[52:55], v[108:111], a[68:71], v[52:55]\n"
"ds_write_addtid_b32 v50 offset:9600\n"
"v_add_f32 v154, v154, v158\n"
"v_mfma_f32_16x16x32_bf16 v[52:55], v[112:115], a[72:75], v[52:55]\n"
"ds_write_addtid_b32 v51 offset:14240\n"
"v_add_f32 v155, v155, v159\n"
"v_mfma_f32_16x16x32_bf16 v[52:55], v[116:119], a[76:79], v[52:55]\n"
"buffer_store_dwordx2 v[0:1], v14, s[40:43], s29 offen offset:0 sc1\n"
"v_add_f32 v152, v152, v160\n"
"v_mfma_f32_16x16x32_bf16 v[52:55], v[120:123], a[80:83], v[52:55]\n"
"v_add_f32 v153, v153, v161\n"
"v_add_f32 v154, v154, v162\n"
"v_mfma_f32_16x16x32_bf16 v[48:51], v[96:99], a[84:87], 0\n"
"v_add_f32 v155, v155, v163\n"
"v_add_f32 v152, v152, v148\n"
"v_mfma_f32_16x16x32_bf16 v[48:51], v[100:103], a[88:91], v[48:51]\n"
"s_add_u32 s58, s50, 0x180000\n"
"ds_write_addtid_b32 v52 offset:512\n"
"v_add_f32 v153, v153, v149\n"
"v_mfma_f32_16x16x32_bf16 v[48:51], v[104:107], a[92:95], v[48:51]\n"
"s_and_b32 s58, s58, 0x3ffffff\n"
"ds_write_addtid_b32 v53 offset:5152\n"
"v_add_f32 v154, v154, v150\n"
"v_mfma_f32_16x16x32_bf16 v[48:51], v[108:111], a[96:99], v[48:51]\n"
"s_add_u32 s56, s40, s58\n"
"ds_write_addtid_b32 v54 offset:9856\n"
"v_add_f32 v155, v155, v151\n"
"v_mfma_f32_16x16x32_bf16 v[48:51], v[112:115], a[100:103], v[48:51]\n"
"s_addc_u32 s57, s41, 0\n"
"ds_write_addtid_b32 v55 offset:14496\n"
"v_cvt_pk_bf16_f32 v46, v152, v153\n"
"v_mfma_f32_16x16x32_bf16 v[48:51], v[116:119], a[104:107], v[48:51]\n"
"s_waitcnt vmcnt(9)\n"
"v_cmp_ne_u64 vcc, s[48:49], v[164:165]\n"
"v_cvt_pk_bf16_f32 v47, v154, v155\n"
"v_mfma_f32_16x16x32_bf16 v[48:51], v[120:123], a[108:111], v[48:51]\n"
"buffer_store_dwordx4 v[44:47], v13, s[40:43], s31 offen sc1\n"
"v_cvt_pk_bf16_f32 v66, v60, v61\n"
"v_cvt_pk_bf16_f32 v67, v62, v63\n"
"v_mfma_f32_16x16x32_bf16 v[52:55], v[144:147], v[144:147], 0\n"
"v_mfma_f32_16x16x32_bf16 v[44:47], v[34:37], v[144:147], 0\n"
"v_mfma_f32_16x16x32_bf16 v[52:55], v[64:67], v[64:67], v[52:55]\n"
"v_mfma_f32_16x16x32_bf16 v[44:47], v[34:37], v[64:67], v[44:47]\n"
"v_mfma_f32_16x16x32_bf16 v[56:59], v[96:99], a[112:115], 0\n"
"s_cbranch_vccnz .LJ_3s\n"
".LJ_3r:\n"
"v_cndmask_b32_e64 v52, v52, v53, s[64:65]\n"
"v_mfma_f32_16x16x32_bf16 v[56:59], v[100:103], a[116:119], v[56:59]\n"
"s_add_u32 s50, s50, 0x180000\n"
"ds_write_addtid_b32 v48 offset:768\n"
"v_cndmask_b32_e64 v54, v54, v55, s[64:65]\n"
"v_mfma_f32_16x16x32_bf16 v[56:59], v[104:107], a[120:123], v[56:59]\n"
"ds_write_addtid_b32 v49 offset:5408\n"
"v_cndmask_b32_e64 v45, v52, v54, s[66:67]\n"
"v_mfma_f32_16x16x32_bf16 v[56:59], v[108:111], a[124:127], v[56:59]\n"
"buffer_store_dwordx2 v[44:45], v23, s[40:43], s28 offen offset:0 sc1\n"
"ds_write_addtid_b32 v50 offset:10112\n"
"v_mfma_f32_16x16x32_bf16 v[56:59], v[112:115], a[128:131], v[56:59]\n"
"ds_write_addtid_b32 v51 offset:14752\n"
"global_load_dwordx4 a[232:235], v5, s[56:57] offset:0 sc1\n"
"v_mfma_f32_16x16x32_bf16 v[56:59], v[116:119], a[132:135], v[56:59]\n"
"ds_bpermute_b32 v44, v21, v64\n"
"v_mfma_f32_16x16x32_bf16 v[56:59], v[120:123], a[136:139], v[56:59]\n"
"buffer_load_dwordx2 v[144:145], v12, s[40:43], s50 offen sc1\n"
"v_mfma_f32_16x16x32_bf16 v[48:51], v[96:99], a[140:143], 0\n"
"ds_bpermute_b32 v45, v21, v65\n"
"v_mfma_f32_16x16x32_bf16 v[48:51], v[100:103], a[144:147], v[48:51]\n"
"ds_write_addtid_b32 v56 offset:1024\n"
"v_mfma_f32_16x16x32_bf16 v[48:51], v[104:107], a[148:151], v[48:51]\n"
"ds_write_addtid_b32 v57 offset:5664\n"
"global_load_dwordx4 a[236:239], v6, s[56:57] offset:0 sc1\n"
"v_mfma_f32_16x16x32_bf16 v[48:51], v[108:111], a[152:155], v[48:51]\n"
"ds_write_addtid_b32 v58 offset:10368\n"
"v_mfma_f32_16x16x32_bf16 v[48:51], v[112:115], a[156:159], v[48:51]\n"
"ds_write_addtid_b32 v59 offset:15008\n"
"v_mfma_f32_16x16x32_bf16 v[48:51], v[116:119], a[160:163], v[48:51]\n"
"ds_bpermute_b32 v46, v21, v66\n"
"v_mfma_f32_16x16x32_bf16 v[48:51], v[120:123], a[164:167], v[48:51]\n"
"ds_bpermute_b32 v47, v21, v67\n"
"v_mfma_f32_16x16x32_bf16 v[68:71], a[168:171], v[96:99], 0\n"
"global_load_dwordx4 a[240:243], v7, s[56:57] offset:0 sc1\n"
"v_mfma_f32_16x16x32_bf16 v[68:71], a[172:175], v[100:103], v[68:71]\n"
"ds_write_addtid_b32 v48 offset:1280\n"
"v_mfma_f32_16x16x32_bf16 v[68:71], a[176:179], v[104:107], v[68:71]\n"
"ds_write_addtid_b32 v49 offset:5920\n"
"v_mfma_f32_16x16x32_bf16 v[68:71], a[180:183], v[108:111], v[68:71]\n"
"ds_write_addtid_b32 v50 offset:10624\n"
"v_mfma_f32_16x16x32_bf16 v[68:71], a[184:187], v[112:115], v[68:71]\n"
"ds_write_addtid_b32 v51 offset:15264\n"
"v_mfma_f32_16x16x32_bf16 v[68:71], a[188:191], v[116:119], v[68:71]\n"
"s_waitcnt lgkmcnt(4)\n"
"buffer_store_dwordx4 v[44:47], v19, s[76:79], s91 offen sc1\n"
"global_load_dwordx4 a[244:247], v8, s[56:57] offset:0 sc1\n"
"v_mfma_f32_16x16x32_bf16 v[68:71], a[192:195], v[120:123], v[68:71]\n"
"v_mfma_f32_16x16x32_bf16 v[72:75], a[196:199], v[96:99], 0\n"
"s_and_b32 s31, s90, 0x3ffffff\n"
"v_mfma_f32_16x16x32_bf16 v[72:75], a[200:203], v[100:103], v[72:75]\n"
"s_waitcnt lgkmcnt(0)\n"
"v_mfma_f32_16x16x32_bf16 v[72:75], a[204:207], v[104:107], v[72:75]\n"
"s_barrier\n"
"v_mfma_f32_16x16x32_bf16 v[72:75], a[208:211], v[108:111], v[72:75]\n"
"s_waitcnt vmcnt(11)\n"
"global_load_dwordx4 a[248:251], v9, s[56:57] offset:0 sc1\n"
"ds_read_b128 v[76:79], v2 offset:0\n"
"v_mfma_f32_16x16x32_bf16 v[72:75], a[212:215], v[112:115], v[72:75]\n"
"s_add_u32 s62, s40, s31\n"
"ds_read_b128 v[80:83], v3 offset:512\n"
"ds_read_b128 v[84:87], v4 offset:1024\n"
"v_mfma_f32_16x16x32_bf16 v[72:75], a[216:219], v[116:119], v[72:75]\n"
"s_addc_u32 s63, s41, 0\n"
"ds_read_b128 v[88:91], v2 offset:256\n"
"ds_read_b128 v[96:99], v3 offset:768\n"
"v_mfma_f32_16x16x32_bf16 v[72:75], a[220:223], v[120:123], v[72:75]\n"
"ds_read_b128 v[100:103], v4 offset:1280\n"
"v_mfma_f32_16x16x32_bf16 v[104:107], v[124:127], a[0:3], 0\n"
"v_cmp_ne_u64 vcc, s[84:85], v[92:93]\n"
"v_mfma_f32_16x16x32_bf16 v[104:107], v[128:131], a[4:7], v[104:107]\n"
"global_load_dwordx4 a[252:255], v10, s[56:57] offset:0 sc1\n"
"v_mfma_f32_16x16x32_bf16 v[104:107], v[132:135], a[8:11], v[104:107]\n"
"s_cbranch_vccnz .LJ_4s\n"
".LJ_4r:\n"
"v_mfma_f32_16x16x32_bf16 v[104:107], v[136:139], a[12:15], v[104:107]\n"
"global_load_dwordx4 v[46:49], v16, s[62:63] offset:-4096 sc1\n"
"v_mfma_f32_16x16x32_bf16 v[104:107], v[140:143], a[16:19], v[104:107]\n"
"s_waitcnt lgkmcnt(3)\n"
"v_add_f32 v76, v76, v80\n"
"v_add_f32 v77, v77, v81\n"
"v_mfma_f32_16x16x32_bf16 v[104:107], a[224:227], a[20:23], v[104:107]\n"
"s_waitcnt vmcnt(12)\n"
"v_add_f32 v78, v78, v82\n"
"v_add_f32 v79, v79, v83\n"
"v_mfma_f32_16x16x32_bf16 v[104:107], a[228:231], a[24:27], v[104:107]\n"
"global_load_dwordx4 v[92:95], v11, s[56:57] offset:0 sc1\n"
"v_add_f32 v76, v76, v84\n"
"v_mfma_f32_16x16x32_bf16 v[80:83], v[124:127], a[28:31], 0\n"
"v_add_f32 v77, v77, v85\n"
"v_add_f32 v78, v78, v86\n"
"v_mfma_f32_16x16x32_bf16 v[80:83], v[128:131], a[32:35], v[80:83]\n"
"ds_write_addtid_b32 v104 offset:1536\n"
"v_add_f32 v79, v79, v87\n"
"v_mfma_f32_16x16x32_bf16 v[80:83], v[132:135], a[36:39], v[80:83]\n"
"ds_write_addtid_b32 v105 offset:6176\n"
"v_add_f32 v76, v76, v68\n"
"v_mfma_f32_16x16x32_bf16 v[80:83], v[136:139], a[40:43], v[80:83]\n"
"ds_write_addtid_b32 v106 offset:10880\n"
"v_add_f32 v77, v77, v69\n"
"v_mfma_f32_16x16x32_bf16 v[80:83], v[140:143], a[44:47], v[80:83]\n"
"ds_write_addtid_b32 v107 offset:15520\n"
"v_add_f32 v78, v78, v70\n"
"v_mfma_f32_16x16x32_bf16 v[80:83], a[224:227], a[48:51], v[80:83]\n"
"global_load_dwordx4 v[50:53], v16, s[62:63] offset:-3072 sc1\n"
"v_add_f32 v79, v79, v71\n"
"v_mfma_f32_16x16x32_bf16 v[80:83], a[228:231], a[52:55], v[80:83]\n"
"s_add_u32 s31, s50, 0xffd00000\n"
"s_and_b32 s29, s31, 0xfbffffff\n"
"v_cvt_pk_bf16_f32 v84, v76, v77\n"
"v_mfma_f32_16x16x32_bf16 v[68:71], v[124:127], a[56:59], 0\n"
"v_cvt_pk_bf16_f32 v85, v78, v79\n"
"v_mfma_f32_16x16x32_bf16 v[68:71], v[128:131], a[60:63], v[68:71]\n"
"s_waitcnt lgkmcnt(4)\n"
"ds_write_addtid_b32 v80 offset:1792\n"
"v_add_f32 v88, v88, v96\n"
"v_mfma_f32_16x16x32_bf16 v[68:71], v[132:135], a[64:67], v[68:71]\n"
"ds_write_addtid_b32 v81 offset:6432\n"
"v_add_f32 v89, v89, v97\n"
"v_mfma_f32_16x16x32_bf16 v[68:71], v[136:139], a[68:71], v[68:71]\n"
"ds_write_addtid_b32 v82 offset:11136\n"
"v_add_f32 v90, v90, v98\n"
"v_mfma_f32_16x16x32_bf16 v[68:71], v[140:143], a[72:75], v[68:71]\n"
"ds_write_addtid_b32 v83 offset:15776\n"
"v_add_f32 v91, v91, v99\n"
"v_mfma_f32_16x16x32_bf16 v[68:71], a[224:227], a[76:79], v[68:71]\n"
"s_waitcnt vmcnt(12)\n"
"v_add_f32 v88, v88, v100\n"
"v_add_f32 v89, v89, v101\n"
"v_mfma_f32_16x16x32_bf16 v[68:71], a[228:231], a[80:83], v[68:71]\n"
"v_add_f32 v90, v90, v102\n"
"v_add_f32 v91, v91, v103\n"
"v_mfma_f32_16x16x32_bf16 v[76:79], v[124:127], a[84:87], 0\n"
"v_add_f32 v88, v88, v72\n"
"v_add_f32 v89, v89, v73\n"
"v_mfma_f32_16x16x32_bf16 v[76:79], v[128:131], a[88:91], v[76:79]\n"
"s_add_u32 s58, s50, 0x180000\n"
"ds_write_addtid_b32 v68 offset:2048\n"
"v_add_f32 v90, v90, v74\n"
"v_mfma_f32_16x16x32_bf16 v[76:79], v[132:135], a[92:95], v[76:79]\n"
"s_and_b32 s58, s58, 0x3ffffff\n"
"ds_write_addtid_b32 v69 offset:6688\n"
"v_add_f32 v91, v91, v75\n"
"v_mfma_f32_16x16x32_bf16 v[76:79], v[136:139], a[96:99], v[76:79]\n"
"s_add_u32 s56, s40, s58\n"
"ds_write_addtid_b32 v70 offset:11392\n"
"v_cvt_pk_bf16_f32 v86, v88, v89\n"
"v_mfma_f32_16x16x32_bf16 v[76:79], v[140:143], a[100:103], v[76:79]\n"
"s_addc_u32 s57, s41, 0\n"
"ds_write_addtid_b32 v71 offset:16032\n"
"v_cvt_pk_bf16_f32 v87, v90, v91\n"
"v_mfma_f32_16x16x32_bf16 v[76:79], a[224:227], a[104:107], v[76:79]\n"
"s_waitcnt vmcnt(9)\n"
"buffer_store_dwordx4 v[84:87], v13, s[40:43], s29 offen sc1\n"
"v_cmp_ne_u64 vcc, s[48:49], v[144:145]\n"
"v_mfma_f32_16x16x32_bf16 v[76:79], a[228:231], a[108:111], v[76:79]\n"
"buffer_store_dwordx2 v[0:1], v14, s[40:43], s30 offen offset:0 sc1\n"
"v_mfma_f32_16x16x32_bf16 v[68:71], v[124:127], a[112:115], 0\n"
"s_cbranch_vccnz .LJ_1s\n"
".LJ_1r:\n"
"v_mfma_f32_16x16x32_bf16 v[68:71], v[128:131], a[116:119], v[68:71]\n"
"s_add_u32 s50, s50, 0x180000\n"
"ds_write_addtid_b32 v76 offset:2304\n"
"global_load_dwordx4 v[54:57], v16, s[62:63] offset:-2048 sc1\n"
"v_mfma_f32_16x16x32_bf16 v[68:71], v[132:135], a[120:123], v[68:71]\n"
"ds_write_addtid_b32 v77 offset:6944\n"
"v_mfma_f32_16x16x32_bf16 v[68:71], v[136:139], a[124:127], v[68:71]\n"
"ds_write_addtid_b32 v78 offset:11648\n"
"v_mfma_f32_16x16x32_bf16 v[68:71], v[140:143], a[128:131], v[68:71]\n"
"ds_write_addtid_b32 v79 offset:16288\n"
"global_load_dwordx4 v[96:99], v5, s[56:57] offset:0 sc1\n"
"v_mfma_f32_16x16x32_bf16 v[68:71], a[224:227], a[132:135], v[68:71]\n"
"v_mfma_f32_16x16x32_bf16 v[68:71], a[228:231], a[136:139], v[68:71]\n"
"buffer_load_dwordx2 v[160:161], v12, s[40:43], s50 offen sc1\n"
"v_mfma_f32_16x16x32_bf16 v[112:115], v[124:127], a[140:143], 0\n"
"global_load_dwordx4 v[58:61], v16, s[62:63] offset:-1024 sc1\n"
"v_mfma_f32_16x16x32_bf16 v[112:115], v[128:131], a[144:147], v[112:115]\n"
"ds_write_addtid_b32 v68 offset:2560\n"
"v_mfma_f32_16x16x32_bf16 v[112:115], v[132:135], a[148:151], v[112:115]\n"
"ds_write_addtid_b32 v69 offset:7200\n"
"global_load_dwordx4 v[100:103], v6, s[56:57] offset:0 sc1\n"
"v_mfma_f32_16x16x32_bf16 v[112:115], v[136:139], a[152:155], v[112:115]\n"
"ds_write_addtid_b32 v70 offset:11904\n"
"v_mfma_f32_16x16x32_bf16 v[112:115], v[140:143], a[156:159], v[112:115]\n"
"s_waitcnt vmcnt(13)\n"
"buffer_store_dwordx2 v[0:1], v24, s[40:43], s28 offen offset:0 sc1\n"
"ds_write_addtid_b32 v71 offset:16544\n"
"v_mfma_f32_16x16x32_bf16 v[112:115], a[224:227], a[160:163], v[112:115]\n"
"global_load_dwordx4 v[62:65], v17, s[62:63] offset:0 sc1\n"
"v_mfma_f32_16x16x32_bf16 v[112:115], a[228:231], a[164:167], v[112:115]\n"
"v_mfma_f32_16x16x32_bf16 v[144:147], a[168:171], v[124:127], 0\n"
"global_load_dwordx4 v[104:107], v7, s[56:57] offset:0 sc1\n"
"v_mfma_f32_16x16x32_bf16 v[144:147], a[172:175], v[128:131], v[144:147]\n"
"ds_write_addtid_b32 v112 offset:2816\n"
"v_mfma_f32_16x16x32_bf16 v[144:147], a[176:179], v[132:135], v[144:147]\n"
"ds_write_addtid_b32 v113 offset:7456\n"
"v_mfma_f32_16x16x32_bf16 v[144:147], a[180:183], v[136:139], v[144:147]\n"
"ds_write_addtid_b32 v114 offset:12160\n"
"global_load_dwordx4 v[70:73], v16, s[62:63] offset:0 sc1\n"
"v_mfma_f32_16x16x32_bf16 v[144:147], a[184:187], v[140:143], v[144:147]\n"
"ds_write_addtid_b32 v115 offset:16800\n"
"v_mfma_f32_16x16x32_bf16 v[144:147], a[188:191], a[224:227], v[144:147]\n"
"global_load_dwordx4 v[108:111], v8, s[56:57] offset:0 sc1\n"
"v_mfma_f32_16x16x32_bf16 v[144:147], a[192:195], a[228:231], v[144:147]\n"
"v_mfma_f32_16x16x32_bf16 v[148:151], a[196:199], v[124:127], 0\n"
"v_mfma_f32_16x16x32_bf16 v[148:151], a[200:203], v[128:131], v[148:151]\n"
"s_waitcnt lgkmcnt(0)\n"
"global_load_dwordx4 v[74:77], v16, s[62:63] offset:1024 sc1\n"
"v_mfma_f32_16x16x32_bf16 v[148:151], a[204:207], v[132:135], v[148:151]\n"
"s_barrier\n"
"v_mfma_f32_16x16x32_bf16 v[148:151], a[208:211], v[136:139], v[148:151]\n"
"global_load_dwordx4 v[112:115], v9, s[56:57] offset:0 sc1\n"
"ds_read_b128 v[124:127], v2 offset:1536\n"
"v_mfma_f32_16x16x32_bf16 v[148:151], a[212:215], v[140:143], v[148:151]\n"
"ds_read_b128 v[128:131], v3 offset:2048\n"
"ds_read_b128 v[132:135], v4 offset:2560\n"
"v_mfma_f32_16x16x32_bf16 v[148:151], a[216:219], a[224:227], v[148:151]\n"
"ds_read_b128 v[136:139], v2 offset:1792\n"
"ds_read_b128 v[140:143], v3 offset:2304\n"
"v_mfma_f32_16x16x32_bf16 v[148:151], a[220:223], a[228:231], v[148:151]\n"
"ds_read_b128 v[152:155], v4 offset:2816\n"
"global_load_dwordx4 v[78:81], v16, s[62:63] offset:2048 sc1\n"
"v_mfma_f32_16x16x32_bf16 v[156:159], a[232:235], a[0:3], 0\n"
"v_mfma_f32_16x16x32_bf16 v[156:159], a[236:239], a[4:7], v[156:159]\n"
"s_waitcnt vmcnt(20)\n"
"global_load_dwordx4 v[116:119], v10, s[56:57] offset:0 sc1\n"
"v_mfma_f32_16x16x32_bf16 v[156:159], a[240:243], a[8:11], v[156:159]\n"
"v_mfma_f32_16x16x32_bf16 v[156:159], a[244:247], a[12:15], v[156:159]\n"
"s_waitcnt vmcnt(19)\n"
"v_mfma_f32_16x16x32_bf16 v[156:159], a[248:251], a[16:19], v[156:159]\n"
"s_waitcnt lgkmcnt(3)\n"
"v_add_f32 v124, v124, v128\n"
"v_add_f32 v125, v125, v129\n"
"v_mfma_f32_16x16x32_bf16 v[156:159], a[252:255], a[20:23], v[156:159]\n"
"s_waitcnt vmcnt(17)\n"
"v_add_f32 v126, v126, v130\n"
"v_add_f32 v127, v127, v131\n"
"v_mfma_f32_16x16x32_bf16 v[156:159], v[92:95], a[24:27], v[156:159]\n"
"global_load_dwordx4 v[120:123], v11, s[56:57] offset:0 sc1\n"
"v_add_f32 v124, v124, v132\n"
"v_mfma_f32_16x16x32_bf16 v[128:131], a[232:235], a[28:31], 0\n"
"v_add_f32 v125, v125, v133\n"
"v_add_f32 v126, v126, v134\n"
"v_mfma_f32_16x16x32_bf16 v[128:131], a[236:239], a[32:35], v[128:131]\n"
"ds_write_addtid_b32 v156 offset:3072\n"
"v_add_f32 v127, v127, v135\n"
"v_mfma_f32_16x16x32_bf16 v[128:131], a[240:243], a[36:39], v[128:131]\n"
"ds_write_addtid_b32 v157 offset:7712\n"
"v_add_f32 v124, v124, v144\n"
"v_mfma_f32_16x16x32_bf16 v[128:131], a[244:247], a[40:43], v[128:131]\n"
"ds_write_addtid_b32 v158 offset:12416\n"
"v_add_f32 v125, v125, v145\n"
"v_mfma_f32_16x16x32_bf16 v[128:131], a[248:251], a[44:47], v[128:131]\n"
"ds_write_addtid_b32 v159 offset:17056\n"
"v_add_f32 v126, v126, v146\n"
"v_mfma_f32_16x16x32_bf16 v[128:131], a[252:255], a[48:51], v[128:131]\n"
"global_load_dwordx4 v[82:85], v16, s[62:63] offset:3072 sc1\n"
"v_add_f32 v127, v127, v147\n"
"v_mfma_f32_16x16x32_bf16 v[128:131], v[92:95], a[52:55], v[128:131]\n"
"s_add_u32 s29, s50, 0xffd00000\n"
"s_and_b32 s30, s29, 0xfbffffff\n"
"v_cvt_pk_bf16_f32 v132, v124, v125\n"
"v_mfma_f32_16x16x32_bf16 v[144:147], a[232:235], a[56:59], 0\n"
"v_cvt_pk_bf16_f32 v133, v126, v127\n"
"v_mfma_f32_16x16x32_bf16 v[144:147], a[236:239], a[60:63], v[144:147]\n"
"s_waitcnt lgkmcnt(4)\n"
"ds_write_addtid_b32 v128 offset:3328\n"
"v_add_f32 v136, v136, v140\n"
"v_mfma_f32_16x16x32_bf16 v[144:147], a[240:243], a[64:67], v[144:147]\n"
"s_mov_b32 s28, s90\n"
"ds_write_addtid_b32 v129 offset:7968\n"
"v_add_f32 v137, v137, v141\n"
"v_mfma_f32_16x16x32_bf16 v[144:147], a[244:247], a[68:71], v[144:147]\n"
"ds_write_addtid_b32 v130 offset:12672\n"
"v_add_f32 v138, v138, v142\n"
"v_mfma_f32_16x16x32_bf16 v[144:147], a[248:251], a[72:75], v[144:147]\n"
"s_waitcnt vmcnt(17)\n"
"buffer_store_dwordx2 v[0:1], v14, s[40:43], s31 offen offset:0 sc1\n"
"ds_write_addtid_b32 v131 offset:17312\n"
"v_mfma_f32_16x16x32_bf16 v[144:147], a[252:255], a[76:79], v[144:147]\n"
"v_add_f32 v139, v139, v143\n"
"v_add_f32 v136, v136, v152\n"
"v_mfma_f32_16x16x32_bf16 v[144:147], v[92:95], a[80:83], v[144:147]\n"
"v_add_f32 v137, v137, v153\n"
"v_add_f32 v138, v138, v154\n"
"v_mfma_f32_16x16x32_bf16 v[128:131], a[232:235], a[84:87], 0\n"
"v_add_f32 v139, v139, v155\n"
"v_add_f32 v136, v136, v148\n"
"v_mfma_f32_16x16x32_bf16 v[128:131], a[236:239], a[88:91], v[128:131]\n"
"s_add_u32 s58, s50, 0x180000\n"
"ds_write_addtid_b32 v144 offset:3584\n"
"v_add_f32 v137, v137, v149\n"
"v_mfma_f32_16x16x32_bf16 v[128:131], a[240:243], a[92:95], v[128:131]\n"
"s_and_b32 s58, s58, 0x3ffffff\n"
"ds_write_addtid_b32 v145 offset:8224\n"
"v_add_f32 v138, v138, v150\n"
"v_mfma_f32_16x16x32_bf16 v[128:131], a[244:247], a[96:99], v[128:131]\n"
"s_add_u32 s56, s40, s58\n"
"ds_write_addtid_b32 v146 offset:12928\n"
"v_add_f32 v139, v139, v151\n"
"v_mfma_f32_16x16x32_bf16 v[128:131], a[248:251], a[100:103], v[128:131]\n"
"s_addc_u32 s57, s41, 0\n"
"ds_write_addtid_b32 v147 offset:17568\n"
"v_cvt_pk_bf16_f32 v134, v136, v137\n"
"v_mfma_f32_16x16x32_bf16 v[128:131], a[252:255], a[104:107], v[128:131]\n"
"s_waitcnt vmcnt(14)\n"
"v_cmp_ne_u64 vcc, s[48:49], v[160:161]\n"
"v_cvt_pk_bf16_f32 v135, v138, v139\n"
"v_mfma_f32_16x16x32_bf16 v[128:131], v[92:95], a[108:111], v[128:131]\n"
"buffer_store_dwordx4 v[132:135], v13, s[40:43], s30 offen sc1\n"
"global_load_dwordx4 v[86:89], v17, s[62:63] offset:1024 sc1\n"
"v_mfma_f32_16x16x32_bf16 v[136:139], a[232:235], a[112:115], 0\n"
"s_cbranch_vccnz .LJ_2s\n"
".LJ_2r:\n"
"v_mov_b32 v44, v26\n"
"v_mfma_f32_16x16x32_bf16 v[136:139], a[236:239], a[116:119], v[136:139]\n"
"s_add_u32 s50, s50, 0x180000\n"
"ds_write_addtid_b32 v128 offset:3840\n"
"v_mov_b32 v45, v27\n"
"v_mfma_f32_16x16x32_bf16 v[136:139], a[240:243], a[120:123], v[136:139]\n"
"ds_write_addtid_b32 v129 offset:8480\n"
"v_mov_b32 v66, v28\n"
"v_mfma_f32_16x16x32_bf16 v[136:139], a[244:247], a[124:127], v[136:139]\n"
"ds_write_addtid_b32 v130 offset:13184\n"
"v_mov_b32 v67, v29\n"
"v_mfma_f32_16x16x32_bf16 v[136:139], a[248:251], a[128:131], v[136:139]\n"
"s_waitcnt vmcnt(12)\n"
"ds_write_addtid_b32 v131 offset:17824\n"
"global_load_dwordx4 v[124:127], v5, s[56:57] offset:0 sc1\n"
"v_mfma_f32_16x16x32_bf16 v[132:135], v[38:41], v[44:47], 0\n"
"s_cmp_ge_i32 s28, s80\n"
"s_cbranch_scc1 .LJ_ws\n"
".LJ_wr:\n"
"v_mfma_f32_16x16x32_bf16 v[132:135], v[38:41], v[52:55], v[132:135]\n"
"s_lshr_b32 s91, s28, s81\n"
"v_mfma_f32_16x16x32_bf16 v[132:135], v[38:41], v[60:63], v[132:135]\n"
"s_bitcmp1_b32 s91, 15\n"
"s_cselect_b32 s31, s83, s82\n"
"v_mfma_f32_16x16x32_bf16 v[128:131], v[38:41], v[48:51], 0\n"
"s_add_u32 s91, s91, s31\n"
"v_mfma_f32_16x16x32_bf16 v[128:131], v[38:41], v[56:59], v[128:131]\n"
"v_mfma_f32_16x16x32_bf16 v[128:131], v[38:41], v[64:67], v[128:131]\n"
"v_cvt_pk_bf16_f32 v144, v132, v133\n"
"v_cvt_pk_bf16_f32 v145, v134, v135\n"
"v_mfma_f32_16x16x32_bf16 v[136:139], a[252:255], a[132:135], v[136:139]\n"
"v_mfma_f32_16x16x32_bf16 v[136:139], v[92:95], a[136:139], v[136:139]\n"
"buffer_load_dwordx2 v[164:165], v12, s[40:43], s50 offen sc1\n"
"v_mfma_f32_16x16x32_bf16 v[44:47], a[232:235], a[140:143], 0\n"
"v_cvt_pk_bf16_f32 v146, v128, v129\n"
"v_cvt_pk_bf16_f32 v147, v130, v131\n"
"v_mfma_f32_16x16x32_bf16 v[44:47], a[236:239], a[144:147], v[44:47]\n"
"ds_write_addtid_b32 v136 offset:4096\n"
"v_mov_b32 v68, v30\n"
"v_mfma_f32_16x16x32_bf16 v[44:47], a[240:243], a[148:151], v[44:47]\n"
"ds_write_addtid_b32 v137 offset:8736\n"
"global_load_dwordx4 v[128:131], v6, s[56:57] offset:0 sc1\n"
"v_mfma_f32_16x16x32_bf16 v[44:47], a[244:247], a[152:155], v[44:47]\n"
"ds_write_addtid_b32 v138 offset:13440\n"
"v_mov_b32 v69, v31\n"
"v_mfma_f32_16x16x32_bf16 v[44:47], a[248:251], a[156:159], v[44:47]\n"
"ds_write_addtid_b32 v139 offset:18080\n"
"v_mov_b32 v90, v32\n"
"v_mfma_f32_16x16x32_bf16 v[44:47], a[252:255], a[160:163], v[44:47]\n"
"ds_bpermute_b32 v48, v21, v144\n"
"v_mfma_f32_16x16x32_bf16 v[44:47], v[92:95], a[164:167], v[44:47]\n"
"ds_bpermute_b32 v49, v21, v145\n"
"v_mfma_f32_16x16x32_bf16 v[52:55], a[168:171], a[232:235], 0\n"
"global_load_dwordx4 v[132:135], v7, s[56:57] offset:0 sc1\n"
"v_mov_b32 v91, v33\n"
"v_mfma_f32_16x16x32_bf16 v[52:55], a[172:175], a[236:239], v[52:55]\n"
"ds_write_addtid_b32 v44 offset:4352\n"
"v_mfma_f32_16x16x32_bf16 v[52:55], a[176:179], a[240:243], v[52:55]\n"
"ds_write_addtid_b32 v45 offset:8992\n"
"v_mfma_f32_16x16x32_bf16 v[52:55], a[180:183], a[244:247], v[52:55]\n"
"ds_write_addtid_b32 v46 offset:13696\n"
"v_mfma_f32_16x16x32_bf16 v[52:55], a[184:187], a[248:251], v[52:55]\n"
"ds_write_addtid_b32 v47 offset:18336\n"
"v_mfma_f32_16x16x32_bf16 v[52:55], a[188:191], a[252:255], v[52:55]\n"
"global_load_dwordx4 v[136:139], v8, s[56:57] offset:0 sc1\n"
"v_mfma_f32_16x16x32_bf16 v[52:55], a[192:195], v[92:95], v[52:55]\n"
"ds_bpermute_b32 v50, v21, v146\n"
"v_mfma_f32_16x16x32_bf16 v[148:151], a[196:199], a[232:235], 0\n"
"ds_bpermute_b32 v51, v21, v147\n"
"v_mfma_f32_16x16x32_bf16 v[148:151], a[200:203], a[236:239], v[148:151]\n"
"s_waitcnt lgkmcnt(2)\n"
"v_mfma_f32_16x16x32_bf16 v[148:151], a[204:207], a[240:243], v[148:151]\n"
"s_barrier\n"
"v_mfma_f32_16x16x32_bf16 v[148:151], a[208:211], a[244:247], v[148:151]\n"
"global_load_dwordx4 v[140:143], v9, s[56:57] offset:0 sc1\n"
"ds_read_b128 v[60:63], v2 offset:3072\n"
"v_mfma_f32_16x16x32_bf16 v[148:151], a[212:215], a[248:251], v[148:151]\n"
"ds_read_b128 v[56:59], v3 offset:3584\n"
"ds_read_b128 v[64:67], v4 offset:4096\n"
"v_mfma_f32_16x16x32_bf16 v[148:151], a[216:219], a[252:255], v[148:151]\n"
"s_add_u32 s90, s50, 0xff980000\n"
"ds_read_b128 v[152:155], v2 offset:3328\n"
"ds_read_b128 v[156:159], v3 offset:3840\n"
"v_mfma_f32_16x16x32_bf16 v[148:151], a[220:223], v[92:95], v[148:151]\n"
"ds_read_b128 v[160:163], v4 offset:4352\n"
"s_branch .LJ_top\n"
".LJ_1s:\n"
"s_cmp_ge_u32 s50, s53\n"
"s_cbranch_scc1 .LJ_end\n"
"s_cmp_ge_u32 s50, s52\n"
"s_cbranch_scc1 .LJ_1r\n"
"s_sub_u32 s60, s50, 0x0\n"
"s_mov_b32 s61, 200000\n"
"s_nop 7\n"
".LJ_1p:\n"
"buffer_load_dwordx2 v[144:145], v12, s[40:43], s60 offen sc1\n"
"s_waitcnt vmcnt(0)\n"
"v_cmp_ne_u64 vcc, s[48:49], v[144:145]\n"
"s_cbranch_vccz .LJ_1px\n"
"s_sleep 1\n"
"s_sub_u32 s61, s61, 1\n"
"s_cbranch_scc0 .LJ_1p\n"
"s_branch .LJ_end\n"
".LJ_1px:\n"
"s_branch .LJ_1r\n"
".LJ_2s:\n"
"s_cmp_ge_u32 s50, s53\n"
"s_cbranch_scc1 .LJ_end\n"
"s_cmp_ge_u32 s50, s52\n"
"s_cbranch_scc1 .LJ_2r\n"
"s_sub_u32 s60, s50, 0x0\n"
"s_mov_b32 s61, 200000\n"
"s_nop 7\n"
".LJ_2p:\n"
"buffer_load_dwordx2 v[160:161], v12, s[40:43], s60 offen sc1\n"
"s_waitcnt vmcnt(0)\n"
"v_cmp_ne_u64 vcc, s[48:49], v[160:161]\n"
"s_cbranch_vccz .LJ_2px\n"
"s_sleep 1\n"
"s_sub_u32 s61, s61, 1\n"
"s_cbranch_scc0 .LJ_2p\n"
"s_branch .LJ_end\n"
".LJ_2px:\n"
"s_branch .LJ_2r\n"
".LJ_3s:\n"
"s_cmp_ge_u32 s50, s53\n"
"s_cbranch_scc1 .LJ_end\n"
"s_cmp_ge_u32 s50, s52\n"
"s_cbranch_scc1 .LJ_3r\n"
"s_sub_u32 s60, s50, 0x0\n"
"s_mov_b32 s61, 200000\n"
"s_nop 7\n"
".LJ_3p:\n"
"buffer_load_dwordx2 v[164:165], v12, s[40:43], s60 offen sc1\n"
"s_waitcnt vmcnt(0)\n"
"v_cmp_ne_u64 vcc, s[48:49], v[164:165]\n"
"s_cbranch_vccz .LJ_3px\n"
"s_sleep 1\n"
"s_sub_u32 s61, s61, 1\n"
"s_cbranch_scc0 .LJ_3p\n"
"s_branch .LJ_end\n"
".LJ_3px:\n"
"s_branch .LJ_3r\n"
".LJ_4s:\n"
"s_cmp_ge_i32 s90, 0x10000000\n"
"s_cselect_b64 s[84:85], 0, s[84:85]\n"
"s_cmp_ge_u32 s90, 0x10000000\n"
"s_cbranch_scc1 .LJ_4r\n"
"s_mov_b32 s61, 200000\n"
"s_nop 7\n"
".LJ_4p:\n"
"buffer_load_dwordx2 v[92:93], v15, s[40:43], s90 offen offset:0 sc1\n"
"s_waitcnt vmcnt(0)\n"
"v_cmp_ne_u64 vcc, s[84:85], v[92:93]\n"
"s_cbranch_vccz .LJ_4px\n"
"s_sleep 1\n"
"s_sub_u32 s61, s61, 1\n"
"s_cbranch_scc0 .LJ_4p\n"
"s_branch .LJ_end\n"
".LJ_4px:\n"
"s_branch .LJ_4r\n"
".LJ_ws:\n"
"s_mov_b64 s[76:77], s[68:69]\n"
"s_mov_b32 s78, s70\n"
"s_nop 7\n"
"v_mov_b32 v18, v20\n"
"v_add_u32 v19, 64, v20\n"
"v_mov_b32 v21, v22\n"
"s_mov_b32 s80, 0x7fffffff\n"
"s_mov_b32 s81, 4\n"
"s_mov_b32 s82, 0xff400000\n"
"s_mov_b32 s83, 0xff3f8800\n"
"s_branch .LJ_wr\n"
".LJ_end:\n"
"s_cmp_lt_u32 s50, s53\n"
"s_cbranch_scc1 .LJ_ne\n"
"s_cmp_eq_u64 s[72:73], 0\n"
"s_cbranch_scc1 .LJ_ne\n"
"v_mov_b32 v12, 0\n"
"global_store_dwordx2 v12, v[0:1], s[72:73] sc1\n"
".LJ_ne:\n"
"s_waitcnt vmcnt(0) lgkmcnt(0)\n"
"s_endpgm\n"
      :: "{v2}"((unsigned)(lds0 + ((wid + 3) & 3) * 18592 + trp)), "{v3}"((unsigned)(lds0 + ((wid + 2) & 3) * 18592 + trp)), "{v4}"((unsigned)(lds0 + ((wid + 1) & 3) * 18592 + trp)), "{v5}"((unsigned)(lane * 16 + f0 * 1024 + ((0 + rot) % 7) * 1024 + 134316032)), "{v6}"((unsigned)(lane * 16 + f0 * 1024 + ((1 + rot) % 7) * 1024 + 134316032)), "{v7}"((unsigned)(lane * 16 + f0 * 1024 + ((2 + rot) % 7) * 1024 + 134316032)), "{v8}"((unsigned)(lane * 16 + f0 * 1024 + ((3 + rot) % 7) * 1024 + 134316032)), "{v9}"((unsigned)(lane * 16 + f0 * 1024 + ((4 + rot) % 7) * 1024 + 134316032)), "{v10}"((unsigned)(lane * 16 + f0 * 1024 + ((5 + rot) % 7) * 1024 + 134316032)), "{v11}"((unsigned)(lane * 16 + f0 * 1024 + ((6 + rot) % 7) * 1024 + 134316032)), "{v12}"((unsigned)((f0 + lane % 7) * 8 + kq * 32 + 353024 + J_GL * 1572864)), "{v13}"((unsigned)(lane * 16 + pair * 4096 + kq * 1024 + 212992)), "{v14}"((unsigned)(lane ? DROP : pair * 64 + kq * 8 + 354304)), "{v15}"((unsigned)((v16 * 2 + ((lane >> 2) & 1)) * 64 + (lane & 3) * 8 + 354304)), "{v16}"((unsigned)(lane * 16 + v16 * 8192 + 134434816)), "{v17}"((unsigned)(lane * 16 + v16 * 2048 + 134283264)), "{v18}"((unsigned)(lane * 16 + v16 * 2048 + 0)), "{v19}"((unsigned)(lane * 16 + v16 * 2048 + 1024)), "{v20}"((unsigned)(((lane >> 4) + (lane & 12)) * 4096 + (lane & 3) * 16 + v16 * 128)), "{v21}"((unsigned)(lane * 4)), "{v22}"((unsigned)((lane & 3) * 64 + (lane & 60))), "{v23}"((unsigned)(lg == (li >> 2) ? v16 * 128 + li * 8 + 67457024 : DROP)), "{v24}"((unsigned)(lane ? DROP : v16 * 8 + 67462912)), "{v26}"((unsigned)(bia0[0])), "{v27}"((unsigned)(bia0[1])), "{v28}"((unsigned)(bia0[2])), "{v29}"((unsigned)(bia0[3])), "{v30}"((unsigned)(bia1[0])), "{v31}"((unsigned)(bia1[1])), "{v32}"((unsigned)(bia1[2])), "{v33}"((unsigned)(bia1[3])), "{v34}"((unsigned)(0x3f803f80)), "{v35}"((unsigned)(0x3f803f80)), "{v36}"((unsigned)(0x3f803f80)), "{v37}"((unsigned)(0x3f803f80)), "{v38}"((unsigned)(sel0)), "{v39}"((unsigned)(sel1)), "{v40}"((unsigned)(sel0)), "{v41}"((unsigned)(sel1)), "{v44}"((unsigned)(vT + tsw)), "{v45}"((unsigned)(vT + (tsw ^ 8))), "{v46}"((unsigned)(vD + ((0 + rot) % 7) * 65536)), "{v47}"((unsigned)(vD + ((1 + rot) % 7) * 65536)), "{v48}"((unsigned)(vD + ((2 + rot) % 7) * 65536)), "{v49}"((unsigned)(vD + ((3 + rot) % 7) * 65536)), "{v50}"((unsigned)(vD + ((4 + rot) % 7) * 65536)), "{v51}"((unsigned)(vD + ((5 + rot) % 7) * 65536)), "{v52}"((unsigned)(vD + ((6 + rot) % 7) * 65536)), "{s[40:43]}"(rS), "{s[44:47]}"(rW), "{s[48:49]}"(wsp), "{s50}"((unsigned)(sA)), "{s51}"((unsigned)(sLo)), "{s52}"((unsigned)(sHi)), "{s53}"((unsigned)(sEx)), "{s54}"((unsigned)(sEnt)), "{s55}"((unsigned)(sLd)), "{s[56:57]}"(wsb + (sLo & 0x3ffffff)), "{s59}"((unsigned)(lds0 + wid * 18592)), "{s[62:63]}"(wsb), "{s[64:65]}"(0xAAAAAAAAAAAAAAAAull), "{s[66:67]}"(0xCCCCCCCCCCCCCCCCull), "{s[68:71]}"(rO), "{s[72:73]}"(ep), "{s[76:79]}"(rS), "{s80}"((unsigned)(201326592)), "{s81}"((unsigned)(0)), "{s82}"((unsigned)(0)), "{s83}"((unsigned)(0)), "{s86}"((unsigned)(sW0 + 64 * ((wid + 1) & 3))), "{s87}"((unsigned)(sW0 + 64 * ((wid + 2) & 3))), "{s88}"((unsigned)(sW0 + 64 * ((wid + 3) & 3))), "{s89}"((unsigned)(sW0 + 64 * ((wid + 4) & 3)))
      : "v255", "a255", "s95", "vcc", "scc", "memory");

    }
  }
}

extern "C" void kernel_launch(void* const* d_in, const int* in_sizes, int n_in, void* d_out, int out_size,
                              void* d_ws, size_t ws_size, hipStream_t stream) {
  Args m{};
  m.x = (const u16*)d_in[0]; m.wqkv = (const u16*)d_in[1]; m.wo = (const u16*)d_in[2]; m.w1 = (const u16*)d_in[4];
  m.b1 = (const u16*)d_in[5]; m.w2 = (const u16*)d_in[6]; m.b2 = (const u16*)d_in[7];
  m.out = (u16*)d_out; m.ws = (char*)d_ws;
  (void)hipFuncSetAttribute((const void*)stream_kernel, hipFuncAttributeMaxDynamicSharedMemorySize, LDS_BYTES);
  hipLaunchKernelGGL(stream_kernel, dim3(256), dim3(256), LDS_BYTES, stream, m);
}
```

```cpp
#include <hip/hip_runtime.h>
#include <stdint.h>
typedef unsigned short u16;
typedef unsigned long long u64;
typedef __attribute__((ext_vector_type(4))) unsigned u32x4;
constexpr unsigned F_X = 0;
constexpr unsigned F_ATT = 32768;
constexpr unsigned F_X1 = 65536;
constexpr unsigned F_H = 98304;
constexpr unsigned F_P = 212992;
constexpr unsigned F_ST1 = 344064;
constexpr unsigned F_ST2 = 348160;
constexpr unsigned F_G = 352256;
constexpr unsigned F_FA = 352512;
constexpr unsigned F_FX1 = 352768;
constexpr unsigned F_FH = 353024;
constexpr unsigned F_FX = 354048;
constexpr unsigned F_FP = 354304;
constexpr unsigned RS = 524288;
constexpr unsigned PADF = 256;
constexpr unsigned LOWB = 64;
constexpr unsigned L_TAB = 98304;
constexpr unsigned LDS_BYTES = 163840;
constexpr int Q_GL = 2;
constexpr int Q_EX = 6;
constexpr int O_GL = 1;
constexpr int O_EX = 3;
constexpr int F_GL = 1;
constexpr int F_EX = 4;
constexpr int G_GL = 1;
constexpr int G_EX = 3;
constexpr int H_GL = 1;
constexpr int H_EX = 3;
constexpr int J_GL = 1;
constexpr int J_EX = 8;
#define MAPV 4
#define FDLY 0
#define PF 42
#define KR 1
#define XP 2
#define OR 1
#define JJ 1
#define DLO 0
#define DLF 450
#define DLG 0
#define SLP 12
#define TW 1
#define DLW 250
#define DLB 300
#define EC 1
#define DLQ 0
#define FR0 0
#define PQ 3
#define QW 6
#define NFL 48
#define PSTX 0
#define CP 1
#define FN 4
#define XF 1
#define NE 16
#define DLX 300

constexpr unsigned DROP = 0x40000000u, WS0 = 1u << 20;
static_assert(WS0 + (PADF + 256 - LOWB) * RS <= (256u << 20), "workspace");
struct Args { const u16 *x, *wqkv, *wo, *w1, *b1, *w2, *b2; u16* out; char* ws;
};
__device__ __forceinline__ u32x4 desc(const void* p, unsigned n) { const u64 a = (u64)p; return (u32x4){(unsigned)a, (unsigned)(a >> 32) & 0xffffu, n, 0x00020000u}; }
__device__ __forceinline__ float lo16(unsigned v) { return __builtin_bit_cast(float, v << 16); }
__device__ __forceinline__ float hi16(unsigned v) { return __builtin_bit_cast(float, v & 0xffff0000u); }

__host__ __device__ inline void decode(int bid, int& role, int& set, int& idx) {
  const int xg = bid & 7, xj = bid >> 3;
  if (xg < 4) {
    if (xj < 16) { role = 0; set = xj & 1; idx = MAPV == 1 || MAPV == 4 ? 8 * xg + (xj >> 1) : MAPV == 2 ? 4 * (xj >> 1) + xg : 4 * (xj >> 1) + ((xg + 2 * set) & 3); }
    else if (MAPV == 4 && SLP && xj >= 30) { role = 2; set = 0; idx = (int)(0x1915110e0d090501ull >> (16 * xg + 8 * (xj & 1))) & 31; }
    else { role = 2; set = xj < 23 ? 1 : xj < 30 ? 2 : 0; idx = 7 * xg + (xj - 16) % 7; }
  } else if (xj < 21) { role = 3; set = xj / 7; idx = (xg - 4) * 7 + xj % 7; }
  else {
    const int k = xj - 21, no = xg == 4 ? 11 : xg == 5 ? 5 : 4;
    if (k < no) { role = 1; set = xg == 4 ? k >> 3 : xg == 5 ? 1 : 2; idx = xg == 4 ? k & 7 : xg == 5 ? 3 + k : (xg - 6) * 4 + k; }
    else if (SLP) { role = 2; set = 0; idx = (int)((xg == 5 ? 0x181716100f0000ull : xg == 6 ? 0x1b1a0a08070302ull : 0x14131206040c0bull) >> (8 * (k - 4))) & 31; }
    else { role = 2; set = 0; idx = (int)((xg == 5 ? 0x19181711100200ull : xg == 6 ? 0x1b1a0b0a090403ull : 0x14131206050d0cull) >> (8 * (k - 4))) & 31; }
  }
}

__global__ __launch_bounds__(256, 1) void stream_kernel(Args a) {
  extern __shared__ __attribute__((aligned(16))) char smem[];
  const int tid = threadIdx.x, lane = tid & 63, wid = __builtin_amdgcn_readfirstlane(tid >> 6), li = lane & 15, lg = lane >> 4;
  const unsigned lds0 = (unsigned)(size_t)smem;
  const u64 tz = __builtin_amdgcn_s_memrealtime();
  const int bp = (blockIdx.x + 2 * (XP - 1)) & 7, bid = (blockIdx.x & ~7) | ((bp & 1) << 2) | (bp >> 1);
  {
    u64 pc; unsigned len;
    asm volatile("s_getpc_b64 %0\n.Lc0:\n s_mov_b32 %1, .LO_end-.Lc0\n s_max_u32 %1, %1, .LH_end-.Lc0\n s_max_u32 %1, %1, .LJ_end-.Lc0\n s_max_u32 %1, %1, .LG_end-.Lc0" : "=s"(pc), "=s"(len)::"scc");
    asm volatile("global_load_dword v252, %0, %1" ::"v"(min(((blockIdx.x >> 3) * 256 + tid) * 8, len)), "s"(pc) : "v252");
  }
  const int pi = (li >> 2) + 4 * (li & 3), xj = bid >> 3, xo = (bid & 7) - 4;
  auto two = [&]() __attribute__((always_inline)) {
    const int hf = xo > 1;
#pragma unroll
    for (int i = 0; i < 191 / (84 - NE) + 1; ++i) {
      const int r0 = min(((i * (84 - NE) + xj * 4 + wid - NE) * 64 + lane) >> (4 - hf), 767);
      asm volatile("global_load_dword v252, %0, %1" ::"v"(((r0 & 3) * 256 + 64 + (r0 >> 2)) * 2048 + (hf ? (lane & 7) + (xo & 1) * 8 : xo ? max(li, 6) : li) * 128), "s"(a.wo) : "v252", "memory");
    }
  };
  auto tw2 = [&]() __attribute__((always_inline)) {
#pragma unroll
    for (int i = 0; i < 3; ++i)
      if (i * 21 + xj < 56) asm volatile("global_load_dword v252, %0, %1" ::"v"((xo * 896 + wid * 224 + (i * 21 + xj) * 4 + lg) * 2048 + li * 128), "s"(a.w2) : "v252", "memory");
  };
  auto conv = [&](int pr, bool early) __attribute__((always_inline)) {
    const __amdgpu_buffer_rsrc_t rb = __builtin_amdgcn_make_buffer_rsrc(a.ws + WS0 - LOWB * RS, 0, (PADF + 256) * RS, 0x00020000);
    const u32x4* src = (const u32x4*)(a.x + ((pr >> 1) * 32 + 2 * pi + (pr & 1)) * 1024 + wid * 256 + lg * 8);
    const u64 ev = __hip_atomic_load((const u64*)a.ws, __ATOMIC_RELAXED, __HIP_MEMORY_SCOPE_AGENT);
    u32x4 v[8];
#pragma unroll
    for (int k = 0; k < 8; ++k) v[k] = src[k * 4];
    u32x4 pf = {};
    if (early && (EC & 2)) asm volatile("global_load_dword v252, %0, %1 sc1" ::"v"(WS0 + (PADF - LOWB + pr) * RS + (lane & 1) * F_G + wid * 8192 * !(lane & 1)), "s"(a.ws) : "v252");
    if (!early && pr >= NE && pr < (MAPV == 4 ? 84 : 104) && (PF & 50) == 2) {
      const int hf = MAPV == 4 ? xo > 1 : xo, g = xj * 4 + wid - NE, n = MAPV == 4 ? (hf ? 767 / (84 - NE) + 1 : 1535 / (84 - NE) + 1) : hf ? 767 / (112 - NE) + 1 : 1535 / (80 - NE) + 1;
      for (int i = 0; i < n; ++i) {
        const int u = g * n + i, r0 = hf ? u : u >> 1, row = r0 < 768 ? (r0 / 192) * 256 + 64 + r0 % 192 : 0;
        asm volatile("global_load_dwordx4 %0, %1, off" : "+v"(pf) : "v"((const char*)a.wo + row * 2048 + (hf ? xo == (MAPV == 4 ? 3 : 2) : u & 1) * 1024 + lane * 16));
      }
    }
    float s = 0.f, q = 0.f;
#pragma unroll
    for (int k = 0; k < 8; ++k) __builtin_amdgcn_raw_buffer_store_b128(v[k], rb, lane * 16 + k * 1024, (PADF + pr) * RS + F_X + wid * 8192, 16);
    if (!early && pr >= NE && pr < 84 && (PF & 16)) two();
    __builtin_amdgcn_sched_barrier(0);
#pragma unroll
    for (int k = 0; k < 8; ++k) {
#pragma unroll
      for (int d = 0; d < 4; ++d) { const float l = lo16(v[k][d]), h = hi16(v[k][d]); s += l + h; q = fmaf(l, l, fmaf(h, h, q)); }
    }
    s += __shfl_xor(s, 16); q += __shfl_xor(q, 16); s += __shfl_xor(s, 32); q += __shfl_xor(q, 32);
    float2* t = (float2*)smem;
    if (lg == 0) t[wid * 16 + li] = make_float2(s, q);
    if (!early && pr >= NE && pr < 84 && (PF & 16)) {
      asm volatile("s_waitcnt vmcnt(%0)" ::"n"(191 / (84 - NE) + 1) : "memory");
    } else asm volatile("s_waitcnt vmcnt(0)" : "+v"(pf)::"memory");
    __syncthreads();
    if (tid < 16) {
      const float2 t0 = t[li], t1 = t[16 + li], t2 = t[32 + li], t3 = t[48 + li];
      const float nm = ((t0.x + t1.x) + (t2.x + t3.x)) * -0x1p-10f;
      const float var = fmaxf(fmaf(-nm, nm, fmaf((t0.y + t1.y) + (t2.y + t3.y), 0x1p-10f, 1e-5f)), 1e-5f);
      const u32x4 g = {__builtin_bit_cast(unsigned, __builtin_amdgcn_rsqf(var)), __builtin_bit_cast(unsigned, nm), ((unsigned)ev + 2u) | 1u, (unsigned)(ev >> 32) + 0x9E3779B9u};
      __builtin_amdgcn_raw_buffer_store_b128(g, rb, li * 16, (PADF + pr) * RS + F_G, 16);
    }
    __syncthreads();
  };
  if (!(bid & 4)) {
    const unsigned o = (wid * 256 + (lane >> 2)) * 6144 + (bid & 7) * 512 + (lane & 3) * 128;
#pragma unroll
    for (int i = 0; i < PQ - 1; ++i) {
      const int j = (bid >> 3) + (PQ == 3 ? i * 16 : i * 16 - 16);
      if ((PQ == 3 && !i) || bid >= 128) asm volatile("global_load_dword v252, %0, %1" ::"v"(o + j / 3 * 16 * 6144 + j % 3 * 2048), "s"(a.wqkv) : "v252");
    }
  }
  int xe = xo;
  asm volatile("" : "+s"(xe));
  if (EC && xe >= 0 && xj < NE / 4) conv(xj * 4 + xo, true);
  if (FN == 4) __builtin_amdgcn_s_barrier();
  int role, set, idx;
  decode(bid, role, set, idx);
  const u64 wsp = (u64)a.ws, wsb = wsp + WS0 - LOWB * RS;
  const u32x4 rS = desc(a.ws + WS0 - LOWB * RS, (PADF + 256) * RS);
  if (role == 2 && xo < 0 && (PF & 1)) {
    const int g = (xj - 16) * 4 + wid;
    const char* p = (const char*)a.wqkv + (((g >> 2) * 2 + 1) * 32 + (g & 3) * 8 + (lane >> 5)) * 6144 + (bid & 7) * 512 + (lane & 31) * 16;
    u32x4 t[12];
    unsigned fl = 0;
    if (PF & 4) fl = __hip_atomic_load((const unsigned*)(a.ws + WS0 + (size_t)(PADF - LOWB + g * 4 + (bid & 7)) * RS + F_ST2 + (lane < 56 ? lane : 0) * 128), __ATOMIC_RELAXED, __HIP_MEMORY_SCOPE_AGENT);
#pragma unroll
    for (int i = 0; i < 12; ++i) t[i] = *(const u32x4*)(p + (i & 3) * 12288 + (i >> 2) * 2048);
#pragma unroll
    for (int i = 0; i < 12; ++i) asm volatile("" ::"v"(t[i]));
    asm volatile("" ::"v"(fl));
  }
  if (role == 2 && xo < 0)
    asm volatile("global_load_dword v252, %0, %1 sc1" ::"v"(WS0 + (PADF - LOWB + (xj * 4 + wid - 64) * 4 + (bid & 7)) * RS + F_ST2 + (lane < 16 ? lane : lane < NFL ? lane + 16 : 0) * 128), "s"(a.ws) : "v252");
  if (role == 2 && xo < 0 && (SLP & 1)) {
    const unsigned c0 = 14 * (xo + 4), c = c0 + ((3 - c0 - (unsigned)((u64)a.w1 >> 7)) & 7) + (lane & 2) * 4;
    asm volatile("global_load_dword v252, %0, %1" ::"v"(((xj * 4 + wid - 64) * 16 + (lane >> 2)) * 7168 + (c < c0 + 14 ? c : c0) * 128 + (lane & 1) * 64), "s"(a.w1) : "v252");
  }
  if (role == 3 && xj * 4 + wid < 56 && (SLP & 2))
    asm volatile("global_load_dword v252, %0, %1" ::"v"((xo * 896 + (xj * 4 + wid) * 16 + (lane >> 2)) * 2048 + (((3 - (unsigned)((u64)a.w2 >> 7)) & 7) + (lane & 2) * 4) * 128 + (lane & 1) * 64), "s"(a.w2) : "v252");
  if (role == 0 || FN == 2 || (FN == 3 && role == 2)) asm volatile("s_waitcnt vmcnt(%0)" ::"n"(QW));
  if (role == 0 && DLQ) while (__builtin_amdgcn_s_memrealtime() - tz < DLQ) __builtin_amdgcn_s_sleep(1);
  if (FN == 3) __builtin_amdgcn_s_barrier();
  if (xo < 0 && (PF & 8) && !FN) {
    const int g = xj * 4 + wid;
    const char* p = (const char*)a.wqkv + (g * 8 + (lane >> 5)) * 6144 + (bid & 7) * 512 + (lane & 31) * 16;
    u32x4 t[12];
#pragma unroll
    for (int i = 0; i < 12; ++i) t[i] = *(const u32x4*)(p + (i & 3) * 12288 + (i >> 2) * 2048);
    unsigned fl = 0;
    if (role == 2) fl = __hip_atomic_load((const unsigned*)(a.ws + WS0 + (size_t)(PADF - LOWB + (g - 64) * 4 + (bid & 7)) * RS + F_ST2 + (lane < 56 ? lane : 0) * 128), __ATOMIC_RELAXED, __HIP_MEMORY_SCOPE_AGENT);
#pragma unroll
    for (int i = 0; i < 12; ++i) asm volatile("" ::"v"(t[i]));
    asm volatile("" ::"v"(fl));
  }
  if (role == 2 && xo < 0) for (int i = 0; i < FDLY; ++i) __builtin_amdgcn_s_sleep(16);
  const int p2 = xj < 4 ? 84 + xj * 4 + xo : xj % 7 > 3 ? 84 + (set * 3 + xj % 7) * 4 + xo : 128;
  if (role == 3 && xj >= NE / 4 && (PF & 32)) {
    while (__builtin_amdgcn_s_memrealtime() - tz < DLW) __builtin_amdgcn_s_sleep(2);
    two();
  }
  for (int pr = role < 3 ? 128 : EC && xj < NE / 4 ? p2 : xj * 4 + xo; pr < 128; pr = pr < 84 ? p2 : 128) {
    if (pr >= NE) while (__builtin_amdgcn_s_memrealtime() - tz < DLX) __builtin_amdgcn_s_sleep(4);
    if (pr >= 84 && (SLP & 32)) {
      while (__builtin_amdgcn_s_memrealtime() - tz < DLG) __builtin_amdgcn_s_sleep(4);
      tw2();
    }
    conv(pr, false);
  }
  const unsigned sLo = (PADF + set) * RS, sLd = lds0 + wid * 40960, vT = sLd + (8 * lg + (li >> 2)) * 64 + (li & 3) * 16;
  const unsigned trp = (li & 3) * 4608 + (li & 1) * 32 + (li & 2) * 64 + (li >> 2) * 64 + lg * 16;
  if (role == 0) {
    const unsigned sA = sLo - 2 * RS, sHi = sLo + 127 * 2 * RS, sEx = sHi + Q_EX * 2 * RS, sEnt = -wid & 3, tsw = 0, rot = KR * !PQ * (set + 2 * (idx & 1));
    const unsigned sW0 = wid * 256 * 6144 + idx * 64, vD = (lane >> 2) * 6144 + (lane & 3) * 16;
    const u32x4 rW = desc(a.wqkv, 1024 * 6144);
    auto ok = [&](int r, int prev) { const int k = lg + 4 * r; return prev ? k >= pi + 9 : (k <= pi && k >= pi - 7); };
    auto qmask = [&](int r, int prev) { return ok(r, prev) ? 0u : 0xff800000u; };
    auto pmask = [&](int d) { return (ok(2 * (d & 1), d >> 1) ? 0xffffu : 0u) | (ok(2 * (d & 1) + 1, d >> 1) ? 0xffff0000u : 0u); };
    asm volatile(
"s_nop 4\n"
"v_mov_b32 v186, 0\n"
"global_load_dwordx2 v[0:1], v186, s[48:49] sc1\n"
"v_mov_b32 v188, 0x3f803f80\n"
"v_mov_b32 v189, 0x3f803f80\n"
"v_mov_b32 v190, 0x3f803f80\n"
"v_mov_b32 v191, 0x3f803f80\n"
"s_add_u32 m0, s55, 0\n"
"s_add_u32 s60, s63, 0\n"
"buffer_load_dwordx4 v53, s[44:47], s60 offen lds\n"
"s_add_u32 m0, s55, 1024\n"
"s_add_u32 s60, s63, 98304\n"
"buffer_load_dwordx4 v53, s[44:47], s60 offen lds\n"
"s_add_u32 m0, s55, 2048\n"
"s_add_u32 s60, s64, 0\n"
"buffer_load_dwordx4 v53, s[44:47], s60 offen lds\n"
"s_add_u32 m0, s55, 3072\n"
"s_add_u32 s60, s64, 98304\n"
"buffer_load_dwordx4 v53, s[44:47], s60 offen lds\n"
"s_add_u32 m0, s55, 4096\n"
"s_add_u32 s60, s65, 0\n"
"buffer_load_dwordx4 v53, s[44:47], s60 offen lds\n"
"s_add_u32 m0, s55, 5120\n"
"s_add_u32 s60, s65, 98304\n"
"buffer_load_dwordx4 v53, s[44:47], s60 offen lds\n"
"s_add_u32 m0, s55, 6144\n"
"s_add_u32 s60, s63, 196608\n"
"buffer_load_dwordx4 v53, s[44:47], s60 offen lds\n"
"s_add_u32 m0, s55, 7168\n"
"s_add_u32 s60, s63, 294912\n"
"buffer_load_dwordx4 v53, s[44:47], s60 offen lds\n"
"s_add_u32 m0, s55, 8192\n"
"s_add_u32 s60, s64, 196608\n"
"buffer_load_dwordx4 v53, s[44:47], s60 offen lds\n"
"s_add_u32 m0, s55, 9216\n"
"s_add_u32 s60, s64, 294912\n"
"buffer_load_dwordx4 v53, s[44:47], s60 offen lds\n"
"s_add_u32 m0, s55, 10240\n"
"s_add_u32 s60, s65, 196608\n"
"buffer_load_dwordx4 v53, s[44:47], s60 offen lds\n"
"s_add_u32 m0, s55, 11264\n"
"s_add_u32 s60, s65, 294912\n"
"buffer_load_dwordx4 v53, s[44:47], s60 offen lds\n"
"s_add_u32 m0, s55, 12288\n"
"s_add_u32 s60, s63, 0\n"
"buffer_load_dwordx4 v54, s[44:47], s60 offen lds\n"
"s_add_u32 m0, s55, 13312\n"
"s_add_u32 s60, s63, 98304\n"
"buffer_load_dwordx4 v54, s[44:47], s60 offen lds\n"
"s_add_u32 m0, s55, 14336\n"
"s_add_u32 s60, s64, 0\n"
"buffer_load_dwordx4 v54, s[44:47], s60 offen lds\n"
"s_add_u32 m0, s55, 15360\n"
"s_add_u32 s60, s64, 98304\n"
"buffer_load_dwordx4 v54, s[44:47], s60 offen lds\n"
"s_add_u32 m0, s55, 16384\n"
"s_add_u32 s60, s65, 0\n"
"buffer_load_dwordx4 v54, s[44:47], s60 offen lds\n"
"s_add_u32 m0, s55, 17408\n"
"s_add_u32 s60, s65, 98304\n"
"buffer_load_dwordx4 v54, s[44:47], s60 offen lds\n"
"s_add_u32 m0, s55, 18432\n"
"s_add_u32 s60, s63, 196608\n"
"buffer_load_dwordx4 v54, s[44:47], s60 offen lds\n"
"s_add_u32 m0, s55, 19456\n"
"s_add_u32 s60, s63, 294912\n"
"buffer_load_dwordx4 v54, s[44:47], s60 offen lds\n"
"s_add_u32 m0, s55, 20480\n"
"s_add_u32 s60, s64, 196608\n"
"buffer_load_dwordx4 v54, s[44:47], s60 offen lds\n"
"s_add_u32 m0, s55, 21504\n"
"s_add_u32 s60, s64, 294912\n"
"buffer_load_dwordx4 v54, s[44:47], s60 offen lds\n"
"s_add_u32 m0, s55, 22528\n"
"s_add_u32 s60, s65, 196608\n"
"buffer_load_dwordx4 v54, s[44:47], s60 offen lds\n"
"s_add_u32 m0, s55, 23552\n"
"s_add_u32 s60, s65, 294912\n"
"buffer_load_dwordx4 v54, s[44:47], s60 offen lds\n"
"s_add_u32 m0, s55, 24576\n"
"s_add_u32 s60, s63, 0\n"
"buffer_load_dwordx4 v55, s[44:47], s60 offen lds\n"
"s_add_u32 m0, s55, 25600\n"
"s_add_u32 s60, s63, 98304\n"
"buffer_load_dwordx4 v55, s[44:47], s60 offen lds\n"
"s_add_u32 m0, s55, 26624\n"
"s_add_u32 s60, s64, 0\n"
"buffer_load_dwordx4 v55, s[44:47], s60 offen lds\n"
"s_add_u32 m0, s55, 27648\n"
"s_add_u32 s60, s64, 98304\n"
"buffer_load_dwordx4 v55, s[44:47], s60 offen lds\n"
"s_add_u32 m0, s55, 28672\n"
"s_add_u32 s60, s65, 0\n"
"buffer_load_dwordx4 v55, s[44:47], s60 offen lds\n"
"s_add_u32 m0, s55, 29696\n"
"s_add_u32 s60, s65, 98304\n"
"buffer_load_dwordx4 v55, s[44:47], s60 offen lds\n"
"s_add_u32 m0, s55, 30720\n"
"s_add_u32 s60, s63, 196608\n"
"buffer_load_dwordx4 v55, s[44:47], s60 offen lds\n"
"s_add_u32 m0, s55, 31744\n"
"s_add_u32 s60, s63, 294912\n"
"buffer_load_dwordx4 v55, s[44:47], s60 offen lds\n"
"s_add_u32 m0, s55, 32768\n"
"s_add_u32 s60, s64, 196608\n"
"buffer_load_dwordx4 v55, s[44:47], s60 offen lds\n"
"s_add_u32 m0, s55, 33792\n"
"s_add_u32 s60, s64, 294912\n"
"buffer_load_dwordx4 v55, s[44:47], s60 offen lds\n"
"s_add_u32 m0, s55, 34816\n"
"s_add_u32 s60, s65, 196608\n"
"buffer_load_dwordx4 v55, s[44:47], s60 offen lds\n"
"s_add_u32 m0, s55, 35840\n"
"s_add_u32 s60, s65, 294912\n"
"buffer_load_dwordx4 v55, s[44:47], s60 offen lds\n"
"s_add_u32 m0, s55, 36864\n"
"s_add_u32 s60, s63, 0\n"
"buffer_load_dwordx4 v56, s[44:47], s60 offen lds\n"
"s_add_u32 m0, s55, 37888\n"
"s_add_u32 s60, s63, 98304\n"
"buffer_load_dwordx4 v56, s[44:47], s60 offen lds\n"
"s_add_u32 m0, s55, 38912\n"
"s_add_u32 s60, s64, 0\n"
"buffer_load_dwordx4 v56, s[44:47], s60 offen lds\n"
"s_add_u32 m0, s55, 39936\n"
"s_add_u32 s60, s64, 98304\n"
"buffer_load_dwordx4 v56, s[44:47], s60 offen lds\n"
"s_waitcnt vmcnt(38)\n"
"ds_read_b64_tr_b16 a[0:1], v48 offset:0\n"
"ds_read_b64_tr_b16 a[2:3], v48 offset:256\n"
"ds_read_b64_tr_b16 a[32:33], v49 offset:0\n"
"ds_read_b64_tr_b16 a[34:35], v49 offset:256\n"
"v_add_u32 v0, 2, v0\n"
"v_add_u32 v1, 0x9e3779b9, v1\n"
"v_or_b32 v0, 1, v0\n"
"v_readfirstlane_b32 s49, v1\n"
"v_readfirstlane_b32 s48, v0\n"
"s_waitcnt vmcnt(36)\n"
"ds_read_b64_tr_b16 a[64:65], v48 offset:2048\n"
"ds_read_b64_tr_b16 a[66:67], v48 offset:2304\n"
"ds_read_b64_tr_b16 a[96:97], v49 offset:2048\n"
"ds_read_b64_tr_b16 a[98:99], v49 offset:2304\n"
"s_waitcnt lgkmcnt(4)\n"
"s_add_u32 m0, s55, 0\n"
"s_add_u32 s60, s65, 0\n"
"buffer_load_dwordx4 v56, s[44:47], s60 offen lds\n"
"s_add_u32 m0, s55, 1024\n"
"s_add_u32 s60, s65, 98304\n"
"buffer_load_dwordx4 v56, s[44:47], s60 offen lds\n"
"v_mfma_f32_16x16x32_bf16 v[192:195], v[188:191], a[0:3], 0\n"
"v_mfma_f32_16x16x32_bf16 v[196:199], v[188:191], a[32:35], 0\n"
"s_waitcnt vmcnt(36)\n"
"ds_read_b64_tr_b16 a[128:129], v48 offset:4096\n"
"ds_read_b64_tr_b16 a[130:131], v48 offset:4352\n"
"ds_read_b64_tr_b16 a[160:161], v49 offset:4096\n"
"ds_read_b64_tr_b16 a[162:163], v49 offset:4352\n"
"s_waitcnt lgkmcnt(4)\n"
"s_add_u32 m0, s55, 2048\n"
"s_add_u32 s60, s63, 196608\n"
"buffer_load_dwordx4 v56, s[44:47], s60 offen lds\n"
"s_add_u32 m0, s55, 3072\n"
"s_add_u32 s60, s63, 294912\n"
"buffer_load_dwordx4 v56, s[44:47], s60 offen lds\n"
"v_mfma_f32_16x16x32_bf16 v[200:203], v[188:191], a[64:67], 0\n"
"v_mfma_f32_16x16x32_bf16 v[204:207], v[188:191], a[96:99], 0\n"
"s_waitcnt vmcnt(36)\n"
"ds_read_b64_tr_b16 a[4:5], v48 offset:6144\n"
"ds_read_b64_tr_b16 a[6:7], v48 offset:6400\n"
"ds_read_b64_tr_b16 a[36:37], v49 offset:6144\n"
"ds_read_b64_tr_b16 a[38:39], v49 offset:6400\n"
"s_waitcnt lgkmcnt(4)\n"
"s_add_u32 m0, s55, 4096\n"
"s_add_u32 s60, s64, 196608\n"
"buffer_load_dwordx4 v56, s[44:47], s60 offen lds\n"
"s_add_u32 m0, s55, 5120\n"
"s_add_u32 s60, s64, 294912\n"
"buffer_load_dwordx4 v56, s[44:47], s60 offen lds\n"
"v_mfma_f32_16x16x32_bf16 v[208:211], a[128:131], v[188:191], 0\n"
"v_mfma_f32_16x16x32_bf16 v[212:215], a[160:163], v[188:191], 0\n"
"s_waitcnt vmcnt(36)\n"
"ds_read_b64_tr_b16 a[68:69], v48 offset:8192\n"
"ds_read_b64_tr_b16 a[70:71], v48 offset:8448\n"
"ds_read_b64_tr_b16 a[100:101], v49 offset:8192\n"
"ds_read_b64_tr_b16 a[102:103], v49 offset:8448\n"
"s_waitcnt lgkmcnt(4)\n"
"s_add_u32 m0, s55, 6144\n"
"s_add_u32 s60, s65, 196608\n"
"buffer_load_dwordx4 v56, s[44:47], s60 offen lds\n"
"s_add_u32 m0, s55, 7168\n"
"s_add_u32 s60, s65, 294912\n"
"buffer_load_dwordx4 v56, s[44:47], s60 offen lds\n"
"v_mfma_f32_16x16x32_bf16 v[192:195], v[188:191], a[4:7], v[192:195]\n"
"v_mfma_f32_16x16x32_bf16 v[196:199], v[188:191], a[36:39], v[196:199]\n"
"s_waitcnt vmcnt(36)\n"
"ds_read_b64_tr_b16 a[132:133], v48 offset:10240\n"
"ds_read_b64_tr_b16 a[134:135], v48 offset:10496\n"
"ds_read_b64_tr_b16 a[164:165], v49 offset:10240\n"
"ds_read_b64_tr_b16 a[166:167], v49 offset:10496\n"
"s_waitcnt lgkmcnt(4)\n"
"v_mfma_f32_16x16x32_bf16 v[200:203], v[188:191], a[68:71], v[200:203]\n"
"v_mfma_f32_16x16x32_bf16 v[204:207], v[188:191], a[100:103], v[204:207]\n"
"s_waitcnt vmcnt(34)\n"
"ds_read_b64_tr_b16 a[8:9], v48 offset:12288\n"
"ds_read_b64_tr_b16 a[10:11], v48 offset:12544\n"
"ds_read_b64_tr_b16 a[40:41], v49 offset:12288\n"
"ds_read_b64_tr_b16 a[42:43], v49 offset:12544\n"
"s_waitcnt lgkmcnt(4)\n"
"v_mfma_f32_16x16x32_bf16 v[208:211], a[132:135], v[188:191], v[208:211]\n"
"v_mfma_f32_16x16x32_bf16 v[212:215], a[164:167], v[188:191], v[212:215]\n"
"s_waitcnt vmcnt(32)\n"
"ds_read_b64_tr_b16 a[72:73], v48 offset:14336\n"
"ds_read_b64_tr_b16 a[74:75], v48 offset:14592\n"
"ds_read_b64_tr_b16 a[104:105], v49 offset:14336\n"
"ds_read_b64_tr_b16 a[106:107], v49 offset:14592\n"
"s_waitcnt lgkmcnt(4)\n"
"v_mfma_f32_16x16x32_bf16 v[192:195], v[188:191], a[8:11], v[192:195]\n"
"v_mfma_f32_16x16x32_bf16 v[196:199], v[188:191], a[40:43], v[196:199]\n"
"s_waitcnt vmcnt(30)\n"
"ds_read_b64_tr_b16 a[136:137], v48 offset:16384\n"
"ds_read_b64_tr_b16 a[138:139], v48 offset:16640\n"
"ds_read_b64_tr_b16 a[168:169], v49 offset:16384\n"
"ds_read_b64_tr_b16 a[170:171], v49 offset:16640\n"
"s_waitcnt lgkmcnt(4)\n"
"v_mfma_f32_16x16x32_bf16 v[200:203], v[188:191], a[72:75], v[200:203]\n"
"v_mfma_f32_16x16x32_bf16 v[204:207], v[188:191], a[104:107], v[204:207]\n"
"s_waitcnt vmcnt(28)\n"
"ds_read_b64_tr_b16 a[12:13], v48 offset:18432\n"
"ds_read_b64_tr_b16 a[14:15], v48 offset:18688\n"
"ds_read_b64_tr_b16 a[44:45], v49 offset:18432\n"
"ds_read_b64_tr_b16 a[46:47], v49 offset:18688\n"
"s_waitcnt lgkmcnt(4)\n"
"v_mfma_f32_16x16x32_bf16 v[208:211], a[136:139], v[188:191], v[208:211]\n"
"v_mfma_f32_16x16x32_bf16 v[212:215], a[168:171], v[188:191], v[212:215]\n"
"s_waitcnt vmcnt(26)\n"
"ds_read_b64_tr_b16 a[76:77], v48 offset:20480\n"
"ds_read_b64_tr_b16 a[78:79], v48 offset:20736\n"
"ds_read_b64_tr_b16 a[108:109], v49 offset:20480\n"
"ds_read_b64_tr_b16 a[110:111], v49 offset:20736\n"
"s_waitcnt lgkmcnt(4)\n"
"v_mfma_f32_16x16x32_bf16 v[192:195], v[188:191], a[12:15], v[192:195]\n"
"v_mfma_f32_16x16x32_bf16 v[196:199], v[188:191], a[44:47], v[196:199]\n"
"s_waitcnt vmcnt(24)\n"
"ds_read_b64_tr_b16 a[140:141], v48 offset:22528\n"
"ds_read_b64_tr_b16 a[142:143], v48 offset:22784\n"
"ds_read_b64_tr_b16 a[172:173], v49 offset:22528\n"
"ds_read_b64_tr_b16 a[174:175], v49 offset:22784\n"
"s_waitcnt lgkmcnt(4)\n"
"v_mfma_f32_16x16x32_bf16 v[200:203], v[188:191], a[76:79], v[200:203]\n"
"v_mfma_f32_16x16x32_bf16 v[204:207], v[188:191], a[108:111], v[204:207]\n"
"s_waitcnt vmcnt(22)\n"
"ds_read_b64_tr_b16 a[16:17], v48 offset:24576\n"
"ds_read_b64_tr_b16 a[18:19], v48 offset:24832\n"
"ds_read_b64_tr_b16 a[48:49], v49 offset:24576\n"
"ds_read_b64_tr_b16 a[50:51], v49 offset:24832\n"
"s_waitcnt lgkmcnt(4)\n"
"v_mfma_f32_16x16x32_bf16 v[208:211], a[140:143], v[188:191], v[208:211]\n"
"v_mfma_f32_16x16x32_bf16 v[212:215], a[172:175], v[188:191], v[212:215]\n"
"s_waitcnt vmcnt(20)\n"
"ds_read_b64_tr_b16 a[80:81], v48 offset:26624\n"
"ds_read_b64_tr_b16 a[82:83], v48 offset:26880\n"
"ds_read_b64_tr_b16 a[112:113], v49 offset:26624\n"
"ds_read_b64_tr_b16 a[114:115], v49 offset:26880\n"
"s_waitcnt lgkmcnt(4)\n"
"v_mfma_f32_16x16x32_bf16 v[192:195], v[188:191], a[16:19], v[192:195]\n"
"v_mfma_f32_16x16x32_bf16 v[196:199], v[188:191], a[48:51], v[196:199]\n"
"s_waitcnt vmcnt(18)\n"
"ds_read_b64_tr_b16 a[144:145], v48 offset:28672\n"
"ds_read_b64_tr_b16 a[146:147], v48 offset:28928\n"
"ds_read_b64_tr_b16 a[176:177], v49 offset:28672\n"
"ds_read_b64_tr_b16 a[178:179], v49 offset:28928\n"
"s_waitcnt lgkmcnt(4)\n"
"v_mfma_f32_16x16x32_bf16 v[200:203], v[188:191], a[80:83], v[200:203]\n"
"v_mfma_f32_16x16x32_bf16 v[204:207], v[188:191], a[112:115], v[204:207]\n"
"s_waitcnt vmcnt(16)\n"
"ds_read_b64_tr_b16 a[20:21], v48 offset:30720\n"
"ds_read_b64_tr_b16 a[22:23], v48 offset:30976\n"
"ds_read_b64_tr_b16 a[52:53], v49 offset:30720\n"
"ds_read_b64_tr_b16 a[54:55], v49 offset:30976\n"
"s_waitcnt lgkmcnt(4)\n"
"v_mfma_f32_16x16x32_bf16 v[208:211], a[144:147], v[188:191], v[208:211]\n"
"v_mfma_f32_16x16x32_bf16 v[212:215], a[176:179], v[188:191], v[212:215]\n"
"s_waitcnt vmcnt(14)\n"
"ds_read_b64_tr_b16 a[84:85], v48 offset:32768\n"
"ds_read_b64_tr_b16 a[86:87], v48 offset:33024\n"
"ds_read_b64_tr_b16 a[116:117], v49 offset:32768\n"
"ds_read_b64_tr_b16 a[118:119], v49 offset:33024\n"
"s_waitcnt lgkmcnt(4)\n"
"v_mfma_f32_16x16x32_bf16 v[192:195], v[188:191], a[20:23], v[192:195]\n"
"v_mfma_f32_16x16x32_bf16 v[196:199], v[188:191], a[52:55], v[196:199]\n"
"s_waitcnt vmcnt(12)\n"
"ds_read_b64_tr_b16 a[148:149], v48 offset:34816\n"
"ds_read_b64_tr_b16 a[150:151], v48 offset:35072\n"
"ds_read_b64_tr_b16 a[180:181], v49 offset:34816\n"
"ds_read_b64_tr_b16 a[182:183], v49 offset:35072\n"
"s_waitcnt lgkmcnt(4)\n"
"v_mfma_f32_16x16x32_bf16 v[200:203], v[188:191], a[84:87], v[200:203]\n"
"v_mfma_f32_16x16x32_bf16 v[204:207], v[188:191], a[116:119], v[204:207]\n"
"s_waitcnt vmcnt(10)\n"
"ds_read_b64_tr_b16 a[24:25], v48 offset:36864\n"
"ds_read_b64_tr_b16 a[26:27], v48 offset:37120\n"
"ds_read_b64_tr_b16 a[56:57], v49 offset:36864\n"
"ds_read_b64_tr_b16 a[58:59], v49 offset:37120\n"
"s_waitcnt lgkmcnt(4)\n"
"v_mfma_f32_16x16x32_bf16 v[208:211], a[148:151], v[188:191], v[208:211]\n"
"v_mfma_f32_16x16x32_bf16 v[212:215], a[180:183], v[188:191], v[212:215]\n"
"s_waitcnt vmcnt(8)\n"
"ds_read_b64_tr_b16 a[88:89], v48 offset:38912\n"
"ds_read_b64_tr_b16 a[90:91], v48 offset:39168\n"
"ds_read_b64_tr_b16 a[120:121], v49 offset:38912\n"
"ds_read_b64_tr_b16 a[122:123], v49 offset:39168\n"
"s_waitcnt lgkmcnt(4)\n"
"v_mfma_f32_16x16x32_bf16 v[192:195], v[188:191], a[24:27], v[192:195]\n"
"v_mfma_f32_16x16x32_bf16 v[196:199], v[188:191], a[56:59], v[196:199]\n"
"buffer_load_dwordx4 v[240:243], v50, s[40:43], s50 offen sc1\n"
"buffer_load_dwordx4 v[244:247], v51, s[40:43], s50 offen sc1\n"
"s_waitcnt vmcnt(8)\n"
"ds_read_b64_tr_b16 a[152:153], v48 offset:0\n"
"ds_read_b64_tr_b16 a[154:155], v48 offset:256\n"
"ds_read_b64_tr_b16 a[184:185], v49 offset:0\n"
"ds_read_b64_tr_b16 a[186:187], v49 offset:256\n"
"s_waitcnt lgkmcnt(4)\n"
"v_mfma_f32_16x16x32_bf16 v[200:203], v[188:191], a[88:91], v[200:203]\n"
"v_mfma_f32_16x16x32_bf16 v[204:207], v[188:191], a[120:123], v[204:207]\n"
"s_waitcnt vmcnt(6)\n"
"ds_read_b64_tr_b16 a[28:29], v48 offset:2048\n"
"ds_read_b64_tr_b16 a[30:31], v48 offset:2304\n"
"ds_read_b64_tr_b16 a[60:61], v49 offset:2048\n"
"ds_read_b64_tr_b16 a[62:63], v49 offset:2304\n"
"s_waitcnt lgkmcnt(4)\n"
"v_mfma_f32_16x16x32_bf16 v[208:211], a[152:155], v[188:191], v[208:211]\n"
"v_mfma_f32_16x16x32_bf16 v[212:215], a[184:187], v[188:191], v[212:215]\n"
"s_waitcnt vmcnt(4)\n"
"ds_read_b64_tr_b16 a[92:93], v48 offset:4096\n"
"ds_read_b64_tr_b16 a[94:95], v48 offset:4352\n"
"ds_read_b64_tr_b16 a[124:125], v49 offset:4096\n"
"ds_read_b64_tr_b16 a[126:127], v49 offset:4352\n"
"s_waitcnt lgkmcnt(4)\n"
"v_mfma_f32_16x16x32_bf16 v[192:195], v[188:191], a[28:31], v[192:195]\n"
"v_mfma_f32_16x16x32_bf16 v[196:199], v[188:191], a[60:63], v[196:199]\n"
"s_waitcnt vmcnt(2)\n"
"ds_read_b64_tr_b16 a[156:157], v48 offset:6144\n"
"ds_read_b64_tr_b16 a[158:159], v48 offset:6400\n"
"ds_read_b64_tr_b16 a[188:189], v49 offset:6144\n"
"ds_read_b64_tr_b16 a[190:191], v49 offset:6400\n"
"s_waitcnt lgkmcnt(4)\n"
"v_mfma_f32_16x16x32_bf16 v[200:203], v[188:191], a[92:95], v[200:203]\n"
"v_mfma_f32_16x16x32_bf16 v[204:207], v[188:191], a[124:127], v[204:207]\n"
"global_load_dwordx4 v[228:231], v52, s[56:57] offset:-4096 sc1\n"
"global_load_dwordx4 v[228:231], v52, s[56:57] offset:-3072 sc1\n"
"global_load_dwordx4 v[228:231], v52, s[56:57] offset:-2048 sc1\n"
"global_load_dwordx4 v[228:231], v52, s[56:57] offset:-1024 sc1\n"
"global_load_dwordx4 v[228:231], v52, s[56:57] offset:0 sc1\n"
"global_load_dwordx4 v[228:231], v52, s[56:57] offset:1024 sc1\n"
"global_load_dwordx4 v[228:231], v52, s[56:57] offset:2048 sc1\n"
"global_load_dwordx4 v[228:231], v52, s[56:57] offset:3072 sc1\n"
"s_waitcnt lgkmcnt(0)\n"
"v_mfma_f32_16x16x32_bf16 v[208:211], a[156:159], v[188:191], v[208:211]\n"
"v_mfma_f32_16x16x32_bf16 v[212:215], a[188:191], v[188:191], v[212:215]\n"
"s_mov_b32 m0, s59\n"
"s_barrier\n"
"ds_write_addtid_b32 v192 offset:0\n"
"ds_write_addtid_b32 v193 offset:4640\n"
"ds_write_addtid_b32 v194 offset:9344\n"
"ds_write_addtid_b32 v195 offset:13984\n"
"ds_write_addtid_b32 v196 offset:256\n"
"ds_write_addtid_b32 v197 offset:4896\n"
"ds_write_addtid_b32 v198 offset:9600\n"
"ds_write_addtid_b32 v199 offset:14240\n"
"ds_write_addtid_b32 v200 offset:512\n"
"ds_write_addtid_b32 v201 offset:5152\n"
"ds_write_addtid_b32 v202 offset:9856\n"
"ds_write_addtid_b32 v203 offset:14496\n"
"ds_write_addtid_b32 v204 offset:768\n"
"ds_write_addtid_b32 v205 offset:5408\n"
"ds_write_addtid_b32 v206 offset:10112\n"
"ds_write_addtid_b32 v207 offset:14752\n"
"ds_write_addtid_b32 v208 offset:1024\n"
"ds_write_addtid_b32 v209 offset:5664\n"
"ds_write_addtid_b32 v210 offset:10368\n"
"ds_write_addtid_b32 v211 offset:15008\n"
"ds_write_addtid_b32 v212 offset:1280\n"
"ds_write_addtid_b32 v213 offset:5920\n"
"ds_write_addtid_b32 v214 offset:10624\n"
"ds_write_addtid_b32 v215 offset:15264\n"
"s_waitcnt lgkmcnt(0)\n"
"s_barrier\n"
"ds_read_b128 v[96:99], v2 offset:0\n"
"ds_read_b128 v[100:103], v3 offset:0\n"
"ds_read_b128 v[104:107], v4 offset:0\n"
"ds_read_b128 v[108:111], v57 offset:0\n"
"ds_read_b128 v[112:115], v2 offset:256\n"
"ds_read_b128 v[116:119], v3 offset:256\n"
"ds_read_b128 v[120:123], v4 offset:256\n"
"ds_read_b128 v[124:127], v57 offset:256\n"
"ds_read_b128 v[128:131], v2 offset:512\n"
"ds_read_b128 v[132:135], v3 offset:512\n"
"ds_read_b128 v[136:139], v4 offset:512\n"
"ds_read_b128 v[140:143], v57 offset:512\n"
"ds_read_b128 v[144:147], v2 offset:768\n"
"ds_read_b128 v[148:151], v3 offset:768\n"
"ds_read_b128 v[152:155], v4 offset:768\n"
"ds_read_b128 v[156:159], v57 offset:768\n"
"ds_read_b128 v[160:163], v2 offset:1024\n"
"ds_read_b128 v[164:167], v3 offset:1024\n"
"ds_read_b128 v[168:171], v4 offset:1024\n"
"ds_read_b128 v[172:175], v57 offset:1024\n"
"ds_read_b128 v[176:179], v2 offset:1280\n"
"ds_read_b128 v[180:183], v3 offset:1280\n"
"ds_read_b128 v[184:187], v4 offset:1280\n"
"ds_read_b128 v[188:191], v57 offset:1280\n"
"s_waitcnt lgkmcnt(0)\n"
"s_barrier\n"
"v_add_f32 v96, v96, v100\n"
"v_add_f32 v104, v104, v108\n"
"v_add_f32 v28, v96, v104\n"
"v_add_f32 v97, v97, v101\n"
"v_add_f32 v105, v105, v109\n"
"v_add_f32 v29, v97, v105\n"
"v_add_f32 v98, v98, v102\n"
"v_add_f32 v106, v106, v110\n"
"v_add_f32 v30, v98, v106\n"
"v_add_f32 v99, v99, v103\n"
"v_add_f32 v107, v107, v111\n"
"v_add_f32 v31, v99, v107\n"
"v_add_f32 v112, v112, v116\n"
"v_add_f32 v120, v120, v124\n"
"v_add_f32 v32, v112, v120\n"
"v_add_f32 v113, v113, v117\n"
"v_add_f32 v121, v121, v125\n"
"v_add_f32 v33, v113, v121\n"
"v_add_f32 v114, v114, v118\n"
"v_add_f32 v122, v122, v126\n"
"v_add_f32 v34, v114, v122\n"
"v_add_f32 v115, v115, v119\n"
"v_add_f32 v123, v123, v127\n"
"v_add_f32 v35, v115, v123\n"
"v_add_f32 v128, v128, v132\n"
"v_add_f32 v136, v136, v140\n"
"v_add_f32 v36, v128, v136\n"
"v_add_f32 v129, v129, v133\n"
"v_add_f32 v137, v137, v141\n"
"v_add_f32 v37, v129, v137\n"
"v_add_f32 v130, v130, v134\n"
"v_add_f32 v138, v138, v142\n"
"v_add_f32 v38, v130, v138\n"
"v_add_f32 v131, v131, v135\n"
"v_add_f32 v139, v139, v143\n"
"v_add_f32 v39, v131, v139\n"
"v_add_f32 v144, v144, v148\n"
"v_add_f32 v152, v152, v156\n"
"v_add_f32 v40, v144, v152\n"
"v_add_f32 v145, v145, v149\n"
"v_add_f32 v153, v153, v157\n"
"v_add_f32 v41, v145, v153\n"
"v_add_f32 v146, v146, v150\n"
"v_add_f32 v154, v154, v158\n"
"v_add_f32 v42, v146, v154\n"
"v_add_f32 v147, v147, v151\n"
"v_add_f32 v155, v155, v159\n"
"v_add_f32 v43, v147, v155\n"
"v_add_f32 v160, v160, v164\n"
"v_add_f32 v168, v168, v172\n"
"v_add_f32 v44, v160, v168\n"
"v_add_f32 v176, v176, v180\n"
"v_add_f32 v184, v184, v188\n"
"v_add_f32 v45, v176, v184\n"
"v_mov_b32 v128, 0\n"
"v_mov_b32 v129, 0\n"
"v_mov_b32 v130, 0\n"
"v_mov_b32 v131, 0\n"
"v_mov_b32 v136, 0\n"
"v_mov_b32 v137, 0\n"
"v_mov_b32 v138, 0\n"
"v_mov_b32 v139, 0\n"
"v_mov_b32 v144, 0\n"
"v_mov_b32 v145, 0\n"
"v_mov_b32 v146, 0\n"
"v_mov_b32 v147, 0\n"
"v_mov_b32 v152, 0\n"
"v_mov_b32 v153, 0\n"
"v_mov_b32 v154, 0\n"
"v_mov_b32 v155, 0\n"
"s_mov_b32 s95, 0x80000000\n"
"s_mov_b32 s94, 0x80000000\n"
"s_waitcnt vmcnt(8)\n"
"v_cmp_ne_u64 vcc, s[48:49], v[242:243]\n"
"s_cmp_lg_u32 s54, 0\n"
"s_cbranch_scc1 .LQ_e0\n"
"v_mov_b32 v128, v240\n"
"v_mov_b32 v129, v241\n"
"v_mov_b32 v130, v242\n"
"v_mov_b32 v131, v243\n"
"v_mov_b32 v136, v244\n"
"v_mov_b32 v137, v245\n"
"v_mov_b32 v138, v246\n"
"v_mov_b32 v139, v247\n"
"s_cbranch_vccnz .LQ_1s\n"
"s_branch .LQ_1r\n"
".LQ_e0:\n"
"s_cmp_lg_u32 s54, 1\n"
"s_cbranch_scc1 .LQ_e1\n"
"v_mov_b32 v136, v240\n"
"v_mov_b32 v137, v241\n"
"v_mov_b32 v138, v242\n"
"v_mov_b32 v139, v243\n"
"v_mov_b32 v144, v244\n"
"v_mov_b32 v145, v245\n"
"v_mov_b32 v146, v246\n"
"v_mov_b32 v147, v247\n"
"s_cbranch_vccnz .LQ_2s\n"
"s_branch .LQ_2r\n"
".LQ_e1:\n"
"s_cmp_lg_u32 s54, 2\n"
"s_cbranch_scc1 .LQ_e2\n"
"v_mov_b32 v144, v240\n"
"v_mov_b32 v145, v241\n"
"v_mov_b32 v146, v242\n"
"v_mov_b32 v147, v243\n"
"v_mov_b32 v152, v244\n"
"v_mov_b32 v153, v245\n"
"v_mov_b32 v154, v246\n"
"v_mov_b32 v155, v247\n"
"s_cbranch_vccnz .LQ_3s\n"
"s_branch .LQ_3r\n"
".LQ_e2:\n"
"v_mov_b32 v152, v240\n"
"v_mov_b32 v153, v241\n"
"v_mov_b32 v154, v242\n"
"v_mov_b32 v155, v243\n"
"v_mov_b32 v128, v244\n"
"v_mov_b32 v129, v245\n"
"v_mov_b32 v130, v246\n"
"v_mov_b32 v131, v247\n"
"s_cbranch_vccnz .LQ_4s\n"
"s_branch .LQ_4r\n"
".LQ_e3:\n"
".p2align 6\n"
".LQ_top:\n"
"v_mfma_f32_16x16x32_bf16 v[112:115], a[128:131], v[48:51], 0\n"
"s_add_u32 s50, s50, 0x100000\n"
"global_load_dwordx4 a[192:195], v5, s[56:57] offset:0 sc1\n"
"v_mfma_f32_16x16x32_bf16 v[112:115], a[132:135], v[52:55], v[112:115]\n"
"ds_write_addtid_b32 v132 offset:4352\n"
"v_mfma_f32_16x16x32_bf16 v[112:115], a[136:139], v[56:59], v[112:115]\n"
"ds_write_addtid_b32 v133 offset:8992\n"
"v_mfma_f32_16x16x32_bf16 v[112:115], a[140:143], v[60:63], v[112:115]\n"
"ds_write_addtid_b32 v134 offset:13696\n"
"v_mfma_f32_16x16x32_bf16 v[112:115], a[144:147], v[64:67], v[112:115]\n"
"ds_write_addtid_b32 v135 offset:18336\n"
"v_mov_b32 v255, v128\n"
"v_mfma_f32_16x16x32_bf16 v[112:115], a[148:151], v[68:71], v[112:115]\n"
"global_load_dwordx4 a[196:199], v5, s[56:57] offset:1024 sc1\n"
"v_mul_f32 v119, v129, v39\n"
"v_mfma_f32_16x16x32_bf16 v[112:115], a[152:155], v[72:75], v[112:115]\n"
"v_mul_f32 v117, v129, v37\n"
"v_mul_f32 v118, v129, v38\n"
"v_mfma_f32_16x16x32_bf16 v[112:115], a[156:159], v[76:79], v[112:115]\n"
"v_mul_f32 v123, v129, v43\n"
"v_mul_f32 v116, v129, v36\n"
"v_mfma_f32_16x16x32_bf16 v[124:127], a[160:163], v[48:51], 0\n"
"v_mul_f32 v121, v129, v41\n"
"v_mul_f32 v122, v129, v42\n"
"v_mfma_f32_16x16x32_bf16 v[124:127], a[164:167], v[52:55], v[124:127]\n"
"ds_write_addtid_b32 v112 offset:0\n"
"v_mul_f32 v120, v129, v40\n"
"v_mfma_f32_16x16x32_bf16 v[124:127], a[168:171], v[56:59], v[124:127]\n"
"s_waitcnt lgkmcnt(1)\n"
"ds_write_addtid_b32 v113 offset:4640\n"
"global_load_dwordx4 a[200:203], v6, s[56:57] offset:0 sc1\n"
"v_mfma_f32_16x16x32_bf16 v[124:127], a[172:175], v[60:63], v[124:127]\n"
"s_barrier\n"
"v_mfma_f32_16x16x32_bf16 v[124:127], a[176:179], v[64:67], v[124:127]\n"
"ds_write_addtid_b32 v114 offset:9344\n"
"ds_read2st64_b32 v[128:129], v12 offset0:0 offset1:1\n"
"v_mfma_f32_16x16x32_bf16 v[124:127], a[180:183], v[68:71], v[124:127]\n"
"ds_write_addtid_b32 v115 offset:13984\n"
"ds_read2st64_b32 v[130:131], v12 offset0:2 offset1:3\n"
"v_mfma_f32_16x16x32_bf16 v[124:127], a[184:187], v[72:75], v[124:127]\n"
"ds_read2st64_b32 v[140:141], v11 offset0:4 offset1:5\n"
"ds_read2st64_b32 v[148:149], v11 offset0:6 offset1:7\n"
"v_mfma_f32_16x16x32_bf16 v[124:127], a[188:191], v[76:79], v[124:127]\n"
"s_add_u32 s95, s50, 0xffc00000\n"
"s_and_b32 s94, s95, 0xfbffffff\n"
"v_mfma_f32_16x16x32_bf16 v[112:115], v[48:51], a[0:3], 0\n"
"global_load_dwordx4 a[204:207], v6, s[56:57] offset:1024 sc1\n"
"v_mfma_f32_16x16x32_bf16 v[112:115], v[52:55], a[4:7], v[112:115]\n"
"ds_write_addtid_b32 v124 offset:256\n"
"v_mfma_f32_16x16x32_bf16 v[112:115], v[56:59], a[8:11], v[112:115]\n"
"s_waitcnt lgkmcnt(3)\n"
"ds_write_addtid_b32 v125 offset:4896\n"
"v_mfma_f32_16x16x32_bf16 v[132:135], v[128:131], v[164:167], v[16:19]\n"
"s_cmp_eq_u32 s95, s62\n"
"s_cbranch_scc1 .LQ_fs\n"
".LQ_fr:\n"
"v_mfma_f32_16x16x32_bf16 v[112:115], v[60:63], a[12:15], v[112:115]\n"
"ds_write_addtid_b32 v126 offset:9600\n"
"v_max_f32 v254, v156, v158\n"
"v_mfma_f32_16x16x32_bf16 v[112:115], v[64:67], a[16:19], v[112:115]\n"
"ds_write_addtid_b32 v127 offset:14240\n"
"global_load_dwordx4 a[208:211], v7, s[56:57] offset:0 sc1\n"
"v_mfma_f32_16x16x32_bf16 v[112:115], v[68:71], a[20:23], v[112:115]\n"
"v_max_f32 v253, v157, v159\n"
"v_max3_f32 v254, v132, v134, v254\n"
"v_mfma_f32_16x16x32_bf16 v[112:115], v[72:75], a[24:27], v[112:115]\n"
"v_max3_f32 v253, v133, v135, v253\n"
"v_max_f32 v252, v254, v253\n"
"v_mfma_f32_16x16x32_bf16 v[112:115], v[76:79], a[28:31], v[112:115]\n"
"v_mov_b32 v251, v252\n"
"v_mfma_f32_16x16x32_bf16 v[124:127], v[48:51], a[32:35], 0\n"
"v_permlane32_swap_b32 v251, v252\n"
"v_mfma_f32_16x16x32_bf16 v[124:127], v[52:55], a[36:39], v[124:127]\n"
"ds_write_addtid_b32 v112 offset:512\n"
"global_load_dwordx4 a[212:215], v7, s[56:57] offset:1024 sc1\n"
"v_mfma_f32_16x16x32_bf16 v[124:127], v[56:59], a[40:43], v[124:127]\n"
"ds_write_addtid_b32 v113 offset:5152\n"
"v_max_f32 v252, v251, v252\n"
"v_mfma_f32_16x16x32_bf16 v[124:127], v[60:63], a[44:47], v[124:127]\n"
"ds_write_addtid_b32 v114 offset:9856\n"
"v_mov_b32 v251, v252\n"
"v_mfma_f32_16x16x32_bf16 v[124:127], v[64:67], a[48:51], v[124:127]\n"
"ds_write_addtid_b32 v115 offset:14496\n"
"v_mfma_f32_16x16x32_bf16 v[124:127], v[68:71], a[52:55], v[124:127]\n"
"v_permlane16_swap_b32 v251, v252\n"
"v_mfma_f32_16x16x32_bf16 v[124:127], v[72:75], a[56:59], v[124:127]\n"
"global_load_dwordx4 a[216:219], v8, s[56:57] offset:0 sc1\n"
"v_max_f32 v252, v251, v252\n"
"v_mfma_f32_16x16x32_bf16 v[124:127], v[76:79], a[60:63], v[124:127]\n"
"v_mul_f32 v252, v252, v250\n"
"v_fma_f32 v254, v254, v250, -v252\n"
"v_mfma_f32_16x16x32_bf16 v[116:119], a[64:67], v[48:51], v[116:119]\n"
"v_fma_f32 v253, v253, v250, -v252\n"
"v_mfma_f32_16x16x32_bf16 v[116:119], a[68:71], v[52:55], v[116:119]\n"
"ds_write_addtid_b32 v124 offset:768\n"
"v_mfma_f32_16x16x32_bf16 v[116:119], a[72:75], v[56:59], v[116:119]\n"
"ds_write_addtid_b32 v125 offset:5408\n"
"v_mfma_f32_16x16x32_bf16 v[116:119], a[76:79], v[60:63], v[116:119]\n"
"ds_write_addtid_b32 v126 offset:10112\n"
"global_load_dwordx4 a[220:223], v8, s[56:57] offset:1024 sc1\n"
"v_mfma_f32_16x16x32_bf16 v[116:119], a[80:83], v[64:67], v[116:119]\n"
"ds_write_addtid_b32 v127 offset:14752\n"
"v_mfma_f32_16x16x32_bf16 v[116:119], a[84:87], v[68:71], v[116:119]\n"
"v_exp_f32 v254, v254\n"
"v_mfma_f32_16x16x32_bf16 v[116:119], a[88:91], v[72:75], v[116:119]\n"
"v_exp_f32 v253, v253\n"
"v_mfma_f32_16x16x32_bf16 v[116:119], a[92:95], v[76:79], v[116:119]\n"
"s_add_u32 s58, s50, 0x100000\n"
"v_add_f32 v252, v254, v253\n"
"v_mov_b32 v251, v252\n"
"v_mfma_f32_16x16x32_bf16 v[120:123], a[96:99], v[48:51], v[120:123]\n"
"s_and_b32 s58, s58, 0x3ffffff\n"
"v_permlane32_swap_b32 v251, v252\n"
"v_mfma_f32_16x16x32_bf16 v[120:123], a[100:103], v[52:55], v[120:123]\n"
"s_add_u32 s56, s40, s58\n"
"v_add_f32 v252, v251, v252\n"
"v_mov_b32 v251, v252\n"
"v_mfma_f32_16x16x32_bf16 v[120:123], a[104:107], v[56:59], v[120:123]\n"
"s_addc_u32 s57, s41, 0\n"
"v_permlane16_swap_b32 v251, v252\n"
"v_mfma_f32_16x16x32_bf16 v[120:123], a[108:111], v[60:63], v[120:123]\n"
"s_waitcnt vmcnt(8)\n"
"v_cmp_ne_u64 vcc, s[48:49], v[154:155]\n"
"v_add_f32 v252, v251, v252\n"
"v_mfma_f32_16x16x32_bf16 v[120:123], a[112:115], v[64:67], v[120:123]\n"
"buffer_load_dwordx4 v[128:131], v9, s[40:43], s50 offen sc1\n"
"v_mfma_f32_16x16x32_bf16 v[120:123], a[116:119], v[68:71], v[120:123]\n"
"s_cbranch_vccnz .LQ_4s\n"
".LQ_4r:\n"
"v_mfma_f32_16x16x32_bf16 v[120:123], a[120:123], v[72:75], v[120:123]\n"
"s_add_u32 s50, s50, 0x100000\n"
"v_rcp_f32 v252, v252\n"
"v_mfma_f32_16x16x32_bf16 v[120:123], a[124:127], v[76:79], v[120:123]\n"
"global_load_dwordx4 a[224:227], v5, s[56:57] offset:0 sc1\n"
"v_mul_f32 v254, v254, v252\n"
"v_mfma_f32_16x16x32_bf16 v[48:51], v[80:83], a[64:67], 0\n"
"v_mul_f32 v253, v253, v252\n"
"v_cvt_pk_bf16_f32 v251, v254, v253\n"
"v_mfma_f32_16x16x32_bf16 v[48:51], v[84:87], a[68:71], v[48:51]\n"
"v_mfma_f32_16x16x32_bf16 v[48:51], v[88:91], a[72:75], v[48:51]\n"
"s_waitcnt lgkmcnt(12)\n"
"v_and_b32 v52, v251, v24\n"
"v_and_b32 v53, v251, v25\n"
"v_mfma_f32_16x16x32_bf16 v[48:51], v[92:95], a[76:79], v[48:51]\n"
"v_and_b32 v54, v251, v26\n"
"v_and_b32 v55, v251, v27\n"
"v_mfma_f32_16x16x32_bf16 v[48:51], v[96:99], a[80:83], v[48:51]\n"
"global_load_dwordx4 a[228:231], v5, s[56:57] offset:1024 sc1\n"
"v_mfma_f32_16x16x32_bf16 v[56:59], v[140:143], v[52:55], 0\n"
"v_mul_f32_dpp v75, v137, v44 row_newbcast:3 row_mask:0xf bank_mask:0xf\n"
"v_mov_b32 v254, v136\n"
"v_mfma_f32_16x16x32_bf16 v[60:63], v[148:151], v[52:55], 0\n"
"v_mul_f32_dpp v73, v137, v44 row_newbcast:1 row_mask:0xf bank_mask:0xf\n"
"v_mul_f32_dpp v74, v137, v44 row_newbcast:2 row_mask:0xf bank_mask:0xf\n"
"v_mfma_f32_16x16x32_bf16 v[48:51], v[100:103], a[84:87], v[48:51]\n"
"v_cvt_pk_bf16_f32 v52, v56, v57\n"
"v_cvt_pk_bf16_f32 v53, v58, v59\n"
"v_mfma_f32_16x16x32_bf16 v[48:51], v[104:107], a[88:91], v[48:51]\n"
"v_cvt_pk_bf16_f32 v54, v60, v61\n"
"v_cvt_pk_bf16_f32 v55, v62, v63\n"
"v_mfma_f32_16x16x32_bf16 v[48:51], v[108:111], a[92:95], v[48:51]\n"
"buffer_store_dwordx4 v[52:55], v13, s[40:43], s94 offen sc1\n"
"v_mul_f32_dpp v114, v137, v45 row_newbcast:2 row_mask:0xf bank_mask:0xf\n"
"v_mul_f32_dpp v115, v137, v45 row_newbcast:3 row_mask:0xf bank_mask:0xf\n"
"v_mul_f32_dpp v72, v137, v44 row_newbcast:0 row_mask:0xf bank_mask:0xf\n"
"v_mfma_f32_16x16x32_bf16 v[52:55], v[80:83], a[96:99], 0\n"
"s_waitcnt lgkmcnt(0)\n"
"v_mul_f32_dpp v112, v137, v45 row_newbcast:0 row_mask:0xf bank_mask:0xf\n"
"v_mul_f32_dpp v113, v137, v45 row_newbcast:1 row_mask:0xf bank_mask:0xf\n"
"v_mfma_f32_16x16x32_bf16 v[52:55], v[84:87], a[100:103], v[52:55]\n"
"s_barrier\n"
"v_mfma_f32_16x16x32_bf16 v[52:55], v[88:91], a[104:107], v[52:55]\n"
"ds_write_addtid_b32 v48 offset:1024\n"
"ds_read_b128 v[56:59], v2 offset:1024\n"
"v_mfma_f32_16x16x32_bf16 v[52:55], v[92:95], a[108:111], v[52:55]\n"
"ds_write_addtid_b32 v49 offset:5664\n"
"ds_read_b128 v[60:63], v3 offset:2048\n"
"v_mfma_f32_16x16x32_bf16 v[52:55], v[96:99], a[112:115], v[52:55]\n"
"ds_write_addtid_b32 v50 offset:10368\n"
"global_load_dwordx4 a[232:235], v6, s[56:57] offset:0 sc1\n"
"v_mfma_f32_16x16x32_bf16 v[52:55], v[100:103], a[116:119], v[52:55]\n"
"ds_write_addtid_b32 v51 offset:15008\n"
"ds_read_b128 v[64:67], v4 offset:3072\n"
"v_mfma_f32_16x16x32_bf16 v[52:55], v[104:107], a[120:123], v[52:55]\n"
"ds_read_b128 v[68:71], v2 offset:1280\n"
"ds_read_b128 v[48:51], v3 offset:2304\n"
"v_mfma_f32_16x16x32_bf16 v[52:55], v[108:111], a[124:127], v[52:55]\n"
"ds_read_b128 v[76:79], v4 offset:3328\n"
"v_mfma_f32_16x16x32_bf16 v[124:127], v[80:83], a[0:3], 0\n"
"global_load_dwordx4 a[236:239], v6, s[56:57] offset:1024 sc1\n"
"v_mfma_f32_16x16x32_bf16 v[124:127], v[84:87], a[4:7], v[124:127]\n"
"ds_write_addtid_b32 v52 offset:1280\n"
"v_mfma_f32_16x16x32_bf16 v[124:127], v[88:91], a[8:11], v[124:127]\n"
"ds_write_addtid_b32 v53 offset:5920\n"
"v_mfma_f32_16x16x32_bf16 v[124:127], v[92:95], a[12:15], v[124:127]\n"
"s_waitcnt lgkmcnt(5)\n"
"ds_write_addtid_b32 v54 offset:10624\n"
"v_add_f32 v56, v56, v60\n"
"v_mfma_f32_16x16x32_bf16 v[124:127], v[96:99], a[16:19], v[124:127]\n"
"ds_write_addtid_b32 v55 offset:15264\n"
"v_add_f32 v57, v57, v61\n"
"v_mfma_f32_16x16x32_bf16 v[124:127], v[100:103], a[20:23], v[124:127]\n"
"global_load_dwordx4 a[240:243], v7, s[56:57] offset:0 sc1\n"
"v_add_f32 v58, v58, v62\n"
"v_mfma_f32_16x16x32_bf16 v[124:127], v[104:107], a[24:27], v[124:127]\n"
"v_add_f32 v59, v59, v63\n"
"v_add_f32 v56, v56, v64\n"
"v_mfma_f32_16x16x32_bf16 v[124:127], v[108:111], a[28:31], v[124:127]\n"
"v_add_f32 v57, v57, v65\n"
"v_add_f32 v58, v58, v66\n"
"v_mfma_f32_16x16x32_bf16 v[52:55], v[80:83], a[32:35], 0\n"
"v_add_f32 v59, v59, v67\n"
"v_add_f32 v56, v56, v116\n"
"v_mfma_f32_16x16x32_bf16 v[52:55], v[84:87], a[36:39], v[52:55]\n"
"ds_write_addtid_b32 v124 offset:1536\n"
"v_add_f32 v57, v57, v117\n"
"v_mfma_f32_16x16x32_bf16 v[52:55], v[88:91], a[40:43], v[52:55]\n"
"ds_write_addtid_b32 v125 offset:6176\n"
"global_load_dwordx4 a[244:247], v7, s[56:57] offset:1024 sc1\n"
"v_mfma_f32_16x16x32_bf16 v[52:55], v[92:95], a[44:47], v[52:55]\n"
"ds_write_addtid_b32 v126 offset:10880\n"
"v_add_f32 v58, v58, v118\n"
"v_mfma_f32_16x16x32_bf16 v[52:55], v[96:99], a[48:51], v[52:55]\n"
"ds_write_addtid_b32 v127 offset:15520\n"
"v_add_f32 v59, v59, v119\n"
"v_mfma_f32_16x16x32_bf16 v[52:55], v[100:103], a[52:55], v[52:55]\n"
"v_mul_f32 v56, v255, v56\n"
"v_mul_f32 v57, v255, v57\n"
"v_mfma_f32_16x16x32_bf16 v[52:55], v[104:107], a[56:59], v[52:55]\n"
"v_mul_f32 v58, v255, v58\n"
"v_mul_f32 v59, v255, v59\n"
"v_mfma_f32_16x16x32_bf16 v[52:55], v[108:111], a[60:63], v[52:55]\n"
"global_load_dwordx4 a[248:251], v8, s[56:57] offset:0 sc1\n"
"v_cvt_pk_bf16_f32 v60, v56, v57\n"
"v_mfma_f32_16x16x32_bf16 v[72:75], v[80:83], a[128:131], v[72:75]\n"
"v_cvt_pk_bf16_f32 v61, v58, v59\n"
"v_mfma_f32_16x16x32_bf16 v[72:75], v[84:87], a[132:135], v[72:75]\n"
"ds_write_addtid_b32 v52 offset:1792\n"
"ds_write_addtid_b32 v60 offset:18592\n"
"v_mfma_f32_16x16x32_bf16 v[72:75], v[88:91], a[136:139], v[72:75]\n"
"ds_write_addtid_b32 v53 offset:6432\n"
"ds_write_addtid_b32 v61 offset:18848\n"
"v_mfma_f32_16x16x32_bf16 v[72:75], v[92:95], a[140:143], v[72:75]\n"
"s_waitcnt lgkmcnt(12)\n"
"ds_write_addtid_b32 v54 offset:11136\n"
"v_add_f32 v68, v68, v48\n"
"v_mfma_f32_16x16x32_bf16 v[72:75], v[96:99], a[144:147], v[72:75]\n"
"ds_write_addtid_b32 v55 offset:15776\n"
"global_load_dwordx4 a[252:255], v8, s[56:57] offset:1024 sc1\n"
"v_mfma_f32_16x16x32_bf16 v[72:75], v[100:103], a[148:151], v[72:75]\n"
"v_add_f32 v69, v69, v49\n"
"v_add_f32 v70, v70, v50\n"
"v_mfma_f32_16x16x32_bf16 v[72:75], v[104:107], a[152:155], v[72:75]\n"
"v_add_f32 v71, v71, v51\n"
"v_add_f32 v68, v68, v76\n"
"v_mfma_f32_16x16x32_bf16 v[72:75], v[108:111], a[156:159], v[72:75]\n"
"v_add_f32 v69, v69, v77\n"
"v_add_f32 v70, v70, v78\n"
"v_mfma_f32_16x16x32_bf16 v[112:115], v[80:83], a[160:163], v[112:115]\n"
"s_add_u32 s58, s50, 0x100000\n"
"v_add_f32 v71, v71, v79\n"
"v_add_f32 v68, v68, v120\n"
"v_mfma_f32_16x16x32_bf16 v[112:115], v[84:87], a[164:167], v[112:115]\n"
"s_and_b32 s58, s58, 0x3ffffff\n"
"v_add_f32 v69, v69, v121\n"
"v_add_f32 v70, v70, v122\n"
"v_mfma_f32_16x16x32_bf16 v[112:115], v[88:91], a[168:171], v[112:115]\n"
"s_add_u32 s56, s40, s58\n"
"v_add_f32 v71, v71, v123\n"
"v_mul_f32 v68, v255, v68\n"
"v_mfma_f32_16x16x32_bf16 v[112:115], v[92:95], a[172:175], v[112:115]\n"
"s_addc_u32 s57, s41, 0\n"
"v_mul_f32 v69, v255, v69\n"
"v_mul_f32 v70, v255, v70\n"
"v_mfma_f32_16x16x32_bf16 v[112:115], v[96:99], a[176:179], v[112:115]\n"
"s_waitcnt vmcnt(9)\n"
"v_cmp_ne_u64 vcc, s[48:49], v[130:131]\n"
"v_mul_f32 v71, v255, v71\n"
"v_mfma_f32_16x16x32_bf16 v[112:115], v[100:103], a[180:183], v[112:115]\n"
"buffer_load_dwordx4 v[136:139], v10, s[40:43], s50 offen sc1\n"
"v_mfma_f32_16x16x32_bf16 v[112:115], v[104:107], a[184:187], v[112:115]\n"
"s_cbranch_vccnz .LQ_1s\n"
".LQ_1r:\n"
"v_cvt_pk_bf16_f32 v62, v68, v69\n"
"v_mfma_f32_16x16x32_bf16 v[112:115], v[108:111], a[188:191], v[112:115]\n"
"v_cvt_pk_bf16_f32 v63, v70, v71\n"
"v_mfma_f32_16x16x32_bf16 v[64:67], a[192:195], a[64:67], 0\n"
"s_add_u32 s50, s50, 0x100000\n"
"global_load_dwordx4 v[48:51], v5, s[56:57] offset:0 sc1\n"
"ds_write_addtid_b32 v62 offset:19104\n"
"v_mfma_f32_16x16x32_bf16 v[64:67], a[196:199], a[68:71], v[64:67]\n"
"ds_write_addtid_b32 v63 offset:19360\n"
"v_mfma_f32_16x16x32_bf16 v[64:67], a[200:203], a[72:75], v[64:67]\n"
"v_mfma_f32_16x16x32_bf16 v[64:67], a[204:207], a[76:79], v[64:67]\n"
"v_mul_f32 v250, 0x3e8293ee, v144\n"
"v_mfma_f32_16x16x32_bf16 v[64:67], a[208:211], a[80:83], v[64:67]\n"
"v_mul_f32 v110, v145, v30\n"
"v_mul_f32 v111, v145, v31\n"
"v_mfma_f32_16x16x32_bf16 v[64:67], a[212:215], a[84:87], v[64:67]\n"
"global_load_dwordx4 v[52:55], v5, s[56:57] offset:1024 sc1\n"
"v_mul_f32 v109, v145, v29\n"
"v_mfma_f32_16x16x32_bf16 v[64:67], a[216:219], a[88:91], v[64:67]\n"
"v_mul_f32 v119, v145, v35\n"
"v_mul_f32 v108, v145, v28\n"
"v_mfma_f32_16x16x32_bf16 v[64:67], a[220:223], a[92:95], v[64:67]\n"
"s_waitcnt vmcnt(9)\n"
"buffer_store_dwordx2 v[0:1], v14, s[40:43], s95 offen offset:0 sc1\n"
"v_mul_f32 v118, v145, v34\n"
"v_mfma_f32_16x16x32_bf16 v[68:71], a[192:195], a[96:99], 0\n"
"s_waitcnt lgkmcnt(0)\n"
"v_mul_f32 v116, v145, v32\n"
"v_mul_f32 v117, v145, v33\n"
"v_mfma_f32_16x16x32_bf16 v[68:71], a[196:199], a[100:103], v[68:71]\n"
"s_barrier\n"
"v_mfma_f32_16x16x32_bf16 v[68:71], a[200:203], a[104:107], v[68:71]\n"
"ds_write_addtid_b32 v64 offset:2048\n"
"global_load_dwordx4 v[56:59], v6, s[56:57] offset:0 sc1\n"
"v_mfma_f32_16x16x32_bf16 v[68:71], a[204:207], a[108:111], v[68:71]\n"
"ds_write_addtid_b32 v65 offset:6688\n"
"ds_read_b128 v[76:79], v2 offset:2560\n"
"v_mfma_f32_16x16x32_bf16 v[68:71], a[208:211], a[112:115], v[68:71]\n"
"ds_write_addtid_b32 v66 offset:11392\n"
"ds_read_b128 v[80:83], v3 offset:3584\n"
"v_mfma_f32_16x16x32_bf16 v[68:71], a[212:215], a[116:119], v[68:71]\n"
"ds_write_addtid_b32 v67 offset:16032\n"
"ds_read_b128 v[84:87], v4 offset:0\n"
"v_mfma_f32_16x16x32_bf16 v[68:71], a[216:219], a[120:123], v[68:71]\n"
"ds_read_b128 v[88:91], v2 offset:2816\n"
"ds_read_b128 v[92:95], v3 offset:3840\n"
"v_mfma_f32_16x16x32_bf16 v[68:71], a[220:223], a[124:127], v[68:71]\n"
"global_load_dwordx4 v[60:63], v6, s[56:57] offset:1024 sc1\n"
"ds_read_b128 v[96:99], v4 offset:256\n"
"v_mfma_f32_16x16x32_bf16 v[100:103], a[128:131], a[192:195], 0\n"
"v_mfma_f32_16x16x32_bf16 v[100:103], a[132:135], a[196:199], v[100:103]\n"
"ds_write_addtid_b32 v68 offset:2304\n"
"v_mfma_f32_16x16x32_bf16 v[100:103], a[136:139], a[200:203], v[100:103]\n"
"ds_write_addtid_b32 v69 offset:6944\n"
"v_mfma_f32_16x16x32_bf16 v[100:103], a[140:143], a[204:207], v[100:103]\n"
"s_waitcnt lgkmcnt(5)\n"
"ds_write_addtid_b32 v70 offset:11648\n"
"v_add_f32 v76, v76, v80\n"
"v_mfma_f32_16x16x32_bf16 v[100:103], a[144:147], a[208:211], v[100:103]\n"
"ds_write_addtid_b32 v71 offset:16288\n"
"global_load_dwordx4 v[64:67], v7, s[56:57] offset:0 sc1\n"
"v_mfma_f32_16x16x32_bf16 v[100:103], a[148:151], a[212:215], v[100:103]\n"
"v_add_f32 v77, v77, v81\n"
"v_add_f32 v78, v78, v82\n"
"v_mfma_f32_16x16x32_bf16 v[100:103], a[152:155], a[216:219], v[100:103]\n"
"v_add_f32 v79, v79, v83\n"
"v_add_f32 v76, v76, v84\n"
"v_mfma_f32_16x16x32_bf16 v[100:103], a[156:159], a[220:223], v[100:103]\n"
"v_add_f32 v77, v77, v85\n"
"v_add_f32 v78, v78, v86\n"
"v_mfma_f32_16x16x32_bf16 v[80:83], a[160:163], a[192:195], 0\n"
"v_add_f32 v79, v79, v87\n"
"v_add_f32 v76, v76, v72\n"
"v_mfma_f32_16x16x32_bf16 v[80:83], a[164:167], a[196:199], v[80:83]\n"
"ds_write_addtid_b32 v100 offset:2560\n"
"global_load_dwordx4 v[68:71], v7, s[56:57] offset:1024 sc1\n"
"v_mfma_f32_16x16x32_bf16 v[80:83], a[168:171], a[200:203], v[80:83]\n"
"ds_write_addtid_b32 v101 offset:7200\n"
"v_add_f32 v77, v77, v73\n"
"v_mfma_f32_16x16x32_bf16 v[80:83], a[172:175], a[204:207], v[80:83]\n"
"ds_write_addtid_b32 v102 offset:11904\n"
"v_add_f32 v78, v78, v74\n"
"v_mfma_f32_16x16x32_bf16 v[80:83], a[176:179], a[208:211], v[80:83]\n"
"ds_write_addtid_b32 v103 offset:16544\n"
"v_add_f32 v79, v79, v75\n"
"v_mfma_f32_16x16x32_bf16 v[80:83], a[180:183], a[212:215], v[80:83]\n"
"v_mul_f32_dpp v76, v254, v76 row_newbcast:0 row_mask:0xf bank_mask:0xf\n"
"v_mul_f32_dpp v77, v254, v77 row_newbcast:1 row_mask:0xf bank_mask:0xf\n"
"v_mfma_f32_16x16x32_bf16 v[80:83], a[184:187], a[216:219], v[80:83]\n"
"global_load_dwordx4 v[72:75], v8, s[56:57] offset:0 sc1\n"
"v_mul_f32_dpp v78, v254, v78 row_newbcast:2 row_mask:0xf bank_mask:0xf\n"
"v_mfma_f32_16x16x32_bf16 v[80:83], a[188:191], a[220:223], v[80:83]\n"
"v_mul_f32_dpp v79, v254, v79 row_newbcast:3 row_mask:0xf bank_mask:0xf\n"
"v_cvt_pk_bf16_f32 v142, v76, v77\n"
"v_mfma_f32_16x16x32_bf16 v[108:111], a[0:3], a[192:195], v[108:111]\n"
"v_cvt_pk_bf16_f32 v143, v78, v79\n"
"v_mfma_f32_16x16x32_bf16 v[108:111], a[4:7], a[196:199], v[108:111]\n"
"ds_write_addtid_b32 v80 offset:2816\n"
"ds_write_addtid_b32 v142 offset:19616\n"
"v_mfma_f32_16x16x32_bf16 v[108:111], a[8:11], a[200:203], v[108:111]\n"
"ds_write_addtid_b32 v81 offset:7456\n"
"ds_write_addtid_b32 v143 offset:19872\n"
"v_mfma_f32_16x16x32_bf16 v[108:111], a[12:15], a[204:207], v[108:111]\n"
"s_waitcnt lgkmcnt(12)\n"
"ds_write_addtid_b32 v82 offset:12160\n"
"global_load_dwordx4 v[76:79], v8, s[56:57] offset:1024 sc1\n"
"v_mfma_f32_16x16x32_bf16 v[108:111], a[16:19], a[208:211], v[108:111]\n"
"ds_write_addtid_b32 v83 offset:16800\n"
"v_add_f32 v88, v88, v92\n"
"v_mfma_f32_16x16x32_bf16 v[108:111], a[20:23], a[212:215], v[108:111]\n"
"v_add_f32 v89, v89, v93\n"
"v_add_f32 v90, v90, v94\n"
"v_mfma_f32_16x16x32_bf16 v[108:111], a[24:27], a[216:219], v[108:111]\n"
"v_add_f32 v91, v91, v95\n"
"v_add_f32 v88, v88, v96\n"
"v_mfma_f32_16x16x32_bf16 v[108:111], a[28:31], a[220:223], v[108:111]\n"
"v_add_f32 v89, v89, v97\n"
"v_add_f32 v90, v90, v98\n"
"v_mfma_f32_16x16x32_bf16 v[116:119], a[32:35], a[192:195], v[116:119]\n"
"v_add_f32 v91, v91, v99\n"
"v_add_f32 v88, v88, v112\n"
"v_mfma_f32_16x16x32_bf16 v[116:119], a[36:39], a[196:199], v[116:119]\n"
"v_add_f32 v89, v89, v113\n"
"v_add_f32 v90, v90, v114\n"
"v_mfma_f32_16x16x32_bf16 v[116:119], a[40:43], a[200:203], v[116:119]\n"
"s_add_u32 s58, s50, 0x100000\n"
"buffer_load_dwordx4 v[144:147], v9, s[40:43], s50 offen sc1\n"
"v_mfma_f32_16x16x32_bf16 v[116:119], a[44:47], a[204:207], v[116:119]\n"
"s_and_b32 s58, s58, 0x3ffffff\n"
"v_add_f32 v91, v91, v115\n"
"v_mul_f32_dpp v88, v254, v88 row_newbcast:0 row_mask:0xf bank_mask:0xf\n"
"v_mfma_f32_16x16x32_bf16 v[116:119], a[48:51], a[208:211], v[116:119]\n"
"s_add_u32 s56, s40, s58\n"
"v_mul_f32_dpp v89, v254, v89 row_newbcast:1 row_mask:0xf bank_mask:0xf\n"
"v_mul_f32_dpp v90, v254, v90 row_newbcast:2 row_mask:0xf bank_mask:0xf\n"
"v_mfma_f32_16x16x32_bf16 v[116:119], a[52:55], a[212:215], v[116:119]\n"
"s_addc_u32 s57, s41, 0\n"
"v_mul_f32_dpp v91, v254, v91 row_newbcast:3 row_mask:0xf bank_mask:0xf\n"
"v_cvt_pk_bf16_f32 v150, v88, v89\n"
"v_mfma_f32_16x16x32_bf16 v[116:119], a[56:59], a[216:219], v[116:119]\n"
"s_waitcnt vmcnt(10)\n"
"v_cmp_ne_u64 vcc, s[48:49], v[138:139]\n"
"v_cvt_pk_bf16_f32 v151, v90, v91\n"
"v_mfma_f32_16x16x32_bf16 v[116:119], a[60:63], a[220:223], v[116:119]\n"
"ds_write_addtid_b32 v150 offset:20128\n"
"ds_write_addtid_b32 v151 offset:20384\n"
"v_mfma_f32_16x16x32_bf16 v[92:95], a[224:227], a[64:67], 0\n"
"s_cbranch_vccnz .LQ_2s\n"
".LQ_2r:\n"
"v_mfma_f32_16x16x32_bf16 v[92:95], a[228:231], a[68:71], v[92:95]\n"
"global_load_dwordx4 v[80:83], v5, s[56:57] offset:0 sc1\n"
"v_mfma_f32_16x16x32_bf16 v[92:95], a[232:235], a[72:75], v[92:95]\n"
"s_add_u32 s50, s50, 0x100000\n"
"v_mfma_f32_16x16x32_bf16 v[92:95], a[236:239], a[76:79], v[92:95]\n"
"v_mfma_f32_16x16x32_bf16 v[92:95], a[240:243], a[80:83], v[92:95]\n"
"v_mfma_f32_16x16x32_bf16 v[92:95], a[244:247], a[84:87], v[92:95]\n"
"v_mfma_f32_16x16x32_bf16 v[92:95], a[248:251], a[88:91], v[92:95]\n"
"global_load_dwordx4 v[84:87], v5, s[56:57] offset:1024 sc1\n"
"v_mfma_f32_16x16x32_bf16 v[92:95], a[252:255], a[92:95], v[92:95]\n"
"v_mfma_f32_16x16x32_bf16 v[100:103], a[224:227], a[96:99], 0\n"
"v_mfma_f32_16x16x32_bf16 v[100:103], a[228:231], a[100:103], v[100:103]\n"
"ds_write_addtid_b32 v92 offset:3072\n"
"v_mfma_f32_16x16x32_bf16 v[100:103], a[232:235], a[104:107], v[100:103]\n"
"s_waitcnt lgkmcnt(1)\n"
"ds_write_addtid_b32 v93 offset:7712\n"
"v_mfma_f32_16x16x32_bf16 v[100:103], a[236:239], a[108:111], v[100:103]\n"
"s_barrier\n"
"v_mfma_f32_16x16x32_bf16 v[100:103], a[240:243], a[112:115], v[100:103]\n"
"ds_write_addtid_b32 v94 offset:12416\n"
"ds_read_b128 v[112:115], v2 offset:4096\n"
"v_mfma_f32_16x16x32_bf16 v[100:103], a[244:247], a[116:119], v[100:103]\n"
"ds_write_addtid_b32 v95 offset:17056\n"
"global_load_dwordx4 v[88:91], v6, s[56:57] offset:0 sc1\n"
"v_mfma_f32_16x16x32_bf16 v[100:103], a[248:251], a[120:123], v[100:103]\n"
"ds_read_b128 v[104:107], v3 offset:512\n"
"ds_read_b128 v[120:123], v4 offset:1536\n"
"v_mfma_f32_16x16x32_bf16 v[100:103], a[252:255], a[124:127], v[100:103]\n"
"global_load_dwordx4 v[92:95], v6, s[56:57] offset:1024 sc1\n"
"ds_read_b128 v[124:127], v2 offset:4352\n"
"v_mfma_f32_16x16x32_bf16 v[132:135], a[128:131], a[224:227], 0\n"
"ds_read_b128 v[152:155], v3 offset:768\n"
"ds_read_b128 v[156:159], v4 offset:1792\n"
"v_mfma_f32_16x16x32_bf16 v[132:135], a[132:135], a[228:231], v[132:135]\n"
"ds_write_addtid_b32 v100 offset:3328\n"
"v_mfma_f32_16x16x32_bf16 v[132:135], a[136:139], a[232:235], v[132:135]\n"
"ds_write_addtid_b32 v101 offset:7968\n"
"v_mfma_f32_16x16x32_bf16 v[132:135], a[140:143], a[236:239], v[132:135]\n"
"ds_write_addtid_b32 v102 offset:12672\n"
"v_mfma_f32_16x16x32_bf16 v[132:135], a[144:147], a[240:243], v[132:135]\n"
"s_waitcnt lgkmcnt(6)\n"
"ds_write_addtid_b32 v103 offset:17312\n"
"global_load_dwordx4 v[96:99], v7, s[56:57] offset:0 sc1\n"
"v_mfma_f32_16x16x32_bf16 v[132:135], a[148:151], a[244:247], v[132:135]\n"
"v_add_f32 v112, v112, v104\n"
"v_add_f32 v113, v113, v105\n"
"v_mfma_f32_16x16x32_bf16 v[132:135], a[152:155], a[248:251], v[132:135]\n"
"v_add_f32 v114, v114, v106\n"
"v_add_f32 v115, v115, v107\n"
"v_mfma_f32_16x16x32_bf16 v[132:135], a[156:159], a[252:255], v[132:135]\n"
"v_add_f32 v112, v112, v120\n"
"v_add_f32 v113, v113, v121\n"
"v_mfma_f32_16x16x32_bf16 v[160:163], a[160:163], a[224:227], 0\n"
"v_add_f32 v114, v114, v122\n"
"v_add_f32 v115, v115, v123\n"
"v_mfma_f32_16x16x32_bf16 v[160:163], a[164:167], a[228:231], v[160:163]\n"
"ds_write_addtid_b32 v132 offset:3584\n"
"global_load_dwordx4 v[100:103], v7, s[56:57] offset:1024 sc1\n"
"v_mfma_f32_16x16x32_bf16 v[160:163], a[168:171], a[232:235], v[160:163]\n"
"ds_write_addtid_b32 v133 offset:8224\n"
"v_add_f32 v112, v112, v108\n"
"v_mfma_f32_16x16x32_bf16 v[160:163], a[172:175], a[236:239], v[160:163]\n"
"ds_write_addtid_b32 v134 offset:12928\n"
"v_add_f32 v113, v113, v109\n"
"v_mfma_f32_16x16x32_bf16 v[160:163], a[176:179], a[240:243], v[160:163]\n"
"ds_write_addtid_b32 v135 offset:17568\n"
"v_add_f32 v114, v114, v110\n"
"v_mfma_f32_16x16x32_bf16 v[160:163], a[180:183], a[244:247], v[160:163]\n"
"v_add_f32 v115, v115, v111\n"
"v_cvt_pk_bf16_f32 v164, v112, v113\n"
"v_mfma_f32_16x16x32_bf16 v[160:163], a[184:187], a[248:251], v[160:163]\n"
"global_load_dwordx4 v[104:107], v8, s[56:57] offset:0 sc1\n"
"v_cvt_pk_bf16_f32 v165, v114, v115\n"
"v_mfma_f32_16x16x32_bf16 v[160:163], a[188:191], a[252:255], v[160:163]\n"
"ds_read2st64_b32 v[120:121], v11 offset0:0 offset1:1\n"
"ds_read2st64_b32 v[122:123], v11 offset0:2 offset1:3\n"
"v_mfma_f32_16x16x32_bf16 v[112:115], a[224:227], a[0:3], 0\n"
"s_waitcnt lgkmcnt(10)\n"
"v_add_f32 v124, v124, v152\n"
"v_add_f32 v125, v125, v153\n"
"v_mfma_f32_16x16x32_bf16 v[112:115], a[228:231], a[4:7], v[112:115]\n"
"ds_write_addtid_b32 v160 offset:3840\n"
"v_add_f32 v126, v126, v154\n"
"v_mfma_f32_16x16x32_bf16 v[112:115], a[232:235], a[8:11], v[112:115]\n"
"ds_write_addtid_b32 v161 offset:8480\n"
"v_add_f32 v127, v127, v155\n"
"v_mfma_f32_16x16x32_bf16 v[112:115], a[236:239], a[12:15], v[112:115]\n"
"ds_write_addtid_b32 v162 offset:13184\n"
"global_load_dwordx4 v[108:111], v8, s[56:57] offset:1024 sc1\n"
"v_mfma_f32_16x16x32_bf16 v[112:115], a[240:243], a[16:19], v[112:115]\n"
"ds_write_addtid_b32 v163 offset:17824\n"
"v_add_f32 v124, v124, v156\n"
"v_mfma_f32_16x16x32_bf16 v[112:115], a[244:247], a[20:23], v[112:115]\n"
"v_add_f32 v125, v125, v157\n"
"v_add_f32 v126, v126, v158\n"
"v_mfma_f32_16x16x32_bf16 v[112:115], a[248:251], a[24:27], v[112:115]\n"
"v_add_f32 v127, v127, v159\n"
"v_add_f32 v124, v124, v116\n"
"v_mfma_f32_16x16x32_bf16 v[112:115], a[252:255], a[28:31], v[112:115]\n"
"v_add_f32 v125, v125, v117\n"
"v_add_f32 v126, v126, v118\n"
"v_mfma_f32_16x16x32_bf16 v[132:135], a[224:227], a[32:35], 0\n"
"s_add_u32 s58, s50, 0x100000\n"
"buffer_load_dwordx4 v[152:155], v9, s[40:43], s50 offen sc1\n"
"v_mfma_f32_16x16x32_bf16 v[132:135], a[228:231], a[36:39], v[132:135]\n"
"s_and_b32 s58, s58, 0x3ffffff\n"
"ds_write_addtid_b32 v112 offset:4096\n"
"v_add_f32 v127, v127, v119\n"
"v_mfma_f32_16x16x32_bf16 v[132:135], a[232:235], a[40:43], v[132:135]\n"
"s_add_u32 s56, s40, s58\n"
"ds_write_addtid_b32 v113 offset:8736\n"
"v_cvt_pk_bf16_f32 v166, v124, v125\n"
"v_mfma_f32_16x16x32_bf16 v[132:135], a[236:239], a[44:47], v[132:135]\n"
"s_addc_u32 s57, s41, 0\n"
"ds_write_addtid_b32 v114 offset:13440\n"
"v_cvt_pk_bf16_f32 v167, v126, v127\n"
"v_mfma_f32_16x16x32_bf16 v[132:135], a[240:243], a[48:51], v[132:135]\n"
"s_waitcnt vmcnt(9)\n"
"ds_write_addtid_b32 v115 offset:18080\n"
"v_cmp_ne_u64 vcc, s[48:49], v[146:147]\n"
"v_mfma_f32_16x16x32_bf16 v[132:135], a[244:247], a[52:55], v[132:135]\n"
"s_waitcnt lgkmcnt(8)\n"
"v_mfma_f32_16x16x32_bf16 v[156:159], v[120:123], v[164:167], v[20:23]\n"
"v_mfma_f32_16x16x32_bf16 v[132:135], a[248:251], a[56:59], v[132:135]\n"
"s_cbranch_vccnz .LQ_3s\n"
".LQ_3r:\n"
"v_mfma_f32_16x16x32_bf16 v[132:135], a[252:255], a[60:63], v[132:135]\n"
"s_branch .LQ_top\n"
".LQ_1s:\n"
"s_cmp_ge_u32 s50, s53\n"
"s_cbranch_scc1 .LQ_end\n"
"s_cmp_ge_u32 s50, s52\n"
"s_cbranch_scc1 .LQ_1r\n"
"s_sub_u32 s60, s50, 0x100000\n"
"s_mov_b32 s61, 200000\n"
"s_nop 7\n"
".LQ_1p:\n"
"buffer_load_dwordx4 v[128:131], v9, s[40:43], s60 offen sc1\n"
"s_waitcnt vmcnt(0)\n"
"v_cmp_ne_u64 vcc, s[48:49], v[130:131]\n"
"s_cbranch_vccz .LQ_1px\n"
"s_sleep 1\n"
"s_sub_u32 s61, s61, 1\n"
"s_cbranch_scc0 .LQ_1p\n"
"s_branch .LQ_end\n"
".LQ_1px:\n"
"s_branch .LQ_1r\n"
".LQ_2s:\n"
"s_cmp_ge_u32 s50, s53\n"
"s_cbranch_scc1 .LQ_end\n"
"s_cmp_ge_u32 s50, s52\n"
"s_cbranch_scc1 .LQ_2r\n"
"s_sub_u32 s60, s50, 0x100000\n"
"s_mov_b32 s61, 200000\n"
"s_nop 7\n"
".LQ_2p:\n"
"buffer_load_dwordx4 v[136:139], v10, s[40:43], s60 offen sc1\n"
"s_waitcnt vmcnt(0)\n"
"v_cmp_ne_u64 vcc, s[48:49], v[138:139]\n"
"s_cbranch_vccz .LQ_2px\n"
"s_sleep 1\n"
"s_sub_u32 s61, s61, 1\n"
"s_cbranch_scc0 .LQ_2p\n"
"s_branch .LQ_end\n"
".LQ_2px:\n"
"s_branch .LQ_2r\n"
".LQ_3s:\n"
"s_cmp_ge_u32 s50, s53\n"
"s_cbranch_scc1 .LQ_end\n"
"s_cmp_ge_u32 s50, s52\n"
"s_cbranch_scc1 .LQ_3r\n"
"s_sub_u32 s60, s50, 0x100000\n"
"s_mov_b32 s61, 200000\n"
"s_nop 7\n"
".LQ_3p:\n"
"buffer_load_dwordx4 v[144:147], v9, s[40:43], s60 offen sc1\n"
"s_waitcnt vmcnt(0)\n"
"v_cmp_ne_u64 vcc, s[48:49], v[146:147]\n"
"s_cbranch_vccz .LQ_3px\n"
"s_sleep 1\n"
"s_sub_u32 s61, s61, 1\n"
"s_cbranch_scc0 .LQ_3p\n"
"s_branch .LQ_end\n"
".LQ_3px:\n"
"s_branch .LQ_3r\n"
".LQ_4s:\n"
"s_cmp_ge_u32 s50, s53\n"
"s_cbranch_scc1 .LQ_end\n"
"s_cmp_ge_u32 s50, s52\n"
"s_cbranch_scc1 .LQ_4r\n"
"s_sub_u32 s60, s50, 0x100000\n"
"s_mov_b32 s61, 200000\n"
"s_nop 7\n"
".LQ_4p:\n"
"buffer_load_dwordx4 v[152:155], v9, s[40:43], s60 offen sc1\n"
"s_waitcnt vmcnt(0)\n"
"v_cmp_ne_u64 vcc, s[48:49], v[154:155]\n"
"s_cbranch_vccz .LQ_4px\n"
"s_sleep 1\n"
"s_sub_u32 s61, s61, 1\n"
"s_cbranch_scc0 .LQ_4p\n"
"s_branch .LQ_end\n"
".LQ_4px:\n"
"s_branch .LQ_4r\n"
".LQ_fs:\n"
"s_nop 7\n"
"v_mov_b32 v156, 0xff800000\n"
"v_mov_b32 v157, 0xff800000\n"
"v_mov_b32 v158, 0xff800000\n"
"v_mov_b32 v159, 0xff800000\n"
"v_mov_b32 v142, 0\n"
"v_mov_b32 v143, 0\n"
"v_mov_b32 v150, 0\n"
"v_mov_b32 v151, 0\n"
"s_add_u32 s62, s62, 0x4000000\n"
"s_branch .LQ_fr\n"
".LQ_end:\n"
"s_waitcnt vmcnt(0) lgkmcnt(0)\n"
"s_endpgm\n"
      :: "{v2}"((unsigned)(lds0 + ((wid + 3) & 3) * 20640 + trp)), "{v3}"((unsigned)(lds0 + ((wid + 2) & 3) * 20640 + trp)), "{v4}"((unsigned)(lds0 + ((wid + 1) & 3) * 20640 + trp)), "{v5}"((unsigned)(lane * 16 + wid * 8192 + ((0 + rot) % 4) * 2048 + 134217728)), "{v6}"((unsigned)(lane * 16 + wid * 8192 + ((1 + rot) % 4) * 2048 + 134217728)), "{v7}"((unsigned)(lane * 16 + wid * 8192 + ((2 + rot) % 4) * 2048 + 134217728)), "{v8}"((unsigned)(lane * 16 + wid * 8192 + ((3 + rot) % 4) * 2048 + 134217728)), "{v9}"((unsigned)(li * 16 + 352256 + Q_GL * 1048576)), "{v10}"((unsigned)(((li + 4 * lg) & 15) * 16 + 352256 + Q_GL * 1048576)), "{v11}"((unsigned)(lds0 + ((wid + 1) & 3) * 20640 + 18592 + lane * 4)), "{v12}"((unsigned)(lds0 + ((wid + 2) & 3) * 20640 + 18592 + lane * 4)), "{v13}"((unsigned)(lane * 16 + idx * 1024 + 32768)), "{v14}"((unsigned)(lane ? DROP : idx * 8 + 352512)), "{v16}"((unsigned)(qmask(0, 0))), "{v17}"((unsigned)(qmask(1, 0))), "{v18}"((unsigned)(qmask(2, 0))), "{v19}"((unsigned)(qmask(3, 0))), "{v20}"((unsigned)(qmask(0, 1))), "{v21}"((unsigned)(qmask(1, 1))), "{v22}"((unsigned)(qmask(2, 1))), "{v23}"((unsigned)(qmask(3, 1))), "{v24}"((unsigned)(pmask(0))), "{v25}"((unsigned)(pmask(1))), "{v26}"((unsigned)(pmask(2))), "{v27}"((unsigned)(pmask(3))), "{v48}"((unsigned)(vT + tsw)), "{v49}"((unsigned)(vT + (tsw ^ 8))), "{v50}"((unsigned)((sEnt == 1 ? (li + 4 * lg) & 15 : li) * 16 + 1400832)), "{v51}"((unsigned)((sEnt == 0 ? (li + 4 * lg) & 15 : li) * 16 + 2449408)), "{v52}"((unsigned)(lane * 16 + wid * 8192 + (idx & 7) * 1048576 + 134221824)), "{v53}"((unsigned)(vD + ((0 + rot) % 4) * 393216)), "{v54}"((unsigned)(vD + ((1 + rot) % 4) * 393216)), "{v55}"((unsigned)(vD + ((2 + rot) % 4) * 393216)), "{v56}"((unsigned)(vD + ((3 + rot) % 4) * 393216)), "{v57}"((unsigned)(lds0 + wid * 20640 + trp)), "{s[40:43]}"(rS), "{s[44:47]}"(rW), "{s[48:49]}"(wsp), "{s50}"((unsigned)(sA)), "{s51}"((unsigned)(sLo)), "{s52}"((unsigned)(sHi)), "{s53}"((unsigned)(sEx)), "{s54}"((unsigned)(sEnt)), "{s55}"((unsigned)(sLd)), "{s[56:57]}"(wsb + (sLo & 0x3ffffff)), "{s59}"((unsigned)(lds0 + wid * 20640)), "{s62}"((unsigned)(sLo)), "{s63}"((unsigned)(sW0 + 0)), "{s64}"((unsigned)(sW0 + 2048)), "{s65}"((unsigned)(sW0 + 4096))
      : "v255", "a255", "s95", "vcc", "scc", "memory");

  }
  while (__builtin_amdgcn_s_memrealtime() - tz < (role == 1 ? DLO : role == 2 ? (xo < 0 ? DLF : DLB) : DLG)) __builtin_amdgcn_s_sleep(4);
  if (role == 2 && xo < 0 && (SLP & 4)) {
#pragma unroll
    for (int i = 0; i < 4 * TW; ++i)
      asm volatile("global_load_dword v252, %0, %1" ::"v"((wid * 256 + i / TW * 64 + (xj - 16) * 4 + lg) * 7168 + (14 * (xo + 4) + min(li, 13)) * 128 + i % TW * 64), "s"(a.w1) : "v252");
  }
  if (role == 3 && (SLP & 8) && !((SLP & 32) && p2 < 128)) tw2();
  const unsigned sA = sLo - 3 * RS, sHi = sLo + (set ? 84 : 85) * 3 * RS;
  if (role == 1) {
    const unsigned sEx = sHi + O_EX * 3 * RS, sEnt = OR ? -wid & 3 : 0, tsw = 0, pair = idx * 4 + wid, rot = 0;
    const unsigned sW0 = wid * 256 * 2048 + idx * 256, vD = (lane >> 2) * 2048 + (lane & 3) * 16;
    const u32x4 rW = desc(a.wo, 1024 * 2048);
    asm volatile(
"s_nop 4\n"
"v_mov_b32 v186, 0\n"
"global_load_dwordx2 v[0:1], v186, s[48:49] sc1\n"
"v_mov_b32 v188, 0x3f803f80\n"
"v_mov_b32 v189, 0x3f803f80\n"
"v_mov_b32 v190, 0x3f803f80\n"
"v_mov_b32 v191, 0x3f803f80\n"
"s_add_u32 m0, s55, 0\n"
"s_add_u32 s60, s66, 0\n"
"buffer_load_dwordx4 v22, s[44:47], s60 offen lds\n"
"s_add_u32 m0, s55, 1024\n"
"s_add_u32 s60, s66, 32768\n"
"buffer_load_dwordx4 v22, s[44:47], s60 offen lds\n"
"s_add_u32 m0, s55, 2048\n"
"s_add_u32 s60, s67, 0\n"
"buffer_load_dwordx4 v22, s[44:47], s60 offen lds\n"
"s_add_u32 m0, s55, 3072\n"
"s_add_u32 s60, s67, 32768\n"
"buffer_load_dwordx4 v22, s[44:47], s60 offen lds\n"
"s_add_u32 m0, s55, 4096\n"
"s_add_u32 s60, s68, 0\n"
"buffer_load_dwordx4 v22, s[44:47], s60 offen lds\n"
"s_add_u32 m0, s55, 5120\n"
"s_add_u32 s60, s68, 32768\n"
"buffer_load_dwordx4 v22, s[44:47], s60 offen lds\n"
"s_add_u32 m0, s55, 6144\n"
"s_add_u32 s60, s69, 0\n"
"buffer_load_dwordx4 v22, s[44:47], s60 offen lds\n"
"s_add_u32 m0, s55, 7168\n"
"s_add_u32 s60, s69, 32768\n"
"buffer_load_dwordx4 v22, s[44:47], s60 offen lds\n"
"s_add_u32 m0, s55, 8192\n"
"s_add_u32 s60, s66, 65536\n"
"buffer_load_dwordx4 v22, s[44:47], s60 offen lds\n"
"s_add_u32 m0, s55, 9216\n"
"s_add_u32 s60, s66, 98304\n"
"buffer_load_dwordx4 v22, s[44:47], s60 offen lds\n"
"s_add_u32 m0, s55, 10240\n"
"s_add_u32 s60, s67, 65536\n"
"buffer_load_dwordx4 v22, s[44:47], s60 offen lds\n"
"s_add_u32 m0, s55, 11264\n"
"s_add_u32 s60, s67, 98304\n"
"buffer_load_dwordx4 v22, s[44:47], s60 offen lds\n"
"s_add_u32 m0, s55, 12288\n"
"s_add_u32 s60, s68, 65536\n"
"buffer_load_dwordx4 v22, s[44:47], s60 offen lds\n"
"s_add_u32 m0, s55, 13312\n"
"s_add_u32 s60, s68, 98304\n"
"buffer_load_dwordx4 v22, s[44:47], s60 offen lds\n"
"s_add_u32 m0, s55, 14336\n"
"s_add_u32 s60, s69, 65536\n"
"buffer_load_dwordx4 v22, s[44:47], s60 offen lds\n"
"s_add_u32 m0, s55, 15360\n"
"s_add_u32 s60, s69, 98304\n"
"buffer_load_dwordx4 v22, s[44:47], s60 offen lds\n"
"s_add_u32 m0, s55, 16384\n"
"s_add_u32 s60, s66, 0\n"
"buffer_load_dwordx4 v23, s[44:47], s60 offen lds\n"
"s_add_u32 m0, s55, 17408\n"
"s_add_u32 s60, s66, 32768\n"
"buffer_load_dwordx4 v23, s[44:47], s60 offen lds\n"
"s_add_u32 m0, s55, 18432\n"
"s_add_u32 s60, s67, 0\n"
"buffer_load_dwordx4 v23, s[44:47], s60 offen lds\n"
"s_add_u32 m0, s55, 19456\n"
"s_add_u32 s60, s67, 32768\n"
"buffer_load_dwordx4 v23, s[44:47], s60 offen lds\n"
"s_add_u32 m0, s55, 20480\n"
"s_add_u32 s60, s68, 0\n"
"buffer_load_dwordx4 v23, s[44:47], s60 offen lds\n"
"s_add_u32 m0, s55, 21504\n"
"s_add_u32 s60, s68, 32768\n"
"buffer_load_dwordx4 v23, s[44:47], s60 offen lds\n"
"s_add_u32 m0, s55, 22528\n"
"s_add_u32 s60, s69, 0\n"
"buffer_load_dwordx4 v23, s[44:47], s60 offen lds\n"
"s_add_u32 m0, s55, 23552\n"
"s_add_u32 s60, s69, 32768\n"
"buffer_load_dwordx4 v23, s[44:47], s60 offen lds\n"
"s_add_u32 m0, s55, 24576\n"
"s_add_u32 s60, s66, 65536\n"
"buffer_load_dwordx4 v23, s[44:47], s60 offen lds\n"
"s_add_u32 m0, s55, 25600\n"
"s_add_u32 s60, s66, 98304\n"
"buffer_load_dwordx4 v23, s[44:47], s60 offen lds\n"
"s_add_u32 m0, s55, 26624\n"
"s_add_u32 s60, s67, 65536\n"
"buffer_load_dwordx4 v23, s[44:47], s60 offen lds\n"
"s_add_u32 m0, s55, 27648\n"
"s_add_u32 s60, s67, 98304\n"
"buffer_load_dwordx4 v23, s[44:47], s60 offen lds\n"
"s_add_u32 m0, s55, 28672\n"
"s_add_u32 s60, s68, 65536\n"
"buffer_load_dwordx4 v23, s[44:47], s60 offen lds\n"
"s_add_u32 m0, s55, 29696\n"
"s_add_u32 s60, s68, 98304\n"
"buffer_load_dwordx4 v23, s[44:47], s60 offen lds\n"
"s_add_u32 m0, s55, 30720\n"
"s_add_u32 s60, s69, 65536\n"
"buffer_load_dwordx4 v23, s[44:47], s60 offen lds\n"
"s_add_u32 m0, s55, 31744\n"
"s_add_u32 s60, s69, 98304\n"
"buffer_load_dwordx4 v23, s[44:47], s60 offen lds\n"
"s_add_u32 m0, s55, 32768\n"
"s_add_u32 s60, s66, 0\n"
"buffer_load_dwordx4 v24, s[44:47], s60 offen lds\n"
"s_add_u32 m0, s55, 33792\n"
"s_add_u32 s60, s66, 32768\n"
"buffer_load_dwordx4 v24, s[44:47], s60 offen lds\n"
"s_add_u32 m0, s55, 34816\n"
"s_add_u32 s60, s67, 0\n"
"buffer_load_dwordx4 v24, s[44:47], s60 offen lds\n"
"s_add_u32 m0, s55, 35840\n"
"s_add_u32 s60, s67, 32768\n"
"buffer_load_dwordx4 v24, s[44:47], s60 offen lds\n"
"s_add_u32 m0, s55, 36864\n"
"s_add_u32 s60, s68, 0\n"
"buffer_load_dwordx4 v24, s[44:47], s60 offen lds\n"
"s_add_u32 m0, s55, 37888\n"
"s_add_u32 s60, s68, 32768\n"
"buffer_load_dwordx4 v24, s[44:47], s60 offen lds\n"
"s_add_u32 m0, s55, 38912\n"
"s_add_u32 s60, s69, 0\n"
"buffer_load_dwordx4 v24, s[44:47], s60 offen lds\n"
"s_add_u32 m0, s55, 39936\n"
"s_add_u32 s60, s69, 32768\n"
"buffer_load_dwordx4 v24, s[44:47], s60 offen lds\n"
"s_waitcnt vmcnt(38)\n"
"ds_read_b64_tr_b16 a[0:1], v20 offset:0\n"
"ds_read_b64_tr_b16 a[2:3], v20 offset:256\n"
"ds_read_b64_tr_b16 a[32:33], v21 offset:0\n"
"ds_read_b64_tr_b16 a[34:35], v21 offset:256\n"
"v_add_u32 v0, 2, v0\n"
"v_add_u32 v1, 0x9e3779b9, v1\n"
"v_or_b32 v0, 1, v0\n"
"v_readfirstlane_b32 s49, v1\n"
"v_readfirstlane_b32 s48, v0\n"
"s_waitcnt vmcnt(36)\n"
"ds_read_b64_tr_b16 a[64:65], v20 offset:2048\n"
"ds_read_b64_tr_b16 a[66:67], v20 offset:2304\n"
"ds_read_b64_tr_b16 a[96:97], v21 offset:2048\n"
"ds_read_b64_tr_b16 a[98:99], v21 offset:2304\n"
"s_waitcnt lgkmcnt(4)\n"
"s_add_u32 m0, s55, 0\n"
"s_add_u32 s60, s66, 65536\n"
"buffer_load_dwordx4 v24, s[44:47], s60 offen lds\n"
"s_add_u32 m0, s55, 1024\n"
"s_add_u32 s60, s66, 98304\n"
"buffer_load_dwordx4 v24, s[44:47], s60 offen lds\n"
"s_waitcnt vmcnt(36)\n"
"ds_read_b64_tr_b16 a[128:129], v20 offset:4096\n"
"ds_read_b64_tr_b16 a[130:131], v20 offset:4352\n"
"ds_read_b64_tr_b16 a[160:161], v21 offset:4096\n"
"ds_read_b64_tr_b16 a[162:163], v21 offset:4352\n"
"s_waitcnt lgkmcnt(4)\n"
"s_add_u32 m0, s55, 2048\n"
"s_add_u32 s60, s67, 65536\n"
"buffer_load_dwordx4 v24, s[44:47], s60 offen lds\n"
"s_add_u32 m0, s55, 3072\n"
"s_add_u32 s60, s67, 98304\n"
"buffer_load_dwordx4 v24, s[44:47], s60 offen lds\n"
"s_waitcnt vmcnt(36)\n"
"ds_read_b64_tr_b16 a[192:193], v20 offset:6144\n"
"ds_read_b64_tr_b16 a[194:195], v20 offset:6400\n"
"ds_read_b64_tr_b16 a[224:225], v21 offset:6144\n"
"ds_read_b64_tr_b16 a[226:227], v21 offset:6400\n"
"s_waitcnt lgkmcnt(4)\n"
"s_add_u32 m0, s55, 4096\n"
"s_add_u32 s60, s68, 65536\n"
"buffer_load_dwordx4 v24, s[44:47], s60 offen lds\n"
"s_add_u32 m0, s55, 5120\n"
"s_add_u32 s60, s68, 98304\n"
"buffer_load_dwordx4 v24, s[44:47], s60 offen lds\n"
"s_waitcnt vmcnt(36)\n"
"ds_read_b64_tr_b16 a[4:5], v20 offset:8192\n"
"ds_read_b64_tr_b16 a[6:7], v20 offset:8448\n"
"ds_read_b64_tr_b16 a[36:37], v21 offset:8192\n"
"ds_read_b64_tr_b16 a[38:39], v21 offset:8448\n"
"s_waitcnt lgkmcnt(4)\n"
"s_add_u32 m0, s55, 6144\n"
"s_add_u32 s60, s69, 65536\n"
"buffer_load_dwordx4 v24, s[44:47], s60 offen lds\n"
"s_add_u32 m0, s55, 7168\n"
"s_add_u32 s60, s69, 98304\n"
"buffer_load_dwordx4 v24, s[44:47], s60 offen lds\n"
"s_waitcnt vmcnt(36)\n"
"ds_read_b64_tr_b16 a[68:69], v20 offset:10240\n"
"ds_read_b64_tr_b16 a[70:71], v20 offset:10496\n"
"ds_read_b64_tr_b16 a[100:101], v21 offset:10240\n"
"ds_read_b64_tr_b16 a[102:103], v21 offset:10496\n"
"s_waitcnt lgkmcnt(4)\n"
"s_add_u32 m0, s55, 8192\n"
"s_add_u32 s60, s66, 0\n"
"buffer_load_dwordx4 v25, s[44:47], s60 offen lds\n"
"s_add_u32 m0, s55, 9216\n"
"s_add_u32 s60, s66, 32768\n"
"buffer_load_dwordx4 v25, s[44:47], s60 offen lds\n"
"s_waitcnt vmcnt(36)\n"
"ds_read_b64_tr_b16 a[132:133], v20 offset:12288\n"
"ds_read_b64_tr_b16 a[134:135], v20 offset:12544\n"
"ds_read_b64_tr_b16 a[164:165], v21 offset:12288\n"
"ds_read_b64_tr_b16 a[166:167], v21 offset:12544\n"
"s_waitcnt lgkmcnt(4)\n"
"s_add_u32 m0, s55, 10240\n"
"s_add_u32 s60, s67, 0\n"
"buffer_load_dwordx4 v25, s[44:47], s60 offen lds\n"
"s_add_u32 m0, s55, 11264\n"
"s_add_u32 s60, s67, 32768\n"
"buffer_load_dwordx4 v25, s[44:47], s60 offen lds\n"
"s_waitcnt vmcnt(36)\n"
"ds_read_b64_tr_b16 a[196:197], v20 offset:14336\n"
"ds_read_b64_tr_b16 a[198:199], v20 offset:14592\n"
"ds_read_b64_tr_b16 a[228:229], v21 offset:14336\n"
"ds_read_b64_tr_b16 a[230:231], v21 offset:14592\n"
"s_waitcnt lgkmcnt(4)\n"
"s_add_u32 m0, s55, 12288\n"
"s_add_u32 s60, s68, 0\n"
"buffer_load_dwordx4 v25, s[44:47], s60 offen lds\n"
"s_add_u32 m0, s55, 13312\n"
"s_add_u32 s60, s68, 32768\n"
"buffer_load_dwordx4 v25, s[44:47], s60 offen lds\n"
"s_waitcnt vmcnt(36)\n"
"ds_read_b64_tr_b16 a[8:9], v20 offset:16384\n"
"ds_read_b64_tr_b16 a[10:11], v20 offset:16640\n"
"ds_read_b64_tr_b16 a[40:41], v21 offset:16384\n"
"ds_read_b64_tr_b16 a[42:43], v21 offset:16640\n"
"s_waitcnt lgkmcnt(4)\n"
"s_add_u32 m0, s55, 14336\n"
"s_add_u32 s60, s69, 0\n"
"buffer_load_dwordx4 v25, s[44:47], s60 offen lds\n"
"s_add_u32 m0, s55, 15360\n"
"s_add_u32 s60, s69, 32768\n"
"buffer_load_dwordx4 v25, s[44:47], s60 offen lds\n"
"s_waitcnt vmcnt(36)\n"
"ds_read_b64_tr_b16 a[72:73], v20 offset:18432\n"
"ds_read_b64_tr_b16 a[74:75], v20 offset:18688\n"
"ds_read_b64_tr_b16 a[104:105], v21 offset:18432\n"
"ds_read_b64_tr_b16 a[106:107], v21 offset:18688\n"
"s_waitcnt lgkmcnt(4)\n"
"s_add_u32 m0, s55, 16384\n"
"s_add_u32 s60, s66, 65536\n"
"buffer_load_dwordx4 v25, s[44:47], s60 offen lds\n"
"s_add_u32 m0, s55, 17408\n"
"s_add_u32 s60, s66, 98304\n"
"buffer_load_dwordx4 v25, s[44:47], s60 offen lds\n"
"s_waitcnt vmcnt(36)\n"
"ds_read_b64_tr_b16 a[136:137], v20 offset:20480\n"
"ds_read_b64_tr_b16 a[138:139], v20 offset:20736\n"
"ds_read_b64_tr_b16 a[168:169], v21 offset:20480\n"
"ds_read_b64_tr_b16 a[170:171], v21 offset:20736\n"
"s_waitcnt lgkmcnt(4)\n"
"s_add_u32 m0, s55, 18432\n"
"s_add_u32 s60, s67, 65536\n"
"buffer_load_dwordx4 v25, s[44:47], s60 offen lds\n"
"s_add_u32 m0, s55, 19456\n"
"s_add_u32 s60, s67, 98304\n"
"buffer_load_dwordx4 v25, s[44:47], s60 offen lds\n"
"s_waitcnt vmcnt(36)\n"
"ds_read_b64_tr_b16 a[200:201], v20 offset:22528\n"
"ds_read_b64_tr_b16 a[202:203], v20 offset:22784\n"
"ds_read_b64_tr_b16 a[232:233], v21 offset:22528\n"
"ds_read_b64_tr_b16 a[234:235], v21 offset:22784\n"
"s_waitcnt lgkmcnt(4)\n"
"s_add_u32 m0, s55, 20480\n"
"s_add_u32 s60, s68, 65536\n"
"buffer_load_dwordx4 v25, s[44:47], s60 offen lds\n"
"s_add_u32 m0, s55, 21504\n"
"s_add_u32 s60, s68, 98304\n"
"buffer_load_dwordx4 v25, s[44:47], s60 offen lds\n"
"s_waitcnt vmcnt(36)\n"
"ds_read_b64_tr_b16 a[12:13], v20 offset:24576\n"
"ds_read_b64_tr_b16 a[14:15], v20 offset:24832\n"
"ds_read_b64_tr_b16 a[44:45], v21 offset:24576\n"
"ds_read_b64_tr_b16 a[46:47], v21 offset:24832\n"
"s_waitcnt lgkmcnt(4)\n"
"s_add_u32 m0, s55, 22528\n"
"s_add_u32 s60, s69, 65536\n"
"buffer_load_dwordx4 v25, s[44:47], s60 offen lds\n"
"s_add_u32 m0, s55, 23552\n"
"s_add_u32 s60, s69, 98304\n"
"buffer_load_dwordx4 v25, s[44:47], s60 offen lds\n"
"s_waitcnt vmcnt(36)\n"
"ds_read_b64_tr_b16 a[76:77], v20 offset:26624\n"
"ds_read_b64_tr_b16 a[78:79], v20 offset:26880\n"
"ds_read_b64_tr_b16 a[108:109], v21 offset:26624\n"
"ds_read_b64_tr_b16 a[110:111], v21 offset:26880\n"
"s_waitcnt lgkmcnt(4)\n"
"s_waitcnt vmcnt(34)\n"
"ds_read_b64_tr_b16 a[140:141], v20 offset:28672\n"
"ds_read_b64_tr_b16 a[142:143], v20 offset:28928\n"
"ds_read_b64_tr_b16 a[172:173], v21 offset:28672\n"
"ds_read_b64_tr_b16 a[174:175], v21 offset:28928\n"
"s_waitcnt lgkmcnt(4)\n"
"s_waitcnt vmcnt(32)\n"
"ds_read_b64_tr_b16 a[204:205], v20 offset:30720\n"
"ds_read_b64_tr_b16 a[206:207], v20 offset:30976\n"
"ds_read_b64_tr_b16 a[236:237], v21 offset:30720\n"
"ds_read_b64_tr_b16 a[238:239], v21 offset:30976\n"
"s_waitcnt lgkmcnt(4)\n"
"s_waitcnt vmcnt(30)\n"
"ds_read_b64_tr_b16 a[16:17], v20 offset:32768\n"
"ds_read_b64_tr_b16 a[18:19], v20 offset:33024\n"
"ds_read_b64_tr_b16 a[48:49], v21 offset:32768\n"
"ds_read_b64_tr_b16 a[50:51], v21 offset:33024\n"
"s_waitcnt lgkmcnt(4)\n"
"s_waitcnt vmcnt(28)\n"
"ds_read_b64_tr_b16 a[80:81], v20 offset:34816\n"
"ds_read_b64_tr_b16 a[82:83], v20 offset:35072\n"
"ds_read_b64_tr_b16 a[112:113], v21 offset:34816\n"
"ds_read_b64_tr_b16 a[114:115], v21 offset:35072\n"
"s_waitcnt lgkmcnt(4)\n"
"s_waitcnt vmcnt(26)\n"
"ds_read_b64_tr_b16 a[144:145], v20 offset:36864\n"
"ds_read_b64_tr_b16 a[146:147], v20 offset:37120\n"
"ds_read_b64_tr_b16 a[176:177], v21 offset:36864\n"
"ds_read_b64_tr_b16 a[178:179], v21 offset:37120\n"
"s_waitcnt lgkmcnt(4)\n"
"s_waitcnt vmcnt(24)\n"
"ds_read_b64_tr_b16 a[208:209], v20 offset:38912\n"
"ds_read_b64_tr_b16 a[210:211], v20 offset:39168\n"
"ds_read_b64_tr_b16 a[240:241], v21 offset:38912\n"
"ds_read_b64_tr_b16 a[242:243], v21 offset:39168\n"
"s_waitcnt lgkmcnt(4)\n"
"s_waitcnt vmcnt(22)\n"
"ds_read_b64_tr_b16 a[20:21], v20 offset:0\n"
"ds_read_b64_tr_b16 a[22:23], v20 offset:256\n"
"ds_read_b64_tr_b16 a[52:53], v21 offset:0\n"
"ds_read_b64_tr_b16 a[54:55], v21 offset:256\n"
"s_waitcnt lgkmcnt(4)\n"
"s_waitcnt vmcnt(20)\n"
"ds_read_b64_tr_b16 a[84:85], v20 offset:2048\n"
"ds_read_b64_tr_b16 a[86:87], v20 offset:2304\n"
"ds_read_b64_tr_b16 a[116:117], v21 offset:2048\n"
"ds_read_b64_tr_b16 a[118:119], v21 offset:2304\n"
"s_waitcnt lgkmcnt(4)\n"
"s_waitcnt vmcnt(18)\n"
"ds_read_b64_tr_b16 a[148:149], v20 offset:4096\n"
"ds_read_b64_tr_b16 a[150:151], v20 offset:4352\n"
"ds_read_b64_tr_b16 a[180:181], v21 offset:4096\n"
"ds_read_b64_tr_b16 a[182:183], v21 offset:4352\n"
"s_waitcnt lgkmcnt(4)\n"
"s_waitcnt vmcnt(16)\n"
"ds_read_b64_tr_b16 a[212:213], v20 offset:6144\n"
"ds_read_b64_tr_b16 a[214:215], v20 offset:6400\n"
"ds_read_b64_tr_b16 a[244:245], v21 offset:6144\n"
"ds_read_b64_tr_b16 a[246:247], v21 offset:6400\n"
"s_waitcnt lgkmcnt(4)\n"
"s_waitcnt vmcnt(14)\n"
"ds_read_b64_tr_b16 a[24:25], v20 offset:8192\n"
"ds_read_b64_tr_b16 a[26:27], v20 offset:8448\n"
"ds_read_b64_tr_b16 a[56:57], v21 offset:8192\n"
"ds_read_b64_tr_b16 a[58:59], v21 offset:8448\n"
"s_waitcnt lgkmcnt(4)\n"
"s_waitcnt vmcnt(12)\n"
"ds_read_b64_tr_b16 a[88:89], v20 offset:10240\n"
"ds_read_b64_tr_b16 a[90:91], v20 offset:10496\n"
"ds_read_b64_tr_b16 a[120:121], v21 offset:10240\n"
"ds_read_b64_tr_b16 a[122:123], v21 offset:10496\n"
"s_waitcnt lgkmcnt(4)\n"
"s_waitcnt vmcnt(10)\n"
"ds_read_b64_tr_b16 a[152:153], v20 offset:12288\n"
"ds_read_b64_tr_b16 a[154:155], v20 offset:12544\n"
"ds_read_b64_tr_b16 a[184:185], v21 offset:12288\n"
"ds_read_b64_tr_b16 a[186:187], v21 offset:12544\n"
"s_waitcnt lgkmcnt(4)\n"
"s_waitcnt vmcnt(8)\n"
"ds_read_b64_tr_b16 a[216:217], v20 offset:14336\n"
"ds_read_b64_tr_b16 a[218:219], v20 offset:14592\n"
"ds_read_b64_tr_b16 a[248:249], v21 offset:14336\n"
"ds_read_b64_tr_b16 a[250:251], v21 offset:14592\n"
"s_waitcnt lgkmcnt(4)\n"
"s_waitcnt vmcnt(6)\n"
"ds_read_b64_tr_b16 a[28:29], v20 offset:16384\n"
"ds_read_b64_tr_b16 a[30:31], v20 offset:16640\n"
"ds_read_b64_tr_b16 a[60:61], v21 offset:16384\n"
"ds_read_b64_tr_b16 a[62:63], v21 offset:16640\n"
"s_waitcnt lgkmcnt(4)\n"
"s_waitcnt vmcnt(4)\n"
"ds_read_b64_tr_b16 a[92:93], v20 offset:18432\n"
"ds_read_b64_tr_b16 a[94:95], v20 offset:18688\n"
"ds_read_b64_tr_b16 a[124:125], v21 offset:18432\n"
"ds_read_b64_tr_b16 a[126:127], v21 offset:18688\n"
"s_waitcnt lgkmcnt(4)\n"
"s_waitcnt vmcnt(2)\n"
"ds_read_b64_tr_b16 a[156:157], v20 offset:20480\n"
"ds_read_b64_tr_b16 a[158:159], v20 offset:20736\n"
"ds_read_b64_tr_b16 a[188:189], v21 offset:20480\n"
"ds_read_b64_tr_b16 a[190:191], v21 offset:20736\n"
"s_waitcnt lgkmcnt(4)\n"
"s_waitcnt vmcnt(0)\n"
"ds_read_b64_tr_b16 a[220:221], v20 offset:22528\n"
"ds_read_b64_tr_b16 a[222:223], v20 offset:22784\n"
"ds_read_b64_tr_b16 a[252:253], v21 offset:22528\n"
"ds_read_b64_tr_b16 a[254:255], v21 offset:22784\n"
"s_waitcnt lgkmcnt(4)\n"
"s_waitcnt lgkmcnt(0)\n"
"s_mov_b32 m0, s59\n"
"s_barrier\n"
"v_mov_b32 v124, 0\n"
"v_mov_b32 v125, 0\n"
"v_mov_b32 v120, 0\n"
"v_mov_b32 v121, 0\n"
"v_mov_b32 v124, 0\n"
"v_mov_b32 v125, 0\n"
"v_mov_b32 v120, 0\n"
"v_mov_b32 v121, 0\n"
"s_mov_b32 s93, 0x80000000\n"
"s_mov_b32 s94, 0x80000000\n"
"s_mov_b32 s95, 0x80000000\n"
"s_mov_b32 s94, 0x80000000\n"
"s_mov_b32 s93, 0x80000000\n"
"s_mov_b32 s94, 0x80000000\n"
"s_mov_b32 s95, 0x80000000\n"
"s_mov_b32 s94, 0x80000000\n"
"s_cmp_eq_u32 s54, 0\n"
"s_cbranch_scc1 .LO_1s\n"
"s_cmp_eq_u32 s54, 1\n"
"s_cbranch_scc1 .LO_2s\n"
"s_cmp_eq_u32 s54, 2\n"
"s_cbranch_scc1 .LO_3s\n"
"s_cmp_eq_u32 s54, 3\n"
"s_cbranch_scc1 .LO_4s\n"
".p2align 6\n"
".LO_top:\n"
"v_mfma_f32_16x16x32_bf16 v[40:43], v[84:87], a[0:3], 0\n"
"global_load_dwordx4 v[20:23], v6, s[56:57] offset:1024 sc1\n"
"v_mfma_f32_16x16x32_bf16 v[40:43], v[88:91], a[4:7], v[40:43]\n"
"s_waitcnt vmcnt(13)\n"
"v_mfma_f32_16x16x32_bf16 v[40:43], v[92:95], a[8:11], v[40:43]\n"
"s_waitcnt lgkmcnt(3)\n"
"v_add_f32 v72, v72, v44\n"
"v_add_f32 v73, v73, v45\n"
"v_mfma_f32_16x16x32_bf16 v[40:43], v[52:55], a[12:15], v[40:43]\n"
"s_waitcnt vmcnt(11)\n"
"v_add_f32 v74, v74, v46\n"
"v_add_f32 v75, v75, v47\n"
"v_mfma_f32_16x16x32_bf16 v[40:43], v[56:59], a[16:19], v[40:43]\n"
"v_add_f32 v72, v72, v48\n"
"v_add_f32 v73, v73, v49\n"
"v_mfma_f32_16x16x32_bf16 v[40:43], v[60:63], a[20:23], v[40:43]\n"
"s_waitcnt vmcnt(8)\n"
"global_load_dwordx4 v[24:27], v7, s[56:57] offset:0 sc1\n"
"v_add_f32 v74, v74, v50\n"
"v_mfma_f32_16x16x32_bf16 v[40:43], v[64:67], a[24:27], v[40:43]\n"
"v_add_f32 v75, v75, v51\n"
"v_add_f32 v72, v72, v112\n"
"v_mfma_f32_16x16x32_bf16 v[40:43], v[68:71], a[28:31], v[40:43]\n"
"v_add_f32 v73, v73, v113\n"
"v_add_f32 v74, v74, v114\n"
"v_mfma_f32_16x16x32_bf16 v[44:47], v[84:87], a[32:35], 0\n"
"global_load_dwordx4 v[28:31], v7, s[56:57] offset:1024 sc1\n"
"v_add_f32 v75, v75, v115\n"
"v_mfma_f32_16x16x32_bf16 v[44:47], v[88:91], a[36:39], v[44:47]\n"
"s_add_u32 s95, s50, 0xffd00000\n"
"s_and_b32 s94, s95, 0xfbffffff\n"
"ds_write_addtid_b32 v40 offset:0\n"
"v_mfma_f32_16x16x32_bf16 v[44:47], v[92:95], a[40:43], v[44:47]\n"
"ds_write_addtid_b32 v41 offset:4640\n"
"v_cvt_pk_bf16_f32 v48, v72, v73\n"
"v_mfma_f32_16x16x32_bf16 v[44:47], v[52:55], a[44:47], v[44:47]\n"
"ds_write_addtid_b32 v42 offset:9344\n"
"v_cvt_pk_bf16_f32 v49, v74, v75\n"
"v_mfma_f32_16x16x32_bf16 v[44:47], v[56:59], a[48:51], v[44:47]\n"
"s_waitcnt lgkmcnt(3)\n"
"ds_write_addtid_b32 v43 offset:13984\n"
"v_add_f32 v76, v76, v80\n"
"v_mfma_f32_16x16x32_bf16 v[44:47], v[60:63], a[52:55], v[44:47]\n"
"global_load_dwordx4 v[32:35], v8, s[56:57] offset:0 sc1\n"
"v_add_f32 v77, v77, v81\n"
"v_mfma_f32_16x16x32_bf16 v[44:47], v[64:67], a[56:59], v[44:47]\n"
"v_add_f32 v78, v78, v82\n"
"v_add_f32 v79, v79, v83\n"
"v_mfma_f32_16x16x32_bf16 v[44:47], v[68:71], a[60:63], v[44:47]\n"
"v_add_f32 v76, v76, v120\n"
"v_add_f32 v77, v77, v121\n"
"v_mfma_f32_16x16x32_bf16 v[40:43], v[84:87], a[64:67], 0\n"
"v_add_f32 v78, v78, v122\n"
"v_add_f32 v79, v79, v123\n"
"v_mfma_f32_16x16x32_bf16 v[40:43], v[88:91], a[68:71], v[40:43]\n"
"ds_write_addtid_b32 v44 offset:256\n"
"v_add_f32 v76, v76, v108\n"
"v_mfma_f32_16x16x32_bf16 v[40:43], v[92:95], a[72:75], v[40:43]\n"
"s_waitcnt vmcnt(9)\n"
"buffer_store_dwordx2 v[0:1], v12, s[40:43], s93 offen offset:0 sc1\n"
"ds_write_addtid_b32 v45 offset:4896\n"
"v_mfma_f32_16x16x32_bf16 v[40:43], v[52:55], a[76:79], v[40:43]\n"
"ds_write_addtid_b32 v46 offset:9600\n"
"v_add_f32 v77, v77, v109\n"
"v_mfma_f32_16x16x32_bf16 v[40:43], v[56:59], a[80:83], v[40:43]\n"
"s_waitcnt vmcnt(9)\n"
"ds_write_addtid_b32 v47 offset:14240\n"
"global_load_dwordx4 v[36:39], v8, s[56:57] offset:1024 sc1\n"
"v_mfma_f32_16x16x32_bf16 v[40:43], v[60:63], a[84:87], v[40:43]\n"
"v_add_f32 v78, v78, v110\n"
"v_add_f32 v79, v79, v111\n"
"v_mfma_f32_16x16x32_bf16 v[40:43], v[64:67], a[88:91], v[40:43]\n"
"v_cvt_pk_bf16_f32 v50, v76, v77\n"
"v_cvt_pk_bf16_f32 v51, v78, v79\n"
"v_mfma_f32_16x16x32_bf16 v[40:43], v[68:71], a[92:95], v[40:43]\n"
"v_lshlrev_b32 v72, 16, v116\n"
"v_and_b32 v73, 0xffff0000, v116\n"
"v_mfma_f32_16x16x32_bf16 v[44:47], v[84:87], a[96:99], 0\n"
"v_lshlrev_b32 v74, 16, v117\n"
"v_and_b32 v75, 0xffff0000, v117\n"
"v_mfma_f32_16x16x32_bf16 v[44:47], v[88:91], a[100:103], v[44:47]\n"
"ds_write_addtid_b32 v40 offset:512\n"
"v_lshlrev_b32 v76, 16, v118\n"
"v_mfma_f32_16x16x32_bf16 v[44:47], v[92:95], a[104:107], v[44:47]\n"
"ds_write_addtid_b32 v41 offset:5152\n"
"v_and_b32 v77, 0xffff0000, v118\n"
"v_mfma_f32_16x16x32_bf16 v[44:47], v[52:55], a[108:111], v[44:47]\n"
"ds_write_addtid_b32 v42 offset:9856\n"
"v_lshlrev_b32 v78, 16, v119\n"
"v_mfma_f32_16x16x32_bf16 v[44:47], v[56:59], a[112:115], v[44:47]\n"
"ds_write_addtid_b32 v43 offset:14496\n"
"v_and_b32 v79, 0xffff0000, v119\n"
"v_mfma_f32_16x16x32_bf16 v[44:47], v[60:63], a[116:119], v[44:47]\n"
"v_mfma_f32_16x16x32_bf16 v[44:47], v[64:67], a[120:123], v[44:47]\n"
"v_mfma_f32_16x16x32_bf16 v[44:47], v[68:71], a[124:127], v[44:47]\n"
"v_mfma_f32_16x16x32_bf16 v[40:43], v[84:87], a[128:131], 0\n"
"v_mfma_f32_16x16x32_bf16 v[40:43], v[88:91], a[132:135], v[40:43]\n"
"ds_write_addtid_b32 v44 offset:768\n"
"v_mfma_f32_16x16x32_bf16 v[40:43], v[92:95], a[136:139], v[40:43]\n"
"buffer_store_dwordx4 v[48:51], v11, s[40:43], s94 offen sc1\n"
"ds_write_addtid_b32 v45 offset:5408\n"
"v_mfma_f32_16x16x32_bf16 v[80:83], v[48:51], v[48:51], 0\n"
"v_mfma_f32_16x16x32_bf16 v[108:111], v[14:17], v[48:51], 0\n"
"v_mfma_f32_16x16x32_bf16 v[40:43], v[52:55], a[140:143], v[40:43]\n"
"ds_write_addtid_b32 v46 offset:10112\n"
"v_cndmask_b32_e64 v80, v80, v81, s[62:63]\n"
"v_mfma_f32_16x16x32_bf16 v[40:43], v[56:59], a[144:147], v[40:43]\n"
"ds_write_addtid_b32 v47 offset:14752\n"
"v_cndmask_b32_e64 v82, v82, v83, s[62:63]\n"
"v_mfma_f32_16x16x32_bf16 v[40:43], v[60:63], a[148:151], v[40:43]\n"
"global_load_dwordx4 v[112:115], v10, s[56:57] sc1\n"
"v_cndmask_b32_e64 v109, v80, v82, s[64:65]\n"
"v_mfma_f32_16x16x32_bf16 v[40:43], v[64:67], a[152:155], v[40:43]\n"
"buffer_store_dwordx2 v[108:109], v13, s[40:43], s94 offen offset:0 sc1\n"
"v_mfma_f32_16x16x32_bf16 v[40:43], v[68:71], a[156:159], v[40:43]\n"
"v_mfma_f32_16x16x32_bf16 v[48:51], v[84:87], a[160:163], 0\n"
"s_add_u32 s58, s50, 0x180000\n"
"v_mfma_f32_16x16x32_bf16 v[48:51], v[88:91], a[164:167], v[48:51]\n"
"s_and_b32 s58, s58, 0x3ffffff\n"
"ds_write_addtid_b32 v40 offset:1024\n"
"v_mfma_f32_16x16x32_bf16 v[48:51], v[92:95], a[168:171], v[48:51]\n"
"s_add_u32 s56, s40, s58\n"
"ds_write_addtid_b32 v41 offset:5664\n"
"v_mfma_f32_16x16x32_bf16 v[48:51], v[52:55], a[172:175], v[48:51]\n"
"s_addc_u32 s57, s41, 0\n"
"ds_write_addtid_b32 v42 offset:10368\n"
"v_mfma_f32_16x16x32_bf16 v[48:51], v[56:59], a[176:179], v[48:51]\n"
"s_waitcnt vmcnt(10)\n"
"ds_write_addtid_b32 v43 offset:15008\n"
"v_cmp_ne_u64 vcc, s[48:49], v[124:125]\n"
"v_mfma_f32_16x16x32_bf16 v[48:51], v[60:63], a[180:183], v[48:51]\n"
"v_mfma_f32_16x16x32_bf16 v[48:51], v[64:67], a[184:187], v[48:51]\n"
"s_cbranch_vccnz .LO_3s\n"
".LO_3r:\n"
"v_mfma_f32_16x16x32_bf16 v[48:51], v[68:71], a[188:191], v[48:51]\n"
"s_add_u32 s50, s50, 0x180000\n"
"global_load_dwordx4 v[40:43], v5, s[56:57] offset:0 sc1\n"
"v_mfma_f32_16x16x32_bf16 v[72:75], a[192:195], v[84:87], v[72:75]\n"
"v_mfma_f32_16x16x32_bf16 v[72:75], a[196:199], v[88:91], v[72:75]\n"
"ds_write_addtid_b32 v48 offset:1280\n"
"v_mfma_f32_16x16x32_bf16 v[72:75], a[200:203], v[92:95], v[72:75]\n"
"ds_write_addtid_b32 v49 offset:5920\n"
"v_mfma_f32_16x16x32_bf16 v[72:75], a[204:207], v[52:55], v[72:75]\n"
"ds_write_addtid_b32 v50 offset:10624\n"
"v_mfma_f32_16x16x32_bf16 v[72:75], a[208:211], v[56:59], v[72:75]\n"
"ds_write_addtid_b32 v51 offset:15264\n"
"global_load_dwordx4 v[44:47], v5, s[56:57] offset:1024 sc1\n"
"v_mfma_f32_16x16x32_bf16 v[72:75], a[212:215], v[60:63], v[72:75]\n"
"v_mfma_f32_16x16x32_bf16 v[72:75], a[216:219], v[64:67], v[72:75]\n"
"v_mfma_f32_16x16x32_bf16 v[72:75], a[220:223], v[68:71], v[72:75]\n"
"v_mfma_f32_16x16x32_bf16 v[76:79], a[224:227], v[84:87], v[76:79]\n"
"s_waitcnt lgkmcnt(0)\n"
"v_mfma_f32_16x16x32_bf16 v[76:79], a[228:231], v[88:91], v[76:79]\n"
"s_barrier\n"
"v_mfma_f32_16x16x32_bf16 v[76:79], a[232:235], v[92:95], v[76:79]\n"
"ds_read_b128 v[80:83], v2 offset:1536\n"
"ds_read_b128 v[84:87], v3 offset:512\n"
"v_mfma_f32_16x16x32_bf16 v[76:79], a[236:239], v[52:55], v[76:79]\n"
"buffer_load_dwordx2 v[120:121], v9, s[40:43], s50 offen sc1\n"
"v_mfma_f32_16x16x32_bf16 v[76:79], a[240:243], v[56:59], v[76:79]\n"
"global_load_dwordx4 v[48:51], v6, s[56:57] offset:0 sc1\n"
"ds_read_b128 v[88:91], v4 offset:2560\n"
"v_mfma_f32_16x16x32_bf16 v[76:79], a[244:247], v[60:63], v[76:79]\n"
"ds_read_b128 v[92:95], v2 offset:1792\n"
"ds_read_b128 v[108:111], v3 offset:768\n"
"v_mfma_f32_16x16x32_bf16 v[76:79], a[248:251], v[64:67], v[76:79]\n"
"global_load_dwordx4 v[52:55], v6, s[56:57] offset:1024 sc1\n"
"ds_read_b128 v[116:119], v4 offset:2816\n"
"v_mfma_f32_16x16x32_bf16 v[76:79], a[252:255], v[68:71], v[76:79]\n"
"v_mfma_f32_16x16x32_bf16 v[64:67], v[96:99], a[0:3], 0\n"
"v_mfma_f32_16x16x32_bf16 v[64:67], v[100:103], a[4:7], v[64:67]\n"
"s_waitcnt vmcnt(13)\n"
"v_mfma_f32_16x16x32_bf16 v[64:67], v[104:107], a[8:11], v[64:67]\n"
"s_waitcnt lgkmcnt(3)\n"
"v_add_f32 v80, v80, v84\n"
"v_add_f32 v81, v81, v85\n"
"v_mfma_f32_16x16x32_bf16 v[64:67], v[20:23], a[12:15], v[64:67]\n"
"s_waitcnt vmcnt(11)\n"
"global_load_dwordx4 v[56:59], v7, s[56:57] offset:0 sc1\n"
"v_add_f32 v82, v82, v86\n"
"v_mfma_f32_16x16x32_bf16 v[64:67], v[24:27], a[16:19], v[64:67]\n"
"v_add_f32 v83, v83, v87\n"
"v_add_f32 v80, v80, v88\n"
"v_mfma_f32_16x16x32_bf16 v[64:67], v[28:31], a[20:23], v[64:67]\n"
"s_waitcnt vmcnt(9)\n"
"v_add_f32 v81, v81, v89\n"
"v_add_f32 v82, v82, v90\n"
"v_mfma_f32_16x16x32_bf16 v[64:67], v[32:35], a[24:27], v[64:67]\n"
"v_add_f32 v83, v83, v91\n"
"v_add_f32 v80, v80, v72\n"
"v_mfma_f32_16x16x32_bf16 v[64:67], v[36:39], a[28:31], v[64:67]\n"
"v_add_f32 v81, v81, v73\n"
"v_add_f32 v82, v82, v74\n"
"v_mfma_f32_16x16x32_bf16 v[84:87], v[96:99], a[32:35], 0\n"
"v_add_f32 v83, v83, v75\n"
"v_mfma_f32_16x16x32_bf16 v[84:87], v[100:103], a[36:39], v[84:87]\n"
"s_add_u32 s93, s50, 0xffd00000\n"
"s_and_b32 s94, s93, 0xfbffffff\n"
"ds_write_addtid_b32 v64 offset:1536\n"
"v_mfma_f32_16x16x32_bf16 v[84:87], v[104:107], a[40:43], v[84:87]\n"
"ds_write_addtid_b32 v65 offset:6176\n"
"v_cvt_pk_bf16_f32 v72, v80, v81\n"
"v_mfma_f32_16x16x32_bf16 v[84:87], v[20:23], a[44:47], v[84:87]\n"
"ds_write_addtid_b32 v66 offset:10880\n"
"global_load_dwordx4 v[60:63], v7, s[56:57] offset:1024 sc1\n"
"v_mfma_f32_16x16x32_bf16 v[84:87], v[24:27], a[48:51], v[84:87]\n"
"ds_write_addtid_b32 v67 offset:15520\n"
"v_cvt_pk_bf16_f32 v73, v82, v83\n"
"v_mfma_f32_16x16x32_bf16 v[84:87], v[28:31], a[52:55], v[84:87]\n"
"s_waitcnt lgkmcnt(4)\n"
"v_add_f32 v92, v92, v108\n"
"v_add_f32 v93, v93, v109\n"
"v_mfma_f32_16x16x32_bf16 v[84:87], v[32:35], a[56:59], v[84:87]\n"
"v_add_f32 v94, v94, v110\n"
"v_add_f32 v95, v95, v111\n"
"v_mfma_f32_16x16x32_bf16 v[84:87], v[36:39], a[60:63], v[84:87]\n"
"v_add_f32 v92, v92, v116\n"
"v_add_f32 v93, v93, v117\n"
"v_mfma_f32_16x16x32_bf16 v[80:83], v[96:99], a[64:67], 0\n"
"global_load_dwordx4 v[64:67], v8, s[56:57] offset:0 sc1\n"
"v_add_f32 v94, v94, v118\n"
"v_mfma_f32_16x16x32_bf16 v[80:83], v[100:103], a[68:71], v[80:83]\n"
"ds_write_addtid_b32 v84 offset:1792\n"
"global_load_dwordx4 v[68:71], v8, s[56:57] offset:1024 sc1\n"
"v_mfma_f32_16x16x32_bf16 v[80:83], v[104:107], a[72:75], v[80:83]\n"
"ds_write_addtid_b32 v85 offset:6432\n"
"v_add_f32 v95, v95, v119\n"
"v_mfma_f32_16x16x32_bf16 v[80:83], v[20:23], a[76:79], v[80:83]\n"
"ds_write_addtid_b32 v86 offset:11136\n"
"v_add_f32 v92, v92, v76\n"
"v_mfma_f32_16x16x32_bf16 v[80:83], v[24:27], a[80:83], v[80:83]\n"
"ds_write_addtid_b32 v87 offset:15776\n"
"v_add_f32 v93, v93, v77\n"
"v_mfma_f32_16x16x32_bf16 v[80:83], v[28:31], a[84:87], v[80:83]\n"
"s_waitcnt vmcnt(10)\n"
"v_add_f32 v94, v94, v78\n"
"v_add_f32 v95, v95, v79\n"
"v_mfma_f32_16x16x32_bf16 v[80:83], v[32:35], a[88:91], v[80:83]\n"
"s_waitcnt vmcnt(9)\n"
"v_cvt_pk_bf16_f32 v74, v92, v93\n"
"v_cvt_pk_bf16_f32 v75, v94, v95\n"
"v_mfma_f32_16x16x32_bf16 v[80:83], v[36:39], a[92:95], v[80:83]\n"
"buffer_store_dwordx4 v[72:75], v11, s[40:43], s94 offen sc1\n"
"v_lshlrev_b32 v84, 16, v112\n"
"v_and_b32 v85, 0xffff0000, v112\n"
"v_lshlrev_b32 v86, 16, v113\n"
"v_mfma_f32_16x16x32_bf16 v[76:79], v[72:75], v[72:75], 0\n"
"buffer_store_dwordx2 v[0:1], v12, s[40:43], s95 offen offset:0 sc1\n"
"v_and_b32 v87, 0xffff0000, v113\n"
"v_mfma_f32_16x16x32_bf16 v[88:91], v[14:17], v[72:75], 0\n"
"v_lshlrev_b32 v92, 16, v114\n"
"v_and_b32 v93, 0xffff0000, v114\n"
"v_mfma_f32_16x16x32_bf16 v[108:111], v[96:99], a[96:99], 0\n"
"v_cndmask_b32_e64 v76, v76, v77, s[62:63]\n"
"v_cndmask_b32_e64 v78, v78, v79, s[62:63]\n"
"v_mfma_f32_16x16x32_bf16 v[108:111], v[100:103], a[100:103], v[108:111]\n"
"ds_write_addtid_b32 v80 offset:2048\n"
"v_cndmask_b32_e64 v89, v76, v78, s[64:65]\n"
"v_mfma_f32_16x16x32_bf16 v[108:111], v[104:107], a[104:107], v[108:111]\n"
"buffer_store_dwordx2 v[88:89], v13, s[40:43], s94 offen offset:0 sc1\n"
"ds_write_addtid_b32 v81 offset:6688\n"
"v_mfma_f32_16x16x32_bf16 v[108:111], v[20:23], a[108:111], v[108:111]\n"
"ds_write_addtid_b32 v82 offset:11392\n"
"v_lshlrev_b32 v94, 16, v115\n"
"v_mfma_f32_16x16x32_bf16 v[108:111], v[24:27], a[112:115], v[108:111]\n"
"ds_write_addtid_b32 v83 offset:16032\n"
"v_and_b32 v95, 0xffff0000, v115\n"
"v_mfma_f32_16x16x32_bf16 v[108:111], v[28:31], a[116:119], v[108:111]\n"
"v_mfma_f32_16x16x32_bf16 v[108:111], v[32:35], a[120:123], v[108:111]\n"
"v_mfma_f32_16x16x32_bf16 v[108:111], v[36:39], a[124:127], v[108:111]\n"
"v_mfma_f32_16x16x32_bf16 v[72:75], v[96:99], a[128:131], 0\n"
"v_mfma_f32_16x16x32_bf16 v[72:75], v[100:103], a[132:135], v[72:75]\n"
"ds_write_addtid_b32 v108 offset:2304\n"
"v_mfma_f32_16x16x32_bf16 v[72:75], v[104:107], a[136:139], v[72:75]\n"
"ds_write_addtid_b32 v109 offset:6944\n"
"v_mfma_f32_16x16x32_bf16 v[72:75], v[20:23], a[140:143], v[72:75]\n"
"ds_write_addtid_b32 v110 offset:11648\n"
"v_mfma_f32_16x16x32_bf16 v[72:75], v[24:27], a[144:147], v[72:75]\n"
"ds_write_addtid_b32 v111 offset:16288\n"
"v_mfma_f32_16x16x32_bf16 v[72:75], v[28:31], a[148:151], v[72:75]\n"
"v_mfma_f32_16x16x32_bf16 v[72:75], v[32:35], a[152:155], v[72:75]\n"
"global_load_dwordx4 v[88:91], v10, s[56:57] sc1\n"
"v_mfma_f32_16x16x32_bf16 v[72:75], v[36:39], a[156:159], v[72:75]\n"
"v_mfma_f32_16x16x32_bf16 v[76:79], v[96:99], a[160:163], 0\n"
"v_mfma_f32_16x16x32_bf16 v[76:79], v[100:103], a[164:167], v[76:79]\n"
"ds_write_addtid_b32 v72 offset:2560\n"
"v_mfma_f32_16x16x32_bf16 v[76:79], v[104:107], a[168:171], v[76:79]\n"
"s_add_u32 s58, s50, 0x180000\n"
"ds_write_addtid_b32 v73 offset:7200\n"
"v_mfma_f32_16x16x32_bf16 v[76:79], v[20:23], a[172:175], v[76:79]\n"
"s_and_b32 s58, s58, 0x3ffffff\n"
"ds_write_addtid_b32 v74 offset:11904\n"
"v_mfma_f32_16x16x32_bf16 v[76:79], v[24:27], a[176:179], v[76:79]\n"
"s_add_u32 s56, s40, s58\n"
"ds_write_addtid_b32 v75 offset:16544\n"
"v_mfma_f32_16x16x32_bf16 v[76:79], v[28:31], a[180:183], v[76:79]\n"
"s_addc_u32 s57, s41, 0\n"
"v_mfma_f32_16x16x32_bf16 v[76:79], v[32:35], a[184:187], v[76:79]\n"
"s_waitcnt vmcnt(10)\n"
"v_cmp_ne_u64 vcc, s[48:49], v[120:121]\n"
"v_mfma_f32_16x16x32_bf16 v[76:79], v[36:39], a[188:191], v[76:79]\n"
"v_mfma_f32_16x16x32_bf16 v[84:87], a[192:195], v[96:99], v[84:87]\n"
"s_cbranch_vccnz .LO_4s\n"
".LO_4r:\n"
"v_mfma_f32_16x16x32_bf16 v[84:87], a[196:199], v[100:103], v[84:87]\n"
"s_add_u32 s50, s50, 0x180000\n"
"ds_write_addtid_b32 v76 offset:2816\n"
"v_mfma_f32_16x16x32_bf16 v[84:87], a[200:203], v[104:107], v[84:87]\n"
"ds_write_addtid_b32 v77 offset:7456\n"
"global_load_dwordx4 v[72:75], v5, s[56:57] offset:0 sc1\n"
"v_mfma_f32_16x16x32_bf16 v[84:87], a[204:207], v[20:23], v[84:87]\n"
"ds_write_addtid_b32 v78 offset:12160\n"
"v_mfma_f32_16x16x32_bf16 v[84:87], a[208:211], v[24:27], v[84:87]\n"
"ds_write_addtid_b32 v79 offset:16800\n"
"v_mfma_f32_16x16x32_bf16 v[84:87], a[212:215], v[28:31], v[84:87]\n"
"v_mfma_f32_16x16x32_bf16 v[84:87], a[216:219], v[32:35], v[84:87]\n"
"v_mfma_f32_16x16x32_bf16 v[84:87], a[220:223], v[36:39], v[84:87]\n"
"global_load_dwordx4 v[76:79], v5, s[56:57] offset:1024 sc1\n"
"v_mfma_f32_16x16x32_bf16 v[92:95], a[224:227], v[96:99], v[92:95]\n"
"s_waitcnt lgkmcnt(0)\n"
"global_load_dwordx4 v[80:83], v6, s[56:57] offset:0 sc1\n"
"v_mfma_f32_16x16x32_bf16 v[92:95], a[228:231], v[100:103], v[92:95]\n"
"s_barrier\n"
"v_mfma_f32_16x16x32_bf16 v[92:95], a[232:235], v[104:107], v[92:95]\n"
"ds_read_b128 v[96:99], v2 offset:0\n"
"ds_read_b128 v[100:103], v3 offset:2048\n"
"v_mfma_f32_16x16x32_bf16 v[92:95], a[236:239], v[20:23], v[92:95]\n"
"ds_read_b128 v[104:107], v4 offset:1024\n"
"ds_read_b128 v[108:111], v2 offset:256\n"
"v_mfma_f32_16x16x32_bf16 v[92:95], a[240:243], v[24:27], v[92:95]\n"
"ds_read_b128 v[112:115], v3 offset:2304\n"
"ds_read_b128 v[116:119], v4 offset:1280\n"
"v_mfma_f32_16x16x32_bf16 v[92:95], a[244:247], v[28:31], v[92:95]\n"
"buffer_load_dwordx2 v[124:125], v9, s[40:43], s50 offen sc1\n"
"v_mfma_f32_16x16x32_bf16 v[92:95], a[248:251], v[32:35], v[92:95]\n"
"v_mfma_f32_16x16x32_bf16 v[92:95], a[252:255], v[36:39], v[92:95]\n"
"v_mfma_f32_16x16x32_bf16 v[120:123], v[40:43], a[0:3], 0\n"
"v_mfma_f32_16x16x32_bf16 v[120:123], v[44:47], a[4:7], v[120:123]\n"
"s_waitcnt vmcnt(12)\n"
"global_load_dwordx4 v[20:23], v6, s[56:57] offset:1024 sc1\n"
"v_mfma_f32_16x16x32_bf16 v[120:123], v[48:51], a[8:11], v[120:123]\n"
"s_waitcnt lgkmcnt(3)\n"
"v_add_f32 v96, v96, v100\n"
"v_add_f32 v97, v97, v101\n"
"v_mfma_f32_16x16x32_bf16 v[120:123], v[52:55], a[12:15], v[120:123]\n"
"s_waitcnt vmcnt(11)\n"
"v_add_f32 v98, v98, v102\n"
"v_add_f32 v99, v99, v103\n"
"v_mfma_f32_16x16x32_bf16 v[120:123], v[56:59], a[16:19], v[120:123]\n"
"v_add_f32 v96, v96, v104\n"
"v_add_f32 v97, v97, v105\n"
"v_mfma_f32_16x16x32_bf16 v[120:123], v[60:63], a[20:23], v[120:123]\n"
"s_waitcnt vmcnt(9)\n"
"v_add_f32 v98, v98, v106\n"
"v_add_f32 v99, v99, v107\n"
"v_mfma_f32_16x16x32_bf16 v[120:123], v[64:67], a[24:27], v[120:123]\n"
"global_load_dwordx4 v[24:27], v7, s[56:57] offset:0 sc1\n"
"v_add_f32 v96, v96, v84\n"
"v_mfma_f32_16x16x32_bf16 v[120:123], v[68:71], a[28:31], v[120:123]\n"
"global_load_dwordx4 v[28:31], v7, s[56:57] offset:1024 sc1\n"
"v_add_f32 v97, v97, v85\n"
"v_mfma_f32_16x16x32_bf16 v[100:103], v[40:43], a[32:35], 0\n"
"v_add_f32 v98, v98, v86\n"
"v_add_f32 v99, v99, v87\n"
"v_mfma_f32_16x16x32_bf16 v[100:103], v[44:47], a[36:39], v[100:103]\n"
"s_add_u32 s95, s50, 0xffd00000\n"
"s_and_b32 s94, s95, 0xfbffffff\n"
"ds_write_addtid_b32 v120 offset:0\n"
"v_mfma_f32_16x16x32_bf16 v[100:103], v[48:51], a[40:43], v[100:103]\n"
"s_waitcnt vmcnt(8)\n"
"buffer_store_dwordx2 v[0:1], v12, s[40:43], s93 offen offset:0 sc1\n"
"ds_write_addtid_b32 v121 offset:4640\n"
"v_mfma_f32_16x16x32_bf16 v[100:103], v[52:55], a[44:47], v[100:103]\n"
"ds_write_addtid_b32 v122 offset:9344\n"
"v_cvt_pk_bf16_f32 v84, v96, v97\n"
"v_mfma_f32_16x16x32_bf16 v[100:103], v[56:59], a[48:51], v[100:103]\n"
"ds_write_addtid_b32 v123 offset:13984\n"
"global_load_dwordx4 v[32:35], v8, s[56:57] offset:0 sc1\n"
"v_mfma_f32_16x16x32_bf16 v[100:103], v[60:63], a[52:55], v[100:103]\n"
"v_cvt_pk_bf16_f32 v85, v98, v99\n"
"v_mfma_f32_16x16x32_bf16 v[100:103], v[64:67], a[56:59], v[100:103]\n"
"s_waitcnt lgkmcnt(4)\n"
"v_add_f32 v108, v108, v112\n"
"v_add_f32 v109, v109, v113\n"
"v_mfma_f32_16x16x32_bf16 v[100:103], v[68:71], a[60:63], v[100:103]\n"
"v_add_f32 v110, v110, v114\n"
"v_add_f32 v111, v111, v115\n"
"v_mfma_f32_16x16x32_bf16 v[96:99], v[40:43], a[64:67], 0\n"
"v_add_f32 v108, v108, v116\n"
"v_add_f32 v109, v109, v117\n"
"v_mfma_f32_16x16x32_bf16 v[96:99], v[44:47], a[68:71], v[96:99]\n"
"ds_write_addtid_b32 v100 offset:256\n"
"v_add_f32 v110, v110, v118\n"
"v_mfma_f32_16x16x32_bf16 v[96:99], v[48:51], a[72:75], v[96:99]\n"
"ds_write_addtid_b32 v101 offset:4896\n"
"v_add_f32 v111, v111, v119\n"
"v_mfma_f32_16x16x32_bf16 v[96:99], v[52:55], a[76:79], v[96:99]\n"
"ds_write_addtid_b32 v102 offset:9600\n"
"v_add_f32 v108, v108, v92\n"
"v_mfma_f32_16x16x32_bf16 v[96:99], v[56:59], a[80:83], v[96:99]\n"
"ds_write_addtid_b32 v103 offset:14240\n"
"v_add_f32 v109, v109, v93\n"
"v_mfma_f32_16x16x32_bf16 v[96:99], v[60:63], a[84:87], v[96:99]\n"
"global_load_dwordx4 v[36:39], v8, s[56:57] offset:1024 sc1\n"
"v_add_f32 v110, v110, v94\n"
"v_mfma_f32_16x16x32_bf16 v[96:99], v[64:67], a[88:91], v[96:99]\n"
"s_waitcnt vmcnt(10)\n"
"v_add_f32 v111, v111, v95\n"
"v_cvt_pk_bf16_f32 v86, v108, v109\n"
"v_mfma_f32_16x16x32_bf16 v[96:99], v[68:71], a[92:95], v[96:99]\n"
"v_cvt_pk_bf16_f32 v87, v110, v111\n"
"v_lshlrev_b32 v100, 16, v88\n"
"v_mfma_f32_16x16x32_bf16 v[92:95], v[40:43], a[96:99], 0\n"
"v_and_b32 v101, 0xffff0000, v88\n"
"v_lshlrev_b32 v102, 16, v89\n"
"v_mfma_f32_16x16x32_bf16 v[92:95], v[44:47], a[100:103], v[92:95]\n"
"ds_write_addtid_b32 v96 offset:512\n"
"v_and_b32 v103, 0xffff0000, v89\n"
"v_mfma_f32_16x16x32_bf16 v[92:95], v[48:51], a[104:107], v[92:95]\n"
"ds_write_addtid_b32 v97 offset:5152\n"
"v_lshlrev_b32 v104, 16, v90\n"
"v_mfma_f32_16x16x32_bf16 v[92:95], v[52:55], a[108:111], v[92:95]\n"
"buffer_store_dwordx4 v[84:87], v11, s[40:43], s94 offen sc1\n"
"ds_write_addtid_b32 v98 offset:9856\n"
"v_and_b32 v105, 0xffff0000, v90\n"
"v_lshlrev_b32 v106, 16, v91\n"
"v_mfma_f32_16x16x32_bf16 v[108:111], v[84:87], v[84:87], 0\n"
"v_and_b32 v107, 0xffff0000, v91\n"
"v_mfma_f32_16x16x32_bf16 v[112:115], v[14:17], v[84:87], 0\n"
"v_mfma_f32_16x16x32_bf16 v[92:95], v[56:59], a[112:115], v[92:95]\n"
"ds_write_addtid_b32 v99 offset:14496\n"
"v_cndmask_b32_e64 v108, v108, v109, s[62:63]\n"
"v_mfma_f32_16x16x32_bf16 v[92:95], v[60:63], a[116:119], v[92:95]\n"
"v_cndmask_b32_e64 v110, v110, v111, s[62:63]\n"
"v_cndmask_b32_e64 v113, v108, v110, s[64:65]\n"
"v_mfma_f32_16x16x32_bf16 v[92:95], v[64:67], a[120:123], v[92:95]\n"
"buffer_store_dwordx2 v[112:113], v13, s[40:43], s94 offen offset:0 sc1\n"
"v_mfma_f32_16x16x32_bf16 v[92:95], v[68:71], a[124:127], v[92:95]\n"
"v_mfma_f32_16x16x32_bf16 v[84:87], v[40:43], a[128:131], 0\n"
"v_mfma_f32_16x16x32_bf16 v[84:87], v[44:47], a[132:135], v[84:87]\n"
"ds_write_addtid_b32 v92 offset:768\n"
"v_mfma_f32_16x16x32_bf16 v[84:87], v[48:51], a[136:139], v[84:87]\n"
"ds_write_addtid_b32 v93 offset:5408\n"
"v_mfma_f32_16x16x32_bf16 v[84:87], v[52:55], a[140:143], v[84:87]\n"
"ds_write_addtid_b32 v94 offset:10112\n"
"v_mfma_f32_16x16x32_bf16 v[84:87], v[56:59], a[144:147], v[84:87]\n"
"ds_write_addtid_b32 v95 offset:14752\n"
"v_mfma_f32_16x16x32_bf16 v[84:87], v[60:63], a[148:151], v[84:87]\n"
"v_mfma_f32_16x16x32_bf16 v[84:87], v[64:67], a[152:155], v[84:87]\n"
"v_mfma_f32_16x16x32_bf16 v[84:87], v[68:71], a[156:159], v[84:87]\n"
"global_load_dwordx4 v[96:99], v10, s[56:57] sc1\n"
"v_mfma_f32_16x16x32_bf16 v[92:95], v[40:43], a[160:163], 0\n"
"v_mfma_f32_16x16x32_bf16 v[92:95], v[44:47], a[164:167], v[92:95]\n"
"s_add_u32 s58, s50, 0x180000\n"
"ds_write_addtid_b32 v84 offset:1024\n"
"v_mfma_f32_16x16x32_bf16 v[92:95], v[48:51], a[168:171], v[92:95]\n"
"s_and_b32 s58, s58, 0x3ffffff\n"
"ds_write_addtid_b32 v85 offset:5664\n"
"v_mfma_f32_16x16x32_bf16 v[92:95], v[52:55], a[172:175], v[92:95]\n"
"s_add_u32 s56, s40, s58\n"
"ds_write_addtid_b32 v86 offset:10368\n"
"v_mfma_f32_16x16x32_bf16 v[92:95], v[56:59], a[176:179], v[92:95]\n"
"s_addc_u32 s57, s41, 0\n"
"ds_write_addtid_b32 v87 offset:15008\n"
"v_mfma_f32_16x16x32_bf16 v[92:95], v[60:63], a[180:183], v[92:95]\n"
"s_waitcnt vmcnt(9)\n"
"v_cmp_ne_u64 vcc, s[48:49], v[124:125]\n"
"v_mfma_f32_16x16x32_bf16 v[92:95], v[64:67], a[184:187], v[92:95]\n"
"v_mfma_f32_16x16x32_bf16 v[92:95], v[68:71], a[188:191], v[92:95]\n"
"s_cbranch_vccnz .LO_1s\n"
".LO_1r:\n"
"v_mfma_f32_16x16x32_bf16 v[100:103], a[192:195], v[40:43], v[100:103]\n"
"s_add_u32 s50, s50, 0x180000\n"
"global_load_dwordx4 v[84:87], v5, s[56:57] offset:0 sc1\n"
"v_mfma_f32_16x16x32_bf16 v[100:103], a[196:199], v[44:47], v[100:103]\n"
"ds_write_addtid_b32 v92 offset:1280\n"
"v_mfma_f32_16x16x32_bf16 v[100:103], a[200:203], v[48:51], v[100:103]\n"
"ds_write_addtid_b32 v93 offset:5920\n"
"v_mfma_f32_16x16x32_bf16 v[100:103], a[204:207], v[52:55], v[100:103]\n"
"ds_write_addtid_b32 v94 offset:10624\n"
"v_mfma_f32_16x16x32_bf16 v[100:103], a[208:211], v[56:59], v[100:103]\n"
"ds_write_addtid_b32 v95 offset:15264\n"
"v_mfma_f32_16x16x32_bf16 v[100:103], a[212:215], v[60:63], v[100:103]\n"
"global_load_dwordx4 v[88:91], v5, s[56:57] offset:1024 sc1\n"
"v_mfma_f32_16x16x32_bf16 v[100:103], a[216:219], v[64:67], v[100:103]\n"
"v_mfma_f32_16x16x32_bf16 v[100:103], a[220:223], v[68:71], v[100:103]\n"
"v_mfma_f32_16x16x32_bf16 v[104:107], a[224:227], v[40:43], v[104:107]\n"
"s_waitcnt lgkmcnt(0)\n"
"v_mfma_f32_16x16x32_bf16 v[104:107], a[228:231], v[44:47], v[104:107]\n"
"s_barrier\n"
"v_mfma_f32_16x16x32_bf16 v[104:107], a[232:235], v[48:51], v[104:107]\n"
"global_load_dwordx4 v[92:95], v6, s[56:57] offset:0 sc1\n"
"ds_read_b128 v[40:43], v2 offset:1536\n"
"v_mfma_f32_16x16x32_bf16 v[104:107], a[236:239], v[52:55], v[104:107]\n"
"ds_read_b128 v[44:47], v3 offset:512\n"
"ds_read_b128 v[48:51], v4 offset:2560\n"
"v_mfma_f32_16x16x32_bf16 v[104:107], a[240:243], v[56:59], v[104:107]\n"
"ds_read_b128 v[108:111], v2 offset:1792\n"
"ds_read_b128 v[112:115], v3 offset:768\n"
"v_mfma_f32_16x16x32_bf16 v[104:107], a[244:247], v[60:63], v[104:107]\n"
"ds_read_b128 v[116:119], v4 offset:2816\n"
"v_mfma_f32_16x16x32_bf16 v[104:107], a[248:251], v[64:67], v[104:107]\n"
"buffer_load_dwordx2 v[120:121], v9, s[40:43], s50 offen sc1\n"
"v_mfma_f32_16x16x32_bf16 v[104:107], a[252:255], v[68:71], v[104:107]\n"
"global_load_dwordx4 v[52:55], v6, s[56:57] offset:1024 sc1\n"
"v_mfma_f32_16x16x32_bf16 v[64:67], v[72:75], a[0:3], 0\n"
"v_mfma_f32_16x16x32_bf16 v[64:67], v[76:79], a[4:7], v[64:67]\n"
"s_waitcnt vmcnt(13)\n"
"v_mfma_f32_16x16x32_bf16 v[64:67], v[80:83], a[8:11], v[64:67]\n"
"s_waitcnt lgkmcnt(3)\n"
"v_add_f32 v40, v40, v44\n"
"v_add_f32 v41, v41, v45\n"
"v_mfma_f32_16x16x32_bf16 v[64:67], v[20:23], a[12:15], v[64:67]\n"
"s_waitcnt vmcnt(11)\n"
"v_add_f32 v42, v42, v46\n"
"v_add_f32 v43, v43, v47\n"
"v_mfma_f32_16x16x32_bf16 v[64:67], v[24:27], a[16:19], v[64:67]\n"
"global_load_dwordx4 v[56:59], v7, s[56:57] offset:0 sc1\n"
"v_add_f32 v40, v40, v48\n"
"v_mfma_f32_16x16x32_bf16 v[64:67], v[28:31], a[20:23], v[64:67]\n"
"s_waitcnt vmcnt(9)\n"
"v_add_f32 v41, v41, v49\n"
"v_add_f32 v42, v42, v50\n"
"v_mfma_f32_16x16x32_bf16 v[64:67], v[32:35], a[24:27], v[64:67]\n"
"v_add_f32 v43, v43, v51\n"
"v_add_f32 v40, v40, v100\n"
"v_mfma_f32_16x16x32_bf16 v[64:67], v[36:39], a[28:31], v[64:67]\n"
"v_add_f32 v41, v41, v101\n"
"v_add_f32 v42, v42, v102\n"
"v_mfma_f32_16x16x32_bf16 v[44:47], v[72:75], a[32:35], 0\n"
"v_add_f32 v43, v43, v103\n"
"v_mfma_f32_16x16x32_bf16 v[44:47], v[76:79], a[36:39], v[44:47]\n"
"ds_write_addtid_b32 v64 offset:1536\n"
"global_load_dwordx4 v[60:63], v7, s[56:57] offset:1024 sc1\n"
"v_mfma_f32_16x16x32_bf16 v[44:47], v[80:83], a[40:43], v[44:47]\n"
"s_add_u32 s93, s50, 0xffd00000\n"
"s_and_b32 s94, s93, 0xfbffffff\n"
"ds_write_addtid_b32 v65 offset:6176\n"
"v_mfma_f32_16x16x32_bf16 v[44:47], v[20:23], a[44:47], v[44:47]\n"
"ds_write_addtid_b32 v66 offset:10880\n"
"v_cvt_pk_bf16_f32 v48, v40, v41\n"
"v_mfma_f32_16x16x32_bf16 v[44:47], v[24:27], a[48:51], v[44:47]\n"
"ds_write_addtid_b32 v67 offset:15520\n"
"v_cvt_pk_bf16_f32 v49, v42, v43\n"
"v_mfma_f32_16x16x32_bf16 v[44:47], v[28:31], a[52:55], v[44:47]\n"
"s_waitcnt lgkmcnt(4)\n"
"v_add_f32 v108, v108, v112\n"
"v_add_f32 v109, v109, v113\n"
"v_mfma_f32_16x16x32_bf16 v[44:47], v[32:35], a[56:59], v[44:47]\n"
"s_waitcnt vmcnt(8)\n"
"global_load_dwordx4 v[64:67], v8, s[56:57] offset:0 sc1\n"
"v_add_f32 v110, v110, v114\n"
"v_mfma_f32_16x16x32_bf16 v[44:47], v[36:39], a[60:63], v[44:47]\n"
"buffer_store_dwordx2 v[0:1], v12, s[40:43], s95 offen offset:0 sc1\n"
"v_add_f32 v111, v111, v115\n"
"v_mfma_f32_16x16x32_bf16 v[40:43], v[72:75], a[64:67], 0\n"
"v_add_f32 v108, v108, v116\n"
"v_add_f32 v109, v109, v117\n"
"v_mfma_f32_16x16x32_bf16 v[40:43], v[76:79], a[68:71], v[40:43]\n"
"ds_write_addtid_b32 v44 offset:1792\n"
"v_add_f32 v110, v110, v118\n"
"v_mfma_f32_16x16x32_bf16 v[40:43], v[80:83], a[72:75], v[40:43]\n"
"ds_write_addtid_b32 v45 offset:6432\n"
"v_add_f32 v111, v111, v119\n"
"v_mfma_f32_16x16x32_bf16 v[40:43], v[20:23], a[76:79], v[40:43]\n"
"ds_write_addtid_b32 v46 offset:11136\n"
"global_load_dwordx4 v[68:71], v8, s[56:57] offset:1024 sc1\n"
"v_mfma_f32_16x16x32_bf16 v[40:43], v[24:27], a[80:83], v[40:43]\n"
"ds_write_addtid_b32 v47 offset:15776\n"
"v_add_f32 v108, v108, v104\n"
"v_mfma_f32_16x16x32_bf16 v[40:43], v[28:31], a[84:87], v[40:43]\n"
"v_add_f32 v109, v109, v105\n"
"v_add_f32 v110, v110, v106\n"
"v_mfma_f32_16x16x32_bf16 v[40:43], v[32:35], a[88:91], v[40:43]\n"
"v_add_f32 v111, v111, v107\n"
"v_cvt_pk_bf16_f32 v50, v108, v109\n"
"v_mfma_f32_16x16x32_bf16 v[40:43], v[36:39], a[92:95], v[40:43]\n"
"s_waitcnt vmcnt(10)\n"
"v_cvt_pk_bf16_f32 v51, v110, v111\n"
"v_lshlrev_b32 v112, 16, v96\n"
"v_mfma_f32_16x16x32_bf16 v[44:47], v[72:75], a[96:99], 0\n"
"v_and_b32 v113, 0xffff0000, v96\n"
"v_lshlrev_b32 v114, 16, v97\n"
"v_mfma_f32_16x16x32_bf16 v[44:47], v[76:79], a[100:103], v[44:47]\n"
"ds_write_addtid_b32 v40 offset:2048\n"
"v_and_b32 v115, 0xffff0000, v97\n"
"v_mfma_f32_16x16x32_bf16 v[44:47], v[80:83], a[104:107], v[44:47]\n"
"ds_write_addtid_b32 v41 offset:6688\n"
"v_lshlrev_b32 v108, 16, v98\n"
"v_mfma_f32_16x16x32_bf16 v[44:47], v[20:23], a[108:111], v[44:47]\n"
"ds_write_addtid_b32 v42 offset:11392\n"
"v_and_b32 v109, 0xffff0000, v98\n"
"v_mfma_f32_16x16x32_bf16 v[44:47], v[24:27], a[112:115], v[44:47]\n"
"ds_write_addtid_b32 v43 offset:16032\n"
"v_lshlrev_b32 v110, 16, v99\n"
"v_mfma_f32_16x16x32_bf16 v[44:47], v[28:31], a[116:119], v[44:47]\n"
"v_and_b32 v111, 0xffff0000, v99\n"
"v_mfma_f32_16x16x32_bf16 v[44:47], v[32:35], a[120:123], v[44:47]\n"
"v_mfma_f32_16x16x32_bf16 v[44:47], v[36:39], a[124:127], v[44:47]\n"
"buffer_store_dwordx4 v[48:51], v11, s[40:43], s94 offen sc1\n"
"v_mfma_f32_16x16x32_bf16 v[40:43], v[48:51], v[48:51], 0\n"
"v_mfma_f32_16x16x32_bf16 v[96:99], v[14:17], v[48:51], 0\n"
"v_mfma_f32_16x16x32_bf16 v[100:103], v[72:75], a[128:131], 0\n"
"v_cndmask_b32_e64 v40, v40, v41, s[62:63]\n"
"v_cndmask_b32_e64 v42, v42, v43, s[62:63]\n"
"v_mfma_f32_16x16x32_bf16 v[100:103], v[76:79], a[132:135], v[100:103]\n"
"ds_write_addtid_b32 v44 offset:2304\n"
"v_cndmask_b32_e64 v97, v40, v42, s[64:65]\n"
"v_mfma_f32_16x16x32_bf16 v[100:103], v[80:83], a[136:139], v[100:103]\n"
"buffer_store_dwordx2 v[96:97], v13, s[40:43], s94 offen offset:0 sc1\n"
"ds_write_addtid_b32 v45 offset:6944\n"
"v_mfma_f32_16x16x32_bf16 v[100:103], v[20:23], a[140:143], v[100:103]\n"
"ds_write_addtid_b32 v46 offset:11648\n"
"v_mfma_f32_16x16x32_bf16 v[100:103], v[24:27], a[144:147], v[100:103]\n"
"ds_write_addtid_b32 v47 offset:16288\n"
"global_load_dwordx4 v[116:119], v10, s[56:57] sc1\n"
"v_mfma_f32_16x16x32_bf16 v[100:103], v[28:31], a[148:151], v[100:103]\n"
"v_mfma_f32_16x16x32_bf16 v[100:103], v[32:35], a[152:155], v[100:103]\n"
"v_mfma_f32_16x16x32_bf16 v[100:103], v[36:39], a[156:159], v[100:103]\n"
"v_mfma_f32_16x16x32_bf16 v[40:43], v[72:75], a[160:163], 0\n"
"v_mfma_f32_16x16x32_bf16 v[40:43], v[76:79], a[164:167], v[40:43]\n"
"ds_write_addtid_b32 v100 offset:2560\n"
"v_mfma_f32_16x16x32_bf16 v[40:43], v[80:83], a[168:171], v[40:43]\n"
"s_add_u32 s58, s50, 0x180000\n"
"ds_write_addtid_b32 v101 offset:7200\n"
"v_mfma_f32_16x16x32_bf16 v[40:43], v[20:23], a[172:175], v[40:43]\n"
"s_and_b32 s58, s58, 0x3ffffff\n"
"ds_write_addtid_b32 v102 offset:11904\n"
"v_mfma_f32_16x16x32_bf16 v[40:43], v[24:27], a[176:179], v[40:43]\n"
"s_add_u32 s56, s40, s58\n"
"ds_write_addtid_b32 v103 offset:16544\n"
"v_mfma_f32_16x16x32_bf16 v[40:43], v[28:31], a[180:183], v[40:43]\n"
"s_addc_u32 s57, s41, 0\n"
"v_mfma_f32_16x16x32_bf16 v[40:43], v[32:35], a[184:187], v[40:43]\n"
"s_waitcnt vmcnt(9)\n"
"v_cmp_ne_u64 vcc, s[48:49], v[120:121]\n"
"v_mfma_f32_16x16x32_bf16 v[40:43], v[36:39], a[188:191], v[40:43]\n"
"v_mfma_f32_16x16x32_bf16 v[112:115], a[192:195], v[72:75], v[112:115]\n"
"s_cbranch_vccnz .LO_2s\n"
".LO_2r:\n"
"v_mfma_f32_16x16x32_bf16 v[112:115], a[196:199], v[76:79], v[112:115]\n"
"s_add_u32 s50, s50, 0x180000\n"
"ds_write_addtid_b32 v40 offset:2816\n"
"global_load_dwordx4 v[96:99], v5, s[56:57] offset:0 sc1\n"
"v_mfma_f32_16x16x32_bf16 v[112:115], a[200:203], v[80:83], v[112:115]\n"
"ds_write_addtid_b32 v41 offset:7456\n"
"v_mfma_f32_16x16x32_bf16 v[112:115], a[204:207], v[20:23], v[112:115]\n"
"ds_write_addtid_b32 v42 offset:12160\n"
"v_mfma_f32_16x16x32_bf16 v[112:115], a[208:211], v[24:27], v[112:115]\n"
"ds_write_addtid_b32 v43 offset:16800\n"
"v_mfma_f32_16x16x32_bf16 v[112:115], a[212:215], v[28:31], v[112:115]\n"
"v_mfma_f32_16x16x32_bf16 v[112:115], a[216:219], v[32:35], v[112:115]\n"
"global_load_dwordx4 v[100:103], v5, s[56:57] offset:1024 sc1\n"
"v_mfma_f32_16x16x32_bf16 v[112:115], a[220:223], v[36:39], v[112:115]\n"
"v_mfma_f32_16x16x32_bf16 v[108:111], a[224:227], v[72:75], v[108:111]\n"
"s_waitcnt lgkmcnt(0)\n"
"v_mfma_f32_16x16x32_bf16 v[108:111], a[228:231], v[76:79], v[108:111]\n"
"s_barrier\n"
"v_mfma_f32_16x16x32_bf16 v[108:111], a[232:235], v[80:83], v[108:111]\n"
"buffer_load_dwordx2 v[124:125], v9, s[40:43], s50 offen sc1\n"
"v_mfma_f32_16x16x32_bf16 v[108:111], a[236:239], v[20:23], v[108:111]\n"
"global_load_dwordx4 v[104:107], v6, s[56:57] offset:0 sc1\n"
"ds_read_b128 v[72:75], v2 offset:0\n"
"v_mfma_f32_16x16x32_bf16 v[108:111], a[240:243], v[24:27], v[108:111]\n"
"ds_read_b128 v[44:47], v3 offset:2048\n"
"ds_read_b128 v[48:51], v4 offset:1024\n"
"v_mfma_f32_16x16x32_bf16 v[108:111], a[244:247], v[28:31], v[108:111]\n"
"ds_read_b128 v[76:79], v2 offset:256\n"
"ds_read_b128 v[80:83], v3 offset:2304\n"
"v_mfma_f32_16x16x32_bf16 v[108:111], a[248:251], v[32:35], v[108:111]\n"
"ds_read_b128 v[120:123], v4 offset:1280\n"
"v_mfma_f32_16x16x32_bf16 v[108:111], a[252:255], v[36:39], v[108:111]\n"
"s_branch .LO_top\n"
".LO_1s:\n"
"s_cmp_ge_u32 s50, s53\n"
"s_cbranch_scc1 .LO_end\n"
"s_cmp_ge_u32 s50, s52\n"
"s_cbranch_scc1 .LO_1r\n"
"s_sub_u32 s60, s50, 0x0\n"
"s_mov_b32 s61, 200000\n"
"s_nop 7\n"
".LO_1p:\n"
"buffer_load_dwordx2 v[124:125], v9, s[40:43], s60 offen sc1\n"
"s_waitcnt vmcnt(0)\n"
"v_cmp_ne_u64 vcc, s[48:49], v[124:125]\n"
"s_cbranch_vccz .LO_1px\n"
"s_sleep 1\n"
"s_sub_u32 s61, s61, 1\n"
"s_cbranch_scc0 .LO_1p\n"
"s_branch .LO_end\n"
".LO_1px:\n"
"s_branch .LO_1r\n"
".LO_2s:\n"
"s_cmp_ge_u32 s50, s53\n"
"s_cbranch_scc1 .LO_end\n"
"s_cmp_ge_u32 s50, s52\n"
"s_cbranch_scc1 .LO_2r\n"
"s_sub_u32 s60, s50, 0x0\n"
"s_mov_b32 s61, 200000\n"
"s_nop 7\n"
".LO_2p:\n"
"buffer_load_dwordx2 v[120:121], v9, s[40:43], s60 offen sc1\n"
"s_waitcnt vmcnt(0)\n"
"v_cmp_ne_u64 vcc, s[48:49], v[120:121]\n"
"s_cbranch_vccz .LO_2px\n"
"s_sleep 1\n"
"s_sub_u32 s61, s61, 1\n"
"s_cbranch_scc0 .LO_2p\n"
"s_branch .LO_end\n"
".LO_2px:\n"
"s_branch .LO_2r\n"
".LO_3s:\n"
"s_cmp_ge_u32 s50, s53\n"
"s_cbranch_scc1 .LO_end\n"
"s_cmp_ge_u32 s50, s52\n"
"s_cbranch_scc1 .LO_3r\n"
"s_sub_u32 s60, s50, 0x0\n"
"s_mov_b32 s61, 200000\n"
"s_nop 7\n"
".LO_3p:\n"
"buffer_load_dwordx2 v[124:125], v9, s[40:43], s60 offen sc1\n"
"s_waitcnt vmcnt(0)\n"
"v_cmp_ne_u64 vcc, s[48:49], v[124:125]\n"
"s_cbranch_vccz .LO_3px\n"
"s_sleep 1\n"
"s_sub_u32 s61, s61, 1\n"
"s_cbranch_scc0 .LO_3p\n"
"s_branch .LO_end\n"
".LO_3px:\n"
"s_branch .LO_3r\n"
".LO_4s:\n"
"s_cmp_ge_u32 s50, s53\n"
"s_cbranch_scc1 .LO_end\n"
"s_cmp_ge_u32 s50, s52\n"
"s_cbranch_scc1 .LO_4r\n"
"s_sub_u32 s60, s50, 0x0\n"
"s_mov_b32 s61, 200000\n"
"s_nop 7\n"
".LO_4p:\n"
"buffer_load_dwordx2 v[120:121], v9, s[40:43], s60 offen sc1\n"
"s_waitcnt vmcnt(0)\n"
"v_cmp_ne_u64 vcc, s[48:49], v[120:121]\n"
"s_cbranch_vccz .LO_4px\n"
"s_sleep 1\n"
"s_sub_u32 s61, s61, 1\n"
"s_cbranch_scc0 .LO_4p\n"
"s_branch .LO_end\n"
".LO_4px:\n"
"s_branch .LO_4r\n"
".LO_end:\n"
"s_waitcnt vmcnt(0) lgkmcnt(0)\n"
"s_endpgm\n"
      :: "{v2}"((unsigned)(lds0 + ((wid + 3) & 3) * 18592 + trp)), "{v3}"((unsigned)(lds0 + ((wid + 2) & 3) * 18592 + trp)), "{v4}"((unsigned)(lds0 + ((wid + 1) & 3) * 18592 + trp)), "{v5}"((unsigned)(lane * 16 + wid * 8192 + ((0 + rot) % 4) * 2048 + 134250496)), "{v6}"((unsigned)(lane * 16 + wid * 8192 + ((1 + rot) % 4) * 2048 + 134250496)), "{v7}"((unsigned)(lane * 16 + wid * 8192 + ((2 + rot) % 4) * 2048 + 134250496)), "{v8}"((unsigned)(lane * 16 + wid * 8192 + ((3 + rot) % 4) * 2048 + 134250496)), "{v9}"((unsigned)((wid * 8 + (lane & 7)) * 8 + 352512 + O_GL * 1572864)), "{v10}"((unsigned)(lane * 16 + pair * 1024 + 134217728)), "{v11}"((unsigned)(lane * 16 + pair * 1024 + 65536)), "{v12}"((unsigned)(lane ? DROP : pair * 8 + 352768)), "{v13}"((unsigned)(lg == (li >> 2) ? pair * 128 + li * 8 + 344064 : DROP)), "{v14}"((unsigned)(0x3f803f80)), "{v15}"((unsigned)(0x3f803f80)), "{v16}"((unsigned)(0x3f803f80)), "{v17}"((unsigned)(0x3f803f80)), "{v20}"((unsigned)(vT + tsw)), "{v21}"((unsigned)(vT + (tsw ^ 8))), "{v22}"((unsigned)(vD + ((0 + rot) % 4) * 131072)), "{v23}"((unsigned)(vD + ((1 + rot) % 4) * 131072)), "{v24}"((unsigned)(vD + ((2 + rot) % 4) * 131072)), "{v25}"((unsigned)(vD + ((3 + rot) % 4) * 131072)), "{s[40:43]}"(rS), "{s[44:47]}"(rW), "{s[48:49]}"(wsp), "{s50}"((unsigned)(sA)), "{s51}"((unsigned)(sLo)), "{s52}"((unsigned)(sHi)), "{s53}"((unsigned)(sEx)), "{s54}"((unsigned)(sEnt)), "{s55}"((unsigned)(sLd)), "{s[56:57]}"(wsb + (sLo & 0x3ffffff)), "{s59}"((unsigned)(lds0 + wid * 18592)), "{s[62:63]}"(0xAAAAAAAAAAAAAAAAull), "{s[64:65]}"(0xCCCCCCCCCCCCCCCCull), "{s66}"((unsigned)(sW0 + 64 * ((wid + 1) & 3))), "{s67}"((unsigned)(sW0 + 64 * ((wid + 2) & 3))), "{s68}"((unsigned)(sW0 + 64 * ((wid + 3) & 3))), "{s69}"((unsigned)(sW0 + 64 * ((wid + 4) & 3)))
      : "v255", "a255", "s95", "vcc", "scc", "memory");

  } else if (role == 2) {
    const unsigned sEx = sHi + F_EX * 3 * RS, sEnt = -wid & 3, tsw = 0, pair = idx * 4 + wid, rot = SLP & 4 ? FR0 * (xo >= 0) : KR * (set ? set & 2 : 1);
    const unsigned sW0 = wid * 256 * 7168 + idx * 256, vD = (lane >> 2) * 7168 + (lane & 3) * 16;
    const u32x4 rW = desc(a.w1, 1024 * 7168);
    asm volatile(
"s_nop 4\n"
"v_mov_b32 v186, 0\n"
"global_load_dwordx2 v[0:1], v186, s[48:49] sc1\n"
"global_load_dwordx4 v[30:33], v38, s[64:65]\n"
"v_mov_b32 v188, 0x3f803f80\n"
"v_mov_b32 v189, 0x3f803f80\n"
"v_mov_b32 v190, 0x3f803f80\n"
"v_mov_b32 v191, 0x3f803f80\n"
"s_add_u32 m0, s55, 0\n"
"s_add_u32 s60, s66, 0\n"
"buffer_load_dwordx4 v39, s[44:47], s60 offen lds\n"
"s_add_u32 m0, s55, 1024\n"
"s_add_u32 s60, s66, 114688\n"
"buffer_load_dwordx4 v39, s[44:47], s60 offen lds\n"
"s_add_u32 m0, s55, 2048\n"
"s_add_u32 s60, s67, 0\n"
"buffer_load_dwordx4 v39, s[44:47], s60 offen lds\n"
"s_add_u32 m0, s55, 3072\n"
"s_add_u32 s60, s67, 114688\n"
"buffer_load_dwordx4 v39, s[44:47], s60 offen lds\n"
"s_add_u32 m0, s55, 4096\n"
"s_add_u32 s60, s68, 0\n"
"buffer_load_dwordx4 v39, s[44:47], s60 offen lds\n"
"s_add_u32 m0, s55, 5120\n"
"s_add_u32 s60, s68, 114688\n"
"buffer_load_dwordx4 v39, s[44:47], s60 offen lds\n"
"s_add_u32 m0, s55, 6144\n"
"s_add_u32 s60, s69, 0\n"
"buffer_load_dwordx4 v39, s[44:47], s60 offen lds\n"
"s_add_u32 m0, s55, 7168\n"
"s_add_u32 s60, s69, 114688\n"
"buffer_load_dwordx4 v39, s[44:47], s60 offen lds\n"
"s_add_u32 m0, s55, 8192\n"
"s_add_u32 s60, s66, 229376\n"
"buffer_load_dwordx4 v39, s[44:47], s60 offen lds\n"
"s_add_u32 m0, s55, 9216\n"
"s_add_u32 s60, s66, 344064\n"
"buffer_load_dwordx4 v39, s[44:47], s60 offen lds\n"
"s_add_u32 m0, s55, 10240\n"
"s_add_u32 s60, s67, 229376\n"
"buffer_load_dwordx4 v39, s[44:47], s60 offen lds\n"
"s_add_u32 m0, s55, 11264\n"
"s_add_u32 s60, s67, 344064\n"
"buffer_load_dwordx4 v39, s[44:47], s60 offen lds\n"
"s_add_u32 m0, s55, 12288\n"
"s_add_u32 s60, s68, 229376\n"
"buffer_load_dwordx4 v39, s[44:47], s60 offen lds\n"
"s_add_u32 m0, s55, 13312\n"
"s_add_u32 s60, s68, 344064\n"
"buffer_load_dwordx4 v39, s[44:47], s60 offen lds\n"
"s_add_u32 m0, s55, 14336\n"
"s_add_u32 s60, s69, 229376\n"
"buffer_load_dwordx4 v39, s[44:47], s60 offen lds\n"
"s_add_u32 m0, s55, 15360\n"
"s_add_u32 s60, s69, 344064\n"
"buffer_load_dwordx4 v39, s[44:47], s60 offen lds\n"
"s_add_u32 m0, s55, 16384\n"
"s_add_u32 s60, s66, 0\n"
"buffer_load_dwordx4 v40, s[44:47], s60 offen lds\n"
"s_add_u32 m0, s55, 17408\n"
"s_add_u32 s60, s66, 114688\n"
"buffer_load_dwordx4 v40, s[44:47], s60 offen lds\n"
"s_add_u32 m0, s55, 18432\n"
"s_add_u32 s60, s67, 0\n"
"buffer_load_dwordx4 v40, s[44:47], s60 offen lds\n"
"s_add_u32 m0, s55, 19456\n"
"s_add_u32 s60, s67, 114688\n"
"buffer_load_dwordx4 v40, s[44:47], s60 offen lds\n"
"s_add_u32 m0, s55, 20480\n"
"s_add_u32 s60, s68, 0\n"
"buffer_load_dwordx4 v40, s[44:47], s60 offen lds\n"
"s_add_u32 m0, s55, 21504\n"
"s_add_u32 s60, s68, 114688\n"
"buffer_load_dwordx4 v40, s[44:47], s60 offen lds\n"
"s_add_u32 m0, s55, 22528\n"
"s_add_u32 s60, s69, 0\n"
"buffer_load_dwordx4 v40, s[44:47], s60 offen lds\n"
"s_add_u32 m0, s55, 23552\n"
"s_add_u32 s60, s69, 114688\n"
"buffer_load_dwordx4 v40, s[44:47], s60 offen lds\n"
"s_add_u32 m0, s55, 24576\n"
"s_add_u32 s60, s66, 229376\n"
"buffer_load_dwordx4 v40, s[44:47], s60 offen lds\n"
"s_add_u32 m0, s55, 25600\n"
"s_add_u32 s60, s66, 344064\n"
"buffer_load_dwordx4 v40, s[44:47], s60 offen lds\n"
"s_add_u32 m0, s55, 26624\n"
"s_add_u32 s60, s67, 229376\n"
"buffer_load_dwordx4 v40, s[44:47], s60 offen lds\n"
"s_add_u32 m0, s55, 27648\n"
"s_add_u32 s60, s67, 344064\n"
"buffer_load_dwordx4 v40, s[44:47], s60 offen lds\n"
"s_add_u32 m0, s55, 28672\n"
"s_add_u32 s60, s68, 229376\n"
"buffer_load_dwordx4 v40, s[44:47], s60 offen lds\n"
"s_add_u32 m0, s55, 29696\n"
"s_add_u32 s60, s68, 344064\n"
"buffer_load_dwordx4 v40, s[44:47], s60 offen lds\n"
"s_add_u32 m0, s55, 30720\n"
"s_add_u32 s60, s69, 229376\n"
"buffer_load_dwordx4 v40, s[44:47], s60 offen lds\n"
"s_add_u32 m0, s55, 31744\n"
"s_add_u32 s60, s69, 344064\n"
"buffer_load_dwordx4 v40, s[44:47], s60 offen lds\n"
"s_add_u32 m0, s55, 32768\n"
"s_add_u32 s60, s66, 0\n"
"buffer_load_dwordx4 v41, s[44:47], s60 offen lds\n"
"s_add_u32 m0, s55, 33792\n"
"s_add_u32 s60, s66, 114688\n"
"buffer_load_dwordx4 v41, s[44:47], s60 offen lds\n"
"s_add_u32 m0, s55, 34816\n"
"s_add_u32 s60, s67, 0\n"
"buffer_load_dwordx4 v41, s[44:47], s60 offen lds\n"
"s_add_u32 m0, s55, 35840\n"
"s_add_u32 s60, s67, 114688\n"
"buffer_load_dwordx4 v41, s[44:47], s60 offen lds\n"
"s_add_u32 m0, s55, 36864\n"
"s_add_u32 s60, s68, 0\n"
"buffer_load_dwordx4 v41, s[44:47], s60 offen lds\n"
"s_add_u32 m0, s55, 37888\n"
"s_add_u32 s60, s68, 114688\n"
"buffer_load_dwordx4 v41, s[44:47], s60 offen lds\n"
"s_add_u32 m0, s55, 38912\n"
"s_add_u32 s60, s69, 0\n"
"buffer_load_dwordx4 v41, s[44:47], s60 offen lds\n"
"s_add_u32 m0, s55, 39936\n"
"s_add_u32 s60, s69, 114688\n"
"buffer_load_dwordx4 v41, s[44:47], s60 offen lds\n"
"s_waitcnt vmcnt(38)\n"
"ds_read_b64_tr_b16 a[0:1], v36 offset:0\n"
"ds_read_b64_tr_b16 a[2:3], v36 offset:256\n"
"ds_read_b64_tr_b16 a[32:33], v37 offset:0\n"
"ds_read_b64_tr_b16 a[34:35], v37 offset:256\n"
"v_add_u32 v0, 2, v0\n"
"v_add_u32 v1, 0x9e3779b9, v1\n"
"v_or_b32 v0, 1, v0\n"
"v_readfirstlane_b32 s49, v1\n"
"v_readfirstlane_b32 s48, v0\n"
"s_waitcnt vmcnt(36)\n"
"ds_read_b64_tr_b16 a[64:65], v36 offset:2048\n"
"ds_read_b64_tr_b16 a[66:67], v36 offset:2304\n"
"ds_read_b64_tr_b16 a[96:97], v37 offset:2048\n"
"ds_read_b64_tr_b16 a[98:99], v37 offset:2304\n"
"s_waitcnt lgkmcnt(4)\n"
"s_add_u32 m0, s55, 0\n"
"s_add_u32 s60, s66, 229376\n"
"buffer_load_dwordx4 v41, s[44:47], s60 offen lds\n"
"s_add_u32 m0, s55, 1024\n"
"s_add_u32 s60, s66, 344064\n"
"buffer_load_dwordx4 v41, s[44:47], s60 offen lds\n"
"v_mfma_f32_16x16x32_bf16 v[192:195], v[188:191], a[0:3], 0\n"
"v_mfma_f32_16x16x32_bf16 v[196:199], v[188:191], a[32:35], 0\n"
"s_waitcnt vmcnt(36)\n"
"ds_read_b64_tr_b16 a[128:129], v36 offset:4096\n"
"ds_read_b64_tr_b16 a[130:131], v36 offset:4352\n"
"ds_read_b64_tr_b16 a[160:161], v37 offset:4096\n"
"ds_read_b64_tr_b16 a[162:163], v37 offset:4352\n"
"s_waitcnt lgkmcnt(4)\n"
"s_add_u32 m0, s55, 2048\n"
"s_add_u32 s60, s67, 229376\n"
"buffer_load_dwordx4 v41, s[44:47], s60 offen lds\n"
"s_add_u32 m0, s55, 3072\n"
"s_add_u32 s60, s67, 344064\n"
"buffer_load_dwordx4 v41, s[44:47], s60 offen lds\n"
"v_mfma_f32_16x16x32_bf16 v[200:203], v[188:191], a[64:67], 0\n"
"v_mfma_f32_16x16x32_bf16 v[204:207], v[188:191], a[96:99], 0\n"
"s_waitcnt vmcnt(36)\n"
"ds_read_b64_tr_b16 a[192:193], v36 offset:6144\n"
"ds_read_b64_tr_b16 a[194:195], v36 offset:6400\n"
"ds_read_b64_tr_b16 a[224:225], v37 offset:6144\n"
"ds_read_b64_tr_b16 a[226:227], v37 offset:6400\n"
"s_waitcnt lgkmcnt(4)\n"
"s_add_u32 m0, s55, 4096\n"
"s_add_u32 s60, s68, 229376\n"
"buffer_load_dwordx4 v41, s[44:47], s60 offen lds\n"
"s_add_u32 m0, s55, 5120\n"
"s_add_u32 s60, s68, 344064\n"
"buffer_load_dwordx4 v41, s[44:47], s60 offen lds\n"
"v_mfma_f32_16x16x32_bf16 v[208:211], v[188:191], a[128:131], 0\n"
"v_mfma_f32_16x16x32_bf16 v[212:215], v[188:191], a[160:163], 0\n"
"s_waitcnt vmcnt(36)\n"
"ds_read_b64_tr_b16 a[4:5], v36 offset:8192\n"
"ds_read_b64_tr_b16 a[6:7], v36 offset:8448\n"
"ds_read_b64_tr_b16 a[36:37], v37 offset:8192\n"
"ds_read_b64_tr_b16 a[38:39], v37 offset:8448\n"
"s_waitcnt lgkmcnt(4)\n"
"s_add_u32 m0, s55, 6144\n"
"s_add_u32 s60, s69, 229376\n"
"buffer_load_dwordx4 v41, s[44:47], s60 offen lds\n"
"s_add_u32 m0, s55, 7168\n"
"s_add_u32 s60, s69, 344064\n"
"buffer_load_dwordx4 v41, s[44:47], s60 offen lds\n"
"v_mfma_f32_16x16x32_bf16 v[216:219], v[188:191], a[192:195], 0\n"
"v_mfma_f32_16x16x32_bf16 v[220:223], v[188:191], a[224:227], 0\n"
"s_waitcnt vmcnt(36)\n"
"ds_read_b64_tr_b16 a[68:69], v36 offset:10240\n"
"ds_read_b64_tr_b16 a[70:71], v36 offset:10496\n"
"ds_read_b64_tr_b16 a[100:101], v37 offset:10240\n"
"ds_read_b64_tr_b16 a[102:103], v37 offset:10496\n"
"s_waitcnt lgkmcnt(4)\n"
"s_add_u32 m0, s55, 8192\n"
"s_add_u32 s60, s66, 0\n"
"buffer_load_dwordx4 v42, s[44:47], s60 offen lds\n"
"s_add_u32 m0, s55, 9216\n"
"s_add_u32 s60, s66, 114688\n"
"buffer_load_dwordx4 v42, s[44:47], s60 offen lds\n"
"v_mfma_f32_16x16x32_bf16 v[192:195], v[188:191], a[4:7], v[192:195]\n"
"v_mfma_f32_16x16x32_bf16 v[196:199], v[188:191], a[36:39], v[196:199]\n"
"s_waitcnt vmcnt(36)\n"
"ds_read_b64_tr_b16 a[132:133], v36 offset:12288\n"
"ds_read_b64_tr_b16 a[134:135], v36 offset:12544\n"
"ds_read_b64_tr_b16 a[164:165], v37 offset:12288\n"
"ds_read_b64_tr_b16 a[166:167], v37 offset:12544\n"
"s_waitcnt lgkmcnt(4)\n"
"s_add_u32 m0, s55, 10240\n"
"s_add_u32 s60, s67, 0\n"
"buffer_load_dwordx4 v42, s[44:47], s60 offen lds\n"
"s_add_u32 m0, s55, 11264\n"
"s_add_u32 s60, s67, 114688\n"
"buffer_load_dwordx4 v42, s[44:47], s60 offen lds\n"
"v_mfma_f32_16x16x32_bf16 v[200:203], v[188:191], a[68:71], v[200:203]\n"
"v_mfma_f32_16x16x32_bf16 v[204:207], v[188:191], a[100:103], v[204:207]\n"
"s_waitcnt vmcnt(36)\n"
"ds_read_b64_tr_b16 a[196:197], v36 offset:14336\n"
"ds_read_b64_tr_b16 a[198:199], v36 offset:14592\n"
"ds_read_b64_tr_b16 a[228:229], v37 offset:14336\n"
"ds_read_b64_tr_b16 a[230:231], v37 offset:14592\n"
"s_waitcnt lgkmcnt(4)\n"
"s_add_u32 m0, s55, 12288\n"
"s_add_u32 s60, s68, 0\n"
"buffer_load_dwordx4 v42, s[44:47], s60 offen lds\n"
"s_add_u32 m0, s55, 13312\n"
"s_add_u32 s60, s68, 114688\n"
"buffer_load_dwordx4 v42, s[44:47], s60 offen lds\n"
"v_mfma_f32_16x16x32_bf16 v[208:211], v[188:191], a[132:135], v[208:211]\n"
"v_mfma_f32_16x16x32_bf16 v[212:215], v[188:191], a[164:167], v[212:215]\n"
"s_waitcnt vmcnt(36)\n"
"ds_read_b64_tr_b16 a[8:9], v36 offset:16384\n"
"ds_read_b64_tr_b16 a[10:11], v36 offset:16640\n"
"ds_read_b64_tr_b16 a[40:41], v37 offset:16384\n"
"ds_read_b64_tr_b16 a[42:43], v37 offset:16640\n"
"s_waitcnt lgkmcnt(4)\n"
"s_add_u32 m0, s55, 14336\n"
"s_add_u32 s60, s69, 0\n"
"buffer_load_dwordx4 v42, s[44:47], s60 offen lds\n"
"s_add_u32 m0, s55, 15360\n"
"s_add_u32 s60, s69, 114688\n"
"buffer_load_dwordx4 v42, s[44:47], s60 offen lds\n"
"v_mfma_f32_16x16x32_bf16 v[216:219], v[188:191], a[196:199], v[216:219]\n"
"v_mfma_f32_16x16x32_bf16 v[220:223], v[188:191], a[228:231], v[220:223]\n"
"s_waitcnt vmcnt(36)\n"
"ds_read_b64_tr_b16 a[72:73], v36 offset:18432\n"
"ds_read_b64_tr_b16 a[74:75], v36 offset:18688\n"
"ds_read_b64_tr_b16 a[104:105], v37 offset:18432\n"
"ds_read_b64_tr_b16 a[106:107], v37 offset:18688\n"
"s_waitcnt lgkmcnt(4)\n"
"s_add_u32 m0, s55, 16384\n"
"s_add_u32 s60, s66, 229376\n"
"buffer_load_dwordx4 v42, s[44:47], s60 offen lds\n"
"s_add_u32 m0, s55, 17408\n"
"s_add_u32 s60, s66, 344064\n"
"buffer_load_dwordx4 v42, s[44:47], s60 offen lds\n"
"v_mfma_f32_16x16x32_bf16 v[192:195], v[188:191], a[8:11], v[192:195]\n"
"v_mfma_f32_16x16x32_bf16 v[196:199], v[188:191], a[40:43], v[196:199]\n"
"s_waitcnt vmcnt(36)\n"
"ds_read_b64_tr_b16 a[136:137], v36 offset:20480\n"
"ds_read_b64_tr_b16 a[138:139], v36 offset:20736\n"
"ds_read_b64_tr_b16 a[168:169], v37 offset:20480\n"
"ds_read_b64_tr_b16 a[170:171], v37 offset:20736\n"
"s_waitcnt lgkmcnt(4)\n"
"s_add_u32 m0, s55, 18432\n"
"s_add_u32 s60, s67, 229376\n"
"buffer_load_dwordx4 v42, s[44:47], s60 offen lds\n"
"s_add_u32 m0, s55, 19456\n"
"s_add_u32 s60, s67, 344064\n"
"buffer_load_dwordx4 v42, s[44:47], s60 offen lds\n"
"v_mfma_f32_16x16x32_bf16 v[200:203], v[188:191], a[72:75], v[200:203]\n"
"v_mfma_f32_16x16x32_bf16 v[204:207], v[188:191], a[104:107], v[204:207]\n"
"s_waitcnt vmcnt(36)\n"
"ds_read_b64_tr_b16 a[200:201], v36 offset:22528\n"
"ds_read_b64_tr_b16 a[202:203], v36 offset:22784\n"
"ds_read_b64_tr_b16 a[232:233], v37 offset:22528\n"
"ds_read_b64_tr_b16 a[234:235], v37 offset:22784\n"
"s_waitcnt lgkmcnt(4)\n"
"s_add_u32 m0, s55, 20480\n"
"s_add_u32 s60, s68, 229376\n"
"buffer_load_dwordx4 v42, s[44:47], s60 offen lds\n"
"s_add_u32 m0, s55, 21504\n"
"s_add_u32 s60, s68, 344064\n"
"buffer_load_dwordx4 v42, s[44:47], s60 offen lds\n"
"v_mfma_f32_16x16x32_bf16 v[208:211], v[188:191], a[136:139], v[208:211]\n"
"v_mfma_f32_16x16x32_bf16 v[212:215], v[188:191], a[168:171], v[212:215]\n"
"s_waitcnt vmcnt(36)\n"
"ds_read_b64_tr_b16 a[12:13], v36 offset:24576\n"
"ds_read_b64_tr_b16 a[14:15], v36 offset:24832\n"
"ds_read_b64_tr_b16 a[44:45], v37 offset:24576\n"
"ds_read_b64_tr_b16 a[46:47], v37 offset:24832\n"
"s_waitcnt lgkmcnt(4)\n"
"s_add_u32 m0, s55, 22528\n"
"s_add_u32 s60, s69, 229376\n"
"buffer_load_dwordx4 v42, s[44:47], s60 offen lds\n"
"s_add_u32 m0, s55, 23552\n"
"s_add_u32 s60, s69, 344064\n"
"buffer_load_dwordx4 v42, s[44:47], s60 offen lds\n"
"v_mfma_f32_16x16x32_bf16 v[216:219], v[188:191], a[200:203], v[216:219]\n"
"v_mfma_f32_16x16x32_bf16 v[220:223], v[188:191], a[232:235], v[220:223]\n"
"s_waitcnt vmcnt(36)\n"
"ds_read_b64_tr_b16 a[76:77], v36 offset:26624\n"
"ds_read_b64_tr_b16 a[78:79], v36 offset:26880\n"
"ds_read_b64_tr_b16 a[108:109], v37 offset:26624\n"
"ds_read_b64_tr_b16 a[110:111], v37 offset:26880\n"
"s_waitcnt lgkmcnt(4)\n"
"v_mfma_f32_16x16x32_bf16 v[192:195], v[188:191], a[12:15], v[192:195]\n"
"v_mfma_f32_16x16x32_bf16 v[196:199], v[188:191], a[44:47], v[196:199]\n"
"s_waitcnt vmcnt(34)\n"
"ds_read_b64_tr_b16 a[140:141], v36 offset:28672\n"
"ds_read_b64_tr_b16 a[142:143], v36 offset:28928\n"
"ds_read_b64_tr_b16 a[172:173], v37 offset:28672\n"
"ds_read_b64_tr_b16 a[174:175], v37 offset:28928\n"
"s_waitcnt lgkmcnt(4)\n"
"v_mfma_f32_16x16x32_bf16 v[200:203], v[188:191], a[76:79], v[200:203]\n"
"v_mfma_f32_16x16x32_bf16 v[204:207], v[188:191], a[108:111], v[204:207]\n"
"s_waitcnt vmcnt(32)\n"
"ds_read_b64_tr_b16 a[204:205], v36 offset:30720\n"
"ds_read_b64_tr_b16 a[206:207], v36 offset:30976\n"
"ds_read_b64_tr_b16 a[236:237], v37 offset:30720\n"
"ds_read_b64_tr_b16 a[238:239], v37 offset:30976\n"
"s_waitcnt lgkmcnt(4)\n"
"v_mfma_f32_16x16x32_bf16 v[208:211], v[188:191], a[140:143], v[208:211]\n"
"v_mfma_f32_16x16x32_bf16 v[212:215], v[188:191], a[172:175], v[212:215]\n"
"s_waitcnt vmcnt(30)\n"
"ds_read_b64_tr_b16 a[16:17], v36 offset:32768\n"
"ds_read_b64_tr_b16 a[18:19], v36 offset:33024\n"
"ds_read_b64_tr_b16 a[48:49], v37 offset:32768\n"
"ds_read_b64_tr_b16 a[50:51], v37 offset:33024\n"
"s_waitcnt lgkmcnt(4)\n"
"v_mfma_f32_16x16x32_bf16 v[216:219], v[188:191], a[204:207], v[216:219]\n"
"v_mfma_f32_16x16x32_bf16 v[220:223], v[188:191], a[236:239], v[220:223]\n"
"s_waitcnt vmcnt(28)\n"
"ds_read_b64_tr_b16 a[80:81], v36 offset:34816\n"
"ds_read_b64_tr_b16 a[82:83], v36 offset:35072\n"
"ds_read_b64_tr_b16 a[112:113], v37 offset:34816\n"
"ds_read_b64_tr_b16 a[114:115], v37 offset:35072\n"
"s_waitcnt lgkmcnt(4)\n"
"v_mfma_f32_16x16x32_bf16 v[192:195], v[188:191], a[16:19], v[192:195]\n"
"v_mfma_f32_16x16x32_bf16 v[196:199], v[188:191], a[48:51], v[196:199]\n"
"s_waitcnt vmcnt(26)\n"
"ds_read_b64_tr_b16 a[144:145], v36 offset:36864\n"
"ds_read_b64_tr_b16 a[146:147], v36 offset:37120\n"
"ds_read_b64_tr_b16 a[176:177], v37 offset:36864\n"
"ds_read_b64_tr_b16 a[178:179], v37 offset:37120\n"
"s_waitcnt lgkmcnt(4)\n"
"v_mfma_f32_16x16x32_bf16 v[200:203], v[188:191], a[80:83], v[200:203]\n"
"v_mfma_f32_16x16x32_bf16 v[204:207], v[188:191], a[112:115], v[204:207]\n"
"s_waitcnt vmcnt(24)\n"
"ds_read_b64_tr_b16 a[208:209], v36 offset:38912\n"
"ds_read_b64_tr_b16 a[210:211], v36 offset:39168\n"
"ds_read_b64_tr_b16 a[240:241], v37 offset:38912\n"
"ds_read_b64_tr_b16 a[242:243], v37 offset:39168\n"
"s_waitcnt lgkmcnt(4)\n"
"v_mfma_f32_16x16x32_bf16 v[208:211], v[188:191], a[144:147], v[208:211]\n"
"v_mfma_f32_16x16x32_bf16 v[212:215], v[188:191], a[176:179], v[212:215]\n"
"s_waitcnt vmcnt(22)\n"
"ds_read_b64_tr_b16 a[20:21], v36 offset:0\n"
"ds_read_b64_tr_b16 a[22:23], v36 offset:256\n"
"ds_read_b64_tr_b16 a[52:53], v37 offset:0\n"
"ds_read_b64_tr_b16 a[54:55], v37 offset:256\n"
"s_waitcnt lgkmcnt(4)\n"
"v_mfma_f32_16x16x32_bf16 v[216:219], v[188:191], a[208:211], v[216:219]\n"
"v_mfma_f32_16x16x32_bf16 v[220:223], v[188:191], a[240:243], v[220:223]\n"
"s_waitcnt vmcnt(20)\n"
"ds_read_b64_tr_b16 a[84:85], v36 offset:2048\n"
"ds_read_b64_tr_b16 a[86:87], v36 offset:2304\n"
"ds_read_b64_tr_b16 a[116:117], v37 offset:2048\n"
"ds_read_b64_tr_b16 a[118:119], v37 offset:2304\n"
"s_waitcnt lgkmcnt(4)\n"
"v_mfma_f32_16x16x32_bf16 v[192:195], v[188:191], a[20:23], v[192:195]\n"
"v_mfma_f32_16x16x32_bf16 v[196:199], v[188:191], a[52:55], v[196:199]\n"
"s_waitcnt vmcnt(18)\n"
"ds_read_b64_tr_b16 a[148:149], v36 offset:4096\n"
"ds_read_b64_tr_b16 a[150:151], v36 offset:4352\n"
"ds_read_b64_tr_b16 a[180:181], v37 offset:4096\n"
"ds_read_b64_tr_b16 a[182:183], v37 offset:4352\n"
"s_waitcnt lgkmcnt(4)\n"
"v_mfma_f32_16x16x32_bf16 v[200:203], v[188:191], a[84:87], v[200:203]\n"
"v_mfma_f32_16x16x32_bf16 v[204:207], v[188:191], a[116:119], v[204:207]\n"
"s_waitcnt vmcnt(16)\n"
"ds_read_b64_tr_b16 a[212:213], v36 offset:6144\n"
"ds_read_b64_tr_b16 a[214:215], v36 offset:6400\n"
"ds_read_b64_tr_b16 a[244:245], v37 offset:6144\n"
"ds_read_b64_tr_b16 a[246:247], v37 offset:6400\n"
"s_waitcnt lgkmcnt(4)\n"
"v_mfma_f32_16x16x32_bf16 v[208:211], v[188:191], a[148:151], v[208:211]\n"
"v_mfma_f32_16x16x32_bf16 v[212:215], v[188:191], a[180:183], v[212:215]\n"
"s_waitcnt vmcnt(14)\n"
"ds_read_b64_tr_b16 a[24:25], v36 offset:8192\n"
"ds_read_b64_tr_b16 a[26:27], v36 offset:8448\n"
"ds_read_b64_tr_b16 a[56:57], v37 offset:8192\n"
"ds_read_b64_tr_b16 a[58:59], v37 offset:8448\n"
"s_waitcnt lgkmcnt(4)\n"
"v_mfma_f32_16x16x32_bf16 v[216:219], v[188:191], a[212:215], v[216:219]\n"
"v_mfma_f32_16x16x32_bf16 v[220:223], v[188:191], a[244:247], v[220:223]\n"
"s_waitcnt vmcnt(12)\n"
"ds_read_b64_tr_b16 a[88:89], v36 offset:10240\n"
"ds_read_b64_tr_b16 a[90:91], v36 offset:10496\n"
"ds_read_b64_tr_b16 a[120:121], v37 offset:10240\n"
"ds_read_b64_tr_b16 a[122:123], v37 offset:10496\n"
"s_waitcnt lgkmcnt(4)\n"
"v_mfma_f32_16x16x32_bf16 v[192:195], v[188:191], a[24:27], v[192:195]\n"
"v_mfma_f32_16x16x32_bf16 v[196:199], v[188:191], a[56:59], v[196:199]\n"
"s_waitcnt vmcnt(10)\n"
"ds_read_b64_tr_b16 a[152:153], v36 offset:12288\n"
"ds_read_b64_tr_b16 a[154:155], v36 offset:12544\n"
"ds_read_b64_tr_b16 a[184:185], v37 offset:12288\n"
"ds_read_b64_tr_b16 a[186:187], v37 offset:12544\n"
"s_waitcnt lgkmcnt(4)\n"
"v_mfma_f32_16x16x32_bf16 v[200:203], v[188:191], a[88:91], v[200:203]\n"
"v_mfma_f32_16x16x32_bf16 v[204:207], v[188:191], a[120:123], v[204:207]\n"
"s_waitcnt vmcnt(8)\n"
"ds_read_b64_tr_b16 a[216:217], v36 offset:14336\n"
"ds_read_b64_tr_b16 a[218:219], v36 offset:14592\n"
"ds_read_b64_tr_b16 a[248:249], v37 offset:14336\n"
"ds_read_b64_tr_b16 a[250:251], v37 offset:14592\n"
"s_waitcnt lgkmcnt(4)\n"
"v_mfma_f32_16x16x32_bf16 v[208:211], v[188:191], a[152:155], v[208:211]\n"
"v_mfma_f32_16x16x32_bf16 v[212:215], v[188:191], a[184:187], v[212:215]\n"
"s_waitcnt vmcnt(6)\n"
"ds_read_b64_tr_b16 a[28:29], v36 offset:16384\n"
"ds_read_b64_tr_b16 a[30:31], v36 offset:16640\n"
"ds_read_b64_tr_b16 a[60:61], v37 offset:16384\n"
"ds_read_b64_tr_b16 a[62:63], v37 offset:16640\n"
"s_waitcnt lgkmcnt(4)\n"
"v_mfma_f32_16x16x32_bf16 v[216:219], v[188:191], a[216:219], v[216:219]\n"
"v_mfma_f32_16x16x32_bf16 v[220:223], v[188:191], a[248:251], v[220:223]\n"
"s_waitcnt vmcnt(4)\n"
"ds_read_b64_tr_b16 a[92:93], v36 offset:18432\n"
"ds_read_b64_tr_b16 a[94:95], v36 offset:18688\n"
"ds_read_b64_tr_b16 a[124:125], v37 offset:18432\n"
"ds_read_b64_tr_b16 a[126:127], v37 offset:18688\n"
"s_waitcnt lgkmcnt(4)\n"
"v_mfma_f32_16x16x32_bf16 v[192:195], v[188:191], a[28:31], v[192:195]\n"
"v_mfma_f32_16x16x32_bf16 v[196:199], v[188:191], a[60:63], v[196:199]\n"
"s_waitcnt vmcnt(2)\n"
"ds_read_b64_tr_b16 a[156:157], v36 offset:20480\n"
"ds_read_b64_tr_b16 a[158:159], v36 offset:20736\n"
"ds_read_b64_tr_b16 a[188:189], v37 offset:20480\n"
"ds_read_b64_tr_b16 a[190:191], v37 offset:20736\n"
"s_waitcnt lgkmcnt(4)\n"
"v_mfma_f32_16x16x32_bf16 v[200:203], v[188:191], a[92:95], v[200:203]\n"
"v_mfma_f32_16x16x32_bf16 v[204:207], v[188:191], a[124:127], v[204:207]\n"
"s_waitcnt vmcnt(0)\n"
"ds_read_b64_tr_b16 a[220:221], v36 offset:22528\n"
"ds_read_b64_tr_b16 a[222:223], v36 offset:22784\n"
"ds_read_b64_tr_b16 a[252:253], v37 offset:22528\n"
"ds_read_b64_tr_b16 a[254:255], v37 offset:22784\n"
"s_waitcnt lgkmcnt(4)\n"
"v_mfma_f32_16x16x32_bf16 v[208:211], v[188:191], a[156:159], v[208:211]\n"
"v_mfma_f32_16x16x32_bf16 v[212:215], v[188:191], a[188:191], v[212:215]\n"
"s_waitcnt lgkmcnt(0)\n"
"v_mfma_f32_16x16x32_bf16 v[216:219], v[188:191], a[220:223], v[216:219]\n"
"v_mfma_f32_16x16x32_bf16 v[220:223], v[188:191], a[252:255], v[220:223]\n"
"s_mov_b32 m0, s59\n"
"s_barrier\n"
"ds_write_addtid_b32 v192 offset:0\n"
"ds_write_addtid_b32 v193 offset:4640\n"
"ds_write_addtid_b32 v194 offset:9344\n"
"ds_write_addtid_b32 v195 offset:13984\n"
"ds_write_addtid_b32 v196 offset:256\n"
"ds_write_addtid_b32 v197 offset:4896\n"
"ds_write_addtid_b32 v198 offset:9600\n"
"ds_write_addtid_b32 v199 offset:14240\n"
"ds_write_addtid_b32 v200 offset:512\n"
"ds_write_addtid_b32 v201 offset:5152\n"
"ds_write_addtid_b32 v202 offset:9856\n"
"ds_write_addtid_b32 v203 offset:14496\n"
"ds_write_addtid_b32 v204 offset:768\n"
"ds_write_addtid_b32 v205 offset:5408\n"
"ds_write_addtid_b32 v206 offset:10112\n"
"ds_write_addtid_b32 v207 offset:14752\n"
"ds_write_addtid_b32 v208 offset:1024\n"
"ds_write_addtid_b32 v209 offset:5664\n"
"ds_write_addtid_b32 v210 offset:10368\n"
"ds_write_addtid_b32 v211 offset:15008\n"
"ds_write_addtid_b32 v212 offset:1280\n"
"ds_write_addtid_b32 v213 offset:5920\n"
"ds_write_addtid_b32 v214 offset:10624\n"
"ds_write_addtid_b32 v215 offset:15264\n"
"ds_write_addtid_b32 v216 offset:1536\n"
"ds_write_addtid_b32 v217 offset:6176\n"
"ds_write_addtid_b32 v218 offset:10880\n"
"ds_write_addtid_b32 v219 offset:15520\n"
"ds_write_addtid_b32 v220 offset:1792\n"
"ds_write_addtid_b32 v221 offset:6432\n"
"ds_write_addtid_b32 v222 offset:11136\n"
"ds_write_addtid_b32 v223 offset:15776\n"
"s_waitcnt lgkmcnt(0)\n"
"s_barrier\n"
"ds_read_b128 v[96:99], v2 offset:0\n"
"ds_read_b128 v[100:103], v3 offset:512\n"
"ds_read_b128 v[104:107], v4 offset:1024\n"
"ds_read_b128 v[108:111], v43 offset:1536\n"
"ds_read_b128 v[112:115], v2 offset:256\n"
"ds_read_b128 v[116:119], v3 offset:768\n"
"ds_read_b128 v[120:123], v4 offset:1280\n"
"ds_read_b128 v[124:127], v43 offset:1792\n"
"s_waitcnt lgkmcnt(0)\n"
"s_barrier\n"
"v_add_f32 v96, v96, v100\n"
"v_add_f32 v104, v104, v108\n"
"v_add_f32 v18, v96, v104\n"
"v_add_f32 v97, v97, v101\n"
"v_add_f32 v105, v105, v109\n"
"v_add_f32 v19, v97, v105\n"
"v_add_f32 v98, v98, v102\n"
"v_add_f32 v106, v106, v110\n"
"v_add_f32 v20, v98, v106\n"
"v_add_f32 v99, v99, v103\n"
"v_add_f32 v107, v107, v111\n"
"v_add_f32 v21, v99, v107\n"
"v_add_f32 v112, v112, v116\n"
"v_add_f32 v120, v120, v124\n"
"v_add_f32 v22, v112, v120\n"
"v_add_f32 v113, v113, v117\n"
"v_add_f32 v121, v121, v125\n"
"v_add_f32 v23, v113, v121\n"
"v_add_f32 v114, v114, v118\n"
"v_add_f32 v122, v122, v126\n"
"v_add_f32 v24, v114, v122\n"
"v_add_f32 v115, v115, v119\n"
"v_add_f32 v123, v123, v127\n"
"v_add_f32 v25, v115, v123\n"
"v_lshlrev_b32 v26, 16, v30\n"
"v_and_b32 v27, 0xffff0000, v30\n"
"v_lshlrev_b32 v28, 16, v31\n"
"v_and_b32 v29, 0xffff0000, v31\n"
"v_lshlrev_b32 v30, 16, v32\n"
"v_and_b32 v31, 0xffff0000, v32\n"
"v_lshlrev_b32 v32, 16, v33\n"
"v_and_b32 v33, 0xffff0000, v33\n"
"v_mov_b32 v134, 0\n"
"v_mov_b32 v135, 0\n"
"v_mov_b32 v154, 0\n"
"v_mov_b32 v155, 0\n"
"v_mov_b32 v144, 0\n"
"v_mov_b32 v145, 0\n"
"v_mov_b32 v138, 0\n"
"v_mov_b32 v139, 0\n"
"s_mov_b32 s93, 0x80000000\n"
"s_mov_b32 s94, 0x80000000\n"
"s_mov_b32 s95, 0x80000000\n"
"s_mov_b32 s94, 0x80000000\n"
"s_mov_b32 s93, 0x80000000\n"
"s_mov_b32 s94, 0x80000000\n"
"s_mov_b32 s95, 0x80000000\n"
"s_mov_b32 s94, 0x80000000\n"
"s_cmp_eq_u32 s54, 0\n"
"s_cbranch_scc1 .LF_1s\n"
"s_cmp_eq_u32 s54, 1\n"
"s_cbranch_scc1 .LF_2s\n"
"s_cmp_eq_u32 s54, 2\n"
"s_cbranch_scc1 .LF_3s\n"
"s_cmp_eq_u32 s54, 3\n"
"s_cbranch_scc1 .LF_4s\n"
".p2align 6\n"
".LF_top:\n"
"v_mfma_f32_16x16x32_bf16 v[48:51], v[100:103], a[0:3], 0\n"
"global_load_dwordx4 v[36:39], v6, s[56:57] offset:1024 sc1\n"
"v_mul_f32 v125, v125, v124\n"
"v_mfma_f32_16x16x32_bf16 v[48:51], v[104:107], a[4:7], v[48:51]\n"
"s_waitcnt vmcnt(10)\n"
"ds_write_addtid_b32 v124 offset:18592\n"
"ds_write_addtid_b32 v125 offset:18848\n"
"v_mfma_f32_16x16x32_bf16 v[48:51], v[108:111], a[8:11], v[48:51]\n"
"v_cvt_pk_bf16_f32 v139, v142, v143\n"
"v_pk_max_i16 v138, v138, 0\n"
"v_mfma_f32_16x16x32_bf16 v[48:51], v[64:67], a[12:15], v[48:51]\n"
"s_waitcnt vmcnt(8)\n"
"ds_read2st64_b32 v[140:141], v14 offset0:0 offset1:1\n"
"v_pk_max_i16 v139, v139, 0\n"
"v_mfma_f32_16x16x32_bf16 v[48:51], v[68:71], a[16:19], v[48:51]\n"
"ds_read_b128 v[56:59], v2 offset:0\n"
"ds_read_b128 v[52:55], v3 offset:2048\n"
"v_mfma_f32_16x16x32_bf16 v[48:51], v[72:75], a[20:23], v[48:51]\n"
"s_waitcnt vmcnt(6)\n"
"global_load_dwordx4 v[40:43], v7, s[56:57] offset:0 sc1\n"
"ds_read_b128 v[60:63], v4 offset:1024\n"
"v_mfma_f32_16x16x32_bf16 v[48:51], v[76:79], a[24:27], v[48:51]\n"
"ds_read_b128 v[84:87], v2 offset:256\n"
"ds_read_b128 v[88:91], v3 offset:2304\n"
"v_mfma_f32_16x16x32_bf16 v[48:51], v[80:83], a[28:31], v[48:51]\n"
"ds_read_b128 v[92:95], v4 offset:1280\n"
"v_mfma_f32_16x16x32_bf16 v[96:99], v[100:103], a[32:35], 0\n"
"v_mfma_f32_16x16x32_bf16 v[96:99], v[104:107], a[36:39], v[96:99]\n"
"ds_write_addtid_b32 v48 offset:0\n"
"v_mfma_f32_16x16x32_bf16 v[96:99], v[108:111], a[40:43], v[96:99]\n"
"ds_write_addtid_b32 v49 offset:4640\n"
"global_load_dwordx4 v[44:47], v7, s[56:57] offset:1024 sc1\n"
"v_mfma_f32_16x16x32_bf16 v[96:99], v[64:67], a[44:47], v[96:99]\n"
"s_waitcnt lgkmcnt(5)\n"
"buffer_store_dwordx4 v[136:139], v15, s[40:43], s94 offen sc1\n"
"ds_write_addtid_b32 v50 offset:9344\n"
"v_add_f32 v56, v56, v52\n"
"v_add_f32 v57, v57, v53\n"
"v_mfma_f32_16x16x32_bf16 v[96:99], v[68:71], a[48:51], v[96:99]\n"
"ds_write_addtid_b32 v51 offset:13984\n"
"v_add_f32 v58, v58, v54\n"
"v_mfma_f32_16x16x32_bf16 v[96:99], v[72:75], a[52:55], v[96:99]\n"
"v_add_f32 v59, v59, v55\n"
"v_add_f32 v56, v56, v60\n"
"v_mfma_f32_16x16x32_bf16 v[96:99], v[76:79], a[56:59], v[96:99]\n"
"v_add_f32 v57, v57, v61\n"
"v_add_f32 v58, v58, v62\n"
"v_mfma_f32_16x16x32_bf16 v[96:99], v[80:83], a[60:63], v[96:99]\n"
"v_add_f32 v59, v59, v63\n"
"v_add_f32 v56, v56, v128\n"
"v_mfma_f32_16x16x32_bf16 v[124:127], v[100:103], a[64:67], 0\n"
"global_load_dwordx4 v[48:51], v8, s[56:57] offset:0 sc1\n"
"v_add_f32 v57, v57, v129\n"
"v_mfma_f32_16x16x32_bf16 v[124:127], v[104:107], a[68:71], v[124:127]\n"
"ds_write_addtid_b32 v96 offset:256\n"
"v_add_f32 v58, v58, v130\n"
"v_mfma_f32_16x16x32_bf16 v[124:127], v[108:111], a[72:75], v[124:127]\n"
"ds_write_addtid_b32 v97 offset:4896\n"
"v_add_f32 v59, v59, v131\n"
"v_mfma_f32_16x16x32_bf16 v[124:127], v[64:67], a[76:79], v[124:127]\n"
"s_add_u32 s95, s50, 0xffd00000\n"
"s_and_b32 s94, s95, 0xfbffffff\n"
"ds_write_addtid_b32 v98 offset:9600\n"
"v_mfma_f32_16x16x32_bf16 v[124:127], v[68:71], a[80:83], v[124:127]\n"
"ds_write_addtid_b32 v99 offset:14240\n"
"global_load_dwordx4 v[52:55], v8, s[56:57] offset:1024 sc1\n"
"v_mfma_f32_16x16x32_bf16 v[124:127], v[72:75], a[84:87], v[124:127]\n"
"v_fma_f32 v60, v18, v141, v26\n"
"v_fma_f32 v61, v19, v141, v27\n"
"v_mfma_f32_16x16x32_bf16 v[124:127], v[76:79], a[88:91], v[124:127]\n"
"v_fma_f32 v62, v20, v141, v28\n"
"v_fma_f32 v63, v21, v141, v29\n"
"v_mfma_f32_16x16x32_bf16 v[124:127], v[80:83], a[92:95], v[124:127]\n"
"v_fma_f32 v56, v56, v140, v60\n"
"v_fma_f32 v57, v57, v140, v61\n"
"v_mfma_f32_16x16x32_bf16 v[96:99], v[100:103], a[96:99], 0\n"
"v_fma_f32 v58, v58, v140, v62\n"
"v_fma_f32 v59, v59, v140, v63\n"
"v_mfma_f32_16x16x32_bf16 v[96:99], v[104:107], a[100:103], v[96:99]\n"
"ds_write_addtid_b32 v124 offset:512\n"
"v_mul_f32_e64 v56, v56, |v56|\n"
"v_mfma_f32_16x16x32_bf16 v[96:99], v[108:111], a[104:107], v[96:99]\n"
"ds_write_addtid_b32 v125 offset:5152\n"
"v_mul_f32_e64 v57, v57, |v57|\n"
"v_mfma_f32_16x16x32_bf16 v[96:99], v[64:67], a[108:111], v[96:99]\n"
"ds_write_addtid_b32 v126 offset:9856\n"
"v_mul_f32_e64 v58, v58, |v58|\n"
"v_mfma_f32_16x16x32_bf16 v[96:99], v[68:71], a[112:115], v[96:99]\n"
"ds_write_addtid_b32 v127 offset:14496\n"
"v_mul_f32_e64 v59, v59, |v59|\n"
"v_mfma_f32_16x16x32_bf16 v[96:99], v[72:75], a[116:119], v[96:99]\n"
"v_cvt_pk_bf16_f32 v128, v56, v57\n"
"v_cvt_pk_bf16_f32 v129, v58, v59\n"
"v_mfma_f32_16x16x32_bf16 v[96:99], v[76:79], a[120:123], v[96:99]\n"
"v_pk_max_i16 v128, v128, 0\n"
"v_pk_max_i16 v129, v129, 0\n"
"v_mfma_f32_16x16x32_bf16 v[96:99], v[80:83], a[124:127], v[96:99]\n"
"s_waitcnt lgkmcnt(12)\n"
"v_add_f32 v84, v84, v88\n"
"v_add_f32 v85, v85, v89\n"
"v_mfma_f32_16x16x32_bf16 v[56:59], v[100:103], a[128:131], 0\n"
"v_add_f32 v86, v86, v90\n"
"v_add_f32 v87, v87, v91\n"
"v_mfma_f32_16x16x32_bf16 v[56:59], v[104:107], a[132:135], v[56:59]\n"
"ds_write_addtid_b32 v96 offset:768\n"
"v_add_f32 v84, v84, v92\n"
"v_mfma_f32_16x16x32_bf16 v[56:59], v[108:111], a[136:139], v[56:59]\n"
"ds_write_addtid_b32 v97 offset:5408\n"
"v_add_f32 v85, v85, v93\n"
"v_mfma_f32_16x16x32_bf16 v[56:59], v[64:67], a[140:143], v[56:59]\n"
"ds_write_addtid_b32 v98 offset:10112\n"
"v_add_f32 v86, v86, v94\n"
"v_mfma_f32_16x16x32_bf16 v[56:59], v[68:71], a[144:147], v[56:59]\n"
"ds_write_addtid_b32 v99 offset:14752\n"
"v_add_f32 v87, v87, v95\n"
"v_mfma_f32_16x16x32_bf16 v[56:59], v[72:75], a[148:151], v[56:59]\n"
"v_add_f32 v84, v84, v132\n"
"v_add_f32 v85, v85, v133\n"
"v_mfma_f32_16x16x32_bf16 v[56:59], v[76:79], a[152:155], v[56:59]\n"
"v_add_f32 v86, v86, v134\n"
"v_add_f32 v87, v87, v135\n"
"v_mfma_f32_16x16x32_bf16 v[56:59], v[80:83], a[156:159], v[56:59]\n"
"v_fma_f32 v60, v22, v141, v30\n"
"v_fma_f32 v61, v23, v141, v31\n"
"v_mfma_f32_16x16x32_bf16 v[88:91], v[100:103], a[160:163], 0\n"
"v_fma_f32 v62, v24, v141, v32\n"
"v_fma_f32 v63, v25, v141, v33\n"
"v_mfma_f32_16x16x32_bf16 v[88:91], v[104:107], a[164:167], v[88:91]\n"
"ds_write_addtid_b32 v56 offset:1024\n"
"v_fma_f32 v84, v84, v140, v60\n"
"v_mfma_f32_16x16x32_bf16 v[88:91], v[108:111], a[168:171], v[88:91]\n"
"s_add_u32 s58, s50, 0x180000\n"
"ds_write_addtid_b32 v57 offset:5664\n"
"v_fma_f32 v85, v85, v140, v61\n"
"v_mfma_f32_16x16x32_bf16 v[88:91], v[64:67], a[172:175], v[88:91]\n"
"s_and_b32 s58, s58, 0x3ffffff\n"
"ds_write_addtid_b32 v58 offset:10368\n"
"v_fma_f32 v86, v86, v140, v62\n"
"v_mfma_f32_16x16x32_bf16 v[88:91], v[68:71], a[176:179], v[88:91]\n"
"s_add_u32 s56, s40, s58\n"
"ds_write_addtid_b32 v59 offset:15008\n"
"v_fma_f32 v87, v87, v140, v63\n"
"v_mfma_f32_16x16x32_bf16 v[88:91], v[72:75], a[180:183], v[88:91]\n"
"s_addc_u32 s57, s41, 0\n"
"v_mul_f32_e64 v84, v84, |v84|\n"
"v_mul_f32_e64 v85, v85, |v85|\n"
"v_mfma_f32_16x16x32_bf16 v[88:91], v[76:79], a[184:187], v[88:91]\n"
"s_waitcnt vmcnt(6)\n"
"v_cmp_ne_u64 vcc, s[48:49], v[144:145]\n"
"v_mul_f32_e64 v86, v86, |v86|\n"
"v_mfma_f32_16x16x32_bf16 v[88:91], v[80:83], a[188:191], v[88:91]\n"
"v_mul_f32_e64 v87, v87, |v87|\n"
"v_cvt_pk_bf16_f32 v130, v84, v85\n"
"v_mfma_f32_16x16x32_bf16 v[92:95], a[192:195], v[100:103], 0\n"
"s_cbranch_vccnz .LF_3s\n"
".LF_3r:\n"
"v_cvt_pk_bf16_f32 v131, v86, v87\n"
"v_mfma_f32_16x16x32_bf16 v[92:95], a[196:199], v[104:107], v[92:95]\n"
"s_add_u32 s50, s50, 0x180000\n"
"ds_write_addtid_b32 v88 offset:1280\n"
"global_load_dwordx4 v[56:59], v5, s[56:57] offset:0 sc1\n"
"v_mfma_f32_16x16x32_bf16 v[92:95], a[200:203], v[108:111], v[92:95]\n"
"ds_write_addtid_b32 v89 offset:5920\n"
"v_pk_max_i16 v130, v130, 0\n"
"v_mfma_f32_16x16x32_bf16 v[92:95], a[204:207], v[64:67], v[92:95]\n"
"ds_write_addtid_b32 v90 offset:10624\n"
"v_pk_max_i16 v131, v131, 0\n"
"v_mfma_f32_16x16x32_bf16 v[92:95], a[208:211], v[68:71], v[92:95]\n"
"ds_write_addtid_b32 v91 offset:15264\n"
"v_mfma_f32_16x16x32_bf16 v[92:95], a[212:215], v[72:75], v[92:95]\n"
"v_mfma_f32_16x16x32_bf16 v[92:95], a[216:219], v[76:79], v[92:95]\n"
"v_mfma_f32_16x16x32_bf16 v[92:95], a[220:223], v[80:83], v[92:95]\n"
"v_mfma_f32_16x16x32_bf16 v[88:91], a[224:227], v[100:103], 0\n"
"s_waitcnt lgkmcnt(0)\n"
"global_load_dwordx4 v[60:63], v5, s[56:57] offset:1024 sc1\n"
"v_mfma_f32_16x16x32_bf16 v[88:91], a[228:231], v[104:107], v[88:91]\n"
"s_barrier\n"
"v_mfma_f32_16x16x32_bf16 v[88:91], a[232:235], v[108:111], v[88:91]\n"
"ds_read2st64_b32 v[136:137], v11 offset0:0 offset1:1\n"
"ds_read_b128 v[96:99], v2 offset:1536\n"
"v_mfma_f32_16x16x32_bf16 v[88:91], a[236:239], v[64:67], v[88:91]\n"
"s_waitcnt vmcnt(4)\n"
"buffer_load_dwordx2 v[138:139], v9, s[40:43], s50 offen sc1\n"
"v_mfma_f32_16x16x32_bf16 v[88:91], a[240:243], v[68:71], v[88:91]\n"
"global_load_dwordx4 v[64:67], v6, s[56:57] offset:0 sc1\n"
"ds_read_b128 v[84:87], v3 offset:512\n"
"v_mfma_f32_16x16x32_bf16 v[88:91], a[244:247], v[72:75], v[88:91]\n"
"ds_read_b128 v[100:103], v4 offset:2560\n"
"ds_read_b128 v[104:107], v2 offset:1792\n"
"v_mfma_f32_16x16x32_bf16 v[88:91], a[248:251], v[76:79], v[88:91]\n"
"ds_read_b128 v[108:111], v3 offset:768\n"
"ds_read_b128 v[124:127], v4 offset:2816\n"
"v_mfma_f32_16x16x32_bf16 v[88:91], a[252:255], v[80:83], v[88:91]\n"
"buffer_store_dwordx2 v[0:1], v16, s[40:43], s93 offen offset:0 sc1\n"
"v_mfma_f32_16x16x32_bf16 v[132:135], v[112:115], a[0:3], 0\n"
"buffer_store_dwordx4 v[128:131], v15, s[40:43], s94 offen sc1\n"
"v_mfma_f32_16x16x32_bf16 v[132:135], v[116:119], a[4:7], v[132:135]\n"
"v_mfma_f32_16x16x32_bf16 v[132:135], v[120:123], a[8:11], v[132:135]\n"
"global_load_dwordx4 v[68:71], v6, s[56:57] offset:1024 sc1\n"
"v_mfma_f32_16x16x32_bf16 v[132:135], v[36:39], a[12:15], v[132:135]\n"
"v_mfma_f32_16x16x32_bf16 v[132:135], v[40:43], a[16:19], v[132:135]\n"
"s_waitcnt lgkmcnt(3)\n"
"v_add_f32 v96, v96, v84\n"
"v_add_f32 v97, v97, v85\n"
"v_mfma_f32_16x16x32_bf16 v[132:135], v[44:47], a[20:23], v[132:135]\n"
"s_waitcnt vmcnt(7)\n"
"global_load_dwordx4 v[72:75], v7, s[56:57] offset:0 sc1\n"
"v_add_f32 v98, v98, v86\n"
"v_mfma_f32_16x16x32_bf16 v[132:135], v[48:51], a[24:27], v[132:135]\n"
"v_add_f32 v99, v99, v87\n"
"v_add_f32 v96, v96, v100\n"
"v_mfma_f32_16x16x32_bf16 v[132:135], v[52:55], a[28:31], v[132:135]\n"
"global_load_dwordx4 v[76:79], v7, s[56:57] offset:1024 sc1\n"
"v_add_f32 v97, v97, v101\n"
"v_mfma_f32_16x16x32_bf16 v[128:131], v[112:115], a[32:35], 0\n"
"v_add_f32 v98, v98, v102\n"
"v_add_f32 v99, v99, v103\n"
"v_mfma_f32_16x16x32_bf16 v[128:131], v[116:119], a[36:39], v[128:131]\n"
"ds_write_addtid_b32 v132 offset:1536\n"
"v_add_f32 v96, v96, v92\n"
"v_mfma_f32_16x16x32_bf16 v[128:131], v[120:123], a[40:43], v[128:131]\n"
"ds_write_addtid_b32 v133 offset:6176\n"
"v_add_f32 v97, v97, v93\n"
"v_mfma_f32_16x16x32_bf16 v[128:131], v[36:39], a[44:47], v[128:131]\n"
"ds_write_addtid_b32 v134 offset:10880\n"
"v_add_f32 v98, v98, v94\n"
"v_mfma_f32_16x16x32_bf16 v[128:131], v[40:43], a[48:51], v[128:131]\n"
"ds_write_addtid_b32 v135 offset:15520\n"
"v_add_f32 v99, v99, v95\n"
"v_mfma_f32_16x16x32_bf16 v[128:131], v[44:47], a[52:55], v[128:131]\n"
"s_add_u32 s93, s50, 0xffd00000\n"
"s_and_b32 s94, s93, 0xfbffffff\n"
"v_fma_f32 v84, v18, v137, v26\n"
"v_mfma_f32_16x16x32_bf16 v[128:131], v[48:51], a[56:59], v[128:131]\n"
"v_fma_f32 v85, v19, v137, v27\n"
"v_fma_f32 v86, v20, v137, v28\n"
"v_mfma_f32_16x16x32_bf16 v[128:131], v[52:55], a[60:63], v[128:131]\n"
"v_fma_f32 v87, v21, v137, v29\n"
"v_fma_f32 v96, v96, v136, v84\n"
"v_mfma_f32_16x16x32_bf16 v[92:95], v[112:115], a[64:67], 0\n"
"v_fma_f32 v97, v97, v136, v85\n"
"v_fma_f32 v98, v98, v136, v86\n"
"v_mfma_f32_16x16x32_bf16 v[92:95], v[116:119], a[68:71], v[92:95]\n"
"ds_write_addtid_b32 v128 offset:1792\n"
"global_load_dwordx4 v[80:83], v8, s[56:57] offset:0 sc1\n"
"v_mfma_f32_16x16x32_bf16 v[92:95], v[120:123], a[72:75], v[92:95]\n"
"ds_write_addtid_b32 v129 offset:6432\n"
"v_fma_f32 v99, v99, v136, v87\n"
"v_mfma_f32_16x16x32_bf16 v[92:95], v[36:39], a[76:79], v[92:95]\n"
"ds_write_addtid_b32 v130 offset:11136\n"
"v_mul_f32_e64 v96, v96, |v96|\n"
"v_mfma_f32_16x16x32_bf16 v[92:95], v[40:43], a[80:83], v[92:95]\n"
"ds_write_addtid_b32 v131 offset:15776\n"
"v_mul_f32_e64 v97, v97, |v97|\n"
"v_mfma_f32_16x16x32_bf16 v[92:95], v[44:47], a[84:87], v[92:95]\n"
"global_load_dwordx4 v[84:87], v8, s[56:57] offset:1024 sc1\n"
"v_mul_f32_e64 v98, v98, |v98|\n"
"v_mfma_f32_16x16x32_bf16 v[92:95], v[48:51], a[88:91], v[92:95]\n"
"v_mul_f32_e64 v99, v99, |v99|\n"
"v_cvt_pk_bf16_f32 v100, v96, v97\n"
"v_mfma_f32_16x16x32_bf16 v[92:95], v[52:55], a[92:95], v[92:95]\n"
"v_cvt_pk_bf16_f32 v101, v98, v99\n"
"v_pk_max_i16 v100, v100, 0\n"
"v_mfma_f32_16x16x32_bf16 v[128:131], v[112:115], a[96:99], 0\n"
"v_pk_max_i16 v101, v101, 0\n"
"v_mfma_f32_16x16x32_bf16 v[128:131], v[116:119], a[100:103], v[128:131]\n"
"s_waitcnt lgkmcnt(8)\n"
"ds_write_addtid_b32 v92 offset:2048\n"
"v_add_f32 v104, v104, v108\n"
"v_mfma_f32_16x16x32_bf16 v[128:131], v[120:123], a[104:107], v[128:131]\n"
"ds_write_addtid_b32 v93 offset:6688\n"
"v_add_f32 v105, v105, v109\n"
"v_mfma_f32_16x16x32_bf16 v[128:131], v[36:39], a[108:111], v[128:131]\n"
"ds_write_addtid_b32 v94 offset:11392\n"
"v_add_f32 v106, v106, v110\n"
"v_mfma_f32_16x16x32_bf16 v[128:131], v[40:43], a[112:115], v[128:131]\n"
"ds_write_addtid_b32 v95 offset:16032\n"
"v_add_f32 v107, v107, v111\n"
"v_mfma_f32_16x16x32_bf16 v[128:131], v[44:47], a[116:119], v[128:131]\n"
"v_add_f32 v104, v104, v124\n"
"v_add_f32 v105, v105, v125\n"
"v_mfma_f32_16x16x32_bf16 v[128:131], v[48:51], a[120:123], v[128:131]\n"
"v_add_f32 v106, v106, v126\n"
"v_add_f32 v107, v107, v127\n"
"v_mfma_f32_16x16x32_bf16 v[128:131], v[52:55], a[124:127], v[128:131]\n"
"v_add_f32 v104, v104, v88\n"
"v_add_f32 v105, v105, v89\n"
"v_mfma_f32_16x16x32_bf16 v[92:95], v[112:115], a[128:131], 0\n"
"v_add_f32 v106, v106, v90\n"
"v_add_f32 v107, v107, v91\n"
"v_mfma_f32_16x16x32_bf16 v[92:95], v[116:119], a[132:135], v[92:95]\n"
"ds_write_addtid_b32 v128 offset:2304\n"
"v_fma_f32 v96, v22, v137, v30\n"
"v_mfma_f32_16x16x32_bf16 v[92:95], v[120:123], a[136:139], v[92:95]\n"
"ds_write_addtid_b32 v129 offset:6944\n"
"v_fma_f32 v97, v23, v137, v31\n"
"v_mfma_f32_16x16x32_bf16 v[92:95], v[36:39], a[140:143], v[92:95]\n"
"ds_write_addtid_b32 v130 offset:11648\n"
"v_fma_f32 v98, v24, v137, v32\n"
"v_mfma_f32_16x16x32_bf16 v[92:95], v[40:43], a[144:147], v[92:95]\n"
"ds_write_addtid_b32 v131 offset:16288\n"
"v_fma_f32 v99, v25, v137, v33\n"
"v_mfma_f32_16x16x32_bf16 v[92:95], v[44:47], a[148:151], v[92:95]\n"
"v_fma_f32 v104, v104, v136, v96\n"
"v_fma_f32 v105, v105, v136, v97\n"
"v_mfma_f32_16x16x32_bf16 v[92:95], v[48:51], a[152:155], v[92:95]\n"
"v_fma_f32 v106, v106, v136, v98\n"
"v_fma_f32 v107, v107, v136, v99\n"
"v_mfma_f32_16x16x32_bf16 v[92:95], v[52:55], a[156:159], v[92:95]\n"
"v_mul_f32_e64 v104, v104, |v104|\n"
"v_mul_f32_e64 v105, v105, |v105|\n"
"v_mfma_f32_16x16x32_bf16 v[96:99], v[112:115], a[160:163], 0\n"
"v_mul_f32_e64 v106, v106, |v106|\n"
"v_mul_f32_e64 v107, v107, |v107|\n"
"v_mfma_f32_16x16x32_bf16 v[96:99], v[116:119], a[164:167], v[96:99]\n"
"ds_write_addtid_b32 v92 offset:2560\n"
"v_cvt_pk_bf16_f32 v102, v104, v105\n"
"v_mfma_f32_16x16x32_bf16 v[96:99], v[120:123], a[168:171], v[96:99]\n"
"s_add_u32 s58, s50, 0x180000\n"
"ds_write_addtid_b32 v93 offset:7200\n"
"v_cvt_pk_bf16_f32 v103, v106, v107\n"
"v_mfma_f32_16x16x32_bf16 v[96:99], v[36:39], a[172:175], v[96:99]\n"
"s_and_b32 s58, s58, 0x3ffffff\n"
"ds_write_addtid_b32 v94 offset:11904\n"
"v_pk_max_i16 v102, v102, 0\n"
"v_mfma_f32_16x16x32_bf16 v[96:99], v[40:43], a[176:179], v[96:99]\n"
"s_add_u32 s56, s40, s58\n"
"ds_write_addtid_b32 v95 offset:16544\n"
"v_pk_max_i16 v103, v103, 0\n"
"v_mfma_f32_16x16x32_bf16 v[96:99], v[44:47], a[180:183], v[96:99]\n"
"s_addc_u32 s57, s41, 0\n"
"buffer_store_dwordx4 v[100:103], v15, s[40:43], s94 offen sc1\n"
"v_mfma_f32_16x16x32_bf16 v[96:99], v[48:51], a[184:187], v[96:99]\n"
"s_waitcnt vmcnt(9)\n"
"v_cmp_ne_u64 vcc, s[48:49], v[138:139]\n"
"v_mfma_f32_16x16x32_bf16 v[96:99], v[52:55], a[188:191], v[96:99]\n"
"v_mfma_f32_16x16x32_bf16 v[100:103], a[192:195], v[112:115], 0\n"
"s_cbranch_vccnz .LF_4s\n"
".LF_4r:\n"
"v_mfma_f32_16x16x32_bf16 v[100:103], a[196:199], v[116:119], v[100:103]\n"
"s_add_u32 s50, s50, 0x180000\n"
"ds_write_addtid_b32 v96 offset:2816\n"
"v_mfma_f32_16x16x32_bf16 v[100:103], a[200:203], v[120:123], v[100:103]\n"
"s_waitcnt vmcnt(6)\n"
"buffer_store_dwordx2 v[0:1], v16, s[40:43], s95 offen offset:0 sc1\n"
"ds_write_addtid_b32 v97 offset:7456\n"
"v_mfma_f32_16x16x32_bf16 v[100:103], a[204:207], v[36:39], v[100:103]\n"
"ds_write_addtid_b32 v98 offset:12160\n"
"global_load_dwordx4 v[88:91], v5, s[56:57] offset:0 sc1\n"
"v_mfma_f32_16x16x32_bf16 v[100:103], a[208:211], v[40:43], v[100:103]\n"
"ds_write_addtid_b32 v99 offset:16800\n"
"v_mfma_f32_16x16x32_bf16 v[100:103], a[212:215], v[44:47], v[100:103]\n"
"v_mfma_f32_16x16x32_bf16 v[100:103], a[216:219], v[48:51], v[100:103]\n"
"v_mfma_f32_16x16x32_bf16 v[100:103], a[220:223], v[52:55], v[100:103]\n"
"v_mfma_f32_16x16x32_bf16 v[104:107], a[224:227], v[112:115], 0\n"
"s_waitcnt lgkmcnt(0)\n"
"global_load_dwordx4 v[92:95], v5, s[56:57] offset:1024 sc1\n"
"v_mfma_f32_16x16x32_bf16 v[104:107], a[228:231], v[116:119], v[104:107]\n"
"s_barrier\n"
"v_mfma_f32_16x16x32_bf16 v[104:107], a[232:235], v[120:123], v[104:107]\n"
"ds_read2st64_b32 v[132:133], v12 offset0:0 offset1:1\n"
"ds_read_b128 v[108:111], v2 offset:0\n"
"v_mfma_f32_16x16x32_bf16 v[104:107], a[236:239], v[36:39], v[104:107]\n"
"ds_read_b128 v[96:99], v3 offset:2048\n"
"ds_read_b128 v[112:115], v4 offset:1024\n"
"v_mfma_f32_16x16x32_bf16 v[104:107], a[240:243], v[40:43], v[104:107]\n"
"buffer_load_dwordx2 v[134:135], v9, s[40:43], s50 offen sc1\n"
"v_mfma_f32_16x16x32_bf16 v[104:107], a[244:247], v[44:47], v[104:107]\n"
"global_load_dwordx4 v[36:39], v6, s[56:57] offset:0 sc1\n"
"ds_read_b128 v[116:119], v2 offset:256\n"
"v_mfma_f32_16x16x32_bf16 v[104:107], a[248:251], v[48:51], v[104:107]\n"
"ds_read_b128 v[120:123], v3 offset:2304\n"
"ds_read_b128 v[124:127], v4 offset:1280\n"
"v_mfma_f32_16x16x32_bf16 v[104:107], a[252:255], v[52:55], v[104:107]\n"
"global_load_dwordx4 v[40:43], v6, s[56:57] offset:1024 sc1\n"
"v_mfma_f32_16x16x32_bf16 v[128:131], v[56:59], a[0:3], 0\n"
"v_mfma_f32_16x16x32_bf16 v[128:131], v[60:63], a[4:7], v[128:131]\n"
"s_waitcnt vmcnt(11)\n"
"v_mfma_f32_16x16x32_bf16 v[128:131], v[64:67], a[8:11], v[128:131]\n"
"s_waitcnt lgkmcnt(3)\n"
"v_add_f32 v108, v108, v96\n"
"v_add_f32 v109, v109, v97\n"
"v_mfma_f32_16x16x32_bf16 v[128:131], v[68:71], a[12:15], v[128:131]\n"
"s_waitcnt vmcnt(9)\n"
"v_add_f32 v110, v110, v98\n"
"v_add_f32 v111, v111, v99\n"
"v_mfma_f32_16x16x32_bf16 v[128:131], v[72:75], a[16:19], v[128:131]\n"
"v_add_f32 v108, v108, v112\n"
"v_add_f32 v109, v109, v113\n"
"v_mfma_f32_16x16x32_bf16 v[128:131], v[76:79], a[20:23], v[128:131]\n"
"s_waitcnt vmcnt(7)\n"
"v_add_f32 v110, v110, v114\n"
"v_add_f32 v111, v111, v115\n"
"v_mfma_f32_16x16x32_bf16 v[128:131], v[80:83], a[24:27], v[128:131]\n"
"v_add_f32 v108, v108, v100\n"
"v_add_f32 v109, v109, v101\n"
"v_mfma_f32_16x16x32_bf16 v[128:131], v[84:87], a[28:31], v[128:131]\n"
"global_load_dwordx4 v[44:47], v7, s[56:57] offset:0 sc1\n"
"v_add_f32 v110, v110, v102\n"
"v_mfma_f32_16x16x32_bf16 v[112:115], v[56:59], a[32:35], 0\n"
"v_add_f32 v111, v111, v103\n"
"v_mfma_f32_16x16x32_bf16 v[112:115], v[60:63], a[36:39], v[112:115]\n"
"ds_write_addtid_b32 v128 offset:0\n"
"global_load_dwordx4 v[48:51], v7, s[56:57] offset:1024 sc1\n"
"v_mfma_f32_16x16x32_bf16 v[112:115], v[64:67], a[40:43], v[112:115]\n"
"s_add_u32 s95, s50, 0xffd00000\n"
"s_and_b32 s94, s95, 0xfbffffff\n"
"ds_write_addtid_b32 v129 offset:4640\n"
"v_mfma_f32_16x16x32_bf16 v[112:115], v[68:71], a[44:47], v[112:115]\n"
"ds_write_addtid_b32 v130 offset:9344\n"
"v_fma_f32 v96, v18, v133, v26\n"
"v_mfma_f32_16x16x32_bf16 v[112:115], v[72:75], a[48:51], v[112:115]\n"
"ds_write_addtid_b32 v131 offset:13984\n"
"v_fma_f32 v97, v19, v133, v27\n"
"v_mfma_f32_16x16x32_bf16 v[112:115], v[76:79], a[52:55], v[112:115]\n"
"v_fma_f32 v98, v20, v133, v28\n"
"v_fma_f32 v99, v21, v133, v29\n"
"v_mfma_f32_16x16x32_bf16 v[112:115], v[80:83], a[56:59], v[112:115]\n"
"v_fma_f32 v108, v108, v132, v96\n"
"v_fma_f32 v109, v109, v132, v97\n"
"v_mfma_f32_16x16x32_bf16 v[112:115], v[84:87], a[60:63], v[112:115]\n"
"v_fma_f32 v110, v110, v132, v98\n"
"v_fma_f32 v111, v111, v132, v99\n"
"v_mfma_f32_16x16x32_bf16 v[100:103], v[56:59], a[64:67], 0\n"
"global_load_dwordx4 v[52:55], v8, s[56:57] offset:0 sc1\n"
"v_mul_f32_e64 v108, v108, |v108|\n"
"v_mfma_f32_16x16x32_bf16 v[100:103], v[60:63], a[68:71], v[100:103]\n"
"ds_write_addtid_b32 v112 offset:256\n"
"v_mul_f32_e64 v109, v109, |v109|\n"
"v_mfma_f32_16x16x32_bf16 v[100:103], v[64:67], a[72:75], v[100:103]\n"
"ds_write_addtid_b32 v113 offset:4896\n"
"v_mul_f32_e64 v110, v110, |v110|\n"
"v_mfma_f32_16x16x32_bf16 v[100:103], v[68:71], a[76:79], v[100:103]\n"
"ds_write_addtid_b32 v114 offset:9600\n"
"v_mul_f32_e64 v111, v111, |v111|\n"
"v_mfma_f32_16x16x32_bf16 v[100:103], v[72:75], a[80:83], v[100:103]\n"
"ds_write_addtid_b32 v115 offset:14240\n"
"global_load_dwordx4 v[96:99], v8, s[56:57] offset:1024 sc1\n"
"v_mfma_f32_16x16x32_bf16 v[100:103], v[76:79], a[84:87], v[100:103]\n"
"v_cvt_pk_bf16_f32 v128, v108, v109\n"
"v_cvt_pk_bf16_f32 v129, v110, v111\n"
"v_mfma_f32_16x16x32_bf16 v[100:103], v[80:83], a[88:91], v[100:103]\n"
"v_pk_max_i16 v128, v128, 0\n"
"v_pk_max_i16 v129, v129, 0\n"
"v_mfma_f32_16x16x32_bf16 v[100:103], v[84:87], a[92:95], v[100:103]\n"
"s_waitcnt lgkmcnt(8)\n"
"v_add_f32 v116, v116, v120\n"
"v_add_f32 v117, v117, v121\n"
"v_mfma_f32_16x16x32_bf16 v[108:111], v[56:59], a[96:99], 0\n"
"v_add_f32 v118, v118, v122\n"
"v_add_f32 v119, v119, v123\n"
"v_mfma_f32_16x16x32_bf16 v[108:111], v[60:63], a[100:103], v[108:111]\n"
"ds_write_addtid_b32 v100 offset:512\n"
"v_add_f32 v116, v116, v124\n"
"v_mfma_f32_16x16x32_bf16 v[108:111], v[64:67], a[104:107], v[108:111]\n"
"ds_write_addtid_b32 v101 offset:5152\n"
"v_add_f32 v117, v117, v125\n"
"v_mfma_f32_16x16x32_bf16 v[108:111], v[68:71], a[108:111], v[108:111]\n"
"ds_write_addtid_b32 v102 offset:9856\n"
"v_add_f32 v118, v118, v126\n"
"v_mfma_f32_16x16x32_bf16 v[108:111], v[72:75], a[112:115], v[108:111]\n"
"ds_write_addtid_b32 v103 offset:14496\n"
"v_add_f32 v119, v119, v127\n"
"v_mfma_f32_16x16x32_bf16 v[108:111], v[76:79], a[116:119], v[108:111]\n"
"s_waitcnt vmcnt(10)\n"
"buffer_store_dwordx2 v[0:1], v16, s[40:43], s93 offen offset:0 sc1\n"
"v_add_f32 v116, v116, v104\n"
"v_mfma_f32_16x16x32_bf16 v[108:111], v[80:83], a[120:123], v[108:111]\n"
"v_add_f32 v117, v117, v105\n"
"v_add_f32 v118, v118, v106\n"
"v_mfma_f32_16x16x32_bf16 v[108:111], v[84:87], a[124:127], v[108:111]\n"
"v_add_f32 v119, v119, v107\n"
"v_fma_f32 v100, v22, v133, v30\n"
"v_mfma_f32_16x16x32_bf16 v[112:115], v[56:59], a[128:131], 0\n"
"v_fma_f32 v101, v23, v133, v31\n"
"v_fma_f32 v102, v24, v133, v32\n"
"v_mfma_f32_16x16x32_bf16 v[112:115], v[60:63], a[132:135], v[112:115]\n"
"ds_write_addtid_b32 v108 offset:768\n"
"v_fma_f32 v103, v25, v133, v33\n"
"v_mfma_f32_16x16x32_bf16 v[112:115], v[64:67], a[136:139], v[112:115]\n"
"ds_write_addtid_b32 v109 offset:5408\n"
"v_fma_f32 v116, v116, v132, v100\n"
"v_mfma_f32_16x16x32_bf16 v[112:115], v[68:71], a[140:143], v[112:115]\n"
"ds_write_addtid_b32 v110 offset:10112\n"
"v_fma_f32 v117, v117, v132, v101\n"
"v_mfma_f32_16x16x32_bf16 v[112:115], v[72:75], a[144:147], v[112:115]\n"
"ds_write_addtid_b32 v111 offset:14752\n"
"v_fma_f32 v118, v118, v132, v102\n"
"v_mfma_f32_16x16x32_bf16 v[112:115], v[76:79], a[148:151], v[112:115]\n"
"v_fma_f32 v119, v119, v132, v103\n"
"v_mul_f32_e64 v116, v116, |v116|\n"
"v_mfma_f32_16x16x32_bf16 v[112:115], v[80:83], a[152:155], v[112:115]\n"
"v_mul_f32_e64 v117, v117, |v117|\n"
"v_mul_f32_e64 v118, v118, |v118|\n"
"v_mfma_f32_16x16x32_bf16 v[112:115], v[84:87], a[156:159], v[112:115]\n"
"v_mul_f32_e64 v119, v119, |v119|\n"
"v_cvt_pk_bf16_f32 v130, v116, v117\n"
"v_mfma_f32_16x16x32_bf16 v[108:111], v[56:59], a[160:163], 0\n"
"v_cvt_pk_bf16_f32 v131, v118, v119\n"
"v_pk_max_i16 v130, v130, 0\n"
"v_mfma_f32_16x16x32_bf16 v[108:111], v[60:63], a[164:167], v[108:111]\n"
"s_add_u32 s58, s50, 0x180000\n"
"ds_write_addtid_b32 v112 offset:1024\n"
"v_pk_max_i16 v131, v131, 0\n"
"v_mfma_f32_16x16x32_bf16 v[108:111], v[64:67], a[168:171], v[108:111]\n"
"s_and_b32 s58, s58, 0x3ffffff\n"
"ds_write_addtid_b32 v113 offset:5664\n"
"v_mfma_f32_16x16x32_bf16 v[108:111], v[68:71], a[172:175], v[108:111]\n"
"s_add_u32 s56, s40, s58\n"
"ds_write_addtid_b32 v114 offset:10368\n"
"v_mfma_f32_16x16x32_bf16 v[108:111], v[72:75], a[176:179], v[108:111]\n"
"s_addc_u32 s57, s41, 0\n"
"ds_write_addtid_b32 v115 offset:15008\n"
"v_mfma_f32_16x16x32_bf16 v[108:111], v[76:79], a[180:183], v[108:111]\n"
"s_waitcnt vmcnt(7)\n"
"buffer_store_dwordx4 v[128:131], v15, s[40:43], s94 offen sc1\n"
"v_cmp_ne_u64 vcc, s[48:49], v[134:135]\n"
"v_mfma_f32_16x16x32_bf16 v[108:111], v[80:83], a[184:187], v[108:111]\n"
"v_mfma_f32_16x16x32_bf16 v[108:111], v[84:87], a[188:191], v[108:111]\n"
"s_cbranch_vccnz .LF_1s\n"
".LF_1r:\n"
"v_mfma_f32_16x16x32_bf16 v[112:115], a[192:195], v[56:59], 0\n"
"s_add_u32 s50, s50, 0x180000\n"
"global_load_dwordx4 v[100:103], v5, s[56:57] offset:0 sc1\n"
"v_mfma_f32_16x16x32_bf16 v[112:115], a[196:199], v[60:63], v[112:115]\n"
"ds_write_addtid_b32 v108 offset:1280\n"
"global_load_dwordx4 v[124:127], v10, s[56:57] offset:0 sc1\n"
"v_mfma_f32_16x16x32_bf16 v[112:115], a[200:203], v[64:67], v[112:115]\n"
"ds_write_addtid_b32 v109 offset:5920\n"
"global_load_dwordx4 v[116:119], v10, s[56:57] offset:1024 sc1\n"
"v_mfma_f32_16x16x32_bf16 v[112:115], a[204:207], v[68:71], v[112:115]\n"
"ds_write_addtid_b32 v110 offset:10624\n"
"global_load_dwordx4 v[120:123], v10, s[56:57] offset:2048 sc1\n"
"v_mfma_f32_16x16x32_bf16 v[112:115], a[208:211], v[72:75], v[112:115]\n"
"ds_write_addtid_b32 v111 offset:15264\n"
"global_load_dwordx4 v[128:131], v10, s[56:57] offset:3072 sc1\n"
"v_mfma_f32_16x16x32_bf16 v[112:115], a[212:215], v[76:79], v[112:115]\n"
"global_load_dwordx4 v[104:107], v5, s[56:57] offset:1024 sc1\n"
"v_mfma_f32_16x16x32_bf16 v[112:115], a[216:219], v[80:83], v[112:115]\n"
"v_mfma_f32_16x16x32_bf16 v[112:115], a[220:223], v[84:87], v[112:115]\n"
"v_mfma_f32_16x16x32_bf16 v[132:135], a[224:227], v[56:59], 0\n"
"s_waitcnt lgkmcnt(0)\n"
"v_mfma_f32_16x16x32_bf16 v[132:135], a[228:231], v[60:63], v[132:135]\n"
"s_barrier\n"
"v_mfma_f32_16x16x32_bf16 v[132:135], a[232:235], v[64:67], v[132:135]\n"
"global_load_dwordx4 v[108:111], v6, s[56:57] offset:0 sc1\n"
"ds_read2st64_b32 v[152:153], v13 offset0:0 offset1:1\n"
"v_mfma_f32_16x16x32_bf16 v[132:135], a[236:239], v[68:71], v[132:135]\n"
"ds_read_b128 v[56:59], v2 offset:1536\n"
"ds_read_b128 v[60:63], v3 offset:512\n"
"v_mfma_f32_16x16x32_bf16 v[132:135], a[240:243], v[72:75], v[132:135]\n"
"ds_read_b128 v[136:139], v4 offset:2560\n"
"ds_read_b128 v[140:143], v2 offset:1792\n"
"v_mfma_f32_16x16x32_bf16 v[132:135], a[244:247], v[76:79], v[132:135]\n"
"buffer_load_dwordx2 v[154:155], v9, s[40:43], s50 offen sc1\n"
"v_mfma_f32_16x16x32_bf16 v[132:135], a[248:251], v[80:83], v[132:135]\n"
"global_load_dwordx4 v[64:67], v6, s[56:57] offset:1024 sc1\n"
"ds_read_b128 v[144:147], v3 offset:768\n"
"v_mfma_f32_16x16x32_bf16 v[132:135], a[252:255], v[84:87], v[132:135]\n"
"ds_read_b128 v[148:151], v4 offset:2816\n"
"v_mfma_f32_16x16x32_bf16 v[76:79], v[88:91], a[0:3], 0\n"
"v_mfma_f32_16x16x32_bf16 v[76:79], v[92:95], a[4:7], v[76:79]\n"
"s_waitcnt vmcnt(15)\n"
"v_mfma_f32_16x16x32_bf16 v[76:79], v[36:39], a[8:11], v[76:79]\n"
"s_waitcnt lgkmcnt(3)\n"
"v_add_f32 v56, v56, v60\n"
"v_add_f32 v57, v57, v61\n"
"v_mfma_f32_16x16x32_bf16 v[76:79], v[40:43], a[12:15], v[76:79]\n"
"s_waitcnt vmcnt(13)\n"
"v_add_f32 v58, v58, v62\n"
"v_add_f32 v59, v59, v63\n"
"v_mfma_f32_16x16x32_bf16 v[76:79], v[44:47], a[16:19], v[76:79]\n"
"global_load_dwordx4 v[68:71], v7, s[56:57] offset:0 sc1\n"
"v_add_f32 v56, v56, v136\n"
"v_mfma_f32_16x16x32_bf16 v[76:79], v[48:51], a[20:23], v[76:79]\n"
"s_waitcnt vmcnt(12)\n"
"v_add_f32 v57, v57, v137\n"
"v_add_f32 v58, v58, v138\n"
"v_mfma_f32_16x16x32_bf16 v[76:79], v[52:55], a[24:27], v[76:79]\n"
"v_add_f32 v59, v59, v139\n"
"v_add_f32 v56, v56, v112\n"
"v_mfma_f32_16x16x32_bf16 v[76:79], v[96:99], a[28:31], v[76:79]\n"
"v_add_f32 v57, v57, v113\n"
"v_add_f32 v58, v58, v114\n"
"v_mfma_f32_16x16x32_bf16 v[60:63], v[88:91], a[32:35], 0\n"
"v_add_f32 v59, v59, v115\n"
"v_mfma_f32_16x16x32_bf16 v[60:63], v[92:95], a[36:39], v[60:63]\n"
"ds_write_addtid_b32 v76 offset:1536\n"
"global_load_dwordx4 v[72:75], v7, s[56:57] offset:1024 sc1\n"
"v_mfma_f32_16x16x32_bf16 v[60:63], v[36:39], a[40:43], v[60:63]\n"
"s_add_u32 s93, s50, 0xffd00000\n"
"s_and_b32 s94, s93, 0xfbffffff\n"
"ds_write_addtid_b32 v77 offset:6176\n"
"v_mfma_f32_16x16x32_bf16 v[60:63], v[40:43], a[44:47], v[60:63]\n"
"ds_write_addtid_b32 v78 offset:10880\n"
"v_fma_f32 v80, v18, v153, v26\n"
"v_mfma_f32_16x16x32_bf16 v[60:63], v[44:47], a[48:51], v[60:63]\n"
"ds_write_addtid_b32 v79 offset:15520\n"
"v_fma_f32 v81, v19, v153, v27\n"
"v_mfma_f32_16x16x32_bf16 v[60:63], v[48:51], a[52:55], v[60:63]\n"
"v_fma_f32 v82, v20, v153, v28\n"
"v_fma_f32 v83, v21, v153, v29\n"
"v_mfma_f32_16x16x32_bf16 v[60:63], v[52:55], a[56:59], v[60:63]\n"
"v_fma_f32 v56, v56, v152, v80\n"
"v_fma_f32 v57, v57, v152, v81\n"
"v_mfma_f32_16x16x32_bf16 v[60:63], v[96:99], a[60:63], v[60:63]\n"
"global_load_dwordx4 v[76:79], v8, s[56:57] offset:0 sc1\n"
"v_fma_f32 v58, v58, v152, v82\n"
"v_mfma_f32_16x16x32_bf16 v[84:87], v[88:91], a[64:67], 0\n"
"v_fma_f32 v59, v59, v152, v83\n"
"v_mul_f32_e64 v56, v56, |v56|\n"
"v_mfma_f32_16x16x32_bf16 v[84:87], v[92:95], a[68:71], v[84:87]\n"
"ds_write_addtid_b32 v60 offset:1792\n"
"v_mul_f32_e64 v57, v57, |v57|\n"
"v_mfma_f32_16x16x32_bf16 v[84:87], v[36:39], a[72:75], v[84:87]\n"
"ds_write_addtid_b32 v61 offset:6432\n"
"v_mul_f32_e64 v58, v58, |v58|\n"
"v_mfma_f32_16x16x32_bf16 v[84:87], v[40:43], a[76:79], v[84:87]\n"
"ds_write_addtid_b32 v62 offset:11136\n"
"global_load_dwordx4 v[80:83], v8, s[56:57] offset:1024 sc1\n"
"v_mfma_f32_16x16x32_bf16 v[84:87], v[44:47], a[80:83], v[84:87]\n"
"ds_write_addtid_b32 v63 offset:15776\n"
"v_mul_f32_e64 v59, v59, |v59|\n"
"v_mfma_f32_16x16x32_bf16 v[84:87], v[48:51], a[84:87], v[84:87]\n"
"v_cvt_pk_bf16_f32 v136, v56, v57\n"
"v_cvt_pk_bf16_f32 v137, v58, v59\n"
"v_mfma_f32_16x16x32_bf16 v[84:87], v[52:55], a[88:91], v[84:87]\n"
"v_pk_max_i16 v136, v136, 0\n"
"v_pk_max_i16 v137, v137, 0\n"
"v_mfma_f32_16x16x32_bf16 v[84:87], v[96:99], a[92:95], v[84:87]\n"
"s_waitcnt lgkmcnt(8)\n"
"v_add_f32 v140, v140, v144\n"
"v_add_f32 v141, v141, v145\n"
"v_mfma_f32_16x16x32_bf16 v[56:59], v[88:91], a[96:99], 0\n"
"v_add_f32 v142, v142, v146\n"
"v_add_f32 v143, v143, v147\n"
"v_mfma_f32_16x16x32_bf16 v[56:59], v[92:95], a[100:103], v[56:59]\n"
"ds_write_addtid_b32 v84 offset:2048\n"
"v_add_f32 v140, v140, v148\n"
"v_mfma_f32_16x16x32_bf16 v[56:59], v[36:39], a[104:107], v[56:59]\n"
"ds_write_addtid_b32 v85 offset:6688\n"
"v_add_f32 v141, v141, v149\n"
"v_mfma_f32_16x16x32_bf16 v[56:59], v[40:43], a[108:111], v[56:59]\n"
"ds_write_addtid_b32 v86 offset:11392\n"
"v_add_f32 v142, v142, v150\n"
"v_mfma_f32_16x16x32_bf16 v[56:59], v[44:47], a[112:115], v[56:59]\n"
"ds_write_addtid_b32 v87 offset:16032\n"
"v_add_f32 v143, v143, v151\n"
"v_mfma_f32_16x16x32_bf16 v[56:59], v[48:51], a[116:119], v[56:59]\n"
"s_waitcnt vmcnt(13)\n"
"buffer_store_dwordx2 v[0:1], v16, s[40:43], s95 offen offset:0 sc1\n"
"v_add_f32 v140, v140, v132\n"
"v_mfma_f32_16x16x32_bf16 v[56:59], v[52:55], a[120:123], v[56:59]\n"
"v_add_f32 v141, v141, v133\n"
"v_add_f32 v142, v142, v134\n"
"v_mfma_f32_16x16x32_bf16 v[56:59], v[96:99], a[124:127], v[56:59]\n"
"v_add_f32 v143, v143, v135\n"
"v_fma_f32 v60, v22, v153, v30\n"
"v_mfma_f32_16x16x32_bf16 v[84:87], v[88:91], a[128:131], 0\n"
"v_fma_f32 v61, v23, v153, v31\n"
"v_fma_f32 v62, v24, v153, v32\n"
"v_mfma_f32_16x16x32_bf16 v[84:87], v[92:95], a[132:135], v[84:87]\n"
"ds_write_addtid_b32 v56 offset:2304\n"
"v_fma_f32 v63, v25, v153, v33\n"
"v_mfma_f32_16x16x32_bf16 v[84:87], v[36:39], a[136:139], v[84:87]\n"
"ds_write_addtid_b32 v57 offset:6944\n"
"v_fma_f32 v140, v140, v152, v60\n"
"v_mfma_f32_16x16x32_bf16 v[84:87], v[40:43], a[140:143], v[84:87]\n"
"ds_write_addtid_b32 v58 offset:11648\n"
"v_fma_f32 v141, v141, v152, v61\n"
"v_mfma_f32_16x16x32_bf16 v[84:87], v[44:47], a[144:147], v[84:87]\n"
"s_waitcnt vmcnt(9)\n"
"ds_write_addtid_b32 v59 offset:16288\n"
"v_add_f32 v124, v124, v116\n"
"v_mfma_f32_16x16x32_bf16 v[84:87], v[48:51], a[148:151], v[84:87]\n"
"v_add_f32 v125, v125, v117\n"
"v_add_f32 v126, v126, v118\n"
"v_mfma_f32_16x16x32_bf16 v[84:87], v[52:55], a[152:155], v[84:87]\n"
"v_add_f32 v127, v127, v119\n"
"v_add_f32 v120, v120, v128\n"
"v_mfma_f32_16x16x32_bf16 v[84:87], v[96:99], a[156:159], v[84:87]\n"
"v_add_f32 v121, v121, v129\n"
"v_add_f32 v122, v122, v130\n"
"v_mfma_f32_16x16x32_bf16 v[56:59], v[88:91], a[160:163], 0\n"
"v_add_f32 v123, v123, v131\n"
"v_add_f32 v124, v124, v120\n"
"v_mfma_f32_16x16x32_bf16 v[56:59], v[92:95], a[164:167], v[56:59]\n"
"ds_write_addtid_b32 v84 offset:2560\n"
"v_add_f32 v125, v125, v121\n"
"v_mfma_f32_16x16x32_bf16 v[56:59], v[36:39], a[168:171], v[56:59]\n"
"s_add_u32 s58, s50, 0x180000\n"
"ds_write_addtid_b32 v85 offset:7200\n"
"v_add_f32 v126, v126, v122\n"
"v_mfma_f32_16x16x32_bf16 v[56:59], v[40:43], a[172:175], v[56:59]\n"
"s_and_b32 s58, s58, 0x3ffffff\n"
"ds_write_addtid_b32 v86 offset:11904\n"
"v_add_f32 v127, v127, v123\n"
"v_mfma_f32_16x16x32_bf16 v[56:59], v[44:47], a[176:179], v[56:59]\n"
"s_add_u32 s56, s40, s58\n"
"ds_write_addtid_b32 v87 offset:16544\n"
"v_fma_f32 v142, v142, v152, v62\n"
"v_mfma_f32_16x16x32_bf16 v[56:59], v[48:51], a[180:183], v[56:59]\n"
"s_addc_u32 s57, s41, 0\n"
"v_permlane32_swap_b32 v124, v126\n"
"v_mfma_f32_16x16x32_bf16 v[56:59], v[52:55], a[184:187], v[56:59]\n"
"s_waitcnt vmcnt(6)\n"
"v_cmp_ne_u64 vcc, s[48:49], v[154:155]\n"
"v_fma_f32 v143, v143, v152, v63\n"
"v_mfma_f32_16x16x32_bf16 v[56:59], v[96:99], a[188:191], v[56:59]\n"
"v_permlane32_swap_b32 v125, v127\n"
"v_mfma_f32_16x16x32_bf16 v[128:131], a[192:195], v[88:91], 0\n"
"s_cbranch_vccnz .LF_2s\n"
".LF_2r:\n"
"v_add_f32 v124, v124, v126\n"
"v_mfma_f32_16x16x32_bf16 v[128:131], a[196:199], v[92:95], v[128:131]\n"
"s_add_u32 s50, s50, 0x180000\n"
"ds_write_addtid_b32 v56 offset:2816\n"
"v_add_f32 v125, v125, v127\n"
"v_mfma_f32_16x16x32_bf16 v[128:131], a[200:203], v[36:39], v[128:131]\n"
"ds_write_addtid_b32 v57 offset:7456\n"
"global_load_dwordx4 v[112:115], v5, s[56:57] offset:0 sc1\n"
"v_mfma_f32_16x16x32_bf16 v[128:131], a[204:207], v[40:43], v[128:131]\n"
"ds_write_addtid_b32 v58 offset:12160\n"
"v_mul_f32_e64 v140, v140, |v140|\n"
"v_mfma_f32_16x16x32_bf16 v[128:131], a[208:211], v[44:47], v[128:131]\n"
"ds_write_addtid_b32 v59 offset:16800\n"
"v_mul_f32_e64 v141, v141, |v141|\n"
"v_mfma_f32_16x16x32_bf16 v[128:131], a[212:215], v[48:51], v[128:131]\n"
"v_permlane16_swap_b32 v124, v125\n"
"v_mfma_f32_16x16x32_bf16 v[128:131], a[216:219], v[52:55], v[128:131]\n"
"v_add_f32 v124, v124, v125\n"
"v_mul_f32_e64 v142, v142, |v142|\n"
"v_mfma_f32_16x16x32_bf16 v[128:131], a[220:223], v[96:99], v[128:131]\n"
"global_load_dwordx4 v[116:119], v5, s[56:57] offset:1024 sc1\n"
"v_add_f32_dpp v125, v124, v124 row_ror:8 row_mask:0xf bank_mask:0xf\n"
"v_mfma_f32_16x16x32_bf16 v[132:135], a[224:227], v[88:91], 0\n"
"s_waitcnt lgkmcnt(0)\n"
"v_mov_b32 v124, v125\n"
"v_mul_f32_e64 v143, v143, |v143|\n"
"v_mfma_f32_16x16x32_bf16 v[132:135], a[228:231], v[92:95], v[132:135]\n"
"s_barrier\n"
"v_mfma_f32_16x16x32_bf16 v[132:135], a[232:235], v[36:39], v[132:135]\n"
"v_permlane16_swap_b32 v125, v124\n"
"v_mfma_f32_16x16x32_bf16 v[132:135], a[236:239], v[40:43], v[132:135]\n"
"global_load_dwordx4 v[120:123], v6, s[56:57] offset:0 sc1\n"
"v_mul_f32 v125, 0xba800000, v125\n"
"v_mfma_f32_16x16x32_bf16 v[132:135], a[240:243], v[44:47], v[132:135]\n"
"buffer_load_dwordx2 v[144:145], v9, s[40:43], s50 offen sc1\n"
"v_mfma_f32_16x16x32_bf16 v[132:135], a[244:247], v[48:51], v[132:135]\n"
"v_fma_f32 v124, v124, s62, v17\n"
"v_fma_f32 v124, -v125, v125, v124\n"
"v_mfma_f32_16x16x32_bf16 v[132:135], a[248:251], v[52:55], v[132:135]\n"
"v_max_f32 v124, v17, v124\n"
"v_cvt_pk_bf16_f32 v138, v140, v141\n"
"v_mfma_f32_16x16x32_bf16 v[132:135], a[252:255], v[96:99], v[132:135]\n"
"v_rsq_f32 v124, v124\n"
"s_branch .LF_top\n"
".LF_1s:\n"
"s_cmp_ge_u32 s50, s53\n"
"s_cbranch_scc1 .LF_end\n"
"s_cmp_ge_u32 s50, s52\n"
"s_cbranch_scc1 .LF_1r\n"
"s_sub_u32 s60, s50, 0x0\n"
"s_mov_b32 s61, 200000\n"
"s_nop 7\n"
".LF_1p:\n"
"buffer_load_dwordx2 v[134:135], v9, s[40:43], s60 offen sc1\n"
"s_waitcnt vmcnt(0)\n"
"v_cmp_ne_u64 vcc, s[48:49], v[134:135]\n"
"s_cbranch_vccz .LF_1px\n"
"s_sleep 1\n"
"s_sub_u32 s61, s61, 1\n"
"s_cbranch_scc0 .LF_1p\n"
"s_branch .LF_end\n"
".LF_1px:\n"
"s_branch .LF_1r\n"
".LF_2s:\n"
"s_cmp_ge_u32 s50, s53\n"
"s_cbranch_scc1 .LF_end\n"
"s_cmp_ge_u32 s50, s52\n"
"s_cbranch_scc1 .LF_2r\n"
"s_sub_u32 s60, s50, 0x0\n"
"s_mov_b32 s61, 200000\n"
"s_nop 7\n"
".LF_2p:\n"
"buffer_load_dwordx2 v[154:155], v9, s[40:43], s60 offen sc1\n"
"s_waitcnt vmcnt(0)\n"
"v_cmp_ne_u64 vcc, s[48:49], v[154:155]\n"
"s_cbranch_vccz .LF_2px\n"
"s_sleep 1\n"
"s_sub_u32 s61, s61, 1\n"
"s_cbranch_scc0 .LF_2p\n"
"s_branch .LF_end\n"
".LF_2px:\n"
"s_branch .LF_2r\n"
".LF_3s:\n"
"s_cmp_ge_u32 s50, s53\n"
"s_cbranch_scc1 .LF_end\n"
"s_cmp_ge_u32 s50, s52\n"
"s_cbranch_scc1 .LF_3r\n"
"s_sub_u32 s60, s50, 0x0\n"
"s_mov_b32 s61, 200000\n"
"s_nop 7\n"
".LF_3p:\n"
"buffer_load_dwordx2 v[144:145], v9, s[40:43], s60 offen sc1\n"
"s_waitcnt vmcnt(0)\n"
"v_cmp_ne_u64 vcc, s[48:49], v[144:145]\n"
"s_cbranch_vccz .LF_3px\n"
"s_sleep 1\n"
"s_sub_u32 s61, s61, 1\n"
"s_cbranch_scc0 .LF_3p\n"
"s_branch .LF_end\n"
".LF_3px:\n"
"s_branch .LF_3r\n"
".LF_4s:\n"
"s_cmp_ge_u32 s50, s53\n"
"s_cbranch_scc1 .LF_end\n"
"s_cmp_ge_u32 s50, s52\n"
"s_cbranch_scc1 .LF_4r\n"
"s_sub_u32 s60, s50, 0x0\n"
"s_mov_b32 s61, 200000\n"
"s_nop 7\n"
".LF_4p:\n"
"buffer_load_dwordx2 v[138:139], v9, s[40:43], s60 offen sc1\n"
"s_waitcnt vmcnt(0)\n"
"v_cmp_ne_u64 vcc, s[48:49], v[138:139]\n"
"s_cbranch_vccz .LF_4px\n"
"s_sleep 1\n"
"s_sub_u32 s61, s61, 1\n"
"s_cbranch_scc0 .LF_4p\n"
"s_branch .LF_end\n"
".LF_4px:\n"
"s_branch .LF_4r\n"
".LF_end:\n"
"s_waitcnt vmcnt(0) lgkmcnt(0)\n"
"s_endpgm\n"
      :: "{v2}"((unsigned)(lds0 + ((wid + 3) & 3) * 19104 + trp)), "{v3}"((unsigned)(lds0 + ((wid + 2) & 3) * 19104 + trp)), "{v4}"((unsigned)(lds0 + ((wid + 1) & 3) * 19104 + trp)), "{v5}"((unsigned)(lane * 16 + wid * 8192 + ((0 + rot) % 4) * 2048 + 134283264)), "{v6}"((unsigned)(lane * 16 + wid * 8192 + ((1 + rot) % 4) * 2048 + 134283264)), "{v7}"((unsigned)(lane * 16 + wid * 8192 + ((2 + rot) % 4) * 2048 + 134283264)), "{v8}"((unsigned)(lane * 16 + wid * 8192 + ((3 + rot) % 4) * 2048 + 134283264)), "{v9}"((unsigned)((lane & 31) * 8 + 352768 + F_GL * 1572864)), "{v10}"((unsigned)(lane * 16 + 134561792)), "{v11}"((unsigned)(lds0 + ((wid + 0) & 3) * 19104 + 18592 + ((li >> 1) + 32 * (li & 1)) * 4)), "{v12}"((unsigned)(lds0 + ((wid + 1) & 3) * 19104 + 18592 + ((li >> 1) + 32 * (li & 1)) * 4)), "{v13}"((unsigned)(lds0 + ((wid + 2) & 3) * 19104 + 18592 + ((li >> 1) + 32 * (li & 1)) * 4)), "{v14}"((unsigned)(lds0 + ((wid + 3) & 3) * 19104 + 18592 + ((li >> 1) + 32 * (li & 1)) * 4)), "{v15}"((unsigned)(lane * 16 + pair * 1024 + 98304)), "{v16}"((unsigned)(lane ? DROP : pair * 8 + idx / 7 * 32 + 353024)), "{v17}"((unsigned)(0x3727c5ac)), "{v36}"((unsigned)(vT + tsw)), "{v37}"((unsigned)(vT + (tsw ^ 8))), "{v38}"((unsigned)(pair * 64 + lg * 16)), "{v39}"((unsigned)(vD + ((0 + rot) % 4) * 458752)), "{v40}"((unsigned)(vD + ((1 + rot) % 4) * 458752)), "{v41}"((unsigned)(vD + ((2 + rot) % 4) * 458752)), "{v42}"((unsigned)(vD + ((3 + rot) % 4) * 458752)), "{v43}"((unsigned)(lds0 + wid * 19104 + trp)), "{s[40:43]}"(rS), "{s[44:47]}"(rW), "{s[48:49]}"(wsp), "{s50}"((unsigned)(sA)), "{s51}"((unsigned)(sLo)), "{s52}"((unsigned)(sHi)), "{s53}"((unsigned)(sEx)), "{s54}"((unsigned)(sEnt)), "{s55}"((unsigned)(sLd)), "{s[56:57]}"(wsb + (sLo & 0x3ffffff)), "{s59}"((unsigned)(lds0 + wid * 19104)), "{s62}"((unsigned)(0x3a800000)), "{s[64:65]}"((u64)a.b1), "{s66}"((unsigned)(sW0 + 64 * ((wid + 1) & 3))), "{s67}"((unsigned)(sW0 + 64 * ((wid + 2) & 3))), "{s68}"((unsigned)(sW0 + 64 * ((wid + 3) & 3))), "{s69}"((unsigned)(sW0 + 64 * ((wid + 4) & 3)))
      : "v255", "a255", "s95", "vcc", "scc", "memory");

  } else {
    const int kq = idx / 7, grp = idx % 7, f0 = kq * 28 + wid * 7, pg0 = grp < 4 ? grp * 5 : 4 + grp * 4, pair = pg0 + wid;
    const unsigned rot = SLP & 8 ? 0 : (set * 5) >> 1, tsw = (kq & 1) * 8, sW0 = f0 * 32 * 2048 + pg0 * 64, vD = (lane >> 2) * 2048 + (lane & 3) * 16;
    const u32x4 rW = desc(a.w2, 3584 * 2048);
    if (idx == 0) {
      const unsigned sEx = sHi + H_EX * 3 * RS, sEnt = -wid & 3;
    asm volatile(
"s_nop 4\n"
"v_mov_b32 v186, 0\n"
"global_load_dwordx2 v[0:1], v186, s[48:49] sc1\n"
"v_mov_b32 v188, 0x3f803f80\n"
"v_mov_b32 v189, 0x3f803f80\n"
"v_mov_b32 v190, 0x3f803f80\n"
"v_mov_b32 v191, 0x3f803f80\n"
"s_add_u32 m0, s55, 0\n"
"s_add_u32 s60, s66, 0\n"
"buffer_load_dwordx4 v50, s[44:47], s60 offen lds\n"
"s_add_u32 m0, s55, 1024\n"
"s_add_u32 s60, s66, 32768\n"
"buffer_load_dwordx4 v50, s[44:47], s60 offen lds\n"
"s_add_u32 m0, s55, 2048\n"
"s_add_u32 s60, s67, 0\n"
"buffer_load_dwordx4 v50, s[44:47], s60 offen lds\n"
"s_add_u32 m0, s55, 3072\n"
"s_add_u32 s60, s67, 32768\n"
"buffer_load_dwordx4 v50, s[44:47], s60 offen lds\n"
"s_add_u32 m0, s55, 4096\n"
"s_add_u32 s60, s68, 0\n"
"buffer_load_dwordx4 v50, s[44:47], s60 offen lds\n"
"s_add_u32 m0, s55, 5120\n"
"s_add_u32 s60, s68, 32768\n"
"buffer_load_dwordx4 v50, s[44:47], s60 offen lds\n"
"s_add_u32 m0, s55, 6144\n"
"s_add_u32 s60, s69, 0\n"
"buffer_load_dwordx4 v50, s[44:47], s60 offen lds\n"
"s_add_u32 m0, s55, 7168\n"
"s_add_u32 s60, s69, 32768\n"
"buffer_load_dwordx4 v50, s[44:47], s60 offen lds\n"
"s_add_u32 m0, s55, 8192\n"
"s_add_u32 s60, s70, 0\n"
"buffer_load_dwordx4 v50, s[44:47], s60 offen lds\n"
"s_add_u32 m0, s55, 9216\n"
"s_add_u32 s60, s70, 32768\n"
"buffer_load_dwordx4 v50, s[44:47], s60 offen lds\n"
"s_add_u32 m0, s55, 10240\n"
"s_add_u32 s60, s66, 0\n"
"buffer_load_dwordx4 v51, s[44:47], s60 offen lds\n"
"s_add_u32 m0, s55, 11264\n"
"s_add_u32 s60, s66, 32768\n"
"buffer_load_dwordx4 v51, s[44:47], s60 offen lds\n"
"s_add_u32 m0, s55, 12288\n"
"s_add_u32 s60, s67, 0\n"
"buffer_load_dwordx4 v51, s[44:47], s60 offen lds\n"
"s_add_u32 m0, s55, 13312\n"
"s_add_u32 s60, s67, 32768\n"
"buffer_load_dwordx4 v51, s[44:47], s60 offen lds\n"
"s_add_u32 m0, s55, 14336\n"
"s_add_u32 s60, s68, 0\n"
"buffer_load_dwordx4 v51, s[44:47], s60 offen lds\n"
"s_add_u32 m0, s55, 15360\n"
"s_add_u32 s60, s68, 32768\n"
"buffer_load_dwordx4 v51, s[44:47], s60 offen lds\n"
"s_add_u32 m0, s55, 16384\n"
"s_add_u32 s60, s69, 0\n"
"buffer_load_dwordx4 v51, s[44:47], s60 offen lds\n"
"s_add_u32 m0, s55, 17408\n"
"s_add_u32 s60, s69, 32768\n"
"buffer_load_dwordx4 v51, s[44:47], s60 offen lds\n"
"s_add_u32 m0, s55, 18432\n"
"s_add_u32 s60, s70, 0\n"
"buffer_load_dwordx4 v51, s[44:47], s60 offen lds\n"
"s_add_u32 m0, s55, 19456\n"
"s_add_u32 s60, s70, 32768\n"
"buffer_load_dwordx4 v51, s[44:47], s60 offen lds\n"
"s_add_u32 m0, s55, 20480\n"
"s_add_u32 s60, s66, 0\n"
"buffer_load_dwordx4 v52, s[44:47], s60 offen lds\n"
"s_add_u32 m0, s55, 21504\n"
"s_add_u32 s60, s66, 32768\n"
"buffer_load_dwordx4 v52, s[44:47], s60 offen lds\n"
"s_add_u32 m0, s55, 22528\n"
"s_add_u32 s60, s67, 0\n"
"buffer_load_dwordx4 v52, s[44:47], s60 offen lds\n"
"s_add_u32 m0, s55, 23552\n"
"s_add_u32 s60, s67, 32768\n"
"buffer_load_dwordx4 v52, s[44:47], s60 offen lds\n"
"s_add_u32 m0, s55, 24576\n"
"s_add_u32 s60, s68, 0\n"
"buffer_load_dwordx4 v52, s[44:47], s60 offen lds\n"
"s_add_u32 m0, s55, 25600\n"
"s_add_u32 s60, s68, 32768\n"
"buffer_load_dwordx4 v52, s[44:47], s60 offen lds\n"
"s_add_u32 m0, s55, 26624\n"
"s_add_u32 s60, s69, 0\n"
"buffer_load_dwordx4 v52, s[44:47], s60 offen lds\n"
"s_add_u32 m0, s55, 27648\n"
"s_add_u32 s60, s69, 32768\n"
"buffer_load_dwordx4 v52, s[44:47], s60 offen lds\n"
"s_add_u32 m0, s55, 28672\n"
"s_add_u32 s60, s70, 0\n"
"buffer_load_dwordx4 v52, s[44:47], s60 offen lds\n"
"s_add_u32 m0, s55, 29696\n"
"s_add_u32 s60, s70, 32768\n"
"buffer_load_dwordx4 v52, s[44:47], s60 offen lds\n"
"s_add_u32 m0, s55, 30720\n"
"s_add_u32 s60, s66, 0\n"
"buffer_load_dwordx4 v53, s[44:47], s60 offen lds\n"
"s_add_u32 m0, s55, 31744\n"
"s_add_u32 s60, s66, 32768\n"
"buffer_load_dwordx4 v53, s[44:47], s60 offen lds\n"
"s_add_u32 m0, s55, 32768\n"
"s_add_u32 s60, s67, 0\n"
"buffer_load_dwordx4 v53, s[44:47], s60 offen lds\n"
"s_add_u32 m0, s55, 33792\n"
"s_add_u32 s60, s67, 32768\n"
"buffer_load_dwordx4 v53, s[44:47], s60 offen lds\n"
"s_add_u32 m0, s55, 34816\n"
"s_add_u32 s60, s68, 0\n"
"buffer_load_dwordx4 v53, s[44:47], s60 offen lds\n"
"s_add_u32 m0, s55, 35840\n"
"s_add_u32 s60, s68, 32768\n"
"buffer_load_dwordx4 v53, s[44:47], s60 offen lds\n"
"s_add_u32 m0, s55, 36864\n"
"s_add_u32 s60, s69, 0\n"
"buffer_load_dwordx4 v53, s[44:47], s60 offen lds\n"
"s_add_u32 m0, s55, 37888\n"
"s_add_u32 s60, s69, 32768\n"
"buffer_load_dwordx4 v53, s[44:47], s60 offen lds\n"
"s_add_u32 m0, s55, 38912\n"
"s_add_u32 s60, s70, 0\n"
"buffer_load_dwordx4 v53, s[44:47], s60 offen lds\n"
"s_add_u32 m0, s55, 39936\n"
"s_add_u32 s60, s70, 32768\n"
"buffer_load_dwordx4 v53, s[44:47], s60 offen lds\n"
"s_waitcnt vmcnt(38)\n"
"ds_read_b64_tr_b16 a[0:1], v48 offset:0\n"
"ds_read_b64_tr_b16 a[2:3], v48 offset:256\n"
"ds_read_b64_tr_b16 a[28:29], v49 offset:0\n"
"ds_read_b64_tr_b16 a[30:31], v49 offset:256\n"
"v_add_u32 v0, 2, v0\n"
"v_add_u32 v1, 0x9e3779b9, v1\n"
"v_or_b32 v0, 1, v0\n"
"v_readfirstlane_b32 s49, v1\n"
"v_readfirstlane_b32 s48, v0\n"
"s_waitcnt vmcnt(36)\n"
"ds_read_b64_tr_b16 a[56:57], v48 offset:2048\n"
"ds_read_b64_tr_b16 a[58:59], v48 offset:2304\n"
"ds_read_b64_tr_b16 a[84:85], v49 offset:2048\n"
"ds_read_b64_tr_b16 a[86:87], v49 offset:2304\n"
"s_waitcnt lgkmcnt(4)\n"
"s_add_u32 m0, s55, 0\n"
"s_add_u32 s60, s66, 0\n"
"buffer_load_dwordx4 v54, s[44:47], s60 offen lds\n"
"s_add_u32 m0, s55, 1024\n"
"s_add_u32 s60, s66, 32768\n"
"buffer_load_dwordx4 v54, s[44:47], s60 offen lds\n"
"s_waitcnt vmcnt(36)\n"
"ds_read_b64_tr_b16 a[112:113], v48 offset:4096\n"
"ds_read_b64_tr_b16 a[114:115], v48 offset:4352\n"
"ds_read_b64_tr_b16 a[140:141], v49 offset:4096\n"
"ds_read_b64_tr_b16 a[142:143], v49 offset:4352\n"
"s_waitcnt lgkmcnt(4)\n"
"s_add_u32 m0, s55, 2048\n"
"s_add_u32 s60, s67, 0\n"
"buffer_load_dwordx4 v54, s[44:47], s60 offen lds\n"
"s_add_u32 m0, s55, 3072\n"
"s_add_u32 s60, s67, 32768\n"
"buffer_load_dwordx4 v54, s[44:47], s60 offen lds\n"
"s_waitcnt vmcnt(36)\n"
"ds_read_b64_tr_b16 a[168:169], v48 offset:6144\n"
"ds_read_b64_tr_b16 a[170:171], v48 offset:6400\n"
"ds_read_b64_tr_b16 a[196:197], v49 offset:6144\n"
"ds_read_b64_tr_b16 a[198:199], v49 offset:6400\n"
"s_waitcnt lgkmcnt(4)\n"
"s_add_u32 m0, s55, 4096\n"
"s_add_u32 s60, s68, 0\n"
"buffer_load_dwordx4 v54, s[44:47], s60 offen lds\n"
"s_add_u32 m0, s55, 5120\n"
"s_add_u32 s60, s68, 32768\n"
"buffer_load_dwordx4 v54, s[44:47], s60 offen lds\n"
"s_waitcnt vmcnt(36)\n"
"ds_read_b64_tr_b16 a[224:225], v48 offset:8192\n"
"ds_read_b64_tr_b16 a[226:227], v48 offset:8448\n"
"ds_read_b64_tr_b16 a[252:253], v49 offset:8192\n"
"ds_read_b64_tr_b16 a[254:255], v49 offset:8448\n"
"s_waitcnt lgkmcnt(4)\n"
"s_add_u32 m0, s55, 6144\n"
"s_add_u32 s60, s69, 0\n"
"buffer_load_dwordx4 v54, s[44:47], s60 offen lds\n"
"s_add_u32 m0, s55, 7168\n"
"s_add_u32 s60, s69, 32768\n"
"buffer_load_dwordx4 v54, s[44:47], s60 offen lds\n"
"s_waitcnt vmcnt(36)\n"
"ds_read_b64_tr_b16 a[4:5], v48 offset:10240\n"
"ds_read_b64_tr_b16 a[6:7], v48 offset:10496\n"
"ds_read_b64_tr_b16 a[32:33], v49 offset:10240\n"
"ds_read_b64_tr_b16 a[34:35], v49 offset:10496\n"
"s_waitcnt lgkmcnt(4)\n"
"s_add_u32 m0, s55, 8192\n"
"s_add_u32 s60, s70, 0\n"
"buffer_load_dwordx4 v54, s[44:47], s60 offen lds\n"
"s_add_u32 m0, s55, 9216\n"
"s_add_u32 s60, s70, 32768\n"
"buffer_load_dwordx4 v54, s[44:47], s60 offen lds\n"
"s_waitcnt vmcnt(36)\n"
"ds_read_b64_tr_b16 a[60:61], v48 offset:12288\n"
"ds_read_b64_tr_b16 a[62:63], v48 offset:12544\n"
"ds_read_b64_tr_b16 a[88:89], v49 offset:12288\n"
"ds_read_b64_tr_b16 a[90:91], v49 offset:12544\n"
"s_waitcnt lgkmcnt(4)\n"
"s_add_u32 m0, s55, 10240\n"
"s_add_u32 s60, s66, 0\n"
"buffer_load_dwordx4 v55, s[44:47], s60 offen lds\n"
"s_add_u32 m0, s55, 11264\n"
"s_add_u32 s60, s66, 32768\n"
"buffer_load_dwordx4 v55, s[44:47], s60 offen lds\n"
"s_waitcnt vmcnt(36)\n"
"ds_read_b64_tr_b16 a[116:117], v48 offset:14336\n"
"ds_read_b64_tr_b16 a[118:119], v48 offset:14592\n"
"ds_read_b64_tr_b16 a[144:145], v49 offset:14336\n"
"ds_read_b64_tr_b16 a[146:147], v49 offset:14592\n"
"s_waitcnt lgkmcnt(4)\n"
"s_add_u32 m0, s55, 12288\n"
"s_add_u32 s60, s67, 0\n"
"buffer_load_dwordx4 v55, s[44:47], s60 offen lds\n"
"s_add_u32 m0, s55, 13312\n"
"s_add_u32 s60, s67, 32768\n"
"buffer_load_dwordx4 v55, s[44:47], s60 offen lds\n"
"s_waitcnt vmcnt(36)\n"
"ds_read_b64_tr_b16 a[172:173], v48 offset:16384\n"
"ds_read_b64_tr_b16 a[174:175], v48 offset:16640\n"
"ds_read_b64_tr_b16 a[200:201], v49 offset:16384\n"
"ds_read_b64_tr_b16 a[202:203], v49 offset:16640\n"
"s_waitcnt lgkmcnt(4)\n"
"s_add_u32 m0, s55, 14336\n"
"s_add_u32 s60, s68, 0\n"
"buffer_load_dwordx4 v55, s[44:47], s60 offen lds\n"
"s_add_u32 m0, s55, 15360\n"
"s_add_u32 s60, s68, 32768\n"
"buffer_load_dwordx4 v55, s[44:47], s60 offen lds\n"
"s_waitcnt vmcnt(36)\n"
"ds_read_b64_tr_b16 a[228:229], v48 offset:18432\n"
"ds_read_b64_tr_b16 a[230:231], v48 offset:18688\n"
"ds_read_b64_tr_b16 v[24:25], v49 offset:18432\n"
"ds_read_b64_tr_b16 v[26:27], v49 offset:18688\n"
"s_waitcnt lgkmcnt(4)\n"
"s_add_u32 m0, s55, 16384\n"
"s_add_u32 s60, s69, 0\n"
"buffer_load_dwordx4 v55, s[44:47], s60 offen lds\n"
"s_add_u32 m0, s55, 17408\n"
"s_add_u32 s60, s69, 32768\n"
"buffer_load_dwordx4 v55, s[44:47], s60 offen lds\n"
"s_waitcnt vmcnt(36)\n"
"ds_read_b64_tr_b16 a[8:9], v48 offset:20480\n"
"ds_read_b64_tr_b16 a[10:11], v48 offset:20736\n"
"ds_read_b64_tr_b16 a[36:37], v49 offset:20480\n"
"ds_read_b64_tr_b16 a[38:39], v49 offset:20736\n"
"s_waitcnt lgkmcnt(4)\n"
"s_add_u32 m0, s55, 18432\n"
"s_add_u32 s60, s70, 0\n"
"buffer_load_dwordx4 v55, s[44:47], s60 offen lds\n"
"s_add_u32 m0, s55, 19456\n"
"s_add_u32 s60, s70, 32768\n"
"buffer_load_dwordx4 v55, s[44:47], s60 offen lds\n"
"s_waitcnt vmcnt(36)\n"
"ds_read_b64_tr_b16 a[64:65], v48 offset:22528\n"
"ds_read_b64_tr_b16 a[66:67], v48 offset:22784\n"
"ds_read_b64_tr_b16 a[92:93], v49 offset:22528\n"
"ds_read_b64_tr_b16 a[94:95], v49 offset:22784\n"
"s_waitcnt lgkmcnt(4)\n"
"s_add_u32 m0, s55, 20480\n"
"s_add_u32 s60, s66, 0\n"
"buffer_load_dwordx4 v56, s[44:47], s60 offen lds\n"
"s_add_u32 m0, s55, 21504\n"
"s_add_u32 s60, s66, 32768\n"
"buffer_load_dwordx4 v56, s[44:47], s60 offen lds\n"
"s_waitcnt vmcnt(36)\n"
"ds_read_b64_tr_b16 a[120:121], v48 offset:24576\n"
"ds_read_b64_tr_b16 a[122:123], v48 offset:24832\n"
"ds_read_b64_tr_b16 a[148:149], v49 offset:24576\n"
"ds_read_b64_tr_b16 a[150:151], v49 offset:24832\n"
"s_waitcnt lgkmcnt(4)\n"
"s_add_u32 m0, s55, 22528\n"
"s_add_u32 s60, s67, 0\n"
"buffer_load_dwordx4 v56, s[44:47], s60 offen lds\n"
"s_add_u32 m0, s55, 23552\n"
"s_add_u32 s60, s67, 32768\n"
"buffer_load_dwordx4 v56, s[44:47], s60 offen lds\n"
"s_waitcnt vmcnt(36)\n"
"ds_read_b64_tr_b16 a[176:177], v48 offset:26624\n"
"ds_read_b64_tr_b16 a[178:179], v48 offset:26880\n"
"ds_read_b64_tr_b16 a[204:205], v49 offset:26624\n"
"ds_read_b64_tr_b16 a[206:207], v49 offset:26880\n"
"s_waitcnt lgkmcnt(4)\n"
"s_add_u32 m0, s55, 24576\n"
"s_add_u32 s60, s68, 0\n"
"buffer_load_dwordx4 v56, s[44:47], s60 offen lds\n"
"s_add_u32 m0, s55, 25600\n"
"s_add_u32 s60, s68, 32768\n"
"buffer_load_dwordx4 v56, s[44:47], s60 offen lds\n"
"s_waitcnt vmcnt(36)\n"
"ds_read_b64_tr_b16 a[232:233], v48 offset:28672\n"
"ds_read_b64_tr_b16 a[234:235], v48 offset:28928\n"
"ds_read_b64_tr_b16 v[28:29], v49 offset:28672\n"
"ds_read_b64_tr_b16 v[30:31], v49 offset:28928\n"
"s_waitcnt lgkmcnt(4)\n"
"s_add_u32 m0, s55, 26624\n"
"s_add_u32 s60, s69, 0\n"
"buffer_load_dwordx4 v56, s[44:47], s60 offen lds\n"
"s_add_u32 m0, s55, 27648\n"
"s_add_u32 s60, s69, 32768\n"
"buffer_load_dwordx4 v56, s[44:47], s60 offen lds\n"
"s_waitcnt vmcnt(36)\n"
"ds_read_b64_tr_b16 a[12:13], v48 offset:30720\n"
"ds_read_b64_tr_b16 a[14:15], v48 offset:30976\n"
"ds_read_b64_tr_b16 a[40:41], v49 offset:30720\n"
"ds_read_b64_tr_b16 a[42:43], v49 offset:30976\n"
"s_waitcnt lgkmcnt(4)\n"
"s_add_u32 m0, s55, 28672\n"
"s_add_u32 s60, s70, 0\n"
"buffer_load_dwordx4 v56, s[44:47], s60 offen lds\n"
"s_add_u32 m0, s55, 29696\n"
"s_add_u32 s60, s70, 32768\n"
"buffer_load_dwordx4 v56, s[44:47], s60 offen lds\n"
"s_waitcnt vmcnt(36)\n"
"ds_read_b64_tr_b16 a[68:69], v48 offset:32768\n"
"ds_read_b64_tr_b16 a[70:71], v48 offset:33024\n"
"ds_read_b64_tr_b16 a[96:97], v49 offset:32768\n"
"ds_read_b64_tr_b16 a[98:99], v49 offset:33024\n"
"s_waitcnt lgkmcnt(4)\n"
"s_waitcnt vmcnt(34)\n"
"ds_read_b64_tr_b16 a[124:125], v48 offset:34816\n"
"ds_read_b64_tr_b16 a[126:127], v48 offset:35072\n"
"ds_read_b64_tr_b16 a[152:153], v49 offset:34816\n"
"ds_read_b64_tr_b16 a[154:155], v49 offset:35072\n"
"s_waitcnt lgkmcnt(4)\n"
"s_waitcnt vmcnt(32)\n"
"ds_read_b64_tr_b16 a[180:181], v48 offset:36864\n"
"ds_read_b64_tr_b16 a[182:183], v48 offset:37120\n"
"ds_read_b64_tr_b16 a[208:209], v49 offset:36864\n"
"ds_read_b64_tr_b16 a[210:211], v49 offset:37120\n"
"s_waitcnt lgkmcnt(4)\n"
"s_waitcnt vmcnt(30)\n"
"ds_read_b64_tr_b16 a[236:237], v48 offset:38912\n"
"ds_read_b64_tr_b16 a[238:239], v48 offset:39168\n"
"ds_read_b64_tr_b16 v[32:33], v49 offset:38912\n"
"ds_read_b64_tr_b16 v[34:35], v49 offset:39168\n"
"s_waitcnt lgkmcnt(4)\n"
"s_waitcnt vmcnt(28)\n"
"ds_read_b64_tr_b16 a[16:17], v48 offset:0\n"
"ds_read_b64_tr_b16 a[18:19], v48 offset:256\n"
"ds_read_b64_tr_b16 a[44:45], v49 offset:0\n"
"ds_read_b64_tr_b16 a[46:47], v49 offset:256\n"
"s_waitcnt lgkmcnt(4)\n"
"s_waitcnt vmcnt(26)\n"
"ds_read_b64_tr_b16 a[72:73], v48 offset:2048\n"
"ds_read_b64_tr_b16 a[74:75], v48 offset:2304\n"
"ds_read_b64_tr_b16 a[100:101], v49 offset:2048\n"
"ds_read_b64_tr_b16 a[102:103], v49 offset:2304\n"
"s_waitcnt lgkmcnt(4)\n"
"s_waitcnt vmcnt(24)\n"
"ds_read_b64_tr_b16 a[128:129], v48 offset:4096\n"
"ds_read_b64_tr_b16 a[130:131], v48 offset:4352\n"
"ds_read_b64_tr_b16 a[156:157], v49 offset:4096\n"
"ds_read_b64_tr_b16 a[158:159], v49 offset:4352\n"
"s_waitcnt lgkmcnt(4)\n"
"s_waitcnt vmcnt(22)\n"
"ds_read_b64_tr_b16 a[184:185], v48 offset:6144\n"
"ds_read_b64_tr_b16 a[186:187], v48 offset:6400\n"
"ds_read_b64_tr_b16 a[212:213], v49 offset:6144\n"
"ds_read_b64_tr_b16 a[214:215], v49 offset:6400\n"
"s_waitcnt lgkmcnt(4)\n"
"s_waitcnt vmcnt(20)\n"
"ds_read_b64_tr_b16 a[240:241], v48 offset:8192\n"
"ds_read_b64_tr_b16 a[242:243], v48 offset:8448\n"
"ds_read_b64_tr_b16 v[36:37], v49 offset:8192\n"
"ds_read_b64_tr_b16 v[38:39], v49 offset:8448\n"
"s_waitcnt lgkmcnt(4)\n"
"s_waitcnt vmcnt(18)\n"
"ds_read_b64_tr_b16 a[20:21], v48 offset:10240\n"
"ds_read_b64_tr_b16 a[22:23], v48 offset:10496\n"
"ds_read_b64_tr_b16 a[48:49], v49 offset:10240\n"
"ds_read_b64_tr_b16 a[50:51], v49 offset:10496\n"
"s_waitcnt lgkmcnt(4)\n"
"s_waitcnt vmcnt(16)\n"
"ds_read_b64_tr_b16 a[76:77], v48 offset:12288\n"
"ds_read_b64_tr_b16 a[78:79], v48 offset:12544\n"
"ds_read_b64_tr_b16 a[104:105], v49 offset:12288\n"
"ds_read_b64_tr_b16 a[106:107], v49 offset:12544\n"
"s_waitcnt lgkmcnt(4)\n"
"s_waitcnt vmcnt(14)\n"
"ds_read_b64_tr_b16 a[132:133], v48 offset:14336\n"
"ds_read_b64_tr_b16 a[134:135], v48 offset:14592\n"
"ds_read_b64_tr_b16 a[160:161], v49 offset:14336\n"
"ds_read_b64_tr_b16 a[162:163], v49 offset:14592\n"
"s_waitcnt lgkmcnt(4)\n"
"s_waitcnt vmcnt(12)\n"
"ds_read_b64_tr_b16 a[188:189], v48 offset:16384\n"
"ds_read_b64_tr_b16 a[190:191], v48 offset:16640\n"
"ds_read_b64_tr_b16 a[216:217], v49 offset:16384\n"
"ds_read_b64_tr_b16 a[218:219], v49 offset:16640\n"
"s_waitcnt lgkmcnt(4)\n"
"s_waitcnt vmcnt(10)\n"
"ds_read_b64_tr_b16 a[244:245], v48 offset:18432\n"
"ds_read_b64_tr_b16 a[246:247], v48 offset:18688\n"
"ds_read_b64_tr_b16 v[40:41], v49 offset:18432\n"
"ds_read_b64_tr_b16 v[42:43], v49 offset:18688\n"
"s_waitcnt lgkmcnt(4)\n"
"s_waitcnt vmcnt(8)\n"
"ds_read_b64_tr_b16 a[24:25], v48 offset:20480\n"
"ds_read_b64_tr_b16 a[26:27], v48 offset:20736\n"
"ds_read_b64_tr_b16 a[52:53], v49 offset:20480\n"
"ds_read_b64_tr_b16 a[54:55], v49 offset:20736\n"
"s_waitcnt lgkmcnt(4)\n"
"s_waitcnt vmcnt(6)\n"
"ds_read_b64_tr_b16 a[80:81], v48 offset:22528\n"
"ds_read_b64_tr_b16 a[82:83], v48 offset:22784\n"
"ds_read_b64_tr_b16 a[108:109], v49 offset:22528\n"
"ds_read_b64_tr_b16 a[110:111], v49 offset:22784\n"
"s_waitcnt lgkmcnt(4)\n"
"s_waitcnt vmcnt(4)\n"
"ds_read_b64_tr_b16 a[136:137], v48 offset:24576\n"
"ds_read_b64_tr_b16 a[138:139], v48 offset:24832\n"
"ds_read_b64_tr_b16 a[164:165], v49 offset:24576\n"
"ds_read_b64_tr_b16 a[166:167], v49 offset:24832\n"
"s_waitcnt lgkmcnt(4)\n"
"s_waitcnt vmcnt(2)\n"
"ds_read_b64_tr_b16 a[192:193], v48 offset:26624\n"
"ds_read_b64_tr_b16 a[194:195], v48 offset:26880\n"
"ds_read_b64_tr_b16 a[220:221], v49 offset:26624\n"
"ds_read_b64_tr_b16 a[222:223], v49 offset:26880\n"
"s_waitcnt lgkmcnt(4)\n"
"s_waitcnt vmcnt(0)\n"
"ds_read_b64_tr_b16 a[248:249], v48 offset:28672\n"
"ds_read_b64_tr_b16 a[250:251], v48 offset:28928\n"
"ds_read_b64_tr_b16 v[44:45], v49 offset:28672\n"
"ds_read_b64_tr_b16 v[46:47], v49 offset:28928\n"
"s_waitcnt lgkmcnt(4)\n"
"s_waitcnt vmcnt(0)\n"
"s_waitcnt lgkmcnt(0)\n"
"s_mov_b32 m0, s59\n"
"s_barrier\n"
"s_mov_b64 s[64:65], s[48:49]\n"
"v_mov_b32 v138, 0\n"
"v_mov_b32 v139, 0\n"
"v_mov_b32 v144, 0\n"
"v_mov_b32 v145, 0\n"
"v_mov_b32 v136, 0\n"
"v_mov_b32 v137, 0\n"
"v_mov_b32 v172, 0\n"
"v_mov_b32 v173, 0\n"
"s_mov_b32 s92, 0x80000000\n"
"s_mov_b32 s93, 0x80000000\n"
"s_mov_b32 s95, 0x80000000\n"
"s_mov_b32 s93, 0x80000000\n"
"s_mov_b32 s93, 0x80000000\n"
"s_mov_b32 s95, 0x80000000\n"
"s_mov_b32 s95, 0x80000000\n"
"s_mov_b32 s94, 0x80000000\n"
"s_mov_b32 s94, 0x80000000\n"
"v_mov_b32 v136, 0\n"
"v_mov_b32 v137, 0\n"
"s_cmp_eq_u32 s54, 0\n"
"s_cbranch_scc1 .LH_1s\n"
"s_cmp_eq_u32 s54, 1\n"
"s_cbranch_scc1 .LH_2s\n"
"s_cmp_eq_u32 s54, 2\n"
"s_cbranch_scc1 .LH_3s\n"
"s_cmp_eq_u32 s54, 3\n"
"s_cbranch_scc1 .LH_4s\n"
".p2align 6\n"
".LH_top:\n"
"v_mfma_f32_16x16x32_bf16 v[60:63], v[104:107], a[0:3], 0\n"
"v_mfma_f32_16x16x32_bf16 v[60:63], v[72:75], a[4:7], v[60:63]\n"
"global_load_dwordx4 v[48:51], v7, s[56:57] offset:0 sc1\n"
"v_mfma_f32_16x16x32_bf16 v[60:63], v[76:79], a[8:11], v[60:63]\n"
"v_mfma_f32_16x16x32_bf16 v[60:63], v[80:83], a[12:15], v[60:63]\n"
"s_waitcnt vmcnt(6)\n"
"v_mfma_f32_16x16x32_bf16 v[60:63], v[84:87], a[16:19], v[60:63]\n"
"s_waitcnt lgkmcnt(3)\n"
"v_add_f32 v100, v100, v68\n"
"v_add_f32 v101, v101, v69\n"
"v_mfma_f32_16x16x32_bf16 v[60:63], v[88:91], a[20:23], v[60:63]\n"
"s_waitcnt vmcnt(5)\n"
"v_add_f32 v102, v102, v70\n"
"v_add_f32 v103, v103, v71\n"
"v_mfma_f32_16x16x32_bf16 v[60:63], v[92:95], a[24:27], v[60:63]\n"
"global_load_dwordx4 v[52:55], v8, s[56:57] offset:0 sc1\n"
"v_add_f32 v100, v100, v96\n"
"v_mfma_f32_16x16x32_bf16 v[64:67], v[104:107], a[28:31], 0\n"
"buffer_load_dwordx2 v[136:137], v12, s[40:43], s50 offen sc1\n"
"v_mfma_f32_16x16x32_bf16 v[64:67], v[72:75], a[32:35], v[64:67]\n"
"ds_write_addtid_b32 v60 offset:0\n"
"v_add_f32 v101, v101, v97\n"
"v_mfma_f32_16x16x32_bf16 v[64:67], v[76:79], a[36:39], v[64:67]\n"
"ds_write_addtid_b32 v61 offset:4640\n"
"global_load_dwordx4 v[56:59], v9, s[56:57] offset:0 sc1\n"
"v_mfma_f32_16x16x32_bf16 v[64:67], v[80:83], a[40:43], v[64:67]\n"
"ds_write_addtid_b32 v62 offset:9344\n"
"v_add_f32 v102, v102, v98\n"
"v_mfma_f32_16x16x32_bf16 v[64:67], v[84:87], a[44:47], v[64:67]\n"
"ds_write_addtid_b32 v63 offset:13984\n"
"v_add_f32 v103, v103, v99\n"
"v_mfma_f32_16x16x32_bf16 v[64:67], v[88:91], a[48:51], v[64:67]\n"
"v_add_f32 v100, v100, v116\n"
"v_add_f32 v101, v101, v117\n"
"v_mfma_f32_16x16x32_bf16 v[64:67], v[92:95], a[52:55], v[64:67]\n"
"v_add_f32 v102, v102, v118\n"
"v_add_f32 v103, v103, v119\n"
"v_mfma_f32_16x16x32_bf16 v[68:71], v[104:107], a[56:59], 0\n"
"s_waitcnt vmcnt(7)\n"
"buffer_store_dwordx2 v[0:1], v14, s[40:43], s93 offen offset:0 sc1\n"
"v_mfma_f32_16x16x32_bf16 v[68:71], v[72:75], a[60:63], v[68:71]\n"
"s_add_u32 s95, s50, 0xffd00000\n"
"s_and_b32 s94, s95, 0xfbffffff\n"
"ds_write_addtid_b32 v64 offset:256\n"
"v_mfma_f32_16x16x32_bf16 v[68:71], v[76:79], a[64:67], v[68:71]\n"
"ds_write_addtid_b32 v65 offset:4896\n"
"global_load_dwordx4 v[60:63], v10, s[56:57] offset:0 sc1\n"
"v_mfma_f32_16x16x32_bf16 v[68:71], v[80:83], a[68:71], v[68:71]\n"
"ds_write_addtid_b32 v66 offset:9600\n"
"v_cvt_pk_bf16_f32 v96, v100, v101\n"
"v_mfma_f32_16x16x32_bf16 v[68:71], v[84:87], a[72:75], v[68:71]\n"
"ds_write_addtid_b32 v67 offset:14240\n"
"v_cvt_pk_bf16_f32 v97, v102, v103\n"
"v_mfma_f32_16x16x32_bf16 v[68:71], v[88:91], a[76:79], v[68:71]\n"
"s_waitcnt lgkmcnt(8)\n"
"v_add_f32 v124, v124, v128\n"
"v_add_f32 v125, v125, v129\n"
"v_mfma_f32_16x16x32_bf16 v[68:71], v[92:95], a[80:83], v[68:71]\n"
"v_add_f32 v126, v126, v130\n"
"v_add_f32 v127, v127, v131\n"
"v_mfma_f32_16x16x32_bf16 v[100:103], v[104:107], a[84:87], 0\n"
"global_load_dwordx4 v[64:67], v11, s[56:57] offset:0 sc1\n"
"v_add_f32 v124, v124, v132\n"
"v_mfma_f32_16x16x32_bf16 v[100:103], v[72:75], a[88:91], v[100:103]\n"
"ds_write_addtid_b32 v68 offset:512\n"
"v_add_f32 v125, v125, v133\n"
"v_mfma_f32_16x16x32_bf16 v[100:103], v[76:79], a[92:95], v[100:103]\n"
"ds_write_addtid_b32 v69 offset:5152\n"
"v_add_f32 v126, v126, v134\n"
"v_mfma_f32_16x16x32_bf16 v[100:103], v[80:83], a[96:99], v[100:103]\n"
"ds_write_addtid_b32 v70 offset:9856\n"
"v_add_f32 v127, v127, v135\n"
"v_mfma_f32_16x16x32_bf16 v[100:103], v[84:87], a[100:103], v[100:103]\n"
"ds_write_addtid_b32 v71 offset:14496\n"
"v_add_f32 v124, v124, v120\n"
"v_mfma_f32_16x16x32_bf16 v[100:103], v[88:91], a[104:107], v[100:103]\n"
"v_add_f32 v125, v125, v121\n"
"v_add_f32 v126, v126, v122\n"
"v_mfma_f32_16x16x32_bf16 v[100:103], v[92:95], a[108:111], v[100:103]\n"
"v_add_f32 v127, v127, v123\n"
"v_cvt_pk_bf16_f32 v98, v124, v125\n"
"v_mfma_f32_16x16x32_bf16 v[68:71], v[104:107], a[112:115], 0\n"
"v_cvt_pk_bf16_f32 v99, v126, v127\n"
"v_mfma_f32_16x16x32_bf16 v[68:71], v[72:75], a[116:119], v[68:71]\n"
"ds_write_addtid_b32 v100 offset:768\n"
"v_mfma_f32_16x16x32_bf16 v[68:71], v[76:79], a[120:123], v[68:71]\n"
"ds_write_addtid_b32 v101 offset:5408\n"
"v_mfma_f32_16x16x32_bf16 v[68:71], v[80:83], a[124:127], v[68:71]\n"
"ds_write_addtid_b32 v102 offset:10112\n"
"v_mfma_f32_16x16x32_bf16 v[68:71], v[84:87], a[128:131], v[68:71]\n"
"ds_write_addtid_b32 v103 offset:14752\n"
"v_mfma_f32_16x16x32_bf16 v[68:71], v[88:91], a[132:135], v[68:71]\n"
"v_mfma_f32_16x16x32_bf16 v[68:71], v[92:95], a[136:139], v[68:71]\n"
"v_mfma_f32_16x16x32_bf16 v[116:119], v[104:107], a[140:143], 0\n"
"v_mfma_f32_16x16x32_bf16 v[116:119], v[72:75], a[144:147], v[116:119]\n"
"ds_write_addtid_b32 v68 offset:1024\n"
"v_mfma_f32_16x16x32_bf16 v[116:119], v[76:79], a[148:151], v[116:119]\n"
"ds_write_addtid_b32 v69 offset:5664\n"
"v_mfma_f32_16x16x32_bf16 v[116:119], v[80:83], a[152:155], v[116:119]\n"
"ds_write_addtid_b32 v70 offset:10368\n"
"v_mfma_f32_16x16x32_bf16 v[116:119], v[84:87], a[156:159], v[116:119]\n"
"ds_write_addtid_b32 v71 offset:15008\n"
"v_mfma_f32_16x16x32_bf16 v[116:119], v[88:91], a[160:163], v[116:119]\n"
"buffer_store_dwordx4 v[96:99], v13, s[40:43], s94 offen sc1\n"
"v_mfma_f32_16x16x32_bf16 v[116:119], v[92:95], a[164:167], v[116:119]\n"
"v_mfma_f32_16x16x32_bf16 v[100:103], a[168:171], v[104:107], 0\n"
"v_mfma_f32_16x16x32_bf16 v[100:103], a[172:175], v[72:75], v[100:103]\n"
"ds_write_addtid_b32 v116 offset:1280\n"
"v_mfma_f32_16x16x32_bf16 v[100:103], a[176:179], v[76:79], v[100:103]\n"
"ds_write_addtid_b32 v117 offset:5920\n"
"v_mfma_f32_16x16x32_bf16 v[100:103], a[180:183], v[80:83], v[100:103]\n"
"ds_write_addtid_b32 v118 offset:10624\n"
"v_mfma_f32_16x16x32_bf16 v[100:103], a[184:187], v[84:87], v[100:103]\n"
"ds_write_addtid_b32 v119 offset:15264\n"
"v_mfma_f32_16x16x32_bf16 v[100:103], a[188:191], v[88:91], v[100:103]\n"
"v_mfma_f32_16x16x32_bf16 v[100:103], a[192:195], v[92:95], v[100:103]\n"
"v_mfma_f32_16x16x32_bf16 v[96:99], a[196:199], v[104:107], 0\n"
"v_mfma_f32_16x16x32_bf16 v[96:99], a[200:203], v[72:75], v[96:99]\n"
"v_mfma_f32_16x16x32_bf16 v[96:99], a[204:207], v[76:79], v[96:99]\n"
"v_mfma_f32_16x16x32_bf16 v[96:99], a[208:211], v[80:83], v[96:99]\n"
"s_add_u32 s58, s50, 0x180000\n"
"v_mfma_f32_16x16x32_bf16 v[96:99], a[212:215], v[84:87], v[96:99]\n"
"s_and_b32 s58, s58, 0x3ffffff\n"
"v_mfma_f32_16x16x32_bf16 v[96:99], a[216:219], v[88:91], v[96:99]\n"
"s_add_u32 s56, s40, s58\n"
"v_mfma_f32_16x16x32_bf16 v[96:99], a[220:223], v[92:95], v[96:99]\n"
"s_addc_u32 s57, s41, 0\n"
"v_mfma_f32_16x16x32_bf16 v[116:119], a[224:227], v[104:107], 0\n"
"s_waitcnt vmcnt(5)\n"
"v_cmp_ne_u64 vcc, s[48:49], v[136:137]\n"
"v_mfma_f32_16x16x32_bf16 v[116:119], a[228:231], v[72:75], v[116:119]\n"
"v_mfma_f32_16x16x32_bf16 v[116:119], a[232:235], v[76:79], v[116:119]\n"
"s_cbranch_vccnz .LH_3s\n"
".LH_3r:\n"
"v_mfma_f32_16x16x32_bf16 v[116:119], a[236:239], v[80:83], v[116:119]\n"
"s_add_u32 s50, s50, 0x180000\n"
"global_load_dwordx4 v[68:71], v5, s[56:57] offset:0 sc1\n"
"v_mfma_f32_16x16x32_bf16 v[116:119], a[240:243], v[84:87], v[116:119]\n"
"v_mfma_f32_16x16x32_bf16 v[116:119], a[244:247], v[88:91], v[116:119]\n"
"v_mfma_f32_16x16x32_bf16 v[116:119], a[248:251], v[92:95], v[116:119]\n"
"v_mfma_f32_16x16x32_bf16 v[120:123], a[252:255], v[104:107], 0\n"
"v_mfma_f32_16x16x32_bf16 v[120:123], v[24:27], v[72:75], v[120:123]\n"
"s_waitcnt lgkmcnt(0)\n"
"v_mfma_f32_16x16x32_bf16 v[120:123], v[28:31], v[76:79], v[120:123]\n"
"s_barrier\n"
"v_mfma_f32_16x16x32_bf16 v[120:123], v[32:35], v[80:83], v[120:123]\n"
"ds_read_b128 v[104:107], v2 offset:2048\n"
"ds_read_b128 v[124:127], v3 offset:512\n"
"v_mfma_f32_16x16x32_bf16 v[120:123], v[36:39], v[84:87], v[120:123]\n"
"global_load_dwordx4 v[72:75], v6, s[56:57] offset:0 sc1\n"
"ds_read_b128 v[128:131], v4 offset:3072\n"
"v_mfma_f32_16x16x32_bf16 v[120:123], v[40:43], v[88:91], v[120:123]\n"
"ds_read_b128 v[132:135], v2 offset:2304\n"
"ds_read_b128 v[136:139], v3 offset:768\n"
"v_mfma_f32_16x16x32_bf16 v[120:123], v[44:47], v[92:95], v[120:123]\n"
"global_load_dwordx4 v[76:79], v7, s[56:57] offset:0 sc1\n"
"ds_read_b128 v[140:143], v4 offset:3328\n"
"v_mfma_f32_16x16x32_bf16 v[88:91], v[108:111], a[0:3], 0\n"
"ds_read_b128 v[144:147], v2 offset:3584\n"
"ds_read_b128 v[148:151], v3 offset:1536\n"
"v_mfma_f32_16x16x32_bf16 v[88:91], v[112:115], a[4:7], v[88:91]\n"
"ds_read_b128 v[152:155], v4 offset:3584\n"
"ds_read_b128 v[156:159], v2 offset:3840\n"
"v_mfma_f32_16x16x32_bf16 v[88:91], v[48:51], a[8:11], v[88:91]\n"
"ds_read_b128 v[160:163], v3 offset:1792\n"
"ds_read_b128 v[164:167], v4 offset:3840\n"
"v_mfma_f32_16x16x32_bf16 v[88:91], v[52:55], a[12:15], v[88:91]\n"
"s_waitcnt vmcnt(5)\n"
"v_mfma_f32_16x16x32_bf16 v[88:91], v[56:59], a[16:19], v[88:91]\n"
"s_waitcnt lgkmcnt(9)\n"
"global_load_dwordx4 v[80:83], v8, s[56:57] offset:0 sc1\n"
"v_add_f32 v104, v104, v124\n"
"v_mfma_f32_16x16x32_bf16 v[88:91], v[60:63], a[20:23], v[88:91]\n"
"s_waitcnt vmcnt(5)\n"
"v_add_f32 v105, v105, v125\n"
"v_add_f32 v106, v106, v126\n"
"v_mfma_f32_16x16x32_bf16 v[88:91], v[64:67], a[24:27], v[88:91]\n"
"s_add_u32 s94, s50, 0xff000000\n"
"v_add_f32 v107, v107, v127\n"
"v_add_f32 v104, v104, v128\n"
"v_mfma_f32_16x16x32_bf16 v[168:171], v[108:111], a[28:31], 0\n"
"buffer_load_dwordx2 v[172:173], v12, s[40:43], s50 offen sc1\n"
"v_mfma_f32_16x16x32_bf16 v[168:171], v[112:115], a[32:35], v[168:171]\n"
"ds_write_addtid_b32 v88 offset:2048\n"
"v_add_f32 v105, v105, v129\n"
"v_mfma_f32_16x16x32_bf16 v[168:171], v[48:51], a[36:39], v[168:171]\n"
"ds_write_addtid_b32 v89 offset:6688\n"
"v_add_f32 v106, v106, v130\n"
"v_mfma_f32_16x16x32_bf16 v[168:171], v[52:55], a[40:43], v[168:171]\n"
"ds_write_addtid_b32 v90 offset:11392\n"
"v_add_f32 v107, v107, v131\n"
"v_mfma_f32_16x16x32_bf16 v[168:171], v[56:59], a[44:47], v[168:171]\n"
"ds_write_addtid_b32 v91 offset:16032\n"
"v_add_f32 v104, v104, v100\n"
"v_mfma_f32_16x16x32_bf16 v[168:171], v[60:63], a[48:51], v[168:171]\n"
"global_load_dwordx4 v[84:87], v9, s[56:57] offset:0 sc1\n"
"v_add_f32 v105, v105, v101\n"
"v_mfma_f32_16x16x32_bf16 v[168:171], v[64:67], a[52:55], v[168:171]\n"
"v_add_f32 v106, v106, v102\n"
"v_add_f32 v107, v107, v103\n"
"v_mfma_f32_16x16x32_bf16 v[124:127], v[108:111], a[56:59], 0\n"
"s_add_u32 s92, s50, 0xffd00000\n"
"s_and_b32 s93, s92, 0xfbffffff\n"
"v_cvt_pk_bf16_f32 v128, v104, v105\n"
"v_mfma_f32_16x16x32_bf16 v[124:127], v[112:115], a[60:63], v[124:127]\n"
"ds_write_addtid_b32 v168 offset:2304\n"
"v_cvt_pk_bf16_f32 v129, v106, v107\n"
"v_mfma_f32_16x16x32_bf16 v[124:127], v[48:51], a[64:67], v[124:127]\n"
"s_waitcnt lgkmcnt(11)\n"
"ds_write_addtid_b32 v169 offset:6944\n"
"v_add_f32 v132, v132, v136\n"
"v_mfma_f32_16x16x32_bf16 v[124:127], v[52:55], a[68:71], v[124:127]\n"
"ds_write_addtid_b32 v170 offset:11648\n"
"v_add_f32 v133, v133, v137\n"
"v_mfma_f32_16x16x32_bf16 v[124:127], v[56:59], a[72:75], v[124:127]\n"
"s_waitcnt vmcnt(6)\n"
"ds_write_addtid_b32 v171 offset:16288\n"
"global_load_dwordx4 v[88:91], v10, s[56:57] offset:0 sc1\n"
"v_mfma_f32_16x16x32_bf16 v[124:127], v[60:63], a[76:79], v[124:127]\n"
"global_load_dwordx4 v[92:95], v11, s[56:57] offset:0 sc1\n"
"v_add_f32 v134, v134, v138\n"
"v_mfma_f32_16x16x32_bf16 v[124:127], v[64:67], a[80:83], v[124:127]\n"
"v_add_f32 v135, v135, v139\n"
"v_add_f32 v132, v132, v140\n"
"v_mfma_f32_16x16x32_bf16 v[100:103], v[108:111], a[84:87], 0\n"
"v_add_f32 v133, v133, v141\n"
"v_add_f32 v134, v134, v142\n"
"v_mfma_f32_16x16x32_bf16 v[100:103], v[112:115], a[88:91], v[100:103]\n"
"ds_write_addtid_b32 v124 offset:2560\n"
"v_add_f32 v135, v135, v143\n"
"v_mfma_f32_16x16x32_bf16 v[100:103], v[48:51], a[92:95], v[100:103]\n"
"ds_write_addtid_b32 v125 offset:7200\n"
"v_add_f32 v132, v132, v96\n"
"v_mfma_f32_16x16x32_bf16 v[100:103], v[52:55], a[96:99], v[100:103]\n"
"ds_write_addtid_b32 v126 offset:11904\n"
"v_add_f32 v133, v133, v97\n"
"v_mfma_f32_16x16x32_bf16 v[100:103], v[56:59], a[100:103], v[100:103]\n"
"ds_write_addtid_b32 v127 offset:16544\n"
"v_add_f32 v134, v134, v98\n"
"v_mfma_f32_16x16x32_bf16 v[100:103], v[60:63], a[104:107], v[100:103]\n"
"v_add_f32 v135, v135, v99\n"
"v_cvt_pk_bf16_f32 v130, v132, v133\n"
"v_mfma_f32_16x16x32_bf16 v[100:103], v[64:67], a[108:111], v[100:103]\n"
"buffer_store_dwordx2 v[0:1], v14, s[40:43], s95 offen offset:0 sc1\n"
"v_cvt_pk_bf16_f32 v131, v134, v135\n"
"v_mfma_f32_16x16x32_bf16 v[96:99], v[108:111], a[112:115], 0\n"
"buffer_store_dwordx4 v[128:131], v13, s[40:43], s93 offen sc1\n"
"v_mfma_f32_16x16x32_bf16 v[96:99], v[112:115], a[116:119], v[96:99]\n"
"s_waitcnt lgkmcnt(15)\n"
"ds_write_addtid_b32 v100 offset:2816\n"
"v_add_f32 v144, v144, v148\n"
"v_mfma_f32_16x16x32_bf16 v[96:99], v[48:51], a[120:123], v[96:99]\n"
"ds_write_addtid_b32 v101 offset:7456\n"
"v_add_f32 v145, v145, v149\n"
"v_mfma_f32_16x16x32_bf16 v[96:99], v[52:55], a[124:127], v[96:99]\n"
"ds_write_addtid_b32 v102 offset:12160\n"
"v_add_f32 v146, v146, v150\n"
"v_mfma_f32_16x16x32_bf16 v[96:99], v[56:59], a[128:131], v[96:99]\n"
"ds_write_addtid_b32 v103 offset:16800\n"
"v_add_f32 v147, v147, v151\n"
"v_mfma_f32_16x16x32_bf16 v[96:99], v[60:63], a[132:135], v[96:99]\n"
"v_add_f32 v144, v144, v152\n"
"v_add_f32 v145, v145, v153\n"
"v_mfma_f32_16x16x32_bf16 v[96:99], v[64:67], a[136:139], v[96:99]\n"
"v_add_f32 v146, v146, v154\n"
"v_add_f32 v147, v147, v155\n"
"v_mfma_f32_16x16x32_bf16 v[100:103], v[108:111], a[140:143], 0\n"
"v_add_f32 v144, v144, v116\n"
"v_add_f32 v145, v145, v117\n"
"v_mfma_f32_16x16x32_bf16 v[100:103], v[112:115], a[144:147], v[100:103]\n"
"ds_write_addtid_b32 v96 offset:3072\n"
"v_add_f32 v146, v146, v118\n"
"v_mfma_f32_16x16x32_bf16 v[100:103], v[48:51], a[148:151], v[100:103]\n"
"ds_write_addtid_b32 v97 offset:7712\n"
"v_add_f32 v147, v147, v119\n"
"v_mfma_f32_16x16x32_bf16 v[100:103], v[52:55], a[152:155], v[100:103]\n"
"ds_write_addtid_b32 v98 offset:12416\n"
"v_cvt_pk_bf16_f32 v104, v144, v145\n"
"v_mfma_f32_16x16x32_bf16 v[100:103], v[56:59], a[156:159], v[100:103]\n"
"ds_write_addtid_b32 v99 offset:17056\n"
"v_cvt_pk_bf16_f32 v105, v146, v147\n"
"v_mfma_f32_16x16x32_bf16 v[100:103], v[60:63], a[160:163], v[100:103]\n"
"s_waitcnt lgkmcnt(15)\n"
"v_add_f32 v156, v156, v160\n"
"v_add_f32 v157, v157, v161\n"
"v_mfma_f32_16x16x32_bf16 v[100:103], v[64:67], a[164:167], v[100:103]\n"
"v_add_f32 v158, v158, v162\n"
"v_add_f32 v159, v159, v163\n"
"v_mfma_f32_16x16x32_bf16 v[96:99], v[108:111], a[224:227], 0\n"
"v_add_f32 v156, v156, v164\n"
"v_add_f32 v157, v157, v165\n"
"v_mfma_f32_16x16x32_bf16 v[96:99], v[112:115], a[228:231], v[96:99]\n"
"ds_write_addtid_b32 v100 offset:3328\n"
"v_add_f32 v158, v158, v166\n"
"v_mfma_f32_16x16x32_bf16 v[96:99], v[48:51], a[232:235], v[96:99]\n"
"ds_write_addtid_b32 v101 offset:7968\n"
"v_add_f32 v159, v159, v167\n"
"v_mfma_f32_16x16x32_bf16 v[96:99], v[52:55], a[236:239], v[96:99]\n"
"ds_write_addtid_b32 v102 offset:12672\n"
"v_add_f32 v156, v156, v120\n"
"v_mfma_f32_16x16x32_bf16 v[96:99], v[56:59], a[240:243], v[96:99]\n"
"ds_write_addtid_b32 v103 offset:17312\n"
"v_add_f32 v157, v157, v121\n"
"v_mfma_f32_16x16x32_bf16 v[96:99], v[60:63], a[244:247], v[96:99]\n"
"v_add_f32 v158, v158, v122\n"
"v_add_f32 v159, v159, v123\n"
"v_mfma_f32_16x16x32_bf16 v[96:99], v[64:67], a[248:251], v[96:99]\n"
"v_cvt_pk_bf16_f32 v106, v156, v157\n"
"v_cvt_pk_bf16_f32 v107, v158, v159\n"
"v_mfma_f32_16x16x32_bf16 v[100:103], v[108:111], a[252:255], 0\n"
"buffer_store_dwordx4 v[104:107], v15, s[40:43], s93 offen sc1\n"
"v_mfma_f32_16x16x32_bf16 v[100:103], v[112:115], v[24:27], v[100:103]\n"
"s_cmp_lt_u32 s94, 0x8000000\n"
"s_cselect_b32 s94, 0x80000000, s94\n"
"ds_write_addtid_b32 v96 offset:3584\n"
"v_mfma_f32_16x16x32_bf16 v[100:103], v[48:51], v[28:31], v[100:103]\n"
"ds_write_addtid_b32 v97 offset:8224\n"
"v_mfma_f32_16x16x32_bf16 v[100:103], v[52:55], v[32:35], v[100:103]\n"
"ds_write_addtid_b32 v98 offset:12928\n"
"v_mfma_f32_16x16x32_bf16 v[100:103], v[56:59], v[36:39], v[100:103]\n"
"ds_write_addtid_b32 v99 offset:17568\n"
"v_mfma_f32_16x16x32_bf16 v[100:103], v[60:63], v[40:43], v[100:103]\n"
"buffer_load_dwordx2 v[136:137], v17, s[40:43], s94 offen offset:0 sc1\n"
"v_mfma_f32_16x16x32_bf16 v[100:103], v[64:67], v[44:47], v[100:103]\n"
"s_add_u32 s58, s50, 0x180000\n"
"v_mfma_f32_16x16x32_bf16 v[104:107], a[168:171], v[108:111], 0\n"
"s_and_b32 s58, s58, 0x3ffffff\n"
"v_mfma_f32_16x16x32_bf16 v[104:107], a[172:175], v[112:115], v[104:107]\n"
"s_add_u32 s56, s40, s58\n"
"ds_write_addtid_b32 v100 offset:3840\n"
"v_mfma_f32_16x16x32_bf16 v[104:107], a[176:179], v[48:51], v[104:107]\n"
"s_addc_u32 s57, s41, 0\n"
"ds_write_addtid_b32 v101 offset:8480\n"
"v_mfma_f32_16x16x32_bf16 v[104:107], a[180:183], v[52:55], v[104:107]\n"
"s_waitcnt vmcnt(7)\n"
"ds_write_addtid_b32 v102 offset:13184\n"
"v_cmp_ne_u64 vcc, s[48:49], v[172:173]\n"
"v_mfma_f32_16x16x32_bf16 v[104:107], a[184:187], v[56:59], v[104:107]\n"
"ds_write_addtid_b32 v103 offset:17824\n"
"v_mfma_f32_16x16x32_bf16 v[104:107], a[188:191], v[60:63], v[104:107]\n"
"s_cbranch_vccnz .LH_4s\n"
".LH_4r:\n"
"v_mfma_f32_16x16x32_bf16 v[104:107], a[192:195], v[64:67], v[104:107]\n"
"s_add_u32 s50, s50, 0x180000\n"
"global_load_dwordx4 v[96:99], v5, s[56:57] offset:0 sc1\n"
"v_mfma_f32_16x16x32_bf16 v[116:119], a[196:199], v[108:111], 0\n"
"global_load_dwordx4 v[100:103], v6, s[56:57] offset:0 sc1\n"
"v_mfma_f32_16x16x32_bf16 v[116:119], a[200:203], v[112:115], v[116:119]\n"
"s_waitcnt lgkmcnt(0)\n"
"v_mfma_f32_16x16x32_bf16 v[116:119], a[204:207], v[48:51], v[116:119]\n"
"s_barrier\n"
"v_mfma_f32_16x16x32_bf16 v[116:119], a[208:211], v[52:55], v[116:119]\n"
"ds_read_b128 v[108:111], v2 offset:0\n"
"ds_read_b128 v[112:115], v3 offset:2560\n"
"v_mfma_f32_16x16x32_bf16 v[116:119], a[212:215], v[56:59], v[116:119]\n"
"ds_read_b128 v[120:123], v4 offset:1024\n"
"ds_read_b128 v[124:127], v2 offset:256\n"
"v_mfma_f32_16x16x32_bf16 v[116:119], a[216:219], v[60:63], v[116:119]\n"
"ds_read_b128 v[128:131], v3 offset:2816\n"
"ds_read_b128 v[132:135], v4 offset:1280\n"
"v_mfma_f32_16x16x32_bf16 v[116:119], a[220:223], v[64:67], v[116:119]\n"
"v_mfma_f32_16x16x32_bf16 v[56:59], v[68:71], a[0:3], 0\n"
"v_mfma_f32_16x16x32_bf16 v[56:59], v[72:75], a[4:7], v[56:59]\n"
"v_mfma_f32_16x16x32_bf16 v[56:59], v[76:79], a[8:11], v[56:59]\n"
"global_load_dwordx4 v[48:51], v7, s[56:57] offset:0 sc1\n"
"v_mfma_f32_16x16x32_bf16 v[56:59], v[80:83], a[12:15], v[56:59]\n"
"s_waitcnt vmcnt(8)\n"
"buffer_load_dwordx2 v[138:139], v12, s[40:43], s50 offen sc1\n"
"v_mfma_f32_16x16x32_bf16 v[56:59], v[84:87], a[16:19], v[56:59]\n"
"s_waitcnt lgkmcnt(3)\n"
"v_add_f32 v108, v108, v112\n"
"v_add_f32 v109, v109, v113\n"
"v_mfma_f32_16x16x32_bf16 v[56:59], v[88:91], a[20:23], v[56:59]\n"
"s_waitcnt vmcnt(8)\n"
"v_add_f32 v110, v110, v114\n"
"v_add_f32 v111, v111, v115\n"
"v_mfma_f32_16x16x32_bf16 v[56:59], v[92:95], a[24:27], v[56:59]\n"
"v_add_f32 v108, v108, v120\n"
"v_add_f32 v109, v109, v121\n"
"v_mfma_f32_16x16x32_bf16 v[64:67], v[68:71], a[28:31], 0\n"
"global_load_dwordx4 v[52:55], v8, s[56:57] offset:0 sc1\n"
"v_add_f32 v110, v110, v122\n"
"v_mfma_f32_16x16x32_bf16 v[64:67], v[72:75], a[32:35], v[64:67]\n"
"ds_write_addtid_b32 v56 offset:0\n"
"v_add_f32 v111, v111, v123\n"
"v_mfma_f32_16x16x32_bf16 v[64:67], v[76:79], a[36:39], v[64:67]\n"
"ds_write_addtid_b32 v57 offset:4640\n"
"v_add_f32 v108, v108, v104\n"
"v_mfma_f32_16x16x32_bf16 v[64:67], v[80:83], a[40:43], v[64:67]\n"
"ds_write_addtid_b32 v58 offset:9344\n"
"v_add_f32 v109, v109, v105\n"
"v_mfma_f32_16x16x32_bf16 v[64:67], v[84:87], a[44:47], v[64:67]\n"
"ds_write_addtid_b32 v59 offset:13984\n"
"v_add_f32 v110, v110, v106\n"
"v_mfma_f32_16x16x32_bf16 v[64:67], v[88:91], a[48:51], v[64:67]\n"
"v_add_f32 v111, v111, v107\n"
"v_mfma_f32_16x16x32_bf16 v[64:67], v[92:95], a[52:55], v[64:67]\n"
"s_add_u32 s95, s50, 0xffd00000\n"
"s_and_b32 s93, s95, 0xfbffffff\n"
"global_load_dwordx4 v[56:59], v9, s[56:57] offset:0 sc1\n"
"v_mfma_f32_16x16x32_bf16 v[104:107], v[68:71], a[56:59], 0\n"
"v_cvt_pk_bf16_f32 v112, v108, v109\n"
"v_cvt_pk_bf16_f32 v113, v110, v111\n"
"v_mfma_f32_16x16x32_bf16 v[104:107], v[72:75], a[60:63], v[104:107]\n"
"s_waitcnt lgkmcnt(4)\n"
"ds_write_addtid_b32 v64 offset:256\n"
"v_add_f32 v124, v124, v128\n"
"v_mfma_f32_16x16x32_bf16 v[104:107], v[76:79], a[64:67], v[104:107]\n"
"ds_write_addtid_b32 v65 offset:4896\n"
"v_add_f32 v125, v125, v129\n"
"v_mfma_f32_16x16x32_bf16 v[104:107], v[80:83], a[68:71], v[104:107]\n"
"ds_write_addtid_b32 v66 offset:9600\n"
"global_load_dwordx4 v[60:63], v10, s[56:57] offset:0 sc1\n"
"v_mfma_f32_16x16x32_bf16 v[104:107], v[84:87], a[72:75], v[104:107]\n"
"ds_write_addtid_b32 v67 offset:14240\n"
"v_add_f32 v126, v126, v130\n"
"v_mfma_f32_16x16x32_bf16 v[104:107], v[88:91], a[76:79], v[104:107]\n"
"v_add_f32 v127, v127, v131\n"
"v_add_f32 v124, v124, v132\n"
"v_mfma_f32_16x16x32_bf16 v[104:107], v[92:95], a[80:83], v[104:107]\n"
"v_add_f32 v125, v125, v133\n"
"v_add_f32 v126, v126, v134\n"
"v_mfma_f32_16x16x32_bf16 v[108:111], v[68:71], a[84:87], 0\n"
"v_add_f32 v127, v127, v135\n"
"v_add_f32 v124, v124, v116\n"
"v_mfma_f32_16x16x32_bf16 v[108:111], v[72:75], a[88:91], v[108:111]\n"
"ds_write_addtid_b32 v104 offset:512\n"
"v_add_f32 v125, v125, v117\n"
"v_mfma_f32_16x16x32_bf16 v[108:111], v[76:79], a[92:95], v[108:111]\n"
"ds_write_addtid_b32 v105 offset:5152\n"
"global_load_dwordx4 v[64:67], v11, s[56:57] offset:0 sc1\n"
"v_mfma_f32_16x16x32_bf16 v[108:111], v[80:83], a[96:99], v[108:111]\n"
"ds_write_addtid_b32 v106 offset:9856\n"
"v_add_f32 v126, v126, v118\n"
"v_mfma_f32_16x16x32_bf16 v[108:111], v[84:87], a[100:103], v[108:111]\n"
"ds_write_addtid_b32 v107 offset:14496\n"
"v_add_f32 v127, v127, v119\n"
"v_mfma_f32_16x16x32_bf16 v[108:111], v[88:91], a[104:107], v[108:111]\n"
"v_cvt_pk_bf16_f32 v114, v124, v125\n"
"v_cvt_pk_bf16_f32 v115, v126, v127\n"
"v_mfma_f32_16x16x32_bf16 v[108:111], v[92:95], a[108:111], v[108:111]\n"
"s_waitcnt vmcnt(9)\n"
"buffer_store_dwordx2 v[0:1], v16, s[40:43], s92 offen offset:0 sc1\n"
"v_mfma_f32_16x16x32_bf16 v[104:107], v[68:71], a[112:115], 0\n"
"v_mfma_f32_16x16x32_bf16 v[104:107], v[72:75], a[116:119], v[104:107]\n"
"ds_write_addtid_b32 v108 offset:768\n"
"v_mfma_f32_16x16x32_bf16 v[104:107], v[76:79], a[120:123], v[104:107]\n"
"buffer_store_dwordx4 v[112:115], v13, s[40:43], s93 offen sc1\n"
"ds_write_addtid_b32 v109 offset:5408\n"
"v_mfma_f32_16x16x32_bf16 v[104:107], v[80:83], a[124:127], v[104:107]\n"
"ds_write_addtid_b32 v110 offset:10112\n"
"v_mfma_f32_16x16x32_bf16 v[104:107], v[84:87], a[128:131], v[104:107]\n"
"s_waitcnt vmcnt(10)\n"
"ds_write_addtid_b32 v111 offset:14752\n"
"v_cmp_ne_u64 vcc, s[64:65], v[136:137]\n"
"v_mfma_f32_16x16x32_bf16 v[104:107], v[88:91], a[132:135], v[104:107]\n"
"v_mfma_f32_16x16x32_bf16 v[104:107], v[92:95], a[136:139], v[104:107]\n"
"s_cbranch_vccnz .LH_5s\n"
".LH_5r:\n"
"v_mfma_f32_16x16x32_bf16 v[108:111], v[68:71], a[140:143], 0\n"
"buffer_load_dwordx4 v[112:115], v18, s[40:43], s94 offen offset:0 sc1\n"
"v_mfma_f32_16x16x32_bf16 v[108:111], v[72:75], a[144:147], v[108:111]\n"
"ds_write_addtid_b32 v104 offset:1024\n"
"v_mfma_f32_16x16x32_bf16 v[108:111], v[76:79], a[148:151], v[108:111]\n"
"ds_write_addtid_b32 v105 offset:5664\n"
"v_mfma_f32_16x16x32_bf16 v[108:111], v[80:83], a[152:155], v[108:111]\n"
"ds_write_addtid_b32 v106 offset:10368\n"
"v_mfma_f32_16x16x32_bf16 v[108:111], v[84:87], a[156:159], v[108:111]\n"
"ds_write_addtid_b32 v107 offset:15008\n"
"v_mfma_f32_16x16x32_bf16 v[108:111], v[88:91], a[160:163], v[108:111]\n"
"buffer_load_dwordx4 v[116:119], v18, s[40:43], s94 offen offset:1024 sc1\n"
"v_mfma_f32_16x16x32_bf16 v[108:111], v[92:95], a[164:167], v[108:111]\n"
"v_mfma_f32_16x16x32_bf16 v[104:107], v[68:71], a[224:227], 0\n"
"v_mfma_f32_16x16x32_bf16 v[104:107], v[72:75], a[228:231], v[104:107]\n"
"ds_write_addtid_b32 v108 offset:1280\n"
"v_mfma_f32_16x16x32_bf16 v[104:107], v[76:79], a[232:235], v[104:107]\n"
"ds_write_addtid_b32 v109 offset:5920\n"
"v_mfma_f32_16x16x32_bf16 v[104:107], v[80:83], a[236:239], v[104:107]\n"
"ds_write_addtid_b32 v110 offset:10624\n"
"v_mfma_f32_16x16x32_bf16 v[104:107], v[84:87], a[240:243], v[104:107]\n"
"ds_write_addtid_b32 v111 offset:15264\n"
"v_mfma_f32_16x16x32_bf16 v[104:107], v[88:91], a[244:247], v[104:107]\n"
"v_mfma_f32_16x16x32_bf16 v[104:107], v[92:95], a[248:251], v[104:107]\n"
"v_mfma_f32_16x16x32_bf16 v[120:123], v[68:71], a[252:255], 0\n"
"v_mfma_f32_16x16x32_bf16 v[120:123], v[72:75], v[24:27], v[120:123]\n"
"s_add_u32 s58, s50, 0x180000\n"
"ds_write_addtid_b32 v104 offset:1536\n"
"v_mfma_f32_16x16x32_bf16 v[120:123], v[76:79], v[28:31], v[120:123]\n"
"s_and_b32 s58, s58, 0x3ffffff\n"
"ds_write_addtid_b32 v105 offset:6176\n"
"v_mfma_f32_16x16x32_bf16 v[120:123], v[80:83], v[32:35], v[120:123]\n"
"s_add_u32 s56, s40, s58\n"
"ds_write_addtid_b32 v106 offset:10880\n"
"v_mfma_f32_16x16x32_bf16 v[120:123], v[84:87], v[36:39], v[120:123]\n"
"s_addc_u32 s57, s41, 0\n"
"ds_write_addtid_b32 v107 offset:15520\n"
"v_mfma_f32_16x16x32_bf16 v[120:123], v[88:91], v[40:43], v[120:123]\n"
"s_waitcnt vmcnt(8)\n"
"v_cmp_ne_u64 vcc, s[48:49], v[138:139]\n"
"v_mfma_f32_16x16x32_bf16 v[120:123], v[92:95], v[44:47], v[120:123]\n"
"v_mfma_f32_16x16x32_bf16 v[108:111], a[168:171], v[68:71], 0\n"
"s_cbranch_vccnz .LH_1s\n"
".LH_1r:\n"
"v_mfma_f32_16x16x32_bf16 v[108:111], a[172:175], v[72:75], v[108:111]\n"
"s_add_u32 s50, s50, 0x180000\n"
"ds_write_addtid_b32 v120 offset:1792\n"
"v_mfma_f32_16x16x32_bf16 v[108:111], a[176:179], v[76:79], v[108:111]\n"
"ds_write_addtid_b32 v121 offset:6432\n"
"v_mfma_f32_16x16x32_bf16 v[108:111], a[180:183], v[80:83], v[108:111]\n"
"ds_write_addtid_b32 v122 offset:11136\n"
"v_mfma_f32_16x16x32_bf16 v[108:111], a[184:187], v[84:87], v[108:111]\n"
"ds_write_addtid_b32 v123 offset:15776\n"
"global_load_dwordx4 v[104:107], v5, s[56:57] offset:0 sc1\n"
"v_mfma_f32_16x16x32_bf16 v[108:111], a[188:191], v[88:91], v[108:111]\n"
"v_mfma_f32_16x16x32_bf16 v[108:111], a[192:195], v[92:95], v[108:111]\n"
"v_mfma_f32_16x16x32_bf16 v[120:123], a[196:199], v[68:71], 0\n"
"v_mfma_f32_16x16x32_bf16 v[120:123], a[200:203], v[72:75], v[120:123]\n"
"s_waitcnt lgkmcnt(0)\n"
"v_mfma_f32_16x16x32_bf16 v[120:123], a[204:207], v[76:79], v[120:123]\n"
"s_barrier\n"
"v_mfma_f32_16x16x32_bf16 v[120:123], a[208:211], v[80:83], v[120:123]\n"
"global_load_dwordx4 v[72:75], v6, s[56:57] offset:0 sc1\n"
"ds_read_b128 v[68:71], v2 offset:2048\n"
"v_mfma_f32_16x16x32_bf16 v[120:123], a[212:215], v[84:87], v[120:123]\n"
"ds_read_b128 v[124:127], v3 offset:512\n"
"ds_read_b128 v[128:131], v4 offset:3072\n"
"v_mfma_f32_16x16x32_bf16 v[120:123], a[216:219], v[88:91], v[120:123]\n"
"ds_read_b128 v[132:135], v2 offset:2304\n"
"ds_read_b128 v[136:139], v3 offset:768\n"
"v_mfma_f32_16x16x32_bf16 v[120:123], a[220:223], v[92:95], v[120:123]\n"
"ds_read_b128 v[140:143], v4 offset:3328\n"
"v_mfma_f32_16x16x32_bf16 v[88:91], v[96:99], a[0:3], 0\n"
"global_load_dwordx4 v[76:79], v7, s[56:57] offset:0 sc1\n"
"v_mfma_f32_16x16x32_bf16 v[88:91], v[100:103], a[4:7], v[88:91]\n"
"s_waitcnt vmcnt(10)\n"
"v_mfma_f32_16x16x32_bf16 v[88:91], v[48:51], a[8:11], v[88:91]\n"
"v_mfma_f32_16x16x32_bf16 v[88:91], v[52:55], a[12:15], v[88:91]\n"
"s_waitcnt vmcnt(8)\n"
"v_mfma_f32_16x16x32_bf16 v[88:91], v[56:59], a[16:19], v[88:91]\n"
"s_waitcnt lgkmcnt(3)\n"
"v_add_f32 v68, v68, v124\n"
"v_add_f32 v69, v69, v125\n"
"v_mfma_f32_16x16x32_bf16 v[88:91], v[60:63], a[20:23], v[88:91]\n"
"s_waitcnt vmcnt(7)\n"
"global_load_dwordx4 v[80:83], v8, s[56:57] offset:0 sc1\n"
"v_add_f32 v70, v70, v126\n"
"v_mfma_f32_16x16x32_bf16 v[88:91], v[64:67], a[24:27], v[88:91]\n"
"buffer_load_dwordx2 v[144:145], v12, s[40:43], s50 offen sc1\n"
"v_mfma_f32_16x16x32_bf16 v[92:95], v[96:99], a[28:31], 0\n"
"v_add_f32 v71, v71, v127\n"
"v_add_f32 v68, v68, v128\n"
"v_mfma_f32_16x16x32_bf16 v[92:95], v[100:103], a[32:35], v[92:95]\n"
"s_waitcnt vmcnt(7)\n"
"buffer_store_dwordx2 v[0:1], v14, s[40:43], s95 offen offset:0 sc1\n"
"ds_write_addtid_b32 v88 offset:2048\n"
"v_mfma_f32_16x16x32_bf16 v[92:95], v[48:51], a[36:39], v[92:95]\n"
"ds_write_addtid_b32 v89 offset:6688\n"
"v_add_f32 v69, v69, v129\n"
"v_mfma_f32_16x16x32_bf16 v[92:95], v[52:55], a[40:43], v[92:95]\n"
"ds_write_addtid_b32 v90 offset:11392\n"
"global_load_dwordx4 v[84:87], v9, s[56:57] offset:0 sc1\n"
"v_mfma_f32_16x16x32_bf16 v[92:95], v[56:59], a[44:47], v[92:95]\n"
"ds_write_addtid_b32 v91 offset:16032\n"
"v_add_f32 v70, v70, v130\n"
"v_mfma_f32_16x16x32_bf16 v[92:95], v[60:63], a[48:51], v[92:95]\n"
"v_add_f32 v71, v71, v131\n"
"v_add_f32 v68, v68, v108\n"
"v_mfma_f32_16x16x32_bf16 v[92:95], v[64:67], a[52:55], v[92:95]\n"
"v_add_f32 v69, v69, v109\n"
"v_add_f32 v70, v70, v110\n"
"v_mfma_f32_16x16x32_bf16 v[124:127], v[96:99], a[56:59], 0\n"
"v_add_f32 v71, v71, v111\n"
"v_mfma_f32_16x16x32_bf16 v[124:127], v[100:103], a[60:63], v[124:127]\n"
"ds_write_addtid_b32 v92 offset:2304\n"
"global_load_dwordx4 v[88:91], v10, s[56:57] offset:0 sc1\n"
"v_mfma_f32_16x16x32_bf16 v[124:127], v[48:51], a[64:67], v[124:127]\n"
"s_add_u32 s93, s50, 0xffd00000\n"
"s_and_b32 s95, s93, 0xfbffffff\n"
"ds_write_addtid_b32 v93 offset:6944\n"
"v_mfma_f32_16x16x32_bf16 v[124:127], v[52:55], a[68:71], v[124:127]\n"
"ds_write_addtid_b32 v94 offset:11648\n"
"v_cvt_pk_bf16_f32 v108, v68, v69\n"
"v_mfma_f32_16x16x32_bf16 v[124:127], v[56:59], a[72:75], v[124:127]\n"
"s_waitcnt vmcnt(8)\n"
"ds_write_addtid_b32 v95 offset:16288\n"
"v_cvt_pk_bf16_f32 v109, v70, v71\n"
"v_mfma_f32_16x16x32_bf16 v[124:127], v[60:63], a[76:79], v[124:127]\n"
"s_waitcnt lgkmcnt(8)\n"
"v_add_f32 v132, v132, v136\n"
"v_add_f32 v133, v133, v137\n"
"v_mfma_f32_16x16x32_bf16 v[124:127], v[64:67], a[80:83], v[124:127]\n"
"global_load_dwordx4 v[92:95], v11, s[56:57] offset:0 sc1\n"
"v_add_f32 v134, v134, v138\n"
"v_mfma_f32_16x16x32_bf16 v[68:71], v[96:99], a[84:87], 0\n"
"v_add_f32 v135, v135, v139\n"
"v_add_f32 v132, v132, v140\n"
"v_mfma_f32_16x16x32_bf16 v[68:71], v[100:103], a[88:91], v[68:71]\n"
"ds_write_addtid_b32 v124 offset:2560\n"
"v_add_f32 v133, v133, v141\n"
"v_mfma_f32_16x16x32_bf16 v[68:71], v[48:51], a[92:95], v[68:71]\n"
"ds_write_addtid_b32 v125 offset:7200\n"
"v_add_f32 v134, v134, v142\n"
"v_mfma_f32_16x16x32_bf16 v[68:71], v[52:55], a[96:99], v[68:71]\n"
"ds_write_addtid_b32 v126 offset:11904\n"
"v_add_f32 v135, v135, v143\n"
"v_mfma_f32_16x16x32_bf16 v[68:71], v[56:59], a[100:103], v[68:71]\n"
"ds_write_addtid_b32 v127 offset:16544\n"
"v_add_f32 v132, v132, v120\n"
"v_mfma_f32_16x16x32_bf16 v[68:71], v[60:63], a[104:107], v[68:71]\n"
"v_add_f32 v133, v133, v121\n"
"v_add_f32 v134, v134, v122\n"
"v_mfma_f32_16x16x32_bf16 v[68:71], v[64:67], a[108:111], v[68:71]\n"
"v_add_f32 v135, v135, v123\n"
"v_cvt_pk_bf16_f32 v110, v132, v133\n"
"v_mfma_f32_16x16x32_bf16 v[124:127], v[96:99], a[112:115], 0\n"
"v_cvt_pk_bf16_f32 v111, v134, v135\n"
"v_add_f32 v112, v112, v116\n"
"v_mfma_f32_16x16x32_bf16 v[124:127], v[100:103], a[116:119], v[124:127]\n"
"ds_write_addtid_b32 v68 offset:2816\n"
"v_add_f32 v113, v113, v117\n"
"v_mfma_f32_16x16x32_bf16 v[124:127], v[48:51], a[120:123], v[124:127]\n"
"ds_write_addtid_b32 v69 offset:7456\n"
"v_add_f32 v114, v114, v118\n"
"v_mfma_f32_16x16x32_bf16 v[124:127], v[52:55], a[124:127], v[124:127]\n"
"ds_write_addtid_b32 v70 offset:12160\n"
"v_add_f32 v115, v115, v119\n"
"v_mfma_f32_16x16x32_bf16 v[124:127], v[56:59], a[128:131], v[124:127]\n"
"ds_write_addtid_b32 v71 offset:16800\n"
"v_mfma_f32_16x16x32_bf16 v[124:127], v[60:63], a[132:135], v[124:127]\n"
"v_permlane32_swap_b32 v112, v114\n"
"v_mfma_f32_16x16x32_bf16 v[124:127], v[64:67], a[136:139], v[124:127]\n"
"v_permlane32_swap_b32 v113, v115\n"
"v_mfma_f32_16x16x32_bf16 v[68:71], v[96:99], a[140:143], 0\n"
"v_add_f32 v112, v112, v114\n"
"v_add_f32 v113, v113, v115\n"
"v_mfma_f32_16x16x32_bf16 v[68:71], v[100:103], a[144:147], v[68:71]\n"
"buffer_store_dwordx4 v[108:111], v13, s[40:43], s95 offen sc1\n"
"ds_write_addtid_b32 v124 offset:3072\n"
"v_permlane16_swap_b32 v112, v113\n"
"v_mfma_f32_16x16x32_bf16 v[68:71], v[48:51], a[148:151], v[68:71]\n"
"ds_write_addtid_b32 v125 offset:7712\n"
"v_add_f32 v112, v112, v113\n"
"v_mfma_f32_16x16x32_bf16 v[68:71], v[52:55], a[152:155], v[68:71]\n"
"ds_write_addtid_b32 v126 offset:12416\n"
"v_add_f32_dpp v113, v112, v112 row_ror:8 row_mask:0xf bank_mask:0xf\n"
"v_mfma_f32_16x16x32_bf16 v[68:71], v[56:59], a[156:159], v[68:71]\n"
"ds_write_addtid_b32 v127 offset:17056\n"
"v_mov_b32 v112, v113\n"
"v_mfma_f32_16x16x32_bf16 v[68:71], v[60:63], a[160:163], v[68:71]\n"
"v_permlane16_swap_b32 v113, v112\n"
"v_mfma_f32_16x16x32_bf16 v[68:71], v[64:67], a[164:167], v[68:71]\n"
"v_mul_f32 v113, 0xba800000, v113\n"
"v_fma_f32 v112, v112, s62, v20\n"
"v_mfma_f32_16x16x32_bf16 v[108:111], v[96:99], a[224:227], 0\n"
"v_fma_f32 v112, -v113, v113, v112\n"
"v_max_f32 v112, v20, v112\n"
"v_mfma_f32_16x16x32_bf16 v[108:111], v[100:103], a[228:231], v[108:111]\n"
"ds_write_addtid_b32 v68 offset:3328\n"
"v_mfma_f32_16x16x32_bf16 v[108:111], v[48:51], a[232:235], v[108:111]\n"
"ds_write_addtid_b32 v69 offset:7968\n"
"v_mfma_f32_16x16x32_bf16 v[108:111], v[52:55], a[236:239], v[108:111]\n"
"ds_write_addtid_b32 v70 offset:12672\n"
"v_mfma_f32_16x16x32_bf16 v[108:111], v[56:59], a[240:243], v[108:111]\n"
"ds_write_addtid_b32 v71 offset:17312\n"
"v_mfma_f32_16x16x32_bf16 v[108:111], v[60:63], a[244:247], v[108:111]\n"
"v_rsq_f32 v112, v112\n"
"v_mfma_f32_16x16x32_bf16 v[108:111], v[64:67], a[248:251], v[108:111]\n"
"v_mov_b32 v114, v0\n"
"v_mov_b32 v115, v1\n"
"v_mfma_f32_16x16x32_bf16 v[68:71], v[96:99], a[252:255], 0\n"
"buffer_store_dwordx4 v[112:115], v19, s[40:43], s94 offen sc1\n"
"v_mfma_f32_16x16x32_bf16 v[68:71], v[100:103], v[24:27], v[68:71]\n"
"s_add_u32 s58, s50, 0x180000\n"
"ds_write_addtid_b32 v108 offset:3584\n"
"v_mfma_f32_16x16x32_bf16 v[68:71], v[48:51], v[28:31], v[68:71]\n"
"s_and_b32 s58, s58, 0x3ffffff\n"
"ds_write_addtid_b32 v109 offset:8224\n"
"v_mfma_f32_16x16x32_bf16 v[68:71], v[52:55], v[32:35], v[68:71]\n"
"s_add_u32 s56, s40, s58\n"
"ds_write_addtid_b32 v110 offset:12928\n"
"v_mfma_f32_16x16x32_bf16 v[68:71], v[56:59], v[36:39], v[68:71]\n"
"s_addc_u32 s57, s41, 0\n"
"ds_write_addtid_b32 v111 offset:17568\n"
"v_mfma_f32_16x16x32_bf16 v[68:71], v[60:63], v[40:43], v[68:71]\n"
"s_waitcnt vmcnt(6)\n"
"v_cmp_ne_u64 vcc, s[48:49], v[144:145]\n"
"v_mfma_f32_16x16x32_bf16 v[68:71], v[64:67], v[44:47], v[68:71]\n"
"v_mfma_f32_16x16x32_bf16 v[116:119], a[168:171], v[96:99], 0\n"
"s_cbranch_vccnz .LH_2s\n"
".LH_2r:\n"
"v_mfma_f32_16x16x32_bf16 v[116:119], a[172:175], v[100:103], v[116:119]\n"
"s_add_u32 s50, s50, 0x180000\n"
"ds_write_addtid_b32 v68 offset:3840\n"
"v_mfma_f32_16x16x32_bf16 v[116:119], a[176:179], v[48:51], v[116:119]\n"
"ds_write_addtid_b32 v69 offset:8480\n"
"v_mfma_f32_16x16x32_bf16 v[116:119], a[180:183], v[52:55], v[116:119]\n"
"ds_write_addtid_b32 v70 offset:13184\n"
"v_mfma_f32_16x16x32_bf16 v[116:119], a[184:187], v[56:59], v[116:119]\n"
"ds_write_addtid_b32 v71 offset:17824\n"
"v_mfma_f32_16x16x32_bf16 v[116:119], a[188:191], v[60:63], v[116:119]\n"
"global_load_dwordx4 v[108:111], v5, s[56:57] offset:0 sc1\n"
"v_mfma_f32_16x16x32_bf16 v[116:119], a[192:195], v[64:67], v[116:119]\n"
"v_mfma_f32_16x16x32_bf16 v[120:123], a[196:199], v[96:99], 0\n"
"v_mfma_f32_16x16x32_bf16 v[120:123], a[200:203], v[100:103], v[120:123]\n"
"s_waitcnt lgkmcnt(0)\n"
"global_load_dwordx4 v[112:115], v6, s[56:57] offset:0 sc1\n"
"v_mfma_f32_16x16x32_bf16 v[120:123], a[204:207], v[48:51], v[120:123]\n"
"s_barrier\n"
"v_mfma_f32_16x16x32_bf16 v[120:123], a[208:211], v[52:55], v[120:123]\n"
"ds_read_b128 v[100:103], v2 offset:0\n"
"ds_read_b128 v[68:71], v3 offset:2560\n"
"v_mfma_f32_16x16x32_bf16 v[120:123], a[212:215], v[56:59], v[120:123]\n"
"ds_read_b128 v[96:99], v4 offset:1024\n"
"ds_read_b128 v[124:127], v2 offset:256\n"
"v_mfma_f32_16x16x32_bf16 v[120:123], a[216:219], v[60:63], v[120:123]\n"
"ds_read_b128 v[128:131], v3 offset:2816\n"
"ds_read_b128 v[132:135], v4 offset:1280\n"
"v_mfma_f32_16x16x32_bf16 v[120:123], a[220:223], v[64:67], v[120:123]\n"
"s_branch .LH_top\n"
".LH_1s:\n"
"s_cmp_ge_u32 s50, s53\n"
"s_cbranch_scc1 .LH_end\n"
"s_cmp_ge_u32 s50, s52\n"
"s_cbranch_scc1 .LH_1r\n"
"s_sub_u32 s60, s50, 0x0\n"
"s_mov_b32 s61, 200000\n"
"s_nop 7\n"
".LH_1p:\n"
"buffer_load_dwordx2 v[138:139], v12, s[40:43], s60 offen sc1\n"
"s_waitcnt vmcnt(0)\n"
"v_cmp_ne_u64 vcc, s[48:49], v[138:139]\n"
"s_cbranch_vccz .LH_1px\n"
"s_sleep 1\n"
"s_sub_u32 s61, s61, 1\n"
"s_cbranch_scc0 .LH_1p\n"
"s_branch .LH_end\n"
".LH_1px:\n"
"s_branch .LH_1r\n"
".LH_2s:\n"
"s_cmp_ge_u32 s50, s53\n"
"s_cbranch_scc1 .LH_end\n"
"s_cmp_ge_u32 s50, s52\n"
"s_cbranch_scc1 .LH_2r\n"
"s_sub_u32 s60, s50, 0x0\n"
"s_mov_b32 s61, 200000\n"
"s_nop 7\n"
".LH_2p:\n"
"buffer_load_dwordx2 v[144:145], v12, s[40:43], s60 offen sc1\n"
"s_waitcnt vmcnt(0)\n"
"v_cmp_ne_u64 vcc, s[48:49], v[144:145]\n"
"s_cbranch_vccz .LH_2px\n"
"s_sleep 1\n"
"s_sub_u32 s61, s61, 1\n"
"s_cbranch_scc0 .LH_2p\n"
"s_branch .LH_end\n"
".LH_2px:\n"
"s_branch .LH_2r\n"
".LH_3s:\n"
"s_cmp_ge_u32 s50, s53\n"
"s_cbranch_scc1 .LH_end\n"
"s_cmp_ge_u32 s50, s52\n"
"s_cbranch_scc1 .LH_3r\n"
"s_sub_u32 s60, s50, 0x0\n"
"s_mov_b32 s61, 200000\n"
"s_nop 7\n"
".LH_3p:\n"
"buffer_load_dwordx2 v[136:137], v12, s[40:43], s60 offen sc1\n"
"s_waitcnt vmcnt(0)\n"
"v_cmp_ne_u64 vcc, s[48:49], v[136:137]\n"
"s_cbranch_vccz .LH_3px\n"
"s_sleep 1\n"
"s_sub_u32 s61, s61, 1\n"
"s_cbranch_scc0 .LH_3p\n"
"s_branch .LH_end\n"
".LH_3px:\n"
"s_branch .LH_3r\n"
".LH_4s:\n"
"s_cmp_ge_u32 s50, s53\n"
"s_cbranch_scc1 .LH_end\n"
"s_cmp_ge_u32 s50, s52\n"
"s_cbranch_scc1 .LH_4r\n"
"s_sub_u32 s60, s50, 0x0\n"
"s_mov_b32 s61, 200000\n"
"s_nop 7\n"
".LH_4p:\n"
"buffer_load_dwordx2 v[172:173], v12, s[40:43], s60 offen sc1\n"
"s_waitcnt vmcnt(0)\n"
"v_cmp_ne_u64 vcc, s[48:49], v[172:173]\n"
"s_cbranch_vccz .LH_4px\n"
"s_sleep 1\n"
"s_sub_u32 s61, s61, 1\n"
"s_cbranch_scc0 .LH_4p\n"
"s_branch .LH_end\n"
".LH_4px:\n"
"s_branch .LH_4r\n"
".LH_5s:\n"
"s_cmp_ge_i32 s94, 0xc000000\n"
"s_cselect_b64 s[64:65], 0, s[64:65]\n"
"s_cmp_ge_u32 s94, 0xc000000\n"
"s_cbranch_scc1 .LH_5r\n"
"s_mov_b32 s61, 200000\n"
"s_nop 7\n"
".LH_5p:\n"
"buffer_load_dwordx2 v[136:137], v17, s[40:43], s94 offen offset:0 sc1\n"
"s_waitcnt vmcnt(0)\n"
"v_cmp_ne_u64 vcc, s[64:65], v[136:137]\n"
"s_cbranch_vccz .LH_5px\n"
"s_sleep 1\n"
"s_sub_u32 s61, s61, 1\n"
"s_cbranch_scc0 .LH_5p\n"
"s_branch .LH_end\n"
".LH_5px:\n"
"s_branch .LH_5r\n"
".LH_end:\n"
"s_waitcnt vmcnt(0) lgkmcnt(0)\n"
"s_endpgm\n"
      :: "{v2}"((unsigned)(lds0 + ((wid + 3) & 3) * 18592 + trp)), "{v3}"((unsigned)(lds0 + ((wid + 2) & 3) * 18592 + trp)), "{v4}"((unsigned)(lds0 + ((wid + 1) & 3) * 18592 + trp)), "{v5}"((unsigned)(lane * 16 + f0 * 1024 + ((0 + rot) % 7) * 1024 + 134316032)), "{v6}"((unsigned)(lane * 16 + f0 * 1024 + ((1 + rot) % 7) * 1024 + 134316032)), "{v7}"((unsigned)(lane * 16 + f0 * 1024 + ((2 + rot) % 7) * 1024 + 134316032)), "{v8}"((unsigned)(lane * 16 + f0 * 1024 + ((3 + rot) % 7) * 1024 + 134316032)), "{v9}"((unsigned)(lane * 16 + f0 * 1024 + ((4 + rot) % 7) * 1024 + 134316032)), "{v10}"((unsigned)(lane * 16 + f0 * 1024 + ((5 + rot) % 7) * 1024 + 134316032)), "{v11}"((unsigned)(lane * 16 + f0 * 1024 + ((6 + rot) % 7) * 1024 + 134316032)), "{v12}"((unsigned)((f0 + lane % 7) * 8 + kq * 32 + 353024 + H_GL * 1572864)), "{v13}"((unsigned)(lane * 16 + pair * 4096 + kq * 1024 + 212992)), "{v14}"((unsigned)(lane ? DROP : pair * 64 + kq * 8 + 354304)), "{v15}"((unsigned)(lane * 16 + (pg0 + 4) * 4096 + kq * 1024 + 212992)), "{v16}"((unsigned)(lane > 1 ? DROP : (lane ? pg0 + 4 : pair) * 64 + kq * 8 + 354304)), "{v17}"((unsigned)((lane & 15) * 8 + 67462912)), "{v18}"((unsigned)(lane * 16 + 67457024)), "{v19}"((unsigned)((lane & 24) ? DROP : (2 * (lane & 7) + (lane >> 5)) * 16 + 67461120)), "{v20}"((unsigned)(0x3727c5ac)), "{v48}"((unsigned)(vT + tsw)), "{v49}"((unsigned)(vT + (tsw ^ 8))), "{v50}"((unsigned)(vD + ((0 + rot) % 7) * 65536)), "{v51}"((unsigned)(vD + ((1 + rot) % 7) * 65536)), "{v52}"((unsigned)(vD + ((2 + rot) % 7) * 65536)), "{v53}"((unsigned)(vD + ((3 + rot) % 7) * 65536)), "{v54}"((unsigned)(vD + ((4 + rot) % 7) * 65536)), "{v55}"((unsigned)(vD + ((5 + rot) % 7) * 65536)), "{v56}"((unsigned)(vD + ((6 + rot) % 7) * 65536)), "{s[40:43]}"(rS), "{s[44:47]}"(rW), "{s[48:49]}"(wsp), "{s50}"((unsigned)(sA)), "{s51}"((unsigned)(sLo)), "{s52}"((unsigned)(sHi)), "{s53}"((unsigned)(sEx)), "{s54}"((unsigned)(sEnt)), "{s55}"((unsigned)(sLd)), "{s[56:57]}"(wsb + (sLo & 0x3ffffff)), "{s59}"((unsigned)(lds0 + wid * 18592)), "{s62}"((unsigned)(0x3a800000)), "{s66}"((unsigned)(sW0 + 64 * ((wid + 1) & 3))), "{s67}"((unsigned)(sW0 + 64 * ((wid + 2) & 3))), "{s68}"((unsigned)(sW0 + 64 * ((wid + 3) & 3))), "{s69}"((unsigned)(sW0 + 64 * ((wid + 4) & 3))), "{s70}"((unsigned)(sW0 + 256))
      : "v255", "a255", "s95", "vcc", "scc", "memory");

    } else if (grp < 4) {
      const unsigned sEx = sHi + G_EX * 3 * RS, sEnt = -wid & 3;
    asm volatile(
"s_nop 4\n"
"v_mov_b32 v186, 0\n"
"global_load_dwordx2 v[0:1], v186, s[48:49] sc1\n"
"v_mov_b32 v188, 0x3f803f80\n"
"v_mov_b32 v189, 0x3f803f80\n"
"v_mov_b32 v190, 0x3f803f80\n"
"v_mov_b32 v191, 0x3f803f80\n"
"s_add_u32 m0, s55, 0\n"
"s_add_u32 s60, s62, 0\n"
"buffer_load_dwordx4 v46, s[44:47], s60 offen lds\n"
"s_add_u32 m0, s55, 1024\n"
"s_add_u32 s60, s62, 32768\n"
"buffer_load_dwordx4 v46, s[44:47], s60 offen lds\n"
"s_add_u32 m0, s55, 2048\n"
"s_add_u32 s60, s63, 0\n"
"buffer_load_dwordx4 v46, s[44:47], s60 offen lds\n"
"s_add_u32 m0, s55, 3072\n"
"s_add_u32 s60, s63, 32768\n"
"buffer_load_dwordx4 v46, s[44:47], s60 offen lds\n"
"s_add_u32 m0, s55, 4096\n"
"s_add_u32 s60, s64, 0\n"
"buffer_load_dwordx4 v46, s[44:47], s60 offen lds\n"
"s_add_u32 m0, s55, 5120\n"
"s_add_u32 s60, s64, 32768\n"
"buffer_load_dwordx4 v46, s[44:47], s60 offen lds\n"
"s_add_u32 m0, s55, 6144\n"
"s_add_u32 s60, s65, 0\n"
"buffer_load_dwordx4 v46, s[44:47], s60 offen lds\n"
"s_add_u32 m0, s55, 7168\n"
"s_add_u32 s60, s65, 32768\n"
"buffer_load_dwordx4 v46, s[44:47], s60 offen lds\n"
"s_add_u32 m0, s55, 8192\n"
"s_add_u32 s60, s66, 0\n"
"buffer_load_dwordx4 v46, s[44:47], s60 offen lds\n"
"s_add_u32 m0, s55, 9216\n"
"s_add_u32 s60, s66, 32768\n"
"buffer_load_dwordx4 v46, s[44:47], s60 offen lds\n"
"s_add_u32 m0, s55, 10240\n"
"s_add_u32 s60, s62, 0\n"
"buffer_load_dwordx4 v47, s[44:47], s60 offen lds\n"
"s_add_u32 m0, s55, 11264\n"
"s_add_u32 s60, s62, 32768\n"
"buffer_load_dwordx4 v47, s[44:47], s60 offen lds\n"
"s_add_u32 m0, s55, 12288\n"
"s_add_u32 s60, s63, 0\n"
"buffer_load_dwordx4 v47, s[44:47], s60 offen lds\n"
"s_add_u32 m0, s55, 13312\n"
"s_add_u32 s60, s63, 32768\n"
"buffer_load_dwordx4 v47, s[44:47], s60 offen lds\n"
"s_add_u32 m0, s55, 14336\n"
"s_add_u32 s60, s64, 0\n"
"buffer_load_dwordx4 v47, s[44:47], s60 offen lds\n"
"s_add_u32 m0, s55, 15360\n"
"s_add_u32 s60, s64, 32768\n"
"buffer_load_dwordx4 v47, s[44:47], s60 offen lds\n"
"s_add_u32 m0, s55, 16384\n"
"s_add_u32 s60, s65, 0\n"
"buffer_load_dwordx4 v47, s[44:47], s60 offen lds\n"
"s_add_u32 m0, s55, 17408\n"
"s_add_u32 s60, s65, 32768\n"
"buffer_load_dwordx4 v47, s[44:47], s60 offen lds\n"
"s_add_u32 m0, s55, 18432\n"
"s_add_u32 s60, s66, 0\n"
"buffer_load_dwordx4 v47, s[44:47], s60 offen lds\n"
"s_add_u32 m0, s55, 19456\n"
"s_add_u32 s60, s66, 32768\n"
"buffer_load_dwordx4 v47, s[44:47], s60 offen lds\n"
"s_add_u32 m0, s55, 20480\n"
"s_add_u32 s60, s62, 0\n"
"buffer_load_dwordx4 v48, s[44:47], s60 offen lds\n"
"s_add_u32 m0, s55, 21504\n"
"s_add_u32 s60, s62, 32768\n"
"buffer_load_dwordx4 v48, s[44:47], s60 offen lds\n"
"s_add_u32 m0, s55, 22528\n"
"s_add_u32 s60, s63, 0\n"
"buffer_load_dwordx4 v48, s[44:47], s60 offen lds\n"
"s_add_u32 m0, s55, 23552\n"
"s_add_u32 s60, s63, 32768\n"
"buffer_load_dwordx4 v48, s[44:47], s60 offen lds\n"
"s_add_u32 m0, s55, 24576\n"
"s_add_u32 s60, s64, 0\n"
"buffer_load_dwordx4 v48, s[44:47], s60 offen lds\n"
"s_add_u32 m0, s55, 25600\n"
"s_add_u32 s60, s64, 32768\n"
"buffer_load_dwordx4 v48, s[44:47], s60 offen lds\n"
"s_add_u32 m0, s55, 26624\n"
"s_add_u32 s60, s65, 0\n"
"buffer_load_dwordx4 v48, s[44:47], s60 offen lds\n"
"s_add_u32 m0, s55, 27648\n"
"s_add_u32 s60, s65, 32768\n"
"buffer_load_dwordx4 v48, s[44:47], s60 offen lds\n"
"s_add_u32 m0, s55, 28672\n"
"s_add_u32 s60, s66, 0\n"
"buffer_load_dwordx4 v48, s[44:47], s60 offen lds\n"
"s_add_u32 m0, s55, 29696\n"
"s_add_u32 s60, s66, 32768\n"
"buffer_load_dwordx4 v48, s[44:47], s60 offen lds\n"
"s_add_u32 m0, s55, 30720\n"
"s_add_u32 s60, s62, 0\n"
"buffer_load_dwordx4 v49, s[44:47], s60 offen lds\n"
"s_add_u32 m0, s55, 31744\n"
"s_add_u32 s60, s62, 32768\n"
"buffer_load_dwordx4 v49, s[44:47], s60 offen lds\n"
"s_add_u32 m0, s55, 32768\n"
"s_add_u32 s60, s63, 0\n"
"buffer_load_dwordx4 v49, s[44:47], s60 offen lds\n"
"s_add_u32 m0, s55, 33792\n"
"s_add_u32 s60, s63, 32768\n"
"buffer_load_dwordx4 v49, s[44:47], s60 offen lds\n"
"s_add_u32 m0, s55, 34816\n"
"s_add_u32 s60, s64, 0\n"
"buffer_load_dwordx4 v49, s[44:47], s60 offen lds\n"
"s_add_u32 m0, s55, 35840\n"
"s_add_u32 s60, s64, 32768\n"
"buffer_load_dwordx4 v49, s[44:47], s60 offen lds\n"
"s_add_u32 m0, s55, 36864\n"
"s_add_u32 s60, s65, 0\n"
"buffer_load_dwordx4 v49, s[44:47], s60 offen lds\n"
"s_add_u32 m0, s55, 37888\n"
"s_add_u32 s60, s65, 32768\n"
"buffer_load_dwordx4 v49, s[44:47], s60 offen lds\n"
"s_add_u32 m0, s55, 38912\n"
"s_add_u32 s60, s66, 0\n"
"buffer_load_dwordx4 v49, s[44:47], s60 offen lds\n"
"s_add_u32 m0, s55, 39936\n"
"s_add_u32 s60, s66, 32768\n"
"buffer_load_dwordx4 v49, s[44:47], s60 offen lds\n"
"s_waitcnt vmcnt(38)\n"
"ds_read_b64_tr_b16 a[0:1], v44 offset:0\n"
"ds_read_b64_tr_b16 a[2:3], v44 offset:256\n"
"ds_read_b64_tr_b16 a[28:29], v45 offset:0\n"
"ds_read_b64_tr_b16 a[30:31], v45 offset:256\n"
"v_add_u32 v0, 2, v0\n"
"v_add_u32 v1, 0x9e3779b9, v1\n"
"v_or_b32 v0, 1, v0\n"
"v_readfirstlane_b32 s49, v1\n"
"v_readfirstlane_b32 s48, v0\n"
"s_waitcnt vmcnt(36)\n"
"ds_read_b64_tr_b16 a[56:57], v44 offset:2048\n"
"ds_read_b64_tr_b16 a[58:59], v44 offset:2304\n"
"ds_read_b64_tr_b16 a[84:85], v45 offset:2048\n"
"ds_read_b64_tr_b16 a[86:87], v45 offset:2304\n"
"s_waitcnt lgkmcnt(4)\n"
"s_add_u32 m0, s55, 0\n"
"s_add_u32 s60, s62, 0\n"
"buffer_load_dwordx4 v50, s[44:47], s60 offen lds\n"
"s_add_u32 m0, s55, 1024\n"
"s_add_u32 s60, s62, 32768\n"
"buffer_load_dwordx4 v50, s[44:47], s60 offen lds\n"
"s_waitcnt vmcnt(36)\n"
"ds_read_b64_tr_b16 a[112:113], v44 offset:4096\n"
"ds_read_b64_tr_b16 a[114:115], v44 offset:4352\n"
"ds_read_b64_tr_b16 a[140:141], v45 offset:4096\n"
"ds_read_b64_tr_b16 a[142:143], v45 offset:4352\n"
"s_waitcnt lgkmcnt(4)\n"
"s_add_u32 m0, s55, 2048\n"
"s_add_u32 s60, s63, 0\n"
"buffer_load_dwordx4 v50, s[44:47], s60 offen lds\n"
"s_add_u32 m0, s55, 3072\n"
"s_add_u32 s60, s63, 32768\n"
"buffer_load_dwordx4 v50, s[44:47], s60 offen lds\n"
"s_waitcnt vmcnt(36)\n"
"ds_read_b64_tr_b16 a[168:169], v44 offset:6144\n"
"ds_read_b64_tr_b16 a[170:171], v44 offset:6400\n"
"ds_read_b64_tr_b16 a[196:197], v45 offset:6144\n"
"ds_read_b64_tr_b16 a[198:199], v45 offset:6400\n"
"s_waitcnt lgkmcnt(4)\n"
"s_add_u32 m0, s55, 4096\n"
"s_add_u32 s60, s64, 0\n"
"buffer_load_dwordx4 v50, s[44:47], s60 offen lds\n"
"s_add_u32 m0, s55, 5120\n"
"s_add_u32 s60, s64, 32768\n"
"buffer_load_dwordx4 v50, s[44:47], s60 offen lds\n"
"s_waitcnt vmcnt(36)\n"
"ds_read_b64_tr_b16 a[224:225], v44 offset:8192\n"
"ds_read_b64_tr_b16 a[226:227], v44 offset:8448\n"
"ds_read_b64_tr_b16 a[252:253], v45 offset:8192\n"
"ds_read_b64_tr_b16 a[254:255], v45 offset:8448\n"
"s_waitcnt lgkmcnt(4)\n"
"s_add_u32 m0, s55, 6144\n"
"s_add_u32 s60, s65, 0\n"
"buffer_load_dwordx4 v50, s[44:47], s60 offen lds\n"
"s_add_u32 m0, s55, 7168\n"
"s_add_u32 s60, s65, 32768\n"
"buffer_load_dwordx4 v50, s[44:47], s60 offen lds\n"
"s_waitcnt vmcnt(36)\n"
"ds_read_b64_tr_b16 a[4:5], v44 offset:10240\n"
"ds_read_b64_tr_b16 a[6:7], v44 offset:10496\n"
"ds_read_b64_tr_b16 a[32:33], v45 offset:10240\n"
"ds_read_b64_tr_b16 a[34:35], v45 offset:10496\n"
"s_waitcnt lgkmcnt(4)\n"
"s_add_u32 m0, s55, 8192\n"
"s_add_u32 s60, s66, 0\n"
"buffer_load_dwordx4 v50, s[44:47], s60 offen lds\n"
"s_add_u32 m0, s55, 9216\n"
"s_add_u32 s60, s66, 32768\n"
"buffer_load_dwordx4 v50, s[44:47], s60 offen lds\n"
"s_waitcnt vmcnt(36)\n"
"ds_read_b64_tr_b16 a[60:61], v44 offset:12288\n"
"ds_read_b64_tr_b16 a[62:63], v44 offset:12544\n"
"ds_read_b64_tr_b16 a[88:89], v45 offset:12288\n"
"ds_read_b64_tr_b16 a[90:91], v45 offset:12544\n"
"s_waitcnt lgkmcnt(4)\n"
"s_add_u32 m0, s55, 10240\n"
"s_add_u32 s60, s62, 0\n"
"buffer_load_dwordx4 v51, s[44:47], s60 offen lds\n"
"s_add_u32 m0, s55, 11264\n"
"s_add_u32 s60, s62, 32768\n"
"buffer_load_dwordx4 v51, s[44:47], s60 offen lds\n"
"s_waitcnt vmcnt(36)\n"
"ds_read_b64_tr_b16 a[116:117], v44 offset:14336\n"
"ds_read_b64_tr_b16 a[118:119], v44 offset:14592\n"
"ds_read_b64_tr_b16 a[144:145], v45 offset:14336\n"
"ds_read_b64_tr_b16 a[146:147], v45 offset:14592\n"
"s_waitcnt lgkmcnt(4)\n"
"s_add_u32 m0, s55, 12288\n"
"s_add_u32 s60, s63, 0\n"
"buffer_load_dwordx4 v51, s[44:47], s60 offen lds\n"
"s_add_u32 m0, s55, 13312\n"
"s_add_u32 s60, s63, 32768\n"
"buffer_load_dwordx4 v51, s[44:47], s60 offen lds\n"
"s_waitcnt vmcnt(36)\n"
"ds_read_b64_tr_b16 a[172:173], v44 offset:16384\n"
"ds_read_b64_tr_b16 a[174:175], v44 offset:16640\n"
"ds_read_b64_tr_b16 a[200:201], v45 offset:16384\n"
"ds_read_b64_tr_b16 a[202:203], v45 offset:16640\n"
"s_waitcnt lgkmcnt(4)\n"
"s_add_u32 m0, s55, 14336\n"
"s_add_u32 s60, s64, 0\n"
"buffer_load_dwordx4 v51, s[44:47], s60 offen lds\n"
"s_add_u32 m0, s55, 15360\n"
"s_add_u32 s60, s64, 32768\n"
"buffer_load_dwordx4 v51, s[44:47], s60 offen lds\n"
"s_waitcnt vmcnt(36)\n"
"ds_read_b64_tr_b16 a[228:229], v44 offset:18432\n"
"ds_read_b64_tr_b16 a[230:231], v44 offset:18688\n"
"ds_read_b64_tr_b16 v[20:21], v45 offset:18432\n"
"ds_read_b64_tr_b16 v[22:23], v45 offset:18688\n"
"s_waitcnt lgkmcnt(4)\n"
"s_add_u32 m0, s55, 16384\n"
"s_add_u32 s60, s65, 0\n"
"buffer_load_dwordx4 v51, s[44:47], s60 offen lds\n"
"s_add_u32 m0, s55, 17408\n"
"s_add_u32 s60, s65, 32768\n"
"buffer_load_dwordx4 v51, s[44:47], s60 offen lds\n"
"s_waitcnt vmcnt(36)\n"
"ds_read_b64_tr_b16 a[8:9], v44 offset:20480\n"
"ds_read_b64_tr_b16 a[10:11], v44 offset:20736\n"
"ds_read_b64_tr_b16 a[36:37], v45 offset:20480\n"
"ds_read_b64_tr_b16 a[38:39], v45 offset:20736\n"
"s_waitcnt lgkmcnt(4)\n"
"s_add_u32 m0, s55, 18432\n"
"s_add_u32 s60, s66, 0\n"
"buffer_load_dwordx4 v51, s[44:47], s60 offen lds\n"
"s_add_u32 m0, s55, 19456\n"
"s_add_u32 s60, s66, 32768\n"
"buffer_load_dwordx4 v51, s[44:47], s60 offen lds\n"
"s_waitcnt vmcnt(36)\n"
"ds_read_b64_tr_b16 a[64:65], v44 offset:22528\n"
"ds_read_b64_tr_b16 a[66:67], v44 offset:22784\n"
"ds_read_b64_tr_b16 a[92:93], v45 offset:22528\n"
"ds_read_b64_tr_b16 a[94:95], v45 offset:22784\n"
"s_waitcnt lgkmcnt(4)\n"
"s_add_u32 m0, s55, 20480\n"
"s_add_u32 s60, s62, 0\n"
"buffer_load_dwordx4 v52, s[44:47], s60 offen lds\n"
"s_add_u32 m0, s55, 21504\n"
"s_add_u32 s60, s62, 32768\n"
"buffer_load_dwordx4 v52, s[44:47], s60 offen lds\n"
"s_waitcnt vmcnt(36)\n"
"ds_read_b64_tr_b16 a[120:121], v44 offset:24576\n"
"ds_read_b64_tr_b16 a[122:123], v44 offset:24832\n"
"ds_read_b64_tr_b16 a[148:149], v45 offset:24576\n"
"ds_read_b64_tr_b16 a[150:151], v45 offset:24832\n"
"s_waitcnt lgkmcnt(4)\n"
"s_add_u32 m0, s55, 22528\n"
"s_add_u32 s60, s63, 0\n"
"buffer_load_dwordx4 v52, s[44:47], s60 offen lds\n"
"s_add_u32 m0, s55, 23552\n"
"s_add_u32 s60, s63, 32768\n"
"buffer_load_dwordx4 v52, s[44:47], s60 offen lds\n"
"s_waitcnt vmcnt(36)\n"
"ds_read_b64_tr_b16 a[176:177], v44 offset:26624\n"
"ds_read_b64_tr_b16 a[178:179], v44 offset:26880\n"
"ds_read_b64_tr_b16 a[204:205], v45 offset:26624\n"
"ds_read_b64_tr_b16 a[206:207], v45 offset:26880\n"
"s_waitcnt lgkmcnt(4)\n"
"s_add_u32 m0, s55, 24576\n"
"s_add_u32 s60, s64, 0\n"
"buffer_load_dwordx4 v52, s[44:47], s60 offen lds\n"
"s_add_u32 m0, s55, 25600\n"
"s_add_u32 s60, s64, 32768\n"
"buffer_load_dwordx4 v52, s[44:47], s60 offen lds\n"
"s_waitcnt vmcnt(36)\n"
"ds_read_b64_tr_b16 a[232:233], v44 offset:28672\n"
"ds_read_b64_tr_b16 a[234:235], v44 offset:28928\n"
"ds_read_b64_tr_b16 v[24:25], v45 offset:28672\n"
"ds_read_b64_tr_b16 v[26:27], v45 offset:28928\n"
"s_waitcnt lgkmcnt(4)\n"
"s_add_u32 m0, s55, 26624\n"
"s_add_u32 s60, s65, 0\n"
"buffer_load_dwordx4 v52, s[44:47], s60 offen lds\n"
"s_add_u32 m0, s55, 27648\n"
"s_add_u32 s60, s65, 32768\n"
"buffer_load_dwordx4 v52, s[44:47], s60 offen lds\n"
"s_waitcnt vmcnt(36)\n"
"ds_read_b64_tr_b16 a[12:13], v44 offset:30720\n"
"ds_read_b64_tr_b16 a[14:15], v44 offset:30976\n"
"ds_read_b64_tr_b16 a[40:41], v45 offset:30720\n"
"ds_read_b64_tr_b16 a[42:43], v45 offset:30976\n"
"s_waitcnt lgkmcnt(4)\n"
"s_add_u32 m0, s55, 28672\n"
"s_add_u32 s60, s66, 0\n"
"buffer_load_dwordx4 v52, s[44:47], s60 offen lds\n"
"s_add_u32 m0, s55, 29696\n"
"s_add_u32 s60, s66, 32768\n"
"buffer_load_dwordx4 v52, s[44:47], s60 offen lds\n"
"s_waitcnt vmcnt(36)\n"
"ds_read_b64_tr_b16 a[68:69], v44 offset:32768\n"
"ds_read_b64_tr_b16 a[70:71], v44 offset:33024\n"
"ds_read_b64_tr_b16 a[96:97], v45 offset:32768\n"
"ds_read_b64_tr_b16 a[98:99], v45 offset:33024\n"
"s_waitcnt lgkmcnt(4)\n"
"s_waitcnt vmcnt(34)\n"
"ds_read_b64_tr_b16 a[124:125], v44 offset:34816\n"
"ds_read_b64_tr_b16 a[126:127], v44 offset:35072\n"
"ds_read_b64_tr_b16 a[152:153], v45 offset:34816\n"
"ds_read_b64_tr_b16 a[154:155], v45 offset:35072\n"
"s_waitcnt lgkmcnt(4)\n"
"s_waitcnt vmcnt(32)\n"
"ds_read_b64_tr_b16 a[180:181], v44 offset:36864\n"
"ds_read_b64_tr_b16 a[182:183], v44 offset:37120\n"
"ds_read_b64_tr_b16 a[208:209], v45 offset:36864\n"
"ds_read_b64_tr_b16 a[210:211], v45 offset:37120\n"
"s_waitcnt lgkmcnt(4)\n"
"s_waitcnt vmcnt(30)\n"
"ds_read_b64_tr_b16 a[236:237], v44 offset:38912\n"
"ds_read_b64_tr_b16 a[238:239], v44 offset:39168\n"
"ds_read_b64_tr_b16 v[28:29], v45 offset:38912\n"
"ds_read_b64_tr_b16 v[30:31], v45 offset:39168\n"
"s_waitcnt lgkmcnt(4)\n"
"s_waitcnt vmcnt(28)\n"
"ds_read_b64_tr_b16 a[16:17], v44 offset:0\n"
"ds_read_b64_tr_b16 a[18:19], v44 offset:256\n"
"ds_read_b64_tr_b16 a[44:45], v45 offset:0\n"
"ds_read_b64_tr_b16 a[46:47], v45 offset:256\n"
"s_waitcnt lgkmcnt(4)\n"
"s_waitcnt vmcnt(26)\n"
"ds_read_b64_tr_b16 a[72:73], v44 offset:2048\n"
"ds_read_b64_tr_b16 a[74:75], v44 offset:2304\n"
"ds_read_b64_tr_b16 a[100:101], v45 offset:2048\n"
"ds_read_b64_tr_b16 a[102:103], v45 offset:2304\n"
"s_waitcnt lgkmcnt(4)\n"
"s_waitcnt vmcnt(24)\n"
"ds_read_b64_tr_b16 a[128:129], v44 offset:4096\n"
"ds_read_b64_tr_b16 a[130:131], v44 offset:4352\n"
"ds_read_b64_tr_b16 a[156:157], v45 offset:4096\n"
"ds_read_b64_tr_b16 a[158:159], v45 offset:4352\n"
"s_waitcnt lgkmcnt(4)\n"
"s_waitcnt vmcnt(22)\n"
"ds_read_b64_tr_b16 a[184:185], v44 offset:6144\n"
"ds_read_b64_tr_b16 a[186:187], v44 offset:6400\n"
"ds_read_b64_tr_b16 a[212:213], v45 offset:6144\n"
"ds_read_b64_tr_b16 a[214:215], v45 offset:6400\n"
"s_waitcnt lgkmcnt(4)\n"
"s_waitcnt vmcnt(20)\n"
"ds_read_b64_tr_b16 a[240:241], v44 offset:8192\n"
"ds_read_b64_tr_b16 a[242:243], v44 offset:8448\n"
"ds_read_b64_tr_b16 v[32:33], v45 offset:8192\n"
"ds_read_b64_tr_b16 v[34:35], v45 offset:8448\n"
"s_waitcnt lgkmcnt(4)\n"
"s_waitcnt vmcnt(18)\n"
"ds_read_b64_tr_b16 a[20:21], v44 offset:10240\n"
"ds_read_b64_tr_b16 a[22:23], v44 offset:10496\n"
"ds_read_b64_tr_b16 a[48:49], v45 offset:10240\n"
"ds_read_b64_tr_b16 a[50:51], v45 offset:10496\n"
"s_waitcnt lgkmcnt(4)\n"
"s_waitcnt vmcnt(16)\n"
"ds_read_b64_tr_b16 a[76:77], v44 offset:12288\n"
"ds_read_b64_tr_b16 a[78:79], v44 offset:12544\n"
"ds_read_b64_tr_b16 a[104:105], v45 offset:12288\n"
"ds_read_b64_tr_b16 a[106:107], v45 offset:12544\n"
"s_waitcnt lgkmcnt(4)\n"
"s_waitcnt vmcnt(14)\n"
"ds_read_b64_tr_b16 a[132:133], v44 offset:14336\n"
"ds_read_b64_tr_b16 a[134:135], v44 offset:14592\n"
"ds_read_b64_tr_b16 a[160:161], v45 offset:14336\n"
"ds_read_b64_tr_b16 a[162:163], v45 offset:14592\n"
"s_waitcnt lgkmcnt(4)\n"
"s_waitcnt vmcnt(12)\n"
"ds_read_b64_tr_b16 a[188:189], v44 offset:16384\n"
"ds_read_b64_tr_b16 a[190:191], v44 offset:16640\n"
"ds_read_b64_tr_b16 a[216:217], v45 offset:16384\n"
"ds_read_b64_tr_b16 a[218:219], v45 offset:16640\n"
"s_waitcnt lgkmcnt(4)\n"
"s_waitcnt vmcnt(10)\n"
"ds_read_b64_tr_b16 a[244:245], v44 offset:18432\n"
"ds_read_b64_tr_b16 a[246:247], v44 offset:18688\n"
"ds_read_b64_tr_b16 v[36:37], v45 offset:18432\n"
"ds_read_b64_tr_b16 v[38:39], v45 offset:18688\n"
"s_waitcnt lgkmcnt(4)\n"
"s_waitcnt vmcnt(8)\n"
"ds_read_b64_tr_b16 a[24:25], v44 offset:20480\n"
"ds_read_b64_tr_b16 a[26:27], v44 offset:20736\n"
"ds_read_b64_tr_b16 a[52:53], v45 offset:20480\n"
"ds_read_b64_tr_b16 a[54:55], v45 offset:20736\n"
"s_waitcnt lgkmcnt(4)\n"
"s_waitcnt vmcnt(6)\n"
"ds_read_b64_tr_b16 a[80:81], v44 offset:22528\n"
"ds_read_b64_tr_b16 a[82:83], v44 offset:22784\n"
"ds_read_b64_tr_b16 a[108:109], v45 offset:22528\n"
"ds_read_b64_tr_b16 a[110:111], v45 offset:22784\n"
"s_waitcnt lgkmcnt(4)\n"
"s_waitcnt vmcnt(4)\n"
"ds_read_b64_tr_b16 a[136:137], v44 offset:24576\n"
"ds_read_b64_tr_b16 a[138:139], v44 offset:24832\n"
"ds_read_b64_tr_b16 a[164:165], v45 offset:24576\n"
"ds_read_b64_tr_b16 a[166:167], v45 offset:24832\n"
"s_waitcnt lgkmcnt(4)\n"
"s_waitcnt vmcnt(2)\n"
"ds_read_b64_tr_b16 a[192:193], v44 offset:26624\n"
"ds_read_b64_tr_b16 a[194:195], v44 offset:26880\n"
"ds_read_b64_tr_b16 a[220:221], v45 offset:26624\n"
"ds_read_b64_tr_b16 a[222:223], v45 offset:26880\n"
"s_waitcnt lgkmcnt(4)\n"
"s_waitcnt vmcnt(0)\n"
"ds_read_b64_tr_b16 a[248:249], v44 offset:28672\n"
"ds_read_b64_tr_b16 a[250:251], v44 offset:28928\n"
"ds_read_b64_tr_b16 v[40:41], v45 offset:28672\n"
"ds_read_b64_tr_b16 v[42:43], v45 offset:28928\n"
"s_waitcnt lgkmcnt(4)\n"
"s_waitcnt vmcnt(0)\n"
"s_waitcnt lgkmcnt(0)\n"
"s_mov_b32 m0, s59\n"
"s_barrier\n"
"v_mov_b32 v128, 0\n"
"v_mov_b32 v129, 0\n"
"v_mov_b32 v132, 0\n"
"v_mov_b32 v133, 0\n"
"v_mov_b32 v136, 0\n"
"v_mov_b32 v137, 0\n"
"v_mov_b32 v168, 0\n"
"v_mov_b32 v169, 0\n"
"s_mov_b32 s93, 0x80000000\n"
"s_mov_b32 s94, 0x80000000\n"
"s_mov_b32 s95, 0x80000000\n"
"s_mov_b32 s94, 0x80000000\n"
"s_mov_b32 s93, 0x80000000\n"
"s_mov_b32 s95, 0x80000000\n"
"s_mov_b32 s95, 0x80000000\n"
"s_mov_b32 s94, 0x80000000\n"
"s_cmp_eq_u32 s54, 0\n"
"s_cbranch_scc1 .LG_1s\n"
"s_cmp_eq_u32 s54, 1\n"
"s_cbranch_scc1 .LG_2s\n"
"s_cmp_eq_u32 s54, 2\n"
"s_cbranch_scc1 .LG_3s\n"
"s_cmp_eq_u32 s54, 3\n"
"s_cbranch_scc1 .LG_4s\n"
".p2align 6\n"
".LG_top:\n"
"v_mfma_f32_16x16x32_bf16 v[56:59], v[100:103], a[0:3], 0\n"
"v_mfma_f32_16x16x32_bf16 v[56:59], v[68:71], a[4:7], v[56:59]\n"
"global_load_dwordx4 v[44:47], v7, s[56:57] offset:0 sc1\n"
"v_mfma_f32_16x16x32_bf16 v[56:59], v[72:75], a[8:11], v[56:59]\n"
"v_mfma_f32_16x16x32_bf16 v[56:59], v[76:79], a[12:15], v[56:59]\n"
"s_waitcnt vmcnt(5)\n"
"v_mfma_f32_16x16x32_bf16 v[56:59], v[80:83], a[16:19], v[56:59]\n"
"s_waitcnt lgkmcnt(3)\n"
"v_add_f32 v120, v120, v64\n"
"v_add_f32 v121, v121, v65\n"
"v_mfma_f32_16x16x32_bf16 v[56:59], v[84:87], a[20:23], v[56:59]\n"
"s_waitcnt vmcnt(4)\n"
"v_add_f32 v122, v122, v66\n"
"v_add_f32 v123, v123, v67\n"
"v_mfma_f32_16x16x32_bf16 v[56:59], v[88:91], a[24:27], v[56:59]\n"
"global_load_dwordx4 v[48:51], v8, s[56:57] offset:0 sc1\n"
"v_add_f32 v120, v120, v92\n"
"v_mfma_f32_16x16x32_bf16 v[60:63], v[100:103], a[28:31], 0\n"
"buffer_load_dwordx2 v[136:137], v12, s[40:43], s50 offen sc1\n"
"v_mfma_f32_16x16x32_bf16 v[60:63], v[68:71], a[32:35], v[60:63]\n"
"ds_write_addtid_b32 v56 offset:0\n"
"v_add_f32 v121, v121, v93\n"
"v_mfma_f32_16x16x32_bf16 v[60:63], v[72:75], a[36:39], v[60:63]\n"
"ds_write_addtid_b32 v57 offset:4640\n"
"global_load_dwordx4 v[52:55], v9, s[56:57] offset:0 sc1\n"
"v_mfma_f32_16x16x32_bf16 v[60:63], v[76:79], a[40:43], v[60:63]\n"
"ds_write_addtid_b32 v58 offset:9344\n"
"v_add_f32 v122, v122, v94\n"
"v_mfma_f32_16x16x32_bf16 v[60:63], v[80:83], a[44:47], v[60:63]\n"
"ds_write_addtid_b32 v59 offset:13984\n"
"v_add_f32 v123, v123, v95\n"
"v_mfma_f32_16x16x32_bf16 v[60:63], v[84:87], a[48:51], v[60:63]\n"
"v_add_f32 v120, v120, v116\n"
"v_add_f32 v121, v121, v117\n"
"v_mfma_f32_16x16x32_bf16 v[60:63], v[88:91], a[52:55], v[60:63]\n"
"v_add_f32 v122, v122, v118\n"
"v_add_f32 v123, v123, v119\n"
"v_mfma_f32_16x16x32_bf16 v[64:67], v[100:103], a[56:59], 0\n"
"s_waitcnt vmcnt(6)\n"
"buffer_store_dwordx2 v[0:1], v14, s[40:43], s93 offen offset:0 sc1\n"
"v_mfma_f32_16x16x32_bf16 v[64:67], v[68:71], a[60:63], v[64:67]\n"
"s_add_u32 s95, s50, 0xffd00000\n"
"s_and_b32 s94, s95, 0xfbffffff\n"
"ds_write_addtid_b32 v60 offset:256\n"
"v_mfma_f32_16x16x32_bf16 v[64:67], v[72:75], a[64:67], v[64:67]\n"
"ds_write_addtid_b32 v61 offset:4896\n"
"global_load_dwordx4 v[56:59], v10, s[56:57] offset:0 sc1\n"
"v_mfma_f32_16x16x32_bf16 v[64:67], v[76:79], a[68:71], v[64:67]\n"
"ds_write_addtid_b32 v62 offset:9600\n"
"v_cvt_pk_bf16_f32 v92, v120, v121\n"
"v_mfma_f32_16x16x32_bf16 v[64:67], v[80:83], a[72:75], v[64:67]\n"
"ds_write_addtid_b32 v63 offset:14240\n"
"v_cvt_pk_bf16_f32 v93, v122, v123\n"
"v_mfma_f32_16x16x32_bf16 v[64:67], v[84:87], a[76:79], v[64:67]\n"
"s_waitcnt lgkmcnt(8)\n"
"v_add_f32 v124, v124, v128\n"
"v_add_f32 v125, v125, v129\n"
"v_mfma_f32_16x16x32_bf16 v[64:67], v[88:91], a[80:83], v[64:67]\n"
"v_add_f32 v126, v126, v130\n"
"v_add_f32 v127, v127, v131\n"
"v_mfma_f32_16x16x32_bf16 v[96:99], v[100:103], a[84:87], 0\n"
"global_load_dwordx4 v[60:63], v11, s[56:57] offset:0 sc1\n"
"v_add_f32 v124, v124, v132\n"
"v_mfma_f32_16x16x32_bf16 v[96:99], v[68:71], a[88:91], v[96:99]\n"
"ds_write_addtid_b32 v64 offset:512\n"
"v_add_f32 v125, v125, v133\n"
"v_mfma_f32_16x16x32_bf16 v[96:99], v[72:75], a[92:95], v[96:99]\n"
"ds_write_addtid_b32 v65 offset:5152\n"
"v_add_f32 v126, v126, v134\n"
"v_mfma_f32_16x16x32_bf16 v[96:99], v[76:79], a[96:99], v[96:99]\n"
"ds_write_addtid_b32 v66 offset:9856\n"
"v_add_f32 v127, v127, v135\n"
"v_mfma_f32_16x16x32_bf16 v[96:99], v[80:83], a[100:103], v[96:99]\n"
"ds_write_addtid_b32 v67 offset:14496\n"
"v_add_f32 v124, v124, v112\n"
"v_mfma_f32_16x16x32_bf16 v[96:99], v[84:87], a[104:107], v[96:99]\n"
"v_add_f32 v125, v125, v113\n"
"v_add_f32 v126, v126, v114\n"
"v_mfma_f32_16x16x32_bf16 v[96:99], v[88:91], a[108:111], v[96:99]\n"
"v_add_f32 v127, v127, v115\n"
"v_cvt_pk_bf16_f32 v94, v124, v125\n"
"v_mfma_f32_16x16x32_bf16 v[64:67], v[100:103], a[112:115], 0\n"
"v_cvt_pk_bf16_f32 v95, v126, v127\n"
"v_mfma_f32_16x16x32_bf16 v[64:67], v[68:71], a[116:119], v[64:67]\n"
"ds_write_addtid_b32 v96 offset:768\n"
"v_mfma_f32_16x16x32_bf16 v[64:67], v[72:75], a[120:123], v[64:67]\n"
"ds_write_addtid_b32 v97 offset:5408\n"
"v_mfma_f32_16x16x32_bf16 v[64:67], v[76:79], a[124:127], v[64:67]\n"
"ds_write_addtid_b32 v98 offset:10112\n"
"v_mfma_f32_16x16x32_bf16 v[64:67], v[80:83], a[128:131], v[64:67]\n"
"ds_write_addtid_b32 v99 offset:14752\n"
"v_mfma_f32_16x16x32_bf16 v[64:67], v[84:87], a[132:135], v[64:67]\n"
"v_mfma_f32_16x16x32_bf16 v[64:67], v[88:91], a[136:139], v[64:67]\n"
"v_mfma_f32_16x16x32_bf16 v[112:115], v[100:103], a[140:143], 0\n"
"v_mfma_f32_16x16x32_bf16 v[112:115], v[68:71], a[144:147], v[112:115]\n"
"ds_write_addtid_b32 v64 offset:1024\n"
"v_mfma_f32_16x16x32_bf16 v[112:115], v[72:75], a[148:151], v[112:115]\n"
"ds_write_addtid_b32 v65 offset:5664\n"
"v_mfma_f32_16x16x32_bf16 v[112:115], v[76:79], a[152:155], v[112:115]\n"
"ds_write_addtid_b32 v66 offset:10368\n"
"v_mfma_f32_16x16x32_bf16 v[112:115], v[80:83], a[156:159], v[112:115]\n"
"ds_write_addtid_b32 v67 offset:15008\n"
"v_mfma_f32_16x16x32_bf16 v[112:115], v[84:87], a[160:163], v[112:115]\n"
"buffer_store_dwordx4 v[92:95], v13, s[40:43], s94 offen sc1\n"
"v_mfma_f32_16x16x32_bf16 v[112:115], v[88:91], a[164:167], v[112:115]\n"
"v_mfma_f32_16x16x32_bf16 v[96:99], a[168:171], v[100:103], 0\n"
"v_mfma_f32_16x16x32_bf16 v[96:99], a[172:175], v[68:71], v[96:99]\n"
"ds_write_addtid_b32 v112 offset:1280\n"
"v_mfma_f32_16x16x32_bf16 v[96:99], a[176:179], v[72:75], v[96:99]\n"
"ds_write_addtid_b32 v113 offset:5920\n"
"v_mfma_f32_16x16x32_bf16 v[96:99], a[180:183], v[76:79], v[96:99]\n"
"ds_write_addtid_b32 v114 offset:10624\n"
"v_mfma_f32_16x16x32_bf16 v[96:99], a[184:187], v[80:83], v[96:99]\n"
"ds_write_addtid_b32 v115 offset:15264\n"
"v_mfma_f32_16x16x32_bf16 v[96:99], a[188:191], v[84:87], v[96:99]\n"
"v_mfma_f32_16x16x32_bf16 v[96:99], a[192:195], v[88:91], v[96:99]\n"
"v_mfma_f32_16x16x32_bf16 v[92:95], a[196:199], v[100:103], 0\n"
"v_mfma_f32_16x16x32_bf16 v[92:95], a[200:203], v[68:71], v[92:95]\n"
"v_mfma_f32_16x16x32_bf16 v[92:95], a[204:207], v[72:75], v[92:95]\n"
"v_mfma_f32_16x16x32_bf16 v[92:95], a[208:211], v[76:79], v[92:95]\n"
"s_add_u32 s58, s50, 0x180000\n"
"v_mfma_f32_16x16x32_bf16 v[92:95], a[212:215], v[80:83], v[92:95]\n"
"s_and_b32 s58, s58, 0x3ffffff\n"
"v_mfma_f32_16x16x32_bf16 v[92:95], a[216:219], v[84:87], v[92:95]\n"
"s_add_u32 s56, s40, s58\n"
"v_mfma_f32_16x16x32_bf16 v[92:95], a[220:223], v[88:91], v[92:95]\n"
"s_addc_u32 s57, s41, 0\n"
"v_mfma_f32_16x16x32_bf16 v[112:115], a[224:227], v[100:103], 0\n"
"s_waitcnt vmcnt(5)\n"
"v_cmp_ne_u64 vcc, s[48:49], v[136:137]\n"
"v_mfma_f32_16x16x32_bf16 v[112:115], a[228:231], v[68:71], v[112:115]\n"
"v_mfma_f32_16x16x32_bf16 v[112:115], a[232:235], v[72:75], v[112:115]\n"
"s_cbranch_vccnz .LG_3s\n"
".LG_3r:\n"
"v_mfma_f32_16x16x32_bf16 v[112:115], a[236:239], v[76:79], v[112:115]\n"
"s_add_u32 s50, s50, 0x180000\n"
"global_load_dwordx4 v[64:67], v5, s[56:57] offset:0 sc1\n"
"v_mfma_f32_16x16x32_bf16 v[112:115], a[240:243], v[80:83], v[112:115]\n"
"v_mfma_f32_16x16x32_bf16 v[112:115], a[244:247], v[84:87], v[112:115]\n"
"v_mfma_f32_16x16x32_bf16 v[112:115], a[248:251], v[88:91], v[112:115]\n"
"v_mfma_f32_16x16x32_bf16 v[116:119], a[252:255], v[100:103], 0\n"
"v_mfma_f32_16x16x32_bf16 v[116:119], v[20:23], v[68:71], v[116:119]\n"
"s_waitcnt lgkmcnt(0)\n"
"v_mfma_f32_16x16x32_bf16 v[116:119], v[24:27], v[72:75], v[116:119]\n"
"s_barrier\n"
"v_mfma_f32_16x16x32_bf16 v[116:119], v[28:31], v[76:79], v[116:119]\n"
"ds_read_b128 v[100:103], v2 offset:2048\n"
"ds_read_b128 v[120:123], v3 offset:512\n"
"v_mfma_f32_16x16x32_bf16 v[116:119], v[32:35], v[80:83], v[116:119]\n"
"global_load_dwordx4 v[68:71], v6, s[56:57] offset:0 sc1\n"
"ds_read_b128 v[124:127], v4 offset:3072\n"
"v_mfma_f32_16x16x32_bf16 v[116:119], v[36:39], v[84:87], v[116:119]\n"
"ds_read_b128 v[128:131], v2 offset:2304\n"
"ds_read_b128 v[132:135], v3 offset:768\n"
"v_mfma_f32_16x16x32_bf16 v[116:119], v[40:43], v[88:91], v[116:119]\n"
"global_load_dwordx4 v[72:75], v7, s[56:57] offset:0 sc1\n"
"ds_read_b128 v[136:139], v4 offset:3328\n"
"v_mfma_f32_16x16x32_bf16 v[84:87], v[104:107], a[0:3], 0\n"
"ds_read_b128 v[140:143], v2 offset:3584\n"
"ds_read_b128 v[144:147], v3 offset:1536\n"
"v_mfma_f32_16x16x32_bf16 v[84:87], v[108:111], a[4:7], v[84:87]\n"
"ds_read_b128 v[148:151], v4 offset:3584\n"
"ds_read_b128 v[152:155], v2 offset:3840\n"
"v_mfma_f32_16x16x32_bf16 v[84:87], v[44:47], a[8:11], v[84:87]\n"
"ds_read_b128 v[156:159], v3 offset:1792\n"
"ds_read_b128 v[160:163], v4 offset:3840\n"
"v_mfma_f32_16x16x32_bf16 v[84:87], v[48:51], a[12:15], v[84:87]\n"
"s_waitcnt vmcnt(5)\n"
"v_mfma_f32_16x16x32_bf16 v[84:87], v[52:55], a[16:19], v[84:87]\n"
"s_waitcnt lgkmcnt(9)\n"
"global_load_dwordx4 v[76:79], v8, s[56:57] offset:0 sc1\n"
"v_add_f32 v100, v100, v120\n"
"v_mfma_f32_16x16x32_bf16 v[84:87], v[56:59], a[20:23], v[84:87]\n"
"s_waitcnt vmcnt(5)\n"
"v_add_f32 v101, v101, v121\n"
"v_add_f32 v102, v102, v122\n"
"v_mfma_f32_16x16x32_bf16 v[84:87], v[60:63], a[24:27], v[84:87]\n"
"v_add_f32 v103, v103, v123\n"
"v_add_f32 v100, v100, v124\n"
"v_mfma_f32_16x16x32_bf16 v[164:167], v[104:107], a[28:31], 0\n"
"buffer_load_dwordx2 v[168:169], v12, s[40:43], s50 offen sc1\n"
"v_mfma_f32_16x16x32_bf16 v[164:167], v[108:111], a[32:35], v[164:167]\n"
"ds_write_addtid_b32 v84 offset:2048\n"
"v_add_f32 v101, v101, v125\n"
"v_mfma_f32_16x16x32_bf16 v[164:167], v[44:47], a[36:39], v[164:167]\n"
"ds_write_addtid_b32 v85 offset:6688\n"
"v_add_f32 v102, v102, v126\n"
"v_mfma_f32_16x16x32_bf16 v[164:167], v[48:51], a[40:43], v[164:167]\n"
"ds_write_addtid_b32 v86 offset:11392\n"
"v_add_f32 v103, v103, v127\n"
"v_mfma_f32_16x16x32_bf16 v[164:167], v[52:55], a[44:47], v[164:167]\n"
"ds_write_addtid_b32 v87 offset:16032\n"
"v_add_f32 v100, v100, v96\n"
"v_mfma_f32_16x16x32_bf16 v[164:167], v[56:59], a[48:51], v[164:167]\n"
"global_load_dwordx4 v[80:83], v9, s[56:57] offset:0 sc1\n"
"v_add_f32 v101, v101, v97\n"
"v_mfma_f32_16x16x32_bf16 v[164:167], v[60:63], a[52:55], v[164:167]\n"
"v_add_f32 v102, v102, v98\n"
"v_add_f32 v103, v103, v99\n"
"v_mfma_f32_16x16x32_bf16 v[120:123], v[104:107], a[56:59], 0\n"
"s_add_u32 s93, s50, 0xffd00000\n"
"s_and_b32 s94, s93, 0xfbffffff\n"
"v_cvt_pk_bf16_f32 v124, v100, v101\n"
"v_mfma_f32_16x16x32_bf16 v[120:123], v[108:111], a[60:63], v[120:123]\n"
"ds_write_addtid_b32 v164 offset:2304\n"
"v_cvt_pk_bf16_f32 v125, v102, v103\n"
"v_mfma_f32_16x16x32_bf16 v[120:123], v[44:47], a[64:67], v[120:123]\n"
"s_waitcnt lgkmcnt(11)\n"
"ds_write_addtid_b32 v165 offset:6944\n"
"v_add_f32 v128, v128, v132\n"
"v_mfma_f32_16x16x32_bf16 v[120:123], v[48:51], a[68:71], v[120:123]\n"
"ds_write_addtid_b32 v166 offset:11648\n"
"v_add_f32 v129, v129, v133\n"
"v_mfma_f32_16x16x32_bf16 v[120:123], v[52:55], a[72:75], v[120:123]\n"
"s_waitcnt vmcnt(6)\n"
"ds_write_addtid_b32 v167 offset:16288\n"
"global_load_dwordx4 v[84:87], v10, s[56:57] offset:0 sc1\n"
"v_mfma_f32_16x16x32_bf16 v[120:123], v[56:59], a[76:79], v[120:123]\n"
"global_load_dwordx4 v[88:91], v11, s[56:57] offset:0 sc1\n"
"v_add_f32 v130, v130, v134\n"
"v_mfma_f32_16x16x32_bf16 v[120:123], v[60:63], a[80:83], v[120:123]\n"
"v_add_f32 v131, v131, v135\n"
"v_add_f32 v128, v128, v136\n"
"v_mfma_f32_16x16x32_bf16 v[96:99], v[104:107], a[84:87], 0\n"
"v_add_f32 v129, v129, v137\n"
"v_add_f32 v130, v130, v138\n"
"v_mfma_f32_16x16x32_bf16 v[96:99], v[108:111], a[88:91], v[96:99]\n"
"ds_write_addtid_b32 v120 offset:2560\n"
"v_add_f32 v131, v131, v139\n"
"v_mfma_f32_16x16x32_bf16 v[96:99], v[44:47], a[92:95], v[96:99]\n"
"ds_write_addtid_b32 v121 offset:7200\n"
"v_add_f32 v128, v128, v92\n"
"v_mfma_f32_16x16x32_bf16 v[96:99], v[48:51], a[96:99], v[96:99]\n"
"ds_write_addtid_b32 v122 offset:11904\n"
"v_add_f32 v129, v129, v93\n"
"v_mfma_f32_16x16x32_bf16 v[96:99], v[52:55], a[100:103], v[96:99]\n"
"ds_write_addtid_b32 v123 offset:16544\n"
"v_add_f32 v130, v130, v94\n"
"v_mfma_f32_16x16x32_bf16 v[96:99], v[56:59], a[104:107], v[96:99]\n"
"v_add_f32 v131, v131, v95\n"
"v_cvt_pk_bf16_f32 v126, v128, v129\n"
"v_mfma_f32_16x16x32_bf16 v[96:99], v[60:63], a[108:111], v[96:99]\n"
"buffer_store_dwordx2 v[0:1], v14, s[40:43], s95 offen offset:0 sc1\n"
"v_cvt_pk_bf16_f32 v127, v130, v131\n"
"v_mfma_f32_16x16x32_bf16 v[92:95], v[104:107], a[112:115], 0\n"
"buffer_store_dwordx4 v[124:127], v13, s[40:43], s94 offen sc1\n"
"v_mfma_f32_16x16x32_bf16 v[92:95], v[108:111], a[116:119], v[92:95]\n"
"s_waitcnt lgkmcnt(15)\n"
"ds_write_addtid_b32 v96 offset:2816\n"
"v_add_f32 v140, v140, v144\n"
"v_mfma_f32_16x16x32_bf16 v[92:95], v[44:47], a[120:123], v[92:95]\n"
"ds_write_addtid_b32 v97 offset:7456\n"
"v_add_f32 v141, v141, v145\n"
"v_mfma_f32_16x16x32_bf16 v[92:95], v[48:51], a[124:127], v[92:95]\n"
"ds_write_addtid_b32 v98 offset:12160\n"
"v_add_f32 v142, v142, v146\n"
"v_mfma_f32_16x16x32_bf16 v[92:95], v[52:55], a[128:131], v[92:95]\n"
"ds_write_addtid_b32 v99 offset:16800\n"
"v_add_f32 v143, v143, v147\n"
"v_mfma_f32_16x16x32_bf16 v[92:95], v[56:59], a[132:135], v[92:95]\n"
"v_add_f32 v140, v140, v148\n"
"v_add_f32 v141, v141, v149\n"
"v_mfma_f32_16x16x32_bf16 v[92:95], v[60:63], a[136:139], v[92:95]\n"
"v_add_f32 v142, v142, v150\n"
"v_add_f32 v143, v143, v151\n"
"v_mfma_f32_16x16x32_bf16 v[96:99], v[104:107], a[140:143], 0\n"
"v_add_f32 v140, v140, v112\n"
"v_add_f32 v141, v141, v113\n"
"v_mfma_f32_16x16x32_bf16 v[96:99], v[108:111], a[144:147], v[96:99]\n"
"ds_write_addtid_b32 v92 offset:3072\n"
"v_add_f32 v142, v142, v114\n"
"v_mfma_f32_16x16x32_bf16 v[96:99], v[44:47], a[148:151], v[96:99]\n"
"ds_write_addtid_b32 v93 offset:7712\n"
"v_add_f32 v143, v143, v115\n"
"v_mfma_f32_16x16x32_bf16 v[96:99], v[48:51], a[152:155], v[96:99]\n"
"ds_write_addtid_b32 v94 offset:12416\n"
"v_cvt_pk_bf16_f32 v100, v140, v141\n"
"v_mfma_f32_16x16x32_bf16 v[96:99], v[52:55], a[156:159], v[96:99]\n"
"ds_write_addtid_b32 v95 offset:17056\n"
"v_cvt_pk_bf16_f32 v101, v142, v143\n"
"v_mfma_f32_16x16x32_bf16 v[96:99], v[56:59], a[160:163], v[96:99]\n"
"s_waitcnt lgkmcnt(15)\n"
"v_add_f32 v152, v152, v156\n"
"v_add_f32 v153, v153, v157\n"
"v_mfma_f32_16x16x32_bf16 v[96:99], v[60:63], a[164:167], v[96:99]\n"
"v_add_f32 v154, v154, v158\n"
"v_add_f32 v155, v155, v159\n"
"v_mfma_f32_16x16x32_bf16 v[92:95], v[104:107], a[224:227], 0\n"
"v_add_f32 v152, v152, v160\n"
"v_add_f32 v153, v153, v161\n"
"v_mfma_f32_16x16x32_bf16 v[92:95], v[108:111], a[228:231], v[92:95]\n"
"ds_write_addtid_b32 v96 offset:3328\n"
"v_add_f32 v154, v154, v162\n"
"v_mfma_f32_16x16x32_bf16 v[92:95], v[44:47], a[232:235], v[92:95]\n"
"ds_write_addtid_b32 v97 offset:7968\n"
"v_add_f32 v155, v155, v163\n"
"v_mfma_f32_16x16x32_bf16 v[92:95], v[48:51], a[236:239], v[92:95]\n"
"ds_write_addtid_b32 v98 offset:12672\n"
"v_add_f32 v152, v152, v116\n"
"v_mfma_f32_16x16x32_bf16 v[92:95], v[52:55], a[240:243], v[92:95]\n"
"ds_write_addtid_b32 v99 offset:17312\n"
"v_add_f32 v153, v153, v117\n"
"v_mfma_f32_16x16x32_bf16 v[92:95], v[56:59], a[244:247], v[92:95]\n"
"v_add_f32 v154, v154, v118\n"
"v_add_f32 v155, v155, v119\n"
"v_mfma_f32_16x16x32_bf16 v[92:95], v[60:63], a[248:251], v[92:95]\n"
"v_cvt_pk_bf16_f32 v102, v152, v153\n"
"v_cvt_pk_bf16_f32 v103, v154, v155\n"
"v_mfma_f32_16x16x32_bf16 v[96:99], v[104:107], a[252:255], 0\n"
"buffer_store_dwordx4 v[100:103], v15, s[40:43], s94 offen sc1\n"
"v_mfma_f32_16x16x32_bf16 v[96:99], v[108:111], v[20:23], v[96:99]\n"
"ds_write_addtid_b32 v92 offset:3584\n"
"v_mfma_f32_16x16x32_bf16 v[96:99], v[44:47], v[24:27], v[96:99]\n"
"ds_write_addtid_b32 v93 offset:8224\n"
"v_mfma_f32_16x16x32_bf16 v[96:99], v[48:51], v[28:31], v[96:99]\n"
"ds_write_addtid_b32 v94 offset:12928\n"
"v_mfma_f32_16x16x32_bf16 v[96:99], v[52:55], v[32:35], v[96:99]\n"
"ds_write_addtid_b32 v95 offset:17568\n"
"v_mfma_f32_16x16x32_bf16 v[96:99], v[56:59], v[36:39], v[96:99]\n"
"v_mfma_f32_16x16x32_bf16 v[96:99], v[60:63], v[40:43], v[96:99]\n"
"s_add_u32 s58, s50, 0x180000\n"
"v_mfma_f32_16x16x32_bf16 v[100:103], a[168:171], v[104:107], 0\n"
"s_and_b32 s58, s58, 0x3ffffff\n"
"v_mfma_f32_16x16x32_bf16 v[100:103], a[172:175], v[108:111], v[100:103]\n"
"s_add_u32 s56, s40, s58\n"
"ds_write_addtid_b32 v96 offset:3840\n"
"v_mfma_f32_16x16x32_bf16 v[100:103], a[176:179], v[44:47], v[100:103]\n"
"s_addc_u32 s57, s41, 0\n"
"ds_write_addtid_b32 v97 offset:8480\n"
"v_mfma_f32_16x16x32_bf16 v[100:103], a[180:183], v[48:51], v[100:103]\n"
"s_waitcnt vmcnt(6)\n"
"ds_write_addtid_b32 v98 offset:13184\n"
"v_cmp_ne_u64 vcc, s[48:49], v[168:169]\n"
"v_mfma_f32_16x16x32_bf16 v[100:103], a[184:187], v[52:55], v[100:103]\n"
"ds_write_addtid_b32 v99 offset:17824\n"
"v_mfma_f32_16x16x32_bf16 v[100:103], a[188:191], v[56:59], v[100:103]\n"
"s_cbranch_vccnz .LG_4s\n"
".LG_4r:\n"
"v_mfma_f32_16x16x32_bf16 v[100:103], a[192:195], v[60:63], v[100:103]\n"
"s_add_u32 s50, s50, 0x180000\n"
"global_load_dwordx4 v[92:95], v5, s[56:57] offset:0 sc1\n"
"v_mfma_f32_16x16x32_bf16 v[112:115], a[196:199], v[104:107], 0\n"
"v_mfma_f32_16x16x32_bf16 v[112:115], a[200:203], v[108:111], v[112:115]\n"
"s_waitcnt lgkmcnt(0)\n"
"v_mfma_f32_16x16x32_bf16 v[112:115], a[204:207], v[44:47], v[112:115]\n"
"s_barrier\n"
"v_mfma_f32_16x16x32_bf16 v[112:115], a[208:211], v[48:51], v[112:115]\n"
"ds_read_b128 v[96:99], v2 offset:0\n"
"ds_read_b128 v[104:107], v3 offset:2560\n"
"v_mfma_f32_16x16x32_bf16 v[112:115], a[212:215], v[52:55], v[112:115]\n"
"ds_read_b128 v[108:111], v4 offset:1024\n"
"ds_read_b128 v[116:119], v2 offset:256\n"
"v_mfma_f32_16x16x32_bf16 v[112:115], a[216:219], v[56:59], v[112:115]\n"
"global_load_dwordx4 v[44:47], v6, s[56:57] offset:0 sc1\n"
"ds_read_b128 v[120:123], v3 offset:2816\n"
"v_mfma_f32_16x16x32_bf16 v[112:115], a[220:223], v[60:63], v[112:115]\n"
"ds_read_b128 v[124:127], v4 offset:1280\n"
"v_mfma_f32_16x16x32_bf16 v[56:59], v[64:67], a[0:3], 0\n"
"v_mfma_f32_16x16x32_bf16 v[56:59], v[68:71], a[4:7], v[56:59]\n"
"v_mfma_f32_16x16x32_bf16 v[56:59], v[72:75], a[8:11], v[56:59]\n"
"global_load_dwordx4 v[48:51], v7, s[56:57] offset:0 sc1\n"
"v_mfma_f32_16x16x32_bf16 v[56:59], v[76:79], a[12:15], v[56:59]\n"
"s_waitcnt vmcnt(7)\n"
"buffer_load_dwordx2 v[128:129], v12, s[40:43], s50 offen sc1\n"
"v_mfma_f32_16x16x32_bf16 v[56:59], v[80:83], a[16:19], v[56:59]\n"
"s_waitcnt lgkmcnt(3)\n"
"v_add_f32 v96, v96, v104\n"
"v_add_f32 v97, v97, v105\n"
"v_mfma_f32_16x16x32_bf16 v[56:59], v[84:87], a[20:23], v[56:59]\n"
"s_waitcnt vmcnt(7)\n"
"v_add_f32 v98, v98, v106\n"
"v_add_f32 v99, v99, v107\n"
"v_mfma_f32_16x16x32_bf16 v[56:59], v[88:91], a[24:27], v[56:59]\n"
"v_add_f32 v96, v96, v108\n"
"v_add_f32 v97, v97, v109\n"
"v_mfma_f32_16x16x32_bf16 v[104:107], v[64:67], a[28:31], 0\n"
"global_load_dwordx4 v[52:55], v8, s[56:57] offset:0 sc1\n"
"v_add_f32 v98, v98, v110\n"
"v_mfma_f32_16x16x32_bf16 v[104:107], v[68:71], a[32:35], v[104:107]\n"
"ds_write_addtid_b32 v56 offset:0\n"
"v_add_f32 v99, v99, v111\n"
"v_mfma_f32_16x16x32_bf16 v[104:107], v[72:75], a[36:39], v[104:107]\n"
"ds_write_addtid_b32 v57 offset:4640\n"
"v_add_f32 v96, v96, v100\n"
"v_mfma_f32_16x16x32_bf16 v[104:107], v[76:79], a[40:43], v[104:107]\n"
"ds_write_addtid_b32 v58 offset:9344\n"
"v_add_f32 v97, v97, v101\n"
"v_mfma_f32_16x16x32_bf16 v[104:107], v[80:83], a[44:47], v[104:107]\n"
"ds_write_addtid_b32 v59 offset:13984\n"
"v_add_f32 v98, v98, v102\n"
"v_mfma_f32_16x16x32_bf16 v[104:107], v[84:87], a[48:51], v[104:107]\n"
"v_add_f32 v99, v99, v103\n"
"v_mfma_f32_16x16x32_bf16 v[104:107], v[88:91], a[52:55], v[104:107]\n"
"s_add_u32 s95, s50, 0xffd00000\n"
"s_and_b32 s94, s95, 0xfbffffff\n"
"global_load_dwordx4 v[56:59], v9, s[56:57] offset:0 sc1\n"
"v_mfma_f32_16x16x32_bf16 v[100:103], v[64:67], a[56:59], 0\n"
"v_cvt_pk_bf16_f32 v108, v96, v97\n"
"v_cvt_pk_bf16_f32 v109, v98, v99\n"
"v_mfma_f32_16x16x32_bf16 v[100:103], v[68:71], a[60:63], v[100:103]\n"
"s_waitcnt lgkmcnt(4)\n"
"ds_write_addtid_b32 v104 offset:256\n"
"v_add_f32 v116, v116, v120\n"
"v_mfma_f32_16x16x32_bf16 v[100:103], v[72:75], a[64:67], v[100:103]\n"
"ds_write_addtid_b32 v105 offset:4896\n"
"v_add_f32 v117, v117, v121\n"
"v_mfma_f32_16x16x32_bf16 v[100:103], v[76:79], a[68:71], v[100:103]\n"
"ds_write_addtid_b32 v106 offset:9600\n"
"global_load_dwordx4 v[60:63], v10, s[56:57] offset:0 sc1\n"
"v_mfma_f32_16x16x32_bf16 v[100:103], v[80:83], a[72:75], v[100:103]\n"
"ds_write_addtid_b32 v107 offset:14240\n"
"v_add_f32 v118, v118, v122\n"
"v_mfma_f32_16x16x32_bf16 v[100:103], v[84:87], a[76:79], v[100:103]\n"
"v_add_f32 v119, v119, v123\n"
"v_add_f32 v116, v116, v124\n"
"v_mfma_f32_16x16x32_bf16 v[100:103], v[88:91], a[80:83], v[100:103]\n"
"v_add_f32 v117, v117, v125\n"
"v_add_f32 v118, v118, v126\n"
"v_mfma_f32_16x16x32_bf16 v[104:107], v[64:67], a[84:87], 0\n"
"v_add_f32 v119, v119, v127\n"
"v_add_f32 v116, v116, v112\n"
"v_mfma_f32_16x16x32_bf16 v[104:107], v[68:71], a[88:91], v[104:107]\n"
"ds_write_addtid_b32 v100 offset:512\n"
"v_add_f32 v117, v117, v113\n"
"v_mfma_f32_16x16x32_bf16 v[104:107], v[72:75], a[92:95], v[104:107]\n"
"ds_write_addtid_b32 v101 offset:5152\n"
"global_load_dwordx4 v[96:99], v11, s[56:57] offset:0 sc1\n"
"v_mfma_f32_16x16x32_bf16 v[104:107], v[76:79], a[96:99], v[104:107]\n"
"ds_write_addtid_b32 v102 offset:9856\n"
"v_add_f32 v118, v118, v114\n"
"v_mfma_f32_16x16x32_bf16 v[104:107], v[80:83], a[100:103], v[104:107]\n"
"ds_write_addtid_b32 v103 offset:14496\n"
"v_add_f32 v119, v119, v115\n"
"v_mfma_f32_16x16x32_bf16 v[104:107], v[84:87], a[104:107], v[104:107]\n"
"v_cvt_pk_bf16_f32 v110, v116, v117\n"
"v_cvt_pk_bf16_f32 v111, v118, v119\n"
"v_mfma_f32_16x16x32_bf16 v[104:107], v[88:91], a[108:111], v[104:107]\n"
"s_waitcnt vmcnt(8)\n"
"buffer_store_dwordx2 v[0:1], v16, s[40:43], s93 offen offset:0 sc1\n"
"v_mfma_f32_16x16x32_bf16 v[100:103], v[64:67], a[112:115], 0\n"
"v_mfma_f32_16x16x32_bf16 v[100:103], v[68:71], a[116:119], v[100:103]\n"
"ds_write_addtid_b32 v104 offset:768\n"
"v_mfma_f32_16x16x32_bf16 v[100:103], v[72:75], a[120:123], v[100:103]\n"
"buffer_store_dwordx4 v[108:111], v13, s[40:43], s94 offen sc1\n"
"ds_write_addtid_b32 v105 offset:5408\n"
"v_mfma_f32_16x16x32_bf16 v[100:103], v[76:79], a[124:127], v[100:103]\n"
"ds_write_addtid_b32 v106 offset:10112\n"
"v_mfma_f32_16x16x32_bf16 v[100:103], v[80:83], a[128:131], v[100:103]\n"
"ds_write_addtid_b32 v107 offset:14752\n"
"v_mfma_f32_16x16x32_bf16 v[100:103], v[84:87], a[132:135], v[100:103]\n"
"v_mfma_f32_16x16x32_bf16 v[100:103], v[88:91], a[136:139], v[100:103]\n"
"v_mfma_f32_16x16x32_bf16 v[108:111], v[64:67], a[140:143], 0\n"
"v_mfma_f32_16x16x32_bf16 v[108:111], v[68:71], a[144:147], v[108:111]\n"
"ds_write_addtid_b32 v100 offset:1024\n"
"v_mfma_f32_16x16x32_bf16 v[108:111], v[72:75], a[148:151], v[108:111]\n"
"ds_write_addtid_b32 v101 offset:5664\n"
"v_mfma_f32_16x16x32_bf16 v[108:111], v[76:79], a[152:155], v[108:111]\n"
"ds_write_addtid_b32 v102 offset:10368\n"
"v_mfma_f32_16x16x32_bf16 v[108:111], v[80:83], a[156:159], v[108:111]\n"
"ds_write_addtid_b32 v103 offset:15008\n"
"v_mfma_f32_16x16x32_bf16 v[108:111], v[84:87], a[160:163], v[108:111]\n"
"v_mfma_f32_16x16x32_bf16 v[108:111], v[88:91], a[164:167], v[108:111]\n"
"v_mfma_f32_16x16x32_bf16 v[104:107], v[64:67], a[224:227], 0\n"
"v_mfma_f32_16x16x32_bf16 v[104:107], v[68:71], a[228:231], v[104:107]\n"
"ds_write_addtid_b32 v108 offset:1280\n"
"v_mfma_f32_16x16x32_bf16 v[104:107], v[72:75], a[232:235], v[104:107]\n"
"ds_write_addtid_b32 v109 offset:5920\n"
"v_mfma_f32_16x16x32_bf16 v[104:107], v[76:79], a[236:239], v[104:107]\n"
"ds_write_addtid_b32 v110 offset:10624\n"
"v_mfma_f32_16x16x32_bf16 v[104:107], v[80:83], a[240:243], v[104:107]\n"
"ds_write_addtid_b32 v111 offset:15264\n"
"v_mfma_f32_16x16x32_bf16 v[104:107], v[84:87], a[244:247], v[104:107]\n"
"v_mfma_f32_16x16x32_bf16 v[104:107], v[88:91], a[248:251], v[104:107]\n"
"v_mfma_f32_16x16x32_bf16 v[112:115], v[64:67], a[252:255], 0\n"
"v_mfma_f32_16x16x32_bf16 v[112:115], v[68:71], v[20:23], v[112:115]\n"
"s_add_u32 s58, s50, 0x180000\n"
"ds_write_addtid_b32 v104 offset:1536\n"
"v_mfma_f32_16x16x32_bf16 v[112:115], v[72:75], v[24:27], v[112:115]\n"
"s_and_b32 s58, s58, 0x3ffffff\n"
"ds_write_addtid_b32 v105 offset:6176\n"
"v_mfma_f32_16x16x32_bf16 v[112:115], v[76:79], v[28:31], v[112:115]\n"
"s_add_u32 s56, s40, s58\n"
"ds_write_addtid_b32 v106 offset:10880\n"
"v_mfma_f32_16x16x32_bf16 v[112:115], v[80:83], v[32:35], v[112:115]\n"
"s_addc_u32 s57, s41, 0\n"
"ds_write_addtid_b32 v107 offset:15520\n"
"v_mfma_f32_16x16x32_bf16 v[112:115], v[84:87], v[36:39], v[112:115]\n"
"s_waitcnt vmcnt(6)\n"
"v_cmp_ne_u64 vcc, s[48:49], v[128:129]\n"
"v_mfma_f32_16x16x32_bf16 v[112:115], v[88:91], v[40:43], v[112:115]\n"
"v_mfma_f32_16x16x32_bf16 v[104:107], a[168:171], v[64:67], 0\n"
"s_cbranch_vccnz .LG_1s\n"
".LG_1r:\n"
"v_mfma_f32_16x16x32_bf16 v[104:107], a[172:175], v[68:71], v[104:107]\n"
"s_add_u32 s50, s50, 0x180000\n"
"ds_write_addtid_b32 v112 offset:1792\n"
"v_mfma_f32_16x16x32_bf16 v[104:107], a[176:179], v[72:75], v[104:107]\n"
"ds_write_addtid_b32 v113 offset:6432\n"
"v_mfma_f32_16x16x32_bf16 v[104:107], a[180:183], v[76:79], v[104:107]\n"
"ds_write_addtid_b32 v114 offset:11136\n"
"v_mfma_f32_16x16x32_bf16 v[104:107], a[184:187], v[80:83], v[104:107]\n"
"ds_write_addtid_b32 v115 offset:15776\n"
"global_load_dwordx4 v[100:103], v5, s[56:57] offset:0 sc1\n"
"v_mfma_f32_16x16x32_bf16 v[104:107], a[188:191], v[84:87], v[104:107]\n"
"v_mfma_f32_16x16x32_bf16 v[104:107], a[192:195], v[88:91], v[104:107]\n"
"v_mfma_f32_16x16x32_bf16 v[108:111], a[196:199], v[64:67], 0\n"
"v_mfma_f32_16x16x32_bf16 v[108:111], a[200:203], v[68:71], v[108:111]\n"
"s_waitcnt lgkmcnt(0)\n"
"v_mfma_f32_16x16x32_bf16 v[108:111], a[204:207], v[72:75], v[108:111]\n"
"s_barrier\n"
"v_mfma_f32_16x16x32_bf16 v[108:111], a[208:211], v[76:79], v[108:111]\n"
"global_load_dwordx4 v[68:71], v6, s[56:57] offset:0 sc1\n"
"ds_read_b128 v[64:67], v2 offset:2048\n"
"v_mfma_f32_16x16x32_bf16 v[108:111], a[212:215], v[80:83], v[108:111]\n"
"ds_read_b128 v[112:115], v3 offset:512\n"
"ds_read_b128 v[116:119], v4 offset:3072\n"
"v_mfma_f32_16x16x32_bf16 v[108:111], a[216:219], v[84:87], v[108:111]\n"
"ds_read_b128 v[120:123], v2 offset:2304\n"
"ds_read_b128 v[124:127], v3 offset:768\n"
"v_mfma_f32_16x16x32_bf16 v[108:111], a[220:223], v[88:91], v[108:111]\n"
"ds_read_b128 v[128:131], v4 offset:3328\n"
"v_mfma_f32_16x16x32_bf16 v[84:87], v[92:95], a[0:3], 0\n"
"global_load_dwordx4 v[72:75], v7, s[56:57] offset:0 sc1\n"
"v_mfma_f32_16x16x32_bf16 v[84:87], v[44:47], a[4:7], v[84:87]\n"
"s_waitcnt vmcnt(8)\n"
"v_mfma_f32_16x16x32_bf16 v[84:87], v[48:51], a[8:11], v[84:87]\n"
"v_mfma_f32_16x16x32_bf16 v[84:87], v[52:55], a[12:15], v[84:87]\n"
"s_waitcnt vmcnt(6)\n"
"v_mfma_f32_16x16x32_bf16 v[84:87], v[56:59], a[16:19], v[84:87]\n"
"s_waitcnt lgkmcnt(3)\n"
"v_add_f32 v64, v64, v112\n"
"v_add_f32 v65, v65, v113\n"
"v_mfma_f32_16x16x32_bf16 v[84:87], v[60:63], a[20:23], v[84:87]\n"
"s_waitcnt vmcnt(5)\n"
"global_load_dwordx4 v[76:79], v8, s[56:57] offset:0 sc1\n"
"v_add_f32 v66, v66, v114\n"
"v_mfma_f32_16x16x32_bf16 v[84:87], v[96:99], a[24:27], v[84:87]\n"
"buffer_load_dwordx2 v[132:133], v12, s[40:43], s50 offen sc1\n"
"v_mfma_f32_16x16x32_bf16 v[88:91], v[92:95], a[28:31], 0\n"
"v_add_f32 v67, v67, v115\n"
"v_add_f32 v64, v64, v116\n"
"v_mfma_f32_16x16x32_bf16 v[88:91], v[44:47], a[32:35], v[88:91]\n"
"s_waitcnt vmcnt(5)\n"
"buffer_store_dwordx2 v[0:1], v14, s[40:43], s95 offen offset:0 sc1\n"
"ds_write_addtid_b32 v84 offset:2048\n"
"v_mfma_f32_16x16x32_bf16 v[88:91], v[48:51], a[36:39], v[88:91]\n"
"ds_write_addtid_b32 v85 offset:6688\n"
"v_add_f32 v65, v65, v117\n"
"v_mfma_f32_16x16x32_bf16 v[88:91], v[52:55], a[40:43], v[88:91]\n"
"ds_write_addtid_b32 v86 offset:11392\n"
"global_load_dwordx4 v[80:83], v9, s[56:57] offset:0 sc1\n"
"v_mfma_f32_16x16x32_bf16 v[88:91], v[56:59], a[44:47], v[88:91]\n"
"ds_write_addtid_b32 v87 offset:16032\n"
"v_add_f32 v66, v66, v118\n"
"v_mfma_f32_16x16x32_bf16 v[88:91], v[60:63], a[48:51], v[88:91]\n"
"v_add_f32 v67, v67, v119\n"
"v_add_f32 v64, v64, v104\n"
"v_mfma_f32_16x16x32_bf16 v[88:91], v[96:99], a[52:55], v[88:91]\n"
"v_add_f32 v65, v65, v105\n"
"v_add_f32 v66, v66, v106\n"
"v_mfma_f32_16x16x32_bf16 v[112:115], v[92:95], a[56:59], 0\n"
"v_add_f32 v67, v67, v107\n"
"v_mfma_f32_16x16x32_bf16 v[112:115], v[44:47], a[60:63], v[112:115]\n"
"ds_write_addtid_b32 v88 offset:2304\n"
"global_load_dwordx4 v[84:87], v10, s[56:57] offset:0 sc1\n"
"v_mfma_f32_16x16x32_bf16 v[112:115], v[48:51], a[64:67], v[112:115]\n"
"s_add_u32 s93, s50, 0xffd00000\n"
"s_and_b32 s95, s93, 0xfbffffff\n"
"ds_write_addtid_b32 v89 offset:6944\n"
"v_mfma_f32_16x16x32_bf16 v[112:115], v[52:55], a[68:71], v[112:115]\n"
"ds_write_addtid_b32 v90 offset:11648\n"
"v_cvt_pk_bf16_f32 v104, v64, v65\n"
"v_mfma_f32_16x16x32_bf16 v[112:115], v[56:59], a[72:75], v[112:115]\n"
"ds_write_addtid_b32 v91 offset:16288\n"
"v_cvt_pk_bf16_f32 v105, v66, v67\n"
"v_mfma_f32_16x16x32_bf16 v[112:115], v[60:63], a[76:79], v[112:115]\n"
"s_waitcnt lgkmcnt(8)\n"
"v_add_f32 v120, v120, v124\n"
"v_add_f32 v121, v121, v125\n"
"v_mfma_f32_16x16x32_bf16 v[112:115], v[96:99], a[80:83], v[112:115]\n"
"global_load_dwordx4 v[88:91], v11, s[56:57] offset:0 sc1\n"
"v_add_f32 v122, v122, v126\n"
"v_mfma_f32_16x16x32_bf16 v[64:67], v[92:95], a[84:87], 0\n"
"v_add_f32 v123, v123, v127\n"
"v_add_f32 v120, v120, v128\n"
"v_mfma_f32_16x16x32_bf16 v[64:67], v[44:47], a[88:91], v[64:67]\n"
"ds_write_addtid_b32 v112 offset:2560\n"
"v_add_f32 v121, v121, v129\n"
"v_mfma_f32_16x16x32_bf16 v[64:67], v[48:51], a[92:95], v[64:67]\n"
"ds_write_addtid_b32 v113 offset:7200\n"
"v_add_f32 v122, v122, v130\n"
"v_mfma_f32_16x16x32_bf16 v[64:67], v[52:55], a[96:99], v[64:67]\n"
"ds_write_addtid_b32 v114 offset:11904\n"
"v_add_f32 v123, v123, v131\n"
"v_mfma_f32_16x16x32_bf16 v[64:67], v[56:59], a[100:103], v[64:67]\n"
"ds_write_addtid_b32 v115 offset:16544\n"
"v_add_f32 v120, v120, v108\n"
"v_mfma_f32_16x16x32_bf16 v[64:67], v[60:63], a[104:107], v[64:67]\n"
"v_add_f32 v121, v121, v109\n"
"v_add_f32 v122, v122, v110\n"
"v_mfma_f32_16x16x32_bf16 v[64:67], v[96:99], a[108:111], v[64:67]\n"
"v_add_f32 v123, v123, v111\n"
"v_cvt_pk_bf16_f32 v106, v120, v121\n"
"v_mfma_f32_16x16x32_bf16 v[112:115], v[92:95], a[112:115], 0\n"
"v_cvt_pk_bf16_f32 v107, v122, v123\n"
"v_mfma_f32_16x16x32_bf16 v[112:115], v[44:47], a[116:119], v[112:115]\n"
"ds_write_addtid_b32 v64 offset:2816\n"
"v_mfma_f32_16x16x32_bf16 v[112:115], v[48:51], a[120:123], v[112:115]\n"
"ds_write_addtid_b32 v65 offset:7456\n"
"v_mfma_f32_16x16x32_bf16 v[112:115], v[52:55], a[124:127], v[112:115]\n"
"ds_write_addtid_b32 v66 offset:12160\n"
"v_mfma_f32_16x16x32_bf16 v[112:115], v[56:59], a[128:131], v[112:115]\n"
"ds_write_addtid_b32 v67 offset:16800\n"
"v_mfma_f32_16x16x32_bf16 v[112:115], v[60:63], a[132:135], v[112:115]\n"
"v_mfma_f32_16x16x32_bf16 v[112:115], v[96:99], a[136:139], v[112:115]\n"
"v_mfma_f32_16x16x32_bf16 v[108:111], v[92:95], a[140:143], 0\n"
"v_mfma_f32_16x16x32_bf16 v[108:111], v[44:47], a[144:147], v[108:111]\n"
"buffer_store_dwordx4 v[104:107], v13, s[40:43], s95 offen sc1\n"
"ds_write_addtid_b32 v112 offset:3072\n"
"v_mfma_f32_16x16x32_bf16 v[108:111], v[48:51], a[148:151], v[108:111]\n"
"ds_write_addtid_b32 v113 offset:7712\n"
"v_mfma_f32_16x16x32_bf16 v[108:111], v[52:55], a[152:155], v[108:111]\n"
"ds_write_addtid_b32 v114 offset:12416\n"
"v_mfma_f32_16x16x32_bf16 v[108:111], v[56:59], a[156:159], v[108:111]\n"
"ds_write_addtid_b32 v115 offset:17056\n"
"v_mfma_f32_16x16x32_bf16 v[108:111], v[60:63], a[160:163], v[108:111]\n"
"v_mfma_f32_16x16x32_bf16 v[108:111], v[96:99], a[164:167], v[108:111]\n"
"v_mfma_f32_16x16x32_bf16 v[64:67], v[92:95], a[224:227], 0\n"
"v_mfma_f32_16x16x32_bf16 v[64:67], v[44:47], a[228:231], v[64:67]\n"
"ds_write_addtid_b32 v108 offset:3328\n"
"v_mfma_f32_16x16x32_bf16 v[64:67], v[48:51], a[232:235], v[64:67]\n"
"ds_write_addtid_b32 v109 offset:7968\n"
"v_mfma_f32_16x16x32_bf16 v[64:67], v[52:55], a[236:239], v[64:67]\n"
"ds_write_addtid_b32 v110 offset:12672\n"
"v_mfma_f32_16x16x32_bf16 v[64:67], v[56:59], a[240:243], v[64:67]\n"
"ds_write_addtid_b32 v111 offset:17312\n"
"v_mfma_f32_16x16x32_bf16 v[64:67], v[60:63], a[244:247], v[64:67]\n"
"v_mfma_f32_16x16x32_bf16 v[64:67], v[96:99], a[248:251], v[64:67]\n"
"v_mfma_f32_16x16x32_bf16 v[112:115], v[92:95], a[252:255], 0\n"
"v_mfma_f32_16x16x32_bf16 v[112:115], v[44:47], v[20:23], v[112:115]\n"
"s_add_u32 s58, s50, 0x180000\n"
"ds_write_addtid_b32 v64 offset:3584\n"
"v_mfma_f32_16x16x32_bf16 v[112:115], v[48:51], v[24:27], v[112:115]\n"
"s_and_b32 s58, s58, 0x3ffffff\n"
"ds_write_addtid_b32 v65 offset:8224\n"
"v_mfma_f32_16x16x32_bf16 v[112:115], v[52:55], v[28:31], v[112:115]\n"
"s_add_u32 s56, s40, s58\n"
"ds_write_addtid_b32 v66 offset:12928\n"
"v_mfma_f32_16x16x32_bf16 v[112:115], v[56:59], v[32:35], v[112:115]\n"
"s_addc_u32 s57, s41, 0\n"
"ds_write_addtid_b32 v67 offset:17568\n"
"v_mfma_f32_16x16x32_bf16 v[112:115], v[60:63], v[36:39], v[112:115]\n"
"s_waitcnt vmcnt(5)\n"
"v_cmp_ne_u64 vcc, s[48:49], v[132:133]\n"
"v_mfma_f32_16x16x32_bf16 v[112:115], v[96:99], v[40:43], v[112:115]\n"
"v_mfma_f32_16x16x32_bf16 v[116:119], a[168:171], v[92:95], 0\n"
"s_cbranch_vccnz .LG_2s\n"
".LG_2r:\n"
"v_mfma_f32_16x16x32_bf16 v[116:119], a[172:175], v[44:47], v[116:119]\n"
"s_add_u32 s50, s50, 0x180000\n"
"ds_write_addtid_b32 v112 offset:3840\n"
"v_mfma_f32_16x16x32_bf16 v[116:119], a[176:179], v[48:51], v[116:119]\n"
"ds_write_addtid_b32 v113 offset:8480\n"
"v_mfma_f32_16x16x32_bf16 v[116:119], a[180:183], v[52:55], v[116:119]\n"
"ds_write_addtid_b32 v114 offset:13184\n"
"v_mfma_f32_16x16x32_bf16 v[116:119], a[184:187], v[56:59], v[116:119]\n"
"ds_write_addtid_b32 v115 offset:17824\n"
"v_mfma_f32_16x16x32_bf16 v[116:119], a[188:191], v[60:63], v[116:119]\n"
"global_load_dwordx4 v[104:107], v5, s[56:57] offset:0 sc1\n"
"v_mfma_f32_16x16x32_bf16 v[116:119], a[192:195], v[96:99], v[116:119]\n"
"v_mfma_f32_16x16x32_bf16 v[112:115], a[196:199], v[92:95], 0\n"
"v_mfma_f32_16x16x32_bf16 v[112:115], a[200:203], v[44:47], v[112:115]\n"
"s_waitcnt lgkmcnt(0)\n"
"global_load_dwordx4 v[108:111], v6, s[56:57] offset:0 sc1\n"
"v_mfma_f32_16x16x32_bf16 v[112:115], a[204:207], v[48:51], v[112:115]\n"
"s_barrier\n"
"v_mfma_f32_16x16x32_bf16 v[112:115], a[208:211], v[52:55], v[112:115]\n"
"ds_read_b128 v[120:123], v2 offset:0\n"
"ds_read_b128 v[64:67], v3 offset:2560\n"
"v_mfma_f32_16x16x32_bf16 v[112:115], a[212:215], v[56:59], v[112:115]\n"
"ds_read_b128 v[92:95], v4 offset:1024\n"
"ds_read_b128 v[124:127], v2 offset:256\n"
"v_mfma_f32_16x16x32_bf16 v[112:115], a[216:219], v[60:63], v[112:115]\n"
"ds_read_b128 v[128:131], v3 offset:2816\n"
"ds_read_b128 v[132:135], v4 offset:1280\n"
"v_mfma_f32_16x16x32_bf16 v[112:115], a[220:223], v[96:99], v[112:115]\n"
"s_branch .LG_top\n"
".LG_1s:\n"
"s_cmp_ge_u32 s50, s53\n"
"s_cbranch_scc1 .LG_end\n"
"s_cmp_ge_u32 s50, s52\n"
"s_cbranch_scc1 .LG_1r\n"
"s_sub_u32 s60, s50, 0x0\n"
"s_mov_b32 s61, 200000\n"
"s_nop 7\n"
".LG_1p:\n"
"buffer_load_dwordx2 v[128:129], v12, s[40:43], s60 offen sc1\n"
"s_waitcnt vmcnt(0)\n"
"v_cmp_ne_u64 vcc, s[48:49], v[128:129]\n"
"s_cbranch_vccz .LG_1px\n"
"s_sleep 1\n"
"s_sub_u32 s61, s61, 1\n"
"s_cbranch_scc0 .LG_1p\n"
"s_branch .LG_end\n"
".LG_1px:\n"
"s_branch .LG_1r\n"
".LG_2s:\n"
"s_cmp_ge_u32 s50, s53\n"
"s_cbranch_scc1 .LG_end\n"
"s_cmp_ge_u32 s50, s52\n"
"s_cbranch_scc1 .LG_2r\n"
"s_sub_u32 s60, s50, 0x0\n"
"s_mov_b32 s61, 200000\n"
"s_nop 7\n"
".LG_2p:\n"
"buffer_load_dwordx2 v[132:133], v12, s[40:43], s60 offen sc1\n"
"s_waitcnt vmcnt(0)\n"
"v_cmp_ne_u64 vcc, s[48:49], v[132:133]\n"
"s_cbranch_vccz .LG_2px\n"
"s_sleep 1\n"
"s_sub_u32 s61, s61, 1\n"
"s_cbranch_scc0 .LG_2p\n"
"s_branch .LG_end\n"
".LG_2px:\n"
"s_branch .LG_2r\n"
".LG_3s:\n"
"s_cmp_ge_u32 s50, s53\n"
"s_cbranch_scc1 .LG_end\n"
"s_cmp_ge_u32 s50, s52\n"
"s_cbranch_scc1 .LG_3r\n"
"s_sub_u32 s60, s50, 0x0\n"
"s_mov_b32 s61, 200000\n"
"s_nop 7\n"
".LG_3p:\n"
"buffer_load_dwordx2 v[136:137], v12, s[40:43], s60 offen sc1\n"
"s_waitcnt vmcnt(0)\n"
"v_cmp_ne_u64 vcc, s[48:49], v[136:137]\n"
"s_cbranch_vccz .LG_3px\n"
"s_sleep 1\n"
"s_sub_u32 s61, s61, 1\n"
"s_cbranch_scc0 .LG_3p\n"
"s_branch .LG_end\n"
".LG_3px:\n"
"s_branch .LG_3r\n"
".LG_4s:\n"
"s_cmp_ge_u32 s50, s53\n"
"s_cbranch_scc1 .LG_end\n"
"s_cmp_ge_u32 s50, s52\n"
"s_cbranch_scc1 .LG_4r\n"
"s_sub_u32 s60, s50, 0x0\n"
"s_mov_b32 s61, 200000\n"
"s_nop 7\n"
".LG_4p:\n"
"buffer_load_dwordx2 v[168:169], v12, s[40:43], s60 offen sc1\n"
"s_waitcnt vmcnt(0)\n"
"v_cmp_ne_u64 vcc, s[48:49], v[168:169]\n"
"s_cbranch_vccz .LG_4px\n"
"s_sleep 1\n"
"s_sub_u32 s61, s61, 1\n"
"s_cbranch_scc0 .LG_4p\n"
"s_branch .LG_end\n"
".LG_4px:\n"
"s_branch .LG_4r\n"
".LG_end:\n"
"s_waitcnt vmcnt(0) lgkmcnt(0)\n"
"s_endpgm\n"
      :: "{v2}"((unsigned)(lds0 + ((wid + 3) & 3) * 18592 + trp)), "{v3}"((unsigned)(lds0 + ((wid + 2) & 3) * 18592 + trp)), "{v4}"((unsigned)(lds0 + ((wid + 1) & 3) * 18592 + trp)), "{v5}"((unsigned)(lane * 16 + f0 * 1024 + ((0 + rot) % 7) * 1024 + 134316032)), "{v6}"((unsigned)(lane * 16 + f0 * 1024 + ((1 + rot) % 7) * 1024 + 134316032)), "{v7}"((unsigned)(lane * 16 + f0 * 1024 + ((2 + rot) % 7) * 1024 + 134316032)), "{v8}"((unsigned)(lane * 16 + f0 * 1024 + ((3 + rot) % 7) * 1024 + 134316032)), "{v9}"((unsigned)(lane * 16 + f0 * 1024 + ((4 + rot) % 7) * 1024 + 134316032)), "{v10}"((unsigned)(lane * 16 + f0 * 1024 + ((5 + rot) % 7) * 1024 + 134316032)), "{v11}"((unsigned)(lane * 16 + f0 * 1024 + ((6 + rot) % 7) * 1024 + 134316032)), "{v12}"((unsigned)((f0 + lane % 7) * 8 + kq * 32 + 353024 + G_GL * 1572864)), "{v13}"((unsigned)(lane * 16 + pair * 4096 + kq * 1024 + 212992)), "{v14}"((unsigned)(lane ? DROP : pair * 64 + kq * 8 + 354304)), "{v15}"((unsigned)(lane * 16 + (pg0 + 4) * 4096 + kq * 1024 + 212992)), "{v16}"((unsigned)(lane > 1 ? DROP : (lane ? pg0 + 4 : pair) * 64 + kq * 8 + 354304)), "{v44}"((unsigned)(vT + tsw)), "{v45}"((unsigned)(vT + (tsw ^ 8))), "{v46}"((unsigned)(vD + ((0 + rot) % 7) * 65536)), "{v47}"((unsigned)(vD + ((1 + rot) % 7) * 65536)), "{v48}"((unsigned)(vD + ((2 + rot) % 7) * 65536)), "{v49}"((unsigned)(vD + ((3 + rot) % 7) * 65536)), "{v50}"((unsigned)(vD + ((4 + rot) % 7) * 65536)), "{v51}"((unsigned)(vD + ((5 + rot) % 7) * 65536)), "{v52}"((unsigned)(vD + ((6 + rot) % 7) * 65536)), "{s[40:43]}"(rS), "{s[44:47]}"(rW), "{s[48:49]}"(wsp), "{s50}"((unsigned)(sA)), "{s51}"((unsigned)(sLo)), "{s52}"((unsigned)(sHi)), "{s53}"((unsigned)(sEx)), "{s54}"((unsigned)(sEnt)), "{s55}"((unsigned)(sLd)), "{s[56:57]}"(wsb + (sLo & 0x3ffffff)), "{s59}"((unsigned)(lds0 + wid * 18592)), "{s62}"((unsigned)(sW0 + 64 * ((wid + 1) & 3))), "{s63}"((unsigned)(sW0 + 64 * ((wid + 2) & 3))), "{s64}"((unsigned)(sW0 + 64 * ((wid + 3) & 3))), "{s65}"((unsigned)(sW0 + 64 * ((wid + 4) & 3))), "{s66}"((unsigned)(sW0 + 256))
      : "v255", "a255", "s95", "vcc", "scc", "memory");

    } else {
      const int v16 = kq * 4 + wid;
      const unsigned sEx = sHi + (J_EX + (set == 1)) * 3 * RS, sEnt = grp - 4;
      const u32x4 rO = desc(a.out, 2048 * 2048), bia0 = *(const u32x4*)(a.b2 + v16 * (32 << JJ) + lg * 8), bia1 = *(const u32x4*)(a.b2 + (JJ ? 32 : 512) + v16 * (32 << JJ) + lg * 8);
      const unsigned se = lg == (li >> 2) ? 0x3f80u << (16 * (li & 1)) : 0u, sel0 = (li & 2) ? 0u : se, sel1 = (li & 2) ? se : 0u;
      const u64 ep = (set == 2 && idx == 27 && wid == 0) ? (u64)a.ws : 0ull;
    asm volatile(
"s_nop 4\n"
"v_mov_b32 v186, 0\n"
"global_load_dwordx2 v[0:1], v186, s[48:49] sc1\n"
"v_mov_b32 v188, 0x3f803f80\n"
"v_mov_b32 v189, 0x3f803f80\n"
"v_mov_b32 v190, 0x3f803f80\n"
"v_mov_b32 v191, 0x3f803f80\n"
"s_add_u32 m0, s55, 0\n"
"s_add_u32 s60, s86, 0\n"
"buffer_load_dwordx4 v46, s[44:47], s60 offen lds\n"
"s_add_u32 m0, s55, 1024\n"
"s_add_u32 s60, s86, 32768\n"
"buffer_load_dwordx4 v46, s[44:47], s60 offen lds\n"
"s_add_u32 m0, s55, 2048\n"
"s_add_u32 s60, s87, 0\n"
"buffer_load_dwordx4 v46, s[44:47], s60 offen lds\n"
"s_add_u32 m0, s55, 3072\n"
"s_add_u32 s60, s87, 32768\n"
"buffer_load_dwordx4 v46, s[44:47], s60 offen lds\n"
"s_add_u32 m0, s55, 4096\n"
"s_add_u32 s60, s88, 0\n"
"buffer_load_dwordx4 v46, s[44:47], s60 offen lds\n"
"s_add_u32 m0, s55, 5120\n"
"s_add_u32 s60, s88, 32768\n"
"buffer_load_dwordx4 v46, s[44:47], s60 offen lds\n"
"s_add_u32 m0, s55, 6144\n"
"s_add_u32 s60, s89, 0\n"
"buffer_load_dwordx4 v46, s[44:47], s60 offen lds\n"
"s_add_u32 m0, s55, 7168\n"
"s_add_u32 s60, s89, 32768\n"
"buffer_load_dwordx4 v46, s[44:47], s60 offen lds\n"
"s_add_u32 m0, s55, 8192\n"
"s_add_u32 s60, s86, 0\n"
"buffer_load_dwordx4 v47, s[44:47], s60 offen lds\n"
"s_add_u32 m0, s55, 9216\n"
"s_add_u32 s60, s86, 32768\n"
"buffer_load_dwordx4 v47, s[44:47], s60 offen lds\n"
"s_add_u32 m0, s55, 10240\n"
"s_add_u32 s60, s87, 0\n"
"buffer_load_dwordx4 v47, s[44:47], s60 offen lds\n"
"s_add_u32 m0, s55, 11264\n"
"s_add_u32 s60, s87, 32768\n"
"buffer_load_dwordx4 v47, s[44:47], s60 offen lds\n"
"s_add_u32 m0, s55, 12288\n"
"s_add_u32 s60, s88, 0\n"
"buffer_load_dwordx4 v47, s[44:47], s60 offen lds\n"
"s_add_u32 m0, s55, 13312\n"
"s_add_u32 s60, s88, 32768\n"
"buffer_load_dwordx4 v47, s[44:47], s60 offen lds\n"
"s_add_u32 m0, s55, 14336\n"
"s_add_u32 s60, s89, 0\n"
"buffer_load_dwordx4 v47, s[44:47], s60 offen lds\n"
"s_add_u32 m0, s55, 15360\n"
"s_add_u32 s60, s89, 32768\n"
"buffer_load_dwordx4 v47, s[44:47], s60 offen lds\n"
"s_add_u32 m0, s55, 16384\n"
"s_add_u32 s60, s86, 0\n"
"buffer_load_dwordx4 v48, s[44:47], s60 offen lds\n"
"s_add_u32 m0, s55, 17408\n"
"s_add_u32 s60, s86, 32768\n"
"buffer_load_dwordx4 v48, s[44:47], s60 offen lds\n"
"s_add_u32 m0, s55, 18432\n"
"s_add_u32 s60, s87, 0\n"
"buffer_load_dwordx4 v48, s[44:47], s60 offen lds\n"
"s_add_u32 m0, s55, 19456\n"
"s_add_u32 s60, s87, 32768\n"
"buffer_load_dwordx4 v48, s[44:47], s60 offen lds\n"
"s_add_u32 m0, s55, 20480\n"
"s_add_u32 s60, s88, 0\n"
"buffer_load_dwordx4 v48, s[44:47], s60 offen lds\n"
"s_add_u32 m0, s55, 21504\n"
"s_add_u32 s60, s88, 32768\n"
"buffer_load_dwordx4 v48, s[44:47], s60 offen lds\n"
"s_add_u32 m0, s55, 22528\n"
"s_add_u32 s60, s89, 0\n"
"buffer_load_dwordx4 v48, s[44:47], s60 offen lds\n"
"s_add_u32 m0, s55, 23552\n"
"s_add_u32 s60, s89, 32768\n"
"buffer_load_dwordx4 v48, s[44:47], s60 offen lds\n"
"s_add_u32 m0, s55, 24576\n"
"s_add_u32 s60, s86, 0\n"
"buffer_load_dwordx4 v49, s[44:47], s60 offen lds\n"
"s_add_u32 m0, s55, 25600\n"
"s_add_u32 s60, s86, 32768\n"
"buffer_load_dwordx4 v49, s[44:47], s60 offen lds\n"
"s_add_u32 m0, s55, 26624\n"
"s_add_u32 s60, s87, 0\n"
"buffer_load_dwordx4 v49, s[44:47], s60 offen lds\n"
"s_add_u32 m0, s55, 27648\n"
"s_add_u32 s60, s87, 32768\n"
"buffer_load_dwordx4 v49, s[44:47], s60 offen lds\n"
"s_add_u32 m0, s55, 28672\n"
"s_add_u32 s60, s88, 0\n"
"buffer_load_dwordx4 v49, s[44:47], s60 offen lds\n"
"s_add_u32 m0, s55, 29696\n"
"s_add_u32 s60, s88, 32768\n"
"buffer_load_dwordx4 v49, s[44:47], s60 offen lds\n"
"s_add_u32 m0, s55, 30720\n"
"s_add_u32 s60, s89, 0\n"
"buffer_load_dwordx4 v49, s[44:47], s60 offen lds\n"
"s_add_u32 m0, s55, 31744\n"
"s_add_u32 s60, s89, 32768\n"
"buffer_load_dwordx4 v49, s[44:47], s60 offen lds\n"
"s_add_u32 m0, s55, 32768\n"
"s_add_u32 s60, s86, 0\n"
"buffer_load_dwordx4 v50, s[44:47], s60 offen lds\n"
"s_add_u32 m0, s55, 33792\n"
"s_add_u32 s60, s86, 32768\n"
"buffer_load_dwordx4 v50, s[44:47], s60 offen lds\n"
"s_add_u32 m0, s55, 34816\n"
"s_add_u32 s60, s87, 0\n"
"buffer_load_dwordx4 v50, s[44:47], s60 offen lds\n"
"s_add_u32 m0, s55, 35840\n"
"s_add_u32 s60, s87, 32768\n"
"buffer_load_dwordx4 v50, s[44:47], s60 offen lds\n"
"s_add_u32 m0, s55, 36864\n"
"s_add_u32 s60, s88, 0\n"
"buffer_load_dwordx4 v50, s[44:47], s60 offen lds\n"
"s_add_u32 m0, s55, 37888\n"
"s_add_u32 s60, s88, 32768\n"
"buffer_load_dwordx4 v50, s[44:47], s60 offen lds\n"
"s_add_u32 m0, s55, 38912\n"
"s_add_u32 s60, s89, 0\n"
"buffer_load_dwordx4 v50, s[44:47], s60 offen lds\n"
"s_add_u32 m0, s55, 39936\n"
"s_add_u32 s60, s89, 32768\n"
"buffer_load_dwordx4 v50, s[44:47], s60 offen lds\n"
"s_waitcnt vmcnt(38)\n"
"ds_read_b64_tr_b16 a[0:1], v44 offset:0\n"
"ds_read_b64_tr_b16 a[2:3], v44 offset:256\n"
"ds_read_b64_tr_b16 a[28:29], v45 offset:0\n"
"ds_read_b64_tr_b16 a[30:31], v45 offset:256\n"
"v_add_u32 v0, 2, v0\n"
"v_add_u32 v1, 0x9e3779b9, v1\n"
"v_or_b32 v0, 1, v0\n"
"v_readfirstlane_b32 s49, v1\n"
"v_readfirstlane_b32 s48, v0\n"
"s_waitcnt vmcnt(36)\n"
"ds_read_b64_tr_b16 a[56:57], v44 offset:2048\n"
"ds_read_b64_tr_b16 a[58:59], v44 offset:2304\n"
"ds_read_b64_tr_b16 a[84:85], v45 offset:2048\n"
"ds_read_b64_tr_b16 a[86:87], v45 offset:2304\n"
"s_waitcnt lgkmcnt(4)\n"
"s_add_u32 m0, s55, 0\n"
"s_add_u32 s60, s86, 0\n"
"buffer_load_dwordx4 v51, s[44:47], s60 offen lds\n"
"s_add_u32 m0, s55, 1024\n"
"s_add_u32 s60, s86, 32768\n"
"buffer_load_dwordx4 v51, s[44:47], s60 offen lds\n"
"s_waitcnt vmcnt(36)\n"
"ds_read_b64_tr_b16 a[112:113], v44 offset:4096\n"
"ds_read_b64_tr_b16 a[114:115], v44 offset:4352\n"
"ds_read_b64_tr_b16 a[140:141], v45 offset:4096\n"
"ds_read_b64_tr_b16 a[142:143], v45 offset:4352\n"
"s_waitcnt lgkmcnt(4)\n"
"s_add_u32 m0, s55, 2048\n"
"s_add_u32 s60, s87, 0\n"
"buffer_load_dwordx4 v51, s[44:47], s60 offen lds\n"
"s_add_u32 m0, s55, 3072\n"
"s_add_u32 s60, s87, 32768\n"
"buffer_load_dwordx4 v51, s[44:47], s60 offen lds\n"
"s_waitcnt vmcnt(36)\n"
"ds_read_b64_tr_b16 a[168:169], v44 offset:6144\n"
"ds_read_b64_tr_b16 a[170:171], v44 offset:6400\n"
"ds_read_b64_tr_b16 a[196:197], v45 offset:6144\n"
"ds_read_b64_tr_b16 a[198:199], v45 offset:6400\n"
"s_waitcnt lgkmcnt(4)\n"
"s_add_u32 m0, s55, 4096\n"
"s_add_u32 s60, s88, 0\n"
"buffer_load_dwordx4 v51, s[44:47], s60 offen lds\n"
"s_add_u32 m0, s55, 5120\n"
"s_add_u32 s60, s88, 32768\n"
"buffer_load_dwordx4 v51, s[44:47], s60 offen lds\n"
"s_waitcnt vmcnt(36)\n"
"ds_read_b64_tr_b16 a[4:5], v44 offset:8192\n"
"ds_read_b64_tr_b16 a[6:7], v44 offset:8448\n"
"ds_read_b64_tr_b16 a[32:33], v45 offset:8192\n"
"ds_read_b64_tr_b16 a[34:35], v45 offset:8448\n"
"s_waitcnt lgkmcnt(4)\n"
"s_add_u32 m0, s55, 6144\n"
"s_add_u32 s60, s89, 0\n"
"buffer_load_dwordx4 v51, s[44:47], s60 offen lds\n"
"s_add_u32 m0, s55, 7168\n"
"s_add_u32 s60, s89, 32768\n"
"buffer_load_dwordx4 v51, s[44:47], s60 offen lds\n"
"s_waitcnt vmcnt(36)\n"
"ds_read_b64_tr_b16 a[60:61], v44 offset:10240\n"
"ds_read_b64_tr_b16 a[62:63], v44 offset:10496\n"
"ds_read_b64_tr_b16 a[88:89], v45 offset:10240\n"
"ds_read_b64_tr_b16 a[90:91], v45 offset:10496\n"
"s_waitcnt lgkmcnt(4)\n"
"s_add_u32 m0, s55, 8192\n"
"s_add_u32 s60, s86, 0\n"
"buffer_load_dwordx4 v52, s[44:47], s60 offen lds\n"
"s_add_u32 m0, s55, 9216\n"
"s_add_u32 s60, s86, 32768\n"
"buffer_load_dwordx4 v52, s[44:47], s60 offen lds\n"
"s_waitcnt vmcnt(36)\n"
"ds_read_b64_tr_b16 a[116:117], v44 offset:12288\n"
"ds_read_b64_tr_b16 a[118:119], v44 offset:12544\n"
"ds_read_b64_tr_b16 a[144:145], v45 offset:12288\n"
"ds_read_b64_tr_b16 a[146:147], v45 offset:12544\n"
"s_waitcnt lgkmcnt(4)\n"
"s_add_u32 m0, s55, 10240\n"
"s_add_u32 s60, s87, 0\n"
"buffer_load_dwordx4 v52, s[44:47], s60 offen lds\n"
"s_add_u32 m0, s55, 11264\n"
"s_add_u32 s60, s87, 32768\n"
"buffer_load_dwordx4 v52, s[44:47], s60 offen lds\n"
"s_waitcnt vmcnt(36)\n"
"ds_read_b64_tr_b16 a[172:173], v44 offset:14336\n"
"ds_read_b64_tr_b16 a[174:175], v44 offset:14592\n"
"ds_read_b64_tr_b16 a[200:201], v45 offset:14336\n"
"ds_read_b64_tr_b16 a[202:203], v45 offset:14592\n"
"s_waitcnt lgkmcnt(4)\n"
"s_add_u32 m0, s55, 12288\n"
"s_add_u32 s60, s88, 0\n"
"buffer_load_dwordx4 v52, s[44:47], s60 offen lds\n"
"s_add_u32 m0, s55, 13312\n"
"s_add_u32 s60, s88, 32768\n"
"buffer_load_dwordx4 v52, s[44:47], s60 offen lds\n"
"s_waitcnt vmcnt(36)\n"
"ds_read_b64_tr_b16 a[8:9], v44 offset:16384\n"
"ds_read_b64_tr_b16 a[10:11], v44 offset:16640\n"
"ds_read_b64_tr_b16 a[36:37], v45 offset:16384\n"
"ds_read_b64_tr_b16 a[38:39], v45 offset:16640\n"
"s_waitcnt lgkmcnt(4)\n"
"s_add_u32 m0, s55, 14336\n"
"s_add_u32 s60, s89, 0\n"
"buffer_load_dwordx4 v52, s[44:47], s60 offen lds\n"
"s_add_u32 m0, s55, 15360\n"
"s_add_u32 s60, s89, 32768\n"
"buffer_load_dwordx4 v52, s[44:47], s60 offen lds\n"
"s_waitcnt vmcnt(36)\n"
"ds_read_b64_tr_b16 a[64:65], v44 offset:18432\n"
"ds_read_b64_tr_b16 a[66:67], v44 offset:18688\n"
"ds_read_b64_tr_b16 a[92:93], v45 offset:18432\n"
"ds_read_b64_tr_b16 a[94:95], v45 offset:18688\n"
"s_waitcnt lgkmcnt(4)\n"
"s_waitcnt vmcnt(34)\n"
"ds_read_b64_tr_b16 a[120:121], v44 offset:20480\n"
"ds_read_b64_tr_b16 a[122:123], v44 offset:20736\n"
"ds_read_b64_tr_b16 a[148:149], v45 offset:20480\n"
"ds_read_b64_tr_b16 a[150:151], v45 offset:20736\n"
"s_waitcnt lgkmcnt(4)\n"
"s_waitcnt vmcnt(32)\n"
"ds_read_b64_tr_b16 a[176:177], v44 offset:22528\n"
"ds_read_b64_tr_b16 a[178:179], v44 offset:22784\n"
"ds_read_b64_tr_b16 a[204:205], v45 offset:22528\n"
"ds_read_b64_tr_b16 a[206:207], v45 offset:22784\n"
"s_waitcnt lgkmcnt(4)\n"
"s_waitcnt vmcnt(30)\n"
"ds_read_b64_tr_b16 a[12:13], v44 offset:24576\n"
"ds_read_b64_tr_b16 a[14:15], v44 offset:24832\n"
"ds_read_b64_tr_b16 a[40:41], v45 offset:24576\n"
"ds_read_b64_tr_b16 a[42:43], v45 offset:24832\n"
"s_waitcnt lgkmcnt(4)\n"
"s_waitcnt vmcnt(28)\n"
"ds_read_b64_tr_b16 a[68:69], v44 offset:26624\n"
"ds_read_b64_tr_b16 a[70:71], v44 offset:26880\n"
"ds_read_b64_tr_b16 a[96:97], v45 offset:26624\n"
"ds_read_b64_tr_b16 a[98:99], v45 offset:26880\n"
"s_waitcnt lgkmcnt(4)\n"
"s_waitcnt vmcnt(26)\n"
"ds_read_b64_tr_b16 a[124:125], v44 offset:28672\n"
"ds_read_b64_tr_b16 a[126:127], v44 offset:28928\n"
"ds_read_b64_tr_b16 a[152:153], v45 offset:28672\n"
"ds_read_b64_tr_b16 a[154:155], v45 offset:28928\n"
"s_waitcnt lgkmcnt(4)\n"
"s_waitcnt vmcnt(24)\n"
"ds_read_b64_tr_b16 a[180:181], v44 offset:30720\n"
"ds_read_b64_tr_b16 a[182:183], v44 offset:30976\n"
"ds_read_b64_tr_b16 a[208:209], v45 offset:30720\n"
"ds_read_b64_tr_b16 a[210:211], v45 offset:30976\n"
"s_waitcnt lgkmcnt(4)\n"
"s_waitcnt vmcnt(22)\n"
"ds_read_b64_tr_b16 a[16:17], v44 offset:32768\n"
"ds_read_b64_tr_b16 a[18:19], v44 offset:33024\n"
"ds_read_b64_tr_b16 a[44:45], v45 offset:32768\n"
"ds_read_b64_tr_b16 a[46:47], v45 offset:33024\n"
"s_waitcnt lgkmcnt(4)\n"
"s_waitcnt vmcnt(20)\n"
"ds_read_b64_tr_b16 a[72:73], v44 offset:34816\n"
"ds_read_b64_tr_b16 a[74:75], v44 offset:35072\n"
"ds_read_b64_tr_b16 a[100:101], v45 offset:34816\n"
"ds_read_b64_tr_b16 a[102:103], v45 offset:35072\n"
"s_waitcnt lgkmcnt(4)\n"
"s_waitcnt vmcnt(18)\n"
"ds_read_b64_tr_b16 a[128:129], v44 offset:36864\n"
"ds_read_b64_tr_b16 a[130:131], v44 offset:37120\n"
"ds_read_b64_tr_b16 a[156:157], v45 offset:36864\n"
"ds_read_b64_tr_b16 a[158:159], v45 offset:37120\n"
"s_waitcnt lgkmcnt(4)\n"
"s_waitcnt vmcnt(16)\n"
"ds_read_b64_tr_b16 a[184:185], v44 offset:38912\n"
"ds_read_b64_tr_b16 a[186:187], v44 offset:39168\n"
"ds_read_b64_tr_b16 a[212:213], v45 offset:38912\n"
"ds_read_b64_tr_b16 a[214:215], v45 offset:39168\n"
"s_waitcnt lgkmcnt(4)\n"
"s_waitcnt vmcnt(14)\n"
"ds_read_b64_tr_b16 a[20:21], v44 offset:0\n"
"ds_read_b64_tr_b16 a[22:23], v44 offset:256\n"
"ds_read_b64_tr_b16 a[48:49], v45 offset:0\n"
"ds_read_b64_tr_b16 a[50:51], v45 offset:256\n"
"s_waitcnt lgkmcnt(4)\n"
"s_waitcnt vmcnt(12)\n"
"ds_read_b64_tr_b16 a[76:77], v44 offset:2048\n"
"ds_read_b64_tr_b16 a[78:79], v44 offset:2304\n"
"ds_read_b64_tr_b16 a[104:105], v45 offset:2048\n"
"ds_read_b64_tr_b16 a[106:107], v45 offset:2304\n"
"s_waitcnt lgkmcnt(4)\n"
"s_waitcnt vmcnt(10)\n"
"ds_read_b64_tr_b16 a[132:133], v44 offset:4096\n"
"ds_read_b64_tr_b16 a[134:135], v44 offset:4352\n"
"ds_read_b64_tr_b16 a[160:161], v45 offset:4096\n"
"ds_read_b64_tr_b16 a[162:163], v45 offset:4352\n"
"s_waitcnt lgkmcnt(4)\n"
"s_waitcnt vmcnt(8)\n"
"ds_read_b64_tr_b16 a[188:189], v44 offset:6144\n"
"ds_read_b64_tr_b16 a[190:191], v44 offset:6400\n"
"ds_read_b64_tr_b16 a[216:217], v45 offset:6144\n"
"ds_read_b64_tr_b16 a[218:219], v45 offset:6400\n"
"s_waitcnt lgkmcnt(4)\n"
"s_waitcnt vmcnt(6)\n"
"ds_read_b64_tr_b16 a[24:25], v44 offset:8192\n"
"ds_read_b64_tr_b16 a[26:27], v44 offset:8448\n"
"ds_read_b64_tr_b16 a[52:53], v45 offset:8192\n"
"ds_read_b64_tr_b16 a[54:55], v45 offset:8448\n"
"s_waitcnt lgkmcnt(4)\n"
"s_waitcnt vmcnt(4)\n"
"ds_read_b64_tr_b16 a[80:81], v44 offset:10240\n"
"ds_read_b64_tr_b16 a[82:83], v44 offset:10496\n"
"ds_read_b64_tr_b16 a[108:109], v45 offset:10240\n"
"ds_read_b64_tr_b16 a[110:111], v45 offset:10496\n"
"s_waitcnt lgkmcnt(4)\n"
"s_waitcnt vmcnt(2)\n"
"ds_read_b64_tr_b16 a[136:137], v44 offset:12288\n"
"ds_read_b64_tr_b16 a[138:139], v44 offset:12544\n"
"ds_read_b64_tr_b16 a[164:165], v45 offset:12288\n"
"ds_read_b64_tr_b16 a[166:167], v45 offset:12544\n"
"s_waitcnt lgkmcnt(4)\n"
"s_waitcnt vmcnt(0)\n"
"ds_read_b64_tr_b16 a[192:193], v44 offset:14336\n"
"ds_read_b64_tr_b16 a[194:195], v44 offset:14592\n"
"ds_read_b64_tr_b16 a[220:221], v45 offset:14336\n"
"ds_read_b64_tr_b16 a[222:223], v45 offset:14592\n"
"s_waitcnt lgkmcnt(4)\n"
"s_waitcnt vmcnt(0)\n"
"s_waitcnt lgkmcnt(0)\n"
"s_mov_b32 m0, s59\n"
"s_barrier\n"
"s_mov_b64 s[84:85], s[48:49]\n"
"v_mov_b32 v144, 0\n"
"v_mov_b32 v145, 0\n"
"v_mov_b32 v160, 0\n"
"v_mov_b32 v161, 0\n"
"v_mov_b32 v164, 0\n"
"v_mov_b32 v165, 0\n"
"s_mov_b32 s29, 0x80000000\n"
"s_mov_b32 s30, 0x80000000\n"
"s_mov_b32 s28, 0x80000000\n"
"s_mov_b32 s31, 0x80000000\n"
"s_mov_b32 s31, 0x80000000\n"
"s_mov_b32 s30, 0x80000000\n"
"s_mov_b32 s90, 0x80000000\n"
"v_mov_b32 v92, 0\n"
"v_mov_b32 v93, 0\n"
"s_mov_b32 s29, 0x80000000\n"
"s_mov_b32 s31, 0x80000000\n"
"s_mov_b32 s30, 0x80000000\n"
"s_mov_b32 s91, 0x80000000\n"
"s_cmp_eq_u32 s54, 0\n"
"s_cbranch_scc1 .LJ_1s\n"
"s_cmp_eq_u32 s54, 1\n"
"s_cbranch_scc1 .LJ_2s\n"
"s_cmp_eq_u32 s54, 2\n"
"s_cbranch_scc1 .LJ_3s\n"
".p2align 6\n"
".LJ_top:\n"
"v_mfma_f32_16x16x32_bf16 v[44:47], v[96:99], a[0:3], 0\n"
"s_cmp_lt_u32 s90, 0x8000000\n"
"s_cselect_b32 s90, 0x80000000, s90\n"
"v_mfma_f32_16x16x32_bf16 v[44:47], v[100:103], a[4:7], v[44:47]\n"
"s_waitcnt vmcnt(15)\n"
"global_load_dwordx4 a[224:227], v10, s[56:57] offset:0 sc1\n"
"v_mfma_f32_16x16x32_bf16 v[44:47], v[104:107], a[8:11], v[44:47]\n"
"s_waitcnt lgkmcnt(6)\n"
"buffer_load_dwordx2 v[92:93], v15, s[40:43], s90 offen offset:0 sc1\n"
"v_mfma_f32_16x16x32_bf16 v[44:47], v[108:111], a[12:15], v[44:47]\n"
"s_waitcnt vmcnt(13)\n"
"buffer_store_dwordx4 v[48:51], v18, s[76:79], s29 offen sc1\n"
"v_mfma_f32_16x16x32_bf16 v[44:47], v[112:115], a[16:19], v[44:47]\n"
"s_waitcnt lgkmcnt(3)\n"
"v_add_f32 v60, v60, v56\n"
"v_add_f32 v61, v61, v57\n"
"v_mfma_f32_16x16x32_bf16 v[44:47], v[116:119], a[20:23], v[44:47]\n"
"s_waitcnt vmcnt(13)\n"
"v_add_f32 v62, v62, v58\n"
"v_add_f32 v63, v63, v59\n"
"v_mfma_f32_16x16x32_bf16 v[44:47], v[120:123], a[24:27], v[44:47]\n"
"global_load_dwordx4 a[228:231], v11, s[56:57] offset:0 sc1\n"
"v_add_f32 v60, v60, v64\n"
"v_mfma_f32_16x16x32_bf16 v[48:51], v[96:99], a[28:31], 0\n"
"v_add_f32 v61, v61, v65\n"
"v_add_f32 v62, v62, v66\n"
"v_mfma_f32_16x16x32_bf16 v[48:51], v[100:103], a[32:35], v[48:51]\n"
"ds_write_addtid_b32 v44 offset:0\n"
"v_add_f32 v63, v63, v67\n"
"v_mfma_f32_16x16x32_bf16 v[48:51], v[104:107], a[36:39], v[48:51]\n"
"ds_write_addtid_b32 v45 offset:4640\n"
"v_add_f32 v60, v60, v52\n"
"v_mfma_f32_16x16x32_bf16 v[48:51], v[108:111], a[40:43], v[48:51]\n"
"ds_write_addtid_b32 v46 offset:9344\n"
"v_add_f32 v61, v61, v53\n"
"v_mfma_f32_16x16x32_bf16 v[48:51], v[112:115], a[44:47], v[48:51]\n"
"ds_write_addtid_b32 v47 offset:13984\n"
"v_add_f32 v62, v62, v54\n"
"v_mfma_f32_16x16x32_bf16 v[48:51], v[116:119], a[48:51], v[48:51]\n"
"v_add_f32 v63, v63, v55\n"
"v_mfma_f32_16x16x32_bf16 v[48:51], v[120:123], a[52:55], v[48:51]\n"
"s_add_u32 s31, s50, 0xffd00000\n"
"s_and_b32 s30, s31, 0xfbffffff\n"
"v_cvt_pk_bf16_f32 v44, v60, v61\n"
"v_mfma_f32_16x16x32_bf16 v[52:55], v[96:99], a[56:59], 0\n"
"s_waitcnt vmcnt(10)\n"
"v_cvt_pk_bf16_f32 v45, v62, v63\n"
"v_mfma_f32_16x16x32_bf16 v[56:59], v[38:41], v[68:71], 0\n"
"v_mfma_f32_16x16x32_bf16 v[56:59], v[38:41], v[76:79], v[56:59]\n"
"v_mfma_f32_16x16x32_bf16 v[56:59], v[38:41], v[84:87], v[56:59]\n"
"v_mfma_f32_16x16x32_bf16 v[60:63], v[38:41], v[72:75], 0\n"
"v_mfma_f32_16x16x32_bf16 v[60:63], v[38:41], v[80:83], v[60:63]\n"
"v_mfma_f32_16x16x32_bf16 v[60:63], v[38:41], v[88:91], v[60:63]\n"
"v_cvt_pk_bf16_f32 v64, v56, v57\n"
"v_cvt_pk_bf16_f32 v65, v58, v59\n"
"v_mfma_f32_16x16x32_bf16 v[52:55], v[100:103], a[60:63], v[52:55]\n"
"s_waitcnt lgkmcnt(4)\n"
"ds_write_addtid_b32 v48 offset:256\n"
"v_add_f32 v152, v152, v156\n"
"v_mfma_f32_16x16x32_bf16 v[52:55], v[104:107], a[64:67], v[52:55]\n"
"ds_write_addtid_b32 v49 offset:4896\n"
"v_add_f32 v153, v153, v157\n"
"v_mfma_f32_16x16x32_bf16 v[52:55], v[108:111], a[68:71], v[52:55]\n"
"ds_write_addtid_b32 v50 offset:9600\n"
"v_add_f32 v154, v154, v158\n"
"v_mfma_f32_16x16x32_bf16 v[52:55], v[112:115], a[72:75], v[52:55]\n"
"ds_write_addtid_b32 v51 offset:14240\n"
"v_add_f32 v155, v155, v159\n"
"v_mfma_f32_16x16x32_bf16 v[52:55], v[116:119], a[76:79], v[52:55]\n"
"buffer_store_dwordx2 v[0:1], v14, s[40:43], s28 offen offset:0 sc1\n"
"v_add_f32 v152, v152, v160\n"
"v_mfma_f32_16x16x32_bf16 v[52:55], v[120:123], a[80:83], v[52:55]\n"
"v_add_f32 v153, v153, v161\n"
"v_add_f32 v154, v154, v162\n"
"v_mfma_f32_16x16x32_bf16 v[48:51], v[96:99], a[84:87], 0\n"
"v_add_f32 v155, v155, v163\n"
"v_add_f32 v152, v152, v148\n"
"v_mfma_f32_16x16x32_bf16 v[48:51], v[100:103], a[88:91], v[48:51]\n"
"s_add_u32 s58, s50, 0x180000\n"
"ds_write_addtid_b32 v52 offset:512\n"
"v_add_f32 v153, v153, v149\n"
"v_mfma_f32_16x16x32_bf16 v[48:51], v[104:107], a[92:95], v[48:51]\n"
"s_and_b32 s58, s58, 0x3ffffff\n"
"ds_write_addtid_b32 v53 offset:5152\n"
"v_add_f32 v154, v154, v150\n"
"v_mfma_f32_16x16x32_bf16 v[48:51], v[108:111], a[96:99], v[48:51]\n"
"s_add_u32 s56, s40, s58\n"
"ds_write_addtid_b32 v54 offset:9856\n"
"v_add_f32 v155, v155, v151\n"
"v_mfma_f32_16x16x32_bf16 v[48:51], v[112:115], a[100:103], v[48:51]\n"
"s_addc_u32 s57, s41, 0\n"
"ds_write_addtid_b32 v55 offset:14496\n"
"v_cvt_pk_bf16_f32 v46, v152, v153\n"
"v_mfma_f32_16x16x32_bf16 v[48:51], v[116:119], a[104:107], v[48:51]\n"
"s_waitcnt vmcnt(9)\n"
"v_cmp_ne_u64 vcc, s[48:49], v[164:165]\n"
"v_cvt_pk_bf16_f32 v47, v154, v155\n"
"v_mfma_f32_16x16x32_bf16 v[48:51], v[120:123], a[108:111], v[48:51]\n"
"buffer_store_dwordx4 v[44:47], v13, s[40:43], s30 offen sc1\n"
"v_cvt_pk_bf16_f32 v66, v60, v61\n"
"v_cvt_pk_bf16_f32 v67, v62, v63\n"
"v_mfma_f32_16x16x32_bf16 v[52:55], v[144:147], v[144:147], 0\n"
"v_mfma_f32_16x16x32_bf16 v[44:47], v[34:37], v[144:147], 0\n"
"v_mfma_f32_16x16x32_bf16 v[52:55], v[64:67], v[64:67], v[52:55]\n"
"v_mfma_f32_16x16x32_bf16 v[44:47], v[34:37], v[64:67], v[44:47]\n"
"v_mfma_f32_16x16x32_bf16 v[56:59], v[96:99], a[112:115], 0\n"
"s_cbranch_vccnz .LJ_3s\n"
".LJ_3r:\n"
"v_cndmask_b32_e64 v52, v52, v53, s[64:65]\n"
"v_mfma_f32_16x16x32_bf16 v[56:59], v[100:103], a[116:119], v[56:59]\n"
"s_add_u32 s50, s50, 0x180000\n"
"ds_write_addtid_b32 v48 offset:768\n"
"v_cndmask_b32_e64 v54, v54, v55, s[64:65]\n"
"v_mfma_f32_16x16x32_bf16 v[56:59], v[104:107], a[120:123], v[56:59]\n"
"ds_write_addtid_b32 v49 offset:5408\n"
"v_cndmask_b32_e64 v45, v52, v54, s[66:67]\n"
"v_mfma_f32_16x16x32_bf16 v[56:59], v[108:111], a[124:127], v[56:59]\n"
"buffer_store_dwordx2 v[44:45], v23, s[40:43], s91 offen offset:0 sc1\n"
"ds_write_addtid_b32 v50 offset:10112\n"
"v_mfma_f32_16x16x32_bf16 v[56:59], v[112:115], a[128:131], v[56:59]\n"
"ds_write_addtid_b32 v51 offset:14752\n"
"global_load_dwordx4 a[232:235], v5, s[56:57] offset:0 sc1\n"
"v_mfma_f32_16x16x32_bf16 v[56:59], v[116:119], a[132:135], v[56:59]\n"
"ds_bpermute_b32 v44, v21, v64\n"
"v_mfma_f32_16x16x32_bf16 v[56:59], v[120:123], a[136:139], v[56:59]\n"
"buffer_load_dwordx2 v[144:145], v12, s[40:43], s50 offen sc1\n"
"v_mfma_f32_16x16x32_bf16 v[48:51], v[96:99], a[140:143], 0\n"
"ds_bpermute_b32 v45, v21, v65\n"
"v_mfma_f32_16x16x32_bf16 v[48:51], v[100:103], a[144:147], v[48:51]\n"
"ds_write_addtid_b32 v56 offset:1024\n"
"v_mfma_f32_16x16x32_bf16 v[48:51], v[104:107], a[148:151], v[48:51]\n"
"ds_write_addtid_b32 v57 offset:5664\n"
"global_load_dwordx4 a[236:239], v6, s[56:57] offset:0 sc1\n"
"v_mfma_f32_16x16x32_bf16 v[48:51], v[108:111], a[152:155], v[48:51]\n"
"ds_write_addtid_b32 v58 offset:10368\n"
"v_mfma_f32_16x16x32_bf16 v[48:51], v[112:115], a[156:159], v[48:51]\n"
"ds_write_addtid_b32 v59 offset:15008\n"
"v_mfma_f32_16x16x32_bf16 v[48:51], v[116:119], a[160:163], v[48:51]\n"
"ds_bpermute_b32 v46, v21, v66\n"
"v_mfma_f32_16x16x32_bf16 v[48:51], v[120:123], a[164:167], v[48:51]\n"
"ds_bpermute_b32 v47, v21, v67\n"
"v_mfma_f32_16x16x32_bf16 v[68:71], a[168:171], v[96:99], 0\n"
"global_load_dwordx4 a[240:243], v7, s[56:57] offset:0 sc1\n"
"v_mfma_f32_16x16x32_bf16 v[68:71], a[172:175], v[100:103], v[68:71]\n"
"ds_write_addtid_b32 v48 offset:1280\n"
"v_mfma_f32_16x16x32_bf16 v[68:71], a[176:179], v[104:107], v[68:71]\n"
"ds_write_addtid_b32 v49 offset:5920\n"
"v_mfma_f32_16x16x32_bf16 v[68:71], a[180:183], v[108:111], v[68:71]\n"
"ds_write_addtid_b32 v50 offset:10624\n"
"v_mfma_f32_16x16x32_bf16 v[68:71], a[184:187], v[112:115], v[68:71]\n"
"ds_write_addtid_b32 v51 offset:15264\n"
"v_mfma_f32_16x16x32_bf16 v[68:71], a[188:191], v[116:119], v[68:71]\n"
"s_waitcnt lgkmcnt(4)\n"
"buffer_store_dwordx4 v[44:47], v19, s[76:79], s29 offen sc1\n"
"global_load_dwordx4 a[244:247], v8, s[56:57] offset:0 sc1\n"
"v_mfma_f32_16x16x32_bf16 v[68:71], a[192:195], v[120:123], v[68:71]\n"
"v_mfma_f32_16x16x32_bf16 v[72:75], a[196:199], v[96:99], 0\n"
"s_and_b32 s30, s90, 0x3ffffff\n"
"v_mfma_f32_16x16x32_bf16 v[72:75], a[200:203], v[100:103], v[72:75]\n"
"s_waitcnt lgkmcnt(0)\n"
"v_mfma_f32_16x16x32_bf16 v[72:75], a[204:207], v[104:107], v[72:75]\n"
"s_barrier\n"
"v_mfma_f32_16x16x32_bf16 v[72:75], a[208:211], v[108:111], v[72:75]\n"
"s_waitcnt vmcnt(11)\n"
"global_load_dwordx4 a[248:251], v9, s[56:57] offset:0 sc1\n"
"ds_read_b128 v[76:79], v2 offset:0\n"
"v_mfma_f32_16x16x32_bf16 v[72:75], a[212:215], v[112:115], v[72:75]\n"
"s_add_u32 s62, s40, s30\n"
"ds_read_b128 v[80:83], v3 offset:512\n"
"ds_read_b128 v[84:87], v4 offset:1024\n"
"v_mfma_f32_16x16x32_bf16 v[72:75], a[216:219], v[116:119], v[72:75]\n"
"s_addc_u32 s63, s41, 0\n"
"ds_read_b128 v[88:91], v2 offset:256\n"
"ds_read_b128 v[96:99], v3 offset:768\n"
"v_mfma_f32_16x16x32_bf16 v[72:75], a[220:223], v[120:123], v[72:75]\n"
"ds_read_b128 v[100:103], v4 offset:1280\n"
"v_mfma_f32_16x16x32_bf16 v[104:107], v[124:127], a[0:3], 0\n"
"v_cmp_ne_u64 vcc, s[84:85], v[92:93]\n"
"v_mfma_f32_16x16x32_bf16 v[104:107], v[128:131], a[4:7], v[104:107]\n"
"global_load_dwordx4 a[252:255], v10, s[56:57] offset:0 sc1\n"
"v_mfma_f32_16x16x32_bf16 v[104:107], v[132:135], a[8:11], v[104:107]\n"
"s_cbranch_vccnz .LJ_4s\n"
".LJ_4r:\n"
"v_mfma_f32_16x16x32_bf16 v[104:107], v[136:139], a[12:15], v[104:107]\n"
"global_load_dwordx4 v[46:49], v16, s[62:63] offset:-4096 sc1\n"
"v_mfma_f32_16x16x32_bf16 v[104:107], v[140:143], a[16:19], v[104:107]\n"
"s_waitcnt lgkmcnt(3)\n"
"v_add_f32 v76, v76, v80\n"
"v_add_f32 v77, v77, v81\n"
"v_mfma_f32_16x16x32_bf16 v[104:107], a[224:227], a[20:23], v[104:107]\n"
"s_waitcnt vmcnt(12)\n"
"v_add_f32 v78, v78, v82\n"
"v_add_f32 v79, v79, v83\n"
"v_mfma_f32_16x16x32_bf16 v[104:107], a[228:231], a[24:27], v[104:107]\n"
"global_load_dwordx4 v[92:95], v11, s[56:57] offset:0 sc1\n"
"v_add_f32 v76, v76, v84\n"
"v_mfma_f32_16x16x32_bf16 v[80:83], v[124:127], a[28:31], 0\n"
"v_add_f32 v77, v77, v85\n"
"v_add_f32 v78, v78, v86\n"
"v_mfma_f32_16x16x32_bf16 v[80:83], v[128:131], a[32:35], v[80:83]\n"
"ds_write_addtid_b32 v104 offset:1536\n"
"v_add_f32 v79, v79, v87\n"
"v_mfma_f32_16x16x32_bf16 v[80:83], v[132:135], a[36:39], v[80:83]\n"
"ds_write_addtid_b32 v105 offset:6176\n"
"v_add_f32 v76, v76, v68\n"
"v_mfma_f32_16x16x32_bf16 v[80:83], v[136:139], a[40:43], v[80:83]\n"
"ds_write_addtid_b32 v106 offset:10880\n"
"v_add_f32 v77, v77, v69\n"
"v_mfma_f32_16x16x32_bf16 v[80:83], v[140:143], a[44:47], v[80:83]\n"
"ds_write_addtid_b32 v107 offset:15520\n"
"v_add_f32 v78, v78, v70\n"
"v_mfma_f32_16x16x32_bf16 v[80:83], a[224:227], a[48:51], v[80:83]\n"
"global_load_dwordx4 v[50:53], v16, s[62:63] offset:-3072 sc1\n"
"v_add_f32 v79, v79, v71\n"
"v_mfma_f32_16x16x32_bf16 v[80:83], a[228:231], a[52:55], v[80:83]\n"
"s_add_u32 s29, s50, 0xffd00000\n"
"s_and_b32 s30, s29, 0xfbffffff\n"
"v_cvt_pk_bf16_f32 v84, v76, v77\n"
"v_mfma_f32_16x16x32_bf16 v[68:71], v[124:127], a[56:59], 0\n"
"v_cvt_pk_bf16_f32 v85, v78, v79\n"
"v_mfma_f32_16x16x32_bf16 v[68:71], v[128:131], a[60:63], v[68:71]\n"
"s_waitcnt lgkmcnt(4)\n"
"ds_write_addtid_b32 v80 offset:1792\n"
"v_add_f32 v88, v88, v96\n"
"v_mfma_f32_16x16x32_bf16 v[68:71], v[132:135], a[64:67], v[68:71]\n"
"ds_write_addtid_b32 v81 offset:6432\n"
"v_add_f32 v89, v89, v97\n"
"v_mfma_f32_16x16x32_bf16 v[68:71], v[136:139], a[68:71], v[68:71]\n"
"ds_write_addtid_b32 v82 offset:11136\n"
"v_add_f32 v90, v90, v98\n"
"v_mfma_f32_16x16x32_bf16 v[68:71], v[140:143], a[72:75], v[68:71]\n"
"ds_write_addtid_b32 v83 offset:15776\n"
"v_add_f32 v91, v91, v99\n"
"v_mfma_f32_16x16x32_bf16 v[68:71], a[224:227], a[76:79], v[68:71]\n"
"s_waitcnt vmcnt(12)\n"
"v_add_f32 v88, v88, v100\n"
"v_add_f32 v89, v89, v101\n"
"v_mfma_f32_16x16x32_bf16 v[68:71], a[228:231], a[80:83], v[68:71]\n"
"v_add_f32 v90, v90, v102\n"
"v_add_f32 v91, v91, v103\n"
"v_mfma_f32_16x16x32_bf16 v[76:79], v[124:127], a[84:87], 0\n"
"v_add_f32 v88, v88, v72\n"
"v_add_f32 v89, v89, v73\n"
"v_mfma_f32_16x16x32_bf16 v[76:79], v[128:131], a[88:91], v[76:79]\n"
"s_add_u32 s58, s50, 0x180000\n"
"ds_write_addtid_b32 v68 offset:2048\n"
"v_add_f32 v90, v90, v74\n"
"v_mfma_f32_16x16x32_bf16 v[76:79], v[132:135], a[92:95], v[76:79]\n"
"s_and_b32 s58, s58, 0x3ffffff\n"
"ds_write_addtid_b32 v69 offset:6688\n"
"v_add_f32 v91, v91, v75\n"
"v_mfma_f32_16x16x32_bf16 v[76:79], v[136:139], a[96:99], v[76:79]\n"
"s_add_u32 s56, s40, s58\n"
"ds_write_addtid_b32 v70 offset:11392\n"
"v_cvt_pk_bf16_f32 v86, v88, v89\n"
"v_mfma_f32_16x16x32_bf16 v[76:79], v[140:143], a[100:103], v[76:79]\n"
"s_addc_u32 s57, s41, 0\n"
"ds_write_addtid_b32 v71 offset:16032\n"
"v_cvt_pk_bf16_f32 v87, v90, v91\n"
"v_mfma_f32_16x16x32_bf16 v[76:79], a[224:227], a[104:107], v[76:79]\n"
"s_waitcnt vmcnt(9)\n"
"buffer_store_dwordx4 v[84:87], v13, s[40:43], s30 offen sc1\n"
"v_cmp_ne_u64 vcc, s[48:49], v[144:145]\n"
"v_mfma_f32_16x16x32_bf16 v[76:79], a[228:231], a[108:111], v[76:79]\n"
"buffer_store_dwordx2 v[0:1], v14, s[40:43], s31 offen offset:0 sc1\n"
"v_mfma_f32_16x16x32_bf16 v[68:71], v[124:127], a[112:115], 0\n"
"s_cbranch_vccnz .LJ_1s\n"
".LJ_1r:\n"
"v_mfma_f32_16x16x32_bf16 v[68:71], v[128:131], a[116:119], v[68:71]\n"
"s_add_u32 s50, s50, 0x180000\n"
"ds_write_addtid_b32 v76 offset:2304\n"
"global_load_dwordx4 v[54:57], v16, s[62:63] offset:-2048 sc1\n"
"v_mfma_f32_16x16x32_bf16 v[68:71], v[132:135], a[120:123], v[68:71]\n"
"ds_write_addtid_b32 v77 offset:6944\n"
"v_mfma_f32_16x16x32_bf16 v[68:71], v[136:139], a[124:127], v[68:71]\n"
"ds_write_addtid_b32 v78 offset:11648\n"
"v_mfma_f32_16x16x32_bf16 v[68:71], v[140:143], a[128:131], v[68:71]\n"
"ds_write_addtid_b32 v79 offset:16288\n"
"global_load_dwordx4 v[96:99], v5, s[56:57] offset:0 sc1\n"
"v_mfma_f32_16x16x32_bf16 v[68:71], a[224:227], a[132:135], v[68:71]\n"
"v_mfma_f32_16x16x32_bf16 v[68:71], a[228:231], a[136:139], v[68:71]\n"
"buffer_load_dwordx2 v[160:161], v12, s[40:43], s50 offen sc1\n"
"v_mfma_f32_16x16x32_bf16 v[112:115], v[124:127], a[140:143], 0\n"
"global_load_dwordx4 v[58:61], v16, s[62:63] offset:-1024 sc1\n"
"v_mfma_f32_16x16x32_bf16 v[112:115], v[128:131], a[144:147], v[112:115]\n"
"ds_write_addtid_b32 v68 offset:2560\n"
"v_mfma_f32_16x16x32_bf16 v[112:115], v[132:135], a[148:151], v[112:115]\n"
"ds_write_addtid_b32 v69 offset:7200\n"
"global_load_dwordx4 v[100:103], v6, s[56:57] offset:0 sc1\n"
"v_mfma_f32_16x16x32_bf16 v[112:115], v[136:139], a[152:155], v[112:115]\n"
"ds_write_addtid_b32 v70 offset:11904\n"
"v_mfma_f32_16x16x32_bf16 v[112:115], v[140:143], a[156:159], v[112:115]\n"
"s_waitcnt vmcnt(13)\n"
"buffer_store_dwordx2 v[0:1], v24, s[40:43], s91 offen offset:0 sc1\n"
"ds_write_addtid_b32 v71 offset:16544\n"
"v_mfma_f32_16x16x32_bf16 v[112:115], a[224:227], a[160:163], v[112:115]\n"
"global_load_dwordx4 v[62:65], v17, s[62:63] offset:0 sc1\n"
"v_mfma_f32_16x16x32_bf16 v[112:115], a[228:231], a[164:167], v[112:115]\n"
"v_mfma_f32_16x16x32_bf16 v[144:147], a[168:171], v[124:127], 0\n"
"global_load_dwordx4 v[104:107], v7, s[56:57] offset:0 sc1\n"
"v_mfma_f32_16x16x32_bf16 v[144:147], a[172:175], v[128:131], v[144:147]\n"
"ds_write_addtid_b32 v112 offset:2816\n"
"v_mfma_f32_16x16x32_bf16 v[144:147], a[176:179], v[132:135], v[144:147]\n"
"ds_write_addtid_b32 v113 offset:7456\n"
"v_mfma_f32_16x16x32_bf16 v[144:147], a[180:183], v[136:139], v[144:147]\n"
"ds_write_addtid_b32 v114 offset:12160\n"
"global_load_dwordx4 v[70:73], v16, s[62:63] offset:0 sc1\n"
"v_mfma_f32_16x16x32_bf16 v[144:147], a[184:187], v[140:143], v[144:147]\n"
"ds_write_addtid_b32 v115 offset:16800\n"
"v_mfma_f32_16x16x32_bf16 v[144:147], a[188:191], a[224:227], v[144:147]\n"
"global_load_dwordx4 v[108:111], v8, s[56:57] offset:0 sc1\n"
"v_mfma_f32_16x16x32_bf16 v[144:147], a[192:195], a[228:231], v[144:147]\n"
"v_mfma_f32_16x16x32_bf16 v[148:151], a[196:199], v[124:127], 0\n"
"v_mfma_f32_16x16x32_bf16 v[148:151], a[200:203], v[128:131], v[148:151]\n"
"s_waitcnt lgkmcnt(0)\n"
"global_load_dwordx4 v[74:77], v16, s[62:63] offset:1024 sc1\n"
"v_mfma_f32_16x16x32_bf16 v[148:151], a[204:207], v[132:135], v[148:151]\n"
"s_barrier\n"
"v_mfma_f32_16x16x32_bf16 v[148:151], a[208:211], v[136:139], v[148:151]\n"
"global_load_dwordx4 v[112:115], v9, s[56:57] offset:0 sc1\n"
"ds_read_b128 v[124:127], v2 offset:1536\n"
"v_mfma_f32_16x16x32_bf16 v[148:151], a[212:215], v[140:143], v[148:151]\n"
"ds_read_b128 v[128:131], v3 offset:2048\n"
"ds_read_b128 v[132:135], v4 offset:2560\n"
"v_mfma_f32_16x16x32_bf16 v[148:151], a[216:219], a[224:227], v[148:151]\n"
"ds_read_b128 v[136:139], v2 offset:1792\n"
"ds_read_b128 v[140:143], v3 offset:2304\n"
"v_mfma_f32_16x16x32_bf16 v[148:151], a[220:223], a[228:231], v[148:151]\n"
"ds_read_b128 v[152:155], v4 offset:2816\n"
"global_load_dwordx4 v[78:81], v16, s[62:63] offset:2048 sc1\n"
"v_mfma_f32_16x16x32_bf16 v[156:159], a[232:235], a[0:3], 0\n"
"v_mfma_f32_16x16x32_bf16 v[156:159], a[236:239], a[4:7], v[156:159]\n"
"s_waitcnt vmcnt(20)\n"
"global_load_dwordx4 v[116:119], v10, s[56:57] offset:0 sc1\n"
"v_mfma_f32_16x16x32_bf16 v[156:159], a[240:243], a[8:11], v[156:159]\n"
"v_mfma_f32_16x16x32_bf16 v[156:159], a[244:247], a[12:15], v[156:159]\n"
"s_waitcnt vmcnt(19)\n"
"v_mfma_f32_16x16x32_bf16 v[156:159], a[248:251], a[16:19], v[156:159]\n"
"s_waitcnt lgkmcnt(3)\n"
"v_add_f32 v124, v124, v128\n"
"v_add_f32 v125, v125, v129\n"
"v_mfma_f32_16x16x32_bf16 v[156:159], a[252:255], a[20:23], v[156:159]\n"
"s_waitcnt vmcnt(17)\n"
"v_add_f32 v126, v126, v130\n"
"v_add_f32 v127, v127, v131\n"
"v_mfma_f32_16x16x32_bf16 v[156:159], v[92:95], a[24:27], v[156:159]\n"
"global_load_dwordx4 v[120:123], v11, s[56:57] offset:0 sc1\n"
"v_add_f32 v124, v124, v132\n"
"v_mfma_f32_16x16x32_bf16 v[128:131], a[232:235], a[28:31], 0\n"
"v_add_f32 v125, v125, v133\n"
"v_add_f32 v126, v126, v134\n"
"v_mfma_f32_16x16x32_bf16 v[128:131], a[236:239], a[32:35], v[128:131]\n"
"ds_write_addtid_b32 v156 offset:3072\n"
"v_add_f32 v127, v127, v135\n"
"v_mfma_f32_16x16x32_bf16 v[128:131], a[240:243], a[36:39], v[128:131]\n"
"ds_write_addtid_b32 v157 offset:7712\n"
"v_add_f32 v124, v124, v144\n"
"v_mfma_f32_16x16x32_bf16 v[128:131], a[244:247], a[40:43], v[128:131]\n"
"ds_write_addtid_b32 v158 offset:12416\n"
"v_add_f32 v125, v125, v145\n"
"v_mfma_f32_16x16x32_bf16 v[128:131], a[248:251], a[44:47], v[128:131]\n"
"ds_write_addtid_b32 v159 offset:17056\n"
"v_add_f32 v126, v126, v146\n"
"v_mfma_f32_16x16x32_bf16 v[128:131], a[252:255], a[48:51], v[128:131]\n"
"global_load_dwordx4 v[82:85], v16, s[62:63] offset:3072 sc1\n"
"v_add_f32 v127, v127, v147\n"
"v_mfma_f32_16x16x32_bf16 v[128:131], v[92:95], a[52:55], v[128:131]\n"
"s_add_u32 s28, s50, 0xffd00000\n"
"s_and_b32 s31, s28, 0xfbffffff\n"
"v_cvt_pk_bf16_f32 v132, v124, v125\n"
"v_mfma_f32_16x16x32_bf16 v[144:147], a[232:235], a[56:59], 0\n"
"v_cvt_pk_bf16_f32 v133, v126, v127\n"
"v_mfma_f32_16x16x32_bf16 v[144:147], a[236:239], a[60:63], v[144:147]\n"
"s_waitcnt lgkmcnt(4)\n"
"ds_write_addtid_b32 v128 offset:3328\n"
"v_add_f32 v136, v136, v140\n"
"v_mfma_f32_16x16x32_bf16 v[144:147], a[240:243], a[64:67], v[144:147]\n"
"s_mov_b32 s91, s90\n"
"ds_write_addtid_b32 v129 offset:7968\n"
"v_add_f32 v137, v137, v141\n"
"v_mfma_f32_16x16x32_bf16 v[144:147], a[244:247], a[68:71], v[144:147]\n"
"ds_write_addtid_b32 v130 offset:12672\n"
"v_add_f32 v138, v138, v142\n"
"v_mfma_f32_16x16x32_bf16 v[144:147], a[248:251], a[72:75], v[144:147]\n"
"s_waitcnt vmcnt(17)\n"
"buffer_store_dwordx2 v[0:1], v14, s[40:43], s29 offen offset:0 sc1\n"
"ds_write_addtid_b32 v131 offset:17312\n"
"v_mfma_f32_16x16x32_bf16 v[144:147], a[252:255], a[76:79], v[144:147]\n"
"v_add_f32 v139, v139, v143\n"
"v_add_f32 v136, v136, v152\n"
"v_mfma_f32_16x16x32_bf16 v[144:147], v[92:95], a[80:83], v[144:147]\n"
"v_add_f32 v137, v137, v153\n"
"v_add_f32 v138, v138, v154\n"
"v_mfma_f32_16x16x32_bf16 v[128:131], a[232:235], a[84:87], 0\n"
"v_add_f32 v139, v139, v155\n"
"v_add_f32 v136, v136, v148\n"
"v_mfma_f32_16x16x32_bf16 v[128:131], a[236:239], a[88:91], v[128:131]\n"
"s_add_u32 s58, s50, 0x180000\n"
"ds_write_addtid_b32 v144 offset:3584\n"
"v_add_f32 v137, v137, v149\n"
"v_mfma_f32_16x16x32_bf16 v[128:131], a[240:243], a[92:95], v[128:131]\n"
"s_and_b32 s58, s58, 0x3ffffff\n"
"ds_write_addtid_b32 v145 offset:8224\n"
"v_add_f32 v138, v138, v150\n"
"v_mfma_f32_16x16x32_bf16 v[128:131], a[244:247], a[96:99], v[128:131]\n"
"s_add_u32 s56, s40, s58\n"
"ds_write_addtid_b32 v146 offset:12928\n"
"v_add_f32 v139, v139, v151\n"
"v_mfma_f32_16x16x32_bf16 v[128:131], a[248:251], a[100:103], v[128:131]\n"
"s_addc_u32 s57, s41, 0\n"
"ds_write_addtid_b32 v147 offset:17568\n"
"v_cvt_pk_bf16_f32 v134, v136, v137\n"
"v_mfma_f32_16x16x32_bf16 v[128:131], a[252:255], a[104:107], v[128:131]\n"
"s_waitcnt vmcnt(14)\n"
"v_cmp_ne_u64 vcc, s[48:49], v[160:161]\n"
"v_cvt_pk_bf16_f32 v135, v138, v139\n"
"v_mfma_f32_16x16x32_bf16 v[128:131], v[92:95], a[108:111], v[128:131]\n"
"buffer_store_dwordx4 v[132:135], v13, s[40:43], s31 offen sc1\n"
"global_load_dwordx4 v[86:89], v17, s[62:63] offset:1024 sc1\n"
"v_mfma_f32_16x16x32_bf16 v[136:139], a[232:235], a[112:115], 0\n"
"s_cbranch_vccnz .LJ_2s\n"
".LJ_2r:\n"
"v_mov_b32 v44, v26\n"
"v_mfma_f32_16x16x32_bf16 v[136:139], a[236:239], a[116:119], v[136:139]\n"
"s_add_u32 s50, s50, 0x180000\n"
"ds_write_addtid_b32 v128 offset:3840\n"
"v_mov_b32 v45, v27\n"
"v_mfma_f32_16x16x32_bf16 v[136:139], a[240:243], a[120:123], v[136:139]\n"
"ds_write_addtid_b32 v129 offset:8480\n"
"v_mov_b32 v66, v28\n"
"v_mfma_f32_16x16x32_bf16 v[136:139], a[244:247], a[124:127], v[136:139]\n"
"ds_write_addtid_b32 v130 offset:13184\n"
"v_mov_b32 v67, v29\n"
"v_mfma_f32_16x16x32_bf16 v[136:139], a[248:251], a[128:131], v[136:139]\n"
"s_waitcnt vmcnt(12)\n"
"ds_write_addtid_b32 v131 offset:17824\n"
"global_load_dwordx4 v[124:127], v5, s[56:57] offset:0 sc1\n"
"v_mfma_f32_16x16x32_bf16 v[132:135], v[38:41], v[44:47], 0\n"
"s_cmp_ge_i32 s91, s80\n"
"s_cbranch_scc1 .LJ_ws\n"
".LJ_wr:\n"
"v_mfma_f32_16x16x32_bf16 v[132:135], v[38:41], v[52:55], v[132:135]\n"
"s_lshr_b32 s29, s91, s81\n"
"v_mfma_f32_16x16x32_bf16 v[132:135], v[38:41], v[60:63], v[132:135]\n"
"s_bitcmp1_b32 s29, 15\n"
"s_cselect_b32 s31, s83, s82\n"
"v_mfma_f32_16x16x32_bf16 v[128:131], v[38:41], v[48:51], 0\n"
"s_add_u32 s29, s29, s31\n"
"v_mfma_f32_16x16x32_bf16 v[128:131], v[38:41], v[56:59], v[128:131]\n"
"v_mfma_f32_16x16x32_bf16 v[128:131], v[38:41], v[64:67], v[128:131]\n"
"v_cvt_pk_bf16_f32 v144, v132, v133\n"
"v_cvt_pk_bf16_f32 v145, v134, v135\n"
"v_mfma_f32_16x16x32_bf16 v[136:139], a[252:255], a[132:135], v[136:139]\n"
"v_mfma_f32_16x16x32_bf16 v[136:139], v[92:95], a[136:139], v[136:139]\n"
"buffer_load_dwordx2 v[164:165], v12, s[40:43], s50 offen sc1\n"
"v_mfma_f32_16x16x32_bf16 v[44:47], a[232:235], a[140:143], 0\n"
"v_cvt_pk_bf16_f32 v146, v128, v129\n"
"v_cvt_pk_bf16_f32 v147, v130, v131\n"
"v_mfma_f32_16x16x32_bf16 v[44:47], a[236:239], a[144:147], v[44:47]\n"
"ds_write_addtid_b32 v136 offset:4096\n"
"v_mov_b32 v68, v30\n"
"v_mfma_f32_16x16x32_bf16 v[44:47], a[240:243], a[148:151], v[44:47]\n"
"ds_write_addtid_b32 v137 offset:8736\n"
"global_load_dwordx4 v[128:131], v6, s[56:57] offset:0 sc1\n"
"v_mfma_f32_16x16x32_bf16 v[44:47], a[244:247], a[152:155], v[44:47]\n"
"ds_write_addtid_b32 v138 offset:13440\n"
"v_mov_b32 v69, v31\n"
"v_mfma_f32_16x16x32_bf16 v[44:47], a[248:251], a[156:159], v[44:47]\n"
"ds_write_addtid_b32 v139 offset:18080\n"
"v_mov_b32 v90, v32\n"
"v_mfma_f32_16x16x32_bf16 v[44:47], a[252:255], a[160:163], v[44:47]\n"
"ds_bpermute_b32 v48, v21, v144\n"
"v_mfma_f32_16x16x32_bf16 v[44:47], v[92:95], a[164:167], v[44:47]\n"
"ds_bpermute_b32 v49, v21, v145\n"
"v_mfma_f32_16x16x32_bf16 v[52:55], a[168:171], a[232:235], 0\n"
"global_load_dwordx4 v[132:135], v7, s[56:57] offset:0 sc1\n"
"v_mov_b32 v91, v33\n"
"v_mfma_f32_16x16x32_bf16 v[52:55], a[172:175], a[236:239], v[52:55]\n"
"ds_write_addtid_b32 v44 offset:4352\n"
"v_mfma_f32_16x16x32_bf16 v[52:55], a[176:179], a[240:243], v[52:55]\n"
"ds_write_addtid_b32 v45 offset:8992\n"
"v_mfma_f32_16x16x32_bf16 v[52:55], a[180:183], a[244:247], v[52:55]\n"
"ds_write_addtid_b32 v46 offset:13696\n"
"v_mfma_f32_16x16x32_bf16 v[52:55], a[184:187], a[248:251], v[52:55]\n"
"ds_write_addtid_b32 v47 offset:18336\n"
"v_mfma_f32_16x16x32_bf16 v[52:55], a[188:191], a[252:255], v[52:55]\n"
"global_load_dwordx4 v[136:139], v8, s[56:57] offset:0 sc1\n"
"v_mfma_f32_16x16x32_bf16 v[52:55], a[192:195], v[92:95], v[52:55]\n"
"ds_bpermute_b32 v50, v21, v146\n"
"v_mfma_f32_16x16x32_bf16 v[148:151], a[196:199], a[232:235], 0\n"
"ds_bpermute_b32 v51, v21, v147\n"
"v_mfma_f32_16x16x32_bf16 v[148:151], a[200:203], a[236:239], v[148:151]\n"
"s_waitcnt lgkmcnt(2)\n"
"v_mfma_f32_16x16x32_bf16 v[148:151], a[204:207], a[240:243], v[148:151]\n"
"s_barrier\n"
"v_mfma_f32_16x16x32_bf16 v[148:151], a[208:211], a[244:247], v[148:151]\n"
"global_load_dwordx4 v[140:143], v9, s[56:57] offset:0 sc1\n"
"ds_read_b128 v[60:63], v2 offset:3072\n"
"v_mfma_f32_16x16x32_bf16 v[148:151], a[212:215], a[248:251], v[148:151]\n"
"ds_read_b128 v[56:59], v3 offset:3584\n"
"ds_read_b128 v[64:67], v4 offset:4096\n"
"v_mfma_f32_16x16x32_bf16 v[148:151], a[216:219], a[252:255], v[148:151]\n"
"s_add_u32 s90, s50, 0xff980000\n"
"ds_read_b128 v[152:155], v2 offset:3328\n"
"ds_read_b128 v[156:159], v3 offset:3840\n"
"v_mfma_f32_16x16x32_bf16 v[148:151], a[220:223], v[92:95], v[148:151]\n"
"ds_read_b128 v[160:163], v4 offset:4352\n"
"s_branch .LJ_top\n"
".LJ_1s:\n"
"s_cmp_ge_u32 s50, s53\n"
"s_cbranch_scc1 .LJ_end\n"
"s_cmp_ge_u32 s50, s52\n"
"s_cbranch_scc1 .LJ_1r\n"
"s_sub_u32 s60, s50, 0x0\n"
"s_mov_b32 s61, 200000\n"
"s_nop 7\n"
".LJ_1p:\n"
"buffer_load_dwordx2 v[144:145], v12, s[40:43], s60 offen sc1\n"
"s_waitcnt vmcnt(0)\n"
"v_cmp_ne_u64 vcc, s[48:49], v[144:145]\n"
"s_cbranch_vccz .LJ_1px\n"
"s_sleep 1\n"
"s_sub_u32 s61, s61, 1\n"
"s_cbranch_scc0 .LJ_1p\n"
"s_branch .LJ_end\n"
".LJ_1px:\n"
"s_branch .LJ_1r\n"
".LJ_2s:\n"
"s_cmp_ge_u32 s50, s53\n"
"s_cbranch_scc1 .LJ_end\n"
"s_cmp_ge_u32 s50, s52\n"
"s_cbranch_scc1 .LJ_2r\n"
"s_sub_u32 s60, s50, 0x0\n"
"s_mov_b32 s61, 200000\n"
"s_nop 7\n"
".LJ_2p:\n"
"buffer_load_dwordx2 v[160:161], v12, s[40:43], s60 offen sc1\n"
"s_waitcnt vmcnt(0)\n"
"v_cmp_ne_u64 vcc, s[48:49], v[160:161]\n"
"s_cbranch_vccz .LJ_2px\n"
"s_sleep 1\n"
"s_sub_u32 s61, s61, 1\n"
"s_cbranch_scc0 .LJ_2p\n"
"s_branch .LJ_end\n"
".LJ_2px:\n"
"s_branch .LJ_2r\n"
".LJ_3s:\n"
"s_cmp_ge_u32 s50, s53\n"
"s_cbranch_scc1 .LJ_end\n"
"s_cmp_ge_u32 s50, s52\n"
"s_cbranch_scc1 .LJ_3r\n"
"s_sub_u32 s60, s50, 0x0\n"
"s_mov_b32 s61, 200000\n"
"s_nop 7\n"
".LJ_3p:\n"
"buffer_load_dwordx2 v[164:165], v12, s[40:43], s60 offen sc1\n"
"s_waitcnt vmcnt(0)\n"
"v_cmp_ne_u64 vcc, s[48:49], v[164:165]\n"
"s_cbranch_vccz .LJ_3px\n"
"s_sleep 1\n"
"s_sub_u32 s61, s61, 1\n"
"s_cbranch_scc0 .LJ_3p\n"
"s_branch .LJ_end\n"
".LJ_3px:\n"
"s_branch .LJ_3r\n"
".LJ_4s:\n"
"s_cmp_ge_i32 s90, 0x10000000\n"
"s_cselect_b64 s[84:85], 0, s[84:85]\n"
"s_cmp_ge_u32 s90, 0x10000000\n"
"s_cbranch_scc1 .LJ_4r\n"
"s_mov_b32 s61, 200000\n"
"s_nop 7\n"
".LJ_4p:\n"
"buffer_load_dwordx2 v[92:93], v15, s[40:43], s90 offen offset:0 sc1\n"
"s_waitcnt vmcnt(0)\n"
"v_cmp_ne_u64 vcc, s[84:85], v[92:93]\n"
"s_cbranch_vccz .LJ_4px\n"
"s_sleep 1\n"
"s_sub_u32 s61, s61, 1\n"
"s_cbranch_scc0 .LJ_4p\n"
"s_branch .LJ_end\n"
".LJ_4px:\n"
"s_branch .LJ_4r\n"
".LJ_ws:\n"
"s_mov_b64 s[76:77], s[68:69]\n"
"s_mov_b32 s78, s70\n"
"s_nop 7\n"
"v_mov_b32 v18, v20\n"
"v_add_u32 v19, 64, v20\n"
"v_mov_b32 v21, v22\n"
"s_mov_b32 s80, 0x7fffffff\n"
"s_mov_b32 s81, 4\n"
"s_mov_b32 s82, 0xff400000\n"
"s_mov_b32 s83, 0xff3f8800\n"
"s_branch .LJ_wr\n"
".LJ_end:\n"
"s_cmp_lt_u32 s50, s53\n"
"s_cbranch_scc1 .LJ_ne\n"
"s_cmp_eq_u64 s[72:73], 0\n"
"s_cbranch_scc1 .LJ_ne\n"
"v_mov_b32 v12, 0\n"
"global_store_dwordx2 v12, v[0:1], s[72:73] sc1\n"
".LJ_ne:\n"
"s_waitcnt vmcnt(0) lgkmcnt(0)\n"
"s_endpgm\n"
      :: "{v2}"((unsigned)(lds0 + ((wid + 3) & 3) * 18592 + trp)), "{v3}"((unsigned)(lds0 + ((wid + 2) & 3) * 18592 + trp)), "{v4}"((unsigned)(lds0 + ((wid + 1) & 3) * 18592 + trp)), "{v5}"((unsigned)(lane * 16 + f0 * 1024 + ((0 + rot) % 7) * 1024 + 134316032)), "{v6}"((unsigned)(lane * 16 + f0 * 1024 + ((1 + rot) % 7) * 1024 + 134316032)), "{v7}"((unsigned)(lane * 16 + f0 * 1024 + ((2 + rot) % 7) * 1024 + 134316032)), "{v8}"((unsigned)(lane * 16 + f0 * 1024 + ((3 + rot) % 7) * 1024 + 134316032)), "{v9}"((unsigned)(lane * 16 + f0 * 1024 + ((4 + rot) % 7) * 1024 + 134316032)), "{v10}"((unsigned)(lane * 16 + f0 * 1024 + ((5 + rot) % 7) * 1024 + 134316032)), "{v11}"((unsigned)(lane * 16 + f0 * 1024 + ((6 + rot) % 7) * 1024 + 134316032)), "{v12}"((unsigned)((f0 + lane % 7) * 8 + kq * 32 + 353024 + J_GL * 1572864)), "{v13}"((unsigned)(lane * 16 + pair * 4096 + kq * 1024 + 212992)), "{v14}"((unsigned)(lane ? DROP : pair * 64 + kq * 8 + 354304)), "{v15}"((unsigned)((v16 * 2 + ((lane >> 2) & 1)) * 64 + (lane & 3) * 8 + 354304)), "{v16}"((unsigned)(lane * 16 + v16 * 8192 + 134434816)), "{v17}"((unsigned)(lane * 16 + v16 * 2048 + 134283264)), "{v18}"((unsigned)(lane * 16 + v16 * 2048 + 0)), "{v19}"((unsigned)(lane * 16 + v16 * 2048 + 1024)), "{v20}"((unsigned)(((lane >> 4) + (lane & 12)) * 4096 + (lane & 3) * 16 + v16 * 128)), "{v21}"((unsigned)(lane * 4)), "{v22}"((unsigned)((lane & 3) * 64 + (lane & 60))), "{v23}"((unsigned)(lg == (li >> 2) ? v16 * 128 + li * 8 + 67457024 : DROP)), "{v24}"((unsigned)(lane ? DROP : v16 * 8 + 67462912)), "{v26}"((unsigned)(bia0[0])), "{v27}"((unsigned)(bia0[1])), "{v28}"((unsigned)(bia0[2])), "{v29}"((unsigned)(bia0[3])), "{v30}"((unsigned)(bia1[0])), "{v31}"((unsigned)(bia1[1])), "{v32}"((unsigned)(bia1[2])), "{v33}"((unsigned)(bia1[3])), "{v34}"((unsigned)(0x3f803f80)), "{v35}"((unsigned)(0x3f803f80)), "{v36}"((unsigned)(0x3f803f80)), "{v37}"((unsigned)(0x3f803f80)), "{v38}"((unsigned)(sel0)), "{v39}"((unsigned)(sel1)), "{v40}"((unsigned)(sel0)), "{v41}"((unsigned)(sel1)), "{v44}"((unsigned)(vT + tsw)), "{v45}"((unsigned)(vT + (tsw ^ 8))), "{v46}"((unsigned)(vD + ((0 + rot) % 7) * 65536)), "{v47}"((unsigned)(vD + ((1 + rot) % 7) * 65536)), "{v48}"((unsigned)(vD + ((2 + rot) % 7) * 65536)), "{v49}"((unsigned)(vD + ((3 + rot) % 7) * 65536)), "{v50}"((unsigned)(vD + ((4 + rot) % 7) * 65536)), "{v51}"((unsigned)(vD + ((5 + rot) % 7) * 65536)), "{v52}"((unsigned)(vD + ((6 + rot) % 7) * 65536)), "{s[40:43]}"(rS), "{s[44:47]}"(rW), "{s[48:49]}"(wsp), "{s50}"((unsigned)(sA)), "{s51}"((unsigned)(sLo)), "{s52}"((unsigned)(sHi)), "{s53}"((unsigned)(sEx)), "{s54}"((unsigned)(sEnt)), "{s55}"((unsigned)(sLd)), "{s[56:57]}"(wsb + (sLo & 0x3ffffff)), "{s59}"((unsigned)(lds0 + wid * 18592)), "{s[62:63]}"(wsb), "{s[64:65]}"(0xAAAAAAAAAAAAAAAAull), "{s[66:67]}"(0xCCCCCCCCCCCCCCCCull), "{s[68:71]}"(rO), "{s[72:73]}"(ep), "{s[76:79]}"(rS), "{s80}"((unsigned)(201326592)), "{s81}"((unsigned)(0)), "{s82}"((unsigned)(0)), "{s83}"((unsigned)(0)), "{s86}"((unsigned)(sW0 + 64 * ((wid + 1) & 3))), "{s87}"((unsigned)(sW0 + 64 * ((wid + 2) & 3))), "{s88}"((unsigned)(sW0 + 64 * ((wid + 3) & 3))), "{s89}"((unsigned)(sW0 + 64 * ((wid + 4) & 3)))
      : "v255", "a255", "s95", "vcc", "scc", "memory");

    }
  }
}

extern "C" void kernel_launch(void* const* d_in, const int* in_sizes, int n_in, void* d_out, int out_size,
                              void* d_ws, size_t ws_size, hipStream_t stream) {
  Args m{};
  m.x = (const u16*)d_in[0]; m.wqkv = (const u16*)d_in[1]; m.wo = (const u16*)d_in[2]; m.w1 = (const u16*)d_in[4];
  m.b1 = (const u16*)d_in[5]; m.w2 = (const u16*)d_in[6]; m.b2 = (const u16*)d_in[7];
  m.out = (u16*)d_out; m.ws = (char*)d_ws;
  (void)hipFuncSetAttribute((const void*)stream_kernel, hipFuncAttributeMaxDynamicSharedMemorySize, LDS_BYTES);
  hipLaunchKernelGGL(stream_kernel, dim3(256), dim3(256), LDS_BYTES, stream, m);
}
```

```cpp
#include <hip/hip_runtime.h>
#include <stdint.h>
typedef unsigned short u16;
typedef unsigned long long u64;
typedef __attribute__((ext_vector_type(4))) unsigned u32x4;
constexpr unsigned F_X = 0;
constexpr unsigned F_ATT = 32768;
constexpr unsigned F_X1 = 65536;
constexpr unsigned F_H = 98304;
constexpr unsigned F_P = 212992;
constexpr unsigned F_ST1 = 344064;
constexpr unsigned F_ST2 = 348160;
constexpr unsigned F_G = 352256;
constexpr unsigned F_FA = 352512;
constexpr unsigned F_FX1 = 352768;
constexpr unsigned F_FH = 353024;
constexpr unsigned F_FX = 354048;
constexpr unsigned F_FP = 354304;
constexpr unsigned RS = 524288;
constexpr unsigned PADF = 256;
constexpr unsigned LOWB = 64;
constexpr unsigned L_TAB = 98304;
constexpr unsigned LDS_BYTES = 163840;
constexpr int Q_GL = 2;
constexpr int Q_EX = 0;
constexpr int Q_TL = 1;
constexpr int O_GL = 1;
constexpr int O_EX = 0;
constexpr int O_TL = 1;
constexpr int F_GL = 1;
constexpr int F_EX = 0;
constexpr int F_TL = 1;
constexpr int G_GL = 1;
constexpr int G_EX = 0;
constexpr int G_TL = 1;
constexpr int H_GL = 1;
constexpr int H_EX = 0;
constexpr int H_TL = 1;
constexpr int J_GL = 1;
constexpr int J_EX = 0;
constexpr int J_TL = 1;
#define MAPV 4
#define FDLY 0
#define PF 42
#define KR 1
#define XP 2
#define OR 1
#define JJ 1
#define DLO 0
#define DLF 450
#define DLG 0
#define SLP 12
#define TW 1
#define DLW 250
#define DLB 300
#define EC 1
#define DLQ 0
#define FR0 0
#define PQ 3
#define QW 6
#define NFL 48
#define PSTX 0
#define CP 1
#define FN 4
#define XF 1
#define P2 0
#define NE 16
#define DLX 300

constexpr unsigned DROP = 0x40000000u, WS0 = 1u << 20;
static_assert(WS0 + (PADF + 256 - LOWB) * RS <= (256u << 20), "workspace");
struct Args { const u16 *x, *wqkv, *wo, *w1, *b1, *w2, *b2; u16* out; char* ws;
};
__device__ __forceinline__ u32x4 desc(const void* p, unsigned n) { const u64 a = (u64)p; return (u32x4){(unsigned)a, (unsigned)(a >> 32) & 0xffffu, n, 0x00020000u}; }
__device__ __forceinline__ float lo16(unsigned v) { return __builtin_bit_cast(float, v << 16); }
__device__ __forceinline__ float hi16(unsigned v) { return __builtin_bit_cast(float, v & 0xffff0000u); }

__host__ __device__ inline void decode(int bid, int& role, int& set, int& idx) {
  const int xg = bid & 7, xj = bid >> 3;
  if (xg < 4) {
    if (xj < 16) { role = 0; set = xj & 1; idx = MAPV == 1 || MAPV == 4 ? 8 * xg + (xj >> 1) : MAPV == 2 ? 4 * (xj >> 1) + xg : 4 * (xj >> 1) + ((xg + 2 * set) & 3); }
    else if (MAPV == 4 && SLP && xj >= 30) { role = 2; set = 0; idx = (int)(0x1915110e0d090501ull >> (16 * xg + 8 * (xj & 1))) & 31; }
    else { role = 2; set = xj < 23 ? 1 : xj < 30 ? 2 : 0; idx = 7 * xg + (xj - 16) % 7; }
  } else if (xj < 21) { role = 3; set = xj / 7; idx = (xg - 4) * 7 + xj % 7; }
  else {
    const int k = xj - 21, no = xg == 4 ? 11 : xg == 5 ? 5 : 4;
    if (k < no) { role = 1; set = xg == 4 ? k >> 3 : xg == 5 ? 1 : 2; idx = xg == 4 ? k & 7 : xg == 5 ? 3 + k : (xg - 6) * 4 + k; }
    else if (SLP) { role = 2; set = 0; idx = (int)((xg == 5 ? 0x181716100f0000ull : xg == 6 ? 0x1b1a0a08070302ull : 0x14131206040c0bull) >> (8 * (k - 4))) & 31; }
    else { role = 2; set = 0; idx = (int)((xg == 5 ? 0x19181711100200ull : xg == 6 ? 0x1b1a0b0a090403ull : 0x14131206050d0cull) >> (8 * (k - 4))) & 31; }
  }
}

__global__ __launch_bounds__(256, 1) void stream_kernel(Args a) {
  extern __shared__ __attribute__((aligned(16))) char smem[];
  const int tid = threadIdx.x, lane = tid & 63, wid = __builtin_amdgcn_readfirstlane(tid >> 6), li = lane & 15, lg = lane >> 4;
  const unsigned lds0 = (unsigned)(size_t)smem;
  const u64 tz = __builtin_amdgcn_s_memrealtime();
  const int bp = (blockIdx.x + 2 * (XP - 1)) & 7, bid = (blockIdx.x & ~7) | ((bp & 1) << 2) | (bp >> 1);
  {
    u64 pc; unsigned len;
    asm volatile("s_getpc_b64 %0\n.Lc0:\n s_mov_b32 %1, .LO_end-.Lc0\n s_max_u32 %1, %1, .LH_end-.Lc0\n s_max_u32 %1, %1, .LJ_end-.Lc0\n s_max_u32 %1, %1, .LG_end-.Lc0" : "=s"(pc), "=s"(len)::"scc");
    asm volatile("global_load_dword v252, %0, %1" ::"v"(min(((blockIdx.x >> 3) * 256 + tid) * 16, len)), "s"(pc) : "v252");
  }
  const int pi = (li >> 2) + 4 * (li & 3), xj = bid >> 3, xo = (bid & 7) - 4;
  auto two = [&]() __attribute__((always_inline)) {
    const int hf = xo > 1;
#pragma unroll
    for (int i = 0; i < 191 / (84 - NE) + 1; ++i) {
      const int r0 = min(((i * (84 - NE) + xj * 4 + wid - NE) * 64 + lane) >> (4 - hf), 767);
      asm volatile("global_load_dword v252, %0, %1" ::"v"(((r0 & 3) * 256 + 64 + (r0 >> 2)) * 2048 + (hf ? (lane & 7) + (xo & 1) * 8 : xo ? max(li, 6) : li) * 128), "s"(a.wo) : "v252", "memory");
    }
  };
  auto tw2 = [&]() __attribute__((always_inline)) {
#pragma unroll
    for (int i = 0; i < 3; ++i)
      if (i * 21 + xj < 56) asm volatile("global_load_dword v252, %0, %1" ::"v"((xo * 896 + wid * 224 + (i * 21 + xj) * 4 + lg) * 2048 + li * 128), "s"(a.w2) : "v252", "memory");
  };
  auto conv = [&](int pr, bool early) __attribute__((always_inline)) {
    const __amdgpu_buffer_rsrc_t rb = __builtin_amdgcn_make_buffer_rsrc(a.ws + WS0 - LOWB * RS, 0, (PADF + 256) * RS, 0x00020000);
    const u32x4* src = (const u32x4*)(a.x + ((pr >> 1) * 32 + 2 * pi + (pr & 1)) * 1024 + wid * 256 + lg * 8);
    const u64 ev = __hip_atomic_load((const u64*)a.ws, __ATOMIC_RELAXED, __HIP_MEMORY_SCOPE_AGENT);
    u32x4 v[8];
#pragma unroll
    for (int k = 0; k < 8; ++k) v[k] = src[k * 4];
    u32x4 pf = {};
    if (early && (EC & 2)) asm volatile("global_load_dword v252, %0, %1 sc1" ::"v"(WS0 + (PADF - LOWB + pr) * RS + (lane & 1) * F_G + wid * 8192 * !(lane & 1)), "s"(a.ws) : "v252");
    if (!early && pr >= NE && pr < (MAPV == 4 ? 84 : 104) && (PF & 50) == 2) {
      const int hf = MAPV == 4 ? xo > 1 : xo, g = xj * 4 + wid - NE, n = MAPV == 4 ? (hf ? 767 / (84 - NE) + 1 : 1535 / (84 - NE) + 1) : hf ? 767 / (112 - NE) + 1 : 1535 / (80 - NE) + 1;
      for (int i = 0; i < n; ++i) {
        const int u = g * n + i, r0 = hf ? u : u >> 1, row = r0 < 768 ? (r0 / 192) * 256 + 64 + r0 % 192 : 0;
        asm volatile("global_load_dwordx4 %0, %1, off" : "+v"(pf) : "v"((const char*)a.wo + row * 2048 + (hf ? xo == (MAPV == 4 ? 3 : 2) : u & 1) * 1024 + lane * 16));
      }
    }
    float s = 0.f, q = 0.f;
#pragma unroll
    for (int k = 0; k < 8; ++k) __builtin_amdgcn_raw_buffer_store_b128(v[k], rb, lane * 16 + k * 1024, (PADF + pr) * RS + F_X + wid * 8192, 16);
    if (!early && pr >= NE && pr < 84 && (PF & 16)) two();
    __builtin_amdgcn_sched_barrier(0);
#pragma unroll
    for (int k = 0; k < 8; ++k) {
#pragma unroll
      for (int d = 0; d < 4; ++d) { const float l = lo16(v[k][d]), h = hi16(v[k][d]); s += l + h; q = fmaf(l, l, fmaf(h, h, q)); }
    }
    s += __shfl_xor(s, 16); q += __shfl_xor(q, 16); s += __shfl_xor(s, 32); q += __shfl_xor(q, 32);
    float2* t = (float2*)smem;
    if (lg == 0) t[wid * 16 + li] = make_float2(s, q);
    if (!early && pr >= NE && pr < 84 && (PF & 16)) {
      asm volatile("s_waitcnt vmcnt(%0)" ::"n"(191 / (84 - NE) + 1) : "memory");
    } else asm volatile("s_waitcnt vmcnt(0)" : "+v"(pf)::"memory");
    __syncthreads();
    if (tid < 16) {
      const float2 t0 = t[li], t1 = t[16 + li], t2 = t[32 + li], t3 = t[48 + li];
      const float nm = ((t0.x + t1.x) + (t2.x + t3.x)) * -0x1p-10f;
      const float var = fmaxf(fmaf(-nm, nm, fmaf((t0.y + t1.y) + (t2.y + t3.y), 0x1p-10f, 1e-5f)), 1e-5f);
      const u32x4 g = {__builtin_bit_cast(unsigned, __builtin_amdgcn_rsqf(var)), __builtin_bit_cast(unsigned, nm), ((unsigned)ev + 2u) | 1u, (unsigned)(ev >> 32) + 0x9E3779B9u};
      __builtin_amdgcn_raw_buffer_store_b128(g, rb, li * 16, (PADF + pr) * RS + F_G, 16);
    }
    __syncthreads();
  };
  if (!(bid & 4)) {
    const unsigned o = (wid * 256 + (lane >> 2)) * 6144 + (bid & 7) * 512 + (lane & 3) * 128;
#pragma unroll
    for (int i = 0; i < PQ - 1; ++i) {
      const int j = (bid >> 3) + (PQ == 3 ? i * 16 : i * 16 - 16);
      if ((PQ == 3 && !i) || bid >= 128) asm volatile("global_load_dword v252, %0, %1" ::"v"(o + j / 3 * 16 * 6144 + j % 3 * 2048), "s"(a.wqkv) : "v252");
    }
  }
  int xe = xo;
  asm volatile("" : "+s"(xe));
  if (EC && xe >= 0 && xj < NE / 4) conv(xj * 4 + xo, true);
  if (FN == 4) __builtin_amdgcn_s_barrier();
  int role, set, idx;
  decode(bid, role, set, idx);
  const u64 wsp = (u64)a.ws, wsb = wsp + WS0 - LOWB * RS;
  const u32x4 rS = desc(a.ws + WS0 - LOWB * RS, (PADF + 256) * RS);
  if (role == 2 && xo < 0 && (PF & 1)) {
    const int g = (xj - 16) * 4 + wid;
    const char* p = (const char*)a.wqkv + (((g >> 2) * 2 + 1) * 32 + (g & 3) * 8 + (lane >> 5)) * 6144 + (bid & 7) * 512 + (lane & 31) * 16;
    u32x4 t[12];
    unsigned fl = 0;
    if (PF & 4) fl = __hip_atomic_load((const unsigned*)(a.ws + WS0 + (size_t)(PADF - LOWB + g * 4 + (bid & 7)) * RS + F_ST2 + (lane < 56 ? lane : 0) * 128), __ATOMIC_RELAXED, __HIP_MEMORY_SCOPE_AGENT);
#pragma unroll
    for (int i = 0; i < 12; ++i) t[i] = *(const u32x4*)(p + (i & 3) * 12288 + (i >> 2) * 2048);
#pragma unroll
    for (int i = 0; i < 12; ++i) asm volatile("" ::"v"(t[i]));
    asm volatile("" ::"v"(fl));
  }
  if (role == 2 && xo < 0)
    asm volatile("global_load_dword v252, %0, %1 sc1" ::"v"(WS0 + (PADF - LOWB + (xj * 4 + wid - 64) * 4 + (bid & 7)) * RS + F_ST2 + (lane < 16 ? lane : lane < NFL ? lane + 16 : 0) * 128), "s"(a.ws) : "v252");
  if (role == 2 && xo < 0 && (SLP & 1)) {
    const unsigned c0 = 14 * (xo + 4), c = c0 + ((3 - c0 - (unsigned)((u64)a.w1 >> 7)) & 7) + (lane & 2) * 4;
    asm volatile("global_load_dword v252, %0, %1" ::"v"(((xj * 4 + wid - 64) * 16 + (lane >> 2)) * 7168 + (c < c0 + 14 ? c : c0) * 128 + (lane & 1) * 64), "s"(a.w1) : "v252");
  }
  if (role == 3 && xj * 4 + wid < 56 && (SLP & 2))
    asm volatile("global_load_dword v252, %0, %1" ::"v"((xo * 896 + (xj * 4 + wid) * 16 + (lane >> 2)) * 2048 + (((3 - (unsigned)((u64)a.w2 >> 7)) & 7) + (lane & 2) * 4) * 128 + (lane & 1) * 64), "s"(a.w2) : "v252");
  if (role == 0 || FN == 2 || (FN == 3 && role == 2)) asm volatile("s_waitcnt vmcnt(%0)" ::"n"(QW));
  if (role == 0 && DLQ) while (__builtin_amdgcn_s_memrealtime() - tz < DLQ) __builtin_amdgcn_s_sleep(1);
  if (FN == 3) __builtin_amdgcn_s_barrier();
  if (xo < 0 && (PF & 8) && !FN) {
    const int g = xj * 4 + wid;
    const char* p = (const char*)a.wqkv + (g * 8 + (lane >> 5)) * 6144 + (bid & 7) * 512 + (lane & 31) * 16;
    u32x4 t[12];
#pragma unroll
    for (int i = 0; i < 12; ++i) t[i] = *(const u32x4*)(p + (i & 3) * 12288 + (i >> 2) * 2048);
    unsigned fl = 0;
    if (role == 2) fl = __hip_atomic_load((const unsigned*)(a.ws + WS0 + (size_t)(PADF - LOWB + (g - 64) * 4 + (bid & 7)) * RS + F_ST2 + (lane < 56 ? lane : 0) * 128), __ATOMIC_RELAXED, __HIP_MEMORY_SCOPE_AGENT);
#pragma unroll
    for (int i = 0; i < 12; ++i) asm volatile("" ::"v"(t[i]));
    asm volatile("" ::"v"(fl));
  }
  if (role == 2 && xo < 0) for (int i = 0; i < FDLY; ++i) __builtin_amdgcn_s_sleep(16);
  const int p2 = xj < 4 ? 84 + xj * 4 + xo : P2 ? (xj % 7 < 4 ? 84 + (set * 4 + xj % 7) * 4 + xo : 128) : xj % 7 > 3 ? 84 + (set * 3 + xj % 7) * 4 + xo : 128;
  if (role == 3 && xj >= NE / 4 && (PF & 32)) {
    while (__builtin_amdgcn_s_memrealtime() - tz < DLW) __builtin_amdgcn_s_sleep(2);
    two();
  }
  for (int pr = role < 3 ? 128 : EC && xj < NE / 4 ? p2 : xj * 4 + xo; pr < 128; pr = pr < 84 ? p2 : 128) {
    if (pr >= NE) while (__builtin_amdgcn_s_memrealtime() - tz < DLX) __builtin_amdgcn_s_sleep(4);
    if (pr >= 84 && (SLP & 32)) {
      while (__builtin_amdgcn_s_memrealtime() - tz < DLG) __builtin_amdgcn_s_sleep(4);
      tw2();
    }
    conv(pr, false);
  }
  const unsigned sLo = (PADF + set) * RS, sLd = lds0 + wid * 40960, vT = sLd + (8 * lg + (li >> 2)) * 64 + (li & 3) * 16;
  const unsigned trp = (li & 3) * 4608 + (li & 1) * 32 + (li & 2) * 64 + (li >> 2) * 64 + lg * 16;
  if (role == 0) {
    const unsigned sA = sLo - 2 * RS, sHi = sLo + 127 * 2 * RS, sEx = sHi + Q_EX * 2 * RS, sEnt = -wid & 3, tsw = 0, rot = KR * !PQ * (set + 2 * (idx & 1));
    const unsigned sW0 = wid * 256 * 6144 + idx * 64, vD = (lane >> 2) * 6144 + (lane & 3) * 16;
    const u32x4 rW = desc(a.wqkv, 1024 * 6144);
    auto ok = [&](int r, int prev) { const int k = lg + 4 * r; return prev ? k >= pi + 9 : (k <= pi && k >= pi - 7); };
    auto qmask = [&](int r, int prev) { return ok(r, prev) ? 0u : 0xff800000u; };
    auto pmask = [&](int d) { return (ok(2 * (d & 1), d >> 1) ? 0xffffu : 0u) | (ok(2 * (d & 1) + 1, d >> 1) ? 0xffff0000u : 0u); };
    asm volatile(
"s_nop 4\n"
"v_mov_b32 v186, 0\n"
"global_load_dwordx2 v[0:1], v186, s[48:49] sc1\n"
"v_mov_b32 v188, 0x3f803f80\n"
"v_mov_b32 v189, 0x3f803f80\n"
"v_mov_b32 v190, 0x3f803f80\n"
"v_mov_b32 v191, 0x3f803f80\n"
"s_add_u32 m0, s55, 0\n"
"s_add_u32 s60, s63, 0\n"
"buffer_load_dwordx4 v53, s[44:47], s60 offen lds\n"
"s_add_u32 m0, s55, 1024\n"
"s_add_u32 s60, s63, 98304\n"
"buffer_load_dwordx4 v53, s[44:47], s60 offen lds\n"
"s_add_u32 m0, s55, 2048\n"
"s_add_u32 s60, s64, 0\n"
"buffer_load_dwordx4 v53, s[44:47], s60 offen lds\n"
"s_add_u32 m0, s55, 3072\n"
"s_add_u32 s60, s64, 98304\n"
"buffer_load_dwordx4 v53, s[44:47], s60 offen lds\n"
"s_add_u32 m0, s55, 4096\n"
"s_add_u32 s60, s65, 0\n"
"buffer_load_dwordx4 v53, s[44:47], s60 offen lds\n"
"s_add_u32 m0, s55, 5120\n"
"s_add_u32 s60, s65, 98304\n"
"buffer_load_dwordx4 v53, s[44:47], s60 offen lds\n"
"s_add_u32 m0, s55, 6144\n"
"s_add_u32 s60, s63, 196608\n"
"buffer_load_dwordx4 v53, s[44:47], s60 offen lds\n"
"s_add_u32 m0, s55, 7168\n"
"s_add_u32 s60, s63, 294912\n"
"buffer_load_dwordx4 v53, s[44:47], s60 offen lds\n"
"s_add_u32 m0, s55, 8192\n"
"s_add_u32 s60, s64, 196608\n"
"buffer_load_dwordx4 v53, s[44:47], s60 offen lds\n"
"s_add_u32 m0, s55, 9216\n"
"s_add_u32 s60, s64, 294912\n"
"buffer_load_dwordx4 v53, s[44:47], s60 offen lds\n"
"s_add_u32 m0, s55, 10240\n"
"s_add_u32 s60, s65, 196608\n"
"buffer_load_dwordx4 v53, s[44:47], s60 offen lds\n"
"s_add_u32 m0, s55, 11264\n"
"s_add_u32 s60, s65, 294912\n"
"buffer_load_dwordx4 v53, s[44:47], s60 offen lds\n"
"s_add_u32 m0, s55, 12288\n"
"s_add_u32 s60, s63, 0\n"
"buffer_load_dwordx4 v54, s[44:47], s60 offen lds\n"
"s_add_u32 m0, s55, 13312\n"
"s_add_u32 s60, s63, 98304\n"
"buffer_load_dwordx4 v54, s[44:47], s60 offen lds\n"
"s_add_u32 m0, s55, 14336\n"
"s_add_u32 s60, s64, 0\n"
"buffer_load_dwordx4 v54, s[44:47], s60 offen lds\n"
"s_add_u32 m0, s55, 15360\n"
"s_add_u32 s60, s64, 98304\n"
"buffer_load_dwordx4 v54, s[44:47], s60 offen lds\n"
"s_add_u32 m0, s55, 16384\n"
"s_add_u32 s60, s65, 0\n"
"buffer_load_dwordx4 v54, s[44:47], s60 offen lds\n"
"s_add_u32 m0, s55, 17408\n"
"s_add_u32 s60, s65, 98304\n"
"buffer_load_dwordx4 v54, s[44:47], s60 offen lds\n"
"s_add_u32 m0, s55, 18432\n"
"s_add_u32 s60, s63, 196608\n"
"buffer_load_dwordx4 v54, s[44:47], s60 offen lds\n"
"s_add_u32 m0, s55, 19456\n"
"s_add_u32 s60, s63, 294912\n"
"buffer_load_dwordx4 v54, s[44:47], s60 offen lds\n"
"s_add_u32 m0, s55, 20480\n"
"s_add_u32 s60, s64, 196608\n"
"buffer_load_dwordx4 v54, s[44:47], s60 offen lds\n"
"s_add_u32 m0, s55, 21504\n"
"s_add_u32 s60, s64, 294912\n"
"buffer_load_dwordx4 v54, s[44:47], s60 offen lds\n"
"s_add_u32 m0, s55, 22528\n"
"s_add_u32 s60, s65, 196608\n"
"buffer_load_dwordx4 v54, s[44:47], s60 offen lds\n"
"s_add_u32 m0, s55, 23552\n"
"s_add_u32 s60, s65, 294912\n"
"buffer_load_dwordx4 v54, s[44:47], s60 offen lds\n"
"s_add_u32 m0, s55, 24576\n"
"s_add_u32 s60, s63, 0\n"
"buffer_load_dwordx4 v55, s[44:47], s60 offen lds\n"
"s_add_u32 m0, s55, 25600\n"
"s_add_u32 s60, s63, 98304\n"
"buffer_load_dwordx4 v55, s[44:47], s60 offen lds\n"
"s_add_u32 m0, s55, 26624\n"
"s_add_u32 s60, s64, 0\n"
"buffer_load_dwordx4 v55, s[44:47], s60 offen lds\n"
"s_add_u32 m0, s55, 27648\n"
"s_add_u32 s60, s64, 98304\n"
"buffer_load_dwordx4 v55, s[44:47], s60 offen lds\n"
"s_add_u32 m0, s55, 28672\n"
"s_add_u32 s60, s65, 0\n"
"buffer_load_dwordx4 v55, s[44:47], s60 offen lds\n"
"s_add_u32 m0, s55, 29696\n"
"s_add_u32 s60, s65, 98304\n"
"buffer_load_dwordx4 v55, s[44:47], s60 offen lds\n"
"s_add_u32 m0, s55, 30720\n"
"s_add_u32 s60, s63, 196608\n"
"buffer_load_dwordx4 v55, s[44:47], s60 offen lds\n"
"s_add_u32 m0, s55, 31744\n"
"s_add_u32 s60, s63, 294912\n"
"buffer_load_dwordx4 v55, s[44:47], s60 offen lds\n"
"s_add_u32 m0, s55, 32768\n"
"s_add_u32 s60, s64, 196608\n"
"buffer_load_dwordx4 v55, s[44:47], s60 offen lds\n"
"s_add_u32 m0, s55, 33792\n"
"s_add_u32 s60, s64, 294912\n"
"buffer_load_dwordx4 v55, s[44:47], s60 offen lds\n"
"s_add_u32 m0, s55, 34816\n"
"s_add_u32 s60, s65, 196608\n"
"buffer_load_dwordx4 v55, s[44:47], s60 offen lds\n"
"s_add_u32 m0, s55, 35840\n"
"s_add_u32 s60, s65, 294912\n"
"buffer_load_dwordx4 v55, s[44:47], s60 offen lds\n"
"s_add_u32 m0, s55, 36864\n"
"s_add_u32 s60, s63, 0\n"
"buffer_load_dwordx4 v56, s[44:47], s60 offen lds\n"
"s_add_u32 m0, s55, 37888\n"
"s_add_u32 s60, s63, 98304\n"
"buffer_load_dwordx4 v56, s[44:47], s60 offen lds\n"
"s_add_u32 m0, s55, 38912\n"
"s_add_u32 s60, s64, 0\n"
"buffer_load_dwordx4 v56, s[44:47], s60 offen lds\n"
"s_add_u32 m0, s55, 39936\n"
"s_add_u32 s60, s64, 98304\n"
"buffer_load_dwordx4 v56, s[44:47], s60 offen lds\n"
"s_waitcnt vmcnt(38)\n"
"ds_read_b64_tr_b16 a[0:1], v48 offset:0\n"
"ds_read_b64_tr_b16 a[2:3], v48 offset:256\n"
"ds_read_b64_tr_b16 a[32:33], v49 offset:0\n"
"ds_read_b64_tr_b16 a[34:35], v49 offset:256\n"
"v_add_u32 v0, 2, v0\n"
"v_add_u32 v1, 0x9e3779b9, v1\n"
"v_or_b32 v0, 1, v0\n"
"v_readfirstlane_b32 s49, v1\n"
"v_readfirstlane_b32 s48, v0\n"
"s_waitcnt vmcnt(36)\n"
"ds_read_b64_tr_b16 a[64:65], v48 offset:2048\n"
"ds_read_b64_tr_b16 a[66:67], v48 offset:2304\n"
"ds_read_b64_tr_b16 a[96:97], v49 offset:2048\n"
"ds_read_b64_tr_b16 a[98:99], v49 offset:2304\n"
"s_waitcnt lgkmcnt(4)\n"
"s_add_u32 m0, s55, 0\n"
"s_add_u32 s60, s65, 0\n"
"buffer_load_dwordx4 v56, s[44:47], s60 offen lds\n"
"s_add_u32 m0, s55, 1024\n"
"s_add_u32 s60, s65, 98304\n"
"buffer_load_dwordx4 v56, s[44:47], s60 offen lds\n"
"v_mfma_f32_16x16x32_bf16 v[192:195], v[188:191], a[0:3], 0\n"
"v_mfma_f32_16x16x32_bf16 v[196:199], v[188:191], a[32:35], 0\n"
"s_waitcnt vmcnt(36)\n"
"ds_read_b64_tr_b16 a[128:129], v48 offset:4096\n"
"ds_read_b64_tr_b16 a[130:131], v48 offset:4352\n"
"ds_read_b64_tr_b16 a[160:161], v49 offset:4096\n"
"ds_read_b64_tr_b16 a[162:163], v49 offset:4352\n"
"s_waitcnt lgkmcnt(4)\n"
"s_add_u32 m0, s55, 2048\n"
"s_add_u32 s60, s63, 196608\n"
"buffer_load_dwordx4 v56, s[44:47], s60 offen lds\n"
"s_add_u32 m0, s55, 3072\n"
"s_add_u32 s60, s63, 294912\n"
"buffer_load_dwordx4 v56, s[44:47], s60 offen lds\n"
"v_mfma_f32_16x16x32_bf16 v[200:203], v[188:191], a[64:67], 0\n"
"v_mfma_f32_16x16x32_bf16 v[204:207], v[188:191], a[96:99], 0\n"
"s_waitcnt vmcnt(36)\n"
"ds_read_b64_tr_b16 a[4:5], v48 offset:6144\n"
"ds_read_b64_tr_b16 a[6:7], v48 offset:6400\n"
"ds_read_b64_tr_b16 a[36:37], v49 offset:6144\n"
"ds_read_b64_tr_b16 a[38:39], v49 offset:6400\n"
"s_waitcnt lgkmcnt(4)\n"
"s_add_u32 m0, s55, 4096\n"
"s_add_u32 s60, s64, 196608\n"
"buffer_load_dwordx4 v56, s[44:47], s60 offen lds\n"
"s_add_u32 m0, s55, 5120\n"
"s_add_u32 s60, s64, 294912\n"
"buffer_load_dwordx4 v56, s[44:47], s60 offen lds\n"
"v_mfma_f32_16x16x32_bf16 v[208:211], a[128:131], v[188:191], 0\n"
"v_mfma_f32_16x16x32_bf16 v[212:215], a[160:163], v[188:191], 0\n"
"s_waitcnt vmcnt(36)\n"
"ds_read_b64_tr_b16 a[68:69], v48 offset:8192\n"
"ds_read_b64_tr_b16 a[70:71], v48 offset:8448\n"
"ds_read_b64_tr_b16 a[100:101], v49 offset:8192\n"
"ds_read_b64_tr_b16 a[102:103], v49 offset:8448\n"
"s_waitcnt lgkmcnt(4)\n"
"s_add_u32 m0, s55, 6144\n"
"s_add_u32 s60, s65, 196608\n"
"buffer_load_dwordx4 v56, s[44:47], s60 offen lds\n"
"s_add_u32 m0, s55, 7168\n"
"s_add_u32 s60, s65, 294912\n"
"buffer_load_dwordx4 v56, s[44:47], s60 offen lds\n"
"v_mfma_f32_16x16x32_bf16 v[192:195], v[188:191], a[4:7], v[192:195]\n"
"v_mfma_f32_16x16x32_bf16 v[196:199], v[188:191], a[36:39], v[196:199]\n"
"s_waitcnt vmcnt(36)\n"
"ds_read_b64_tr_b16 a[132:133], v48 offset:10240\n"
"ds_read_b64_tr_b16 a[134:135], v48 offset:10496\n"
"ds_read_b64_tr_b16 a[164:165], v49 offset:10240\n"
"ds_read_b64_tr_b16 a[166:167], v49 offset:10496\n"
"s_waitcnt lgkmcnt(4)\n"
"v_mfma_f32_16x16x32_bf16 v[200:203], v[188:191], a[68:71], v[200:203]\n"
"v_mfma_f32_16x16x32_bf16 v[204:207], v[188:191], a[100:103], v[204:207]\n"
"s_waitcnt vmcnt(34)\n"
"ds_read_b64_tr_b16 a[8:9], v48 offset:12288\n"
"ds_read_b64_tr_b16 a[10:11], v48 offset:12544\n"
"ds_read_b64_tr_b16 a[40:41], v49 offset:12288\n"
"ds_read_b64_tr_b16 a[42:43], v49 offset:12544\n"
"s_waitcnt lgkmcnt(4)\n"
"v_mfma_f32_16x16x32_bf16 v[208:211], a[132:135], v[188:191], v[208:211]\n"
"v_mfma_f32_16x16x32_bf16 v[212:215], a[164:167], v[188:191], v[212:215]\n"
"s_waitcnt vmcnt(32)\n"
"ds_read_b64_tr_b16 a[72:73], v48 offset:14336\n"
"ds_read_b64_tr_b16 a[74:75], v48 offset:14592\n"
"ds_read_b64_tr_b16 a[104:105], v49 offset:14336\n"
"ds_read_b64_tr_b16 a[106:107], v49 offset:14592\n"
"s_waitcnt lgkmcnt(4)\n"
"v_mfma_f32_16x16x32_bf16 v[192:195], v[188:191], a[8:11], v[192:195]\n"
"v_mfma_f32_16x16x32_bf16 v[196:199], v[188:191], a[40:43], v[196:199]\n"
"s_waitcnt vmcnt(30)\n"
"ds_read_b64_tr_b16 a[136:137], v48 offset:16384\n"
"ds_read_b64_tr_b16 a[138:139], v48 offset:16640\n"
"ds_read_b64_tr_b16 a[168:169], v49 offset:16384\n"
"ds_read_b64_tr_b16 a[170:171], v49 offset:16640\n"
"s_waitcnt lgkmcnt(4)\n"
"v_mfma_f32_16x16x32_bf16 v[200:203], v[188:191], a[72:75], v[200:203]\n"
"v_mfma_f32_16x16x32_bf16 v[204:207], v[188:191], a[104:107], v[204:207]\n"
"s_waitcnt vmcnt(28)\n"
"ds_read_b64_tr_b16 a[12:13], v48 offset:18432\n"
"ds_read_b64_tr_b16 a[14:15], v48 offset:18688\n"
"ds_read_b64_tr_b16 a[44:45], v49 offset:18432\n"
"ds_read_b64_tr_b16 a[46:47], v49 offset:18688\n"
"s_waitcnt lgkmcnt(4)\n"
"v_mfma_f32_16x16x32_bf16 v[208:211], a[136:139], v[188:191], v[208:211]\n"
"v_mfma_f32_16x16x32_bf16 v[212:215], a[168:171], v[188:191], v[212:215]\n"
"s_waitcnt vmcnt(26)\n"
"ds_read_b64_tr_b16 a[76:77], v48 offset:20480\n"
"ds_read_b64_tr_b16 a[78:79], v48 offset:20736\n"
"ds_read_b64_tr_b16 a[108:109], v49 offset:20480\n"
"ds_read_b64_tr_b16 a[110:111], v49 offset:20736\n"
"s_waitcnt lgkmcnt(4)\n"
"v_mfma_f32_16x16x32_bf16 v[192:195], v[188:191], a[12:15], v[192:195]\n"
"v_mfma_f32_16x16x32_bf16 v[196:199], v[188:191], a[44:47], v[196:199]\n"
"s_waitcnt vmcnt(24)\n"
"ds_read_b64_tr_b16 a[140:141], v48 offset:22528\n"
"ds_read_b64_tr_b16 a[142:143], v48 offset:22784\n"
"ds_read_b64_tr_b16 a[172:173], v49 offset:22528\n"
"ds_read_b64_tr_b16 a[174:175], v49 offset:22784\n"
"s_waitcnt lgkmcnt(4)\n"
"v_mfma_f32_16x16x32_bf16 v[200:203], v[188:191], a[76:79], v[200:203]\n"
"v_mfma_f32_16x16x32_bf16 v[204:207], v[188:191], a[108:111], v[204:207]\n"
"s_waitcnt vmcnt(22)\n"
"ds_read_b64_tr_b16 a[16:17], v48 offset:24576\n"
"ds_read_b64_tr_b16 a[18:19], v48 offset:24832\n"
"ds_read_b64_tr_b16 a[48:49], v49 offset:24576\n"
"ds_read_b64_tr_b16 a[50:51], v49 offset:24832\n"
"s_waitcnt lgkmcnt(4)\n"
"v_mfma_f32_16x16x32_bf16 v[208:211], a[140:143], v[188:191], v[208:211]\n"
"v_mfma_f32_16x16x32_bf16 v[212:215], a[172:175], v[188:191], v[212:215]\n"
"s_waitcnt vmcnt(20)\n"
"ds_read_b64_tr_b16 a[80:81], v48 offset:26624\n"
"ds_read_b64_tr_b16 a[82:83], v48 offset:26880\n"
"ds_read_b64_tr_b16 a[112:113], v49 offset:26624\n"
"ds_read_b64_tr_b16 a[114:115], v49 offset:26880\n"
"s_waitcnt lgkmcnt(4)\n"
"v_mfma_f32_16x16x32_bf16 v[192:195], v[188:191], a[16:19], v[192:195]\n"
"v_mfma_f32_16x16x32_bf16 v[196:199], v[188:191], a[48:51], v[196:199]\n"
"s_waitcnt vmcnt(18)\n"
"ds_read_b64_tr_b16 a[144:145], v48 offset:28672\n"
"ds_read_b64_tr_b16 a[146:147], v48 offset:28928\n"
"ds_read_b64_tr_b16 a[176:177], v49 offset:28672\n"
"ds_read_b64_tr_b16 a[178:179], v49 offset:28928\n"
"s_waitcnt lgkmcnt(4)\n"
"v_mfma_f32_16x16x32_bf16 v[200:203], v[188:191], a[80:83], v[200:203]\n"
"v_mfma_f32_16x16x32_bf16 v[204:207], v[188:191], a[112:115], v[204:207]\n"
"s_waitcnt vmcnt(16)\n"
"ds_read_b64_tr_b16 a[20:21], v48 offset:30720\n"
"ds_read_b64_tr_b16 a[22:23], v48 offset:30976\n"
"ds_read_b64_tr_b16 a[52:53], v49 offset:30720\n"
"ds_read_b64_tr_b16 a[54:55], v49 offset:30976\n"
"s_waitcnt lgkmcnt(4)\n"
"v_mfma_f32_16x16x32_bf16 v[208:211], a[144:147], v[188:191], v[208:211]\n"
"v_mfma_f32_16x16x32_bf16 v[212:215], a[176:179], v[188:191], v[212:215]\n"
"s_waitcnt vmcnt(14)\n"
"ds_read_b64_tr_b16 a[84:85], v48 offset:32768\n"
"ds_read_b64_tr_b16 a[86:87], v48 offset:33024\n"
"ds_read_b64_tr_b16 a[116:117], v49 offset:32768\n"
"ds_read_b64_tr_b16 a[118:119], v49 offset:33024\n"
"s_waitcnt lgkmcnt(4)\n"
"v_mfma_f32_16x16x32_bf16 v[192:195], v[188:191], a[20:23], v[192:195]\n"
"v_mfma_f32_16x16x32_bf16 v[196:199], v[188:191], a[52:55], v[196:199]\n"
"s_waitcnt vmcnt(12)\n"
"ds_read_b64_tr_b16 a[148:149], v48 offset:34816\n"
"ds_read_b64_tr_b16 a[150:151], v48 offset:35072\n"
"ds_read_b64_tr_b16 a[180:181], v49 offset:34816\n"
"ds_read_b64_tr_b16 a[182:183], v49 offset:35072\n"
"s_waitcnt lgkmcnt(4)\n"
"v_mfma_f32_16x16x32_bf16 v[200:203], v[188:191], a[84:87], v[200:203]\n"
"v_mfma_f32_16x16x32_bf16 v[204:207], v[188:191], a[116:119], v[204:207]\n"
"s_waitcnt vmcnt(10)\n"
"ds_read_b64_tr_b16 a[24:25], v48 offset:36864\n"
"ds_read_b64_tr_b16 a[26:27], v48 offset:37120\n"
"ds_read_b64_tr_b16 a[56:57], v49 offset:36864\n"
"ds_read_b64_tr_b16 a[58:59], v49 offset:37120\n"
"s_waitcnt lgkmcnt(4)\n"
"v_mfma_f32_16x16x32_bf16 v[208:211], a[148:151], v[188:191], v[208:211]\n"
"v_mfma_f32_16x16x32_bf16 v[212:215], a[180:183], v[188:191], v[212:215]\n"
"s_waitcnt vmcnt(8)\n"
"ds_read_b64_tr_b16 a[88:89], v48 offset:38912\n"
"ds_read_b64_tr_b16 a[90:91], v48 offset:39168\n"
"ds_read_b64_tr_b16 a[120:121], v49 offset:38912\n"
"ds_read_b64_tr_b16 a[122:123], v49 offset:39168\n"
"s_waitcnt lgkmcnt(4)\n"
"v_mfma_f32_16x16x32_bf16 v[192:195], v[188:191], a[24:27], v[192:195]\n"
"v_mfma_f32_16x16x32_bf16 v[196:199], v[188:191], a[56:59], v[196:199]\n"
"buffer_load_dwordx4 v[240:243], v50, s[40:43], s50 offen sc1\n"
"buffer_load_dwordx4 v[244:247], v51, s[40:43], s50 offen sc1\n"
"s_waitcnt vmcnt(8)\n"
"ds_read_b64_tr_b16 a[152:153], v48 offset:0\n"
"ds_read_b64_tr_b16 a[154:155], v48 offset:256\n"
"ds_read_b64_tr_b16 a[184:185], v49 offset:0\n"
"ds_read_b64_tr_b16 a[186:187], v49 offset:256\n"
"s_waitcnt lgkmcnt(4)\n"
"v_mfma_f32_16x16x32_bf16 v[200:203], v[188:191], a[88:91], v[200:203]\n"
"v_mfma_f32_16x16x32_bf16 v[204:207], v[188:191], a[120:123], v[204:207]\n"
"s_waitcnt vmcnt(6)\n"
"ds_read_b64_tr_b16 a[28:29], v48 offset:2048\n"
"ds_read_b64_tr_b16 a[30:31], v48 offset:2304\n"
"ds_read_b64_tr_b16 a[60:61], v49 offset:2048\n"
"ds_read_b64_tr_b16 a[62:63], v49 offset:2304\n"
"s_waitcnt lgkmcnt(4)\n"
"v_mfma_f32_16x16x32_bf16 v[208:211], a[152:155], v[188:191], v[208:211]\n"
"v_mfma_f32_16x16x32_bf16 v[212:215], a[184:187], v[188:191], v[212:215]\n"
"s_waitcnt vmcnt(4)\n"
"ds_read_b64_tr_b16 a[92:93], v48 offset:4096\n"
"ds_read_b64_tr_b16 a[94:95], v48 offset:4352\n"
"ds_read_b64_tr_b16 a[124:125], v49 offset:4096\n"
"ds_read_b64_tr_b16 a[126:127], v49 offset:4352\n"
"s_waitcnt lgkmcnt(4)\n"
"v_mfma_f32_16x16x32_bf16 v[192:195], v[188:191], a[28:31], v[192:195]\n"
"v_mfma_f32_16x16x32_bf16 v[196:199], v[188:191], a[60:63], v[196:199]\n"
"s_waitcnt vmcnt(2)\n"
"ds_read_b64_tr_b16 a[156:157], v48 offset:6144\n"
"ds_read_b64_tr_b16 a[158:159], v48 offset:6400\n"
"ds_read_b64_tr_b16 a[188:189], v49 offset:6144\n"
"ds_read_b64_tr_b16 a[190:191], v49 offset:6400\n"
"s_waitcnt lgkmcnt(4)\n"
"v_mfma_f32_16x16x32_bf16 v[200:203], v[188:191], a[92:95], v[200:203]\n"
"v_mfma_f32_16x16x32_bf16 v[204:207], v[188:191], a[124:127], v[204:207]\n"
"global_load_dwordx4 v[228:231], v52, s[56:57] offset:-4096 sc1\n"
"global_load_dwordx4 v[228:231], v52, s[56:57] offset:-3072 sc1\n"
"global_load_dwordx4 v[228:231], v52, s[56:57] offset:-2048 sc1\n"
"global_load_dwordx4 v[228:231], v52, s[56:57] offset:-1024 sc1\n"
"global_load_dwordx4 v[228:231], v52, s[56:57] offset:0 sc1\n"
"global_load_dwordx4 v[228:231], v52, s[56:57] offset:1024 sc1\n"
"global_load_dwordx4 v[228:231], v52, s[56:57] offset:2048 sc1\n"
"global_load_dwordx4 v[228:231], v52, s[56:57] offset:3072 sc1\n"
"s_waitcnt lgkmcnt(0)\n"
"v_mfma_f32_16x16x32_bf16 v[208:211], a[156:159], v[188:191], v[208:211]\n"
"v_mfma_f32_16x16x32_bf16 v[212:215], a[188:191], v[188:191], v[212:215]\n"
"s_mov_b32 m0, s59\n"
"s_barrier\n"
"ds_write_addtid_b32 v192 offset:0\n"
"ds_write_addtid_b32 v193 offset:4640\n"
"ds_write_addtid_b32 v194 offset:9344\n"
"ds_write_addtid_b32 v195 offset:13984\n"
"ds_write_addtid_b32 v196 offset:256\n"
"ds_write_addtid_b32 v197 offset:4896\n"
"ds_write_addtid_b32 v198 offset:9600\n"
"ds_write_addtid_b32 v199 offset:14240\n"
"ds_write_addtid_b32 v200 offset:512\n"
"ds_write_addtid_b32 v201 offset:5152\n"
"ds_write_addtid_b32 v202 offset:9856\n"
"ds_write_addtid_b32 v203 offset:14496\n"
"ds_write_addtid_b32 v204 offset:768\n"
"ds_write_addtid_b32 v205 offset:5408\n"
"ds_write_addtid_b32 v206 offset:10112\n"
"ds_write_addtid_b32 v207 offset:14752\n"
"ds_write_addtid_b32 v208 offset:1024\n"
"ds_write_addtid_b32 v209 offset:5664\n"
"ds_write_addtid_b32 v210 offset:10368\n"
"ds_write_addtid_b32 v211 offset:15008\n"
"ds_write_addtid_b32 v212 offset:1280\n"
"ds_write_addtid_b32 v213 offset:5920\n"
"ds_write_addtid_b32 v214 offset:10624\n"
"ds_write_addtid_b32 v215 offset:15264\n"
"s_waitcnt lgkmcnt(0)\n"
"s_barrier\n"
"ds_read_b128 v[96:99], v2 offset:0\n"
"ds_read_b128 v[100:103], v3 offset:0\n"
"ds_read_b128 v[104:107], v4 offset:0\n"
"ds_read_b128 v[108:111], v57 offset:0\n"
"ds_read_b128 v[112:115], v2 offset:256\n"
"ds_read_b128 v[116:119], v3 offset:256\n"
"ds_read_b128 v[120:123], v4 offset:256\n"
"ds_read_b128 v[124:127], v57 offset:256\n"
"ds_read_b128 v[128:131], v2 offset:512\n"
"ds_read_b128 v[132:135], v3 offset:512\n"
"ds_read_b128 v[136:139], v4 offset:512\n"
"ds_read_b128 v[140:143], v57 offset:512\n"
"ds_read_b128 v[144:147], v2 offset:768\n"
"ds_read_b128 v[148:151], v3 offset:768\n"
"ds_read_b128 v[152:155], v4 offset:768\n"
"ds_read_b128 v[156:159], v57 offset:768\n"
"ds_read_b128 v[160:163], v2 offset:1024\n"
"ds_read_b128 v[164:167], v3 offset:1024\n"
"ds_read_b128 v[168:171], v4 offset:1024\n"
"ds_read_b128 v[172:175], v57 offset:1024\n"
"ds_read_b128 v[176:179], v2 offset:1280\n"
"ds_read_b128 v[180:183], v3 offset:1280\n"
"ds_read_b128 v[184:187], v4 offset:1280\n"
"ds_read_b128 v[188:191], v57 offset:1280\n"
"s_waitcnt lgkmcnt(0)\n"
"s_barrier\n"
"v_add_f32 v96, v96, v100\n"
"v_add_f32 v104, v104, v108\n"
"v_add_f32 v28, v96, v104\n"
"v_add_f32 v97, v97, v101\n"
"v_add_f32 v105, v105, v109\n"
"v_add_f32 v29, v97, v105\n"
"v_add_f32 v98, v98, v102\n"
"v_add_f32 v106, v106, v110\n"
"v_add_f32 v30, v98, v106\n"
"v_add_f32 v99, v99, v103\n"
"v_add_f32 v107, v107, v111\n"
"v_add_f32 v31, v99, v107\n"
"v_add_f32 v112, v112, v116\n"
"v_add_f32 v120, v120, v124\n"
"v_add_f32 v32, v112, v120\n"
"v_add_f32 v113, v113, v117\n"
"v_add_f32 v121, v121, v125\n"
"v_add_f32 v33, v113, v121\n"
"v_add_f32 v114, v114, v118\n"
"v_add_f32 v122, v122, v126\n"
"v_add_f32 v34, v114, v122\n"
"v_add_f32 v115, v115, v119\n"
"v_add_f32 v123, v123, v127\n"
"v_add_f32 v35, v115, v123\n"
"v_add_f32 v128, v128, v132\n"
"v_add_f32 v136, v136, v140\n"
"v_add_f32 v36, v128, v136\n"
"v_add_f32 v129, v129, v133\n"
"v_add_f32 v137, v137, v141\n"
"v_add_f32 v37, v129, v137\n"
"v_add_f32 v130, v130, v134\n"
"v_add_f32 v138, v138, v142\n"
"v_add_f32 v38, v130, v138\n"
"v_add_f32 v131, v131, v135\n"
"v_add_f32 v139, v139, v143\n"
"v_add_f32 v39, v131, v139\n"
"v_add_f32 v144, v144, v148\n"
"v_add_f32 v152, v152, v156\n"
"v_add_f32 v40, v144, v152\n"
"v_add_f32 v145, v145, v149\n"
"v_add_f32 v153, v153, v157\n"
"v_add_f32 v41, v145, v153\n"
"v_add_f32 v146, v146, v150\n"
"v_add_f32 v154, v154, v158\n"
"v_add_f32 v42, v146, v154\n"
"v_add_f32 v147, v147, v151\n"
"v_add_f32 v155, v155, v159\n"
"v_add_f32 v43, v147, v155\n"
"v_add_f32 v160, v160, v164\n"
"v_add_f32 v168, v168, v172\n"
"v_add_f32 v44, v160, v168\n"
"v_add_f32 v176, v176, v180\n"
"v_add_f32 v184, v184, v188\n"
"v_add_f32 v45, v176, v184\n"
"s_mov_b32 s94, 0x80000000\n"
"s_mov_b32 s95, 0x80000000\n"
"v_mov_b32 v128, 0\n"
"v_mov_b32 v129, 0\n"
"v_mov_b32 v130, 0\n"
"v_mov_b32 v131, 0\n"
"v_mov_b32 v136, 0\n"
"v_mov_b32 v137, 0\n"
"v_mov_b32 v138, 0\n"
"v_mov_b32 v139, 0\n"
"v_mov_b32 v144, 0\n"
"v_mov_b32 v145, 0\n"
"v_mov_b32 v146, 0\n"
"v_mov_b32 v147, 0\n"
"v_mov_b32 v152, 0\n"
"v_mov_b32 v153, 0\n"
"v_mov_b32 v154, 0\n"
"v_mov_b32 v155, 0\n"
"s_waitcnt vmcnt(8)\n"
"v_cmp_ne_u64 vcc, s[48:49], v[242:243]\n"
"s_cmp_lg_u32 s54, 0\n"
"s_cbranch_scc1 .LQ_e0\n"
"v_mov_b32 v128, v240\n"
"v_mov_b32 v129, v241\n"
"v_mov_b32 v130, v242\n"
"v_mov_b32 v131, v243\n"
"v_mov_b32 v136, v244\n"
"v_mov_b32 v137, v245\n"
"v_mov_b32 v138, v246\n"
"v_mov_b32 v139, v247\n"
"s_cbranch_vccnz .LQ_1s\n"
"s_branch .LQ_1r\n"
".LQ_e0:\n"
"s_cmp_lg_u32 s54, 1\n"
"s_cbranch_scc1 .LQ_e1\n"
"v_mov_b32 v136, v240\n"
"v_mov_b32 v137, v241\n"
"v_mov_b32 v138, v242\n"
"v_mov_b32 v139, v243\n"
"v_mov_b32 v144, v244\n"
"v_mov_b32 v145, v245\n"
"v_mov_b32 v146, v246\n"
"v_mov_b32 v147, v247\n"
"s_cbranch_vccnz .LQ_2s\n"
"s_branch .LQ_2r\n"
".LQ_e1:\n"
"s_cmp_lg_u32 s54, 2\n"
"s_cbranch_scc1 .LQ_e2\n"
"v_mov_b32 v144, v240\n"
"v_mov_b32 v145, v241\n"
"v_mov_b32 v146, v242\n"
"v_mov_b32 v147, v243\n"
"v_mov_b32 v152, v244\n"
"v_mov_b32 v153, v245\n"
"v_mov_b32 v154, v246\n"
"v_mov_b32 v155, v247\n"
"s_cbranch_vccnz .LQ_3s\n"
"s_branch .LQ_3r\n"
".LQ_e2:\n"
"v_mov_b32 v152, v240\n"
"v_mov_b32 v153, v241\n"
"v_mov_b32 v154, v242\n"
"v_mov_b32 v155, v243\n"
"v_mov_b32 v128, v244\n"
"v_mov_b32 v129, v245\n"
"v_mov_b32 v130, v246\n"
"v_mov_b32 v131, v247\n"
"s_cbranch_vccnz .LQ_4s\n"
"s_branch .LQ_4r\n"
".LQ_e3:\n"
".p2align 6\n"
".LQ_top:\n"
"v_mfma_f32_16x16x32_bf16 v[112:115], a[128:131], v[48:51], 0\n"
"s_add_u32 s50, s50, 0x100000\n"
"global_load_dwordx4 a[192:195], v5, s[56:57] offset:0 sc1\n"
"v_mfma_f32_16x16x32_bf16 v[112:115], a[132:135], v[52:55], v[112:115]\n"
"ds_write_addtid_b32 v132 offset:4352\n"
"v_mfma_f32_16x16x32_bf16 v[112:115], a[136:139], v[56:59], v[112:115]\n"
"ds_write_addtid_b32 v133 offset:8992\n"
"v_mfma_f32_16x16x32_bf16 v[112:115], a[140:143], v[60:63], v[112:115]\n"
"ds_write_addtid_b32 v134 offset:13696\n"
"v_mfma_f32_16x16x32_bf16 v[112:115], a[144:147], v[64:67], v[112:115]\n"
"ds_write_addtid_b32 v135 offset:18336\n"
"v_mov_b32 v254, v128\n"
"v_mfma_f32_16x16x32_bf16 v[112:115], a[148:151], v[68:71], v[112:115]\n"
"global_load_dwordx4 a[196:199], v5, s[56:57] offset:1024 sc1\n"
"v_mul_f32 v119, v129, v39\n"
"v_mfma_f32_16x16x32_bf16 v[112:115], a[152:155], v[72:75], v[112:115]\n"
"v_mul_f32 v117, v129, v37\n"
"v_mul_f32 v118, v129, v38\n"
"v_mfma_f32_16x16x32_bf16 v[112:115], a[156:159], v[76:79], v[112:115]\n"
"v_mul_f32 v123, v129, v43\n"
"v_mul_f32 v116, v129, v36\n"
"v_mfma_f32_16x16x32_bf16 v[124:127], a[160:163], v[48:51], 0\n"
"v_mul_f32 v121, v129, v41\n"
"v_mul_f32 v122, v129, v42\n"
"v_mfma_f32_16x16x32_bf16 v[124:127], a[164:167], v[52:55], v[124:127]\n"
"ds_write_addtid_b32 v112 offset:0\n"
"v_mul_f32 v120, v129, v40\n"
"v_mfma_f32_16x16x32_bf16 v[124:127], a[168:171], v[56:59], v[124:127]\n"
"s_waitcnt lgkmcnt(1)\n"
"ds_write_addtid_b32 v113 offset:4640\n"
"global_load_dwordx4 a[200:203], v6, s[56:57] offset:0 sc1\n"
"v_mfma_f32_16x16x32_bf16 v[124:127], a[172:175], v[60:63], v[124:127]\n"
"s_barrier\n"
"v_mfma_f32_16x16x32_bf16 v[124:127], a[176:179], v[64:67], v[124:127]\n"
"ds_write_addtid_b32 v114 offset:9344\n"
"ds_read2st64_b32 v[128:129], v12 offset0:0 offset1:1\n"
"v_mfma_f32_16x16x32_bf16 v[124:127], a[180:183], v[68:71], v[124:127]\n"
"ds_write_addtid_b32 v115 offset:13984\n"
"ds_read2st64_b32 v[130:131], v12 offset0:2 offset1:3\n"
"v_mfma_f32_16x16x32_bf16 v[124:127], a[184:187], v[72:75], v[124:127]\n"
"ds_read2st64_b32 v[140:141], v11 offset0:4 offset1:5\n"
"ds_read2st64_b32 v[148:149], v11 offset0:6 offset1:7\n"
"v_mfma_f32_16x16x32_bf16 v[124:127], a[188:191], v[76:79], v[124:127]\n"
"s_add_u32 s94, s50, 0xffc00000\n"
"s_and_b32 s95, s94, 0xfbffffff\n"
"v_mfma_f32_16x16x32_bf16 v[112:115], v[48:51], a[0:3], 0\n"
"global_load_dwordx4 a[204:207], v6, s[56:57] offset:1024 sc1\n"
"v_mfma_f32_16x16x32_bf16 v[112:115], v[52:55], a[4:7], v[112:115]\n"
"ds_write_addtid_b32 v124 offset:256\n"
"v_mfma_f32_16x16x32_bf16 v[112:115], v[56:59], a[8:11], v[112:115]\n"
"s_waitcnt lgkmcnt(3)\n"
"ds_write_addtid_b32 v125 offset:4896\n"
"v_mfma_f32_16x16x32_bf16 v[132:135], v[128:131], v[164:167], v[16:19]\n"
"s_cmp_eq_u32 s94, s62\n"
"s_cbranch_scc1 .LQ_fs\n"
".LQ_fr:\n"
"v_mfma_f32_16x16x32_bf16 v[112:115], v[60:63], a[12:15], v[112:115]\n"
"ds_write_addtid_b32 v126 offset:9600\n"
"v_max_f32 v253, v156, v158\n"
"v_mfma_f32_16x16x32_bf16 v[112:115], v[64:67], a[16:19], v[112:115]\n"
"ds_write_addtid_b32 v127 offset:14240\n"
"global_load_dwordx4 a[208:211], v7, s[56:57] offset:0 sc1\n"
"v_mfma_f32_16x16x32_bf16 v[112:115], v[68:71], a[20:23], v[112:115]\n"
"v_max_f32 v252, v157, v159\n"
"v_max3_f32 v253, v132, v134, v253\n"
"v_mfma_f32_16x16x32_bf16 v[112:115], v[72:75], a[24:27], v[112:115]\n"
"v_max3_f32 v252, v133, v135, v252\n"
"v_max_f32 v251, v253, v252\n"
"v_mfma_f32_16x16x32_bf16 v[112:115], v[76:79], a[28:31], v[112:115]\n"
"v_mov_b32 v250, v251\n"
"v_mfma_f32_16x16x32_bf16 v[124:127], v[48:51], a[32:35], 0\n"
"v_permlane32_swap_b32 v250, v251\n"
"v_mfma_f32_16x16x32_bf16 v[124:127], v[52:55], a[36:39], v[124:127]\n"
"ds_write_addtid_b32 v112 offset:512\n"
"global_load_dwordx4 a[212:215], v7, s[56:57] offset:1024 sc1\n"
"v_mfma_f32_16x16x32_bf16 v[124:127], v[56:59], a[40:43], v[124:127]\n"
"ds_write_addtid_b32 v113 offset:5152\n"
"v_max_f32 v251, v250, v251\n"
"v_mfma_f32_16x16x32_bf16 v[124:127], v[60:63], a[44:47], v[124:127]\n"
"ds_write_addtid_b32 v114 offset:9856\n"
"v_mov_b32 v250, v251\n"
"v_mfma_f32_16x16x32_bf16 v[124:127], v[64:67], a[48:51], v[124:127]\n"
"ds_write_addtid_b32 v115 offset:14496\n"
"v_mfma_f32_16x16x32_bf16 v[124:127], v[68:71], a[52:55], v[124:127]\n"
"v_permlane16_swap_b32 v250, v251\n"
"v_mfma_f32_16x16x32_bf16 v[124:127], v[72:75], a[56:59], v[124:127]\n"
"global_load_dwordx4 a[216:219], v8, s[56:57] offset:0 sc1\n"
"v_max_f32 v251, v250, v251\n"
"v_mfma_f32_16x16x32_bf16 v[124:127], v[76:79], a[60:63], v[124:127]\n"
"v_mul_f32 v251, v251, v249\n"
"v_fma_f32 v253, v253, v249, -v251\n"
"v_mfma_f32_16x16x32_bf16 v[116:119], a[64:67], v[48:51], v[116:119]\n"
"v_fma_f32 v252, v252, v249, -v251\n"
"v_mfma_f32_16x16x32_bf16 v[116:119], a[68:71], v[52:55], v[116:119]\n"
"ds_write_addtid_b32 v124 offset:768\n"
"v_mfma_f32_16x16x32_bf16 v[116:119], a[72:75], v[56:59], v[116:119]\n"
"ds_write_addtid_b32 v125 offset:5408\n"
"v_mfma_f32_16x16x32_bf16 v[116:119], a[76:79], v[60:63], v[116:119]\n"
"ds_write_addtid_b32 v126 offset:10112\n"
"global_load_dwordx4 a[220:223], v8, s[56:57] offset:1024 sc1\n"
"v_mfma_f32_16x16x32_bf16 v[116:119], a[80:83], v[64:67], v[116:119]\n"
"ds_write_addtid_b32 v127 offset:14752\n"
"v_mfma_f32_16x16x32_bf16 v[116:119], a[84:87], v[68:71], v[116:119]\n"
"v_exp_f32 v253, v253\n"
"v_mfma_f32_16x16x32_bf16 v[116:119], a[88:91], v[72:75], v[116:119]\n"
"v_exp_f32 v252, v252\n"
"v_mfma_f32_16x16x32_bf16 v[116:119], a[92:95], v[76:79], v[116:119]\n"
"s_add_u32 s58, s50, 0x100000\n"
"v_add_f32 v251, v253, v252\n"
"v_mov_b32 v250, v251\n"
"v_mfma_f32_16x16x32_bf16 v[120:123], a[96:99], v[48:51], v[120:123]\n"
"s_and_b32 s58, s58, 0x3ffffff\n"
"v_permlane32_swap_b32 v250, v251\n"
"v_mfma_f32_16x16x32_bf16 v[120:123], a[100:103], v[52:55], v[120:123]\n"
"s_add_u32 s56, s40, s58\n"
"v_add_f32 v251, v250, v251\n"
"v_mov_b32 v250, v251\n"
"v_mfma_f32_16x16x32_bf16 v[120:123], a[104:107], v[56:59], v[120:123]\n"
"s_addc_u32 s57, s41, 0\n"
"v_permlane16_swap_b32 v250, v251\n"
"v_mfma_f32_16x16x32_bf16 v[120:123], a[108:111], v[60:63], v[120:123]\n"
"s_waitcnt vmcnt(8)\n"
"v_cmp_ne_u64 vcc, s[48:49], v[154:155]\n"
"v_add_f32 v251, v250, v251\n"
"v_mfma_f32_16x16x32_bf16 v[120:123], a[112:115], v[64:67], v[120:123]\n"
"buffer_load_dwordx4 v[128:131], v9, s[40:43], s50 offen sc1\n"
"v_mfma_f32_16x16x32_bf16 v[120:123], a[116:119], v[68:71], v[120:123]\n"
"s_cbranch_vccnz .LQ_4s\n"
".LQ_4r:\n"
"v_mfma_f32_16x16x32_bf16 v[120:123], a[120:123], v[72:75], v[120:123]\n"
"s_add_u32 s50, s50, 0x100000\n"
"v_rcp_f32 v251, v251\n"
"v_mfma_f32_16x16x32_bf16 v[120:123], a[124:127], v[76:79], v[120:123]\n"
"global_load_dwordx4 a[224:227], v5, s[56:57] offset:0 sc1\n"
"v_mul_f32 v253, v253, v251\n"
"v_mfma_f32_16x16x32_bf16 v[48:51], v[80:83], a[64:67], 0\n"
"v_mul_f32 v252, v252, v251\n"
"v_cvt_pk_bf16_f32 v250, v253, v252\n"
"v_mfma_f32_16x16x32_bf16 v[48:51], v[84:87], a[68:71], v[48:51]\n"
"v_mfma_f32_16x16x32_bf16 v[48:51], v[88:91], a[72:75], v[48:51]\n"
"s_waitcnt lgkmcnt(12)\n"
"v_and_b32 v52, v250, v24\n"
"v_and_b32 v53, v250, v25\n"
"v_mfma_f32_16x16x32_bf16 v[48:51], v[92:95], a[76:79], v[48:51]\n"
"v_and_b32 v54, v250, v26\n"
"v_and_b32 v55, v250, v27\n"
"v_mfma_f32_16x16x32_bf16 v[48:51], v[96:99], a[80:83], v[48:51]\n"
"global_load_dwordx4 a[228:231], v5, s[56:57] offset:1024 sc1\n"
"v_mfma_f32_16x16x32_bf16 v[56:59], v[140:143], v[52:55], 0\n"
"v_mul_f32_dpp v75, v137, v44 row_newbcast:3 row_mask:0xf bank_mask:0xf\n"
"v_mov_b32 v253, v136\n"
"v_mfma_f32_16x16x32_bf16 v[60:63], v[148:151], v[52:55], 0\n"
"v_mul_f32_dpp v73, v137, v44 row_newbcast:1 row_mask:0xf bank_mask:0xf\n"
"v_mul_f32_dpp v74, v137, v44 row_newbcast:2 row_mask:0xf bank_mask:0xf\n"
"v_mfma_f32_16x16x32_bf16 v[48:51], v[100:103], a[84:87], v[48:51]\n"
"v_cvt_pk_bf16_f32 v52, v56, v57\n"
"v_cvt_pk_bf16_f32 v53, v58, v59\n"
"v_mfma_f32_16x16x32_bf16 v[48:51], v[104:107], a[88:91], v[48:51]\n"
"v_cvt_pk_bf16_f32 v54, v60, v61\n"
"v_cvt_pk_bf16_f32 v55, v62, v63\n"
"v_mfma_f32_16x16x32_bf16 v[48:51], v[108:111], a[92:95], v[48:51]\n"
"buffer_store_dwordx4 v[52:55], v13, s[40:43], s95 offen sc1\n"
"v_mul_f32_dpp v114, v137, v45 row_newbcast:2 row_mask:0xf bank_mask:0xf\n"
"v_mul_f32_dpp v115, v137, v45 row_newbcast:3 row_mask:0xf bank_mask:0xf\n"
"v_mul_f32_dpp v72, v137, v44 row_newbcast:0 row_mask:0xf bank_mask:0xf\n"
"v_mfma_f32_16x16x32_bf16 v[52:55], v[80:83], a[96:99], 0\n"
"s_waitcnt lgkmcnt(0)\n"
"v_mul_f32_dpp v112, v137, v45 row_newbcast:0 row_mask:0xf bank_mask:0xf\n"
"v_mul_f32_dpp v113, v137, v45 row_newbcast:1 row_mask:0xf bank_mask:0xf\n"
"v_mfma_f32_16x16x32_bf16 v[52:55], v[84:87], a[100:103], v[52:55]\n"
"s_barrier\n"
"v_mfma_f32_16x16x32_bf16 v[52:55], v[88:91], a[104:107], v[52:55]\n"
"ds_write_addtid_b32 v48 offset:1024\n"
"ds_read_b128 v[56:59], v2 offset:1024\n"
"v_mfma_f32_16x16x32_bf16 v[52:55], v[92:95], a[108:111], v[52:55]\n"
"ds_write_addtid_b32 v49 offset:5664\n"
"ds_read_b128 v[60:63], v3 offset:2048\n"
"v_mfma_f32_16x16x32_bf16 v[52:55], v[96:99], a[112:115], v[52:55]\n"
"ds_write_addtid_b32 v50 offset:10368\n"
"global_load_dwordx4 a[232:235], v6, s[56:57] offset:0 sc1\n"
"v_mfma_f32_16x16x32_bf16 v[52:55], v[100:103], a[116:119], v[52:55]\n"
"ds_write_addtid_b32 v51 offset:15008\n"
"ds_read_b128 v[64:67], v4 offset:3072\n"
"v_mfma_f32_16x16x32_bf16 v[52:55], v[104:107], a[120:123], v[52:55]\n"
"ds_read_b128 v[68:71], v2 offset:1280\n"
"ds_read_b128 v[48:51], v3 offset:2304\n"
"v_mfma_f32_16x16x32_bf16 v[52:55], v[108:111], a[124:127], v[52:55]\n"
"ds_read_b128 v[76:79], v4 offset:3328\n"
"v_mfma_f32_16x16x32_bf16 v[124:127], v[80:83], a[0:3], 0\n"
"global_load_dwordx4 a[236:239], v6, s[56:57] offset:1024 sc1\n"
"v_mfma_f32_16x16x32_bf16 v[124:127], v[84:87], a[4:7], v[124:127]\n"
"ds_write_addtid_b32 v52 offset:1280\n"
"v_mfma_f32_16x16x32_bf16 v[124:127], v[88:91], a[8:11], v[124:127]\n"
"ds_write_addtid_b32 v53 offset:5920\n"
"v_mfma_f32_16x16x32_bf16 v[124:127], v[92:95], a[12:15], v[124:127]\n"
"s_waitcnt lgkmcnt(5)\n"
"ds_write_addtid_b32 v54 offset:10624\n"
"v_add_f32 v56, v56, v60\n"
"v_mfma_f32_16x16x32_bf16 v[124:127], v[96:99], a[16:19], v[124:127]\n"
"ds_write_addtid_b32 v55 offset:15264\n"
"v_add_f32 v57, v57, v61\n"
"v_mfma_f32_16x16x32_bf16 v[124:127], v[100:103], a[20:23], v[124:127]\n"
"global_load_dwordx4 a[240:243], v7, s[56:57] offset:0 sc1\n"
"v_add_f32 v58, v58, v62\n"
"v_mfma_f32_16x16x32_bf16 v[124:127], v[104:107], a[24:27], v[124:127]\n"
"v_add_f32 v59, v59, v63\n"
"v_add_f32 v56, v56, v64\n"
"v_mfma_f32_16x16x32_bf16 v[124:127], v[108:111], a[28:31], v[124:127]\n"
"v_add_f32 v57, v57, v65\n"
"v_add_f32 v58, v58, v66\n"
"v_mfma_f32_16x16x32_bf16 v[52:55], v[80:83], a[32:35], 0\n"
"v_add_f32 v59, v59, v67\n"
"v_add_f32 v56, v56, v116\n"
"v_mfma_f32_16x16x32_bf16 v[52:55], v[84:87], a[36:39], v[52:55]\n"
"ds_write_addtid_b32 v124 offset:1536\n"
"v_add_f32 v57, v57, v117\n"
"v_mfma_f32_16x16x32_bf16 v[52:55], v[88:91], a[40:43], v[52:55]\n"
"ds_write_addtid_b32 v125 offset:6176\n"
"global_load_dwordx4 a[244:247], v7, s[56:57] offset:1024 sc1\n"
"v_mfma_f32_16x16x32_bf16 v[52:55], v[92:95], a[44:47], v[52:55]\n"
"ds_write_addtid_b32 v126 offset:10880\n"
"v_add_f32 v58, v58, v118\n"
"v_mfma_f32_16x16x32_bf16 v[52:55], v[96:99], a[48:51], v[52:55]\n"
"ds_write_addtid_b32 v127 offset:15520\n"
"v_add_f32 v59, v59, v119\n"
"v_mfma_f32_16x16x32_bf16 v[52:55], v[100:103], a[52:55], v[52:55]\n"
"v_mul_f32 v56, v254, v56\n"
"v_mul_f32 v57, v254, v57\n"
"v_mfma_f32_16x16x32_bf16 v[52:55], v[104:107], a[56:59], v[52:55]\n"
"v_mul_f32 v58, v254, v58\n"
"v_mul_f32 v59, v254, v59\n"
"v_mfma_f32_16x16x32_bf16 v[52:55], v[108:111], a[60:63], v[52:55]\n"
"global_load_dwordx4 a[248:251], v8, s[56:57] offset:0 sc1\n"
"v_cvt_pk_bf16_f32 v60, v56, v57\n"
"v_mfma_f32_16x16x32_bf16 v[72:75], v[80:83], a[128:131], v[72:75]\n"
"v_cvt_pk_bf16_f32 v61, v58, v59\n"
"v_mfma_f32_16x16x32_bf16 v[72:75], v[84:87], a[132:135], v[72:75]\n"
"ds_write_addtid_b32 v52 offset:1792\n"
"ds_write_addtid_b32 v60 offset:18592\n"
"v_mfma_f32_16x16x32_bf16 v[72:75], v[88:91], a[136:139], v[72:75]\n"
"ds_write_addtid_b32 v53 offset:6432\n"
"ds_write_addtid_b32 v61 offset:18848\n"
"v_mfma_f32_16x16x32_bf16 v[72:75], v[92:95], a[140:143], v[72:75]\n"
"s_waitcnt lgkmcnt(12)\n"
"ds_write_addtid_b32 v54 offset:11136\n"
"v_add_f32 v68, v68, v48\n"
"v_mfma_f32_16x16x32_bf16 v[72:75], v[96:99], a[144:147], v[72:75]\n"
"ds_write_addtid_b32 v55 offset:15776\n"
"global_load_dwordx4 a[252:255], v8, s[56:57] offset:1024 sc1\n"
"v_mfma_f32_16x16x32_bf16 v[72:75], v[100:103], a[148:151], v[72:75]\n"
"v_add_f32 v69, v69, v49\n"
"v_add_f32 v70, v70, v50\n"
"v_mfma_f32_16x16x32_bf16 v[72:75], v[104:107], a[152:155], v[72:75]\n"
"v_add_f32 v71, v71, v51\n"
"v_add_f32 v68, v68, v76\n"
"v_mfma_f32_16x16x32_bf16 v[72:75], v[108:111], a[156:159], v[72:75]\n"
"v_add_f32 v69, v69, v77\n"
"v_add_f32 v70, v70, v78\n"
"v_mfma_f32_16x16x32_bf16 v[112:115], v[80:83], a[160:163], v[112:115]\n"
"s_add_u32 s58, s50, 0x100000\n"
"v_add_f32 v71, v71, v79\n"
"v_add_f32 v68, v68, v120\n"
"v_mfma_f32_16x16x32_bf16 v[112:115], v[84:87], a[164:167], v[112:115]\n"
"s_and_b32 s58, s58, 0x3ffffff\n"
"v_add_f32 v69, v69, v121\n"
"v_add_f32 v70, v70, v122\n"
"v_mfma_f32_16x16x32_bf16 v[112:115], v[88:91], a[168:171], v[112:115]\n"
"s_add_u32 s56, s40, s58\n"
"v_add_f32 v71, v71, v123\n"
"v_mul_f32 v68, v254, v68\n"
"v_mfma_f32_16x16x32_bf16 v[112:115], v[92:95], a[172:175], v[112:115]\n"
"s_addc_u32 s57, s41, 0\n"
"v_mul_f32 v69, v254, v69\n"
"v_mul_f32 v70, v254, v70\n"
"v_mfma_f32_16x16x32_bf16 v[112:115], v[96:99], a[176:179], v[112:115]\n"
"s_waitcnt vmcnt(9)\n"
"v_cmp_ne_u64 vcc, s[48:49], v[130:131]\n"
"v_mul_f32 v71, v254, v71\n"
"v_mfma_f32_16x16x32_bf16 v[112:115], v[100:103], a[180:183], v[112:115]\n"
"buffer_load_dwordx4 v[136:139], v10, s[40:43], s50 offen sc1\n"
"v_mfma_f32_16x16x32_bf16 v[112:115], v[104:107], a[184:187], v[112:115]\n"
"s_cbranch_vccnz .LQ_1s\n"
".LQ_1r:\n"
"v_cvt_pk_bf16_f32 v62, v68, v69\n"
"v_mfma_f32_16x16x32_bf16 v[112:115], v[108:111], a[188:191], v[112:115]\n"
"v_cvt_pk_bf16_f32 v63, v70, v71\n"
"v_mfma_f32_16x16x32_bf16 v[64:67], a[192:195], a[64:67], 0\n"
"s_add_u32 s50, s50, 0x100000\n"
"global_load_dwordx4 v[48:51], v5, s[56:57] offset:0 sc1\n"
"ds_write_addtid_b32 v62 offset:19104\n"
"v_mfma_f32_16x16x32_bf16 v[64:67], a[196:199], a[68:71], v[64:67]\n"
"ds_write_addtid_b32 v63 offset:19360\n"
"v_mfma_f32_16x16x32_bf16 v[64:67], a[200:203], a[72:75], v[64:67]\n"
"v_mfma_f32_16x16x32_bf16 v[64:67], a[204:207], a[76:79], v[64:67]\n"
"v_mul_f32 v249, 0x3e8293ee, v144\n"
"v_mfma_f32_16x16x32_bf16 v[64:67], a[208:211], a[80:83], v[64:67]\n"
"v_mul_f32 v110, v145, v30\n"
"v_mul_f32 v111, v145, v31\n"
"v_mfma_f32_16x16x32_bf16 v[64:67], a[212:215], a[84:87], v[64:67]\n"
"global_load_dwordx4 v[52:55], v5, s[56:57] offset:1024 sc1\n"
"v_mul_f32 v109, v145, v29\n"
"v_mfma_f32_16x16x32_bf16 v[64:67], a[216:219], a[88:91], v[64:67]\n"
"v_mul_f32 v119, v145, v35\n"
"v_mul_f32 v108, v145, v28\n"
"v_mfma_f32_16x16x32_bf16 v[64:67], a[220:223], a[92:95], v[64:67]\n"
"s_waitcnt vmcnt(9)\n"
"buffer_store_dwordx2 v[0:1], v14, s[40:43], s94 offen offset:0 sc1\n"
"v_mul_f32 v118, v145, v34\n"
"v_mfma_f32_16x16x32_bf16 v[68:71], a[192:195], a[96:99], 0\n"
"s_waitcnt lgkmcnt(0)\n"
"v_mul_f32 v116, v145, v32\n"
"v_mul_f32 v117, v145, v33\n"
"v_mfma_f32_16x16x32_bf16 v[68:71], a[196:199], a[100:103], v[68:71]\n"
"s_barrier\n"
"v_mfma_f32_16x16x32_bf16 v[68:71], a[200:203], a[104:107], v[68:71]\n"
"ds_write_addtid_b32 v64 offset:2048\n"
"global_load_dwordx4 v[56:59], v6, s[56:57] offset:0 sc1\n"
"v_mfma_f32_16x16x32_bf16 v[68:71], a[204:207], a[108:111], v[68:71]\n"
"ds_write_addtid_b32 v65 offset:6688\n"
"ds_read_b128 v[76:79], v2 offset:2560\n"
"v_mfma_f32_16x16x32_bf16 v[68:71], a[208:211], a[112:115], v[68:71]\n"
"ds_write_addtid_b32 v66 offset:11392\n"
"ds_read_b128 v[80:83], v3 offset:3584\n"
"v_mfma_f32_16x16x32_bf16 v[68:71], a[212:215], a[116:119], v[68:71]\n"
"ds_write_addtid_b32 v67 offset:16032\n"
"ds_read_b128 v[84:87], v4 offset:0\n"
"v_mfma_f32_16x16x32_bf16 v[68:71], a[216:219], a[120:123], v[68:71]\n"
"ds_read_b128 v[88:91], v2 offset:2816\n"
"ds_read_b128 v[92:95], v3 offset:3840\n"
"v_mfma_f32_16x16x32_bf16 v[68:71], a[220:223], a[124:127], v[68:71]\n"
"global_load_dwordx4 v[60:63], v6, s[56:57] offset:1024 sc1\n"
"ds_read_b128 v[96:99], v4 offset:256\n"
"v_mfma_f32_16x16x32_bf16 v[100:103], a[128:131], a[192:195], 0\n"
"v_mfma_f32_16x16x32_bf16 v[100:103], a[132:135], a[196:199], v[100:103]\n"
"ds_write_addtid_b32 v68 offset:2304\n"
"v_mfma_f32_16x16x32_bf16 v[100:103], a[136:139], a[200:203], v[100:103]\n"
"ds_write_addtid_b32 v69 offset:6944\n"
"v_mfma_f32_16x16x32_bf16 v[100:103], a[140:143], a[204:207], v[100:103]\n"
"s_waitcnt lgkmcnt(5)\n"
"ds_write_addtid_b32 v70 offset:11648\n"
"v_add_f32 v76, v76, v80\n"
"v_mfma_f32_16x16x32_bf16 v[100:103], a[144:147], a[208:211], v[100:103]\n"
"ds_write_addtid_b32 v71 offset:16288\n"
"global_load_dwordx4 v[64:67], v7, s[56:57] offset:0 sc1\n"
"v_mfma_f32_16x16x32_bf16 v[100:103], a[148:151], a[212:215], v[100:103]\n"
"v_add_f32 v77, v77, v81\n"
"v_add_f32 v78, v78, v82\n"
"v_mfma_f32_16x16x32_bf16 v[100:103], a[152:155], a[216:219], v[100:103]\n"
"v_add_f32 v79, v79, v83\n"
"v_add_f32 v76, v76, v84\n"
"v_mfma_f32_16x16x32_bf16 v[100:103], a[156:159], a[220:223], v[100:103]\n"
"v_add_f32 v77, v77, v85\n"
"v_add_f32 v78, v78, v86\n"
"v_mfma_f32_16x16x32_bf16 v[80:83], a[160:163], a[192:195], 0\n"
"v_add_f32 v79, v79, v87\n"
"v_add_f32 v76, v76, v72\n"
"v_mfma_f32_16x16x32_bf16 v[80:83], a[164:167], a[196:199], v[80:83]\n"
"ds_write_addtid_b32 v100 offset:2560\n"
"global_load_dwordx4 v[68:71], v7, s[56:57] offset:1024 sc1\n"
"v_mfma_f32_16x16x32_bf16 v[80:83], a[168:171], a[200:203], v[80:83]\n"
"ds_write_addtid_b32 v101 offset:7200\n"
"v_add_f32 v77, v77, v73\n"
"v_mfma_f32_16x16x32_bf16 v[80:83], a[172:175], a[204:207], v[80:83]\n"
"ds_write_addtid_b32 v102 offset:11904\n"
"v_add_f32 v78, v78, v74\n"
"v_mfma_f32_16x16x32_bf16 v[80:83], a[176:179], a[208:211], v[80:83]\n"
"ds_write_addtid_b32 v103 offset:16544\n"
"v_add_f32 v79, v79, v75\n"
"v_mfma_f32_16x16x32_bf16 v[80:83], a[180:183], a[212:215], v[80:83]\n"
"v_mul_f32_dpp v76, v253, v76 row_newbcast:0 row_mask:0xf bank_mask:0xf\n"
"v_mul_f32_dpp v77, v253, v77 row_newbcast:1 row_mask:0xf bank_mask:0xf\n"
"v_mfma_f32_16x16x32_bf16 v[80:83], a[184:187], a[216:219], v[80:83]\n"
"global_load_dwordx4 v[72:75], v8, s[56:57] offset:0 sc1\n"
"v_mul_f32_dpp v78, v253, v78 row_newbcast:2 row_mask:0xf bank_mask:0xf\n"
"v_mfma_f32_16x16x32_bf16 v[80:83], a[188:191], a[220:223], v[80:83]\n"
"v_mul_f32_dpp v79, v253, v79 row_newbcast:3 row_mask:0xf bank_mask:0xf\n"
"v_cvt_pk_bf16_f32 v142, v76, v77\n"
"v_mfma_f32_16x16x32_bf16 v[108:111], a[0:3], a[192:195], v[108:111]\n"
"v_cvt_pk_bf16_f32 v143, v78, v79\n"
"v_mfma_f32_16x16x32_bf16 v[108:111], a[4:7], a[196:199], v[108:111]\n"
"ds_write_addtid_b32 v80 offset:2816\n"
"ds_write_addtid_b32 v142 offset:19616\n"
"v_mfma_f32_16x16x32_bf16 v[108:111], a[8:11], a[200:203], v[108:111]\n"
"ds_write_addtid_b32 v81 offset:7456\n"
"ds_write_addtid_b32 v143 offset:19872\n"
"v_mfma_f32_16x16x32_bf16 v[108:111], a[12:15], a[204:207], v[108:111]\n"
"s_waitcnt lgkmcnt(12)\n"
"ds_write_addtid_b32 v82 offset:12160\n"
"global_load_dwordx4 v[76:79], v8, s[56:57] offset:1024 sc1\n"
"v_mfma_f32_16x16x32_bf16 v[108:111], a[16:19], a[208:211], v[108:111]\n"
"ds_write_addtid_b32 v83 offset:16800\n"
"v_add_f32 v88, v88, v92\n"
"v_mfma_f32_16x16x32_bf16 v[108:111], a[20:23], a[212:215], v[108:111]\n"
"v_add_f32 v89, v89, v93\n"
"v_add_f32 v90, v90, v94\n"
"v_mfma_f32_16x16x32_bf16 v[108:111], a[24:27], a[216:219], v[108:111]\n"
"v_add_f32 v91, v91, v95\n"
"v_add_f32 v88, v88, v96\n"
"v_mfma_f32_16x16x32_bf16 v[108:111], a[28:31], a[220:223], v[108:111]\n"
"v_add_f32 v89, v89, v97\n"
"v_add_f32 v90, v90, v98\n"
"v_mfma_f32_16x16x32_bf16 v[116:119], a[32:35], a[192:195], v[116:119]\n"
"v_add_f32 v91, v91, v99\n"
"v_add_f32 v88, v88, v112\n"
"v_mfma_f32_16x16x32_bf16 v[116:119], a[36:39], a[196:199], v[116:119]\n"
"v_add_f32 v89, v89, v113\n"
"v_add_f32 v90, v90, v114\n"
"v_mfma_f32_16x16x32_bf16 v[116:119], a[40:43], a[200:203], v[116:119]\n"
"s_add_u32 s58, s50, 0x100000\n"
"buffer_load_dwordx4 v[144:147], v9, s[40:43], s50 offen sc1\n"
"v_mfma_f32_16x16x32_bf16 v[116:119], a[44:47], a[204:207], v[116:119]\n"
"s_and_b32 s58, s58, 0x3ffffff\n"
"v_add_f32 v91, v91, v115\n"
"v_mul_f32_dpp v88, v253, v88 row_newbcast:0 row_mask:0xf bank_mask:0xf\n"
"v_mfma_f32_16x16x32_bf16 v[116:119], a[48:51], a[208:211], v[116:119]\n"
"s_add_u32 s56, s40, s58\n"
"v_mul_f32_dpp v89, v253, v89 row_newbcast:1 row_mask:0xf bank_mask:0xf\n"
"v_mul_f32_dpp v90, v253, v90 row_newbcast:2 row_mask:0xf bank_mask:0xf\n"
"v_mfma_f32_16x16x32_bf16 v[116:119], a[52:55], a[212:215], v[116:119]\n"
"s_addc_u32 s57, s41, 0\n"
"v_mul_f32_dpp v91, v253, v91 row_newbcast:3 row_mask:0xf bank_mask:0xf\n"
"v_cvt_pk_bf16_f32 v150, v88, v89\n"
"v_mfma_f32_16x16x32_bf16 v[116:119], a[56:59], a[216:219], v[116:119]\n"
"s_waitcnt vmcnt(10)\n"
"v_cmp_ne_u64 vcc, s[48:49], v[138:139]\n"
"v_cvt_pk_bf16_f32 v151, v90, v91\n"
"v_mfma_f32_16x16x32_bf16 v[116:119], a[60:63], a[220:223], v[116:119]\n"
"ds_write_addtid_b32 v150 offset:20128\n"
"ds_write_addtid_b32 v151 offset:20384\n"
"v_mfma_f32_16x16x32_bf16 v[92:95], a[224:227], a[64:67], 0\n"
"s_cbranch_vccnz .LQ_2s\n"
".LQ_2r:\n"
"v_mfma_f32_16x16x32_bf16 v[92:95], a[228:231], a[68:71], v[92:95]\n"
"global_load_dwordx4 v[80:83], v5, s[56:57] offset:0 sc1\n"
"v_mfma_f32_16x16x32_bf16 v[92:95], a[232:235], a[72:75], v[92:95]\n"
"s_add_u32 s50, s50, 0x100000\n"
"v_mfma_f32_16x16x32_bf16 v[92:95], a[236:239], a[76:79], v[92:95]\n"
"v_mfma_f32_16x16x32_bf16 v[92:95], a[240:243], a[80:83], v[92:95]\n"
"v_mfma_f32_16x16x32_bf16 v[92:95], a[244:247], a[84:87], v[92:95]\n"
"v_mfma_f32_16x16x32_bf16 v[92:95], a[248:251], a[88:91], v[92:95]\n"
"global_load_dwordx4 v[84:87], v5, s[56:57] offset:1024 sc1\n"
"v_mfma_f32_16x16x32_bf16 v[92:95], a[252:255], a[92:95], v[92:95]\n"
"v_mfma_f32_16x16x32_bf16 v[100:103], a[224:227], a[96:99], 0\n"
"v_mfma_f32_16x16x32_bf16 v[100:103], a[228:231], a[100:103], v[100:103]\n"
"ds_write_addtid_b32 v92 offset:3072\n"
"v_mfma_f32_16x16x32_bf16 v[100:103], a[232:235], a[104:107], v[100:103]\n"
"s_waitcnt lgkmcnt(1)\n"
"ds_write_addtid_b32 v93 offset:7712\n"
"v_mfma_f32_16x16x32_bf16 v[100:103], a[236:239], a[108:111], v[100:103]\n"
"s_barrier\n"
"v_mfma_f32_16x16x32_bf16 v[100:103], a[240:243], a[112:115], v[100:103]\n"
"ds_write_addtid_b32 v94 offset:12416\n"
"ds_read_b128 v[112:115], v2 offset:4096\n"
"v_mfma_f32_16x16x32_bf16 v[100:103], a[244:247], a[116:119], v[100:103]\n"
"ds_write_addtid_b32 v95 offset:17056\n"
"global_load_dwordx4 v[88:91], v6, s[56:57] offset:0 sc1\n"
"v_mfma_f32_16x16x32_bf16 v[100:103], a[248:251], a[120:123], v[100:103]\n"
"ds_read_b128 v[104:107], v3 offset:512\n"
"ds_read_b128 v[120:123], v4 offset:1536\n"
"v_mfma_f32_16x16x32_bf16 v[100:103], a[252:255], a[124:127], v[100:103]\n"
"global_load_dwordx4 v[92:95], v6, s[56:57] offset:1024 sc1\n"
"ds_read_b128 v[124:127], v2 offset:4352\n"
"v_mfma_f32_16x16x32_bf16 v[132:135], a[128:131], a[224:227], 0\n"
"ds_read_b128 v[152:155], v3 offset:768\n"
"ds_read_b128 v[156:159], v4 offset:1792\n"
"v_mfma_f32_16x16x32_bf16 v[132:135], a[132:135], a[228:231], v[132:135]\n"
"ds_write_addtid_b32 v100 offset:3328\n"
"v_mfma_f32_16x16x32_bf16 v[132:135], a[136:139], a[232:235], v[132:135]\n"
"ds_write_addtid_b32 v101 offset:7968\n"
"v_mfma_f32_16x16x32_bf16 v[132:135], a[140:143], a[236:239], v[132:135]\n"
"ds_write_addtid_b32 v102 offset:12672\n"
"v_mfma_f32_16x16x32_bf16 v[132:135], a[144:147], a[240:243], v[132:135]\n"
"s_waitcnt lgkmcnt(6)\n"
"ds_write_addtid_b32 v103 offset:17312\n"
"global_load_dwordx4 v[96:99], v7, s[56:57] offset:0 sc1\n"
"v_mfma_f32_16x16x32_bf16 v[132:135], a[148:151], a[244:247], v[132:135]\n"
"v_add_f32 v112, v112, v104\n"
"v_add_f32 v113, v113, v105\n"
"v_mfma_f32_16x16x32_bf16 v[132:135], a[152:155], a[248:251], v[132:135]\n"
"v_add_f32 v114, v114, v106\n"
"v_add_f32 v115, v115, v107\n"
"v_mfma_f32_16x16x32_bf16 v[132:135], a[156:159], a[252:255], v[132:135]\n"
"v_add_f32 v112, v112, v120\n"
"v_add_f32 v113, v113, v121\n"
"v_mfma_f32_16x16x32_bf16 v[160:163], a[160:163], a[224:227], 0\n"
"v_add_f32 v114, v114, v122\n"
"v_add_f32 v115, v115, v123\n"
"v_mfma_f32_16x16x32_bf16 v[160:163], a[164:167], a[228:231], v[160:163]\n"
"ds_write_addtid_b32 v132 offset:3584\n"
"global_load_dwordx4 v[100:103], v7, s[56:57] offset:1024 sc1\n"
"v_mfma_f32_16x16x32_bf16 v[160:163], a[168:171], a[232:235], v[160:163]\n"
"ds_write_addtid_b32 v133 offset:8224\n"
"v_add_f32 v112, v112, v108\n"
"v_mfma_f32_16x16x32_bf16 v[160:163], a[172:175], a[236:239], v[160:163]\n"
"ds_write_addtid_b32 v134 offset:12928\n"
"v_add_f32 v113, v113, v109\n"
"v_mfma_f32_16x16x32_bf16 v[160:163], a[176:179], a[240:243], v[160:163]\n"
"ds_write_addtid_b32 v135 offset:17568\n"
"v_add_f32 v114, v114, v110\n"
"v_mfma_f32_16x16x32_bf16 v[160:163], a[180:183], a[244:247], v[160:163]\n"
"v_add_f32 v115, v115, v111\n"
"v_cvt_pk_bf16_f32 v164, v112, v113\n"
"v_mfma_f32_16x16x32_bf16 v[160:163], a[184:187], a[248:251], v[160:163]\n"
"global_load_dwordx4 v[104:107], v8, s[56:57] offset:0 sc1\n"
"v_cvt_pk_bf16_f32 v165, v114, v115\n"
"v_mfma_f32_16x16x32_bf16 v[160:163], a[188:191], a[252:255], v[160:163]\n"
"ds_read2st64_b32 v[120:121], v11 offset0:0 offset1:1\n"
"ds_read2st64_b32 v[122:123], v11 offset0:2 offset1:3\n"
"v_mfma_f32_16x16x32_bf16 v[112:115], a[224:227], a[0:3], 0\n"
"s_waitcnt lgkmcnt(10)\n"
"v_add_f32 v124, v124, v152\n"
"v_add_f32 v125, v125, v153\n"
"v_mfma_f32_16x16x32_bf16 v[112:115], a[228:231], a[4:7], v[112:115]\n"
"ds_write_addtid_b32 v160 offset:3840\n"
"v_add_f32 v126, v126, v154\n"
"v_mfma_f32_16x16x32_bf16 v[112:115], a[232:235], a[8:11], v[112:115]\n"
"ds_write_addtid_b32 v161 offset:8480\n"
"v_add_f32 v127, v127, v155\n"
"v_mfma_f32_16x16x32_bf16 v[112:115], a[236:239], a[12:15], v[112:115]\n"
"ds_write_addtid_b32 v162 offset:13184\n"
"global_load_dwordx4 v[108:111], v8, s[56:57] offset:1024 sc1\n"
"v_mfma_f32_16x16x32_bf16 v[112:115], a[240:243], a[16:19], v[112:115]\n"
"ds_write_addtid_b32 v163 offset:17824\n"
"v_add_f32 v124, v124, v156\n"
"v_mfma_f32_16x16x32_bf16 v[112:115], a[244:247], a[20:23], v[112:115]\n"
"v_add_f32 v125, v125, v157\n"
"v_add_f32 v126, v126, v158\n"
"v_mfma_f32_16x16x32_bf16 v[112:115], a[248:251], a[24:27], v[112:115]\n"
"v_add_f32 v127, v127, v159\n"
"v_add_f32 v124, v124, v116\n"
"v_mfma_f32_16x16x32_bf16 v[112:115], a[252:255], a[28:31], v[112:115]\n"
"v_add_f32 v125, v125, v117\n"
"v_add_f32 v126, v126, v118\n"
"v_mfma_f32_16x16x32_bf16 v[132:135], a[224:227], a[32:35], 0\n"
"s_add_u32 s58, s50, 0x100000\n"
"buffer_load_dwordx4 v[152:155], v9, s[40:43], s50 offen sc1\n"
"v_mfma_f32_16x16x32_bf16 v[132:135], a[228:231], a[36:39], v[132:135]\n"
"s_and_b32 s58, s58, 0x3ffffff\n"
"ds_write_addtid_b32 v112 offset:4096\n"
"v_add_f32 v127, v127, v119\n"
"v_mfma_f32_16x16x32_bf16 v[132:135], a[232:235], a[40:43], v[132:135]\n"
"s_add_u32 s56, s40, s58\n"
"ds_write_addtid_b32 v113 offset:8736\n"
"v_cvt_pk_bf16_f32 v166, v124, v125\n"
"v_mfma_f32_16x16x32_bf16 v[132:135], a[236:239], a[44:47], v[132:135]\n"
"s_addc_u32 s57, s41, 0\n"
"ds_write_addtid_b32 v114 offset:13440\n"
"v_cvt_pk_bf16_f32 v167, v126, v127\n"
"v_mfma_f32_16x16x32_bf16 v[132:135], a[240:243], a[48:51], v[132:135]\n"
"s_waitcnt vmcnt(9)\n"
"ds_write_addtid_b32 v115 offset:18080\n"
"v_cmp_ne_u64 vcc, s[48:49], v[146:147]\n"
"v_mfma_f32_16x16x32_bf16 v[132:135], a[244:247], a[52:55], v[132:135]\n"
"s_waitcnt lgkmcnt(8)\n"
"v_mfma_f32_16x16x32_bf16 v[156:159], v[120:123], v[164:167], v[20:23]\n"
"v_mfma_f32_16x16x32_bf16 v[132:135], a[248:251], a[56:59], v[132:135]\n"
"s_cbranch_vccnz .LQ_3s\n"
".LQ_3r:\n"
"v_mfma_f32_16x16x32_bf16 v[132:135], a[252:255], a[60:63], v[132:135]\n"
"s_branch .LQ_top\n"
".LQ_1s:\n"
"s_cmp_ge_u32 s50, s53\n"
"s_cbranch_scc1 .LQ_t0\n"
"s_cmp_ge_u32 s50, s52\n"
"s_cbranch_scc1 .LQ_1r\n"
"s_sub_u32 s60, s50, 0x100000\n"
"s_mov_b32 s61, 200000\n"
"s_nop 7\n"
".LQ_1p:\n"
"buffer_load_dwordx4 v[128:131], v9, s[40:43], s60 offen sc1\n"
"s_waitcnt vmcnt(0)\n"
"v_cmp_ne_u64 vcc, s[48:49], v[130:131]\n"
"s_cbranch_vccz .LQ_1px\n"
"s_sleep 1\n"
"s_sub_u32 s61, s61, 1\n"
"s_cbranch_scc0 .LQ_1p\n"
"s_branch .LQ_end\n"
".LQ_1px:\n"
"s_branch .LQ_1r\n"
".LQ_2s:\n"
"s_cmp_ge_u32 s50, s53\n"
"s_cbranch_scc1 .LQ_t1\n"
"s_cmp_ge_u32 s50, s52\n"
"s_cbranch_scc1 .LQ_2r\n"
"s_sub_u32 s60, s50, 0x100000\n"
"s_mov_b32 s61, 200000\n"
"s_nop 7\n"
".LQ_2p:\n"
"buffer_load_dwordx4 v[136:139], v10, s[40:43], s60 offen sc1\n"
"s_waitcnt vmcnt(0)\n"
"v_cmp_ne_u64 vcc, s[48:49], v[138:139]\n"
"s_cbranch_vccz .LQ_2px\n"
"s_sleep 1\n"
"s_sub_u32 s61, s61, 1\n"
"s_cbranch_scc0 .LQ_2p\n"
"s_branch .LQ_end\n"
".LQ_2px:\n"
"s_branch .LQ_2r\n"
".LQ_3s:\n"
"s_cmp_ge_u32 s50, s53\n"
"s_cbranch_scc1 .LQ_t2\n"
"s_cmp_ge_u32 s50, s52\n"
"s_cbranch_scc1 .LQ_3r\n"
"s_sub_u32 s60, s50, 0x100000\n"
"s_mov_b32 s61, 200000\n"
"s_nop 7\n"
".LQ_3p:\n"
"buffer_load_dwordx4 v[144:147], v9, s[40:43], s60 offen sc1\n"
"s_waitcnt vmcnt(0)\n"
"v_cmp_ne_u64 vcc, s[48:49], v[146:147]\n"
"s_cbranch_vccz .LQ_3px\n"
"s_sleep 1\n"
"s_sub_u32 s61, s61, 1\n"
"s_cbranch_scc0 .LQ_3p\n"
"s_branch .LQ_end\n"
".LQ_3px:\n"
"s_branch .LQ_3r\n"
".LQ_4s:\n"
"s_cmp_ge_u32 s50, s53\n"
"s_cbranch_scc1 .LQ_t3\n"
"s_cmp_ge_u32 s50, s52\n"
"s_cbranch_scc1 .LQ_4r\n"
"s_sub_u32 s60, s50, 0x100000\n"
"s_mov_b32 s61, 200000\n"
"s_nop 7\n"
".LQ_4p:\n"
"buffer_load_dwordx4 v[152:155], v9, s[40:43], s60 offen sc1\n"
"s_waitcnt vmcnt(0)\n"
"v_cmp_ne_u64 vcc, s[48:49], v[154:155]\n"
"s_cbranch_vccz .LQ_4px\n"
"s_sleep 1\n"
"s_sub_u32 s61, s61, 1\n"
"s_cbranch_scc0 .LQ_4p\n"
"s_branch .LQ_end\n"
".LQ_4px:\n"
"s_branch .LQ_4r\n"
".LQ_fs:\n"
"s_nop 7\n"
"v_mov_b32 v156, 0xff800000\n"
"v_mov_b32 v157, 0xff800000\n"
"v_mov_b32 v158, 0xff800000\n"
"v_mov_b32 v159, 0xff800000\n"
"v_mov_b32 v142, 0\n"
"v_mov_b32 v143, 0\n"
"v_mov_b32 v150, 0\n"
"v_mov_b32 v151, 0\n"
"s_add_u32 s62, s62, 0x4000000\n"
"s_branch .LQ_fr\n"
".LQ_ft0k7s:\n"
"s_nop 7\n"
"v_mov_b32 v156, 0xff800000\n"
"v_mov_b32 v157, 0xff800000\n"
"v_mov_b32 v158, 0xff800000\n"
"v_mov_b32 v159, 0xff800000\n"
"v_mov_b32 v142, 0\n"
"v_mov_b32 v143, 0\n"
"v_mov_b32 v150, 0\n"
"v_mov_b32 v151, 0\n"
"s_add_u32 s62, s62, 0x4000000\n"
"s_branch .LQ_ft0k7r\n"
".LQ_ft1k7s:\n"
"s_nop 7\n"
"v_mov_b32 v156, 0xff800000\n"
"v_mov_b32 v157, 0xff800000\n"
"v_mov_b32 v158, 0xff800000\n"
"v_mov_b32 v159, 0xff800000\n"
"v_mov_b32 v142, 0\n"
"v_mov_b32 v143, 0\n"
"v_mov_b32 v150, 0\n"
"v_mov_b32 v151, 0\n"
"s_add_u32 s62, s62, 0x4000000\n"
"s_branch .LQ_ft1k7r\n"
".LQ_ft2k7s:\n"
"s_nop 7\n"
"v_mov_b32 v156, 0xff800000\n"
"v_mov_b32 v157, 0xff800000\n"
"v_mov_b32 v158, 0xff800000\n"
"v_mov_b32 v159, 0xff800000\n"
"v_mov_b32 v142, 0\n"
"v_mov_b32 v143, 0\n"
"v_mov_b32 v150, 0\n"
"v_mov_b32 v151, 0\n"
"s_add_u32 s62, s62, 0x4000000\n"
"s_branch .LQ_ft2k7r\n"
".LQ_ft3k0s:\n"
"s_nop 7\n"
"v_mov_b32 v156, 0xff800000\n"
"v_mov_b32 v157, 0xff800000\n"
"v_mov_b32 v158, 0xff800000\n"
"v_mov_b32 v159, 0xff800000\n"
"v_mov_b32 v142, 0\n"
"v_mov_b32 v143, 0\n"
"v_mov_b32 v150, 0\n"
"v_mov_b32 v151, 0\n"
"s_add_u32 s62, s62, 0x4000000\n"
"s_branch .LQ_ft3k0r\n"
".LQ_t0:\n"
"v_cvt_pk_bf16_f32 v62, v68, v69\n"
"v_mfma_f32_16x16x32_bf16 v[112:115], v[108:111], a[188:191], v[112:115]\n"
"s_waitcnt vmcnt(17)\n"
"v_cvt_pk_bf16_f32 v63, v70, v71\n"
"v_mfma_f32_16x16x32_bf16 v[64:67], a[192:195], a[64:67], 0\n"
"ds_write_addtid_b32 v62 offset:19104\n"
"v_mfma_f32_16x16x32_bf16 v[64:67], a[196:199], a[68:71], v[64:67]\n"
"s_waitcnt vmcnt(15)\n"
"ds_write_addtid_b32 v63 offset:19360\n"
"v_mfma_f32_16x16x32_bf16 v[64:67], a[200:203], a[72:75], v[64:67]\n"
"v_mfma_f32_16x16x32_bf16 v[64:67], a[204:207], a[76:79], v[64:67]\n"
"s_waitcnt vmcnt(13)\n"
"v_mul_f32 v249, 0x3e8293ee, v144\n"
"v_mfma_f32_16x16x32_bf16 v[64:67], a[208:211], a[80:83], v[64:67]\n"
"v_mul_f32 v110, v145, v30\n"
"v_mul_f32 v111, v145, v31\n"
"v_mfma_f32_16x16x32_bf16 v[64:67], a[212:215], a[84:87], v[64:67]\n"
"s_waitcnt vmcnt(11)\n"
"v_mul_f32 v109, v145, v29\n"
"v_mfma_f32_16x16x32_bf16 v[64:67], a[216:219], a[88:91], v[64:67]\n"
"v_mul_f32 v119, v145, v35\n"
"v_mul_f32 v108, v145, v28\n"
"v_mfma_f32_16x16x32_bf16 v[64:67], a[220:223], a[92:95], v[64:67]\n"
"s_waitcnt vmcnt(7)\n"
"buffer_store_dwordx2 v[0:1], v14, s[40:43], s94 offen offset:0 sc1\n"
"v_mul_f32 v118, v145, v34\n"
"v_mfma_f32_16x16x32_bf16 v[68:71], a[192:195], a[96:99], 0\n"
"s_waitcnt lgkmcnt(0)\n"
"v_mul_f32 v116, v145, v32\n"
"v_mul_f32 v117, v145, v33\n"
"v_mfma_f32_16x16x32_bf16 v[68:71], a[196:199], a[100:103], v[68:71]\n"
"s_barrier\n"
"v_mfma_f32_16x16x32_bf16 v[68:71], a[200:203], a[104:107], v[68:71]\n"
"ds_write_addtid_b32 v64 offset:2048\n"
"v_mfma_f32_16x16x32_bf16 v[68:71], a[204:207], a[108:111], v[68:71]\n"
"ds_write_addtid_b32 v65 offset:6688\n"
"ds_read_b128 v[76:79], v2 offset:2560\n"
"v_mfma_f32_16x16x32_bf16 v[68:71], a[208:211], a[112:115], v[68:71]\n"
"ds_write_addtid_b32 v66 offset:11392\n"
"ds_read_b128 v[80:83], v3 offset:3584\n"
"v_mfma_f32_16x16x32_bf16 v[68:71], a[212:215], a[116:119], v[68:71]\n"
"ds_write_addtid_b32 v67 offset:16032\n"
"ds_read_b128 v[84:87], v4 offset:0\n"
"v_mfma_f32_16x16x32_bf16 v[68:71], a[216:219], a[120:123], v[68:71]\n"
"ds_read_b128 v[88:91], v2 offset:2816\n"
"ds_read_b128 v[92:95], v3 offset:3840\n"
"v_mfma_f32_16x16x32_bf16 v[68:71], a[220:223], a[124:127], v[68:71]\n"
"ds_read_b128 v[96:99], v4 offset:256\n"
"v_mfma_f32_16x16x32_bf16 v[100:103], a[128:131], a[192:195], 0\n"
"v_mfma_f32_16x16x32_bf16 v[100:103], a[132:135], a[196:199], v[100:103]\n"
"ds_write_addtid_b32 v68 offset:2304\n"
"v_mfma_f32_16x16x32_bf16 v[100:103], a[136:139], a[200:203], v[100:103]\n"
"ds_write_addtid_b32 v69 offset:6944\n"
"v_mfma_f32_16x16x32_bf16 v[100:103], a[140:143], a[204:207], v[100:103]\n"
"s_waitcnt lgkmcnt(5)\n"
"ds_write_addtid_b32 v70 offset:11648\n"
"v_add_f32 v76, v76, v80\n"
"v_mfma_f32_16x16x32_bf16 v[100:103], a[144:147], a[208:211], v[100:103]\n"
"ds_write_addtid_b32 v71 offset:16288\n"
"v_mfma_f32_16x16x32_bf16 v[100:103], a[148:151], a[212:215], v[100:103]\n"
"v_add_f32 v77, v77, v81\n"
"v_add_f32 v78, v78, v82\n"
"v_mfma_f32_16x16x32_bf16 v[100:103], a[152:155], a[216:219], v[100:103]\n"
"v_add_f32 v79, v79, v83\n"
"v_add_f32 v76, v76, v84\n"
"v_mfma_f32_16x16x32_bf16 v[100:103], a[156:159], a[220:223], v[100:103]\n"
"v_add_f32 v77, v77, v85\n"
"v_add_f32 v78, v78, v86\n"
"v_mfma_f32_16x16x32_bf16 v[80:83], a[160:163], a[192:195], 0\n"
"v_add_f32 v79, v79, v87\n"
"v_add_f32 v76, v76, v72\n"
"v_mfma_f32_16x16x32_bf16 v[80:83], a[164:167], a[196:199], v[80:83]\n"
"ds_write_addtid_b32 v100 offset:2560\n"
"v_mfma_f32_16x16x32_bf16 v[80:83], a[168:171], a[200:203], v[80:83]\n"
"ds_write_addtid_b32 v101 offset:7200\n"
"v_add_f32 v77, v77, v73\n"
"v_mfma_f32_16x16x32_bf16 v[80:83], a[172:175], a[204:207], v[80:83]\n"
"ds_write_addtid_b32 v102 offset:11904\n"
"v_add_f32 v78, v78, v74\n"
"v_mfma_f32_16x16x32_bf16 v[80:83], a[176:179], a[208:211], v[80:83]\n"
"ds_write_addtid_b32 v103 offset:16544\n"
"v_add_f32 v79, v79, v75\n"
"v_mfma_f32_16x16x32_bf16 v[80:83], a[180:183], a[212:215], v[80:83]\n"
"v_mul_f32_dpp v76, v253, v76 row_newbcast:0 row_mask:0xf bank_mask:0xf\n"
"v_mul_f32_dpp v77, v253, v77 row_newbcast:1 row_mask:0xf bank_mask:0xf\n"
"v_mfma_f32_16x16x32_bf16 v[80:83], a[184:187], a[216:219], v[80:83]\n"
"v_mul_f32_dpp v78, v253, v78 row_newbcast:2 row_mask:0xf bank_mask:0xf\n"
"v_mfma_f32_16x16x32_bf16 v[80:83], a[188:191], a[220:223], v[80:83]\n"
"v_mul_f32_dpp v79, v253, v79 row_newbcast:3 row_mask:0xf bank_mask:0xf\n"
"v_cvt_pk_bf16_f32 v142, v76, v77\n"
"v_mfma_f32_16x16x32_bf16 v[108:111], a[0:3], a[192:195], v[108:111]\n"
"v_cvt_pk_bf16_f32 v143, v78, v79\n"
"v_mfma_f32_16x16x32_bf16 v[108:111], a[4:7], a[196:199], v[108:111]\n"
"ds_write_addtid_b32 v80 offset:2816\n"
"ds_write_addtid_b32 v142 offset:19616\n"
"v_mfma_f32_16x16x32_bf16 v[108:111], a[8:11], a[200:203], v[108:111]\n"
"ds_write_addtid_b32 v81 offset:7456\n"
"ds_write_addtid_b32 v143 offset:19872\n"
"v_mfma_f32_16x16x32_bf16 v[108:111], a[12:15], a[204:207], v[108:111]\n"
"s_waitcnt lgkmcnt(12)\n"
"ds_write_addtid_b32 v82 offset:12160\n"
"v_mfma_f32_16x16x32_bf16 v[108:111], a[16:19], a[208:211], v[108:111]\n"
"ds_write_addtid_b32 v83 offset:16800\n"
"v_add_f32 v88, v88, v92\n"
"v_mfma_f32_16x16x32_bf16 v[108:111], a[20:23], a[212:215], v[108:111]\n"
"v_add_f32 v89, v89, v93\n"
"v_add_f32 v90, v90, v94\n"
"v_mfma_f32_16x16x32_bf16 v[108:111], a[24:27], a[216:219], v[108:111]\n"
"v_add_f32 v91, v91, v95\n"
"v_add_f32 v88, v88, v96\n"
"v_mfma_f32_16x16x32_bf16 v[108:111], a[28:31], a[220:223], v[108:111]\n"
"v_add_f32 v89, v89, v97\n"
"v_add_f32 v90, v90, v98\n"
"v_mfma_f32_16x16x32_bf16 v[116:119], a[32:35], a[192:195], v[116:119]\n"
"v_add_f32 v91, v91, v99\n"
"v_add_f32 v88, v88, v112\n"
"v_mfma_f32_16x16x32_bf16 v[116:119], a[36:39], a[196:199], v[116:119]\n"
"v_add_f32 v89, v89, v113\n"
"v_add_f32 v90, v90, v114\n"
"v_mfma_f32_16x16x32_bf16 v[116:119], a[40:43], a[200:203], v[116:119]\n"
"v_mfma_f32_16x16x32_bf16 v[116:119], a[44:47], a[204:207], v[116:119]\n"
"v_add_f32 v91, v91, v115\n"
"v_mul_f32_dpp v88, v253, v88 row_newbcast:0 row_mask:0xf bank_mask:0xf\n"
"v_mfma_f32_16x16x32_bf16 v[116:119], a[48:51], a[208:211], v[116:119]\n"
"v_mul_f32_dpp v89, v253, v89 row_newbcast:1 row_mask:0xf bank_mask:0xf\n"
"v_mul_f32_dpp v90, v253, v90 row_newbcast:2 row_mask:0xf bank_mask:0xf\n"
"v_mfma_f32_16x16x32_bf16 v[116:119], a[52:55], a[212:215], v[116:119]\n"
"v_mul_f32_dpp v91, v253, v91 row_newbcast:3 row_mask:0xf bank_mask:0xf\n"
"v_cvt_pk_bf16_f32 v150, v88, v89\n"
"v_mfma_f32_16x16x32_bf16 v[116:119], a[56:59], a[216:219], v[116:119]\n"
"s_waitcnt vmcnt(1)\n"
"v_cvt_pk_bf16_f32 v151, v90, v91\n"
"v_mfma_f32_16x16x32_bf16 v[116:119], a[60:63], a[220:223], v[116:119]\n"
"s_waitcnt vmcnt(9)\n"
"ds_write_addtid_b32 v150 offset:20128\n"
"ds_write_addtid_b32 v151 offset:20384\n"
"v_mfma_f32_16x16x32_bf16 v[92:95], a[224:227], a[64:67], 0\n"
"v_mfma_f32_16x16x32_bf16 v[92:95], a[228:231], a[68:71], v[92:95]\n"
"s_waitcnt vmcnt(6)\n"
"v_mfma_f32_16x16x32_bf16 v[92:95], a[232:235], a[72:75], v[92:95]\n"
"v_mfma_f32_16x16x32_bf16 v[92:95], a[236:239], a[76:79], v[92:95]\n"
"s_waitcnt vmcnt(4)\n"
"v_mfma_f32_16x16x32_bf16 v[92:95], a[240:243], a[80:83], v[92:95]\n"
"v_mfma_f32_16x16x32_bf16 v[92:95], a[244:247], a[84:87], v[92:95]\n"
"s_waitcnt vmcnt(2)\n"
"v_mfma_f32_16x16x32_bf16 v[92:95], a[248:251], a[88:91], v[92:95]\n"
"v_mfma_f32_16x16x32_bf16 v[92:95], a[252:255], a[92:95], v[92:95]\n"
"v_mfma_f32_16x16x32_bf16 v[100:103], a[224:227], a[96:99], 0\n"
"v_mfma_f32_16x16x32_bf16 v[100:103], a[228:231], a[100:103], v[100:103]\n"
"ds_write_addtid_b32 v92 offset:3072\n"
"v_mfma_f32_16x16x32_bf16 v[100:103], a[232:235], a[104:107], v[100:103]\n"
"s_waitcnt lgkmcnt(1)\n"
"ds_write_addtid_b32 v93 offset:7712\n"
"v_mfma_f32_16x16x32_bf16 v[100:103], a[236:239], a[108:111], v[100:103]\n"
"s_barrier\n"
"v_mfma_f32_16x16x32_bf16 v[100:103], a[240:243], a[112:115], v[100:103]\n"
"ds_write_addtid_b32 v94 offset:12416\n"
"ds_read_b128 v[112:115], v2 offset:4096\n"
"v_mfma_f32_16x16x32_bf16 v[100:103], a[244:247], a[116:119], v[100:103]\n"
"ds_write_addtid_b32 v95 offset:17056\n"
"v_mfma_f32_16x16x32_bf16 v[100:103], a[248:251], a[120:123], v[100:103]\n"
"ds_read_b128 v[104:107], v3 offset:512\n"
"ds_read_b128 v[120:123], v4 offset:1536\n"
"v_mfma_f32_16x16x32_bf16 v[100:103], a[252:255], a[124:127], v[100:103]\n"
"ds_read_b128 v[124:127], v2 offset:4352\n"
"v_mfma_f32_16x16x32_bf16 v[132:135], a[128:131], a[224:227], 0\n"
"ds_read_b128 v[152:155], v3 offset:768\n"
"ds_read_b128 v[156:159], v4 offset:1792\n"
"v_mfma_f32_16x16x32_bf16 v[132:135], a[132:135], a[228:231], v[132:135]\n"
"ds_write_addtid_b32 v100 offset:3328\n"
"v_mfma_f32_16x16x32_bf16 v[132:135], a[136:139], a[232:235], v[132:135]\n"
"ds_write_addtid_b32 v101 offset:7968\n"
"v_mfma_f32_16x16x32_bf16 v[132:135], a[140:143], a[236:239], v[132:135]\n"
"ds_write_addtid_b32 v102 offset:12672\n"
"v_mfma_f32_16x16x32_bf16 v[132:135], a[144:147], a[240:243], v[132:135]\n"
"s_waitcnt lgkmcnt(6)\n"
"ds_write_addtid_b32 v103 offset:17312\n"
"v_mfma_f32_16x16x32_bf16 v[132:135], a[148:151], a[244:247], v[132:135]\n"
"v_add_f32 v112, v112, v104\n"
"v_add_f32 v113, v113, v105\n"
"v_mfma_f32_16x16x32_bf16 v[132:135], a[152:155], a[248:251], v[132:135]\n"
"v_add_f32 v114, v114, v106\n"
"v_add_f32 v115, v115, v107\n"
"v_mfma_f32_16x16x32_bf16 v[132:135], a[156:159], a[252:255], v[132:135]\n"
"v_add_f32 v112, v112, v120\n"
"v_add_f32 v113, v113, v121\n"
"v_mfma_f32_16x16x32_bf16 v[160:163], a[160:163], a[224:227], 0\n"
"v_add_f32 v114, v114, v122\n"
"v_add_f32 v115, v115, v123\n"
"v_mfma_f32_16x16x32_bf16 v[160:163], a[164:167], a[228:231], v[160:163]\n"
"ds_write_addtid_b32 v132 offset:3584\n"
"v_mfma_f32_16x16x32_bf16 v[160:163], a[168:171], a[232:235], v[160:163]\n"
"ds_write_addtid_b32 v133 offset:8224\n"
"v_add_f32 v112, v112, v108\n"
"v_mfma_f32_16x16x32_bf16 v[160:163], a[172:175], a[236:239], v[160:163]\n"
"ds_write_addtid_b32 v134 offset:12928\n"
"v_add_f32 v113, v113, v109\n"
"v_mfma_f32_16x16x32_bf16 v[160:163], a[176:179], a[240:243], v[160:163]\n"
"ds_write_addtid_b32 v135 offset:17568\n"
"v_add_f32 v114, v114, v110\n"
"v_mfma_f32_16x16x32_bf16 v[160:163], a[180:183], a[244:247], v[160:163]\n"
"v_add_f32 v115, v115, v111\n"
"v_cvt_pk_bf16_f32 v164, v112, v113\n"
"v_mfma_f32_16x16x32_bf16 v[160:163], a[184:187], a[248:251], v[160:163]\n"
"v_cvt_pk_bf16_f32 v165, v114, v115\n"
"v_mfma_f32_16x16x32_bf16 v[160:163], a[188:191], a[252:255], v[160:163]\n"
"ds_read2st64_b32 v[120:121], v11 offset0:0 offset1:1\n"
"ds_read2st64_b32 v[122:123], v11 offset0:2 offset1:3\n"
"v_mfma_f32_16x16x32_bf16 v[112:115], a[224:227], a[0:3], 0\n"
"s_waitcnt lgkmcnt(10)\n"
"v_add_f32 v124, v124, v152\n"
"v_add_f32 v125, v125, v153\n"
"v_mfma_f32_16x16x32_bf16 v[112:115], a[228:231], a[4:7], v[112:115]\n"
"ds_write_addtid_b32 v160 offset:3840\n"
"v_add_f32 v126, v126, v154\n"
"v_mfma_f32_16x16x32_bf16 v[112:115], a[232:235], a[8:11], v[112:115]\n"
"ds_write_addtid_b32 v161 offset:8480\n"
"v_add_f32 v127, v127, v155\n"
"v_mfma_f32_16x16x32_bf16 v[112:115], a[236:239], a[12:15], v[112:115]\n"
"ds_write_addtid_b32 v162 offset:13184\n"
"v_mfma_f32_16x16x32_bf16 v[112:115], a[240:243], a[16:19], v[112:115]\n"
"ds_write_addtid_b32 v163 offset:17824\n"
"v_add_f32 v124, v124, v156\n"
"v_mfma_f32_16x16x32_bf16 v[112:115], a[244:247], a[20:23], v[112:115]\n"
"v_add_f32 v125, v125, v157\n"
"v_add_f32 v126, v126, v158\n"
"v_mfma_f32_16x16x32_bf16 v[112:115], a[248:251], a[24:27], v[112:115]\n"
"v_add_f32 v127, v127, v159\n"
"v_add_f32 v124, v124, v116\n"
"v_mfma_f32_16x16x32_bf16 v[112:115], a[252:255], a[28:31], v[112:115]\n"
"v_add_f32 v125, v125, v117\n"
"v_add_f32 v126, v126, v118\n"
"v_mfma_f32_16x16x32_bf16 v[132:135], a[224:227], a[32:35], 0\n"
"v_mfma_f32_16x16x32_bf16 v[132:135], a[228:231], a[36:39], v[132:135]\n"
"ds_write_addtid_b32 v112 offset:4096\n"
"v_add_f32 v127, v127, v119\n"
"v_mfma_f32_16x16x32_bf16 v[132:135], a[232:235], a[40:43], v[132:135]\n"
"ds_write_addtid_b32 v113 offset:8736\n"
"v_cvt_pk_bf16_f32 v166, v124, v125\n"
"v_mfma_f32_16x16x32_bf16 v[132:135], a[236:239], a[44:47], v[132:135]\n"
"ds_write_addtid_b32 v114 offset:13440\n"
"v_cvt_pk_bf16_f32 v167, v126, v127\n"
"v_mfma_f32_16x16x32_bf16 v[132:135], a[240:243], a[48:51], v[132:135]\n"
"ds_write_addtid_b32 v115 offset:18080\n"
"v_mfma_f32_16x16x32_bf16 v[132:135], a[244:247], a[52:55], v[132:135]\n"
"s_waitcnt lgkmcnt(8)\n"
"v_mfma_f32_16x16x32_bf16 v[156:159], v[120:123], v[164:167], v[20:23]\n"
"v_mfma_f32_16x16x32_bf16 v[132:135], a[248:251], a[56:59], v[132:135]\n"
"v_mfma_f32_16x16x32_bf16 v[132:135], a[252:255], a[60:63], v[132:135]\n"
"s_nop 7\n"
"ds_write_addtid_b32 v132 offset:4352\n"
"ds_write_addtid_b32 v133 offset:8992\n"
"ds_write_addtid_b32 v134 offset:13696\n"
"ds_write_addtid_b32 v135 offset:18336\n"
"s_waitcnt lgkmcnt(0)\n"
"s_barrier\n"
"ds_read2st64_b32 v[128:129], v12 offset0:0 offset1:1\n"
"ds_read2st64_b32 v[130:131], v12 offset0:2 offset1:3\n"
"ds_read2st64_b32 v[140:141], v11 offset0:4 offset1:5\n"
"ds_read2st64_b32 v[148:149], v11 offset0:6 offset1:7\n"
"s_add_u32 s94, s50, 0xfff00000\n"
"s_and_b32 s95, s94, 0xfbffffff\n"
"s_waitcnt lgkmcnt(2)\n"
"v_mfma_f32_16x16x32_bf16 v[132:135], v[128:131], v[164:167], v[16:19]\n"
"s_cmp_eq_u32 s94, s62\n"
"s_cbranch_scc1 .LQ_ft0k7s\n"
".LQ_ft0k7r:\n"
"v_max_f32 v253, v156, v158\n"
"v_max_f32 v252, v157, v159\n"
"s_nop 3\n"
"v_max3_f32 v253, v132, v134, v253\n"
"v_max3_f32 v252, v133, v135, v252\n"
"v_max_f32 v251, v253, v252\n"
"v_mov_b32 v250, v251\n"
"s_nop 1\n"
"v_permlane32_swap_b32 v250, v251\n"
"v_max_f32 v251, v250, v251\n"
"v_mov_b32 v250, v251\n"
"s_nop 1\n"
"v_permlane16_swap_b32 v250, v251\n"
"v_max_f32 v251, v250, v251\n"
"v_mul_f32 v251, v251, v249\n"
"v_fma_f32 v253, v253, v249, -v251\n"
"v_fma_f32 v252, v252, v249, -v251\n"
"v_exp_f32 v253, v253\n"
"v_exp_f32 v252, v252\n"
"s_nop 0\n"
"v_add_f32 v251, v253, v252\n"
"v_mov_b32 v250, v251\n"
"s_nop 1\n"
"v_permlane32_swap_b32 v250, v251\n"
"v_add_f32 v251, v250, v251\n"
"v_mov_b32 v250, v251\n"
"s_nop 1\n"
"v_permlane16_swap_b32 v250, v251\n"
"v_add_f32 v251, v250, v251\n"
"v_rcp_f32 v251, v251\n"
"s_nop 0\n"
"v_mul_f32 v253, v253, v251\n"
"v_mul_f32 v252, v252, v251\n"
"v_cvt_pk_bf16_f32 v250, v253, v252\n"
"s_waitcnt lgkmcnt(0)\n"
"v_and_b32 v52, v250, v24\n"
"v_and_b32 v53, v250, v25\n"
"v_and_b32 v54, v250, v26\n"
"v_and_b32 v55, v250, v27\n"
"s_nop 1\n"
"v_mfma_f32_16x16x32_bf16 v[56:59], v[140:143], v[52:55], 0\n"
"v_mfma_f32_16x16x32_bf16 v[60:63], v[148:151], v[52:55], 0\n"
"s_nop 6\n"
"v_cvt_pk_bf16_f32 v52, v56, v57\n"
"v_cvt_pk_bf16_f32 v53, v58, v59\n"
"v_cvt_pk_bf16_f32 v54, v60, v61\n"
"v_cvt_pk_bf16_f32 v55, v62, v63\n"
"buffer_store_dwordx4 v[52:55], v13, s[40:43], s95 offen sc1\n"
"s_barrier\n"
"s_waitcnt vmcnt(0)\n"
"buffer_store_dwordx2 v[0:1], v14, s[40:43], s94 offen offset:0 sc1\n"
"s_branch .LQ_end\n"
".LQ_t1:\n"
"v_mfma_f32_16x16x32_bf16 v[92:95], a[228:231], a[68:71], v[92:95]\n"
"s_waitcnt vmcnt(15)\n"
"v_mfma_f32_16x16x32_bf16 v[92:95], a[232:235], a[72:75], v[92:95]\n"
"v_mfma_f32_16x16x32_bf16 v[92:95], a[236:239], a[76:79], v[92:95]\n"
"s_waitcnt vmcnt(13)\n"
"v_mfma_f32_16x16x32_bf16 v[92:95], a[240:243], a[80:83], v[92:95]\n"
"v_mfma_f32_16x16x32_bf16 v[92:95], a[244:247], a[84:87], v[92:95]\n"
"s_waitcnt vmcnt(11)\n"
"v_mfma_f32_16x16x32_bf16 v[92:95], a[248:251], a[88:91], v[92:95]\n"
"v_mfma_f32_16x16x32_bf16 v[92:95], a[252:255], a[92:95], v[92:95]\n"
"v_mfma_f32_16x16x32_bf16 v[100:103], a[224:227], a[96:99], 0\n"
"v_mfma_f32_16x16x32_bf16 v[100:103], a[228:231], a[100:103], v[100:103]\n"
"ds_write_addtid_b32 v92 offset:3072\n"
"v_mfma_f32_16x16x32_bf16 v[100:103], a[232:235], a[104:107], v[100:103]\n"
"s_waitcnt lgkmcnt(1)\n"
"ds_write_addtid_b32 v93 offset:7712\n"
"v_mfma_f32_16x16x32_bf16 v[100:103], a[236:239], a[108:111], v[100:103]\n"
"s_barrier\n"
"v_mfma_f32_16x16x32_bf16 v[100:103], a[240:243], a[112:115], v[100:103]\n"
"ds_write_addtid_b32 v94 offset:12416\n"
"ds_read_b128 v[112:115], v2 offset:4096\n"
"v_mfma_f32_16x16x32_bf16 v[100:103], a[244:247], a[116:119], v[100:103]\n"
"ds_write_addtid_b32 v95 offset:17056\n"
"v_mfma_f32_16x16x32_bf16 v[100:103], a[248:251], a[120:123], v[100:103]\n"
"ds_read_b128 v[104:107], v3 offset:512\n"
"ds_read_b128 v[120:123], v4 offset:1536\n"
"v_mfma_f32_16x16x32_bf16 v[100:103], a[252:255], a[124:127], v[100:103]\n"
"ds_read_b128 v[124:127], v2 offset:4352\n"
"v_mfma_f32_16x16x32_bf16 v[132:135], a[128:131], a[224:227], 0\n"
"ds_read_b128 v[152:155], v3 offset:768\n"
"ds_read_b128 v[156:159], v4 offset:1792\n"
"v_mfma_f32_16x16x32_bf16 v[132:135], a[132:135], a[228:231], v[132:135]\n"
"ds_write_addtid_b32 v100 offset:3328\n"
"v_mfma_f32_16x16x32_bf16 v[132:135], a[136:139], a[232:235], v[132:135]\n"
"ds_write_addtid_b32 v101 offset:7968\n"
"v_mfma_f32_16x16x32_bf16 v[132:135], a[140:143], a[236:239], v[132:135]\n"
"ds_write_addtid_b32 v102 offset:12672\n"
"v_mfma_f32_16x16x32_bf16 v[132:135], a[144:147], a[240:243], v[132:135]\n"
"s_waitcnt lgkmcnt(6)\n"
"ds_write_addtid_b32 v103 offset:17312\n"
"v_mfma_f32_16x16x32_bf16 v[132:135], a[148:151], a[244:247], v[132:135]\n"
"v_add_f32 v112, v112, v104\n"
"v_add_f32 v113, v113, v105\n"
"v_mfma_f32_16x16x32_bf16 v[132:135], a[152:155], a[248:251], v[132:135]\n"
"v_add_f32 v114, v114, v106\n"
"v_add_f32 v115, v115, v107\n"
"v_mfma_f32_16x16x32_bf16 v[132:135], a[156:159], a[252:255], v[132:135]\n"
"v_add_f32 v112, v112, v120\n"
"v_add_f32 v113, v113, v121\n"
"v_mfma_f32_16x16x32_bf16 v[160:163], a[160:163], a[224:227], 0\n"
"v_add_f32 v114, v114, v122\n"
"v_add_f32 v115, v115, v123\n"
"v_mfma_f32_16x16x32_bf16 v[160:163], a[164:167], a[228:231], v[160:163]\n"
"ds_write_addtid_b32 v132 offset:3584\n"
"v_mfma_f32_16x16x32_bf16 v[160:163], a[168:171], a[232:235], v[160:163]\n"
"ds_write_addtid_b32 v133 offset:8224\n"
"v_add_f32 v112, v112, v108\n"
"v_mfma_f32_16x16x32_bf16 v[160:163], a[172:175], a[236:239], v[160:163]\n"
"ds_write_addtid_b32 v134 offset:12928\n"
"v_add_f32 v113, v113, v109\n"
"v_mfma_f32_16x16x32_bf16 v[160:163], a[176:179], a[240:243], v[160:163]\n"
"ds_write_addtid_b32 v135 offset:17568\n"
"v_add_f32 v114, v114, v110\n"
"v_mfma_f32_16x16x32_bf16 v[160:163], a[180:183], a[244:247], v[160:163]\n"
"v_add_f32 v115, v115, v111\n"
"v_cvt_pk_bf16_f32 v164, v112, v113\n"
"v_mfma_f32_16x16x32_bf16 v[160:163], a[184:187], a[248:251], v[160:163]\n"
"v_cvt_pk_bf16_f32 v165, v114, v115\n"
"v_mfma_f32_16x16x32_bf16 v[160:163], a[188:191], a[252:255], v[160:163]\n"
"ds_read2st64_b32 v[120:121], v11 offset0:0 offset1:1\n"
"ds_read2st64_b32 v[122:123], v11 offset0:2 offset1:3\n"
"v_mfma_f32_16x16x32_bf16 v[112:115], a[224:227], a[0:3], 0\n"
"s_waitcnt lgkmcnt(10)\n"
"v_add_f32 v124, v124, v152\n"
"v_add_f32 v125, v125, v153\n"
"v_mfma_f32_16x16x32_bf16 v[112:115], a[228:231], a[4:7], v[112:115]\n"
"ds_write_addtid_b32 v160 offset:3840\n"
"v_add_f32 v126, v126, v154\n"
"v_mfma_f32_16x16x32_bf16 v[112:115], a[232:235], a[8:11], v[112:115]\n"
"ds_write_addtid_b32 v161 offset:8480\n"
"v_add_f32 v127, v127, v155\n"
"v_mfma_f32_16x16x32_bf16 v[112:115], a[236:239], a[12:15], v[112:115]\n"
"ds_write_addtid_b32 v162 offset:13184\n"
"v_mfma_f32_16x16x32_bf16 v[112:115], a[240:243], a[16:19], v[112:115]\n"
"ds_write_addtid_b32 v163 offset:17824\n"
"v_add_f32 v124, v124, v156\n"
"v_mfma_f32_16x16x32_bf16 v[112:115], a[244:247], a[20:23], v[112:115]\n"
"v_add_f32 v125, v125, v157\n"
"v_add_f32 v126, v126, v158\n"
"v_mfma_f32_16x16x32_bf16 v[112:115], a[248:251], a[24:27], v[112:115]\n"
"v_add_f32 v127, v127, v159\n"
"v_add_f32 v124, v124, v116\n"
"v_mfma_f32_16x16x32_bf16 v[112:115], a[252:255], a[28:31], v[112:115]\n"
"v_add_f32 v125, v125, v117\n"
"v_add_f32 v126, v126, v118\n"
"v_mfma_f32_16x16x32_bf16 v[132:135], a[224:227], a[32:35], 0\n"
"v_mfma_f32_16x16x32_bf16 v[132:135], a[228:231], a[36:39], v[132:135]\n"
"ds_write_addtid_b32 v112 offset:4096\n"
"v_add_f32 v127, v127, v119\n"
"v_mfma_f32_16x16x32_bf16 v[132:135], a[232:235], a[40:43], v[132:135]\n"
"ds_write_addtid_b32 v113 offset:8736\n"
"v_cvt_pk_bf16_f32 v166, v124, v125\n"
"v_mfma_f32_16x16x32_bf16 v[132:135], a[236:239], a[44:47], v[132:135]\n"
"ds_write_addtid_b32 v114 offset:13440\n"
"v_cvt_pk_bf16_f32 v167, v126, v127\n"
"v_mfma_f32_16x16x32_bf16 v[132:135], a[240:243], a[48:51], v[132:135]\n"
"s_waitcnt vmcnt(0)\n"
"ds_write_addtid_b32 v115 offset:18080\n"
"v_mfma_f32_16x16x32_bf16 v[132:135], a[244:247], a[52:55], v[132:135]\n"
"s_waitcnt lgkmcnt(8)\n"
"v_mfma_f32_16x16x32_bf16 v[156:159], v[120:123], v[164:167], v[20:23]\n"
"v_mfma_f32_16x16x32_bf16 v[132:135], a[248:251], a[56:59], v[132:135]\n"
"v_mfma_f32_16x16x32_bf16 v[132:135], a[252:255], a[60:63], v[132:135]\n"
"s_waitcnt vmcnt(8)\n"
"v_mfma_f32_16x16x32_bf16 v[112:115], a[128:131], v[48:51], 0\n"
"v_mfma_f32_16x16x32_bf16 v[112:115], a[132:135], v[52:55], v[112:115]\n"
"s_waitcnt vmcnt(5)\n"
"ds_write_addtid_b32 v132 offset:4352\n"
"v_mfma_f32_16x16x32_bf16 v[112:115], a[136:139], v[56:59], v[112:115]\n"
"ds_write_addtid_b32 v133 offset:8992\n"
"v_mfma_f32_16x16x32_bf16 v[112:115], a[140:143], v[60:63], v[112:115]\n"
"s_waitcnt vmcnt(3)\n"
"ds_write_addtid_b32 v134 offset:13696\n"
"v_mfma_f32_16x16x32_bf16 v[112:115], a[144:147], v[64:67], v[112:115]\n"
"ds_write_addtid_b32 v135 offset:18336\n"
"v_mov_b32 v254, v128\n"
"v_mfma_f32_16x16x32_bf16 v[112:115], a[148:151], v[68:71], v[112:115]\n"
"s_waitcnt vmcnt(1)\n"
"v_mul_f32 v119, v129, v39\n"
"v_mfma_f32_16x16x32_bf16 v[112:115], a[152:155], v[72:75], v[112:115]\n"
"v_mul_f32 v117, v129, v37\n"
"v_mul_f32 v118, v129, v38\n"
"v_mfma_f32_16x16x32_bf16 v[112:115], a[156:159], v[76:79], v[112:115]\n"
"v_mul_f32 v123, v129, v43\n"
"v_mul_f32 v116, v129, v36\n"
"v_mfma_f32_16x16x32_bf16 v[124:127], a[160:163], v[48:51], 0\n"
"v_mul_f32 v121, v129, v41\n"
"v_mul_f32 v122, v129, v42\n"
"v_mfma_f32_16x16x32_bf16 v[124:127], a[164:167], v[52:55], v[124:127]\n"
"ds_write_addtid_b32 v112 offset:0\n"
"v_mul_f32 v120, v129, v40\n"
"v_mfma_f32_16x16x32_bf16 v[124:127], a[168:171], v[56:59], v[124:127]\n"
"s_waitcnt lgkmcnt(1)\n"
"ds_write_addtid_b32 v113 offset:4640\n"
"v_mfma_f32_16x16x32_bf16 v[124:127], a[172:175], v[60:63], v[124:127]\n"
"s_barrier\n"
"v_mfma_f32_16x16x32_bf16 v[124:127], a[176:179], v[64:67], v[124:127]\n"
"ds_write_addtid_b32 v114 offset:9344\n"
"ds_read2st64_b32 v[128:129], v12 offset0:0 offset1:1\n"
"v_mfma_f32_16x16x32_bf16 v[124:127], a[180:183], v[68:71], v[124:127]\n"
"ds_write_addtid_b32 v115 offset:13984\n"
"ds_read2st64_b32 v[130:131], v12 offset0:2 offset1:3\n"
"v_mfma_f32_16x16x32_bf16 v[124:127], a[184:187], v[72:75], v[124:127]\n"
"ds_read2st64_b32 v[140:141], v11 offset0:4 offset1:5\n"
"ds_read2st64_b32 v[148:149], v11 offset0:6 offset1:7\n"
"v_mfma_f32_16x16x32_bf16 v[124:127], a[188:191], v[76:79], v[124:127]\n"
"s_add_u32 s94, s50, 0xffe00000\n"
"s_and_b32 s95, s94, 0xfbffffff\n"
"v_mfma_f32_16x16x32_bf16 v[112:115], v[48:51], a[0:3], 0\n"
"v_mfma_f32_16x16x32_bf16 v[112:115], v[52:55], a[4:7], v[112:115]\n"
"ds_write_addtid_b32 v124 offset:256\n"
"v_mfma_f32_16x16x32_bf16 v[112:115], v[56:59], a[8:11], v[112:115]\n"
"s_waitcnt lgkmcnt(3)\n"
"ds_write_addtid_b32 v125 offset:4896\n"
"v_mfma_f32_16x16x32_bf16 v[132:135], v[128:131], v[164:167], v[16:19]\n"
"s_cmp_eq_u32 s94, s62\n"
"s_cbranch_scc1 .LQ_ft1k7s\n"
".LQ_ft1k7r:\n"
"v_mfma_f32_16x16x32_bf16 v[112:115], v[60:63], a[12:15], v[112:115]\n"
"ds_write_addtid_b32 v126 offset:9600\n"
"v_max_f32 v253, v156, v158\n"
"v_mfma_f32_16x16x32_bf16 v[112:115], v[64:67], a[16:19], v[112:115]\n"
"ds_write_addtid_b32 v127 offset:14240\n"
"v_mfma_f32_16x16x32_bf16 v[112:115], v[68:71], a[20:23], v[112:115]\n"
"v_max_f32 v252, v157, v159\n"
"v_max3_f32 v253, v132, v134, v253\n"
"v_mfma_f32_16x16x32_bf16 v[112:115], v[72:75], a[24:27], v[112:115]\n"
"v_max3_f32 v252, v133, v135, v252\n"
"v_max_f32 v251, v253, v252\n"
"v_mfma_f32_16x16x32_bf16 v[112:115], v[76:79], a[28:31], v[112:115]\n"
"v_mov_b32 v250, v251\n"
"v_mfma_f32_16x16x32_bf16 v[124:127], v[48:51], a[32:35], 0\n"
"v_permlane32_swap_b32 v250, v251\n"
"v_mfma_f32_16x16x32_bf16 v[124:127], v[52:55], a[36:39], v[124:127]\n"
"ds_write_addtid_b32 v112 offset:512\n"
"v_mfma_f32_16x16x32_bf16 v[124:127], v[56:59], a[40:43], v[124:127]\n"
"ds_write_addtid_b32 v113 offset:5152\n"
"v_max_f32 v251, v250, v251\n"
"v_mfma_f32_16x16x32_bf16 v[124:127], v[60:63], a[44:47], v[124:127]\n"
"ds_write_addtid_b32 v114 offset:9856\n"
"v_mov_b32 v250, v251\n"
"v_mfma_f32_16x16x32_bf16 v[124:127], v[64:67], a[48:51], v[124:127]\n"
"ds_write_addtid_b32 v115 offset:14496\n"
"v_mfma_f32_16x16x32_bf16 v[124:127], v[68:71], a[52:55], v[124:127]\n"
"v_permlane16_swap_b32 v250, v251\n"
"v_mfma_f32_16x16x32_bf16 v[124:127], v[72:75], a[56:59], v[124:127]\n"
"v_max_f32 v251, v250, v251\n"
"v_mfma_f32_16x16x32_bf16 v[124:127], v[76:79], a[60:63], v[124:127]\n"
"v_mul_f32 v251, v251, v249\n"
"v_fma_f32 v253, v253, v249, -v251\n"
"v_mfma_f32_16x16x32_bf16 v[116:119], a[64:67], v[48:51], v[116:119]\n"
"v_fma_f32 v252, v252, v249, -v251\n"
"v_mfma_f32_16x16x32_bf16 v[116:119], a[68:71], v[52:55], v[116:119]\n"
"ds_write_addtid_b32 v124 offset:768\n"
"v_mfma_f32_16x16x32_bf16 v[116:119], a[72:75], v[56:59], v[116:119]\n"
"ds_write_addtid_b32 v125 offset:5408\n"
"v_mfma_f32_16x16x32_bf16 v[116:119], a[76:79], v[60:63], v[116:119]\n"
"ds_write_addtid_b32 v126 offset:10112\n"
"v_mfma_f32_16x16x32_bf16 v[116:119], a[80:83], v[64:67], v[116:119]\n"
"ds_write_addtid_b32 v127 offset:14752\n"
"v_mfma_f32_16x16x32_bf16 v[116:119], a[84:87], v[68:71], v[116:119]\n"
"v_exp_f32 v253, v253\n"
"v_mfma_f32_16x16x32_bf16 v[116:119], a[88:91], v[72:75], v[116:119]\n"
"v_exp_f32 v252, v252\n"
"v_mfma_f32_16x16x32_bf16 v[116:119], a[92:95], v[76:79], v[116:119]\n"
"v_add_f32 v251, v253, v252\n"
"v_mov_b32 v250, v251\n"
"v_mfma_f32_16x16x32_bf16 v[120:123], a[96:99], v[48:51], v[120:123]\n"
"v_permlane32_swap_b32 v250, v251\n"
"v_mfma_f32_16x16x32_bf16 v[120:123], a[100:103], v[52:55], v[120:123]\n"
"v_add_f32 v251, v250, v251\n"
"v_mov_b32 v250, v251\n"
"v_mfma_f32_16x16x32_bf16 v[120:123], a[104:107], v[56:59], v[120:123]\n"
"v_permlane16_swap_b32 v250, v251\n"
"v_mfma_f32_16x16x32_bf16 v[120:123], a[108:111], v[60:63], v[120:123]\n"
"v_add_f32 v251, v250, v251\n"
"v_mfma_f32_16x16x32_bf16 v[120:123], a[112:115], v[64:67], v[120:123]\n"
"v_mfma_f32_16x16x32_bf16 v[120:123], a[116:119], v[68:71], v[120:123]\n"
"v_mfma_f32_16x16x32_bf16 v[120:123], a[120:123], v[72:75], v[120:123]\n"
"v_rcp_f32 v251, v251\n"
"v_mfma_f32_16x16x32_bf16 v[120:123], a[124:127], v[76:79], v[120:123]\n"
"v_mul_f32 v253, v253, v251\n"
"v_mul_f32 v252, v252, v251\n"
"v_cvt_pk_bf16_f32 v250, v253, v252\n"
"s_waitcnt lgkmcnt(12)\n"
"v_and_b32 v52, v250, v24\n"
"v_and_b32 v53, v250, v25\n"
"v_and_b32 v54, v250, v26\n"
"v_and_b32 v55, v250, v27\n"
"s_nop 1\n"
"v_mfma_f32_16x16x32_bf16 v[56:59], v[140:143], v[52:55], 0\n"
"v_mfma_f32_16x16x32_bf16 v[60:63], v[148:151], v[52:55], 0\n"
"s_nop 6\n"
"v_cvt_pk_bf16_f32 v52, v56, v57\n"
"v_cvt_pk_bf16_f32 v53, v58, v59\n"
"v_cvt_pk_bf16_f32 v54, v60, v61\n"
"v_cvt_pk_bf16_f32 v55, v62, v63\n"
"buffer_store_dwordx4 v[52:55], v13, s[40:43], s95 offen sc1\n"
"s_waitcnt lgkmcnt(0)\n"
"s_barrier\n"
"ds_read_b128 v[56:59], v2 offset:1024\n"
"ds_read_b128 v[60:63], v3 offset:2048\n"
"ds_read_b128 v[64:67], v4 offset:3072\n"
"ds_read_b128 v[68:71], v2 offset:1280\n"
"ds_read_b128 v[48:51], v3 offset:2304\n"
"ds_read_b128 v[76:79], v4 offset:3328\n"
"s_waitcnt lgkmcnt(3)\n"
"v_add_f32 v56, v56, v60\n"
"v_add_f32 v57, v57, v61\n"
"v_add_f32 v58, v58, v62\n"
"v_add_f32 v59, v59, v63\n"
"v_add_f32 v56, v56, v64\n"
"v_add_f32 v57, v57, v65\n"
"v_add_f32 v58, v58, v66\n"
"v_add_f32 v59, v59, v67\n"
"v_add_f32 v56, v56, v116\n"
"v_add_f32 v57, v57, v117\n"
"v_add_f32 v58, v58, v118\n"
"v_add_f32 v59, v59, v119\n"
"v_mul_f32 v56, v254, v56\n"
"v_mul_f32 v57, v254, v57\n"
"v_mul_f32 v58, v254, v58\n"
"v_mul_f32 v59, v254, v59\n"
"v_cvt_pk_bf16_f32 v60, v56, v57\n"
"v_cvt_pk_bf16_f32 v61, v58, v59\n"
"ds_write_addtid_b32 v60 offset:18592\n"
"ds_write_addtid_b32 v61 offset:18848\n"
"s_waitcnt lgkmcnt(2)\n"
"v_add_f32 v68, v68, v48\n"
"v_add_f32 v69, v69, v49\n"
"v_add_f32 v70, v70, v50\n"
"v_add_f32 v71, v71, v51\n"
"v_add_f32 v68, v68, v76\n"
"v_add_f32 v69, v69, v77\n"
"v_add_f32 v70, v70, v78\n"
"v_add_f32 v71, v71, v79\n"
"v_add_f32 v68, v68, v120\n"
"v_add_f32 v69, v69, v121\n"
"v_add_f32 v70, v70, v122\n"
"v_add_f32 v71, v71, v123\n"
"v_mul_f32 v68, v254, v68\n"
"v_mul_f32 v69, v254, v69\n"
"v_mul_f32 v70, v254, v70\n"
"v_mul_f32 v71, v254, v71\n"
"v_cvt_pk_bf16_f32 v62, v68, v69\n"
"v_cvt_pk_bf16_f32 v63, v70, v71\n"
"ds_write_addtid_b32 v62 offset:19104\n"
"ds_write_addtid_b32 v63 offset:19360\n"
"s_waitcnt vmcnt(0)\n"
"buffer_store_dwordx2 v[0:1], v14, s[40:43], s94 offen offset:0 sc1\n"
"s_waitcnt lgkmcnt(0)\n"
"s_barrier\n"
"s_branch .LQ_end\n"
".LQ_t2:\n"
"v_mfma_f32_16x16x32_bf16 v[132:135], a[252:255], a[60:63], v[132:135]\n"
"s_waitcnt vmcnt(17)\n"
"v_mfma_f32_16x16x32_bf16 v[112:115], a[128:131], v[48:51], 0\n"
"v_mfma_f32_16x16x32_bf16 v[112:115], a[132:135], v[52:55], v[112:115]\n"
"s_waitcnt vmcnt(14)\n"
"ds_write_addtid_b32 v132 offset:4352\n"
"v_mfma_f32_16x16x32_bf16 v[112:115], a[136:139], v[56:59], v[112:115]\n"
"ds_write_addtid_b32 v133 offset:8992\n"
"v_mfma_f32_16x16x32_bf16 v[112:115], a[140:143], v[60:63], v[112:115]\n"
"s_waitcnt vmcnt(12)\n"
"ds_write_addtid_b32 v134 offset:13696\n"
"v_mfma_f32_16x16x32_bf16 v[112:115], a[144:147], v[64:67], v[112:115]\n"
"ds_write_addtid_b32 v135 offset:18336\n"
"v_mov_b32 v254, v128\n"
"v_mfma_f32_16x16x32_bf16 v[112:115], a[148:151], v[68:71], v[112:115]\n"
"s_waitcnt vmcnt(10)\n"
"v_mul_f32 v119, v129, v39\n"
"v_mfma_f32_16x16x32_bf16 v[112:115], a[152:155], v[72:75], v[112:115]\n"
"v_mul_f32 v117, v129, v37\n"
"v_mul_f32 v118, v129, v38\n"
"v_mfma_f32_16x16x32_bf16 v[112:115], a[156:159], v[76:79], v[112:115]\n"
"v_mul_f32 v123, v129, v43\n"
"v_mul_f32 v116, v129, v36\n"
"v_mfma_f32_16x16x32_bf16 v[124:127], a[160:163], v[48:51], 0\n"
"v_mul_f32 v121, v129, v41\n"
"v_mul_f32 v122, v129, v42\n"
"v_mfma_f32_16x16x32_bf16 v[124:127], a[164:167], v[52:55], v[124:127]\n"
"ds_write_addtid_b32 v112 offset:0\n"
"v_mul_f32 v120, v129, v40\n"
"v_mfma_f32_16x16x32_bf16 v[124:127], a[168:171], v[56:59], v[124:127]\n"
"s_waitcnt lgkmcnt(1)\n"
"ds_write_addtid_b32 v113 offset:4640\n"
"v_mfma_f32_16x16x32_bf16 v[124:127], a[172:175], v[60:63], v[124:127]\n"
"s_barrier\n"
"v_mfma_f32_16x16x32_bf16 v[124:127], a[176:179], v[64:67], v[124:127]\n"
"ds_write_addtid_b32 v114 offset:9344\n"
"ds_read2st64_b32 v[128:129], v12 offset0:0 offset1:1\n"
"v_mfma_f32_16x16x32_bf16 v[124:127], a[180:183], v[68:71], v[124:127]\n"
"ds_write_addtid_b32 v115 offset:13984\n"
"ds_read2st64_b32 v[130:131], v12 offset0:2 offset1:3\n"
"v_mfma_f32_16x16x32_bf16 v[124:127], a[184:187], v[72:75], v[124:127]\n"
"ds_read2st64_b32 v[140:141], v11 offset0:4 offset1:5\n"
"ds_read2st64_b32 v[148:149], v11 offset0:6 offset1:7\n"
"v_mfma_f32_16x16x32_bf16 v[124:127], a[188:191], v[76:79], v[124:127]\n"
"s_add_u32 s94, s50, 0xffd00000\n"
"s_and_b32 s95, s94, 0xfbffffff\n"
"v_mfma_f32_16x16x32_bf16 v[112:115], v[48:51], a[0:3], 0\n"
"v_mfma_f32_16x16x32_bf16 v[112:115], v[52:55], a[4:7], v[112:115]\n"
"ds_write_addtid_b32 v124 offset:256\n"
"v_mfma_f32_16x16x32_bf16 v[112:115], v[56:59], a[8:11], v[112:115]\n"
"s_waitcnt lgkmcnt(3)\n"
"ds_write_addtid_b32 v125 offset:4896\n"
"v_mfma_f32_16x16x32_bf16 v[132:135], v[128:131], v[164:167], v[16:19]\n"
"s_cmp_eq_u32 s94, s62\n"
"s_cbranch_scc1 .LQ_ft2k7s\n"
".LQ_ft2k7r:\n"
"v_mfma_f32_16x16x32_bf16 v[112:115], v[60:63], a[12:15], v[112:115]\n"
"ds_write_addtid_b32 v126 offset:9600\n"
"v_max_f32 v253, v156, v158\n"
"v_mfma_f32_16x16x32_bf16 v[112:115], v[64:67], a[16:19], v[112:115]\n"
"ds_write_addtid_b32 v127 offset:14240\n"
"v_mfma_f32_16x16x32_bf16 v[112:115], v[68:71], a[20:23], v[112:115]\n"
"v_max_f32 v252, v157, v159\n"
"v_max3_f32 v253, v132, v134, v253\n"
"v_mfma_f32_16x16x32_bf16 v[112:115], v[72:75], a[24:27], v[112:115]\n"
"v_max3_f32 v252, v133, v135, v252\n"
"v_max_f32 v251, v253, v252\n"
"v_mfma_f32_16x16x32_bf16 v[112:115], v[76:79], a[28:31], v[112:115]\n"
"v_mov_b32 v250, v251\n"
"v_mfma_f32_16x16x32_bf16 v[124:127], v[48:51], a[32:35], 0\n"
"v_permlane32_swap_b32 v250, v251\n"
"v_mfma_f32_16x16x32_bf16 v[124:127], v[52:55], a[36:39], v[124:127]\n"
"ds_write_addtid_b32 v112 offset:512\n"
"v_mfma_f32_16x16x32_bf16 v[124:127], v[56:59], a[40:43], v[124:127]\n"
"ds_write_addtid_b32 v113 offset:5152\n"
"v_max_f32 v251, v250, v251\n"
"v_mfma_f32_16x16x32_bf16 v[124:127], v[60:63], a[44:47], v[124:127]\n"
"ds_write_addtid_b32 v114 offset:9856\n"
"v_mov_b32 v250, v251\n"
"v_mfma_f32_16x16x32_bf16 v[124:127], v[64:67], a[48:51], v[124:127]\n"
"ds_write_addtid_b32 v115 offset:14496\n"
"v_mfma_f32_16x16x32_bf16 v[124:127], v[68:71], a[52:55], v[124:127]\n"
"v_permlane16_swap_b32 v250, v251\n"
"v_mfma_f32_16x16x32_bf16 v[124:127], v[72:75], a[56:59], v[124:127]\n"
"v_max_f32 v251, v250, v251\n"
"v_mfma_f32_16x16x32_bf16 v[124:127], v[76:79], a[60:63], v[124:127]\n"
"v_mul_f32 v251, v251, v249\n"
"v_fma_f32 v253, v253, v249, -v251\n"
"v_mfma_f32_16x16x32_bf16 v[116:119], a[64:67], v[48:51], v[116:119]\n"
"v_fma_f32 v252, v252, v249, -v251\n"
"v_mfma_f32_16x16x32_bf16 v[116:119], a[68:71], v[52:55], v[116:119]\n"
"ds_write_addtid_b32 v124 offset:768\n"
"v_mfma_f32_16x16x32_bf16 v[116:119], a[72:75], v[56:59], v[116:119]\n"
"ds_write_addtid_b32 v125 offset:5408\n"
"v_mfma_f32_16x16x32_bf16 v[116:119], a[76:79], v[60:63], v[116:119]\n"
"ds_write_addtid_b32 v126 offset:10112\n"
"v_mfma_f32_16x16x32_bf16 v[116:119], a[80:83], v[64:67], v[116:119]\n"
"ds_write_addtid_b32 v127 offset:14752\n"
"v_mfma_f32_16x16x32_bf16 v[116:119], a[84:87], v[68:71], v[116:119]\n"
"v_exp_f32 v253, v253\n"
"v_mfma_f32_16x16x32_bf16 v[116:119], a[88:91], v[72:75], v[116:119]\n"
"v_exp_f32 v252, v252\n"
"v_mfma_f32_16x16x32_bf16 v[116:119], a[92:95], v[76:79], v[116:119]\n"
"v_add_f32 v251, v253, v252\n"
"v_mov_b32 v250, v251\n"
"v_mfma_f32_16x16x32_bf16 v[120:123], a[96:99], v[48:51], v[120:123]\n"
"v_permlane32_swap_b32 v250, v251\n"
"v_mfma_f32_16x16x32_bf16 v[120:123], a[100:103], v[52:55], v[120:123]\n"
"v_add_f32 v251, v250, v251\n"
"v_mov_b32 v250, v251\n"
"v_mfma_f32_16x16x32_bf16 v[120:123], a[104:107], v[56:59], v[120:123]\n"
"v_permlane16_swap_b32 v250, v251\n"
"v_mfma_f32_16x16x32_bf16 v[120:123], a[108:111], v[60:63], v[120:123]\n"
"s_waitcnt vmcnt(0)\n"
"v_add_f32 v251, v250, v251\n"
"v_mfma_f32_16x16x32_bf16 v[120:123], a[112:115], v[64:67], v[120:123]\n"
"v_mfma_f32_16x16x32_bf16 v[120:123], a[116:119], v[68:71], v[120:123]\n"
"v_mfma_f32_16x16x32_bf16 v[120:123], a[120:123], v[72:75], v[120:123]\n"
"v_rcp_f32 v251, v251\n"
"v_mfma_f32_16x16x32_bf16 v[120:123], a[124:127], v[76:79], v[120:123]\n"
"s_waitcnt vmcnt(7)\n"
"v_mul_f32 v253, v253, v251\n"
"v_mfma_f32_16x16x32_bf16 v[48:51], v[80:83], a[64:67], 0\n"
"v_mul_f32 v252, v252, v251\n"
"v_cvt_pk_bf16_f32 v250, v253, v252\n"
"v_mfma_f32_16x16x32_bf16 v[48:51], v[84:87], a[68:71], v[48:51]\n"
"s_waitcnt vmcnt(5)\n"
"v_mfma_f32_16x16x32_bf16 v[48:51], v[88:91], a[72:75], v[48:51]\n"
"s_waitcnt lgkmcnt(12)\n"
"v_and_b32 v52, v250, v24\n"
"v_and_b32 v53, v250, v25\n"
"v_mfma_f32_16x16x32_bf16 v[48:51], v[92:95], a[76:79], v[48:51]\n"
"s_waitcnt vmcnt(3)\n"
"v_and_b32 v54, v250, v26\n"
"v_and_b32 v55, v250, v27\n"
"v_mfma_f32_16x16x32_bf16 v[48:51], v[96:99], a[80:83], v[48:51]\n"
"v_mfma_f32_16x16x32_bf16 v[56:59], v[140:143], v[52:55], 0\n"
"v_mul_f32_dpp v75, v137, v44 row_newbcast:3 row_mask:0xf bank_mask:0xf\n"
"v_mov_b32 v253, v136\n"
"v_mfma_f32_16x16x32_bf16 v[60:63], v[148:151], v[52:55], 0\n"
"v_mul_f32_dpp v73, v137, v44 row_newbcast:1 row_mask:0xf bank_mask:0xf\n"
"v_mul_f32_dpp v74, v137, v44 row_newbcast:2 row_mask:0xf bank_mask:0xf\n"
"v_mfma_f32_16x16x32_bf16 v[48:51], v[100:103], a[84:87], v[48:51]\n"
"s_waitcnt vmcnt(1)\n"
"v_cvt_pk_bf16_f32 v52, v56, v57\n"
"v_cvt_pk_bf16_f32 v53, v58, v59\n"
"v_mfma_f32_16x16x32_bf16 v[48:51], v[104:107], a[88:91], v[48:51]\n"
"v_cvt_pk_bf16_f32 v54, v60, v61\n"
"v_cvt_pk_bf16_f32 v55, v62, v63\n"
"v_mfma_f32_16x16x32_bf16 v[48:51], v[108:111], a[92:95], v[48:51]\n"
"buffer_store_dwordx4 v[52:55], v13, s[40:43], s95 offen sc1\n"
"v_mul_f32_dpp v114, v137, v45 row_newbcast:2 row_mask:0xf bank_mask:0xf\n"
"v_mul_f32_dpp v115, v137, v45 row_newbcast:3 row_mask:0xf bank_mask:0xf\n"
"v_mul_f32_dpp v72, v137, v44 row_newbcast:0 row_mask:0xf bank_mask:0xf\n"
"v_mfma_f32_16x16x32_bf16 v[52:55], v[80:83], a[96:99], 0\n"
"s_waitcnt lgkmcnt(0)\n"
"v_mul_f32_dpp v112, v137, v45 row_newbcast:0 row_mask:0xf bank_mask:0xf\n"
"v_mul_f32_dpp v113, v137, v45 row_newbcast:1 row_mask:0xf bank_mask:0xf\n"
"v_mfma_f32_16x16x32_bf16 v[52:55], v[84:87], a[100:103], v[52:55]\n"
"s_barrier\n"
"v_mfma_f32_16x16x32_bf16 v[52:55], v[88:91], a[104:107], v[52:55]\n"
"ds_write_addtid_b32 v48 offset:1024\n"
"ds_read_b128 v[56:59], v2 offset:1024\n"
"v_mfma_f32_16x16x32_bf16 v[52:55], v[92:95], a[108:111], v[52:55]\n"
"ds_write_addtid_b32 v49 offset:5664\n"
"ds_read_b128 v[60:63], v3 offset:2048\n"
"v_mfma_f32_16x16x32_bf16 v[52:55], v[96:99], a[112:115], v[52:55]\n"
"ds_write_addtid_b32 v50 offset:10368\n"
"v_mfma_f32_16x16x32_bf16 v[52:55], v[100:103], a[116:119], v[52:55]\n"
"ds_write_addtid_b32 v51 offset:15008\n"
"ds_read_b128 v[64:67], v4 offset:3072\n"
"v_mfma_f32_16x16x32_bf16 v[52:55], v[104:107], a[120:123], v[52:55]\n"
"ds_read_b128 v[68:71], v2 offset:1280\n"
"ds_read_b128 v[48:51], v3 offset:2304\n"
"v_mfma_f32_16x16x32_bf16 v[52:55], v[108:111], a[124:127], v[52:55]\n"
"ds_read_b128 v[76:79], v4 offset:3328\n"
"v_mfma_f32_16x16x32_bf16 v[124:127], v[80:83], a[0:3], 0\n"
"v_mfma_f32_16x16x32_bf16 v[124:127], v[84:87], a[4:7], v[124:127]\n"
"ds_write_addtid_b32 v52 offset:1280\n"
"v_mfma_f32_16x16x32_bf16 v[124:127], v[88:91], a[8:11], v[124:127]\n"
"ds_write_addtid_b32 v53 offset:5920\n"
"v_mfma_f32_16x16x32_bf16 v[124:127], v[92:95], a[12:15], v[124:127]\n"
"s_waitcnt lgkmcnt(5)\n"
"ds_write_addtid_b32 v54 offset:10624\n"
"v_add_f32 v56, v56, v60\n"
"v_mfma_f32_16x16x32_bf16 v[124:127], v[96:99], a[16:19], v[124:127]\n"
"ds_write_addtid_b32 v55 offset:15264\n"
"v_add_f32 v57, v57, v61\n"
"v_mfma_f32_16x16x32_bf16 v[124:127], v[100:103], a[20:23], v[124:127]\n"
"v_add_f32 v58, v58, v62\n"
"v_mfma_f32_16x16x32_bf16 v[124:127], v[104:107], a[24:27], v[124:127]\n"
"v_add_f32 v59, v59, v63\n"
"v_add_f32 v56, v56, v64\n"
"v_mfma_f32_16x16x32_bf16 v[124:127], v[108:111], a[28:31], v[124:127]\n"
"v_add_f32 v57, v57, v65\n"
"v_add_f32 v58, v58, v66\n"
"v_mfma_f32_16x16x32_bf16 v[52:55], v[80:83], a[32:35], 0\n"
"v_add_f32 v59, v59, v67\n"
"v_add_f32 v56, v56, v116\n"
"v_mfma_f32_16x16x32_bf16 v[52:55], v[84:87], a[36:39], v[52:55]\n"
"ds_write_addtid_b32 v124 offset:1536\n"
"v_add_f32 v57, v57, v117\n"
"v_mfma_f32_16x16x32_bf16 v[52:55], v[88:91], a[40:43], v[52:55]\n"
"ds_write_addtid_b32 v125 offset:6176\n"
"v_mfma_f32_16x16x32_bf16 v[52:55], v[92:95], a[44:47], v[52:55]\n"
"ds_write_addtid_b32 v126 offset:10880\n"
"v_add_f32 v58, v58, v118\n"
"v_mfma_f32_16x16x32_bf16 v[52:55], v[96:99], a[48:51], v[52:55]\n"
"ds_write_addtid_b32 v127 offset:15520\n"
"v_add_f32 v59, v59, v119\n"
"v_mfma_f32_16x16x32_bf16 v[52:55], v[100:103], a[52:55], v[52:55]\n"
"v_mul_f32 v56, v254, v56\n"
"v_mul_f32 v57, v254, v57\n"
"v_mfma_f32_16x16x32_bf16 v[52:55], v[104:107], a[56:59], v[52:55]\n"
"v_mul_f32 v58, v254, v58\n"
"v_mul_f32 v59, v254, v59\n"
"v_mfma_f32_16x16x32_bf16 v[52:55], v[108:111], a[60:63], v[52:55]\n"
"v_cvt_pk_bf16_f32 v60, v56, v57\n"
"v_mfma_f32_16x16x32_bf16 v[72:75], v[80:83], a[128:131], v[72:75]\n"
"v_cvt_pk_bf16_f32 v61, v58, v59\n"
"v_mfma_f32_16x16x32_bf16 v[72:75], v[84:87], a[132:135], v[72:75]\n"
"ds_write_addtid_b32 v52 offset:1792\n"
"ds_write_addtid_b32 v60 offset:18592\n"
"v_mfma_f32_16x16x32_bf16 v[72:75], v[88:91], a[136:139], v[72:75]\n"
"ds_write_addtid_b32 v53 offset:6432\n"
"ds_write_addtid_b32 v61 offset:18848\n"
"v_mfma_f32_16x16x32_bf16 v[72:75], v[92:95], a[140:143], v[72:75]\n"
"s_waitcnt lgkmcnt(12)\n"
"ds_write_addtid_b32 v54 offset:11136\n"
"v_add_f32 v68, v68, v48\n"
"v_mfma_f32_16x16x32_bf16 v[72:75], v[96:99], a[144:147], v[72:75]\n"
"ds_write_addtid_b32 v55 offset:15776\n"
"v_mfma_f32_16x16x32_bf16 v[72:75], v[100:103], a[148:151], v[72:75]\n"
"v_add_f32 v69, v69, v49\n"
"v_add_f32 v70, v70, v50\n"
"v_mfma_f32_16x16x32_bf16 v[72:75], v[104:107], a[152:155], v[72:75]\n"
"v_add_f32 v71, v71, v51\n"
"v_add_f32 v68, v68, v76\n"
"v_mfma_f32_16x16x32_bf16 v[72:75], v[108:111], a[156:159], v[72:75]\n"
"v_add_f32 v69, v69, v77\n"
"v_add_f32 v70, v70, v78\n"
"v_mfma_f32_16x16x32_bf16 v[112:115], v[80:83], a[160:163], v[112:115]\n"
"v_add_f32 v71, v71, v79\n"
"v_add_f32 v68, v68, v120\n"
"v_mfma_f32_16x16x32_bf16 v[112:115], v[84:87], a[164:167], v[112:115]\n"
"v_add_f32 v69, v69, v121\n"
"v_add_f32 v70, v70, v122\n"
"v_mfma_f32_16x16x32_bf16 v[112:115], v[88:91], a[168:171], v[112:115]\n"
"v_add_f32 v71, v71, v123\n"
"v_mul_f32 v68, v254, v68\n"
"v_mfma_f32_16x16x32_bf16 v[112:115], v[92:95], a[172:175], v[112:115]\n"
"v_mul_f32 v69, v254, v69\n"
"v_mul_f32 v70, v254, v70\n"
"v_mfma_f32_16x16x32_bf16 v[112:115], v[96:99], a[176:179], v[112:115]\n"
"v_mul_f32 v71, v254, v71\n"
"v_mfma_f32_16x16x32_bf16 v[112:115], v[100:103], a[180:183], v[112:115]\n"
"v_mfma_f32_16x16x32_bf16 v[112:115], v[104:107], a[184:187], v[112:115]\n"
"v_cvt_pk_bf16_f32 v62, v68, v69\n"
"v_mfma_f32_16x16x32_bf16 v[112:115], v[108:111], a[188:191], v[112:115]\n"
"v_cvt_pk_bf16_f32 v63, v70, v71\n"
"ds_write_addtid_b32 v62 offset:19104\n"
"ds_write_addtid_b32 v63 offset:19360\n"
"s_waitcnt vmcnt(0)\n"
"buffer_store_dwordx2 v[0:1], v14, s[40:43], s94 offen offset:0 sc1\n"
"s_waitcnt lgkmcnt(0)\n"
"s_barrier\n"
"ds_read_b128 v[76:79], v2 offset:2560\n"
"ds_read_b128 v[80:83], v3 offset:3584\n"
"ds_read_b128 v[84:87], v4 offset:0\n"
"ds_read_b128 v[88:91], v2 offset:2816\n"
"ds_read_b128 v[92:95], v3 offset:3840\n"
"ds_read_b128 v[96:99], v4 offset:256\n"
"s_waitcnt lgkmcnt(3)\n"
"v_add_f32 v76, v76, v80\n"
"v_add_f32 v77, v77, v81\n"
"v_add_f32 v78, v78, v82\n"
"v_add_f32 v79, v79, v83\n"
"v_add_f32 v76, v76, v84\n"
"v_add_f32 v77, v77, v85\n"
"v_add_f32 v78, v78, v86\n"
"v_add_f32 v79, v79, v87\n"
"v_add_f32 v76, v76, v72\n"
"v_add_f32 v77, v77, v73\n"
"v_add_f32 v78, v78, v74\n"
"v_add_f32 v79, v79, v75\n"
"v_mul_f32_dpp v76, v253, v76 row_newbcast:0 row_mask:0xf bank_mask:0xf\n"
"v_mul_f32_dpp v77, v253, v77 row_newbcast:1 row_mask:0xf bank_mask:0xf\n"
"v_mul_f32_dpp v78, v253, v78 row_newbcast:2 row_mask:0xf bank_mask:0xf\n"
"v_mul_f32_dpp v79, v253, v79 row_newbcast:3 row_mask:0xf bank_mask:0xf\n"
"v_cvt_pk_bf16_f32 v142, v76, v77\n"
"v_cvt_pk_bf16_f32 v143, v78, v79\n"
"ds_write_addtid_b32 v142 offset:19616\n"
"ds_write_addtid_b32 v143 offset:19872\n"
"s_waitcnt lgkmcnt(2)\n"
"v_add_f32 v88, v88, v92\n"
"v_add_f32 v89, v89, v93\n"
"v_add_f32 v90, v90, v94\n"
"v_add_f32 v91, v91, v95\n"
"v_add_f32 v88, v88, v96\n"
"v_add_f32 v89, v89, v97\n"
"v_add_f32 v90, v90, v98\n"
"v_add_f32 v91, v91, v99\n"
"v_add_f32 v88, v88, v112\n"
"v_add_f32 v89, v89, v113\n"
"v_add_f32 v90, v90, v114\n"
"v_add_f32 v91, v91, v115\n"
"v_mul_f32_dpp v88, v253, v88 row_newbcast:0 row_mask:0xf bank_mask:0xf\n"
"v_mul_f32_dpp v89, v253, v89 row_newbcast:1 row_mask:0xf bank_mask:0xf\n"
"v_mul_f32_dpp v90, v253, v90 row_newbcast:2 row_mask:0xf bank_mask:0xf\n"
"v_mul_f32_dpp v91, v253, v91 row_newbcast:3 row_mask:0xf bank_mask:0xf\n"
"v_cvt_pk_bf16_f32 v150, v88, v89\n"
"v_cvt_pk_bf16_f32 v151, v90, v91\n"
"ds_write_addtid_b32 v150 offset:20128\n"
"ds_write_addtid_b32 v151 offset:20384\n"
"s_waitcnt lgkmcnt(0)\n"
"s_barrier\n"
"s_branch .LQ_end\n"
".LQ_t3:\n"
"v_mfma_f32_16x16x32_bf16 v[120:123], a[120:123], v[72:75], v[120:123]\n"
"v_rcp_f32 v251, v251\n"
"v_mfma_f32_16x16x32_bf16 v[120:123], a[124:127], v[76:79], v[120:123]\n"
"s_waitcnt vmcnt(16)\n"
"v_mul_f32 v253, v253, v251\n"
"v_mfma_f32_16x16x32_bf16 v[48:51], v[80:83], a[64:67], 0\n"
"v_mul_f32 v252, v252, v251\n"
"v_cvt_pk_bf16_f32 v250, v253, v252\n"
"v_mfma_f32_16x16x32_bf16 v[48:51], v[84:87], a[68:71], v[48:51]\n"
"s_waitcnt vmcnt(14)\n"
"v_mfma_f32_16x16x32_bf16 v[48:51], v[88:91], a[72:75], v[48:51]\n"
"s_waitcnt lgkmcnt(12)\n"
"v_and_b32 v52, v250, v24\n"
"v_and_b32 v53, v250, v25\n"
"v_mfma_f32_16x16x32_bf16 v[48:51], v[92:95], a[76:79], v[48:51]\n"
"s_waitcnt vmcnt(12)\n"
"v_and_b32 v54, v250, v26\n"
"v_and_b32 v55, v250, v27\n"
"v_mfma_f32_16x16x32_bf16 v[48:51], v[96:99], a[80:83], v[48:51]\n"
"v_mfma_f32_16x16x32_bf16 v[56:59], v[140:143], v[52:55], 0\n"
"v_mul_f32_dpp v75, v137, v44 row_newbcast:3 row_mask:0xf bank_mask:0xf\n"
"v_mov_b32 v253, v136\n"
"v_mfma_f32_16x16x32_bf16 v[60:63], v[148:151], v[52:55], 0\n"
"v_mul_f32_dpp v73, v137, v44 row_newbcast:1 row_mask:0xf bank_mask:0xf\n"
"v_mul_f32_dpp v74, v137, v44 row_newbcast:2 row_mask:0xf bank_mask:0xf\n"
"v_mfma_f32_16x16x32_bf16 v[48:51], v[100:103], a[84:87], v[48:51]\n"
"s_waitcnt vmcnt(10)\n"
"v_cvt_pk_bf16_f32 v52, v56, v57\n"
"v_cvt_pk_bf16_f32 v53, v58, v59\n"
"v_mfma_f32_16x16x32_bf16 v[48:51], v[104:107], a[88:91], v[48:51]\n"
"v_cvt_pk_bf16_f32 v54, v60, v61\n"
"v_cvt_pk_bf16_f32 v55, v62, v63\n"
"v_mfma_f32_16x16x32_bf16 v[48:51], v[108:111], a[92:95], v[48:51]\n"
"buffer_store_dwordx4 v[52:55], v13, s[40:43], s95 offen sc1\n"
"v_mul_f32_dpp v114, v137, v45 row_newbcast:2 row_mask:0xf bank_mask:0xf\n"
"v_mul_f32_dpp v115, v137, v45 row_newbcast:3 row_mask:0xf bank_mask:0xf\n"
"v_mul_f32_dpp v72, v137, v44 row_newbcast:0 row_mask:0xf bank_mask:0xf\n"
"v_mfma_f32_16x16x32_bf16 v[52:55], v[80:83], a[96:99], 0\n"
"s_waitcnt lgkmcnt(0)\n"
"v_mul_f32_dpp v112, v137, v45 row_newbcast:0 row_mask:0xf bank_mask:0xf\n"
"v_mul_f32_dpp v113, v137, v45 row_newbcast:1 row_mask:0xf bank_mask:0xf\n"
"v_mfma_f32_16x16x32_bf16 v[52:55], v[84:87], a[100:103], v[52:55]\n"
"s_barrier\n"
"v_mfma_f32_16x16x32_bf16 v[52:55], v[88:91], a[104:107], v[52:55]\n"
"ds_write_addtid_b32 v48 offset:1024\n"
"ds_read_b128 v[56:59], v2 offset:1024\n"
"v_mfma_f32_16x16x32_bf16 v[52:55], v[92:95], a[108:111], v[52:55]\n"
"ds_write_addtid_b32 v49 offset:5664\n"
"ds_read_b128 v[60:63], v3 offset:2048\n"
"v_mfma_f32_16x16x32_bf16 v[52:55], v[96:99], a[112:115], v[52:55]\n"
"ds_write_addtid_b32 v50 offset:10368\n"
"v_mfma_f32_16x16x32_bf16 v[52:55], v[100:103], a[116:119], v[52:55]\n"
"ds_write_addtid_b32 v51 offset:15008\n"
"ds_read_b128 v[64:67], v4 offset:3072\n"
"v_mfma_f32_16x16x32_bf16 v[52:55], v[104:107], a[120:123], v[52:55]\n"
"ds_read_b128 v[68:71], v2 offset:1280\n"
"ds_read_b128 v[48:51], v3 offset:2304\n"
"v_mfma_f32_16x16x32_bf16 v[52:55], v[108:111], a[124:127], v[52:55]\n"
"ds_read_b128 v[76:79], v4 offset:3328\n"
"v_mfma_f32_16x16x32_bf16 v[124:127], v[80:83], a[0:3], 0\n"
"v_mfma_f32_16x16x32_bf16 v[124:127], v[84:87], a[4:7], v[124:127]\n"
"ds_write_addtid_b32 v52 offset:1280\n"
"v_mfma_f32_16x16x32_bf16 v[124:127], v[88:91], a[8:11], v[124:127]\n"
"ds_write_addtid_b32 v53 offset:5920\n"
"v_mfma_f32_16x16x32_bf16 v[124:127], v[92:95], a[12:15], v[124:127]\n"
"s_waitcnt lgkmcnt(5)\n"
"ds_write_addtid_b32 v54 offset:10624\n"
"v_add_f32 v56, v56, v60\n"
"v_mfma_f32_16x16x32_bf16 v[124:127], v[96:99], a[16:19], v[124:127]\n"
"ds_write_addtid_b32 v55 offset:15264\n"
"v_add_f32 v57, v57, v61\n"
"v_mfma_f32_16x16x32_bf16 v[124:127], v[100:103], a[20:23], v[124:127]\n"
"v_add_f32 v58, v58, v62\n"
"v_mfma_f32_16x16x32_bf16 v[124:127], v[104:107], a[24:27], v[124:127]\n"
"v_add_f32 v59, v59, v63\n"
"v_add_f32 v56, v56, v64\n"
"v_mfma_f32_16x16x32_bf16 v[124:127], v[108:111], a[28:31], v[124:127]\n"
"v_add_f32 v57, v57, v65\n"
"v_add_f32 v58, v58, v66\n"
"v_mfma_f32_16x16x32_bf16 v[52:55], v[80:83], a[32:35], 0\n"
"v_add_f32 v59, v59, v67\n"
"v_add_f32 v56, v56, v116\n"
"v_mfma_f32_16x16x32_bf16 v[52:55], v[84:87], a[36:39], v[52:55]\n"
"ds_write_addtid_b32 v124 offset:1536\n"
"v_add_f32 v57, v57, v117\n"
"v_mfma_f32_16x16x32_bf16 v[52:55], v[88:91], a[40:43], v[52:55]\n"
"ds_write_addtid_b32 v125 offset:6176\n"
"v_mfma_f32_16x16x32_bf16 v[52:55], v[92:95], a[44:47], v[52:55]\n"
"ds_write_addtid_b32 v126 offset:10880\n"
"v_add_f32 v58, v58, v118\n"
"v_mfma_f32_16x16x32_bf16 v[52:55], v[96:99], a[48:51], v[52:55]\n"
"ds_write_addtid_b32 v127 offset:15520\n"
"v_add_f32 v59, v59, v119\n"
"v_mfma_f32_16x16x32_bf16 v[52:55], v[100:103], a[52:55], v[52:55]\n"
"v_mul_f32 v56, v254, v56\n"
"v_mul_f32 v57, v254, v57\n"
"v_mfma_f32_16x16x32_bf16 v[52:55], v[104:107], a[56:59], v[52:55]\n"
"v_mul_f32 v58, v254, v58\n"
"v_mul_f32 v59, v254, v59\n"
"v_mfma_f32_16x16x32_bf16 v[52:55], v[108:111], a[60:63], v[52:55]\n"
"v_cvt_pk_bf16_f32 v60, v56, v57\n"
"v_mfma_f32_16x16x32_bf16 v[72:75], v[80:83], a[128:131], v[72:75]\n"
"v_cvt_pk_bf16_f32 v61, v58, v59\n"
"v_mfma_f32_16x16x32_bf16 v[72:75], v[84:87], a[132:135], v[72:75]\n"
"ds_write_addtid_b32 v52 offset:1792\n"
"ds_write_addtid_b32 v60 offset:18592\n"
"v_mfma_f32_16x16x32_bf16 v[72:75], v[88:91], a[136:139], v[72:75]\n"
"ds_write_addtid_b32 v53 offset:6432\n"
"ds_write_addtid_b32 v61 offset:18848\n"
"v_mfma_f32_16x16x32_bf16 v[72:75], v[92:95], a[140:143], v[72:75]\n"
"s_waitcnt lgkmcnt(12)\n"
"ds_write_addtid_b32 v54 offset:11136\n"
"v_add_f32 v68, v68, v48\n"
"v_mfma_f32_16x16x32_bf16 v[72:75], v[96:99], a[144:147], v[72:75]\n"
"ds_write_addtid_b32 v55 offset:15776\n"
"v_mfma_f32_16x16x32_bf16 v[72:75], v[100:103], a[148:151], v[72:75]\n"
"v_add_f32 v69, v69, v49\n"
"v_add_f32 v70, v70, v50\n"
"v_mfma_f32_16x16x32_bf16 v[72:75], v[104:107], a[152:155], v[72:75]\n"
"v_add_f32 v71, v71, v51\n"
"v_add_f32 v68, v68, v76\n"
"v_mfma_f32_16x16x32_bf16 v[72:75], v[108:111], a[156:159], v[72:75]\n"
"v_add_f32 v69, v69, v77\n"
"v_add_f32 v70, v70, v78\n"
"v_mfma_f32_16x16x32_bf16 v[112:115], v[80:83], a[160:163], v[112:115]\n"
"v_add_f32 v71, v71, v79\n"
"v_add_f32 v68, v68, v120\n"
"v_mfma_f32_16x16x32_bf16 v[112:115], v[84:87], a[164:167], v[112:115]\n"
"v_add_f32 v69, v69, v121\n"
"v_add_f32 v70, v70, v122\n"
"v_mfma_f32_16x16x32_bf16 v[112:115], v[88:91], a[168:171], v[112:115]\n"
"v_add_f32 v71, v71, v123\n"
"v_mul_f32 v68, v254, v68\n"
"v_mfma_f32_16x16x32_bf16 v[112:115], v[92:95], a[172:175], v[112:115]\n"
"v_mul_f32 v69, v254, v69\n"
"v_mul_f32 v70, v254, v70\n"
"v_mfma_f32_16x16x32_bf16 v[112:115], v[96:99], a[176:179], v[112:115]\n"
"s_waitcnt vmcnt(1)\n"
"v_mul_f32 v71, v254, v71\n"
"v_mfma_f32_16x16x32_bf16 v[112:115], v[100:103], a[180:183], v[112:115]\n"
"v_mfma_f32_16x16x32_bf16 v[112:115], v[104:107], a[184:187], v[112:115]\n"
"v_cvt_pk_bf16_f32 v62, v68, v69\n"
"v_mfma_f32_16x16x32_bf16 v[112:115], v[108:111], a[188:191], v[112:115]\n"
"s_waitcnt vmcnt(8)\n"
"v_cvt_pk_bf16_f32 v63, v70, v71\n"
"v_mfma_f32_16x16x32_bf16 v[64:67], a[192:195], a[64:67], 0\n"
"ds_write_addtid_b32 v62 offset:19104\n"
"v_mfma_f32_16x16x32_bf16 v[64:67], a[196:199], a[68:71], v[64:67]\n"
"s_waitcnt vmcnt(6)\n"
"ds_write_addtid_b32 v63 offset:19360\n"
"v_mfma_f32_16x16x32_bf16 v[64:67], a[200:203], a[72:75], v[64:67]\n"
"v_mfma_f32_16x16x32_bf16 v[64:67], a[204:207], a[76:79], v[64:67]\n"
"s_waitcnt vmcnt(4)\n"
"v_mul_f32 v249, 0x3e8293ee, v144\n"
"v_mfma_f32_16x16x32_bf16 v[64:67], a[208:211], a[80:83], v[64:67]\n"
"v_mul_f32 v110, v145, v30\n"
"v_mul_f32 v111, v145, v31\n"
"v_mfma_f32_16x16x32_bf16 v[64:67], a[212:215], a[84:87], v[64:67]\n"
"s_waitcnt vmcnt(2)\n"
"v_mul_f32 v109, v145, v29\n"
"v_mfma_f32_16x16x32_bf16 v[64:67], a[216:219], a[88:91], v[64:67]\n"
"v_mul_f32 v119, v145, v35\n"
"v_mul_f32 v108, v145, v28\n"
"v_mfma_f32_16x16x32_bf16 v[64:67], a[220:223], a[92:95], v[64:67]\n"
"s_waitcnt vmcnt(0)\n"
"buffer_store_dwordx2 v[0:1], v14, s[40:43], s94 offen offset:0 sc1\n"
"v_mul_f32 v118, v145, v34\n"
"v_mfma_f32_16x16x32_bf16 v[68:71], a[192:195], a[96:99], 0\n"
"s_waitcnt lgkmcnt(0)\n"
"v_mul_f32 v116, v145, v32\n"
"v_mul_f32 v117, v145, v33\n"
"v_mfma_f32_16x16x32_bf16 v[68:71], a[196:199], a[100:103], v[68:71]\n"
"s_barrier\n"
"v_mfma_f32_16x16x32_bf16 v[68:71], a[200:203], a[104:107], v[68:71]\n"
"ds_write_addtid_b32 v64 offset:2048\n"
"v_mfma_f32_16x16x32_bf16 v[68:71], a[204:207], a[108:111], v[68:71]\n"
"ds_write_addtid_b32 v65 offset:6688\n"
"ds_read_b128 v[76:79], v2 offset:2560\n"
"v_mfma_f32_16x16x32_bf16 v[68:71], a[208:211], a[112:115], v[68:71]\n"
"ds_write_addtid_b32 v66 offset:11392\n"
"ds_read_b128 v[80:83], v3 offset:3584\n"
"v_mfma_f32_16x16x32_bf16 v[68:71], a[212:215], a[116:119], v[68:71]\n"
"ds_write_addtid_b32 v67 offset:16032\n"
"ds_read_b128 v[84:87], v4 offset:0\n"
"v_mfma_f32_16x16x32_bf16 v[68:71], a[216:219], a[120:123], v[68:71]\n"
"ds_read_b128 v[88:91], v2 offset:2816\n"
"ds_read_b128 v[92:95], v3 offset:3840\n"
"v_mfma_f32_16x16x32_bf16 v[68:71], a[220:223], a[124:127], v[68:71]\n"
"ds_read_b128 v[96:99], v4 offset:256\n"
"v_mfma_f32_16x16x32_bf16 v[100:103], a[128:131], a[192:195], 0\n"
"v_mfma_f32_16x16x32_bf16 v[100:103], a[132:135], a[196:199], v[100:103]\n"
"ds_write_addtid_b32 v68 offset:2304\n"
"v_mfma_f32_16x16x32_bf16 v[100:103], a[136:139], a[200:203], v[100:103]\n"
"ds_write_addtid_b32 v69 offset:6944\n"
"v_mfma_f32_16x16x32_bf16 v[100:103], a[140:143], a[204:207], v[100:103]\n"
"s_waitcnt lgkmcnt(5)\n"
"ds_write_addtid_b32 v70 offset:11648\n"
"v_add_f32 v76, v76, v80\n"
"v_mfma_f32_16x16x32_bf16 v[100:103], a[144:147], a[208:211], v[100:103]\n"
"ds_write_addtid_b32 v71 offset:16288\n"
"v_mfma_f32_16x16x32_bf16 v[100:103], a[148:151], a[212:215], v[100:103]\n"
"v_add_f32 v77, v77, v81\n"
"v_add_f32 v78, v78, v82\n"
"v_mfma_f32_16x16x32_bf16 v[100:103], a[152:155], a[216:219], v[100:103]\n"
"v_add_f32 v79, v79, v83\n"
"v_add_f32 v76, v76, v84\n"
"v_mfma_f32_16x16x32_bf16 v[100:103], a[156:159], a[220:223], v[100:103]\n"
"v_add_f32 v77, v77, v85\n"
"v_add_f32 v78, v78, v86\n"
"v_mfma_f32_16x16x32_bf16 v[80:83], a[160:163], a[192:195], 0\n"
"v_add_f32 v79, v79, v87\n"
"v_add_f32 v76, v76, v72\n"
"v_mfma_f32_16x16x32_bf16 v[80:83], a[164:167], a[196:199], v[80:83]\n"
"ds_write_addtid_b32 v100 offset:2560\n"
"v_mfma_f32_16x16x32_bf16 v[80:83], a[168:171], a[200:203], v[80:83]\n"
"ds_write_addtid_b32 v101 offset:7200\n"
"v_add_f32 v77, v77, v73\n"
"v_mfma_f32_16x16x32_bf16 v[80:83], a[172:175], a[204:207], v[80:83]\n"
"ds_write_addtid_b32 v102 offset:11904\n"
"v_add_f32 v78, v78, v74\n"
"v_mfma_f32_16x16x32_bf16 v[80:83], a[176:179], a[208:211], v[80:83]\n"
"ds_write_addtid_b32 v103 offset:16544\n"
"v_add_f32 v79, v79, v75\n"
"v_mfma_f32_16x16x32_bf16 v[80:83], a[180:183], a[212:215], v[80:83]\n"
"v_mul_f32_dpp v76, v253, v76 row_newbcast:0 row_mask:0xf bank_mask:0xf\n"
"v_mul_f32_dpp v77, v253, v77 row_newbcast:1 row_mask:0xf bank_mask:0xf\n"
"v_mfma_f32_16x16x32_bf16 v[80:83], a[184:187], a[216:219], v[80:83]\n"
"v_mul_f32_dpp v78, v253, v78 row_newbcast:2 row_mask:0xf bank_mask:0xf\n"
"v_mfma_f32_16x16x32_bf16 v[80:83], a[188:191], a[220:223], v[80:83]\n"
"v_mul_f32_dpp v79, v253, v79 row_newbcast:3 row_mask:0xf bank_mask:0xf\n"
"v_cvt_pk_bf16_f32 v142, v76, v77\n"
"v_mfma_f32_16x16x32_bf16 v[108:111], a[0:3], a[192:195], v[108:111]\n"
"v_cvt_pk_bf16_f32 v143, v78, v79\n"
"v_mfma_f32_16x16x32_bf16 v[108:111], a[4:7], a[196:199], v[108:111]\n"
"ds_write_addtid_b32 v80 offset:2816\n"
"ds_write_addtid_b32 v142 offset:19616\n"
"v_mfma_f32_16x16x32_bf16 v[108:111], a[8:11], a[200:203], v[108:111]\n"
"ds_write_addtid_b32 v81 offset:7456\n"
"ds_write_addtid_b32 v143 offset:19872\n"
"v_mfma_f32_16x16x32_bf16 v[108:111], a[12:15], a[204:207], v[108:111]\n"
"s_waitcnt lgkmcnt(12)\n"
"ds_write_addtid_b32 v82 offset:12160\n"
"v_mfma_f32_16x16x32_bf16 v[108:111], a[16:19], a[208:211], v[108:111]\n"
"ds_write_addtid_b32 v83 offset:16800\n"
"v_add_f32 v88, v88, v92\n"
"v_mfma_f32_16x16x32_bf16 v[108:111], a[20:23], a[212:215], v[108:111]\n"
"v_add_f32 v89, v89, v93\n"
"v_add_f32 v90, v90, v94\n"
"v_mfma_f32_16x16x32_bf16 v[108:111], a[24:27], a[216:219], v[108:111]\n"
"v_add_f32 v91, v91, v95\n"
"v_add_f32 v88, v88, v96\n"
"v_mfma_f32_16x16x32_bf16 v[108:111], a[28:31], a[220:223], v[108:111]\n"
"v_add_f32 v89, v89, v97\n"
"v_add_f32 v90, v90, v98\n"
"v_mfma_f32_16x16x32_bf16 v[116:119], a[32:35], a[192:195], v[116:119]\n"
"v_add_f32 v91, v91, v99\n"
"v_add_f32 v88, v88, v112\n"
"v_mfma_f32_16x16x32_bf16 v[116:119], a[36:39], a[196:199], v[116:119]\n"
"v_add_f32 v89, v89, v113\n"
"v_add_f32 v90, v90, v114\n"
"v_mfma_f32_16x16x32_bf16 v[116:119], a[40:43], a[200:203], v[116:119]\n"
"v_mfma_f32_16x16x32_bf16 v[116:119], a[44:47], a[204:207], v[116:119]\n"
"v_add_f32 v91, v91, v115\n"
"v_mul_f32_dpp v88, v253, v88 row_newbcast:0 row_mask:0xf bank_mask:0xf\n"
"v_mfma_f32_16x16x32_bf16 v[116:119], a[48:51], a[208:211], v[116:119]\n"
"v_mul_f32_dpp v89, v253, v89 row_newbcast:1 row_mask:0xf bank_mask:0xf\n"
"v_mul_f32_dpp v90, v253, v90 row_newbcast:2 row_mask:0xf bank_mask:0xf\n"
"v_mfma_f32_16x16x32_bf16 v[116:119], a[52:55], a[212:215], v[116:119]\n"
"v_mul_f32_dpp v91, v253, v91 row_newbcast:3 row_mask:0xf bank_mask:0xf\n"
"v_cvt_pk_bf16_f32 v150, v88, v89\n"
"v_mfma_f32_16x16x32_bf16 v[116:119], a[56:59], a[216:219], v[116:119]\n"
"v_cvt_pk_bf16_f32 v151, v90, v91\n"
"v_mfma_f32_16x16x32_bf16 v[116:119], a[60:63], a[220:223], v[116:119]\n"
"ds_write_addtid_b32 v150 offset:20128\n"
"ds_write_addtid_b32 v151 offset:20384\n"
"s_waitcnt lgkmcnt(0)\n"
"s_barrier\n"
"ds_read_b128 v[112:115], v2 offset:4096\n"
"ds_read_b128 v[104:107], v3 offset:512\n"
"ds_read_b128 v[120:123], v4 offset:1536\n"
"ds_read_b128 v[124:127], v2 offset:4352\n"
"ds_read_b128 v[152:155], v3 offset:768\n"
"ds_read_b128 v[156:159], v4 offset:1792\n"
"s_waitcnt lgkmcnt(3)\n"
"v_add_f32 v112, v112, v104\n"
"v_add_f32 v113, v113, v105\n"
"v_add_f32 v114, v114, v106\n"
"v_add_f32 v115, v115, v107\n"
"v_add_f32 v112, v112, v120\n"
"v_add_f32 v113, v113, v121\n"
"v_add_f32 v114, v114, v122\n"
"v_add_f32 v115, v115, v123\n"
"v_add_f32 v112, v112, v108\n"
"v_add_f32 v113, v113, v109\n"
"v_add_f32 v114, v114, v110\n"
"v_add_f32 v115, v115, v111\n"
"v_cvt_pk_bf16_f32 v164, v112, v113\n"
"v_cvt_pk_bf16_f32 v165, v114, v115\n"
"ds_read2st64_b32 v[120:121], v11 offset0:0 offset1:1\n"
"ds_read2st64_b32 v[122:123], v11 offset0:2 offset1:3\n"
"s_waitcnt lgkmcnt(2)\n"
"v_add_f32 v124, v124, v152\n"
"v_add_f32 v125, v125, v153\n"
"v_add_f32 v126, v126, v154\n"
"v_add_f32 v127, v127, v155\n"
"v_add_f32 v124, v124, v156\n"
"v_add_f32 v125, v125, v157\n"
"v_add_f32 v126, v126, v158\n"
"v_add_f32 v127, v127, v159\n"
"v_add_f32 v124, v124, v116\n"
"v_add_f32 v125, v125, v117\n"
"v_add_f32 v126, v126, v118\n"
"v_add_f32 v127, v127, v119\n"
"v_cvt_pk_bf16_f32 v166, v124, v125\n"
"v_cvt_pk_bf16_f32 v167, v126, v127\n"
"s_waitcnt lgkmcnt(0)\n"
"s_nop 0\n"
"v_mfma_f32_16x16x32_bf16 v[156:159], v[120:123], v[164:167], v[20:23]\n"
"s_barrier\n"
"ds_read2st64_b32 v[128:129], v12 offset0:0 offset1:1\n"
"ds_read2st64_b32 v[130:131], v12 offset0:2 offset1:3\n"
"ds_read2st64_b32 v[140:141], v11 offset0:4 offset1:5\n"
"ds_read2st64_b32 v[148:149], v11 offset0:6 offset1:7\n"
"s_add_u32 s94, s50, 0x0\n"
"s_and_b32 s95, s94, 0xfbffffff\n"
"s_waitcnt lgkmcnt(2)\n"
"v_mfma_f32_16x16x32_bf16 v[132:135], v[128:131], v[164:167], v[16:19]\n"
"s_cmp_eq_u32 s94, s62\n"
"s_cbranch_scc1 .LQ_ft3k0s\n"
".LQ_ft3k0r:\n"
"v_max_f32 v253, v156, v158\n"
"v_max_f32 v252, v157, v159\n"
"s_nop 3\n"
"v_max3_f32 v253, v132, v134, v253\n"
"v_max3_f32 v252, v133, v135, v252\n"
"v_max_f32 v251, v253, v252\n"
"v_mov_b32 v250, v251\n"
"s_nop 1\n"
"v_permlane32_swap_b32 v250, v251\n"
"v_max_f32 v251, v250, v251\n"
"v_mov_b32 v250, v251\n"
"s_nop 1\n"
"v_permlane16_swap_b32 v250, v251\n"
"v_max_f32 v251, v250, v251\n"
"v_mul_f32 v251, v251, v249\n"
"v_fma_f32 v253, v253, v249, -v251\n"
"v_fma_f32 v252, v252, v249, -v251\n"
"v_exp_f32 v253, v253\n"
"v_exp_f32 v252, v252\n"
"s_nop 0\n"
"v_add_f32 v251, v253, v252\n"
"v_mov_b32 v250, v251\n"
"s_nop 1\n"
"v_permlane32_swap_b32 v250, v251\n"
"v_add_f32 v251, v250, v251\n"
"v_mov_b32 v250, v251\n"
"s_nop 1\n"
"v_permlane16_swap_b32 v250, v251\n"
"v_add_f32 v251, v250, v251\n"
"v_rcp_f32 v251, v251\n"
"s_nop 0\n"
"v_mul_f32 v253, v253, v251\n"
"v_mul_f32 v252, v252, v251\n"
"v_cvt_pk_bf16_f32 v250, v253, v252\n"
"s_waitcnt lgkmcnt(0)\n"
"v_and_b32 v52, v250, v24\n"
"v_and_b32 v53, v250, v25\n"
"v_and_b32 v54, v250, v26\n"
"v_and_b32 v55, v250, v27\n"
"s_nop 1\n"
"v_mfma_f32_16x16x32_bf16 v[56:59], v[140:143], v[52:55], 0\n"
"v_mfma_f32_16x16x32_bf16 v[60:63], v[148:151], v[52:55], 0\n"
"s_nop 6\n"
"v_cvt_pk_bf16_f32 v52, v56, v57\n"
"v_cvt_pk_bf16_f32 v53, v58, v59\n"
"v_cvt_pk_bf16_f32 v54, v60, v61\n"
"v_cvt_pk_bf16_f32 v55, v62, v63\n"
"buffer_store_dwordx4 v[52:55], v13, s[40:43], s95 offen sc1\n"
"s_waitcnt vmcnt(0)\n"
"buffer_store_dwordx2 v[0:1], v14, s[40:43], s94 offen offset:0 sc1\n"
"s_branch .LQ_end\n"
".LQ_end:\n"
"s_waitcnt vmcnt(0) lgkmcnt(0)\n"
"s_endpgm\n"
      :: "{v2}"((unsigned)(lds0 + ((wid + 3) & 3) * 20640 + trp)), "{v3}"((unsigned)(lds0 + ((wid + 2) & 3) * 20640 + trp)), "{v4}"((unsigned)(lds0 + ((wid + 1) & 3) * 20640 + trp)), "{v5}"((unsigned)(lane * 16 + wid * 8192 + ((0 + rot) % 4) * 2048 + 134217728)), "{v6}"((unsigned)(lane * 16 + wid * 8192 + ((1 + rot) % 4) * 2048 + 134217728)), "{v7}"((unsigned)(lane * 16 + wid * 8192 + ((2 + rot) % 4) * 2048 + 134217728)), "{v8}"((unsigned)(lane * 16 + wid * 8192 + ((3 + rot) % 4) * 2048 + 134217728)), "{v9}"((unsigned)(li * 16 + 352256 + Q_GL * 1048576)), "{v10}"((unsigned)(((li + 4 * lg) & 15) * 16 + 352256 + Q_GL * 1048576)), "{v11}"((unsigned)(lds0 + ((wid + 1) & 3) * 20640 + 18592 + lane * 4)), "{v12}"((unsigned)(lds0 + ((wid + 2) & 3) * 20640 + 18592 + lane * 4)), "{v13}"((unsigned)(lane * 16 + idx * 1024 + 32768)), "{v14}"((unsigned)(lane ? DROP : idx * 8 + 352512)), "{v16}"((unsigned)(qmask(0, 0))), "{v17}"((unsigned)(qmask(1, 0))), "{v18}"((unsigned)(qmask(2, 0))), "{v19}"((unsigned)(qmask(3, 0))), "{v20}"((unsigned)(qmask(0, 1))), "{v21}"((unsigned)(qmask(1, 1))), "{v22}"((unsigned)(qmask(2, 1))), "{v23}"((unsigned)(qmask(3, 1))), "{v24}"((unsigned)(pmask(0))), "{v25}"((unsigned)(pmask(1))), "{v26}"((unsigned)(pmask(2))), "{v27}"((unsigned)(pmask(3))), "{v48}"((unsigned)(vT + tsw)), "{v49}"((unsigned)(vT + (tsw ^ 8))), "{v50}"((unsigned)((sEnt == 1 ? (li + 4 * lg) & 15 : li) * 16 + 1400832)), "{v51}"((unsigned)((sEnt == 0 ? (li + 4 * lg) & 15 : li) * 16 + 2449408)), "{v52}"((unsigned)(lane * 16 + wid * 8192 + (idx & 7) * 1048576 + 134221824)), "{v53}"((unsigned)(vD + ((0 + rot) % 4) * 393216)), "{v54}"((unsigned)(vD + ((1 + rot) % 4) * 393216)), "{v55}"((unsigned)(vD + ((2 + rot) % 4) * 393216)), "{v56}"((unsigned)(vD + ((3 + rot) % 4) * 393216)), "{v57}"((unsigned)(lds0 + wid * 20640 + trp)), "{s[40:43]}"(rS), "{s[44:47]}"(rW), "{s[48:49]}"(wsp), "{s50}"((unsigned)(sA)), "{s51}"((unsigned)(sLo)), "{s52}"((unsigned)(sHi)), "{s53}"((unsigned)(sEx)), "{s54}"((unsigned)(sEnt)), "{s55}"((unsigned)(sLd)), "{s[56:57]}"(wsb + (sLo & 0x3ffffff)), "{s59}"((unsigned)(lds0 + wid * 20640)), "{s62}"((unsigned)(sLo)), "{s63}"((unsigned)(sW0 + 0)), "{s64}"((unsigned)(sW0 + 2048)), "{s65}"((unsigned)(sW0 + 4096))
      : "v255", "a255", "s95", "vcc", "scc", "memory");

  }
  while (__builtin_amdgcn_s_memrealtime() - tz < (role == 1 ? DLO : role == 2 ? (xo < 0 ? DLF : DLB) : DLG)) __builtin_amdgcn_s_sleep(4);
  if (role == 2 && xo < 0 && (SLP & 4)) {
#pragma unroll
    for (int i = 0; i < 4 * TW; ++i)
      asm volatile("global_load_dword v252, %0, %1" ::"v"((wid * 256 + i / TW * 64 + (xj - 16) * 4 + lg) * 7168 + (14 * (xo + 4) + min(li, 13)) * 128 + i % TW * 64), "s"(a.w1) : "v252");
  }
  if (role == 3 && (SLP & 8) && !((SLP & 32) && p2 < 128)) tw2();
  const unsigned sA = sLo - 3 * RS, sHi = sLo + (set ? 84 : 85) * 3 * RS;
  if (role == 1) {
    const unsigned sEx = sHi + O_EX * 3 * RS, sEnt = OR ? -wid & 3 : 0, tsw = 0, pair = idx * 4 + wid, rot = 0;
    const unsigned sW0 = wid * 256 * 2048 + idx * 256, vD = (lane >> 2) * 2048 + (lane & 3) * 16;
    const u32x4 rW = desc(a.wo, 1024 * 2048);
    asm volatile(
"s_nop 4\n"
"v_mov_b32 v186, 0\n"
"global_load_dwordx2 v[0:1], v186, s[48:49] sc1\n"
"v_mov_b32 v188, 0x3f803f80\n"
"v_mov_b32 v189, 0x3f803f80\n"
"v_mov_b32 v190, 0x3f803f80\n"
"v_mov_b32 v191, 0x3f803f80\n"
"s_add_u32 m0, s55, 0\n"
"s_add_u32 s60, s66, 0\n"
"buffer_load_dwordx4 v22, s[44:47], s60 offen lds\n"
"s_add_u32 m0, s55, 1024\n"
"s_add_u32 s60, s66, 32768\n"
"buffer_load_dwordx4 v22, s[44:47], s60 offen lds\n"
"s_add_u32 m0, s55, 2048\n"
"s_add_u32 s60, s67, 0\n"
"buffer_load_dwordx4 v22, s[44:47], s60 offen lds\n"
"s_add_u32 m0, s55, 3072\n"
"s_add_u32 s60, s67, 32768\n"
"buffer_load_dwordx4 v22, s[44:47], s60 offen lds\n"
"s_add_u32 m0, s55, 4096\n"
"s_add_u32 s60, s68, 0\n"
"buffer_load_dwordx4 v22, s[44:47], s60 offen lds\n"
"s_add_u32 m0, s55, 5120\n"
"s_add_u32 s60, s68, 32768\n"
"buffer_load_dwordx4 v22, s[44:47], s60 offen lds\n"
"s_add_u32 m0, s55, 6144\n"
"s_add_u32 s60, s69, 0\n"
"buffer_load_dwordx4 v22, s[44:47], s60 offen lds\n"
"s_add_u32 m0, s55, 7168\n"
"s_add_u32 s60, s69, 32768\n"
"buffer_load_dwordx4 v22, s[44:47], s60 offen lds\n"
"s_add_u32 m0, s55, 8192\n"
"s_add_u32 s60, s66, 65536\n"
"buffer_load_dwordx4 v22, s[44:47], s60 offen lds\n"
"s_add_u32 m0, s55, 9216\n"
"s_add_u32 s60, s66, 98304\n"
"buffer_load_dwordx4 v22, s[44:47], s60 offen lds\n"
"s_add_u32 m0, s55, 10240\n"
"s_add_u32 s60, s67, 65536\n"
"buffer_load_dwordx4 v22, s[44:47], s60 offen lds\n"
"s_add_u32 m0, s55, 11264\n"
"s_add_u32 s60, s67, 98304\n"
"buffer_load_dwordx4 v22, s[44:47], s60 offen lds\n"
"s_add_u32 m0, s55, 12288\n"
"s_add_u32 s60, s68, 65536\n"
"buffer_load_dwordx4 v22, s[44:47], s60 offen lds\n"
"s_add_u32 m0, s55, 13312\n"
"s_add_u32 s60, s68, 98304\n"
"buffer_load_dwordx4 v22, s[44:47], s60 offen lds\n"
"s_add_u32 m0, s55, 14336\n"
"s_add_u32 s60, s69, 65536\n"
"buffer_load_dwordx4 v22, s[44:47], s60 offen lds\n"
"s_add_u32 m0, s55, 15360\n"
"s_add_u32 s60, s69, 98304\n"
"buffer_load_dwordx4 v22, s[44:47], s60 offen lds\n"
"s_add_u32 m0, s55, 16384\n"
"s_add_u32 s60, s66, 0\n"
"buffer_load_dwordx4 v23, s[44:47], s60 offen lds\n"
"s_add_u32 m0, s55, 17408\n"
"s_add_u32 s60, s66, 32768\n"
"buffer_load_dwordx4 v23, s[44:47], s60 offen lds\n"
"s_add_u32 m0, s55, 18432\n"
"s_add_u32 s60, s67, 0\n"
"buffer_load_dwordx4 v23, s[44:47], s60 offen lds\n"
"s_add_u32 m0, s55, 19456\n"
"s_add_u32 s60, s67, 32768\n"
"buffer_load_dwordx4 v23, s[44:47], s60 offen lds\n"
"s_add_u32 m0, s55, 20480\n"
"s_add_u32 s60, s68, 0\n"
"buffer_load_dwordx4 v23, s[44:47], s60 offen lds\n"
"s_add_u32 m0, s55, 21504\n"
"s_add_u32 s60, s68, 32768\n"
"buffer_load_dwordx4 v23, s[44:47], s60 offen lds\n"
"s_add_u32 m0, s55, 22528\n"
"s_add_u32 s60, s69, 0\n"
"buffer_load_dwordx4 v23, s[44:47], s60 offen lds\n"
"s_add_u32 m0, s55, 23552\n"
"s_add_u32 s60, s69, 32768\n"
"buffer_load_dwordx4 v23, s[44:47], s60 offen lds\n"
"s_add_u32 m0, s55, 24576\n"
"s_add_u32 s60, s66, 65536\n"
"buffer_load_dwordx4 v23, s[44:47], s60 offen lds\n"
"s_add_u32 m0, s55, 25600\n"
"s_add_u32 s60, s66, 98304\n"
"buffer_load_dwordx4 v23, s[44:47], s60 offen lds\n"
"s_add_u32 m0, s55, 26624\n"
"s_add_u32 s60, s67, 65536\n"
"buffer_load_dwordx4 v23, s[44:47], s60 offen lds\n"
"s_add_u32 m0, s55, 27648\n"
"s_add_u32 s60, s67, 98304\n"
"buffer_load_dwordx4 v23, s[44:47], s60 offen lds\n"
"s_add_u32 m0, s55, 28672\n"
"s_add_u32 s60, s68, 65536\n"
"buffer_load_dwordx4 v23, s[44:47], s60 offen lds\n"
"s_add_u32 m0, s55, 29696\n"
"s_add_u32 s60, s68, 98304\n"
"buffer_load_dwordx4 v23, s[44:47], s60 offen lds\n"
"s_add_u32 m0, s55, 30720\n"
"s_add_u32 s60, s69, 65536\n"
"buffer_load_dwordx4 v23, s[44:47], s60 offen lds\n"
"s_add_u32 m0, s55, 31744\n"
"s_add_u32 s60, s69, 98304\n"
"buffer_load_dwordx4 v23, s[44:47], s60 offen lds\n"
"s_add_u32 m0, s55, 32768\n"
"s_add_u32 s60, s66, 0\n"
"buffer_load_dwordx4 v24, s[44:47], s60 offen lds\n"
"s_add_u32 m0, s55, 33792\n"
"s_add_u32 s60, s66, 32768\n"
"buffer_load_dwordx4 v24, s[44:47], s60 offen lds\n"
"s_add_u32 m0, s55, 34816\n"
"s_add_u32 s60, s67, 0\n"
"buffer_load_dwordx4 v24, s[44:47], s60 offen lds\n"
"s_add_u32 m0, s55, 35840\n"
"s_add_u32 s60, s67, 32768\n"
"buffer_load_dwordx4 v24, s[44:47], s60 offen lds\n"
"s_add_u32 m0, s55, 36864\n"
"s_add_u32 s60, s68, 0\n"
"buffer_load_dwordx4 v24, s[44:47], s60 offen lds\n"
"s_add_u32 m0, s55, 37888\n"
"s_add_u32 s60, s68, 32768\n"
"buffer_load_dwordx4 v24, s[44:47], s60 offen lds\n"
"s_add_u32 m0, s55, 38912\n"
"s_add_u32 s60, s69, 0\n"
"buffer_load_dwordx4 v24, s[44:47], s60 offen lds\n"
"s_add_u32 m0, s55, 39936\n"
"s_add_u32 s60, s69, 32768\n"
"buffer_load_dwordx4 v24, s[44:47], s60 offen lds\n"
"s_waitcnt vmcnt(38)\n"
"ds_read_b64_tr_b16 a[0:1], v20 offset:0\n"
"ds_read_b64_tr_b16 a[2:3], v20 offset:256\n"
"ds_read_b64_tr_b16 a[32:33], v21 offset:0\n"
"ds_read_b64_tr_b16 a[34:35], v21 offset:256\n"
"v_add_u32 v0, 2, v0\n"
"v_add_u32 v1, 0x9e3779b9, v1\n"
"v_or_b32 v0, 1, v0\n"
"v_readfirstlane_b32 s49, v1\n"
"v_readfirstlane_b32 s48, v0\n"
"s_waitcnt vmcnt(36)\n"
"ds_read_b64_tr_b16 a[64:65], v20 offset:2048\n"
"ds_read_b64_tr_b16 a[66:67], v20 offset:2304\n"
"ds_read_b64_tr_b16 a[96:97], v21 offset:2048\n"
"ds_read_b64_tr_b16 a[98:99], v21 offset:2304\n"
"s_waitcnt lgkmcnt(4)\n"
"s_add_u32 m0, s55, 0\n"
"s_add_u32 s60, s66, 65536\n"
"buffer_load_dwordx4 v24, s[44:47], s60 offen lds\n"
"s_add_u32 m0, s55, 1024\n"
"s_add_u32 s60, s66, 98304\n"
"buffer_load_dwordx4 v24, s[44:47], s60 offen lds\n"
"s_waitcnt vmcnt(36)\n"
"ds_read_b64_tr_b16 a[128:129], v20 offset:4096\n"
"ds_read_b64_tr_b16 a[130:131], v20 offset:4352\n"
"ds_read_b64_tr_b16 a[160:161], v21 offset:4096\n"
"ds_read_b64_tr_b16 a[162:163], v21 offset:4352\n"
"s_waitcnt lgkmcnt(4)\n"
"s_add_u32 m0, s55, 2048\n"
"s_add_u32 s60, s67, 65536\n"
"buffer_load_dwordx4 v24, s[44:47], s60 offen lds\n"
"s_add_u32 m0, s55, 3072\n"
"s_add_u32 s60, s67, 98304\n"
"buffer_load_dwordx4 v24, s[44:47], s60 offen lds\n"
"s_waitcnt vmcnt(36)\n"
"ds_read_b64_tr_b16 a[192:193], v20 offset:6144\n"
"ds_read_b64_tr_b16 a[194:195], v20 offset:6400\n"
"ds_read_b64_tr_b16 a[224:225], v21 offset:6144\n"
"ds_read_b64_tr_b16 a[226:227], v21 offset:6400\n"
"s_waitcnt lgkmcnt(4)\n"
"s_add_u32 m0, s55, 4096\n"
"s_add_u32 s60, s68, 65536\n"
"buffer_load_dwordx4 v24, s[44:47], s60 offen lds\n"
"s_add_u32 m0, s55, 5120\n"
"s_add_u32 s60, s68, 98304\n"
"buffer_load_dwordx4 v24, s[44:47], s60 offen lds\n"
"s_waitcnt vmcnt(36)\n"
"ds_read_b64_tr_b16 a[4:5], v20 offset:8192\n"
"ds_read_b64_tr_b16 a[6:7], v20 offset:8448\n"
"ds_read_b64_tr_b16 a[36:37], v21 offset:8192\n"
"ds_read_b64_tr_b16 a[38:39], v21 offset:8448\n"
"s_waitcnt lgkmcnt(4)\n"
"s_add_u32 m0, s55, 6144\n"
"s_add_u32 s60, s69, 65536\n"
"buffer_load_dwordx4 v24, s[44:47], s60 offen lds\n"
"s_add_u32 m0, s55, 7168\n"
"s_add_u32 s60, s69, 98304\n"
"buffer_load_dwordx4 v24, s[44:47], s60 offen lds\n"
"s_waitcnt vmcnt(36)\n"
"ds_read_b64_tr_b16 a[68:69], v20 offset:10240\n"
"ds_read_b64_tr_b16 a[70:71], v20 offset:10496\n"
"ds_read_b64_tr_b16 a[100:101], v21 offset:10240\n"
"ds_read_b64_tr_b16 a[102:103], v21 offset:10496\n"
"s_waitcnt lgkmcnt(4)\n"
"s_add_u32 m0, s55, 8192\n"
"s_add_u32 s60, s66, 0\n"
"buffer_load_dwordx4 v25, s[44:47], s60 offen lds\n"
"s_add_u32 m0, s55, 9216\n"
"s_add_u32 s60, s66, 32768\n"
"buffer_load_dwordx4 v25, s[44:47], s60 offen lds\n"
"s_waitcnt vmcnt(36)\n"
"ds_read_b64_tr_b16 a[132:133], v20 offset:12288\n"
"ds_read_b64_tr_b16 a[134:135], v20 offset:12544\n"
"ds_read_b64_tr_b16 a[164:165], v21 offset:12288\n"
"ds_read_b64_tr_b16 a[166:167], v21 offset:12544\n"
"s_waitcnt lgkmcnt(4)\n"
"s_add_u32 m0, s55, 10240\n"
"s_add_u32 s60, s67, 0\n"
"buffer_load_dwordx4 v25, s[44:47], s60 offen lds\n"
"s_add_u32 m0, s55, 11264\n"
"s_add_u32 s60, s67, 32768\n"
"buffer_load_dwordx4 v25, s[44:47], s60 offen lds\n"
"s_waitcnt vmcnt(36)\n"
"ds_read_b64_tr_b16 a[196:197], v20 offset:14336\n"
"ds_read_b64_tr_b16 a[198:199], v20 offset:14592\n"
"ds_read_b64_tr_b16 a[228:229], v21 offset:14336\n"
"ds_read_b64_tr_b16 a[230:231], v21 offset:14592\n"
"s_waitcnt lgkmcnt(4)\n"
"s_add_u32 m0, s55, 12288\n"
"s_add_u32 s60, s68, 0\n"
"buffer_load_dwordx4 v25, s[44:47], s60 offen lds\n"
"s_add_u32 m0, s55, 13312\n"
"s_add_u32 s60, s68, 32768\n"
"buffer_load_dwordx4 v25, s[44:47], s60 offen lds\n"
"s_waitcnt vmcnt(36)\n"
"ds_read_b64_tr_b16 a[8:9], v20 offset:16384\n"
"ds_read_b64_tr_b16 a[10:11], v20 offset:16640\n"
"ds_read_b64_tr_b16 a[40:41], v21 offset:16384\n"
"ds_read_b64_tr_b16 a[42:43], v21 offset:16640\n"
"s_waitcnt lgkmcnt(4)\n"
"s_add_u32 m0, s55, 14336\n"
"s_add_u32 s60, s69, 0\n"
"buffer_load_dwordx4 v25, s[44:47], s60 offen lds\n"
"s_add_u32 m0, s55, 15360\n"
"s_add_u32 s60, s69, 32768\n"
"buffer_load_dwordx4 v25, s[44:47], s60 offen lds\n"
"s_waitcnt vmcnt(36)\n"
"ds_read_b64_tr_b16 a[72:73], v20 offset:18432\n"
"ds_read_b64_tr_b16 a[74:75], v20 offset:18688\n"
"ds_read_b64_tr_b16 a[104:105], v21 offset:18432\n"
"ds_read_b64_tr_b16 a[106:107], v21 offset:18688\n"
"s_waitcnt lgkmcnt(4)\n"
"s_add_u32 m0, s55, 16384\n"
"s_add_u32 s60, s66, 65536\n"
"buffer_load_dwordx4 v25, s[44:47], s60 offen lds\n"
"s_add_u32 m0, s55, 17408\n"
"s_add_u32 s60, s66, 98304\n"
"buffer_load_dwordx4 v25, s[44:47], s60 offen lds\n"
"s_waitcnt vmcnt(36)\n"
"ds_read_b64_tr_b16 a[136:137], v20 offset:20480\n"
"ds_read_b64_tr_b16 a[138:139], v20 offset:20736\n"
"ds_read_b64_tr_b16 a[168:169], v21 offset:20480\n"
"ds_read_b64_tr_b16 a[170:171], v21 offset:20736\n"
"s_waitcnt lgkmcnt(4)\n"
"s_add_u32 m0, s55, 18432\n"
"s_add_u32 s60, s67, 65536\n"
"buffer_load_dwordx4 v25, s[44:47], s60 offen lds\n"
"s_add_u32 m0, s55, 19456\n"
"s_add_u32 s60, s67, 98304\n"
"buffer_load_dwordx4 v25, s[44:47], s60 offen lds\n"
"s_waitcnt vmcnt(36)\n"
"ds_read_b64_tr_b16 a[200:201], v20 offset:22528\n"
"ds_read_b64_tr_b16 a[202:203], v20 offset:22784\n"
"ds_read_b64_tr_b16 a[232:233], v21 offset:22528\n"
"ds_read_b64_tr_b16 a[234:235], v21 offset:22784\n"
"s_waitcnt lgkmcnt(4)\n"
"s_add_u32 m0, s55, 20480\n"
"s_add_u32 s60, s68, 65536\n"
"buffer_load_dwordx4 v25, s[44:47], s60 offen lds\n"
"s_add_u32 m0, s55, 21504\n"
"s_add_u32 s60, s68, 98304\n"
"buffer_load_dwordx4 v25, s[44:47], s60 offen lds\n"
"s_waitcnt vmcnt(36)\n"
"ds_read_b64_tr_b16 a[12:13], v20 offset:24576\n"
"ds_read_b64_tr_b16 a[14:15], v20 offset:24832\n"
"ds_read_b64_tr_b16 a[44:45], v21 offset:24576\n"
"ds_read_b64_tr_b16 a[46:47], v21 offset:24832\n"
"s_waitcnt lgkmcnt(4)\n"
"s_add_u32 m0, s55, 22528\n"
"s_add_u32 s60, s69, 65536\n"
"buffer_load_dwordx4 v25, s[44:47], s60 offen lds\n"
"s_add_u32 m0, s55, 23552\n"
"s_add_u32 s60, s69, 98304\n"
"buffer_load_dwordx4 v25, s[44:47], s60 offen lds\n"
"s_waitcnt vmcnt(36)\n"
"ds_read_b64_tr_b16 a[76:77], v20 offset:26624\n"
"ds_read_b64_tr_b16 a[78:79], v20 offset:26880\n"
"ds_read_b64_tr_b16 a[108:109], v21 offset:26624\n"
"ds_read_b64_tr_b16 a[110:111], v21 offset:26880\n"
"s_waitcnt lgkmcnt(4)\n"
"s_waitcnt vmcnt(34)\n"
"ds_read_b64_tr_b16 a[140:141], v20 offset:28672\n"
"ds_read_b64_tr_b16 a[142:143], v20 offset:28928\n"
"ds_read_b64_tr_b16 a[172:173], v21 offset:28672\n"
"ds_read_b64_tr_b16 a[174:175], v21 offset:28928\n"
"s_waitcnt lgkmcnt(4)\n"
"s_waitcnt vmcnt(32)\n"
"ds_read_b64_tr_b16 a[204:205], v20 offset:30720\n"
"ds_read_b64_tr_b16 a[206:207], v20 offset:30976\n"
"ds_read_b64_tr_b16 a[236:237], v21 offset:30720\n"
"ds_read_b64_tr_b16 a[238:239], v21 offset:30976\n"
"s_waitcnt lgkmcnt(4)\n"
"s_waitcnt vmcnt(30)\n"
"ds_read_b64_tr_b16 a[16:17], v20 offset:32768\n"
"ds_read_b64_tr_b16 a[18:19], v20 offset:33024\n"
"ds_read_b64_tr_b16 a[48:49], v21 offset:32768\n"
"ds_read_b64_tr_b16 a[50:51], v21 offset:33024\n"
"s_waitcnt lgkmcnt(4)\n"
"s_waitcnt vmcnt(28)\n"
"ds_read_b64_tr_b16 a[80:81], v20 offset:34816\n"
"ds_read_b64_tr_b16 a[82:83], v20 offset:35072\n"
"ds_read_b64_tr_b16 a[112:113], v21 offset:34816\n"
"ds_read_b64_tr_b16 a[114:115], v21 offset:35072\n"
"s_waitcnt lgkmcnt(4)\n"
"s_waitcnt vmcnt(26)\n"
"ds_read_b64_tr_b16 a[144:145], v20 offset:36864\n"
"ds_read_b64_tr_b16 a[146:147], v20 offset:37120\n"
"ds_read_b64_tr_b16 a[176:177], v21 offset:36864\n"
"ds_read_b64_tr_b16 a[178:179], v21 offset:37120\n"
"s_waitcnt lgkmcnt(4)\n"
"s_waitcnt vmcnt(24)\n"
"ds_read_b64_tr_b16 a[208:209], v20 offset:38912\n"
"ds_read_b64_tr_b16 a[210:211], v20 offset:39168\n"
"ds_read_b64_tr_b16 a[240:241], v21 offset:38912\n"
"ds_read_b64_tr_b16 a[242:243], v21 offset:39168\n"
"s_waitcnt lgkmcnt(4)\n"
"s_waitcnt vmcnt(22)\n"
"ds_read_b64_tr_b16 a[20:21], v20 offset:0\n"
"ds_read_b64_tr_b16 a[22:23], v20 offset:256\n"
"ds_read_b64_tr_b16 a[52:53], v21 offset:0\n"
"ds_read_b64_tr_b16 a[54:55], v21 offset:256\n"
"s_waitcnt lgkmcnt(4)\n"
"s_waitcnt vmcnt(20)\n"
"ds_read_b64_tr_b16 a[84:85], v20 offset:2048\n"
"ds_read_b64_tr_b16 a[86:87], v20 offset:2304\n"
"ds_read_b64_tr_b16 a[116:117], v21 offset:2048\n"
"ds_read_b64_tr_b16 a[118:119], v21 offset:2304\n"
"s_waitcnt lgkmcnt(4)\n"
"s_waitcnt vmcnt(18)\n"
"ds_read_b64_tr_b16 a[148:149], v20 offset:4096\n"
"ds_read_b64_tr_b16 a[150:151], v20 offset:4352\n"
"ds_read_b64_tr_b16 a[180:181], v21 offset:4096\n"
"ds_read_b64_tr_b16 a[182:183], v21 offset:4352\n"
"s_waitcnt lgkmcnt(4)\n"
"s_waitcnt vmcnt(16)\n"
"ds_read_b64_tr_b16 a[212:213], v20 offset:6144\n"
"ds_read_b64_tr_b16 a[214:215], v20 offset:6400\n"
"ds_read_b64_tr_b16 a[244:245], v21 offset:6144\n"
"ds_read_b64_tr_b16 a[246:247], v21 offset:6400\n"
"s_waitcnt lgkmcnt(4)\n"
"s_waitcnt vmcnt(14)\n"
"ds_read_b64_tr_b16 a[24:25], v20 offset:8192\n"
"ds_read_b64_tr_b16 a[26:27], v20 offset:8448\n"
"ds_read_b64_tr_b16 a[56:57], v21 offset:8192\n"
"ds_read_b64_tr_b16 a[58:59], v21 offset:8448\n"
"s_waitcnt lgkmcnt(4)\n"
"s_waitcnt vmcnt(12)\n"
"ds_read_b64_tr_b16 a[88:89], v20 offset:10240\n"
"ds_read_b64_tr_b16 a[90:91], v20 offset:10496\n"
"ds_read_b64_tr_b16 a[120:121], v21 offset:10240\n"
"ds_read_b64_tr_b16 a[122:123], v21 offset:10496\n"
"s_waitcnt lgkmcnt(4)\n"
"s_waitcnt vmcnt(10)\n"
"ds_read_b64_tr_b16 a[152:153], v20 offset:12288\n"
"ds_read_b64_tr_b16 a[154:155], v20 offset:12544\n"
"ds_read_b64_tr_b16 a[184:185], v21 offset:12288\n"
"ds_read_b64_tr_b16 a[186:187], v21 offset:12544\n"
"s_waitcnt lgkmcnt(4)\n"
"s_waitcnt vmcnt(8)\n"
"ds_read_b64_tr_b16 a[216:217], v20 offset:14336\n"
"ds_read_b64_tr_b16 a[218:219], v20 offset:14592\n"
"ds_read_b64_tr_b16 a[248:249], v21 offset:14336\n"
"ds_read_b64_tr_b16 a[250:251], v21 offset:14592\n"
"s_waitcnt lgkmcnt(4)\n"
"s_waitcnt vmcnt(6)\n"
"ds_read_b64_tr_b16 a[28:29], v20 offset:16384\n"
"ds_read_b64_tr_b16 a[30:31], v20 offset:16640\n"
"ds_read_b64_tr_b16 a[60:61], v21 offset:16384\n"
"ds_read_b64_tr_b16 a[62:63], v21 offset:16640\n"
"s_waitcnt lgkmcnt(4)\n"
"s_waitcnt vmcnt(4)\n"
"ds_read_b64_tr_b16 a[92:93], v20 offset:18432\n"
"ds_read_b64_tr_b16 a[94:95], v20 offset:18688\n"
"ds_read_b64_tr_b16 a[124:125], v21 offset:18432\n"
"ds_read_b64_tr_b16 a[126:127], v21 offset:18688\n"
"s_waitcnt lgkmcnt(4)\n"
"s_waitcnt vmcnt(2)\n"
"ds_read_b64_tr_b16 a[156:157], v20 offset:20480\n"
"ds_read_b64_tr_b16 a[158:159], v20 offset:20736\n"
"ds_read_b64_tr_b16 a[188:189], v21 offset:20480\n"
"ds_read_b64_tr_b16 a[190:191], v21 offset:20736\n"
"s_waitcnt lgkmcnt(4)\n"
"s_waitcnt vmcnt(0)\n"
"ds_read_b64_tr_b16 a[220:221], v20 offset:22528\n"
"ds_read_b64_tr_b16 a[222:223], v20 offset:22784\n"
"ds_read_b64_tr_b16 a[252:253], v21 offset:22528\n"
"ds_read_b64_tr_b16 a[254:255], v21 offset:22784\n"
"s_waitcnt lgkmcnt(4)\n"
"s_waitcnt lgkmcnt(0)\n"
"s_mov_b32 m0, s59\n"
"s_barrier\n"
"s_mov_b32 s93, 0x80000000\n"
"s_mov_b32 s94, 0x80000000\n"
"s_mov_b32 s95, 0x80000000\n"
"s_mov_b32 s94, 0x80000000\n"
"s_mov_b32 s93, 0x80000000\n"
"s_mov_b32 s94, 0x80000000\n"
"s_mov_b32 s95, 0x80000000\n"
"s_mov_b32 s94, 0x80000000\n"
"v_mov_b32 v124, 0\n"
"v_mov_b32 v125, 0\n"
"v_mov_b32 v120, 0\n"
"v_mov_b32 v121, 0\n"
"v_mov_b32 v124, 0\n"
"v_mov_b32 v125, 0\n"
"v_mov_b32 v120, 0\n"
"v_mov_b32 v121, 0\n"
"s_cmp_eq_u32 s54, 0\n"
"s_cbranch_scc1 .LO_1s\n"
"s_cmp_eq_u32 s54, 1\n"
"s_cbranch_scc1 .LO_2s\n"
"s_cmp_eq_u32 s54, 2\n"
"s_cbranch_scc1 .LO_3s\n"
"s_cmp_eq_u32 s54, 3\n"
"s_cbranch_scc1 .LO_4s\n"
".p2align 6\n"
".LO_top:\n"
"v_mfma_f32_16x16x32_bf16 v[40:43], v[84:87], a[0:3], 0\n"
"global_load_dwordx4 v[20:23], v6, s[56:57] offset:1024 sc1\n"
"v_mfma_f32_16x16x32_bf16 v[40:43], v[88:91], a[4:7], v[40:43]\n"
"s_waitcnt vmcnt(13)\n"
"v_mfma_f32_16x16x32_bf16 v[40:43], v[92:95], a[8:11], v[40:43]\n"
"s_waitcnt lgkmcnt(3)\n"
"v_add_f32 v72, v72, v44\n"
"v_add_f32 v73, v73, v45\n"
"v_mfma_f32_16x16x32_bf16 v[40:43], v[52:55], a[12:15], v[40:43]\n"
"s_waitcnt vmcnt(11)\n"
"v_add_f32 v74, v74, v46\n"
"v_add_f32 v75, v75, v47\n"
"v_mfma_f32_16x16x32_bf16 v[40:43], v[56:59], a[16:19], v[40:43]\n"
"v_add_f32 v72, v72, v48\n"
"v_add_f32 v73, v73, v49\n"
"v_mfma_f32_16x16x32_bf16 v[40:43], v[60:63], a[20:23], v[40:43]\n"
"s_waitcnt vmcnt(8)\n"
"global_load_dwordx4 v[24:27], v7, s[56:57] offset:0 sc1\n"
"v_add_f32 v74, v74, v50\n"
"v_mfma_f32_16x16x32_bf16 v[40:43], v[64:67], a[24:27], v[40:43]\n"
"v_add_f32 v75, v75, v51\n"
"v_add_f32 v72, v72, v112\n"
"v_mfma_f32_16x16x32_bf16 v[40:43], v[68:71], a[28:31], v[40:43]\n"
"v_add_f32 v73, v73, v113\n"
"v_add_f32 v74, v74, v114\n"
"v_mfma_f32_16x16x32_bf16 v[44:47], v[84:87], a[32:35], 0\n"
"global_load_dwordx4 v[28:31], v7, s[56:57] offset:1024 sc1\n"
"v_add_f32 v75, v75, v115\n"
"v_mfma_f32_16x16x32_bf16 v[44:47], v[88:91], a[36:39], v[44:47]\n"
"s_add_u32 s95, s50, 0xffd00000\n"
"s_and_b32 s94, s95, 0xfbffffff\n"
"ds_write_addtid_b32 v40 offset:0\n"
"v_mfma_f32_16x16x32_bf16 v[44:47], v[92:95], a[40:43], v[44:47]\n"
"ds_write_addtid_b32 v41 offset:4640\n"
"v_cvt_pk_bf16_f32 v48, v72, v73\n"
"v_mfma_f32_16x16x32_bf16 v[44:47], v[52:55], a[44:47], v[44:47]\n"
"ds_write_addtid_b32 v42 offset:9344\n"
"v_cvt_pk_bf16_f32 v49, v74, v75\n"
"v_mfma_f32_16x16x32_bf16 v[44:47], v[56:59], a[48:51], v[44:47]\n"
"s_waitcnt lgkmcnt(3)\n"
"ds_write_addtid_b32 v43 offset:13984\n"
"v_add_f32 v76, v76, v80\n"
"v_mfma_f32_16x16x32_bf16 v[44:47], v[60:63], a[52:55], v[44:47]\n"
"global_load_dwordx4 v[32:35], v8, s[56:57] offset:0 sc1\n"
"v_add_f32 v77, v77, v81\n"
"v_mfma_f32_16x16x32_bf16 v[44:47], v[64:67], a[56:59], v[44:47]\n"
"v_add_f32 v78, v78, v82\n"
"v_add_f32 v79, v79, v83\n"
"v_mfma_f32_16x16x32_bf16 v[44:47], v[68:71], a[60:63], v[44:47]\n"
"v_add_f32 v76, v76, v120\n"
"v_add_f32 v77, v77, v121\n"
"v_mfma_f32_16x16x32_bf16 v[40:43], v[84:87], a[64:67], 0\n"
"v_add_f32 v78, v78, v122\n"
"v_add_f32 v79, v79, v123\n"
"v_mfma_f32_16x16x32_bf16 v[40:43], v[88:91], a[68:71], v[40:43]\n"
"ds_write_addtid_b32 v44 offset:256\n"
"v_add_f32 v76, v76, v108\n"
"v_mfma_f32_16x16x32_bf16 v[40:43], v[92:95], a[72:75], v[40:43]\n"
"s_waitcnt vmcnt(9)\n"
"buffer_store_dwordx2 v[0:1], v12, s[40:43], s93 offen offset:0 sc1\n"
"ds_write_addtid_b32 v45 offset:4896\n"
"v_mfma_f32_16x16x32_bf16 v[40:43], v[52:55], a[76:79], v[40:43]\n"
"ds_write_addtid_b32 v46 offset:9600\n"
"v_add_f32 v77, v77, v109\n"
"v_mfma_f32_16x16x32_bf16 v[40:43], v[56:59], a[80:83], v[40:43]\n"
"s_waitcnt vmcnt(9)\n"
"ds_write_addtid_b32 v47 offset:14240\n"
"global_load_dwordx4 v[36:39], v8, s[56:57] offset:1024 sc1\n"
"v_mfma_f32_16x16x32_bf16 v[40:43], v[60:63], a[84:87], v[40:43]\n"
"v_add_f32 v78, v78, v110\n"
"v_add_f32 v79, v79, v111\n"
"v_mfma_f32_16x16x32_bf16 v[40:43], v[64:67], a[88:91], v[40:43]\n"
"v_cvt_pk_bf16_f32 v50, v76, v77\n"
"v_cvt_pk_bf16_f32 v51, v78, v79\n"
"v_mfma_f32_16x16x32_bf16 v[40:43], v[68:71], a[92:95], v[40:43]\n"
"v_lshlrev_b32 v72, 16, v116\n"
"v_and_b32 v73, 0xffff0000, v116\n"
"v_mfma_f32_16x16x32_bf16 v[44:47], v[84:87], a[96:99], 0\n"
"v_lshlrev_b32 v74, 16, v117\n"
"v_and_b32 v75, 0xffff0000, v117\n"
"v_mfma_f32_16x16x32_bf16 v[44:47], v[88:91], a[100:103], v[44:47]\n"
"ds_write_addtid_b32 v40 offset:512\n"
"v_lshlrev_b32 v76, 16, v118\n"
"v_mfma_f32_16x16x32_bf16 v[44:47], v[92:95], a[104:107], v[44:47]\n"
"ds_write_addtid_b32 v41 offset:5152\n"
"v_and_b32 v77, 0xffff0000, v118\n"
"v_mfma_f32_16x16x32_bf16 v[44:47], v[52:55], a[108:111], v[44:47]\n"
"ds_write_addtid_b32 v42 offset:9856\n"
"v_lshlrev_b32 v78, 16, v119\n"
"v_mfma_f32_16x16x32_bf16 v[44:47], v[56:59], a[112:115], v[44:47]\n"
"ds_write_addtid_b32 v43 offset:14496\n"
"v_and_b32 v79, 0xffff0000, v119\n"
"v_mfma_f32_16x16x32_bf16 v[44:47], v[60:63], a[116:119], v[44:47]\n"
"v_mfma_f32_16x16x32_bf16 v[44:47], v[64:67], a[120:123], v[44:47]\n"
"v_mfma_f32_16x16x32_bf16 v[44:47], v[68:71], a[124:127], v[44:47]\n"
"v_mfma_f32_16x16x32_bf16 v[40:43], v[84:87], a[128:131], 0\n"
"v_mfma_f32_16x16x32_bf16 v[40:43], v[88:91], a[132:135], v[40:43]\n"
"ds_write_addtid_b32 v44 offset:768\n"
"v_mfma_f32_16x16x32_bf16 v[40:43], v[92:95], a[136:139], v[40:43]\n"
"buffer_store_dwordx4 v[48:51], v11, s[40:43], s94 offen sc1\n"
"ds_write_addtid_b32 v45 offset:5408\n"
"v_mfma_f32_16x16x32_bf16 v[80:83], v[48:51], v[48:51], 0\n"
"v_mfma_f32_16x16x32_bf16 v[108:111], v[14:17], v[48:51], 0\n"
"v_mfma_f32_16x16x32_bf16 v[40:43], v[52:55], a[140:143], v[40:43]\n"
"ds_write_addtid_b32 v46 offset:10112\n"
"v_cndmask_b32_e64 v80, v80, v81, s[62:63]\n"
"v_mfma_f32_16x16x32_bf16 v[40:43], v[56:59], a[144:147], v[40:43]\n"
"ds_write_addtid_b32 v47 offset:14752\n"
"v_cndmask_b32_e64 v82, v82, v83, s[62:63]\n"
"v_mfma_f32_16x16x32_bf16 v[40:43], v[60:63], a[148:151], v[40:43]\n"
"global_load_dwordx4 v[112:115], v10, s[56:57] sc1\n"
"v_cndmask_b32_e64 v109, v80, v82, s[64:65]\n"
"v_mfma_f32_16x16x32_bf16 v[40:43], v[64:67], a[152:155], v[40:43]\n"
"buffer_store_dwordx2 v[108:109], v13, s[40:43], s94 offen offset:0 sc1\n"
"v_mfma_f32_16x16x32_bf16 v[40:43], v[68:71], a[156:159], v[40:43]\n"
"v_mfma_f32_16x16x32_bf16 v[48:51], v[84:87], a[160:163], 0\n"
"s_add_u32 s58, s50, 0x180000\n"
"v_mfma_f32_16x16x32_bf16 v[48:51], v[88:91], a[164:167], v[48:51]\n"
"s_and_b32 s58, s58, 0x3ffffff\n"
"ds_write_addtid_b32 v40 offset:1024\n"
"v_mfma_f32_16x16x32_bf16 v[48:51], v[92:95], a[168:171], v[48:51]\n"
"s_add_u32 s56, s40, s58\n"
"ds_write_addtid_b32 v41 offset:5664\n"
"v_mfma_f32_16x16x32_bf16 v[48:51], v[52:55], a[172:175], v[48:51]\n"
"s_addc_u32 s57, s41, 0\n"
"ds_write_addtid_b32 v42 offset:10368\n"
"v_mfma_f32_16x16x32_bf16 v[48:51], v[56:59], a[176:179], v[48:51]\n"
"s_waitcnt vmcnt(10)\n"
"ds_write_addtid_b32 v43 offset:15008\n"
"v_cmp_ne_u64 vcc, s[48:49], v[124:125]\n"
"v_mfma_f32_16x16x32_bf16 v[48:51], v[60:63], a[180:183], v[48:51]\n"
"v_mfma_f32_16x16x32_bf16 v[48:51], v[64:67], a[184:187], v[48:51]\n"
"s_cbranch_vccnz .LO_3s\n"
".LO_3r:\n"
"v_mfma_f32_16x16x32_bf16 v[48:51], v[68:71], a[188:191], v[48:51]\n"
"s_add_u32 s50, s50, 0x180000\n"
"global_load_dwordx4 v[40:43], v5, s[56:57] offset:0 sc1\n"
"v_mfma_f32_16x16x32_bf16 v[72:75], a[192:195], v[84:87], v[72:75]\n"
"v_mfma_f32_16x16x32_bf16 v[72:75], a[196:199], v[88:91], v[72:75]\n"
"ds_write_addtid_b32 v48 offset:1280\n"
"v_mfma_f32_16x16x32_bf16 v[72:75], a[200:203], v[92:95], v[72:75]\n"
"ds_write_addtid_b32 v49 offset:5920\n"
"v_mfma_f32_16x16x32_bf16 v[72:75], a[204:207], v[52:55], v[72:75]\n"
"ds_write_addtid_b32 v50 offset:10624\n"
"v_mfma_f32_16x16x32_bf16 v[72:75], a[208:211], v[56:59], v[72:75]\n"
"ds_write_addtid_b32 v51 offset:15264\n"
"global_load_dwordx4 v[44:47], v5, s[56:57] offset:1024 sc1\n"
"v_mfma_f32_16x16x32_bf16 v[72:75], a[212:215], v[60:63], v[72:75]\n"
"v_mfma_f32_16x16x32_bf16 v[72:75], a[216:219], v[64:67], v[72:75]\n"
"v_mfma_f32_16x16x32_bf16 v[72:75], a[220:223], v[68:71], v[72:75]\n"
"v_mfma_f32_16x16x32_bf16 v[76:79], a[224:227], v[84:87], v[76:79]\n"
"s_waitcnt lgkmcnt(0)\n"
"v_mfma_f32_16x16x32_bf16 v[76:79], a[228:231], v[88:91], v[76:79]\n"
"s_barrier\n"
"v_mfma_f32_16x16x32_bf16 v[76:79], a[232:235], v[92:95], v[76:79]\n"
"ds_read_b128 v[80:83], v2 offset:1536\n"
"ds_read_b128 v[84:87], v3 offset:512\n"
"v_mfma_f32_16x16x32_bf16 v[76:79], a[236:239], v[52:55], v[76:79]\n"
"buffer_load_dwordx2 v[120:121], v9, s[40:43], s50 offen sc1\n"
"v_mfma_f32_16x16x32_bf16 v[76:79], a[240:243], v[56:59], v[76:79]\n"
"global_load_dwordx4 v[48:51], v6, s[56:57] offset:0 sc1\n"
"ds_read_b128 v[88:91], v4 offset:2560\n"
"v_mfma_f32_16x16x32_bf16 v[76:79], a[244:247], v[60:63], v[76:79]\n"
"ds_read_b128 v[92:95], v2 offset:1792\n"
"ds_read_b128 v[108:111], v3 offset:768\n"
"v_mfma_f32_16x16x32_bf16 v[76:79], a[248:251], v[64:67], v[76:79]\n"
"global_load_dwordx4 v[52:55], v6, s[56:57] offset:1024 sc1\n"
"ds_read_b128 v[116:119], v4 offset:2816\n"
"v_mfma_f32_16x16x32_bf16 v[76:79], a[252:255], v[68:71], v[76:79]\n"
"v_mfma_f32_16x16x32_bf16 v[64:67], v[96:99], a[0:3], 0\n"
"v_mfma_f32_16x16x32_bf16 v[64:67], v[100:103], a[4:7], v[64:67]\n"
"s_waitcnt vmcnt(13)\n"
"v_mfma_f32_16x16x32_bf16 v[64:67], v[104:107], a[8:11], v[64:67]\n"
"s_waitcnt lgkmcnt(3)\n"
"v_add_f32 v80, v80, v84\n"
"v_add_f32 v81, v81, v85\n"
"v_mfma_f32_16x16x32_bf16 v[64:67], v[20:23], a[12:15], v[64:67]\n"
"s_waitcnt vmcnt(11)\n"
"global_load_dwordx4 v[56:59], v7, s[56:57] offset:0 sc1\n"
"v_add_f32 v82, v82, v86\n"
"v_mfma_f32_16x16x32_bf16 v[64:67], v[24:27], a[16:19], v[64:67]\n"
"v_add_f32 v83, v83, v87\n"
"v_add_f32 v80, v80, v88\n"
"v_mfma_f32_16x16x32_bf16 v[64:67], v[28:31], a[20:23], v[64:67]\n"
"s_waitcnt vmcnt(9)\n"
"v_add_f32 v81, v81, v89\n"
"v_add_f32 v82, v82, v90\n"
"v_mfma_f32_16x16x32_bf16 v[64:67], v[32:35], a[24:27], v[64:67]\n"
"v_add_f32 v83, v83, v91\n"
"v_add_f32 v80, v80, v72\n"
"v_mfma_f32_16x16x32_bf16 v[64:67], v[36:39], a[28:31], v[64:67]\n"
"v_add_f32 v81, v81, v73\n"
"v_add_f32 v82, v82, v74\n"
"v_mfma_f32_16x16x32_bf16 v[84:87], v[96:99], a[32:35], 0\n"
"v_add_f32 v83, v83, v75\n"
"v_mfma_f32_16x16x32_bf16 v[84:87], v[100:103], a[36:39], v[84:87]\n"
"s_add_u32 s93, s50, 0xffd00000\n"
"s_and_b32 s94, s93, 0xfbffffff\n"
"ds_write_addtid_b32 v64 offset:1536\n"
"v_mfma_f32_16x16x32_bf16 v[84:87], v[104:107], a[40:43], v[84:87]\n"
"ds_write_addtid_b32 v65 offset:6176\n"
"v_cvt_pk_bf16_f32 v72, v80, v81\n"
"v_mfma_f32_16x16x32_bf16 v[84:87], v[20:23], a[44:47], v[84:87]\n"
"ds_write_addtid_b32 v66 offset:10880\n"
"global_load_dwordx4 v[60:63], v7, s[56:57] offset:1024 sc1\n"
"v_mfma_f32_16x16x32_bf16 v[84:87], v[24:27], a[48:51], v[84:87]\n"
"ds_write_addtid_b32 v67 offset:15520\n"
"v_cvt_pk_bf16_f32 v73, v82, v83\n"
"v_mfma_f32_16x16x32_bf16 v[84:87], v[28:31], a[52:55], v[84:87]\n"
"s_waitcnt lgkmcnt(4)\n"
"v_add_f32 v92, v92, v108\n"
"v_add_f32 v93, v93, v109\n"
"v_mfma_f32_16x16x32_bf16 v[84:87], v[32:35], a[56:59], v[84:87]\n"
"v_add_f32 v94, v94, v110\n"
"v_add_f32 v95, v95, v111\n"
"v_mfma_f32_16x16x32_bf16 v[84:87], v[36:39], a[60:63], v[84:87]\n"
"v_add_f32 v92, v92, v116\n"
"v_add_f32 v93, v93, v117\n"
"v_mfma_f32_16x16x32_bf16 v[80:83], v[96:99], a[64:67], 0\n"
"global_load_dwordx4 v[64:67], v8, s[56:57] offset:0 sc1\n"
"v_add_f32 v94, v94, v118\n"
"v_mfma_f32_16x16x32_bf16 v[80:83], v[100:103], a[68:71], v[80:83]\n"
"ds_write_addtid_b32 v84 offset:1792\n"
"global_load_dwordx4 v[68:71], v8, s[56:57] offset:1024 sc1\n"
"v_mfma_f32_16x16x32_bf16 v[80:83], v[104:107], a[72:75], v[80:83]\n"
"ds_write_addtid_b32 v85 offset:6432\n"
"v_add_f32 v95, v95, v119\n"
"v_mfma_f32_16x16x32_bf16 v[80:83], v[20:23], a[76:79], v[80:83]\n"
"ds_write_addtid_b32 v86 offset:11136\n"
"v_add_f32 v92, v92, v76\n"
"v_mfma_f32_16x16x32_bf16 v[80:83], v[24:27], a[80:83], v[80:83]\n"
"ds_write_addtid_b32 v87 offset:15776\n"
"v_add_f32 v93, v93, v77\n"
"v_mfma_f32_16x16x32_bf16 v[80:83], v[28:31], a[84:87], v[80:83]\n"
"s_waitcnt vmcnt(10)\n"
"v_add_f32 v94, v94, v78\n"
"v_add_f32 v95, v95, v79\n"
"v_mfma_f32_16x16x32_bf16 v[80:83], v[32:35], a[88:91], v[80:83]\n"
"s_waitcnt vmcnt(9)\n"
"v_cvt_pk_bf16_f32 v74, v92, v93\n"
"v_cvt_pk_bf16_f32 v75, v94, v95\n"
"v_mfma_f32_16x16x32_bf16 v[80:83], v[36:39], a[92:95], v[80:83]\n"
"buffer_store_dwordx4 v[72:75], v11, s[40:43], s94 offen sc1\n"
"v_lshlrev_b32 v84, 16, v112\n"
"v_and_b32 v85, 0xffff0000, v112\n"
"v_lshlrev_b32 v86, 16, v113\n"
"v_mfma_f32_16x16x32_bf16 v[76:79], v[72:75], v[72:75], 0\n"
"buffer_store_dwordx2 v[0:1], v12, s[40:43], s95 offen offset:0 sc1\n"
"v_and_b32 v87, 0xffff0000, v113\n"
"v_mfma_f32_16x16x32_bf16 v[88:91], v[14:17], v[72:75], 0\n"
"v_lshlrev_b32 v92, 16, v114\n"
"v_and_b32 v93, 0xffff0000, v114\n"
"v_mfma_f32_16x16x32_bf16 v[108:111], v[96:99], a[96:99], 0\n"
"v_cndmask_b32_e64 v76, v76, v77, s[62:63]\n"
"v_cndmask_b32_e64 v78, v78, v79, s[62:63]\n"
"v_mfma_f32_16x16x32_bf16 v[108:111], v[100:103], a[100:103], v[108:111]\n"
"ds_write_addtid_b32 v80 offset:2048\n"
"v_cndmask_b32_e64 v89, v76, v78, s[64:65]\n"
"v_mfma_f32_16x16x32_bf16 v[108:111], v[104:107], a[104:107], v[108:111]\n"
"buffer_store_dwordx2 v[88:89], v13, s[40:43], s94 offen offset:0 sc1\n"
"ds_write_addtid_b32 v81 offset:6688\n"
"v_mfma_f32_16x16x32_bf16 v[108:111], v[20:23], a[108:111], v[108:111]\n"
"ds_write_addtid_b32 v82 offset:11392\n"
"v_lshlrev_b32 v94, 16, v115\n"
"v_mfma_f32_16x16x32_bf16 v[108:111], v[24:27], a[112:115], v[108:111]\n"
"ds_write_addtid_b32 v83 offset:16032\n"
"v_and_b32 v95, 0xffff0000, v115\n"
"v_mfma_f32_16x16x32_bf16 v[108:111], v[28:31], a[116:119], v[108:111]\n"
"v_mfma_f32_16x16x32_bf16 v[108:111], v[32:35], a[120:123], v[108:111]\n"
"v_mfma_f32_16x16x32_bf16 v[108:111], v[36:39], a[124:127], v[108:111]\n"
"v_mfma_f32_16x16x32_bf16 v[72:75], v[96:99], a[128:131], 0\n"
"v_mfma_f32_16x16x32_bf16 v[72:75], v[100:103], a[132:135], v[72:75]\n"
"ds_write_addtid_b32 v108 offset:2304\n"
"v_mfma_f32_16x16x32_bf16 v[72:75], v[104:107], a[136:139], v[72:75]\n"
"ds_write_addtid_b32 v109 offset:6944\n"
"v_mfma_f32_16x16x32_bf16 v[72:75], v[20:23], a[140:143], v[72:75]\n"
"ds_write_addtid_b32 v110 offset:11648\n"
"v_mfma_f32_16x16x32_bf16 v[72:75], v[24:27], a[144:147], v[72:75]\n"
"ds_write_addtid_b32 v111 offset:16288\n"
"v_mfma_f32_16x16x32_bf16 v[72:75], v[28:31], a[148:151], v[72:75]\n"
"v_mfma_f32_16x16x32_bf16 v[72:75], v[32:35], a[152:155], v[72:75]\n"
"global_load_dwordx4 v[88:91], v10, s[56:57] sc1\n"
"v_mfma_f32_16x16x32_bf16 v[72:75], v[36:39], a[156:159], v[72:75]\n"
"v_mfma_f32_16x16x32_bf16 v[76:79], v[96:99], a[160:163], 0\n"
"v_mfma_f32_16x16x32_bf16 v[76:79], v[100:103], a[164:167], v[76:79]\n"
"ds_write_addtid_b32 v72 offset:2560\n"
"v_mfma_f32_16x16x32_bf16 v[76:79], v[104:107], a[168:171], v[76:79]\n"
"s_add_u32 s58, s50, 0x180000\n"
"ds_write_addtid_b32 v73 offset:7200\n"
"v_mfma_f32_16x16x32_bf16 v[76:79], v[20:23], a[172:175], v[76:79]\n"
"s_and_b32 s58, s58, 0x3ffffff\n"
"ds_write_addtid_b32 v74 offset:11904\n"
"v_mfma_f32_16x16x32_bf16 v[76:79], v[24:27], a[176:179], v[76:79]\n"
"s_add_u32 s56, s40, s58\n"
"ds_write_addtid_b32 v75 offset:16544\n"
"v_mfma_f32_16x16x32_bf16 v[76:79], v[28:31], a[180:183], v[76:79]\n"
"s_addc_u32 s57, s41, 0\n"
"v_mfma_f32_16x16x32_bf16 v[76:79], v[32:35], a[184:187], v[76:79]\n"
"s_waitcnt vmcnt(10)\n"
"v_cmp_ne_u64 vcc, s[48:49], v[120:121]\n"
"v_mfma_f32_16x16x32_bf16 v[76:79], v[36:39], a[188:191], v[76:79]\n"
"v_mfma_f32_16x16x32_bf16 v[84:87], a[192:195], v[96:99], v[84:87]\n"
"s_cbranch_vccnz .LO_4s\n"
".LO_4r:\n"
"v_mfma_f32_16x16x32_bf16 v[84:87], a[196:199], v[100:103], v[84:87]\n"
"s_add_u32 s50, s50, 0x180000\n"
"ds_write_addtid_b32 v76 offset:2816\n"
"v_mfma_f32_16x16x32_bf16 v[84:87], a[200:203], v[104:107], v[84:87]\n"
"ds_write_addtid_b32 v77 offset:7456\n"
"global_load_dwordx4 v[72:75], v5, s[56:57] offset:0 sc1\n"
"v_mfma_f32_16x16x32_bf16 v[84:87], a[204:207], v[20:23], v[84:87]\n"
"ds_write_addtid_b32 v78 offset:12160\n"
"v_mfma_f32_16x16x32_bf16 v[84:87], a[208:211], v[24:27], v[84:87]\n"
"ds_write_addtid_b32 v79 offset:16800\n"
"v_mfma_f32_16x16x32_bf16 v[84:87], a[212:215], v[28:31], v[84:87]\n"
"v_mfma_f32_16x16x32_bf16 v[84:87], a[216:219], v[32:35], v[84:87]\n"
"v_mfma_f32_16x16x32_bf16 v[84:87], a[220:223], v[36:39], v[84:87]\n"
"global_load_dwordx4 v[76:79], v5, s[56:57] offset:1024 sc1\n"
"v_mfma_f32_16x16x32_bf16 v[92:95], a[224:227], v[96:99], v[92:95]\n"
"s_waitcnt lgkmcnt(0)\n"
"global_load_dwordx4 v[80:83], v6, s[56:57] offset:0 sc1\n"
"v_mfma_f32_16x16x32_bf16 v[92:95], a[228:231], v[100:103], v[92:95]\n"
"s_barrier\n"
"v_mfma_f32_16x16x32_bf16 v[92:95], a[232:235], v[104:107], v[92:95]\n"
"ds_read_b128 v[96:99], v2 offset:0\n"
"ds_read_b128 v[100:103], v3 offset:2048\n"
"v_mfma_f32_16x16x32_bf16 v[92:95], a[236:239], v[20:23], v[92:95]\n"
"ds_read_b128 v[104:107], v4 offset:1024\n"
"ds_read_b128 v[108:111], v2 offset:256\n"
"v_mfma_f32_16x16x32_bf16 v[92:95], a[240:243], v[24:27], v[92:95]\n"
"ds_read_b128 v[112:115], v3 offset:2304\n"
"ds_read_b128 v[116:119], v4 offset:1280\n"
"v_mfma_f32_16x16x32_bf16 v[92:95], a[244:247], v[28:31], v[92:95]\n"
"buffer_load_dwordx2 v[124:125], v9, s[40:43], s50 offen sc1\n"
"v_mfma_f32_16x16x32_bf16 v[92:95], a[248:251], v[32:35], v[92:95]\n"
"v_mfma_f32_16x16x32_bf16 v[92:95], a[252:255], v[36:39], v[92:95]\n"
"v_mfma_f32_16x16x32_bf16 v[120:123], v[40:43], a[0:3], 0\n"
"v_mfma_f32_16x16x32_bf16 v[120:123], v[44:47], a[4:7], v[120:123]\n"
"s_waitcnt vmcnt(12)\n"
"global_load_dwordx4 v[20:23], v6, s[56:57] offset:1024 sc1\n"
"v_mfma_f32_16x16x32_bf16 v[120:123], v[48:51], a[8:11], v[120:123]\n"
"s_waitcnt lgkmcnt(3)\n"
"v_add_f32 v96, v96, v100\n"
"v_add_f32 v97, v97, v101\n"
"v_mfma_f32_16x16x32_bf16 v[120:123], v[52:55], a[12:15], v[120:123]\n"
"s_waitcnt vmcnt(11)\n"
"v_add_f32 v98, v98, v102\n"
"v_add_f32 v99, v99, v103\n"
"v_mfma_f32_16x16x32_bf16 v[120:123], v[56:59], a[16:19], v[120:123]\n"
"v_add_f32 v96, v96, v104\n"
"v_add_f32 v97, v97, v105\n"
"v_mfma_f32_16x16x32_bf16 v[120:123], v[60:63], a[20:23], v[120:123]\n"
"s_waitcnt vmcnt(9)\n"
"v_add_f32 v98, v98, v106\n"
"v_add_f32 v99, v99, v107\n"
"v_mfma_f32_16x16x32_bf16 v[120:123], v[64:67], a[24:27], v[120:123]\n"
"global_load_dwordx4 v[24:27], v7, s[56:57] offset:0 sc1\n"
"v_add_f32 v96, v96, v84\n"
"v_mfma_f32_16x16x32_bf16 v[120:123], v[68:71], a[28:31], v[120:123]\n"
"global_load_dwordx4 v[28:31], v7, s[56:57] offset:1024 sc1\n"
"v_add_f32 v97, v97, v85\n"
"v_mfma_f32_16x16x32_bf16 v[100:103], v[40:43], a[32:35], 0\n"
"v_add_f32 v98, v98, v86\n"
"v_add_f32 v99, v99, v87\n"
"v_mfma_f32_16x16x32_bf16 v[100:103], v[44:47], a[36:39], v[100:103]\n"
"s_add_u32 s95, s50, 0xffd00000\n"
"s_and_b32 s94, s95, 0xfbffffff\n"
"ds_write_addtid_b32 v120 offset:0\n"
"v_mfma_f32_16x16x32_bf16 v[100:103], v[48:51], a[40:43], v[100:103]\n"
"s_waitcnt vmcnt(8)\n"
"buffer_store_dwordx2 v[0:1], v12, s[40:43], s93 offen offset:0 sc1\n"
"ds_write_addtid_b32 v121 offset:4640\n"
"v_mfma_f32_16x16x32_bf16 v[100:103], v[52:55], a[44:47], v[100:103]\n"
"ds_write_addtid_b32 v122 offset:9344\n"
"v_cvt_pk_bf16_f32 v84, v96, v97\n"
"v_mfma_f32_16x16x32_bf16 v[100:103], v[56:59], a[48:51], v[100:103]\n"
"ds_write_addtid_b32 v123 offset:13984\n"
"global_load_dwordx4 v[32:35], v8, s[56:57] offset:0 sc1\n"
"v_mfma_f32_16x16x32_bf16 v[100:103], v[60:63], a[52:55], v[100:103]\n"
"v_cvt_pk_bf16_f32 v85, v98, v99\n"
"v_mfma_f32_16x16x32_bf16 v[100:103], v[64:67], a[56:59], v[100:103]\n"
"s_waitcnt lgkmcnt(4)\n"
"v_add_f32 v108, v108, v112\n"
"v_add_f32 v109, v109, v113\n"
"v_mfma_f32_16x16x32_bf16 v[100:103], v[68:71], a[60:63], v[100:103]\n"
"v_add_f32 v110, v110, v114\n"
"v_add_f32 v111, v111, v115\n"
"v_mfma_f32_16x16x32_bf16 v[96:99], v[40:43], a[64:67], 0\n"
"v_add_f32 v108, v108, v116\n"
"v_add_f32 v109, v109, v117\n"
"v_mfma_f32_16x16x32_bf16 v[96:99], v[44:47], a[68:71], v[96:99]\n"
"ds_write_addtid_b32 v100 offset:256\n"
"v_add_f32 v110, v110, v118\n"
"v_mfma_f32_16x16x32_bf16 v[96:99], v[48:51], a[72:75], v[96:99]\n"
"ds_write_addtid_b32 v101 offset:4896\n"
"v_add_f32 v111, v111, v119\n"
"v_mfma_f32_16x16x32_bf16 v[96:99], v[52:55], a[76:79], v[96:99]\n"
"ds_write_addtid_b32 v102 offset:9600\n"
"v_add_f32 v108, v108, v92\n"
"v_mfma_f32_16x16x32_bf16 v[96:99], v[56:59], a[80:83], v[96:99]\n"
"ds_write_addtid_b32 v103 offset:14240\n"
"v_add_f32 v109, v109, v93\n"
"v_mfma_f32_16x16x32_bf16 v[96:99], v[60:63], a[84:87], v[96:99]\n"
"global_load_dwordx4 v[36:39], v8, s[56:57] offset:1024 sc1\n"
"v_add_f32 v110, v110, v94\n"
"v_mfma_f32_16x16x32_bf16 v[96:99], v[64:67], a[88:91], v[96:99]\n"
"s_waitcnt vmcnt(10)\n"
"v_add_f32 v111, v111, v95\n"
"v_cvt_pk_bf16_f32 v86, v108, v109\n"
"v_mfma_f32_16x16x32_bf16 v[96:99], v[68:71], a[92:95], v[96:99]\n"
"v_cvt_pk_bf16_f32 v87, v110, v111\n"
"v_lshlrev_b32 v100, 16, v88\n"
"v_mfma_f32_16x16x32_bf16 v[92:95], v[40:43], a[96:99], 0\n"
"v_and_b32 v101, 0xffff0000, v88\n"
"v_lshlrev_b32 v102, 16, v89\n"
"v_mfma_f32_16x16x32_bf16 v[92:95], v[44:47], a[100:103], v[92:95]\n"
"ds_write_addtid_b32 v96 offset:512\n"
"v_and_b32 v103, 0xffff0000, v89\n"
"v_mfma_f32_16x16x32_bf16 v[92:95], v[48:51], a[104:107], v[92:95]\n"
"ds_write_addtid_b32 v97 offset:5152\n"
"v_lshlrev_b32 v104, 16, v90\n"
"v_mfma_f32_16x16x32_bf16 v[92:95], v[52:55], a[108:111], v[92:95]\n"
"buffer_store_dwordx4 v[84:87], v11, s[40:43], s94 offen sc1\n"
"ds_write_addtid_b32 v98 offset:9856\n"
"v_and_b32 v105, 0xffff0000, v90\n"
"v_lshlrev_b32 v106, 16, v91\n"
"v_mfma_f32_16x16x32_bf16 v[108:111], v[84:87], v[84:87], 0\n"
"v_and_b32 v107, 0xffff0000, v91\n"
"v_mfma_f32_16x16x32_bf16 v[112:115], v[14:17], v[84:87], 0\n"
"v_mfma_f32_16x16x32_bf16 v[92:95], v[56:59], a[112:115], v[92:95]\n"
"ds_write_addtid_b32 v99 offset:14496\n"
"v_cndmask_b32_e64 v108, v108, v109, s[62:63]\n"
"v_mfma_f32_16x16x32_bf16 v[92:95], v[60:63], a[116:119], v[92:95]\n"
"v_cndmask_b32_e64 v110, v110, v111, s[62:63]\n"
"v_cndmask_b32_e64 v113, v108, v110, s[64:65]\n"
"v_mfma_f32_16x16x32_bf16 v[92:95], v[64:67], a[120:123], v[92:95]\n"
"buffer_store_dwordx2 v[112:113], v13, s[40:43], s94 offen offset:0 sc1\n"
"v_mfma_f32_16x16x32_bf16 v[92:95], v[68:71], a[124:127], v[92:95]\n"
"v_mfma_f32_16x16x32_bf16 v[84:87], v[40:43], a[128:131], 0\n"
"v_mfma_f32_16x16x32_bf16 v[84:87], v[44:47], a[132:135], v[84:87]\n"
"ds_write_addtid_b32 v92 offset:768\n"
"v_mfma_f32_16x16x32_bf16 v[84:87], v[48:51], a[136:139], v[84:87]\n"
"ds_write_addtid_b32 v93 offset:5408\n"
"v_mfma_f32_16x16x32_bf16 v[84:87], v[52:55], a[140:143], v[84:87]\n"
"ds_write_addtid_b32 v94 offset:10112\n"
"v_mfma_f32_16x16x32_bf16 v[84:87], v[56:59], a[144:147], v[84:87]\n"
"ds_write_addtid_b32 v95 offset:14752\n"
"v_mfma_f32_16x16x32_bf16 v[84:87], v[60:63], a[148:151], v[84:87]\n"
"v_mfma_f32_16x16x32_bf16 v[84:87], v[64:67], a[152:155], v[84:87]\n"
"v_mfma_f32_16x16x32_bf16 v[84:87], v[68:71], a[156:159], v[84:87]\n"
"global_load_dwordx4 v[96:99], v10, s[56:57] sc1\n"
"v_mfma_f32_16x16x32_bf16 v[92:95], v[40:43], a[160:163], 0\n"
"v_mfma_f32_16x16x32_bf16 v[92:95], v[44:47], a[164:167], v[92:95]\n"
"s_add_u32 s58, s50, 0x180000\n"
"ds_write_addtid_b32 v84 offset:1024\n"
"v_mfma_f32_16x16x32_bf16 v[92:95], v[48:51], a[168:171], v[92:95]\n"
"s_and_b32 s58, s58, 0x3ffffff\n"
"ds_write_addtid_b32 v85 offset:5664\n"
"v_mfma_f32_16x16x32_bf16 v[92:95], v[52:55], a[172:175], v[92:95]\n"
"s_add_u32 s56, s40, s58\n"
"ds_write_addtid_b32 v86 offset:10368\n"
"v_mfma_f32_16x16x32_bf16 v[92:95], v[56:59], a[176:179], v[92:95]\n"
"s_addc_u32 s57, s41, 0\n"
"ds_write_addtid_b32 v87 offset:15008\n"
"v_mfma_f32_16x16x32_bf16 v[92:95], v[60:63], a[180:183], v[92:95]\n"
"s_waitcnt vmcnt(9)\n"
"v_cmp_ne_u64 vcc, s[48:49], v[124:125]\n"
"v_mfma_f32_16x16x32_bf16 v[92:95], v[64:67], a[184:187], v[92:95]\n"
"v_mfma_f32_16x16x32_bf16 v[92:95], v[68:71], a[188:191], v[92:95]\n"
"s_cbranch_vccnz .LO_1s\n"
".LO_1r:\n"
"v_mfma_f32_16x16x32_bf16 v[100:103], a[192:195], v[40:43], v[100:103]\n"
"s_add_u32 s50, s50, 0x180000\n"
"global_load_dwordx4 v[84:87], v5, s[56:57] offset:0 sc1\n"
"v_mfma_f32_16x16x32_bf16 v[100:103], a[196:199], v[44:47], v[100:103]\n"
"ds_write_addtid_b32 v92 offset:1280\n"
"v_mfma_f32_16x16x32_bf16 v[100:103], a[200:203], v[48:51], v[100:103]\n"
"ds_write_addtid_b32 v93 offset:5920\n"
"v_mfma_f32_16x16x32_bf16 v[100:103], a[204:207], v[52:55], v[100:103]\n"
"ds_write_addtid_b32 v94 offset:10624\n"
"v_mfma_f32_16x16x32_bf16 v[100:103], a[208:211], v[56:59], v[100:103]\n"
"ds_write_addtid_b32 v95 offset:15264\n"
"v_mfma_f32_16x16x32_bf16 v[100:103], a[212:215], v[60:63], v[100:103]\n"
"global_load_dwordx4 v[88:91], v5, s[56:57] offset:1024 sc1\n"
"v_mfma_f32_16x16x32_bf16 v[100:103], a[216:219], v[64:67], v[100:103]\n"
"v_mfma_f32_16x16x32_bf16 v[100:103], a[220:223], v[68:71], v[100:103]\n"
"v_mfma_f32_16x16x32_bf16 v[104:107], a[224:227], v[40:43], v[104:107]\n"
"s_waitcnt lgkmcnt(0)\n"
"v_mfma_f32_16x16x32_bf16 v[104:107], a[228:231], v[44:47], v[104:107]\n"
"s_barrier\n"
"v_mfma_f32_16x16x32_bf16 v[104:107], a[232:235], v[48:51], v[104:107]\n"
"global_load_dwordx4 v[92:95], v6, s[56:57] offset:0 sc1\n"
"ds_read_b128 v[40:43], v2 offset:1536\n"
"v_mfma_f32_16x16x32_bf16 v[104:107], a[236:239], v[52:55], v[104:107]\n"
"ds_read_b128 v[44:47], v3 offset:512\n"
"ds_read_b128 v[48:51], v4 offset:2560\n"
"v_mfma_f32_16x16x32_bf16 v[104:107], a[240:243], v[56:59], v[104:107]\n"
"ds_read_b128 v[108:111], v2 offset:1792\n"
"ds_read_b128 v[112:115], v3 offset:768\n"
"v_mfma_f32_16x16x32_bf16 v[104:107], a[244:247], v[60:63], v[104:107]\n"
"ds_read_b128 v[116:119], v4 offset:2816\n"
"v_mfma_f32_16x16x32_bf16 v[104:107], a[248:251], v[64:67], v[104:107]\n"
"buffer_load_dwordx2 v[120:121], v9, s[40:43], s50 offen sc1\n"
"v_mfma_f32_16x16x32_bf16 v[104:107], a[252:255], v[68:71], v[104:107]\n"
"global_load_dwordx4 v[52:55], v6, s[56:57] offset:1024 sc1\n"
"v_mfma_f32_16x16x32_bf16 v[64:67], v[72:75], a[0:3], 0\n"
"v_mfma_f32_16x16x32_bf16 v[64:67], v[76:79], a[4:7], v[64:67]\n"
"s_waitcnt vmcnt(13)\n"
"v_mfma_f32_16x16x32_bf16 v[64:67], v[80:83], a[8:11], v[64:67]\n"
"s_waitcnt lgkmcnt(3)\n"
"v_add_f32 v40, v40, v44\n"
"v_add_f32 v41, v41, v45\n"
"v_mfma_f32_16x16x32_bf16 v[64:67], v[20:23], a[12:15], v[64:67]\n"
"s_waitcnt vmcnt(11)\n"
"v_add_f32 v42, v42, v46\n"
"v_add_f32 v43, v43, v47\n"
"v_mfma_f32_16x16x32_bf16 v[64:67], v[24:27], a[16:19], v[64:67]\n"
"global_load_dwordx4 v[56:59], v7, s[56:57] offset:0 sc1\n"
"v_add_f32 v40, v40, v48\n"
"v_mfma_f32_16x16x32_bf16 v[64:67], v[28:31], a[20:23], v[64:67]\n"
"s_waitcnt vmcnt(9)\n"
"v_add_f32 v41, v41, v49\n"
"v_add_f32 v42, v42, v50\n"
"v_mfma_f32_16x16x32_bf16 v[64:67], v[32:35], a[24:27], v[64:67]\n"
"v_add_f32 v43, v43, v51\n"
"v_add_f32 v40, v40, v100\n"
"v_mfma_f32_16x16x32_bf16 v[64:67], v[36:39], a[28:31], v[64:67]\n"
"v_add_f32 v41, v41, v101\n"
"v_add_f32 v42, v42, v102\n"
"v_mfma_f32_16x16x32_bf16 v[44:47], v[72:75], a[32:35], 0\n"
"v_add_f32 v43, v43, v103\n"
"v_mfma_f32_16x16x32_bf16 v[44:47], v[76:79], a[36:39], v[44:47]\n"
"ds_write_addtid_b32 v64 offset:1536\n"
"global_load_dwordx4 v[60:63], v7, s[56:57] offset:1024 sc1\n"
"v_mfma_f32_16x16x32_bf16 v[44:47], v[80:83], a[40:43], v[44:47]\n"
"s_add_u32 s93, s50, 0xffd00000\n"
"s_and_b32 s94, s93, 0xfbffffff\n"
"ds_write_addtid_b32 v65 offset:6176\n"
"v_mfma_f32_16x16x32_bf16 v[44:47], v[20:23], a[44:47], v[44:47]\n"
"ds_write_addtid_b32 v66 offset:10880\n"
"v_cvt_pk_bf16_f32 v48, v40, v41\n"
"v_mfma_f32_16x16x32_bf16 v[44:47], v[24:27], a[48:51], v[44:47]\n"
"ds_write_addtid_b32 v67 offset:15520\n"
"v_cvt_pk_bf16_f32 v49, v42, v43\n"
"v_mfma_f32_16x16x32_bf16 v[44:47], v[28:31], a[52:55], v[44:47]\n"
"s_waitcnt lgkmcnt(4)\n"
"v_add_f32 v108, v108, v112\n"
"v_add_f32 v109, v109, v113\n"
"v_mfma_f32_16x16x32_bf16 v[44:47], v[32:35], a[56:59], v[44:47]\n"
"s_waitcnt vmcnt(8)\n"
"global_load_dwordx4 v[64:67], v8, s[56:57] offset:0 sc1\n"
"v_add_f32 v110, v110, v114\n"
"v_mfma_f32_16x16x32_bf16 v[44:47], v[36:39], a[60:63], v[44:47]\n"
"buffer_store_dwordx2 v[0:1], v12, s[40:43], s95 offen offset:0 sc1\n"
"v_add_f32 v111, v111, v115\n"
"v_mfma_f32_16x16x32_bf16 v[40:43], v[72:75], a[64:67], 0\n"
"v_add_f32 v108, v108, v116\n"
"v_add_f32 v109, v109, v117\n"
"v_mfma_f32_16x16x32_bf16 v[40:43], v[76:79], a[68:71], v[40:43]\n"
"ds_write_addtid_b32 v44 offset:1792\n"
"v_add_f32 v110, v110, v118\n"
"v_mfma_f32_16x16x32_bf16 v[40:43], v[80:83], a[72:75], v[40:43]\n"
"ds_write_addtid_b32 v45 offset:6432\n"
"v_add_f32 v111, v111, v119\n"
"v_mfma_f32_16x16x32_bf16 v[40:43], v[20:23], a[76:79], v[40:43]\n"
"ds_write_addtid_b32 v46 offset:11136\n"
"global_load_dwordx4 v[68:71], v8, s[56:57] offset:1024 sc1\n"
"v_mfma_f32_16x16x32_bf16 v[40:43], v[24:27], a[80:83], v[40:43]\n"
"ds_write_addtid_b32 v47 offset:15776\n"
"v_add_f32 v108, v108, v104\n"
"v_mfma_f32_16x16x32_bf16 v[40:43], v[28:31], a[84:87], v[40:43]\n"
"v_add_f32 v109, v109, v105\n"
"v_add_f32 v110, v110, v106\n"
"v_mfma_f32_16x16x32_bf16 v[40:43], v[32:35], a[88:91], v[40:43]\n"
"v_add_f32 v111, v111, v107\n"
"v_cvt_pk_bf16_f32 v50, v108, v109\n"
"v_mfma_f32_16x16x32_bf16 v[40:43], v[36:39], a[92:95], v[40:43]\n"
"s_waitcnt vmcnt(10)\n"
"v_cvt_pk_bf16_f32 v51, v110, v111\n"
"v_lshlrev_b32 v112, 16, v96\n"
"v_mfma_f32_16x16x32_bf16 v[44:47], v[72:75], a[96:99], 0\n"
"v_and_b32 v113, 0xffff0000, v96\n"
"v_lshlrev_b32 v114, 16, v97\n"
"v_mfma_f32_16x16x32_bf16 v[44:47], v[76:79], a[100:103], v[44:47]\n"
"ds_write_addtid_b32 v40 offset:2048\n"
"v_and_b32 v115, 0xffff0000, v97\n"
"v_mfma_f32_16x16x32_bf16 v[44:47], v[80:83], a[104:107], v[44:47]\n"
"ds_write_addtid_b32 v41 offset:6688\n"
"v_lshlrev_b32 v108, 16, v98\n"
"v_mfma_f32_16x16x32_bf16 v[44:47], v[20:23], a[108:111], v[44:47]\n"
"ds_write_addtid_b32 v42 offset:11392\n"
"v_and_b32 v109, 0xffff0000, v98\n"
"v_mfma_f32_16x16x32_bf16 v[44:47], v[24:27], a[112:115], v[44:47]\n"
"ds_write_addtid_b32 v43 offset:16032\n"
"v_lshlrev_b32 v110, 16, v99\n"
"v_mfma_f32_16x16x32_bf16 v[44:47], v[28:31], a[116:119], v[44:47]\n"
"v_and_b32 v111, 0xffff0000, v99\n"
"v_mfma_f32_16x16x32_bf16 v[44:47], v[32:35], a[120:123], v[44:47]\n"
"v_mfma_f32_16x16x32_bf16 v[44:47], v[36:39], a[124:127], v[44:47]\n"
"buffer_store_dwordx4 v[48:51], v11, s[40:43], s94 offen sc1\n"
"v_mfma_f32_16x16x32_bf16 v[40:43], v[48:51], v[48:51], 0\n"
"v_mfma_f32_16x16x32_bf16 v[96:99], v[14:17], v[48:51], 0\n"
"v_mfma_f32_16x16x32_bf16 v[100:103], v[72:75], a[128:131], 0\n"
"v_cndmask_b32_e64 v40, v40, v41, s[62:63]\n"
"v_cndmask_b32_e64 v42, v42, v43, s[62:63]\n"
"v_mfma_f32_16x16x32_bf16 v[100:103], v[76:79], a[132:135], v[100:103]\n"
"ds_write_addtid_b32 v44 offset:2304\n"
"v_cndmask_b32_e64 v97, v40, v42, s[64:65]\n"
"v_mfma_f32_16x16x32_bf16 v[100:103], v[80:83], a[136:139], v[100:103]\n"
"buffer_store_dwordx2 v[96:97], v13, s[40:43], s94 offen offset:0 sc1\n"
"ds_write_addtid_b32 v45 offset:6944\n"
"v_mfma_f32_16x16x32_bf16 v[100:103], v[20:23], a[140:143], v[100:103]\n"
"ds_write_addtid_b32 v46 offset:11648\n"
"v_mfma_f32_16x16x32_bf16 v[100:103], v[24:27], a[144:147], v[100:103]\n"
"ds_write_addtid_b32 v47 offset:16288\n"
"global_load_dwordx4 v[116:119], v10, s[56:57] sc1\n"
"v_mfma_f32_16x16x32_bf16 v[100:103], v[28:31], a[148:151], v[100:103]\n"
"v_mfma_f32_16x16x32_bf16 v[100:103], v[32:35], a[152:155], v[100:103]\n"
"v_mfma_f32_16x16x32_bf16 v[100:103], v[36:39], a[156:159], v[100:103]\n"
"v_mfma_f32_16x16x32_bf16 v[40:43], v[72:75], a[160:163], 0\n"
"v_mfma_f32_16x16x32_bf16 v[40:43], v[76:79], a[164:167], v[40:43]\n"
"ds_write_addtid_b32 v100 offset:2560\n"
"v_mfma_f32_16x16x32_bf16 v[40:43], v[80:83], a[168:171], v[40:43]\n"
"s_add_u32 s58, s50, 0x180000\n"
"ds_write_addtid_b32 v101 offset:7200\n"
"v_mfma_f32_16x16x32_bf16 v[40:43], v[20:23], a[172:175], v[40:43]\n"
"s_and_b32 s58, s58, 0x3ffffff\n"
"ds_write_addtid_b32 v102 offset:11904\n"
"v_mfma_f32_16x16x32_bf16 v[40:43], v[24:27], a[176:179], v[40:43]\n"
"s_add_u32 s56, s40, s58\n"
"ds_write_addtid_b32 v103 offset:16544\n"
"v_mfma_f32_16x16x32_bf16 v[40:43], v[28:31], a[180:183], v[40:43]\n"
"s_addc_u32 s57, s41, 0\n"
"v_mfma_f32_16x16x32_bf16 v[40:43], v[32:35], a[184:187], v[40:43]\n"
"s_waitcnt vmcnt(9)\n"
"v_cmp_ne_u64 vcc, s[48:49], v[120:121]\n"
"v_mfma_f32_16x16x32_bf16 v[40:43], v[36:39], a[188:191], v[40:43]\n"
"v_mfma_f32_16x16x32_bf16 v[112:115], a[192:195], v[72:75], v[112:115]\n"
"s_cbranch_vccnz .LO_2s\n"
".LO_2r:\n"
"v_mfma_f32_16x16x32_bf16 v[112:115], a[196:199], v[76:79], v[112:115]\n"
"s_add_u32 s50, s50, 0x180000\n"
"ds_write_addtid_b32 v40 offset:2816\n"
"global_load_dwordx4 v[96:99], v5, s[56:57] offset:0 sc1\n"
"v_mfma_f32_16x16x32_bf16 v[112:115], a[200:203], v[80:83], v[112:115]\n"
"ds_write_addtid_b32 v41 offset:7456\n"
"v_mfma_f32_16x16x32_bf16 v[112:115], a[204:207], v[20:23], v[112:115]\n"
"ds_write_addtid_b32 v42 offset:12160\n"
"v_mfma_f32_16x16x32_bf16 v[112:115], a[208:211], v[24:27], v[112:115]\n"
"ds_write_addtid_b32 v43 offset:16800\n"
"v_mfma_f32_16x16x32_bf16 v[112:115], a[212:215], v[28:31], v[112:115]\n"
"v_mfma_f32_16x16x32_bf16 v[112:115], a[216:219], v[32:35], v[112:115]\n"
"global_load_dwordx4 v[100:103], v5, s[56:57] offset:1024 sc1\n"
"v_mfma_f32_16x16x32_bf16 v[112:115], a[220:223], v[36:39], v[112:115]\n"
"v_mfma_f32_16x16x32_bf16 v[108:111], a[224:227], v[72:75], v[108:111]\n"
"s_waitcnt lgkmcnt(0)\n"
"v_mfma_f32_16x16x32_bf16 v[108:111], a[228:231], v[76:79], v[108:111]\n"
"s_barrier\n"
"v_mfma_f32_16x16x32_bf16 v[108:111], a[232:235], v[80:83], v[108:111]\n"
"buffer_load_dwordx2 v[124:125], v9, s[40:43], s50 offen sc1\n"
"v_mfma_f32_16x16x32_bf16 v[108:111], a[236:239], v[20:23], v[108:111]\n"
"global_load_dwordx4 v[104:107], v6, s[56:57] offset:0 sc1\n"
"ds_read_b128 v[72:75], v2 offset:0\n"
"v_mfma_f32_16x16x32_bf16 v[108:111], a[240:243], v[24:27], v[108:111]\n"
"ds_read_b128 v[44:47], v3 offset:2048\n"
"ds_read_b128 v[48:51], v4 offset:1024\n"
"v_mfma_f32_16x16x32_bf16 v[108:111], a[244:247], v[28:31], v[108:111]\n"
"ds_read_b128 v[76:79], v2 offset:256\n"
"ds_read_b128 v[80:83], v3 offset:2304\n"
"v_mfma_f32_16x16x32_bf16 v[108:111], a[248:251], v[32:35], v[108:111]\n"
"ds_read_b128 v[120:123], v4 offset:1280\n"
"v_mfma_f32_16x16x32_bf16 v[108:111], a[252:255], v[36:39], v[108:111]\n"
"s_branch .LO_top\n"
".LO_1s:\n"
"s_cmp_ge_u32 s50, s53\n"
"s_cbranch_scc1 .LO_t0\n"
"s_cmp_ge_u32 s50, s52\n"
"s_cbranch_scc1 .LO_1r\n"
"s_sub_u32 s60, s50, 0x0\n"
"s_mov_b32 s61, 200000\n"
"s_nop 7\n"
".LO_1p:\n"
"buffer_load_dwordx2 v[124:125], v9, s[40:43], s60 offen sc1\n"
"s_waitcnt vmcnt(0)\n"
"v_cmp_ne_u64 vcc, s[48:49], v[124:125]\n"
"s_cbranch_vccz .LO_1px\n"
"s_sleep 1\n"
"s_sub_u32 s61, s61, 1\n"
"s_cbranch_scc0 .LO_1p\n"
"s_branch .LO_end\n"
".LO_1px:\n"
"s_branch .LO_1r\n"
".LO_2s:\n"
"s_cmp_ge_u32 s50, s53\n"
"s_cbranch_scc1 .LO_t1\n"
"s_cmp_ge_u32 s50, s52\n"
"s_cbranch_scc1 .LO_2r\n"
"s_sub_u32 s60, s50, 0x0\n"
"s_mov_b32 s61, 200000\n"
"s_nop 7\n"
".LO_2p:\n"
"buffer_load_dwordx2 v[120:121], v9, s[40:43], s60 offen sc1\n"
"s_waitcnt vmcnt(0)\n"
"v_cmp_ne_u64 vcc, s[48:49], v[120:121]\n"
"s_cbranch_vccz .LO_2px\n"
"s_sleep 1\n"
"s_sub_u32 s61, s61, 1\n"
"s_cbranch_scc0 .LO_2p\n"
"s_branch .LO_end\n"
".LO_2px:\n"
"s_branch .LO_2r\n"
".LO_3s:\n"
"s_cmp_ge_u32 s50, s53\n"
"s_cbranch_scc1 .LO_t2\n"
"s_cmp_ge_u32 s50, s52\n"
"s_cbranch_scc1 .LO_3r\n"
"s_sub_u32 s60, s50, 0x0\n"
"s_mov_b32 s61, 200000\n"
"s_nop 7\n"
".LO_3p:\n"
"buffer_load_dwordx2 v[124:125], v9, s[40:43], s60 offen sc1\n"
"s_waitcnt vmcnt(0)\n"
"v_cmp_ne_u64 vcc, s[48:49], v[124:125]\n"
"s_cbranch_vccz .LO_3px\n"
"s_sleep 1\n"
"s_sub_u32 s61, s61, 1\n"
"s_cbranch_scc0 .LO_3p\n"
"s_branch .LO_end\n"
".LO_3px:\n"
"s_branch .LO_3r\n"
".LO_4s:\n"
"s_cmp_ge_u32 s50, s53\n"
"s_cbranch_scc1 .LO_t3\n"
"s_cmp_ge_u32 s50, s52\n"
"s_cbranch_scc1 .LO_4r\n"
"s_sub_u32 s60, s50, 0x0\n"
"s_mov_b32 s61, 200000\n"
"s_nop 7\n"
".LO_4p:\n"
"buffer_load_dwordx2 v[120:121], v9, s[40:43], s60 offen sc1\n"
"s_waitcnt vmcnt(0)\n"
"v_cmp_ne_u64 vcc, s[48:49], v[120:121]\n"
"s_cbranch_vccz .LO_4px\n"
"s_sleep 1\n"
"s_sub_u32 s61, s61, 1\n"
"s_cbranch_scc0 .LO_4p\n"
"s_branch .LO_end\n"
".LO_4px:\n"
"s_branch .LO_4r\n"
".LO_t0:\n"
"v_mfma_f32_16x16x32_bf16 v[100:103], a[192:195], v[40:43], v[100:103]\n"
"v_mfma_f32_16x16x32_bf16 v[100:103], a[196:199], v[44:47], v[100:103]\n"
"ds_write_addtid_b32 v92 offset:1280\n"
"v_mfma_f32_16x16x32_bf16 v[100:103], a[200:203], v[48:51], v[100:103]\n"
"ds_write_addtid_b32 v93 offset:5920\n"
"v_mfma_f32_16x16x32_bf16 v[100:103], a[204:207], v[52:55], v[100:103]\n"
"ds_write_addtid_b32 v94 offset:10624\n"
"v_mfma_f32_16x16x32_bf16 v[100:103], a[208:211], v[56:59], v[100:103]\n"
"ds_write_addtid_b32 v95 offset:15264\n"
"v_mfma_f32_16x16x32_bf16 v[100:103], a[212:215], v[60:63], v[100:103]\n"
"v_mfma_f32_16x16x32_bf16 v[100:103], a[216:219], v[64:67], v[100:103]\n"
"v_mfma_f32_16x16x32_bf16 v[100:103], a[220:223], v[68:71], v[100:103]\n"
"v_mfma_f32_16x16x32_bf16 v[104:107], a[224:227], v[40:43], v[104:107]\n"
"s_waitcnt lgkmcnt(0)\n"
"v_mfma_f32_16x16x32_bf16 v[104:107], a[228:231], v[44:47], v[104:107]\n"
"s_barrier\n"
"v_mfma_f32_16x16x32_bf16 v[104:107], a[232:235], v[48:51], v[104:107]\n"
"ds_read_b128 v[40:43], v2 offset:1536\n"
"v_mfma_f32_16x16x32_bf16 v[104:107], a[236:239], v[52:55], v[104:107]\n"
"ds_read_b128 v[44:47], v3 offset:512\n"
"ds_read_b128 v[48:51], v4 offset:2560\n"
"v_mfma_f32_16x16x32_bf16 v[104:107], a[240:243], v[56:59], v[104:107]\n"
"ds_read_b128 v[108:111], v2 offset:1792\n"
"ds_read_b128 v[112:115], v3 offset:768\n"
"v_mfma_f32_16x16x32_bf16 v[104:107], a[244:247], v[60:63], v[104:107]\n"
"ds_read_b128 v[116:119], v4 offset:2816\n"
"v_mfma_f32_16x16x32_bf16 v[104:107], a[248:251], v[64:67], v[104:107]\n"
"v_mfma_f32_16x16x32_bf16 v[104:107], a[252:255], v[68:71], v[104:107]\n"
"s_waitcnt vmcnt(11)\n"
"v_mfma_f32_16x16x32_bf16 v[64:67], v[72:75], a[0:3], 0\n"
"v_mfma_f32_16x16x32_bf16 v[64:67], v[76:79], a[4:7], v[64:67]\n"
"s_waitcnt vmcnt(8)\n"
"v_mfma_f32_16x16x32_bf16 v[64:67], v[80:83], a[8:11], v[64:67]\n"
"s_waitcnt lgkmcnt(3)\n"
"v_add_f32 v40, v40, v44\n"
"v_add_f32 v41, v41, v45\n"
"v_mfma_f32_16x16x32_bf16 v[64:67], v[20:23], a[12:15], v[64:67]\n"
"s_waitcnt vmcnt(6)\n"
"v_add_f32 v42, v42, v46\n"
"v_add_f32 v43, v43, v47\n"
"v_mfma_f32_16x16x32_bf16 v[64:67], v[24:27], a[16:19], v[64:67]\n"
"v_add_f32 v40, v40, v48\n"
"v_mfma_f32_16x16x32_bf16 v[64:67], v[28:31], a[20:23], v[64:67]\n"
"s_waitcnt vmcnt(3)\n"
"v_add_f32 v41, v41, v49\n"
"v_add_f32 v42, v42, v50\n"
"v_mfma_f32_16x16x32_bf16 v[64:67], v[32:35], a[24:27], v[64:67]\n"
"v_add_f32 v43, v43, v51\n"
"v_add_f32 v40, v40, v100\n"
"v_mfma_f32_16x16x32_bf16 v[64:67], v[36:39], a[28:31], v[64:67]\n"
"v_add_f32 v41, v41, v101\n"
"v_add_f32 v42, v42, v102\n"
"v_mfma_f32_16x16x32_bf16 v[44:47], v[72:75], a[32:35], 0\n"
"v_add_f32 v43, v43, v103\n"
"v_mfma_f32_16x16x32_bf16 v[44:47], v[76:79], a[36:39], v[44:47]\n"
"ds_write_addtid_b32 v64 offset:1536\n"
"v_mfma_f32_16x16x32_bf16 v[44:47], v[80:83], a[40:43], v[44:47]\n"
"s_add_u32 s93, s50, 0xffe80000\n"
"s_and_b32 s94, s93, 0xfbffffff\n"
"ds_write_addtid_b32 v65 offset:6176\n"
"v_mfma_f32_16x16x32_bf16 v[44:47], v[20:23], a[44:47], v[44:47]\n"
"ds_write_addtid_b32 v66 offset:10880\n"
"v_cvt_pk_bf16_f32 v48, v40, v41\n"
"v_mfma_f32_16x16x32_bf16 v[44:47], v[24:27], a[48:51], v[44:47]\n"
"ds_write_addtid_b32 v67 offset:15520\n"
"v_cvt_pk_bf16_f32 v49, v42, v43\n"
"v_mfma_f32_16x16x32_bf16 v[44:47], v[28:31], a[52:55], v[44:47]\n"
"s_waitcnt lgkmcnt(4)\n"
"v_add_f32 v108, v108, v112\n"
"v_add_f32 v109, v109, v113\n"
"v_mfma_f32_16x16x32_bf16 v[44:47], v[32:35], a[56:59], v[44:47]\n"
"s_waitcnt vmcnt(1)\n"
"v_add_f32 v110, v110, v114\n"
"v_mfma_f32_16x16x32_bf16 v[44:47], v[36:39], a[60:63], v[44:47]\n"
"buffer_store_dwordx2 v[0:1], v12, s[40:43], s95 offen offset:0 sc1\n"
"v_add_f32 v111, v111, v115\n"
"v_mfma_f32_16x16x32_bf16 v[40:43], v[72:75], a[64:67], 0\n"
"v_add_f32 v108, v108, v116\n"
"v_add_f32 v109, v109, v117\n"
"v_mfma_f32_16x16x32_bf16 v[40:43], v[76:79], a[68:71], v[40:43]\n"
"ds_write_addtid_b32 v44 offset:1792\n"
"v_add_f32 v110, v110, v118\n"
"v_mfma_f32_16x16x32_bf16 v[40:43], v[80:83], a[72:75], v[40:43]\n"
"ds_write_addtid_b32 v45 offset:6432\n"
"v_add_f32 v111, v111, v119\n"
"v_mfma_f32_16x16x32_bf16 v[40:43], v[20:23], a[76:79], v[40:43]\n"
"ds_write_addtid_b32 v46 offset:11136\n"
"v_mfma_f32_16x16x32_bf16 v[40:43], v[24:27], a[80:83], v[40:43]\n"
"ds_write_addtid_b32 v47 offset:15776\n"
"v_add_f32 v108, v108, v104\n"
"v_mfma_f32_16x16x32_bf16 v[40:43], v[28:31], a[84:87], v[40:43]\n"
"v_add_f32 v109, v109, v105\n"
"v_add_f32 v110, v110, v106\n"
"v_mfma_f32_16x16x32_bf16 v[40:43], v[32:35], a[88:91], v[40:43]\n"
"v_add_f32 v111, v111, v107\n"
"v_cvt_pk_bf16_f32 v50, v108, v109\n"
"v_mfma_f32_16x16x32_bf16 v[40:43], v[36:39], a[92:95], v[40:43]\n"
"s_waitcnt vmcnt(1)\n"
"v_cvt_pk_bf16_f32 v51, v110, v111\n"
"v_lshlrev_b32 v112, 16, v96\n"
"v_mfma_f32_16x16x32_bf16 v[44:47], v[72:75], a[96:99], 0\n"
"v_and_b32 v113, 0xffff0000, v96\n"
"v_lshlrev_b32 v114, 16, v97\n"
"v_mfma_f32_16x16x32_bf16 v[44:47], v[76:79], a[100:103], v[44:47]\n"
"ds_write_addtid_b32 v40 offset:2048\n"
"v_and_b32 v115, 0xffff0000, v97\n"
"v_mfma_f32_16x16x32_bf16 v[44:47], v[80:83], a[104:107], v[44:47]\n"
"ds_write_addtid_b32 v41 offset:6688\n"
"v_lshlrev_b32 v108, 16, v98\n"
"v_mfma_f32_16x16x32_bf16 v[44:47], v[20:23], a[108:111], v[44:47]\n"
"ds_write_addtid_b32 v42 offset:11392\n"
"v_and_b32 v109, 0xffff0000, v98\n"
"v_mfma_f32_16x16x32_bf16 v[44:47], v[24:27], a[112:115], v[44:47]\n"
"ds_write_addtid_b32 v43 offset:16032\n"
"v_lshlrev_b32 v110, 16, v99\n"
"v_mfma_f32_16x16x32_bf16 v[44:47], v[28:31], a[116:119], v[44:47]\n"
"v_and_b32 v111, 0xffff0000, v99\n"
"v_mfma_f32_16x16x32_bf16 v[44:47], v[32:35], a[120:123], v[44:47]\n"
"v_mfma_f32_16x16x32_bf16 v[44:47], v[36:39], a[124:127], v[44:47]\n"
"buffer_store_dwordx4 v[48:51], v11, s[40:43], s94 offen sc1\n"
"v_mfma_f32_16x16x32_bf16 v[40:43], v[48:51], v[48:51], 0\n"
"v_mfma_f32_16x16x32_bf16 v[96:99], v[14:17], v[48:51], 0\n"
"v_mfma_f32_16x16x32_bf16 v[100:103], v[72:75], a[128:131], 0\n"
"v_cndmask_b32_e64 v40, v40, v41, s[62:63]\n"
"v_cndmask_b32_e64 v42, v42, v43, s[62:63]\n"
"v_mfma_f32_16x16x32_bf16 v[100:103], v[76:79], a[132:135], v[100:103]\n"
"ds_write_addtid_b32 v44 offset:2304\n"
"v_cndmask_b32_e64 v97, v40, v42, s[64:65]\n"
"v_mfma_f32_16x16x32_bf16 v[100:103], v[80:83], a[136:139], v[100:103]\n"
"buffer_store_dwordx2 v[96:97], v13, s[40:43], s94 offen offset:0 sc1\n"
"ds_write_addtid_b32 v45 offset:6944\n"
"v_mfma_f32_16x16x32_bf16 v[100:103], v[20:23], a[140:143], v[100:103]\n"
"ds_write_addtid_b32 v46 offset:11648\n"
"v_mfma_f32_16x16x32_bf16 v[100:103], v[24:27], a[144:147], v[100:103]\n"
"ds_write_addtid_b32 v47 offset:16288\n"
"v_mfma_f32_16x16x32_bf16 v[100:103], v[28:31], a[148:151], v[100:103]\n"
"v_mfma_f32_16x16x32_bf16 v[100:103], v[32:35], a[152:155], v[100:103]\n"
"v_mfma_f32_16x16x32_bf16 v[100:103], v[36:39], a[156:159], v[100:103]\n"
"v_mfma_f32_16x16x32_bf16 v[40:43], v[72:75], a[160:163], 0\n"
"v_mfma_f32_16x16x32_bf16 v[40:43], v[76:79], a[164:167], v[40:43]\n"
"ds_write_addtid_b32 v100 offset:2560\n"
"v_mfma_f32_16x16x32_bf16 v[40:43], v[80:83], a[168:171], v[40:43]\n"
"ds_write_addtid_b32 v101 offset:7200\n"
"v_mfma_f32_16x16x32_bf16 v[40:43], v[20:23], a[172:175], v[40:43]\n"
"ds_write_addtid_b32 v102 offset:11904\n"
"v_mfma_f32_16x16x32_bf16 v[40:43], v[24:27], a[176:179], v[40:43]\n"
"ds_write_addtid_b32 v103 offset:16544\n"
"v_mfma_f32_16x16x32_bf16 v[40:43], v[28:31], a[180:183], v[40:43]\n"
"v_mfma_f32_16x16x32_bf16 v[40:43], v[32:35], a[184:187], v[40:43]\n"
"v_mfma_f32_16x16x32_bf16 v[40:43], v[36:39], a[188:191], v[40:43]\n"
"v_mfma_f32_16x16x32_bf16 v[112:115], a[192:195], v[72:75], v[112:115]\n"
"v_mfma_f32_16x16x32_bf16 v[112:115], a[196:199], v[76:79], v[112:115]\n"
"ds_write_addtid_b32 v40 offset:2816\n"
"v_mfma_f32_16x16x32_bf16 v[112:115], a[200:203], v[80:83], v[112:115]\n"
"ds_write_addtid_b32 v41 offset:7456\n"
"v_mfma_f32_16x16x32_bf16 v[112:115], a[204:207], v[20:23], v[112:115]\n"
"ds_write_addtid_b32 v42 offset:12160\n"
"v_mfma_f32_16x16x32_bf16 v[112:115], a[208:211], v[24:27], v[112:115]\n"
"ds_write_addtid_b32 v43 offset:16800\n"
"v_mfma_f32_16x16x32_bf16 v[112:115], a[212:215], v[28:31], v[112:115]\n"
"v_mfma_f32_16x16x32_bf16 v[112:115], a[216:219], v[32:35], v[112:115]\n"
"v_mfma_f32_16x16x32_bf16 v[112:115], a[220:223], v[36:39], v[112:115]\n"
"v_mfma_f32_16x16x32_bf16 v[108:111], a[224:227], v[72:75], v[108:111]\n"
"s_waitcnt lgkmcnt(0)\n"
"v_mfma_f32_16x16x32_bf16 v[108:111], a[228:231], v[76:79], v[108:111]\n"
"s_barrier\n"
"v_mfma_f32_16x16x32_bf16 v[108:111], a[232:235], v[80:83], v[108:111]\n"
"v_mfma_f32_16x16x32_bf16 v[108:111], a[236:239], v[20:23], v[108:111]\n"
"ds_read_b128 v[72:75], v2 offset:0\n"
"v_mfma_f32_16x16x32_bf16 v[108:111], a[240:243], v[24:27], v[108:111]\n"
"ds_read_b128 v[44:47], v3 offset:2048\n"
"ds_read_b128 v[48:51], v4 offset:1024\n"
"v_mfma_f32_16x16x32_bf16 v[108:111], a[244:247], v[28:31], v[108:111]\n"
"ds_read_b128 v[76:79], v2 offset:256\n"
"ds_read_b128 v[80:83], v3 offset:2304\n"
"v_mfma_f32_16x16x32_bf16 v[108:111], a[248:251], v[32:35], v[108:111]\n"
"ds_read_b128 v[120:123], v4 offset:1280\n"
"v_mfma_f32_16x16x32_bf16 v[108:111], a[252:255], v[36:39], v[108:111]\n"
"s_waitcnt lgkmcnt(3)\n"
"v_add_f32 v72, v72, v44\n"
"v_add_f32 v73, v73, v45\n"
"v_add_f32 v74, v74, v46\n"
"v_add_f32 v75, v75, v47\n"
"v_add_f32 v72, v72, v48\n"
"v_add_f32 v73, v73, v49\n"
"v_add_f32 v74, v74, v50\n"
"v_add_f32 v75, v75, v51\n"
"v_add_f32 v72, v72, v112\n"
"v_add_f32 v73, v73, v113\n"
"v_add_f32 v74, v74, v114\n"
"v_add_f32 v75, v75, v115\n"
"s_add_u32 s95, s50, 0x0\n"
"s_and_b32 s94, s95, 0xfbffffff\n"
"v_cvt_pk_bf16_f32 v48, v72, v73\n"
"v_cvt_pk_bf16_f32 v49, v74, v75\n"
"s_waitcnt lgkmcnt(0)\n"
"v_add_f32 v76, v76, v80\n"
"v_add_f32 v77, v77, v81\n"
"v_add_f32 v78, v78, v82\n"
"v_add_f32 v79, v79, v83\n"
"v_add_f32 v76, v76, v120\n"
"v_add_f32 v77, v77, v121\n"
"v_add_f32 v78, v78, v122\n"
"v_add_f32 v79, v79, v123\n"
"v_add_f32 v76, v76, v108\n"
"s_waitcnt vmcnt(0)\n"
"buffer_store_dwordx2 v[0:1], v12, s[40:43], s93 offen offset:0 sc1\n"
"v_add_f32 v77, v77, v109\n"
"v_add_f32 v78, v78, v110\n"
"v_add_f32 v79, v79, v111\n"
"v_cvt_pk_bf16_f32 v50, v76, v77\n"
"v_cvt_pk_bf16_f32 v51, v78, v79\n"
"buffer_store_dwordx4 v[48:51], v11, s[40:43], s94 offen sc1\n"
"s_nop 0\n"
"v_mfma_f32_16x16x32_bf16 v[80:83], v[48:51], v[48:51], 0\n"
"v_mfma_f32_16x16x32_bf16 v[108:111], v[14:17], v[48:51], 0\n"
"s_nop 6\n"
"v_cndmask_b32_e64 v80, v80, v81, s[62:63]\n"
"v_cndmask_b32_e64 v82, v82, v83, s[62:63]\n"
"v_cndmask_b32_e64 v109, v80, v82, s[64:65]\n"
"buffer_store_dwordx2 v[108:109], v13, s[40:43], s94 offen offset:0 sc1\n"
"s_waitcnt vmcnt(0)\n"
"buffer_store_dwordx2 v[0:1], v12, s[40:43], s95 offen offset:0 sc1\n"
"s_branch .LO_end\n"
".LO_t1:\n"
"v_mfma_f32_16x16x32_bf16 v[112:115], a[196:199], v[76:79], v[112:115]\n"
"ds_write_addtid_b32 v40 offset:2816\n"
"v_mfma_f32_16x16x32_bf16 v[112:115], a[200:203], v[80:83], v[112:115]\n"
"ds_write_addtid_b32 v41 offset:7456\n"
"v_mfma_f32_16x16x32_bf16 v[112:115], a[204:207], v[20:23], v[112:115]\n"
"ds_write_addtid_b32 v42 offset:12160\n"
"v_mfma_f32_16x16x32_bf16 v[112:115], a[208:211], v[24:27], v[112:115]\n"
"ds_write_addtid_b32 v43 offset:16800\n"
"v_mfma_f32_16x16x32_bf16 v[112:115], a[212:215], v[28:31], v[112:115]\n"
"v_mfma_f32_16x16x32_bf16 v[112:115], a[216:219], v[32:35], v[112:115]\n"
"v_mfma_f32_16x16x32_bf16 v[112:115], a[220:223], v[36:39], v[112:115]\n"
"v_mfma_f32_16x16x32_bf16 v[108:111], a[224:227], v[72:75], v[108:111]\n"
"s_waitcnt lgkmcnt(0)\n"
"v_mfma_f32_16x16x32_bf16 v[108:111], a[228:231], v[76:79], v[108:111]\n"
"s_barrier\n"
"v_mfma_f32_16x16x32_bf16 v[108:111], a[232:235], v[80:83], v[108:111]\n"
"v_mfma_f32_16x16x32_bf16 v[108:111], a[236:239], v[20:23], v[108:111]\n"
"ds_read_b128 v[72:75], v2 offset:0\n"
"v_mfma_f32_16x16x32_bf16 v[108:111], a[240:243], v[24:27], v[108:111]\n"
"ds_read_b128 v[44:47], v3 offset:2048\n"
"ds_read_b128 v[48:51], v4 offset:1024\n"
"v_mfma_f32_16x16x32_bf16 v[108:111], a[244:247], v[28:31], v[108:111]\n"
"ds_read_b128 v[76:79], v2 offset:256\n"
"ds_read_b128 v[80:83], v3 offset:2304\n"
"v_mfma_f32_16x16x32_bf16 v[108:111], a[248:251], v[32:35], v[108:111]\n"
"ds_read_b128 v[120:123], v4 offset:1280\n"
"v_mfma_f32_16x16x32_bf16 v[108:111], a[252:255], v[36:39], v[108:111]\n"
"s_waitcnt vmcnt(11)\n"
"v_mfma_f32_16x16x32_bf16 v[40:43], v[84:87], a[0:3], 0\n"
"v_mfma_f32_16x16x32_bf16 v[40:43], v[88:91], a[4:7], v[40:43]\n"
"s_waitcnt vmcnt(8)\n"
"v_mfma_f32_16x16x32_bf16 v[40:43], v[92:95], a[8:11], v[40:43]\n"
"s_waitcnt lgkmcnt(3)\n"
"v_add_f32 v72, v72, v44\n"
"v_add_f32 v73, v73, v45\n"
"v_mfma_f32_16x16x32_bf16 v[40:43], v[52:55], a[12:15], v[40:43]\n"
"s_waitcnt vmcnt(6)\n"
"v_add_f32 v74, v74, v46\n"
"v_add_f32 v75, v75, v47\n"
"v_mfma_f32_16x16x32_bf16 v[40:43], v[56:59], a[16:19], v[40:43]\n"
"v_add_f32 v72, v72, v48\n"
"v_add_f32 v73, v73, v49\n"
"v_mfma_f32_16x16x32_bf16 v[40:43], v[60:63], a[20:23], v[40:43]\n"
"s_waitcnt vmcnt(3)\n"
"v_add_f32 v74, v74, v50\n"
"v_mfma_f32_16x16x32_bf16 v[40:43], v[64:67], a[24:27], v[40:43]\n"
"v_add_f32 v75, v75, v51\n"
"v_add_f32 v72, v72, v112\n"
"v_mfma_f32_16x16x32_bf16 v[40:43], v[68:71], a[28:31], v[40:43]\n"
"v_add_f32 v73, v73, v113\n"
"v_add_f32 v74, v74, v114\n"
"v_mfma_f32_16x16x32_bf16 v[44:47], v[84:87], a[32:35], 0\n"
"v_add_f32 v75, v75, v115\n"
"v_mfma_f32_16x16x32_bf16 v[44:47], v[88:91], a[36:39], v[44:47]\n"
"s_add_u32 s95, s50, 0xffe80000\n"
"s_and_b32 s94, s95, 0xfbffffff\n"
"ds_write_addtid_b32 v40 offset:0\n"
"v_mfma_f32_16x16x32_bf16 v[44:47], v[92:95], a[40:43], v[44:47]\n"
"ds_write_addtid_b32 v41 offset:4640\n"
"v_cvt_pk_bf16_f32 v48, v72, v73\n"
"v_mfma_f32_16x16x32_bf16 v[44:47], v[52:55], a[44:47], v[44:47]\n"
"ds_write_addtid_b32 v42 offset:9344\n"
"v_cvt_pk_bf16_f32 v49, v74, v75\n"
"v_mfma_f32_16x16x32_bf16 v[44:47], v[56:59], a[48:51], v[44:47]\n"
"s_waitcnt lgkmcnt(3)\n"
"ds_write_addtid_b32 v43 offset:13984\n"
"v_add_f32 v76, v76, v80\n"
"v_mfma_f32_16x16x32_bf16 v[44:47], v[60:63], a[52:55], v[44:47]\n"
"v_add_f32 v77, v77, v81\n"
"v_mfma_f32_16x16x32_bf16 v[44:47], v[64:67], a[56:59], v[44:47]\n"
"v_add_f32 v78, v78, v82\n"
"v_add_f32 v79, v79, v83\n"
"v_mfma_f32_16x16x32_bf16 v[44:47], v[68:71], a[60:63], v[44:47]\n"
"v_add_f32 v76, v76, v120\n"
"v_add_f32 v77, v77, v121\n"
"v_mfma_f32_16x16x32_bf16 v[40:43], v[84:87], a[64:67], 0\n"
"v_add_f32 v78, v78, v122\n"
"v_add_f32 v79, v79, v123\n"
"v_mfma_f32_16x16x32_bf16 v[40:43], v[88:91], a[68:71], v[40:43]\n"
"ds_write_addtid_b32 v44 offset:256\n"
"v_add_f32 v76, v76, v108\n"
"v_mfma_f32_16x16x32_bf16 v[40:43], v[92:95], a[72:75], v[40:43]\n"
"s_waitcnt vmcnt(1)\n"
"buffer_store_dwordx2 v[0:1], v12, s[40:43], s93 offen offset:0 sc1\n"
"ds_write_addtid_b32 v45 offset:4896\n"
"v_mfma_f32_16x16x32_bf16 v[40:43], v[52:55], a[76:79], v[40:43]\n"
"ds_write_addtid_b32 v46 offset:9600\n"
"v_add_f32 v77, v77, v109\n"
"v_mfma_f32_16x16x32_bf16 v[40:43], v[56:59], a[80:83], v[40:43]\n"
"s_waitcnt vmcnt(1)\n"
"ds_write_addtid_b32 v47 offset:14240\n"
"v_mfma_f32_16x16x32_bf16 v[40:43], v[60:63], a[84:87], v[40:43]\n"
"v_add_f32 v78, v78, v110\n"
"v_add_f32 v79, v79, v111\n"
"v_mfma_f32_16x16x32_bf16 v[40:43], v[64:67], a[88:91], v[40:43]\n"
"v_cvt_pk_bf16_f32 v50, v76, v77\n"
"v_cvt_pk_bf16_f32 v51, v78, v79\n"
"v_mfma_f32_16x16x32_bf16 v[40:43], v[68:71], a[92:95], v[40:43]\n"
"v_lshlrev_b32 v72, 16, v116\n"
"v_and_b32 v73, 0xffff0000, v116\n"
"v_mfma_f32_16x16x32_bf16 v[44:47], v[84:87], a[96:99], 0\n"
"v_lshlrev_b32 v74, 16, v117\n"
"v_and_b32 v75, 0xffff0000, v117\n"
"v_mfma_f32_16x16x32_bf16 v[44:47], v[88:91], a[100:103], v[44:47]\n"
"ds_write_addtid_b32 v40 offset:512\n"
"v_lshlrev_b32 v76, 16, v118\n"
"v_mfma_f32_16x16x32_bf16 v[44:47], v[92:95], a[104:107], v[44:47]\n"
"ds_write_addtid_b32 v41 offset:5152\n"
"v_and_b32 v77, 0xffff0000, v118\n"
"v_mfma_f32_16x16x32_bf16 v[44:47], v[52:55], a[108:111], v[44:47]\n"
"ds_write_addtid_b32 v42 offset:9856\n"
"v_lshlrev_b32 v78, 16, v119\n"
"v_mfma_f32_16x16x32_bf16 v[44:47], v[56:59], a[112:115], v[44:47]\n"
"ds_write_addtid_b32 v43 offset:14496\n"
"v_and_b32 v79, 0xffff0000, v119\n"
"v_mfma_f32_16x16x32_bf16 v[44:47], v[60:63], a[116:119], v[44:47]\n"
"v_mfma_f32_16x16x32_bf16 v[44:47], v[64:67], a[120:123], v[44:47]\n"
"v_mfma_f32_16x16x32_bf16 v[44:47], v[68:71], a[124:127], v[44:47]\n"
"v_mfma_f32_16x16x32_bf16 v[40:43], v[84:87], a[128:131], 0\n"
"v_mfma_f32_16x16x32_bf16 v[40:43], v[88:91], a[132:135], v[40:43]\n"
"ds_write_addtid_b32 v44 offset:768\n"
"v_mfma_f32_16x16x32_bf16 v[40:43], v[92:95], a[136:139], v[40:43]\n"
"buffer_store_dwordx4 v[48:51], v11, s[40:43], s94 offen sc1\n"
"ds_write_addtid_b32 v45 offset:5408\n"
"v_mfma_f32_16x16x32_bf16 v[80:83], v[48:51], v[48:51], 0\n"
"v_mfma_f32_16x16x32_bf16 v[108:111], v[14:17], v[48:51], 0\n"
"v_mfma_f32_16x16x32_bf16 v[40:43], v[52:55], a[140:143], v[40:43]\n"
"ds_write_addtid_b32 v46 offset:10112\n"
"v_cndmask_b32_e64 v80, v80, v81, s[62:63]\n"
"v_mfma_f32_16x16x32_bf16 v[40:43], v[56:59], a[144:147], v[40:43]\n"
"ds_write_addtid_b32 v47 offset:14752\n"
"v_cndmask_b32_e64 v82, v82, v83, s[62:63]\n"
"v_mfma_f32_16x16x32_bf16 v[40:43], v[60:63], a[148:151], v[40:43]\n"
"v_cndmask_b32_e64 v109, v80, v82, s[64:65]\n"
"v_mfma_f32_16x16x32_bf16 v[40:43], v[64:67], a[152:155], v[40:43]\n"
"buffer_store_dwordx2 v[108:109], v13, s[40:43], s94 offen offset:0 sc1\n"
"v_mfma_f32_16x16x32_bf16 v[40:43], v[68:71], a[156:159], v[40:43]\n"
"v_mfma_f32_16x16x32_bf16 v[48:51], v[84:87], a[160:163], 0\n"
"v_mfma_f32_16x16x32_bf16 v[48:51], v[88:91], a[164:167], v[48:51]\n"
"ds_write_addtid_b32 v40 offset:1024\n"
"v_mfma_f32_16x16x32_bf16 v[48:51], v[92:95], a[168:171], v[48:51]\n"
"ds_write_addtid_b32 v41 offset:5664\n"
"v_mfma_f32_16x16x32_bf16 v[48:51], v[52:55], a[172:175], v[48:51]\n"
"ds_write_addtid_b32 v42 offset:10368\n"
"v_mfma_f32_16x16x32_bf16 v[48:51], v[56:59], a[176:179], v[48:51]\n"
"ds_write_addtid_b32 v43 offset:15008\n"
"v_mfma_f32_16x16x32_bf16 v[48:51], v[60:63], a[180:183], v[48:51]\n"
"v_mfma_f32_16x16x32_bf16 v[48:51], v[64:67], a[184:187], v[48:51]\n"
"v_mfma_f32_16x16x32_bf16 v[48:51], v[68:71], a[188:191], v[48:51]\n"
"v_mfma_f32_16x16x32_bf16 v[72:75], a[192:195], v[84:87], v[72:75]\n"
"v_mfma_f32_16x16x32_bf16 v[72:75], a[196:199], v[88:91], v[72:75]\n"
"ds_write_addtid_b32 v48 offset:1280\n"
"v_mfma_f32_16x16x32_bf16 v[72:75], a[200:203], v[92:95], v[72:75]\n"
"ds_write_addtid_b32 v49 offset:5920\n"
"v_mfma_f32_16x16x32_bf16 v[72:75], a[204:207], v[52:55], v[72:75]\n"
"ds_write_addtid_b32 v50 offset:10624\n"
"v_mfma_f32_16x16x32_bf16 v[72:75], a[208:211], v[56:59], v[72:75]\n"
"ds_write_addtid_b32 v51 offset:15264\n"
"v_mfma_f32_16x16x32_bf16 v[72:75], a[212:215], v[60:63], v[72:75]\n"
"v_mfma_f32_16x16x32_bf16 v[72:75], a[216:219], v[64:67], v[72:75]\n"
"v_mfma_f32_16x16x32_bf16 v[72:75], a[220:223], v[68:71], v[72:75]\n"
"v_mfma_f32_16x16x32_bf16 v[76:79], a[224:227], v[84:87], v[76:79]\n"
"s_waitcnt lgkmcnt(0)\n"
"v_mfma_f32_16x16x32_bf16 v[76:79], a[228:231], v[88:91], v[76:79]\n"
"s_barrier\n"
"v_mfma_f32_16x16x32_bf16 v[76:79], a[232:235], v[92:95], v[76:79]\n"
"ds_read_b128 v[80:83], v2 offset:1536\n"
"ds_read_b128 v[84:87], v3 offset:512\n"
"v_mfma_f32_16x16x32_bf16 v[76:79], a[236:239], v[52:55], v[76:79]\n"
"v_mfma_f32_16x16x32_bf16 v[76:79], a[240:243], v[56:59], v[76:79]\n"
"ds_read_b128 v[88:91], v4 offset:2560\n"
"v_mfma_f32_16x16x32_bf16 v[76:79], a[244:247], v[60:63], v[76:79]\n"
"ds_read_b128 v[92:95], v2 offset:1792\n"
"ds_read_b128 v[108:111], v3 offset:768\n"
"v_mfma_f32_16x16x32_bf16 v[76:79], a[248:251], v[64:67], v[76:79]\n"
"ds_read_b128 v[116:119], v4 offset:2816\n"
"v_mfma_f32_16x16x32_bf16 v[76:79], a[252:255], v[68:71], v[76:79]\n"
"s_waitcnt lgkmcnt(3)\n"
"v_add_f32 v80, v80, v84\n"
"v_add_f32 v81, v81, v85\n"
"v_add_f32 v82, v82, v86\n"
"v_add_f32 v83, v83, v87\n"
"v_add_f32 v80, v80, v88\n"
"v_add_f32 v81, v81, v89\n"
"v_add_f32 v82, v82, v90\n"
"v_add_f32 v83, v83, v91\n"
"v_add_f32 v80, v80, v72\n"
"v_add_f32 v81, v81, v73\n"
"v_add_f32 v82, v82, v74\n"
"v_add_f32 v83, v83, v75\n"
"s_add_u32 s93, s50, 0x0\n"
"s_and_b32 s94, s93, 0xfbffffff\n"
"v_cvt_pk_bf16_f32 v72, v80, v81\n"
"v_cvt_pk_bf16_f32 v73, v82, v83\n"
"s_waitcnt lgkmcnt(0)\n"
"v_add_f32 v92, v92, v108\n"
"v_add_f32 v93, v93, v109\n"
"v_add_f32 v94, v94, v110\n"
"v_add_f32 v95, v95, v111\n"
"v_add_f32 v92, v92, v116\n"
"v_add_f32 v93, v93, v117\n"
"v_add_f32 v94, v94, v118\n"
"v_add_f32 v95, v95, v119\n"
"v_add_f32 v92, v92, v76\n"
"v_add_f32 v93, v93, v77\n"
"v_add_f32 v94, v94, v78\n"
"v_add_f32 v95, v95, v79\n"
"s_waitcnt vmcnt(0)\n"
"v_cvt_pk_bf16_f32 v74, v92, v93\n"
"v_cvt_pk_bf16_f32 v75, v94, v95\n"
"buffer_store_dwordx4 v[72:75], v11, s[40:43], s94 offen sc1\n"
"s_nop 0\n"
"v_mfma_f32_16x16x32_bf16 v[76:79], v[72:75], v[72:75], 0\n"
"buffer_store_dwordx2 v[0:1], v12, s[40:43], s95 offen offset:0 sc1\n"
"v_mfma_f32_16x16x32_bf16 v[88:91], v[14:17], v[72:75], 0\n"
"s_nop 5\n"
"v_cndmask_b32_e64 v76, v76, v77, s[62:63]\n"
"v_cndmask_b32_e64 v78, v78, v79, s[62:63]\n"
"v_cndmask_b32_e64 v89, v76, v78, s[64:65]\n"
"buffer_store_dwordx2 v[88:89], v13, s[40:43], s94 offen offset:0 sc1\n"
"s_waitcnt vmcnt(0)\n"
"buffer_store_dwordx2 v[0:1], v12, s[40:43], s93 offen offset:0 sc1\n"
"s_branch .LO_end\n"
".LO_t2:\n"
"v_mfma_f32_16x16x32_bf16 v[48:51], v[68:71], a[188:191], v[48:51]\n"
"v_mfma_f32_16x16x32_bf16 v[72:75], a[192:195], v[84:87], v[72:75]\n"
"v_mfma_f32_16x16x32_bf16 v[72:75], a[196:199], v[88:91], v[72:75]\n"
"ds_write_addtid_b32 v48 offset:1280\n"
"v_mfma_f32_16x16x32_bf16 v[72:75], a[200:203], v[92:95], v[72:75]\n"
"ds_write_addtid_b32 v49 offset:5920\n"
"v_mfma_f32_16x16x32_bf16 v[72:75], a[204:207], v[52:55], v[72:75]\n"
"ds_write_addtid_b32 v50 offset:10624\n"
"v_mfma_f32_16x16x32_bf16 v[72:75], a[208:211], v[56:59], v[72:75]\n"
"ds_write_addtid_b32 v51 offset:15264\n"
"v_mfma_f32_16x16x32_bf16 v[72:75], a[212:215], v[60:63], v[72:75]\n"
"v_mfma_f32_16x16x32_bf16 v[72:75], a[216:219], v[64:67], v[72:75]\n"
"v_mfma_f32_16x16x32_bf16 v[72:75], a[220:223], v[68:71], v[72:75]\n"
"v_mfma_f32_16x16x32_bf16 v[76:79], a[224:227], v[84:87], v[76:79]\n"
"s_waitcnt lgkmcnt(0)\n"
"v_mfma_f32_16x16x32_bf16 v[76:79], a[228:231], v[88:91], v[76:79]\n"
"s_barrier\n"
"v_mfma_f32_16x16x32_bf16 v[76:79], a[232:235], v[92:95], v[76:79]\n"
"ds_read_b128 v[80:83], v2 offset:1536\n"
"ds_read_b128 v[84:87], v3 offset:512\n"
"v_mfma_f32_16x16x32_bf16 v[76:79], a[236:239], v[52:55], v[76:79]\n"
"v_mfma_f32_16x16x32_bf16 v[76:79], a[240:243], v[56:59], v[76:79]\n"
"ds_read_b128 v[88:91], v4 offset:2560\n"
"v_mfma_f32_16x16x32_bf16 v[76:79], a[244:247], v[60:63], v[76:79]\n"
"ds_read_b128 v[92:95], v2 offset:1792\n"
"ds_read_b128 v[108:111], v3 offset:768\n"
"v_mfma_f32_16x16x32_bf16 v[76:79], a[248:251], v[64:67], v[76:79]\n"
"ds_read_b128 v[116:119], v4 offset:2816\n"
"v_mfma_f32_16x16x32_bf16 v[76:79], a[252:255], v[68:71], v[76:79]\n"
"s_waitcnt vmcnt(11)\n"
"v_mfma_f32_16x16x32_bf16 v[64:67], v[96:99], a[0:3], 0\n"
"v_mfma_f32_16x16x32_bf16 v[64:67], v[100:103], a[4:7], v[64:67]\n"
"s_waitcnt vmcnt(8)\n"
"v_mfma_f32_16x16x32_bf16 v[64:67], v[104:107], a[8:11], v[64:67]\n"
"s_waitcnt lgkmcnt(3)\n"
"v_add_f32 v80, v80, v84\n"
"v_add_f32 v81, v81, v85\n"
"v_mfma_f32_16x16x32_bf16 v[64:67], v[20:23], a[12:15], v[64:67]\n"
"s_waitcnt vmcnt(6)\n"
"v_add_f32 v82, v82, v86\n"
"v_mfma_f32_16x16x32_bf16 v[64:67], v[24:27], a[16:19], v[64:67]\n"
"v_add_f32 v83, v83, v87\n"
"v_add_f32 v80, v80, v88\n"
"v_mfma_f32_16x16x32_bf16 v[64:67], v[28:31], a[20:23], v[64:67]\n"
"s_waitcnt vmcnt(3)\n"
"v_add_f32 v81, v81, v89\n"
"v_add_f32 v82, v82, v90\n"
"v_mfma_f32_16x16x32_bf16 v[64:67], v[32:35], a[24:27], v[64:67]\n"
"v_add_f32 v83, v83, v91\n"
"v_add_f32 v80, v80, v72\n"
"v_mfma_f32_16x16x32_bf16 v[64:67], v[36:39], a[28:31], v[64:67]\n"
"v_add_f32 v81, v81, v73\n"
"v_add_f32 v82, v82, v74\n"
"v_mfma_f32_16x16x32_bf16 v[84:87], v[96:99], a[32:35], 0\n"
"v_add_f32 v83, v83, v75\n"
"v_mfma_f32_16x16x32_bf16 v[84:87], v[100:103], a[36:39], v[84:87]\n"
"s_add_u32 s93, s50, 0xffe80000\n"
"s_and_b32 s94, s93, 0xfbffffff\n"
"ds_write_addtid_b32 v64 offset:1536\n"
"v_mfma_f32_16x16x32_bf16 v[84:87], v[104:107], a[40:43], v[84:87]\n"
"ds_write_addtid_b32 v65 offset:6176\n"
"v_cvt_pk_bf16_f32 v72, v80, v81\n"
"v_mfma_f32_16x16x32_bf16 v[84:87], v[20:23], a[44:47], v[84:87]\n"
"ds_write_addtid_b32 v66 offset:10880\n"
"v_mfma_f32_16x16x32_bf16 v[84:87], v[24:27], a[48:51], v[84:87]\n"
"ds_write_addtid_b32 v67 offset:15520\n"
"v_cvt_pk_bf16_f32 v73, v82, v83\n"
"v_mfma_f32_16x16x32_bf16 v[84:87], v[28:31], a[52:55], v[84:87]\n"
"s_waitcnt lgkmcnt(4)\n"
"v_add_f32 v92, v92, v108\n"
"v_add_f32 v93, v93, v109\n"
"v_mfma_f32_16x16x32_bf16 v[84:87], v[32:35], a[56:59], v[84:87]\n"
"v_add_f32 v94, v94, v110\n"
"v_add_f32 v95, v95, v111\n"
"v_mfma_f32_16x16x32_bf16 v[84:87], v[36:39], a[60:63], v[84:87]\n"
"v_add_f32 v92, v92, v116\n"
"v_add_f32 v93, v93, v117\n"
"v_mfma_f32_16x16x32_bf16 v[80:83], v[96:99], a[64:67], 0\n"
"v_add_f32 v94, v94, v118\n"
"v_mfma_f32_16x16x32_bf16 v[80:83], v[100:103], a[68:71], v[80:83]\n"
"ds_write_addtid_b32 v84 offset:1792\n"
"v_mfma_f32_16x16x32_bf16 v[80:83], v[104:107], a[72:75], v[80:83]\n"
"ds_write_addtid_b32 v85 offset:6432\n"
"v_add_f32 v95, v95, v119\n"
"v_mfma_f32_16x16x32_bf16 v[80:83], v[20:23], a[76:79], v[80:83]\n"
"ds_write_addtid_b32 v86 offset:11136\n"
"v_add_f32 v92, v92, v76\n"
"v_mfma_f32_16x16x32_bf16 v[80:83], v[24:27], a[80:83], v[80:83]\n"
"ds_write_addtid_b32 v87 offset:15776\n"
"v_add_f32 v93, v93, v77\n"
"v_mfma_f32_16x16x32_bf16 v[80:83], v[28:31], a[84:87], v[80:83]\n"
"s_waitcnt vmcnt(1)\n"
"v_add_f32 v94, v94, v78\n"
"v_add_f32 v95, v95, v79\n"
"v_mfma_f32_16x16x32_bf16 v[80:83], v[32:35], a[88:91], v[80:83]\n"
"s_waitcnt vmcnt(0)\n"
"v_cvt_pk_bf16_f32 v74, v92, v93\n"
"v_cvt_pk_bf16_f32 v75, v94, v95\n"
"v_mfma_f32_16x16x32_bf16 v[80:83], v[36:39], a[92:95], v[80:83]\n"
"buffer_store_dwordx4 v[72:75], v11, s[40:43], s94 offen sc1\n"
"v_lshlrev_b32 v84, 16, v112\n"
"v_and_b32 v85, 0xffff0000, v112\n"
"v_lshlrev_b32 v86, 16, v113\n"
"v_mfma_f32_16x16x32_bf16 v[76:79], v[72:75], v[72:75], 0\n"
"buffer_store_dwordx2 v[0:1], v12, s[40:43], s95 offen offset:0 sc1\n"
"v_and_b32 v87, 0xffff0000, v113\n"
"v_mfma_f32_16x16x32_bf16 v[88:91], v[14:17], v[72:75], 0\n"
"v_lshlrev_b32 v92, 16, v114\n"
"v_and_b32 v93, 0xffff0000, v114\n"
"v_mfma_f32_16x16x32_bf16 v[108:111], v[96:99], a[96:99], 0\n"
"v_cndmask_b32_e64 v76, v76, v77, s[62:63]\n"
"v_cndmask_b32_e64 v78, v78, v79, s[62:63]\n"
"v_mfma_f32_16x16x32_bf16 v[108:111], v[100:103], a[100:103], v[108:111]\n"
"ds_write_addtid_b32 v80 offset:2048\n"
"v_cndmask_b32_e64 v89, v76, v78, s[64:65]\n"
"v_mfma_f32_16x16x32_bf16 v[108:111], v[104:107], a[104:107], v[108:111]\n"
"buffer_store_dwordx2 v[88:89], v13, s[40:43], s94 offen offset:0 sc1\n"
"ds_write_addtid_b32 v81 offset:6688\n"
"v_mfma_f32_16x16x32_bf16 v[108:111], v[20:23], a[108:111], v[108:111]\n"
"ds_write_addtid_b32 v82 offset:11392\n"
"v_lshlrev_b32 v94, 16, v115\n"
"v_mfma_f32_16x16x32_bf16 v[108:111], v[24:27], a[112:115], v[108:111]\n"
"ds_write_addtid_b32 v83 offset:16032\n"
"v_and_b32 v95, 0xffff0000, v115\n"
"v_mfma_f32_16x16x32_bf16 v[108:111], v[28:31], a[116:119], v[108:111]\n"
"v_mfma_f32_16x16x32_bf16 v[108:111], v[32:35], a[120:123], v[108:111]\n"
"v_mfma_f32_16x16x32_bf16 v[108:111], v[36:39], a[124:127], v[108:111]\n"
"v_mfma_f32_16x16x32_bf16 v[72:75], v[96:99], a[128:131], 0\n"
"v_mfma_f32_16x16x32_bf16 v[72:75], v[100:103], a[132:135], v[72:75]\n"
"ds_write_addtid_b32 v108 offset:2304\n"
"v_mfma_f32_16x16x32_bf16 v[72:75], v[104:107], a[136:139], v[72:75]\n"
"ds_write_addtid_b32 v109 offset:6944\n"
"v_mfma_f32_16x16x32_bf16 v[72:75], v[20:23], a[140:143], v[72:75]\n"
"ds_write_addtid_b32 v110 offset:11648\n"
"v_mfma_f32_16x16x32_bf16 v[72:75], v[24:27], a[144:147], v[72:75]\n"
"ds_write_addtid_b32 v111 offset:16288\n"
"v_mfma_f32_16x16x32_bf16 v[72:75], v[28:31], a[148:151], v[72:75]\n"
"v_mfma_f32_16x16x32_bf16 v[72:75], v[32:35], a[152:155], v[72:75]\n"
"v_mfma_f32_16x16x32_bf16 v[72:75], v[36:39], a[156:159], v[72:75]\n"
"v_mfma_f32_16x16x32_bf16 v[76:79], v[96:99], a[160:163], 0\n"
"v_mfma_f32_16x16x32_bf16 v[76:79], v[100:103], a[164:167], v[76:79]\n"
"ds_write_addtid_b32 v72 offset:2560\n"
"v_mfma_f32_16x16x32_bf16 v[76:79], v[104:107], a[168:171], v[76:79]\n"
"ds_write_addtid_b32 v73 offset:7200\n"
"v_mfma_f32_16x16x32_bf16 v[76:79], v[20:23], a[172:175], v[76:79]\n"
"ds_write_addtid_b32 v74 offset:11904\n"
"v_mfma_f32_16x16x32_bf16 v[76:79], v[24:27], a[176:179], v[76:79]\n"
"ds_write_addtid_b32 v75 offset:16544\n"
"v_mfma_f32_16x16x32_bf16 v[76:79], v[28:31], a[180:183], v[76:79]\n"
"v_mfma_f32_16x16x32_bf16 v[76:79], v[32:35], a[184:187], v[76:79]\n"
"v_mfma_f32_16x16x32_bf16 v[76:79], v[36:39], a[188:191], v[76:79]\n"
"v_mfma_f32_16x16x32_bf16 v[84:87], a[192:195], v[96:99], v[84:87]\n"
"v_mfma_f32_16x16x32_bf16 v[84:87], a[196:199], v[100:103], v[84:87]\n"
"ds_write_addtid_b32 v76 offset:2816\n"
"v_mfma_f32_16x16x32_bf16 v[84:87], a[200:203], v[104:107], v[84:87]\n"
"ds_write_addtid_b32 v77 offset:7456\n"
"v_mfma_f32_16x16x32_bf16 v[84:87], a[204:207], v[20:23], v[84:87]\n"
"ds_write_addtid_b32 v78 offset:12160\n"
"v_mfma_f32_16x16x32_bf16 v[84:87], a[208:211], v[24:27], v[84:87]\n"
"ds_write_addtid_b32 v79 offset:16800\n"
"v_mfma_f32_16x16x32_bf16 v[84:87], a[212:215], v[28:31], v[84:87]\n"
"v_mfma_f32_16x16x32_bf16 v[84:87], a[216:219], v[32:35], v[84:87]\n"
"v_mfma_f32_16x16x32_bf16 v[84:87], a[220:223], v[36:39], v[84:87]\n"
"v_mfma_f32_16x16x32_bf16 v[92:95], a[224:227], v[96:99], v[92:95]\n"
"s_waitcnt lgkmcnt(0)\n"
"v_mfma_f32_16x16x32_bf16 v[92:95], a[228:231], v[100:103], v[92:95]\n"
"s_barrier\n"
"v_mfma_f32_16x16x32_bf16 v[92:95], a[232:235], v[104:107], v[92:95]\n"
"ds_read_b128 v[96:99], v2 offset:0\n"
"ds_read_b128 v[100:103], v3 offset:2048\n"
"v_mfma_f32_16x16x32_bf16 v[92:95], a[236:239], v[20:23], v[92:95]\n"
"ds_read_b128 v[104:107], v4 offset:1024\n"
"ds_read_b128 v[108:111], v2 offset:256\n"
"v_mfma_f32_16x16x32_bf16 v[92:95], a[240:243], v[24:27], v[92:95]\n"
"ds_read_b128 v[112:115], v3 offset:2304\n"
"ds_read_b128 v[116:119], v4 offset:1280\n"
"v_mfma_f32_16x16x32_bf16 v[92:95], a[244:247], v[28:31], v[92:95]\n"
"v_mfma_f32_16x16x32_bf16 v[92:95], a[248:251], v[32:35], v[92:95]\n"
"v_mfma_f32_16x16x32_bf16 v[92:95], a[252:255], v[36:39], v[92:95]\n"
"s_waitcnt lgkmcnt(3)\n"
"v_add_f32 v96, v96, v100\n"
"v_add_f32 v97, v97, v101\n"
"v_add_f32 v98, v98, v102\n"
"v_add_f32 v99, v99, v103\n"
"v_add_f32 v96, v96, v104\n"
"v_add_f32 v97, v97, v105\n"
"v_add_f32 v98, v98, v106\n"
"v_add_f32 v99, v99, v107\n"
"v_add_f32 v96, v96, v84\n"
"v_add_f32 v97, v97, v85\n"
"v_add_f32 v98, v98, v86\n"
"v_add_f32 v99, v99, v87\n"
"s_add_u32 s95, s50, 0x0\n"
"s_and_b32 s94, s95, 0xfbffffff\n"
"s_waitcnt vmcnt(0)\n"
"buffer_store_dwordx2 v[0:1], v12, s[40:43], s93 offen offset:0 sc1\n"
"v_cvt_pk_bf16_f32 v84, v96, v97\n"
"v_cvt_pk_bf16_f32 v85, v98, v99\n"
"s_waitcnt lgkmcnt(0)\n"
"v_add_f32 v108, v108, v112\n"
"v_add_f32 v109, v109, v113\n"
"v_add_f32 v110, v110, v114\n"
"v_add_f32 v111, v111, v115\n"
"v_add_f32 v108, v108, v116\n"
"v_add_f32 v109, v109, v117\n"
"v_add_f32 v110, v110, v118\n"
"v_add_f32 v111, v111, v119\n"
"v_add_f32 v108, v108, v92\n"
"v_add_f32 v109, v109, v93\n"
"v_add_f32 v110, v110, v94\n"
"v_add_f32 v111, v111, v95\n"
"v_cvt_pk_bf16_f32 v86, v108, v109\n"
"v_cvt_pk_bf16_f32 v87, v110, v111\n"
"buffer_store_dwordx4 v[84:87], v11, s[40:43], s94 offen sc1\n"
"s_nop 0\n"
"v_mfma_f32_16x16x32_bf16 v[108:111], v[84:87], v[84:87], 0\n"
"v_mfma_f32_16x16x32_bf16 v[112:115], v[14:17], v[84:87], 0\n"
"s_nop 6\n"
"v_cndmask_b32_e64 v108, v108, v109, s[62:63]\n"
"v_cndmask_b32_e64 v110, v110, v111, s[62:63]\n"
"v_cndmask_b32_e64 v113, v108, v110, s[64:65]\n"
"buffer_store_dwordx2 v[112:113], v13, s[40:43], s94 offen offset:0 sc1\n"
"s_waitcnt vmcnt(0)\n"
"buffer_store_dwordx2 v[0:1], v12, s[40:43], s95 offen offset:0 sc1\n"
"s_branch .LO_end\n"
".LO_t3:\n"
"v_mfma_f32_16x16x32_bf16 v[84:87], a[196:199], v[100:103], v[84:87]\n"
"ds_write_addtid_b32 v76 offset:2816\n"
"v_mfma_f32_16x16x32_bf16 v[84:87], a[200:203], v[104:107], v[84:87]\n"
"ds_write_addtid_b32 v77 offset:7456\n"
"v_mfma_f32_16x16x32_bf16 v[84:87], a[204:207], v[20:23], v[84:87]\n"
"ds_write_addtid_b32 v78 offset:12160\n"
"v_mfma_f32_16x16x32_bf16 v[84:87], a[208:211], v[24:27], v[84:87]\n"
"ds_write_addtid_b32 v79 offset:16800\n"
"v_mfma_f32_16x16x32_bf16 v[84:87], a[212:215], v[28:31], v[84:87]\n"
"v_mfma_f32_16x16x32_bf16 v[84:87], a[216:219], v[32:35], v[84:87]\n"
"v_mfma_f32_16x16x32_bf16 v[84:87], a[220:223], v[36:39], v[84:87]\n"
"v_mfma_f32_16x16x32_bf16 v[92:95], a[224:227], v[96:99], v[92:95]\n"
"s_waitcnt lgkmcnt(0)\n"
"v_mfma_f32_16x16x32_bf16 v[92:95], a[228:231], v[100:103], v[92:95]\n"
"s_barrier\n"
"v_mfma_f32_16x16x32_bf16 v[92:95], a[232:235], v[104:107], v[92:95]\n"
"ds_read_b128 v[96:99], v2 offset:0\n"
"ds_read_b128 v[100:103], v3 offset:2048\n"
"v_mfma_f32_16x16x32_bf16 v[92:95], a[236:239], v[20:23], v[92:95]\n"
"ds_read_b128 v[104:107], v4 offset:1024\n"
"ds_read_b128 v[108:111], v2 offset:256\n"
"v_mfma_f32_16x16x32_bf16 v[92:95], a[240:243], v[24:27], v[92:95]\n"
"ds_read_b128 v[112:115], v3 offset:2304\n"
"ds_read_b128 v[116:119], v4 offset:1280\n"
"v_mfma_f32_16x16x32_bf16 v[92:95], a[244:247], v[28:31], v[92:95]\n"
"v_mfma_f32_16x16x32_bf16 v[92:95], a[248:251], v[32:35], v[92:95]\n"
"v_mfma_f32_16x16x32_bf16 v[92:95], a[252:255], v[36:39], v[92:95]\n"
"s_waitcnt vmcnt(11)\n"
"v_mfma_f32_16x16x32_bf16 v[120:123], v[40:43], a[0:3], 0\n"
"v_mfma_f32_16x16x32_bf16 v[120:123], v[44:47], a[4:7], v[120:123]\n"
"s_waitcnt vmcnt(8)\n"
"v_mfma_f32_16x16x32_bf16 v[120:123], v[48:51], a[8:11], v[120:123]\n"
"s_waitcnt lgkmcnt(3)\n"
"v_add_f32 v96, v96, v100\n"
"v_add_f32 v97, v97, v101\n"
"v_mfma_f32_16x16x32_bf16 v[120:123], v[52:55], a[12:15], v[120:123]\n"
"s_waitcnt vmcnt(6)\n"
"v_add_f32 v98, v98, v102\n"
"v_add_f32 v99, v99, v103\n"
"v_mfma_f32_16x16x32_bf16 v[120:123], v[56:59], a[16:19], v[120:123]\n"
"v_add_f32 v96, v96, v104\n"
"v_add_f32 v97, v97, v105\n"
"v_mfma_f32_16x16x32_bf16 v[120:123], v[60:63], a[20:23], v[120:123]\n"
"s_waitcnt vmcnt(4)\n"
"v_add_f32 v98, v98, v106\n"
"v_add_f32 v99, v99, v107\n"
"v_mfma_f32_16x16x32_bf16 v[120:123], v[64:67], a[24:27], v[120:123]\n"
"v_add_f32 v96, v96, v84\n"
"v_mfma_f32_16x16x32_bf16 v[120:123], v[68:71], a[28:31], v[120:123]\n"
"v_add_f32 v97, v97, v85\n"
"v_mfma_f32_16x16x32_bf16 v[100:103], v[40:43], a[32:35], 0\n"
"v_add_f32 v98, v98, v86\n"
"v_add_f32 v99, v99, v87\n"
"v_mfma_f32_16x16x32_bf16 v[100:103], v[44:47], a[36:39], v[100:103]\n"
"s_add_u32 s95, s50, 0xffe80000\n"
"s_and_b32 s94, s95, 0xfbffffff\n"
"ds_write_addtid_b32 v120 offset:0\n"
"v_mfma_f32_16x16x32_bf16 v[100:103], v[48:51], a[40:43], v[100:103]\n"
"s_waitcnt vmcnt(1)\n"
"buffer_store_dwordx2 v[0:1], v12, s[40:43], s93 offen offset:0 sc1\n"
"ds_write_addtid_b32 v121 offset:4640\n"
"v_mfma_f32_16x16x32_bf16 v[100:103], v[52:55], a[44:47], v[100:103]\n"
"ds_write_addtid_b32 v122 offset:9344\n"
"v_cvt_pk_bf16_f32 v84, v96, v97\n"
"v_mfma_f32_16x16x32_bf16 v[100:103], v[56:59], a[48:51], v[100:103]\n"
"ds_write_addtid_b32 v123 offset:13984\n"
"v_mfma_f32_16x16x32_bf16 v[100:103], v[60:63], a[52:55], v[100:103]\n"
"v_cvt_pk_bf16_f32 v85, v98, v99\n"
"v_mfma_f32_16x16x32_bf16 v[100:103], v[64:67], a[56:59], v[100:103]\n"
"s_waitcnt lgkmcnt(4)\n"
"v_add_f32 v108, v108, v112\n"
"v_add_f32 v109, v109, v113\n"
"v_mfma_f32_16x16x32_bf16 v[100:103], v[68:71], a[60:63], v[100:103]\n"
"v_add_f32 v110, v110, v114\n"
"v_add_f32 v111, v111, v115\n"
"v_mfma_f32_16x16x32_bf16 v[96:99], v[40:43], a[64:67], 0\n"
"v_add_f32 v108, v108, v116\n"
"v_add_f32 v109, v109, v117\n"
"v_mfma_f32_16x16x32_bf16 v[96:99], v[44:47], a[68:71], v[96:99]\n"
"ds_write_addtid_b32 v100 offset:256\n"
"v_add_f32 v110, v110, v118\n"
"v_mfma_f32_16x16x32_bf16 v[96:99], v[48:51], a[72:75], v[96:99]\n"
"ds_write_addtid_b32 v101 offset:4896\n"
"v_add_f32 v111, v111, v119\n"
"v_mfma_f32_16x16x32_bf16 v[96:99], v[52:55], a[76:79], v[96:99]\n"
"ds_write_addtid_b32 v102 offset:9600\n"
"v_add_f32 v108, v108, v92\n"
"v_mfma_f32_16x16x32_bf16 v[96:99], v[56:59], a[80:83], v[96:99]\n"
"ds_write_addtid_b32 v103 offset:14240\n"
"v_add_f32 v109, v109, v93\n"
"v_mfma_f32_16x16x32_bf16 v[96:99], v[60:63], a[84:87], v[96:99]\n"
"v_add_f32 v110, v110, v94\n"
"v_mfma_f32_16x16x32_bf16 v[96:99], v[64:67], a[88:91], v[96:99]\n"
"s_waitcnt vmcnt(1)\n"
"v_add_f32 v111, v111, v95\n"
"v_cvt_pk_bf16_f32 v86, v108, v109\n"
"v_mfma_f32_16x16x32_bf16 v[96:99], v[68:71], a[92:95], v[96:99]\n"
"v_cvt_pk_bf16_f32 v87, v110, v111\n"
"v_lshlrev_b32 v100, 16, v88\n"
"v_mfma_f32_16x16x32_bf16 v[92:95], v[40:43], a[96:99], 0\n"
"v_and_b32 v101, 0xffff0000, v88\n"
"v_lshlrev_b32 v102, 16, v89\n"
"v_mfma_f32_16x16x32_bf16 v[92:95], v[44:47], a[100:103], v[92:95]\n"
"ds_write_addtid_b32 v96 offset:512\n"
"v_and_b32 v103, 0xffff0000, v89\n"
"v_mfma_f32_16x16x32_bf16 v[92:95], v[48:51], a[104:107], v[92:95]\n"
"ds_write_addtid_b32 v97 offset:5152\n"
"v_lshlrev_b32 v104, 16, v90\n"
"v_mfma_f32_16x16x32_bf16 v[92:95], v[52:55], a[108:111], v[92:95]\n"
"buffer_store_dwordx4 v[84:87], v11, s[40:43], s94 offen sc1\n"
"ds_write_addtid_b32 v98 offset:9856\n"
"v_and_b32 v105, 0xffff0000, v90\n"
"v_lshlrev_b32 v106, 16, v91\n"
"v_mfma_f32_16x16x32_bf16 v[108:111], v[84:87], v[84:87], 0\n"
"v_and_b32 v107, 0xffff0000, v91\n"
"v_mfma_f32_16x16x32_bf16 v[112:115], v[14:17], v[84:87], 0\n"
"v_mfma_f32_16x16x32_bf16 v[92:95], v[56:59], a[112:115], v[92:95]\n"
"ds_write_addtid_b32 v99 offset:14496\n"
"v_cndmask_b32_e64 v108, v108, v109, s[62:63]\n"
"v_mfma_f32_16x16x32_bf16 v[92:95], v[60:63], a[116:119], v[92:95]\n"
"v_cndmask_b32_e64 v110, v110, v111, s[62:63]\n"
"v_cndmask_b32_e64 v113, v108, v110, s[64:65]\n"
"v_mfma_f32_16x16x32_bf16 v[92:95], v[64:67], a[120:123], v[92:95]\n"
"buffer_store_dwordx2 v[112:113], v13, s[40:43], s94 offen offset:0 sc1\n"
"v_mfma_f32_16x16x32_bf16 v[92:95], v[68:71], a[124:127], v[92:95]\n"
"v_mfma_f32_16x16x32_bf16 v[84:87], v[40:43], a[128:131], 0\n"
"v_mfma_f32_16x16x32_bf16 v[84:87], v[44:47], a[132:135], v[84:87]\n"
"ds_write_addtid_b32 v92 offset:768\n"
"v_mfma_f32_16x16x32_bf16 v[84:87], v[48:51], a[136:139], v[84:87]\n"
"ds_write_addtid_b32 v93 offset:5408\n"
"v_mfma_f32_16x16x32_bf16 v[84:87], v[52:55], a[140:143], v[84:87]\n"
"ds_write_addtid_b32 v94 offset:10112\n"
"v_mfma_f32_16x16x32_bf16 v[84:87], v[56:59], a[144:147], v[84:87]\n"
"ds_write_addtid_b32 v95 offset:14752\n"
"v_mfma_f32_16x16x32_bf16 v[84:87], v[60:63], a[148:151], v[84:87]\n"
"v_mfma_f32_16x16x32_bf16 v[84:87], v[64:67], a[152:155], v[84:87]\n"
"v_mfma_f32_16x16x32_bf16 v[84:87], v[68:71], a[156:159], v[84:87]\n"
"v_mfma_f32_16x16x32_bf16 v[92:95], v[40:43], a[160:163], 0\n"
"v_mfma_f32_16x16x32_bf16 v[92:95], v[44:47], a[164:167], v[92:95]\n"
"ds_write_addtid_b32 v84 offset:1024\n"
"v_mfma_f32_16x16x32_bf16 v[92:95], v[48:51], a[168:171], v[92:95]\n"
"ds_write_addtid_b32 v85 offset:5664\n"
"v_mfma_f32_16x16x32_bf16 v[92:95], v[52:55], a[172:175], v[92:95]\n"
"ds_write_addtid_b32 v86 offset:10368\n"
"v_mfma_f32_16x16x32_bf16 v[92:95], v[56:59], a[176:179], v[92:95]\n"
"ds_write_addtid_b32 v87 offset:15008\n"
"v_mfma_f32_16x16x32_bf16 v[92:95], v[60:63], a[180:183], v[92:95]\n"
"v_mfma_f32_16x16x32_bf16 v[92:95], v[64:67], a[184:187], v[92:95]\n"
"v_mfma_f32_16x16x32_bf16 v[92:95], v[68:71], a[188:191], v[92:95]\n"
"v_mfma_f32_16x16x32_bf16 v[100:103], a[192:195], v[40:43], v[100:103]\n"
"v_mfma_f32_16x16x32_bf16 v[100:103], a[196:199], v[44:47], v[100:103]\n"
"ds_write_addtid_b32 v92 offset:1280\n"
"v_mfma_f32_16x16x32_bf16 v[100:103], a[200:203], v[48:51], v[100:103]\n"
"ds_write_addtid_b32 v93 offset:5920\n"
"v_mfma_f32_16x16x32_bf16 v[100:103], a[204:207], v[52:55], v[100:103]\n"
"ds_write_addtid_b32 v94 offset:10624\n"
"v_mfma_f32_16x16x32_bf16 v[100:103], a[208:211], v[56:59], v[100:103]\n"
"ds_write_addtid_b32 v95 offset:15264\n"
"v_mfma_f32_16x16x32_bf16 v[100:103], a[212:215], v[60:63], v[100:103]\n"
"v_mfma_f32_16x16x32_bf16 v[100:103], a[216:219], v[64:67], v[100:103]\n"
"v_mfma_f32_16x16x32_bf16 v[100:103], a[220:223], v[68:71], v[100:103]\n"
"v_mfma_f32_16x16x32_bf16 v[104:107], a[224:227], v[40:43], v[104:107]\n"
"s_waitcnt lgkmcnt(0)\n"
"v_mfma_f32_16x16x32_bf16 v[104:107], a[228:231], v[44:47], v[104:107]\n"
"s_barrier\n"
"v_mfma_f32_16x16x32_bf16 v[104:107], a[232:235], v[48:51], v[104:107]\n"
"ds_read_b128 v[40:43], v2 offset:1536\n"
"v_mfma_f32_16x16x32_bf16 v[104:107], a[236:239], v[52:55], v[104:107]\n"
"ds_read_b128 v[44:47], v3 offset:512\n"
"ds_read_b128 v[48:51], v4 offset:2560\n"
"v_mfma_f32_16x16x32_bf16 v[104:107], a[240:243], v[56:59], v[104:107]\n"
"ds_read_b128 v[108:111], v2 offset:1792\n"
"ds_read_b128 v[112:115], v3 offset:768\n"
"v_mfma_f32_16x16x32_bf16 v[104:107], a[244:247], v[60:63], v[104:107]\n"
"ds_read_b128 v[116:119], v4 offset:2816\n"
"v_mfma_f32_16x16x32_bf16 v[104:107], a[248:251], v[64:67], v[104:107]\n"
"v_mfma_f32_16x16x32_bf16 v[104:107], a[252:255], v[68:71], v[104:107]\n"
"s_waitcnt lgkmcnt(3)\n"
"v_add_f32 v40, v40, v44\n"
"v_add_f32 v41, v41, v45\n"
"v_add_f32 v42, v42, v46\n"
"v_add_f32 v43, v43, v47\n"
"v_add_f32 v40, v40, v48\n"
"v_add_f32 v41, v41, v49\n"
"v_add_f32 v42, v42, v50\n"
"v_add_f32 v43, v43, v51\n"
"v_add_f32 v40, v40, v100\n"
"v_add_f32 v41, v41, v101\n"
"v_add_f32 v42, v42, v102\n"
"v_add_f32 v43, v43, v103\n"
"s_add_u32 s93, s50, 0x0\n"
"s_and_b32 s94, s93, 0xfbffffff\n"
"v_cvt_pk_bf16_f32 v48, v40, v41\n"
"v_cvt_pk_bf16_f32 v49, v42, v43\n"
"s_waitcnt lgkmcnt(0)\n"
"v_add_f32 v108, v108, v112\n"
"v_add_f32 v109, v109, v113\n"
"s_waitcnt vmcnt(0)\n"
"v_add_f32 v110, v110, v114\n"
"buffer_store_dwordx2 v[0:1], v12, s[40:43], s95 offen offset:0 sc1\n"
"v_add_f32 v111, v111, v115\n"
"v_add_f32 v108, v108, v116\n"
"v_add_f32 v109, v109, v117\n"
"v_add_f32 v110, v110, v118\n"
"v_add_f32 v111, v111, v119\n"
"v_add_f32 v108, v108, v104\n"
"v_add_f32 v109, v109, v105\n"
"v_add_f32 v110, v110, v106\n"
"v_add_f32 v111, v111, v107\n"
"v_cvt_pk_bf16_f32 v50, v108, v109\n"
"v_cvt_pk_bf16_f32 v51, v110, v111\n"
"buffer_store_dwordx4 v[48:51], v11, s[40:43], s94 offen sc1\n"
"s_nop 0\n"
"v_mfma_f32_16x16x32_bf16 v[40:43], v[48:51], v[48:51], 0\n"
"v_mfma_f32_16x16x32_bf16 v[96:99], v[14:17], v[48:51], 0\n"
"s_nop 6\n"
"v_cndmask_b32_e64 v40, v40, v41, s[62:63]\n"
"v_cndmask_b32_e64 v42, v42, v43, s[62:63]\n"
"v_cndmask_b32_e64 v97, v40, v42, s[64:65]\n"
"buffer_store_dwordx2 v[96:97], v13, s[40:43], s94 offen offset:0 sc1\n"
"s_waitcnt vmcnt(0)\n"
"buffer_store_dwordx2 v[0:1], v12, s[40:43], s93 offen offset:0 sc1\n"
"s_branch .LO_end\n"
".LO_end:\n"
"s_waitcnt vmcnt(0) lgkmcnt(0)\n"
"s_endpgm\n"
      :: "{v2}"((unsigned)(lds0 + ((wid + 3) & 3) * 18592 + trp)), "{v3}"((unsigned)(lds0 + ((wid + 2) & 3) * 18592 + trp)), "{v4}"((unsigned)(lds0 + ((wid + 1) & 3) * 18592 + trp)), "{v5}"((unsigned)(lane * 16 + wid * 8192 + ((0 + rot) % 4) * 2048 + 134250496)), "{v6}"((unsigned)(lane * 16 + wid * 8192 + ((1 + rot) % 4) * 2048 + 134250496)), "{v7}"((unsigned)(lane * 16 + wid * 8192 + ((2 + rot) % 4) * 2048 + 134250496)), "{v8}"((unsigned)(lane * 16 + wid * 8192 + ((3 + rot) % 4) * 2048 + 134250496)), "{v9}"((unsigned)((wid * 8 + (lane & 7)) * 8 + 352512 + O_GL * 1572864)), "{v10}"((unsigned)(lane * 16 + pair * 1024 + 134217728)), "{v11}"((unsigned)(lane * 16 + pair * 1024 + 65536)), "{v12}"((unsigned)(lane ? DROP : pair * 8 + 352768)), "{v13}"((unsigned)(lg == (li >> 2) ? pair * 128 + li * 8 + 344064 : DROP)), "{v14}"((unsigned)(0x3f803f80)), "{v15}"((unsigned)(0x3f803f80)), "{v16}"((unsigned)(0x3f803f80)), "{v17}"((unsigned)(0x3f803f80)), "{v20}"((unsigned)(vT + tsw)), "{v21}"((unsigned)(vT + (tsw ^ 8))), "{v22}"((unsigned)(vD + ((0 + rot) % 4) * 131072)), "{v23}"((unsigned)(vD + ((1 + rot) % 4) * 131072)), "{v24}"((unsigned)(vD + ((2 + rot) % 4) * 131072)), "{v25}"((unsigned)(vD + ((3 + rot) % 4) * 131072)), "{s[40:43]}"(rS), "{s[44:47]}"(rW), "{s[48:49]}"(wsp), "{s50}"((unsigned)(sA)), "{s51}"((unsigned)(sLo)), "{s52}"((unsigned)(sHi)), "{s53}"((unsigned)(sEx)), "{s54}"((unsigned)(sEnt)), "{s55}"((unsigned)(sLd)), "{s[56:57]}"(wsb + (sLo & 0x3ffffff)), "{s59}"((unsigned)(lds0 + wid * 18592)), "{s[62:63]}"(0xAAAAAAAAAAAAAAAAull), "{s[64:65]}"(0xCCCCCCCCCCCCCCCCull), "{s66}"((unsigned)(sW0 + 64 * ((wid + 1) & 3))), "{s67}"((unsigned)(sW0 + 64 * ((wid + 2) & 3))), "{s68}"((unsigned)(sW0 + 64 * ((wid + 3) & 3))), "{s69}"((unsigned)(sW0 + 64 * ((wid + 4) & 3)))
      : "v255", "a255", "s95", "vcc", "scc", "memory");

  } else if (role == 2) {
    const unsigned sEx = sHi + F_EX * 3 * RS, sEnt = -wid & 3, tsw = 0, pair = idx * 4 + wid, rot = SLP & 4 ? FR0 * (xo >= 0) : KR * (set ? set & 2 : 1);
    const unsigned sW0 = wid * 256 * 7168 + idx * 256, vD = (lane >> 2) * 7168 + (lane & 3) * 16;
    const u32x4 rW = desc(a.w1, 1024 * 7168);
    asm volatile(
"s_nop 4\n"
"v_mov_b32 v186, 0\n"
"global_load_dwordx2 v[0:1], v186, s[48:49] sc1\n"
"global_load_dwordx4 v[30:33], v38, s[64:65]\n"
"v_mov_b32 v188, 0x3f803f80\n"
"v_mov_b32 v189, 0x3f803f80\n"
"v_mov_b32 v190, 0x3f803f80\n"
"v_mov_b32 v191, 0x3f803f80\n"
"s_add_u32 m0, s55, 0\n"
"s_add_u32 s60, s66, 0\n"
"buffer_load_dwordx4 v39, s[44:47], s60 offen lds\n"
"s_add_u32 m0, s55, 1024\n"
"s_add_u32 s60, s66, 114688\n"
"buffer_load_dwordx4 v39, s[44:47], s60 offen lds\n"
"s_add_u32 m0, s55, 2048\n"
"s_add_u32 s60, s67, 0\n"
"buffer_load_dwordx4 v39, s[44:47], s60 offen lds\n"
"s_add_u32 m0, s55, 3072\n"
"s_add_u32 s60, s67, 114688\n"
"buffer_load_dwordx4 v39, s[44:47], s60 offen lds\n"
"s_add_u32 m0, s55, 4096\n"
"s_add_u32 s60, s68, 0\n"
"buffer_load_dwordx4 v39, s[44:47], s60 offen lds\n"
"s_add_u32 m0, s55, 5120\n"
"s_add_u32 s60, s68, 114688\n"
"buffer_load_dwordx4 v39, s[44:47], s60 offen lds\n"
"s_add_u32 m0, s55, 6144\n"
"s_add_u32 s60, s69, 0\n"
"buffer_load_dwordx4 v39, s[44:47], s60 offen lds\n"
"s_add_u32 m0, s55, 7168\n"
"s_add_u32 s60, s69, 114688\n"
"buffer_load_dwordx4 v39, s[44:47], s60 offen lds\n"
"s_add_u32 m0, s55, 8192\n"
"s_add_u32 s60, s66, 229376\n"
"buffer_load_dwordx4 v39, s[44:47], s60 offen lds\n"
"s_add_u32 m0, s55, 9216\n"
"s_add_u32 s60, s66, 344064\n"
"buffer_load_dwordx4 v39, s[44:47], s60 offen lds\n"
"s_add_u32 m0, s55, 10240\n"
"s_add_u32 s60, s67, 229376\n"
"buffer_load_dwordx4 v39, s[44:47], s60 offen lds\n"
"s_add_u32 m0, s55, 11264\n"
"s_add_u32 s60, s67, 344064\n"
"buffer_load_dwordx4 v39, s[44:47], s60 offen lds\n"
"s_add_u32 m0, s55, 12288\n"
"s_add_u32 s60, s68, 229376\n"
"buffer_load_dwordx4 v39, s[44:47], s60 offen lds\n"
"s_add_u32 m0, s55, 13312\n"
"s_add_u32 s60, s68, 344064\n"
"buffer_load_dwordx4 v39, s[44:47], s60 offen lds\n"
"s_add_u32 m0, s55, 14336\n"
"s_add_u32 s60, s69, 229376\n"
"buffer_load_dwordx4 v39, s[44:47], s60 offen lds\n"
"s_add_u32 m0, s55, 15360\n"
"s_add_u32 s60, s69, 344064\n"
"buffer_load_dwordx4 v39, s[44:47], s60 offen lds\n"
"s_add_u32 m0, s55, 16384\n"
"s_add_u32 s60, s66, 0\n"
"buffer_load_dwordx4 v40, s[44:47], s60 offen lds\n"
"s_add_u32 m0, s55, 17408\n"
"s_add_u32 s60, s66, 114688\n"
"buffer_load_dwordx4 v40, s[44:47], s60 offen lds\n"
"s_add_u32 m0, s55, 18432\n"
"s_add_u32 s60, s67, 0\n"
"buffer_load_dwordx4 v40, s[44:47], s60 offen lds\n"
"s_add_u32 m0, s55, 19456\n"
"s_add_u32 s60, s67, 114688\n"
"buffer_load_dwordx4 v40, s[44:47], s60 offen lds\n"
"s_add_u32 m0, s55, 20480\n"
"s_add_u32 s60, s68, 0\n"
"buffer_load_dwordx4 v40, s[44:47], s60 offen lds\n"
"s_add_u32 m0, s55, 21504\n"
"s_add_u32 s60, s68, 114688\n"
"buffer_load_dwordx4 v40, s[44:47], s60 offen lds\n"
"s_add_u32 m0, s55, 22528\n"
"s_add_u32 s60, s69, 0\n"
"buffer_load_dwordx4 v40, s[44:47], s60 offen lds\n"
"s_add_u32 m0, s55, 23552\n"
"s_add_u32 s60, s69, 114688\n"
"buffer_load_dwordx4 v40, s[44:47], s60 offen lds\n"
"s_add_u32 m0, s55, 24576\n"
"s_add_u32 s60, s66, 229376\n"
"buffer_load_dwordx4 v40, s[44:47], s60 offen lds\n"
"s_add_u32 m0, s55, 25600\n"
"s_add_u32 s60, s66, 344064\n"
"buffer_load_dwordx4 v40, s[44:47], s60 offen lds\n"
"s_add_u32 m0, s55, 26624\n"
"s_add_u32 s60, s67, 229376\n"
"buffer_load_dwordx4 v40, s[44:47], s60 offen lds\n"
"s_add_u32 m0, s55, 27648\n"
"s_add_u32 s60, s67, 344064\n"
"buffer_load_dwordx4 v40, s[44:47], s60 offen lds\n"
"s_add_u32 m0, s55, 28672\n"
"s_add_u32 s60, s68, 229376\n"
"buffer_load_dwordx4 v40, s[44:47], s60 offen lds\n"
"s_add_u32 m0, s55, 29696\n"
"s_add_u32 s60, s68, 344064\n"
"buffer_load_dwordx4 v40, s[44:47], s60 offen lds\n"
"s_add_u32 m0, s55, 30720\n"
"s_add_u32 s60, s69, 229376\n"
"buffer_load_dwordx4 v40, s[44:47], s60 offen lds\n"
"s_add_u32 m0, s55, 31744\n"
"s_add_u32 s60, s69, 344064\n"
"buffer_load_dwordx4 v40, s[44:47], s60 offen lds\n"
"s_add_u32 m0, s55, 32768\n"
"s_add_u32 s60, s66, 0\n"
"buffer_load_dwordx4 v41, s[44:47], s60 offen lds\n"
"s_add_u32 m0, s55, 33792\n"
"s_add_u32 s60, s66, 114688\n"
"buffer_load_dwordx4 v41, s[44:47], s60 offen lds\n"
"s_add_u32 m0, s55, 34816\n"
"s_add_u32 s60, s67, 0\n"
"buffer_load_dwordx4 v41, s[44:47], s60 offen lds\n"
"s_add_u32 m0, s55, 35840\n"
"s_add_u32 s60, s67, 114688\n"
"buffer_load_dwordx4 v41, s[44:47], s60 offen lds\n"
"s_add_u32 m0, s55, 36864\n"
"s_add_u32 s60, s68, 0\n"
"buffer_load_dwordx4 v41, s[44:47], s60 offen lds\n"
"s_add_u32 m0, s55, 37888\n"
"s_add_u32 s60, s68, 114688\n"
"buffer_load_dwordx4 v41, s[44:47], s60 offen lds\n"
"s_add_u32 m0, s55, 38912\n"
"s_add_u32 s60, s69, 0\n"
"buffer_load_dwordx4 v41, s[44:47], s60 offen lds\n"
"s_add_u32 m0, s55, 39936\n"
"s_add_u32 s60, s69, 114688\n"
"buffer_load_dwordx4 v41, s[44:47], s60 offen lds\n"
"s_waitcnt vmcnt(38)\n"
"ds_read_b64_tr_b16 a[0:1], v36 offset:0\n"
"ds_read_b64_tr_b16 a[2:3], v36 offset:256\n"
"ds_read_b64_tr_b16 a[32:33], v37 offset:0\n"
"ds_read_b64_tr_b16 a[34:35], v37 offset:256\n"
"v_add_u32 v0, 2, v0\n"
"v_add_u32 v1, 0x9e3779b9, v1\n"
"v_or_b32 v0, 1, v0\n"
"v_readfirstlane_b32 s49, v1\n"
"v_readfirstlane_b32 s48, v0\n"
"s_waitcnt vmcnt(36)\n"
"ds_read_b64_tr_b16 a[64:65], v36 offset:2048\n"
"ds_read_b64_tr_b16 a[66:67], v36 offset:2304\n"
"ds_read_b64_tr_b16 a[96:97], v37 offset:2048\n"
"ds_read_b64_tr_b16 a[98:99], v37 offset:2304\n"
"s_waitcnt lgkmcnt(4)\n"
"s_add_u32 m0, s55, 0\n"
"s_add_u32 s60, s66, 229376\n"
"buffer_load_dwordx4 v41, s[44:47], s60 offen lds\n"
"s_add_u32 m0, s55, 1024\n"
"s_add_u32 s60, s66, 344064\n"
"buffer_load_dwordx4 v41, s[44:47], s60 offen lds\n"
"v_mfma_f32_16x16x32_bf16 v[192:195], v[188:191], a[0:3], 0\n"
"v_mfma_f32_16x16x32_bf16 v[196:199], v[188:191], a[32:35], 0\n"
"s_waitcnt vmcnt(36)\n"
"ds_read_b64_tr_b16 a[128:129], v36 offset:4096\n"
"ds_read_b64_tr_b16 a[130:131], v36 offset:4352\n"
"ds_read_b64_tr_b16 a[160:161], v37 offset:4096\n"
"ds_read_b64_tr_b16 a[162:163], v37 offset:4352\n"
"s_waitcnt lgkmcnt(4)\n"
"s_add_u32 m0, s55, 2048\n"
"s_add_u32 s60, s67, 229376\n"
"buffer_load_dwordx4 v41, s[44:47], s60 offen lds\n"
"s_add_u32 m0, s55, 3072\n"
"s_add_u32 s60, s67, 344064\n"
"buffer_load_dwordx4 v41, s[44:47], s60 offen lds\n"
"v_mfma_f32_16x16x32_bf16 v[200:203], v[188:191], a[64:67], 0\n"
"v_mfma_f32_16x16x32_bf16 v[204:207], v[188:191], a[96:99], 0\n"
"s_waitcnt vmcnt(36)\n"
"ds_read_b64_tr_b16 a[192:193], v36 offset:6144\n"
"ds_read_b64_tr_b16 a[194:195], v36 offset:6400\n"
"ds_read_b64_tr_b16 a[224:225], v37 offset:6144\n"
"ds_read_b64_tr_b16 a[226:227], v37 offset:6400\n"
"s_waitcnt lgkmcnt(4)\n"
"s_add_u32 m0, s55, 4096\n"
"s_add_u32 s60, s68, 229376\n"
"buffer_load_dwordx4 v41, s[44:47], s60 offen lds\n"
"s_add_u32 m0, s55, 5120\n"
"s_add_u32 s60, s68, 344064\n"
"buffer_load_dwordx4 v41, s[44:47], s60 offen lds\n"
"v_mfma_f32_16x16x32_bf16 v[208:211], v[188:191], a[128:131], 0\n"
"v_mfma_f32_16x16x32_bf16 v[212:215], v[188:191], a[160:163], 0\n"
"s_waitcnt vmcnt(36)\n"
"ds_read_b64_tr_b16 a[4:5], v36 offset:8192\n"
"ds_read_b64_tr_b16 a[6:7], v36 offset:8448\n"
"ds_read_b64_tr_b16 a[36:37], v37 offset:8192\n"
"ds_read_b64_tr_b16 a[38:39], v37 offset:8448\n"
"s_waitcnt lgkmcnt(4)\n"
"s_add_u32 m0, s55, 6144\n"
"s_add_u32 s60, s69, 229376\n"
"buffer_load_dwordx4 v41, s[44:47], s60 offen lds\n"
"s_add_u32 m0, s55, 7168\n"
"s_add_u32 s60, s69, 344064\n"
"buffer_load_dwordx4 v41, s[44:47], s60 offen lds\n"
"v_mfma_f32_16x16x32_bf16 v[216:219], v[188:191], a[192:195], 0\n"
"v_mfma_f32_16x16x32_bf16 v[220:223], v[188:191], a[224:227], 0\n"
"s_waitcnt vmcnt(36)\n"
"ds_read_b64_tr_b16 a[68:69], v36 offset:10240\n"
"ds_read_b64_tr_b16 a[70:71], v36 offset:10496\n"
"ds_read_b64_tr_b16 a[100:101], v37 offset:10240\n"
"ds_read_b64_tr_b16 a[102:103], v37 offset:10496\n"
"s_waitcnt lgkmcnt(4)\n"
"s_add_u32 m0, s55, 8192\n"
"s_add_u32 s60, s66, 0\n"
"buffer_load_dwordx4 v42, s[44:47], s60 offen lds\n"
"s_add_u32 m0, s55, 9216\n"
"s_add_u32 s60, s66, 114688\n"
"buffer_load_dwordx4 v42, s[44:47], s60 offen lds\n"
"v_mfma_f32_16x16x32_bf16 v[192:195], v[188:191], a[4:7], v[192:195]\n"
"v_mfma_f32_16x16x32_bf16 v[196:199], v[188:191], a[36:39], v[196:199]\n"
"s_waitcnt vmcnt(36)\n"
"ds_read_b64_tr_b16 a[132:133], v36 offset:12288\n"
"ds_read_b64_tr_b16 a[134:135], v36 offset:12544\n"
"ds_read_b64_tr_b16 a[164:165], v37 offset:12288\n"
"ds_read_b64_tr_b16 a[166:167], v37 offset:12544\n"
"s_waitcnt lgkmcnt(4)\n"
"s_add_u32 m0, s55, 10240\n"
"s_add_u32 s60, s67, 0\n"
"buffer_load_dwordx4 v42, s[44:47], s60 offen lds\n"
"s_add_u32 m0, s55, 11264\n"
"s_add_u32 s60, s67, 114688\n"
"buffer_load_dwordx4 v42, s[44:47], s60 offen lds\n"
"v_mfma_f32_16x16x32_bf16 v[200:203], v[188:191], a[68:71], v[200:203]\n"
"v_mfma_f32_16x16x32_bf16 v[204:207], v[188:191], a[100:103], v[204:207]\n"
"s_waitcnt vmcnt(36)\n"
"ds_read_b64_tr_b16 a[196:197], v36 offset:14336\n"
"ds_read_b64_tr_b16 a[198:199], v36 offset:14592\n"
"ds_read_b64_tr_b16 a[228:229], v37 offset:14336\n"
"ds_read_b64_tr_b16 a[230:231], v37 offset:14592\n"
"s_waitcnt lgkmcnt(4)\n"
"s_add_u32 m0, s55, 12288\n"
"s_add_u32 s60, s68, 0\n"
"buffer_load_dwordx4 v42, s[44:47], s60 offen lds\n"
"s_add_u32 m0, s55, 13312\n"
"s_add_u32 s60, s68, 114688\n"
"buffer_load_dwordx4 v42, s[44:47], s60 offen lds\n"
"v_mfma_f32_16x16x32_bf16 v[208:211], v[188:191], a[132:135], v[208:211]\n"
"v_mfma_f32_16x16x32_bf16 v[212:215], v[188:191], a[164:167], v[212:215]\n"
"s_waitcnt vmcnt(36)\n"
"ds_read_b64_tr_b16 a[8:9], v36 offset:16384\n"
"ds_read_b64_tr_b16 a[10:11], v36 offset:16640\n"
"ds_read_b64_tr_b16 a[40:41], v37 offset:16384\n"
"ds_read_b64_tr_b16 a[42:43], v37 offset:16640\n"
"s_waitcnt lgkmcnt(4)\n"
"s_add_u32 m0, s55, 14336\n"
"s_add_u32 s60, s69, 0\n"
"buffer_load_dwordx4 v42, s[44:47], s60 offen lds\n"
"s_add_u32 m0, s55, 15360\n"
"s_add_u32 s60, s69, 114688\n"
"buffer_load_dwordx4 v42, s[44:47], s60 offen lds\n"
"v_mfma_f32_16x16x32_bf16 v[216:219], v[188:191], a[196:199], v[216:219]\n"
"v_mfma_f32_16x16x32_bf16 v[220:223], v[188:191], a[228:231], v[220:223]\n"
"s_waitcnt vmcnt(36)\n"
"ds_read_b64_tr_b16 a[72:73], v36 offset:18432\n"
"ds_read_b64_tr_b16 a[74:75], v36 offset:18688\n"
"ds_read_b64_tr_b16 a[104:105], v37 offset:18432\n"
"ds_read_b64_tr_b16 a[106:107], v37 offset:18688\n"
"s_waitcnt lgkmcnt(4)\n"
"s_add_u32 m0, s55, 16384\n"
"s_add_u32 s60, s66, 229376\n"
"buffer_load_dwordx4 v42, s[44:47], s60 offen lds\n"
"s_add_u32 m0, s55, 17408\n"
"s_add_u32 s60, s66, 344064\n"
"buffer_load_dwordx4 v42, s[44:47], s60 offen lds\n"
"v_mfma_f32_16x16x32_bf16 v[192:195], v[188:191], a[8:11], v[192:195]\n"
"v_mfma_f32_16x16x32_bf16 v[196:199], v[188:191], a[40:43], v[196:199]\n"
"s_waitcnt vmcnt(36)\n"
"ds_read_b64_tr_b16 a[136:137], v36 offset:20480\n"
"ds_read_b64_tr_b16 a[138:139], v36 offset:20736\n"
"ds_read_b64_tr_b16 a[168:169], v37 offset:20480\n"
"ds_read_b64_tr_b16 a[170:171], v37 offset:20736\n"
"s_waitcnt lgkmcnt(4)\n"
"s_add_u32 m0, s55, 18432\n"
"s_add_u32 s60, s67, 229376\n"
"buffer_load_dwordx4 v42, s[44:47], s60 offen lds\n"
"s_add_u32 m0, s55, 19456\n"
"s_add_u32 s60, s67, 344064\n"
"buffer_load_dwordx4 v42, s[44:47], s60 offen lds\n"
"v_mfma_f32_16x16x32_bf16 v[200:203], v[188:191], a[72:75], v[200:203]\n"
"v_mfma_f32_16x16x32_bf16 v[204:207], v[188:191], a[104:107], v[204:207]\n"
"s_waitcnt vmcnt(36)\n"
"ds_read_b64_tr_b16 a[200:201], v36 offset:22528\n"
"ds_read_b64_tr_b16 a[202:203], v36 offset:22784\n"
"ds_read_b64_tr_b16 a[232:233], v37 offset:22528\n"
"ds_read_b64_tr_b16 a[234:235], v37 offset:22784\n"
"s_waitcnt lgkmcnt(4)\n"
"s_add_u32 m0, s55, 20480\n"
"s_add_u32 s60, s68, 229376\n"
"buffer_load_dwordx4 v42, s[44:47], s60 offen lds\n"
"s_add_u32 m0, s55, 21504\n"
"s_add_u32 s60, s68, 344064\n"
"buffer_load_dwordx4 v42, s[44:47], s60 offen lds\n"
"v_mfma_f32_16x16x32_bf16 v[208:211], v[188:191], a[136:139], v[208:211]\n"
"v_mfma_f32_16x16x32_bf16 v[212:215], v[188:191], a[168:171], v[212:215]\n"
"s_waitcnt vmcnt(36)\n"
"ds_read_b64_tr_b16 a[12:13], v36 offset:24576\n"
"ds_read_b64_tr_b16 a[14:15], v36 offset:24832\n"
"ds_read_b64_tr_b16 a[44:45], v37 offset:24576\n"
"ds_read_b64_tr_b16 a[46:47], v37 offset:24832\n"
"s_waitcnt lgkmcnt(4)\n"
"s_add_u32 m0, s55, 22528\n"
"s_add_u32 s60, s69, 229376\n"
"buffer_load_dwordx4 v42, s[44:47], s60 offen lds\n"
"s_add_u32 m0, s55, 23552\n"
"s_add_u32 s60, s69, 344064\n"
"buffer_load_dwordx4 v42, s[44:47], s60 offen lds\n"
"v_mfma_f32_16x16x32_bf16 v[216:219], v[188:191], a[200:203], v[216:219]\n"
"v_mfma_f32_16x16x32_bf16 v[220:223], v[188:191], a[232:235], v[220:223]\n"
"s_waitcnt vmcnt(36)\n"
"ds_read_b64_tr_b16 a[76:77], v36 offset:26624\n"
"ds_read_b64_tr_b16 a[78:79], v36 offset:26880\n"
"ds_read_b64_tr_b16 a[108:109], v37 offset:26624\n"
"ds_read_b64_tr_b16 a[110:111], v37 offset:26880\n"
"s_waitcnt lgkmcnt(4)\n"
"v_mfma_f32_16x16x32_bf16 v[192:195], v[188:191], a[12:15], v[192:195]\n"
"v_mfma_f32_16x16x32_bf16 v[196:199], v[188:191], a[44:47], v[196:199]\n"
"s_waitcnt vmcnt(34)\n"
"ds_read_b64_tr_b16 a[140:141], v36 offset:28672\n"
"ds_read_b64_tr_b16 a[142:143], v36 offset:28928\n"
"ds_read_b64_tr_b16 a[172:173], v37 offset:28672\n"
"ds_read_b64_tr_b16 a[174:175], v37 offset:28928\n"
"s_waitcnt lgkmcnt(4)\n"
"v_mfma_f32_16x16x32_bf16 v[200:203], v[188:191], a[76:79], v[200:203]\n"
"v_mfma_f32_16x16x32_bf16 v[204:207], v[188:191], a[108:111], v[204:207]\n"
"s_waitcnt vmcnt(32)\n"
"ds_read_b64_tr_b16 a[204:205], v36 offset:30720\n"
"ds_read_b64_tr_b16 a[206:207], v36 offset:30976\n"
"ds_read_b64_tr_b16 a[236:237], v37 offset:30720\n"
"ds_read_b64_tr_b16 a[238:239], v37 offset:30976\n"
"s_waitcnt lgkmcnt(4)\n"
"v_mfma_f32_16x16x32_bf16 v[208:211], v[188:191], a[140:143], v[208:211]\n"
"v_mfma_f32_16x16x32_bf16 v[212:215], v[188:191], a[172:175], v[212:215]\n"
"s_waitcnt vmcnt(30)\n"
"ds_read_b64_tr_b16 a[16:17], v36 offset:32768\n"
"ds_read_b64_tr_b16 a[18:19], v36 offset:33024\n"
"ds_read_b64_tr_b16 a[48:49], v37 offset:32768\n"
"ds_read_b64_tr_b16 a[50:51], v37 offset:33024\n"
"s_waitcnt lgkmcnt(4)\n"
"v_mfma_f32_16x16x32_bf16 v[216:219], v[188:191], a[204:207], v[216:219]\n"
"v_mfma_f32_16x16x32_bf16 v[220:223], v[188:191], a[236:239], v[220:223]\n"
"s_waitcnt vmcnt(28)\n"
"ds_read_b64_tr_b16 a[80:81], v36 offset:34816\n"
"ds_read_b64_tr_b16 a[82:83], v36 offset:35072\n"
"ds_read_b64_tr_b16 a[112:113], v37 offset:34816\n"
"ds_read_b64_tr_b16 a[114:115], v37 offset:35072\n"
"s_waitcnt lgkmcnt(4)\n"
"v_mfma_f32_16x16x32_bf16 v[192:195], v[188:191], a[16:19], v[192:195]\n"
"v_mfma_f32_16x16x32_bf16 v[196:199], v[188:191], a[48:51], v[196:199]\n"
"s_waitcnt vmcnt(26)\n"
"ds_read_b64_tr_b16 a[144:145], v36 offset:36864\n"
"ds_read_b64_tr_b16 a[146:147], v36 offset:37120\n"
"ds_read_b64_tr_b16 a[176:177], v37 offset:36864\n"
"ds_read_b64_tr_b16 a[178:179], v37 offset:37120\n"
"s_waitcnt lgkmcnt(4)\n"
"v_mfma_f32_16x16x32_bf16 v[200:203], v[188:191], a[80:83], v[200:203]\n"
"v_mfma_f32_16x16x32_bf16 v[204:207], v[188:191], a[112:115], v[204:207]\n"
"s_waitcnt vmcnt(24)\n"
"ds_read_b64_tr_b16 a[208:209], v36 offset:38912\n"
"ds_read_b64_tr_b16 a[210:211], v36 offset:39168\n"
"ds_read_b64_tr_b16 a[240:241], v37 offset:38912\n"
"ds_read_b64_tr_b16 a[242:243], v37 offset:39168\n"
"s_waitcnt lgkmcnt(4)\n"
"v_mfma_f32_16x16x32_bf16 v[208:211], v[188:191], a[144:147], v[208:211]\n"
"v_mfma_f32_16x16x32_bf16 v[212:215], v[188:191], a[176:179], v[212:215]\n"
"s_waitcnt vmcnt(22)\n"
"ds_read_b64_tr_b16 a[20:21], v36 offset:0\n"
"ds_read_b64_tr_b16 a[22:23], v36 offset:256\n"
"ds_read_b64_tr_b16 a[52:53], v37 offset:0\n"
"ds_read_b64_tr_b16 a[54:55], v37 offset:256\n"
"s_waitcnt lgkmcnt(4)\n"
"v_mfma_f32_16x16x32_bf16 v[216:219], v[188:191], a[208:211], v[216:219]\n"
"v_mfma_f32_16x16x32_bf16 v[220:223], v[188:191], a[240:243], v[220:223]\n"
"s_waitcnt vmcnt(20)\n"
"ds_read_b64_tr_b16 a[84:85], v36 offset:2048\n"
"ds_read_b64_tr_b16 a[86:87], v36 offset:2304\n"
"ds_read_b64_tr_b16 a[116:117], v37 offset:2048\n"
"ds_read_b64_tr_b16 a[118:119], v37 offset:2304\n"
"s_waitcnt lgkmcnt(4)\n"
"v_mfma_f32_16x16x32_bf16 v[192:195], v[188:191], a[20:23], v[192:195]\n"
"v_mfma_f32_16x16x32_bf16 v[196:199], v[188:191], a[52:55], v[196:199]\n"
"s_waitcnt vmcnt(18)\n"
"ds_read_b64_tr_b16 a[148:149], v36 offset:4096\n"
"ds_read_b64_tr_b16 a[150:151], v36 offset:4352\n"
"ds_read_b64_tr_b16 a[180:181], v37 offset:4096\n"
"ds_read_b64_tr_b16 a[182:183], v37 offset:4352\n"
"s_waitcnt lgkmcnt(4)\n"
"v_mfma_f32_16x16x32_bf16 v[200:203], v[188:191], a[84:87], v[200:203]\n"
"v_mfma_f32_16x16x32_bf16 v[204:207], v[188:191], a[116:119], v[204:207]\n"
"s_waitcnt vmcnt(16)\n"
"ds_read_b64_tr_b16 a[212:213], v36 offset:6144\n"
"ds_read_b64_tr_b16 a[214:215], v36 offset:6400\n"
"ds_read_b64_tr_b16 a[244:245], v37 offset:6144\n"
"ds_read_b64_tr_b16 a[246:247], v37 offset:6400\n"
"s_waitcnt lgkmcnt(4)\n"
"v_mfma_f32_16x16x32_bf16 v[208:211], v[188:191], a[148:151], v[208:211]\n"
"v_mfma_f32_16x16x32_bf16 v[212:215], v[188:191], a[180:183], v[212:215]\n"
"s_waitcnt vmcnt(14)\n"
"ds_read_b64_tr_b16 a[24:25], v36 offset:8192\n"
"ds_read_b64_tr_b16 a[26:27], v36 offset:8448\n"
"ds_read_b64_tr_b16 a[56:57], v37 offset:8192\n"
"ds_read_b64_tr_b16 a[58:59], v37 offset:8448\n"
"s_waitcnt lgkmcnt(4)\n"
"v_mfma_f32_16x16x32_bf16 v[216:219], v[188:191], a[212:215], v[216:219]\n"
"v_mfma_f32_16x16x32_bf16 v[220:223], v[188:191], a[244:247], v[220:223]\n"
"s_waitcnt vmcnt(12)\n"
"ds_read_b64_tr_b16 a[88:89], v36 offset:10240\n"
"ds_read_b64_tr_b16 a[90:91], v36 offset:10496\n"
"ds_read_b64_tr_b16 a[120:121], v37 offset:10240\n"
"ds_read_b64_tr_b16 a[122:123], v37 offset:10496\n"
"s_waitcnt lgkmcnt(4)\n"
"v_mfma_f32_16x16x32_bf16 v[192:195], v[188:191], a[24:27], v[192:195]\n"
"v_mfma_f32_16x16x32_bf16 v[196:199], v[188:191], a[56:59], v[196:199]\n"
"s_waitcnt vmcnt(10)\n"
"ds_read_b64_tr_b16 a[152:153], v36 offset:12288\n"
"ds_read_b64_tr_b16 a[154:155], v36 offset:12544\n"
"ds_read_b64_tr_b16 a[184:185], v37 offset:12288\n"
"ds_read_b64_tr_b16 a[186:187], v37 offset:12544\n"
"s_waitcnt lgkmcnt(4)\n"
"v_mfma_f32_16x16x32_bf16 v[200:203], v[188:191], a[88:91], v[200:203]\n"
"v_mfma_f32_16x16x32_bf16 v[204:207], v[188:191], a[120:123], v[204:207]\n"
"s_waitcnt vmcnt(8)\n"
"ds_read_b64_tr_b16 a[216:217], v36 offset:14336\n"
"ds_read_b64_tr_b16 a[218:219], v36 offset:14592\n"
"ds_read_b64_tr_b16 a[248:249], v37 offset:14336\n"
"ds_read_b64_tr_b16 a[250:251], v37 offset:14592\n"
"s_waitcnt lgkmcnt(4)\n"
"v_mfma_f32_16x16x32_bf16 v[208:211], v[188:191], a[152:155], v[208:211]\n"
"v_mfma_f32_16x16x32_bf16 v[212:215], v[188:191], a[184:187], v[212:215]\n"
"s_waitcnt vmcnt(6)\n"
"ds_read_b64_tr_b16 a[28:29], v36 offset:16384\n"
"ds_read_b64_tr_b16 a[30:31], v36 offset:16640\n"
"ds_read_b64_tr_b16 a[60:61], v37 offset:16384\n"
"ds_read_b64_tr_b16 a[62:63], v37 offset:16640\n"
"s_waitcnt lgkmcnt(4)\n"
"v_mfma_f32_16x16x32_bf16 v[216:219], v[188:191], a[216:219], v[216:219]\n"
"v_mfma_f32_16x16x32_bf16 v[220:223], v[188:191], a[248:251], v[220:223]\n"
"s_waitcnt vmcnt(4)\n"
"ds_read_b64_tr_b16 a[92:93], v36 offset:18432\n"
"ds_read_b64_tr_b16 a[94:95], v36 offset:18688\n"
"ds_read_b64_tr_b16 a[124:125], v37 offset:18432\n"
"ds_read_b64_tr_b16 a[126:127], v37 offset:18688\n"
"s_waitcnt lgkmcnt(4)\n"
"v_mfma_f32_16x16x32_bf16 v[192:195], v[188:191], a[28:31], v[192:195]\n"
"v_mfma_f32_16x16x32_bf16 v[196:199], v[188:191], a[60:63], v[196:199]\n"
"s_waitcnt vmcnt(2)\n"
"ds_read_b64_tr_b16 a[156:157], v36 offset:20480\n"
"ds_read_b64_tr_b16 a[158:159], v36 offset:20736\n"
"ds_read_b64_tr_b16 a[188:189], v37 offset:20480\n"
"ds_read_b64_tr_b16 a[190:191], v37 offset:20736\n"
"s_waitcnt lgkmcnt(4)\n"
"v_mfma_f32_16x16x32_bf16 v[200:203], v[188:191], a[92:95], v[200:203]\n"
"v_mfma_f32_16x16x32_bf16 v[204:207], v[188:191], a[124:127], v[204:207]\n"
"s_waitcnt vmcnt(0)\n"
"ds_read_b64_tr_b16 a[220:221], v36 offset:22528\n"
"ds_read_b64_tr_b16 a[222:223], v36 offset:22784\n"
"ds_read_b64_tr_b16 a[252:253], v37 offset:22528\n"
"ds_read_b64_tr_b16 a[254:255], v37 offset:22784\n"
"s_waitcnt lgkmcnt(4)\n"
"v_mfma_f32_16x16x32_bf16 v[208:211], v[188:191], a[156:159], v[208:211]\n"
"v_mfma_f32_16x16x32_bf16 v[212:215], v[188:191], a[188:191], v[212:215]\n"
"s_waitcnt lgkmcnt(0)\n"
"v_mfma_f32_16x16x32_bf16 v[216:219], v[188:191], a[220:223], v[216:219]\n"
"v_mfma_f32_16x16x32_bf16 v[220:223], v[188:191], a[252:255], v[220:223]\n"
"s_mov_b32 m0, s59\n"
"s_barrier\n"
"ds_write_addtid_b32 v192 offset:0\n"
"ds_write_addtid_b32 v193 offset:4640\n"
"ds_write_addtid_b32 v194 offset:9344\n"
"ds_write_addtid_b32 v195 offset:13984\n"
"ds_write_addtid_b32 v196 offset:256\n"
"ds_write_addtid_b32 v197 offset:4896\n"
"ds_write_addtid_b32 v198 offset:9600\n"
"ds_write_addtid_b32 v199 offset:14240\n"
"ds_write_addtid_b32 v200 offset:512\n"
"ds_write_addtid_b32 v201 offset:5152\n"
"ds_write_addtid_b32 v202 offset:9856\n"
"ds_write_addtid_b32 v203 offset:14496\n"
"ds_write_addtid_b32 v204 offset:768\n"
"ds_write_addtid_b32 v205 offset:5408\n"
"ds_write_addtid_b32 v206 offset:10112\n"
"ds_write_addtid_b32 v207 offset:14752\n"
"ds_write_addtid_b32 v208 offset:1024\n"
"ds_write_addtid_b32 v209 offset:5664\n"
"ds_write_addtid_b32 v210 offset:10368\n"
"ds_write_addtid_b32 v211 offset:15008\n"
"ds_write_addtid_b32 v212 offset:1280\n"
"ds_write_addtid_b32 v213 offset:5920\n"
"ds_write_addtid_b32 v214 offset:10624\n"
"ds_write_addtid_b32 v215 offset:15264\n"
"ds_write_addtid_b32 v216 offset:1536\n"
"ds_write_addtid_b32 v217 offset:6176\n"
"ds_write_addtid_b32 v218 offset:10880\n"
"ds_write_addtid_b32 v219 offset:15520\n"
"ds_write_addtid_b32 v220 offset:1792\n"
"ds_write_addtid_b32 v221 offset:6432\n"
"ds_write_addtid_b32 v222 offset:11136\n"
"ds_write_addtid_b32 v223 offset:15776\n"
"s_waitcnt lgkmcnt(0)\n"
"s_barrier\n"
"ds_read_b128 v[96:99], v2 offset:0\n"
"ds_read_b128 v[100:103], v3 offset:512\n"
"ds_read_b128 v[104:107], v4 offset:1024\n"
"ds_read_b128 v[108:111], v43 offset:1536\n"
"ds_read_b128 v[112:115], v2 offset:256\n"
"ds_read_b128 v[116:119], v3 offset:768\n"
"ds_read_b128 v[120:123], v4 offset:1280\n"
"ds_read_b128 v[124:127], v43 offset:1792\n"
"s_waitcnt lgkmcnt(0)\n"
"s_barrier\n"
"v_add_f32 v96, v96, v100\n"
"v_add_f32 v104, v104, v108\n"
"v_add_f32 v18, v96, v104\n"
"v_add_f32 v97, v97, v101\n"
"v_add_f32 v105, v105, v109\n"
"v_add_f32 v19, v97, v105\n"
"v_add_f32 v98, v98, v102\n"
"v_add_f32 v106, v106, v110\n"
"v_add_f32 v20, v98, v106\n"
"v_add_f32 v99, v99, v103\n"
"v_add_f32 v107, v107, v111\n"
"v_add_f32 v21, v99, v107\n"
"v_add_f32 v112, v112, v116\n"
"v_add_f32 v120, v120, v124\n"
"v_add_f32 v22, v112, v120\n"
"v_add_f32 v113, v113, v117\n"
"v_add_f32 v121, v121, v125\n"
"v_add_f32 v23, v113, v121\n"
"v_add_f32 v114, v114, v118\n"
"v_add_f32 v122, v122, v126\n"
"v_add_f32 v24, v114, v122\n"
"v_add_f32 v115, v115, v119\n"
"v_add_f32 v123, v123, v127\n"
"v_add_f32 v25, v115, v123\n"
"v_lshlrev_b32 v26, 16, v30\n"
"v_and_b32 v27, 0xffff0000, v30\n"
"v_lshlrev_b32 v28, 16, v31\n"
"v_and_b32 v29, 0xffff0000, v31\n"
"v_lshlrev_b32 v30, 16, v32\n"
"v_and_b32 v31, 0xffff0000, v32\n"
"v_lshlrev_b32 v32, 16, v33\n"
"v_and_b32 v33, 0xffff0000, v33\n"
"s_mov_b32 s93, 0x80000000\n"
"s_mov_b32 s94, 0x80000000\n"
"s_mov_b32 s95, 0x80000000\n"
"s_mov_b32 s94, 0x80000000\n"
"s_mov_b32 s93, 0x80000000\n"
"s_mov_b32 s94, 0x80000000\n"
"s_mov_b32 s95, 0x80000000\n"
"s_mov_b32 s94, 0x80000000\n"
"v_mov_b32 v134, 0\n"
"v_mov_b32 v135, 0\n"
"v_mov_b32 v154, 0\n"
"v_mov_b32 v155, 0\n"
"v_mov_b32 v144, 0\n"
"v_mov_b32 v145, 0\n"
"v_mov_b32 v138, 0\n"
"v_mov_b32 v139, 0\n"
"s_cmp_eq_u32 s54, 0\n"
"s_cbranch_scc1 .LF_1s\n"
"s_cmp_eq_u32 s54, 1\n"
"s_cbranch_scc1 .LF_2s\n"
"s_cmp_eq_u32 s54, 2\n"
"s_cbranch_scc1 .LF_3s\n"
"s_cmp_eq_u32 s54, 3\n"
"s_cbranch_scc1 .LF_4s\n"
".p2align 6\n"
".LF_top:\n"
"v_mfma_f32_16x16x32_bf16 v[48:51], v[100:103], a[0:3], 0\n"
"global_load_dwordx4 v[36:39], v6, s[56:57] offset:1024 sc1\n"
"v_mul_f32 v125, v125, v124\n"
"v_mfma_f32_16x16x32_bf16 v[48:51], v[104:107], a[4:7], v[48:51]\n"
"s_waitcnt vmcnt(10)\n"
"ds_write_addtid_b32 v124 offset:18592\n"
"ds_write_addtid_b32 v125 offset:18848\n"
"v_mfma_f32_16x16x32_bf16 v[48:51], v[108:111], a[8:11], v[48:51]\n"
"v_cvt_pk_bf16_f32 v139, v142, v143\n"
"v_pk_max_i16 v138, v138, 0\n"
"v_mfma_f32_16x16x32_bf16 v[48:51], v[64:67], a[12:15], v[48:51]\n"
"s_waitcnt vmcnt(8)\n"
"ds_read2st64_b32 v[140:141], v14 offset0:0 offset1:1\n"
"v_pk_max_i16 v139, v139, 0\n"
"v_mfma_f32_16x16x32_bf16 v[48:51], v[68:71], a[16:19], v[48:51]\n"
"ds_read_b128 v[56:59], v2 offset:0\n"
"ds_read_b128 v[52:55], v3 offset:2048\n"
"v_mfma_f32_16x16x32_bf16 v[48:51], v[72:75], a[20:23], v[48:51]\n"
"s_waitcnt vmcnt(6)\n"
"global_load_dwordx4 v[40:43], v7, s[56:57] offset:0 sc1\n"
"ds_read_b128 v[60:63], v4 offset:1024\n"
"v_mfma_f32_16x16x32_bf16 v[48:51], v[76:79], a[24:27], v[48:51]\n"
"ds_read_b128 v[84:87], v2 offset:256\n"
"ds_read_b128 v[88:91], v3 offset:2304\n"
"v_mfma_f32_16x16x32_bf16 v[48:51], v[80:83], a[28:31], v[48:51]\n"
"ds_read_b128 v[92:95], v4 offset:1280\n"
"v_mfma_f32_16x16x32_bf16 v[96:99], v[100:103], a[32:35], 0\n"
"v_mfma_f32_16x16x32_bf16 v[96:99], v[104:107], a[36:39], v[96:99]\n"
"ds_write_addtid_b32 v48 offset:0\n"
"v_mfma_f32_16x16x32_bf16 v[96:99], v[108:111], a[40:43], v[96:99]\n"
"ds_write_addtid_b32 v49 offset:4640\n"
"global_load_dwordx4 v[44:47], v7, s[56:57] offset:1024 sc1\n"
"v_mfma_f32_16x16x32_bf16 v[96:99], v[64:67], a[44:47], v[96:99]\n"
"s_waitcnt lgkmcnt(5)\n"
"buffer_store_dwordx4 v[136:139], v15, s[40:43], s94 offen sc1\n"
"ds_write_addtid_b32 v50 offset:9344\n"
"v_add_f32 v56, v56, v52\n"
"v_add_f32 v57, v57, v53\n"
"v_mfma_f32_16x16x32_bf16 v[96:99], v[68:71], a[48:51], v[96:99]\n"
"ds_write_addtid_b32 v51 offset:13984\n"
"v_add_f32 v58, v58, v54\n"
"v_mfma_f32_16x16x32_bf16 v[96:99], v[72:75], a[52:55], v[96:99]\n"
"v_add_f32 v59, v59, v55\n"
"v_add_f32 v56, v56, v60\n"
"v_mfma_f32_16x16x32_bf16 v[96:99], v[76:79], a[56:59], v[96:99]\n"
"v_add_f32 v57, v57, v61\n"
"v_add_f32 v58, v58, v62\n"
"v_mfma_f32_16x16x32_bf16 v[96:99], v[80:83], a[60:63], v[96:99]\n"
"v_add_f32 v59, v59, v63\n"
"v_add_f32 v56, v56, v128\n"
"v_mfma_f32_16x16x32_bf16 v[124:127], v[100:103], a[64:67], 0\n"
"global_load_dwordx4 v[48:51], v8, s[56:57] offset:0 sc1\n"
"v_add_f32 v57, v57, v129\n"
"v_mfma_f32_16x16x32_bf16 v[124:127], v[104:107], a[68:71], v[124:127]\n"
"ds_write_addtid_b32 v96 offset:256\n"
"v_add_f32 v58, v58, v130\n"
"v_mfma_f32_16x16x32_bf16 v[124:127], v[108:111], a[72:75], v[124:127]\n"
"ds_write_addtid_b32 v97 offset:4896\n"
"v_add_f32 v59, v59, v131\n"
"v_mfma_f32_16x16x32_bf16 v[124:127], v[64:67], a[76:79], v[124:127]\n"
"s_add_u32 s95, s50, 0xffd00000\n"
"s_and_b32 s94, s95, 0xfbffffff\n"
"ds_write_addtid_b32 v98 offset:9600\n"
"v_mfma_f32_16x16x32_bf16 v[124:127], v[68:71], a[80:83], v[124:127]\n"
"ds_write_addtid_b32 v99 offset:14240\n"
"global_load_dwordx4 v[52:55], v8, s[56:57] offset:1024 sc1\n"
"v_mfma_f32_16x16x32_bf16 v[124:127], v[72:75], a[84:87], v[124:127]\n"
"v_fma_f32 v60, v18, v141, v26\n"
"v_fma_f32 v61, v19, v141, v27\n"
"v_mfma_f32_16x16x32_bf16 v[124:127], v[76:79], a[88:91], v[124:127]\n"
"v_fma_f32 v62, v20, v141, v28\n"
"v_fma_f32 v63, v21, v141, v29\n"
"v_mfma_f32_16x16x32_bf16 v[124:127], v[80:83], a[92:95], v[124:127]\n"
"v_fma_f32 v56, v56, v140, v60\n"
"v_fma_f32 v57, v57, v140, v61\n"
"v_mfma_f32_16x16x32_bf16 v[96:99], v[100:103], a[96:99], 0\n"
"v_fma_f32 v58, v58, v140, v62\n"
"v_fma_f32 v59, v59, v140, v63\n"
"v_mfma_f32_16x16x32_bf16 v[96:99], v[104:107], a[100:103], v[96:99]\n"
"ds_write_addtid_b32 v124 offset:512\n"
"v_mul_f32_e64 v56, v56, |v56|\n"
"v_mfma_f32_16x16x32_bf16 v[96:99], v[108:111], a[104:107], v[96:99]\n"
"ds_write_addtid_b32 v125 offset:5152\n"
"v_mul_f32_e64 v57, v57, |v57|\n"
"v_mfma_f32_16x16x32_bf16 v[96:99], v[64:67], a[108:111], v[96:99]\n"
"ds_write_addtid_b32 v126 offset:9856\n"
"v_mul_f32_e64 v58, v58, |v58|\n"
"v_mfma_f32_16x16x32_bf16 v[96:99], v[68:71], a[112:115], v[96:99]\n"
"ds_write_addtid_b32 v127 offset:14496\n"
"v_mul_f32_e64 v59, v59, |v59|\n"
"v_mfma_f32_16x16x32_bf16 v[96:99], v[72:75], a[116:119], v[96:99]\n"
"v_cvt_pk_bf16_f32 v128, v56, v57\n"
"v_cvt_pk_bf16_f32 v129, v58, v59\n"
"v_mfma_f32_16x16x32_bf16 v[96:99], v[76:79], a[120:123], v[96:99]\n"
"v_pk_max_i16 v128, v128, 0\n"
"v_pk_max_i16 v129, v129, 0\n"
"v_mfma_f32_16x16x32_bf16 v[96:99], v[80:83], a[124:127], v[96:99]\n"
"s_waitcnt lgkmcnt(12)\n"
"v_add_f32 v84, v84, v88\n"
"v_add_f32 v85, v85, v89\n"
"v_mfma_f32_16x16x32_bf16 v[56:59], v[100:103], a[128:131], 0\n"
"v_add_f32 v86, v86, v90\n"
"v_add_f32 v87, v87, v91\n"
"v_mfma_f32_16x16x32_bf16 v[56:59], v[104:107], a[132:135], v[56:59]\n"
"ds_write_addtid_b32 v96 offset:768\n"
"v_add_f32 v84, v84, v92\n"
"v_mfma_f32_16x16x32_bf16 v[56:59], v[108:111], a[136:139], v[56:59]\n"
"ds_write_addtid_b32 v97 offset:5408\n"
"v_add_f32 v85, v85, v93\n"
"v_mfma_f32_16x16x32_bf16 v[56:59], v[64:67], a[140:143], v[56:59]\n"
"ds_write_addtid_b32 v98 offset:10112\n"
"v_add_f32 v86, v86, v94\n"
"v_mfma_f32_16x16x32_bf16 v[56:59], v[68:71], a[144:147], v[56:59]\n"
"ds_write_addtid_b32 v99 offset:14752\n"
"v_add_f32 v87, v87, v95\n"
"v_mfma_f32_16x16x32_bf16 v[56:59], v[72:75], a[148:151], v[56:59]\n"
"v_add_f32 v84, v84, v132\n"
"v_add_f32 v85, v85, v133\n"
"v_mfma_f32_16x16x32_bf16 v[56:59], v[76:79], a[152:155], v[56:59]\n"
"v_add_f32 v86, v86, v134\n"
"v_add_f32 v87, v87, v135\n"
"v_mfma_f32_16x16x32_bf16 v[56:59], v[80:83], a[156:159], v[56:59]\n"
"v_fma_f32 v60, v22, v141, v30\n"
"v_fma_f32 v61, v23, v141, v31\n"
"v_mfma_f32_16x16x32_bf16 v[88:91], v[100:103], a[160:163], 0\n"
"v_fma_f32 v62, v24, v141, v32\n"
"v_fma_f32 v63, v25, v141, v33\n"
"v_mfma_f32_16x16x32_bf16 v[88:91], v[104:107], a[164:167], v[88:91]\n"
"ds_write_addtid_b32 v56 offset:1024\n"
"v_fma_f32 v84, v84, v140, v60\n"
"v_mfma_f32_16x16x32_bf16 v[88:91], v[108:111], a[168:171], v[88:91]\n"
"s_add_u32 s58, s50, 0x180000\n"
"ds_write_addtid_b32 v57 offset:5664\n"
"v_fma_f32 v85, v85, v140, v61\n"
"v_mfma_f32_16x16x32_bf16 v[88:91], v[64:67], a[172:175], v[88:91]\n"
"s_and_b32 s58, s58, 0x3ffffff\n"
"ds_write_addtid_b32 v58 offset:10368\n"
"v_fma_f32 v86, v86, v140, v62\n"
"v_mfma_f32_16x16x32_bf16 v[88:91], v[68:71], a[176:179], v[88:91]\n"
"s_add_u32 s56, s40, s58\n"
"ds_write_addtid_b32 v59 offset:15008\n"
"v_fma_f32 v87, v87, v140, v63\n"
"v_mfma_f32_16x16x32_bf16 v[88:91], v[72:75], a[180:183], v[88:91]\n"
"s_addc_u32 s57, s41, 0\n"
"v_mul_f32_e64 v84, v84, |v84|\n"
"v_mul_f32_e64 v85, v85, |v85|\n"
"v_mfma_f32_16x16x32_bf16 v[88:91], v[76:79], a[184:187], v[88:91]\n"
"s_waitcnt vmcnt(6)\n"
"v_cmp_ne_u64 vcc, s[48:49], v[144:145]\n"
"v_mul_f32_e64 v86, v86, |v86|\n"
"v_mfma_f32_16x16x32_bf16 v[88:91], v[80:83], a[188:191], v[88:91]\n"
"v_mul_f32_e64 v87, v87, |v87|\n"
"v_cvt_pk_bf16_f32 v130, v84, v85\n"
"v_mfma_f32_16x16x32_bf16 v[92:95], a[192:195], v[100:103], 0\n"
"s_cbranch_vccnz .LF_3s\n"
".LF_3r:\n"
"v_cvt_pk_bf16_f32 v131, v86, v87\n"
"v_mfma_f32_16x16x32_bf16 v[92:95], a[196:199], v[104:107], v[92:95]\n"
"s_add_u32 s50, s50, 0x180000\n"
"ds_write_addtid_b32 v88 offset:1280\n"
"global_load_dwordx4 v[56:59], v5, s[56:57] offset:0 sc1\n"
"v_mfma_f32_16x16x32_bf16 v[92:95], a[200:203], v[108:111], v[92:95]\n"
"ds_write_addtid_b32 v89 offset:5920\n"
"v_pk_max_i16 v130, v130, 0\n"
"v_mfma_f32_16x16x32_bf16 v[92:95], a[204:207], v[64:67], v[92:95]\n"
"ds_write_addtid_b32 v90 offset:10624\n"
"v_pk_max_i16 v131, v131, 0\n"
"v_mfma_f32_16x16x32_bf16 v[92:95], a[208:211], v[68:71], v[92:95]\n"
"ds_write_addtid_b32 v91 offset:15264\n"
"v_mfma_f32_16x16x32_bf16 v[92:95], a[212:215], v[72:75], v[92:95]\n"
"v_mfma_f32_16x16x32_bf16 v[92:95], a[216:219], v[76:79], v[92:95]\n"
"v_mfma_f32_16x16x32_bf16 v[92:95], a[220:223], v[80:83], v[92:95]\n"
"v_mfma_f32_16x16x32_bf16 v[88:91], a[224:227], v[100:103], 0\n"
"s_waitcnt lgkmcnt(0)\n"
"global_load_dwordx4 v[60:63], v5, s[56:57] offset:1024 sc1\n"
"v_mfma_f32_16x16x32_bf16 v[88:91], a[228:231], v[104:107], v[88:91]\n"
"s_barrier\n"
"v_mfma_f32_16x16x32_bf16 v[88:91], a[232:235], v[108:111], v[88:91]\n"
"ds_read2st64_b32 v[136:137], v11 offset0:0 offset1:1\n"
"ds_read_b128 v[96:99], v2 offset:1536\n"
"v_mfma_f32_16x16x32_bf16 v[88:91], a[236:239], v[64:67], v[88:91]\n"
"s_waitcnt vmcnt(4)\n"
"buffer_load_dwordx2 v[138:139], v9, s[40:43], s50 offen sc1\n"
"v_mfma_f32_16x16x32_bf16 v[88:91], a[240:243], v[68:71], v[88:91]\n"
"global_load_dwordx4 v[64:67], v6, s[56:57] offset:0 sc1\n"
"ds_read_b128 v[84:87], v3 offset:512\n"
"v_mfma_f32_16x16x32_bf16 v[88:91], a[244:247], v[72:75], v[88:91]\n"
"ds_read_b128 v[100:103], v4 offset:2560\n"
"ds_read_b128 v[104:107], v2 offset:1792\n"
"v_mfma_f32_16x16x32_bf16 v[88:91], a[248:251], v[76:79], v[88:91]\n"
"ds_read_b128 v[108:111], v3 offset:768\n"
"ds_read_b128 v[124:127], v4 offset:2816\n"
"v_mfma_f32_16x16x32_bf16 v[88:91], a[252:255], v[80:83], v[88:91]\n"
"buffer_store_dwordx2 v[0:1], v16, s[40:43], s93 offen offset:0 sc1\n"
"v_mfma_f32_16x16x32_bf16 v[132:135], v[112:115], a[0:3], 0\n"
"buffer_store_dwordx4 v[128:131], v15, s[40:43], s94 offen sc1\n"
"v_mfma_f32_16x16x32_bf16 v[132:135], v[116:119], a[4:7], v[132:135]\n"
"v_mfma_f32_16x16x32_bf16 v[132:135], v[120:123], a[8:11], v[132:135]\n"
"global_load_dwordx4 v[68:71], v6, s[56:57] offset:1024 sc1\n"
"v_mfma_f32_16x16x32_bf16 v[132:135], v[36:39], a[12:15], v[132:135]\n"
"v_mfma_f32_16x16x32_bf16 v[132:135], v[40:43], a[16:19], v[132:135]\n"
"s_waitcnt lgkmcnt(3)\n"
"v_add_f32 v96, v96, v84\n"
"v_add_f32 v97, v97, v85\n"
"v_mfma_f32_16x16x32_bf16 v[132:135], v[44:47], a[20:23], v[132:135]\n"
"s_waitcnt vmcnt(7)\n"
"global_load_dwordx4 v[72:75], v7, s[56:57] offset:0 sc1\n"
"v_add_f32 v98, v98, v86\n"
"v_mfma_f32_16x16x32_bf16 v[132:135], v[48:51], a[24:27], v[132:135]\n"
"v_add_f32 v99, v99, v87\n"
"v_add_f32 v96, v96, v100\n"
"v_mfma_f32_16x16x32_bf16 v[132:135], v[52:55], a[28:31], v[132:135]\n"
"global_load_dwordx4 v[76:79], v7, s[56:57] offset:1024 sc1\n"
"v_add_f32 v97, v97, v101\n"
"v_mfma_f32_16x16x32_bf16 v[128:131], v[112:115], a[32:35], 0\n"
"v_add_f32 v98, v98, v102\n"
"v_add_f32 v99, v99, v103\n"
"v_mfma_f32_16x16x32_bf16 v[128:131], v[116:119], a[36:39], v[128:131]\n"
"ds_write_addtid_b32 v132 offset:1536\n"
"v_add_f32 v96, v96, v92\n"
"v_mfma_f32_16x16x32_bf16 v[128:131], v[120:123], a[40:43], v[128:131]\n"
"ds_write_addtid_b32 v133 offset:6176\n"
"v_add_f32 v97, v97, v93\n"
"v_mfma_f32_16x16x32_bf16 v[128:131], v[36:39], a[44:47], v[128:131]\n"
"ds_write_addtid_b32 v134 offset:10880\n"
"v_add_f32 v98, v98, v94\n"
"v_mfma_f32_16x16x32_bf16 v[128:131], v[40:43], a[48:51], v[128:131]\n"
"ds_write_addtid_b32 v135 offset:15520\n"
"v_add_f32 v99, v99, v95\n"
"v_mfma_f32_16x16x32_bf16 v[128:131], v[44:47], a[52:55], v[128:131]\n"
"s_add_u32 s93, s50, 0xffd00000\n"
"s_and_b32 s94, s93, 0xfbffffff\n"
"v_fma_f32 v84, v18, v137, v26\n"
"v_mfma_f32_16x16x32_bf16 v[128:131], v[48:51], a[56:59], v[128:131]\n"
"v_fma_f32 v85, v19, v137, v27\n"
"v_fma_f32 v86, v20, v137, v28\n"
"v_mfma_f32_16x16x32_bf16 v[128:131], v[52:55], a[60:63], v[128:131]\n"
"v_fma_f32 v87, v21, v137, v29\n"
"v_fma_f32 v96, v96, v136, v84\n"
"v_mfma_f32_16x16x32_bf16 v[92:95], v[112:115], a[64:67], 0\n"
"v_fma_f32 v97, v97, v136, v85\n"
"v_fma_f32 v98, v98, v136, v86\n"
"v_mfma_f32_16x16x32_bf16 v[92:95], v[116:119], a[68:71], v[92:95]\n"
"ds_write_addtid_b32 v128 offset:1792\n"
"global_load_dwordx4 v[80:83], v8, s[56:57] offset:0 sc1\n"
"v_mfma_f32_16x16x32_bf16 v[92:95], v[120:123], a[72:75], v[92:95]\n"
"ds_write_addtid_b32 v129 offset:6432\n"
"v_fma_f32 v99, v99, v136, v87\n"
"v_mfma_f32_16x16x32_bf16 v[92:95], v[36:39], a[76:79], v[92:95]\n"
"ds_write_addtid_b32 v130 offset:11136\n"
"v_mul_f32_e64 v96, v96, |v96|\n"
"v_mfma_f32_16x16x32_bf16 v[92:95], v[40:43], a[80:83], v[92:95]\n"
"ds_write_addtid_b32 v131 offset:15776\n"
"v_mul_f32_e64 v97, v97, |v97|\n"
"v_mfma_f32_16x16x32_bf16 v[92:95], v[44:47], a[84:87], v[92:95]\n"
"global_load_dwordx4 v[84:87], v8, s[56:57] offset:1024 sc1\n"
"v_mul_f32_e64 v98, v98, |v98|\n"
"v_mfma_f32_16x16x32_bf16 v[92:95], v[48:51], a[88:91], v[92:95]\n"
"v_mul_f32_e64 v99, v99, |v99|\n"
"v_cvt_pk_bf16_f32 v100, v96, v97\n"
"v_mfma_f32_16x16x32_bf16 v[92:95], v[52:55], a[92:95], v[92:95]\n"
"v_cvt_pk_bf16_f32 v101, v98, v99\n"
"v_pk_max_i16 v100, v100, 0\n"
"v_mfma_f32_16x16x32_bf16 v[128:131], v[112:115], a[96:99], 0\n"
"v_pk_max_i16 v101, v101, 0\n"
"v_mfma_f32_16x16x32_bf16 v[128:131], v[116:119], a[100:103], v[128:131]\n"
"s_waitcnt lgkmcnt(8)\n"
"ds_write_addtid_b32 v92 offset:2048\n"
"v_add_f32 v104, v104, v108\n"
"v_mfma_f32_16x16x32_bf16 v[128:131], v[120:123], a[104:107], v[128:131]\n"
"ds_write_addtid_b32 v93 offset:6688\n"
"v_add_f32 v105, v105, v109\n"
"v_mfma_f32_16x16x32_bf16 v[128:131], v[36:39], a[108:111], v[128:131]\n"
"ds_write_addtid_b32 v94 offset:11392\n"
"v_add_f32 v106, v106, v110\n"
"v_mfma_f32_16x16x32_bf16 v[128:131], v[40:43], a[112:115], v[128:131]\n"
"ds_write_addtid_b32 v95 offset:16032\n"
"v_add_f32 v107, v107, v111\n"
"v_mfma_f32_16x16x32_bf16 v[128:131], v[44:47], a[116:119], v[128:131]\n"
"v_add_f32 v104, v104, v124\n"
"v_add_f32 v105, v105, v125\n"
"v_mfma_f32_16x16x32_bf16 v[128:131], v[48:51], a[120:123], v[128:131]\n"
"v_add_f32 v106, v106, v126\n"
"v_add_f32 v107, v107, v127\n"
"v_mfma_f32_16x16x32_bf16 v[128:131], v[52:55], a[124:127], v[128:131]\n"
"v_add_f32 v104, v104, v88\n"
"v_add_f32 v105, v105, v89\n"
"v_mfma_f32_16x16x32_bf16 v[92:95], v[112:115], a[128:131], 0\n"
"v_add_f32 v106, v106, v90\n"
"v_add_f32 v107, v107, v91\n"
"v_mfma_f32_16x16x32_bf16 v[92:95], v[116:119], a[132:135], v[92:95]\n"
"ds_write_addtid_b32 v128 offset:2304\n"
"v_fma_f32 v96, v22, v137, v30\n"
"v_mfma_f32_16x16x32_bf16 v[92:95], v[120:123], a[136:139], v[92:95]\n"
"ds_write_addtid_b32 v129 offset:6944\n"
"v_fma_f32 v97, v23, v137, v31\n"
"v_mfma_f32_16x16x32_bf16 v[92:95], v[36:39], a[140:143], v[92:95]\n"
"ds_write_addtid_b32 v130 offset:11648\n"
"v_fma_f32 v98, v24, v137, v32\n"
"v_mfma_f32_16x16x32_bf16 v[92:95], v[40:43], a[144:147], v[92:95]\n"
"ds_write_addtid_b32 v131 offset:16288\n"
"v_fma_f32 v99, v25, v137, v33\n"
"v_mfma_f32_16x16x32_bf16 v[92:95], v[44:47], a[148:151], v[92:95]\n"
"v_fma_f32 v104, v104, v136, v96\n"
"v_fma_f32 v105, v105, v136, v97\n"
"v_mfma_f32_16x16x32_bf16 v[92:95], v[48:51], a[152:155], v[92:95]\n"
"v_fma_f32 v106, v106, v136, v98\n"
"v_fma_f32 v107, v107, v136, v99\n"
"v_mfma_f32_16x16x32_bf16 v[92:95], v[52:55], a[156:159], v[92:95]\n"
"v_mul_f32_e64 v104, v104, |v104|\n"
"v_mul_f32_e64 v105, v105, |v105|\n"
"v_mfma_f32_16x16x32_bf16 v[96:99], v[112:115], a[160:163], 0\n"
"v_mul_f32_e64 v106, v106, |v106|\n"
"v_mul_f32_e64 v107, v107, |v107|\n"
"v_mfma_f32_16x16x32_bf16 v[96:99], v[116:119], a[164:167], v[96:99]\n"
"ds_write_addtid_b32 v92 offset:2560\n"
"v_cvt_pk_bf16_f32 v102, v104, v105\n"
"v_mfma_f32_16x16x32_bf16 v[96:99], v[120:123], a[168:171], v[96:99]\n"
"s_add_u32 s58, s50, 0x180000\n"
"ds_write_addtid_b32 v93 offset:7200\n"
"v_cvt_pk_bf16_f32 v103, v106, v107\n"
"v_mfma_f32_16x16x32_bf16 v[96:99], v[36:39], a[172:175], v[96:99]\n"
"s_and_b32 s58, s58, 0x3ffffff\n"
"ds_write_addtid_b32 v94 offset:11904\n"
"v_pk_max_i16 v102, v102, 0\n"
"v_mfma_f32_16x16x32_bf16 v[96:99], v[40:43], a[176:179], v[96:99]\n"
"s_add_u32 s56, s40, s58\n"
"ds_write_addtid_b32 v95 offset:16544\n"
"v_pk_max_i16 v103, v103, 0\n"
"v_mfma_f32_16x16x32_bf16 v[96:99], v[44:47], a[180:183], v[96:99]\n"
"s_addc_u32 s57, s41, 0\n"
"buffer_store_dwordx4 v[100:103], v15, s[40:43], s94 offen sc1\n"
"v_mfma_f32_16x16x32_bf16 v[96:99], v[48:51], a[184:187], v[96:99]\n"
"s_waitcnt vmcnt(9)\n"
"v_cmp_ne_u64 vcc, s[48:49], v[138:139]\n"
"v_mfma_f32_16x16x32_bf16 v[96:99], v[52:55], a[188:191], v[96:99]\n"
"v_mfma_f32_16x16x32_bf16 v[100:103], a[192:195], v[112:115], 0\n"
"s_cbranch_vccnz .LF_4s\n"
".LF_4r:\n"
"v_mfma_f32_16x16x32_bf16 v[100:103], a[196:199], v[116:119], v[100:103]\n"
"s_add_u32 s50, s50, 0x180000\n"
"ds_write_addtid_b32 v96 offset:2816\n"
"v_mfma_f32_16x16x32_bf16 v[100:103], a[200:203], v[120:123], v[100:103]\n"
"s_waitcnt vmcnt(6)\n"
"buffer_store_dwordx2 v[0:1], v16, s[40:43], s95 offen offset:0 sc1\n"
"ds_write_addtid_b32 v97 offset:7456\n"
"v_mfma_f32_16x16x32_bf16 v[100:103], a[204:207], v[36:39], v[100:103]\n"
"ds_write_addtid_b32 v98 offset:12160\n"
"global_load_dwordx4 v[88:91], v5, s[56:57] offset:0 sc1\n"
"v_mfma_f32_16x16x32_bf16 v[100:103], a[208:211], v[40:43], v[100:103]\n"
"ds_write_addtid_b32 v99 offset:16800\n"
"v_mfma_f32_16x16x32_bf16 v[100:103], a[212:215], v[44:47], v[100:103]\n"
"v_mfma_f32_16x16x32_bf16 v[100:103], a[216:219], v[48:51], v[100:103]\n"
"v_mfma_f32_16x16x32_bf16 v[100:103], a[220:223], v[52:55], v[100:103]\n"
"v_mfma_f32_16x16x32_bf16 v[104:107], a[224:227], v[112:115], 0\n"
"s_waitcnt lgkmcnt(0)\n"
"global_load_dwordx4 v[92:95], v5, s[56:57] offset:1024 sc1\n"
"v_mfma_f32_16x16x32_bf16 v[104:107], a[228:231], v[116:119], v[104:107]\n"
"s_barrier\n"
"v_mfma_f32_16x16x32_bf16 v[104:107], a[232:235], v[120:123], v[104:107]\n"
"ds_read2st64_b32 v[132:133], v12 offset0:0 offset1:1\n"
"ds_read_b128 v[108:111], v2 offset:0\n"
"v_mfma_f32_16x16x32_bf16 v[104:107], a[236:239], v[36:39], v[104:107]\n"
"ds_read_b128 v[96:99], v3 offset:2048\n"
"ds_read_b128 v[112:115], v4 offset:1024\n"
"v_mfma_f32_16x16x32_bf16 v[104:107], a[240:243], v[40:43], v[104:107]\n"
"buffer_load_dwordx2 v[134:135], v9, s[40:43], s50 offen sc1\n"
"v_mfma_f32_16x16x32_bf16 v[104:107], a[244:247], v[44:47], v[104:107]\n"
"global_load_dwordx4 v[36:39], v6, s[56:57] offset:0 sc1\n"
"ds_read_b128 v[116:119], v2 offset:256\n"
"v_mfma_f32_16x16x32_bf16 v[104:107], a[248:251], v[48:51], v[104:107]\n"
"ds_read_b128 v[120:123], v3 offset:2304\n"
"ds_read_b128 v[124:127], v4 offset:1280\n"
"v_mfma_f32_16x16x32_bf16 v[104:107], a[252:255], v[52:55], v[104:107]\n"
"global_load_dwordx4 v[40:43], v6, s[56:57] offset:1024 sc1\n"
"v_mfma_f32_16x16x32_bf16 v[128:131], v[56:59], a[0:3], 0\n"
"v_mfma_f32_16x16x32_bf16 v[128:131], v[60:63], a[4:7], v[128:131]\n"
"s_waitcnt vmcnt(11)\n"
"v_mfma_f32_16x16x32_bf16 v[128:131], v[64:67], a[8:11], v[128:131]\n"
"s_waitcnt lgkmcnt(3)\n"
"v_add_f32 v108, v108, v96\n"
"v_add_f32 v109, v109, v97\n"
"v_mfma_f32_16x16x32_bf16 v[128:131], v[68:71], a[12:15], v[128:131]\n"
"s_waitcnt vmcnt(9)\n"
"v_add_f32 v110, v110, v98\n"
"v_add_f32 v111, v111, v99\n"
"v_mfma_f32_16x16x32_bf16 v[128:131], v[72:75], a[16:19], v[128:131]\n"
"v_add_f32 v108, v108, v112\n"
"v_add_f32 v109, v109, v113\n"
"v_mfma_f32_16x16x32_bf16 v[128:131], v[76:79], a[20:23], v[128:131]\n"
"s_waitcnt vmcnt(7)\n"
"v_add_f32 v110, v110, v114\n"
"v_add_f32 v111, v111, v115\n"
"v_mfma_f32_16x16x32_bf16 v[128:131], v[80:83], a[24:27], v[128:131]\n"
"v_add_f32 v108, v108, v100\n"
"v_add_f32 v109, v109, v101\n"
"v_mfma_f32_16x16x32_bf16 v[128:131], v[84:87], a[28:31], v[128:131]\n"
"global_load_dwordx4 v[44:47], v7, s[56:57] offset:0 sc1\n"
"v_add_f32 v110, v110, v102\n"
"v_mfma_f32_16x16x32_bf16 v[112:115], v[56:59], a[32:35], 0\n"
"v_add_f32 v111, v111, v103\n"
"v_mfma_f32_16x16x32_bf16 v[112:115], v[60:63], a[36:39], v[112:115]\n"
"ds_write_addtid_b32 v128 offset:0\n"
"global_load_dwordx4 v[48:51], v7, s[56:57] offset:1024 sc1\n"
"v_mfma_f32_16x16x32_bf16 v[112:115], v[64:67], a[40:43], v[112:115]\n"
"s_add_u32 s95, s50, 0xffd00000\n"
"s_and_b32 s94, s95, 0xfbffffff\n"
"ds_write_addtid_b32 v129 offset:4640\n"
"v_mfma_f32_16x16x32_bf16 v[112:115], v[68:71], a[44:47], v[112:115]\n"
"ds_write_addtid_b32 v130 offset:9344\n"
"v_fma_f32 v96, v18, v133, v26\n"
"v_mfma_f32_16x16x32_bf16 v[112:115], v[72:75], a[48:51], v[112:115]\n"
"ds_write_addtid_b32 v131 offset:13984\n"
"v_fma_f32 v97, v19, v133, v27\n"
"v_mfma_f32_16x16x32_bf16 v[112:115], v[76:79], a[52:55], v[112:115]\n"
"v_fma_f32 v98, v20, v133, v28\n"
"v_fma_f32 v99, v21, v133, v29\n"
"v_mfma_f32_16x16x32_bf16 v[112:115], v[80:83], a[56:59], v[112:115]\n"
"v_fma_f32 v108, v108, v132, v96\n"
"v_fma_f32 v109, v109, v132, v97\n"
"v_mfma_f32_16x16x32_bf16 v[112:115], v[84:87], a[60:63], v[112:115]\n"
"v_fma_f32 v110, v110, v132, v98\n"
"v_fma_f32 v111, v111, v132, v99\n"
"v_mfma_f32_16x16x32_bf16 v[100:103], v[56:59], a[64:67], 0\n"
"global_load_dwordx4 v[52:55], v8, s[56:57] offset:0 sc1\n"
"v_mul_f32_e64 v108, v108, |v108|\n"
"v_mfma_f32_16x16x32_bf16 v[100:103], v[60:63], a[68:71], v[100:103]\n"
"ds_write_addtid_b32 v112 offset:256\n"
"v_mul_f32_e64 v109, v109, |v109|\n"
"v_mfma_f32_16x16x32_bf16 v[100:103], v[64:67], a[72:75], v[100:103]\n"
"ds_write_addtid_b32 v113 offset:4896\n"
"v_mul_f32_e64 v110, v110, |v110|\n"
"v_mfma_f32_16x16x32_bf16 v[100:103], v[68:71], a[76:79], v[100:103]\n"
"ds_write_addtid_b32 v114 offset:9600\n"
"v_mul_f32_e64 v111, v111, |v111|\n"
"v_mfma_f32_16x16x32_bf16 v[100:103], v[72:75], a[80:83], v[100:103]\n"
"ds_write_addtid_b32 v115 offset:14240\n"
"global_load_dwordx4 v[96:99], v8, s[56:57] offset:1024 sc1\n"
"v_mfma_f32_16x16x32_bf16 v[100:103], v[76:79], a[84:87], v[100:103]\n"
"v_cvt_pk_bf16_f32 v128, v108, v109\n"
"v_cvt_pk_bf16_f32 v129, v110, v111\n"
"v_mfma_f32_16x16x32_bf16 v[100:103], v[80:83], a[88:91], v[100:103]\n"
"v_pk_max_i16 v128, v128, 0\n"
"v_pk_max_i16 v129, v129, 0\n"
"v_mfma_f32_16x16x32_bf16 v[100:103], v[84:87], a[92:95], v[100:103]\n"
"s_waitcnt lgkmcnt(8)\n"
"v_add_f32 v116, v116, v120\n"
"v_add_f32 v117, v117, v121\n"
"v_mfma_f32_16x16x32_bf16 v[108:111], v[56:59], a[96:99], 0\n"
"v_add_f32 v118, v118, v122\n"
"v_add_f32 v119, v119, v123\n"
"v_mfma_f32_16x16x32_bf16 v[108:111], v[60:63], a[100:103], v[108:111]\n"
"ds_write_addtid_b32 v100 offset:512\n"
"v_add_f32 v116, v116, v124\n"
"v_mfma_f32_16x16x32_bf16 v[108:111], v[64:67], a[104:107], v[108:111]\n"
"ds_write_addtid_b32 v101 offset:5152\n"
"v_add_f32 v117, v117, v125\n"
"v_mfma_f32_16x16x32_bf16 v[108:111], v[68:71], a[108:111], v[108:111]\n"
"ds_write_addtid_b32 v102 offset:9856\n"
"v_add_f32 v118, v118, v126\n"
"v_mfma_f32_16x16x32_bf16 v[108:111], v[72:75], a[112:115], v[108:111]\n"
"ds_write_addtid_b32 v103 offset:14496\n"
"v_add_f32 v119, v119, v127\n"
"v_mfma_f32_16x16x32_bf16 v[108:111], v[76:79], a[116:119], v[108:111]\n"
"s_waitcnt vmcnt(10)\n"
"buffer_store_dwordx2 v[0:1], v16, s[40:43], s93 offen offset:0 sc1\n"
"v_add_f32 v116, v116, v104\n"
"v_mfma_f32_16x16x32_bf16 v[108:111], v[80:83], a[120:123], v[108:111]\n"
"v_add_f32 v117, v117, v105\n"
"v_add_f32 v118, v118, v106\n"
"v_mfma_f32_16x16x32_bf16 v[108:111], v[84:87], a[124:127], v[108:111]\n"
"v_add_f32 v119, v119, v107\n"
"v_fma_f32 v100, v22, v133, v30\n"
"v_mfma_f32_16x16x32_bf16 v[112:115], v[56:59], a[128:131], 0\n"
"v_fma_f32 v101, v23, v133, v31\n"
"v_fma_f32 v102, v24, v133, v32\n"
"v_mfma_f32_16x16x32_bf16 v[112:115], v[60:63], a[132:135], v[112:115]\n"
"ds_write_addtid_b32 v108 offset:768\n"
"v_fma_f32 v103, v25, v133, v33\n"
"v_mfma_f32_16x16x32_bf16 v[112:115], v[64:67], a[136:139], v[112:115]\n"
"ds_write_addtid_b32 v109 offset:5408\n"
"v_fma_f32 v116, v116, v132, v100\n"
"v_mfma_f32_16x16x32_bf16 v[112:115], v[68:71], a[140:143], v[112:115]\n"
"ds_write_addtid_b32 v110 offset:10112\n"
"v_fma_f32 v117, v117, v132, v101\n"
"v_mfma_f32_16x16x32_bf16 v[112:115], v[72:75], a[144:147], v[112:115]\n"
"ds_write_addtid_b32 v111 offset:14752\n"
"v_fma_f32 v118, v118, v132, v102\n"
"v_mfma_f32_16x16x32_bf16 v[112:115], v[76:79], a[148:151], v[112:115]\n"
"v_fma_f32 v119, v119, v132, v103\n"
"v_mul_f32_e64 v116, v116, |v116|\n"
"v_mfma_f32_16x16x32_bf16 v[112:115], v[80:83], a[152:155], v[112:115]\n"
"v_mul_f32_e64 v117, v117, |v117|\n"
"v_mul_f32_e64 v118, v118, |v118|\n"
"v_mfma_f32_16x16x32_bf16 v[112:115], v[84:87], a[156:159], v[112:115]\n"
"v_mul_f32_e64 v119, v119, |v119|\n"
"v_cvt_pk_bf16_f32 v130, v116, v117\n"
"v_mfma_f32_16x16x32_bf16 v[108:111], v[56:59], a[160:163], 0\n"
"v_cvt_pk_bf16_f32 v131, v118, v119\n"
"v_pk_max_i16 v130, v130, 0\n"
"v_mfma_f32_16x16x32_bf16 v[108:111], v[60:63], a[164:167], v[108:111]\n"
"s_add_u32 s58, s50, 0x180000\n"
"ds_write_addtid_b32 v112 offset:1024\n"
"v_pk_max_i16 v131, v131, 0\n"
"v_mfma_f32_16x16x32_bf16 v[108:111], v[64:67], a[168:171], v[108:111]\n"
"s_and_b32 s58, s58, 0x3ffffff\n"
"ds_write_addtid_b32 v113 offset:5664\n"
"v_mfma_f32_16x16x32_bf16 v[108:111], v[68:71], a[172:175], v[108:111]\n"
"s_add_u32 s56, s40, s58\n"
"ds_write_addtid_b32 v114 offset:10368\n"
"v_mfma_f32_16x16x32_bf16 v[108:111], v[72:75], a[176:179], v[108:111]\n"
"s_addc_u32 s57, s41, 0\n"
"ds_write_addtid_b32 v115 offset:15008\n"
"v_mfma_f32_16x16x32_bf16 v[108:111], v[76:79], a[180:183], v[108:111]\n"
"s_waitcnt vmcnt(7)\n"
"buffer_store_dwordx4 v[128:131], v15, s[40:43], s94 offen sc1\n"
"v_cmp_ne_u64 vcc, s[48:49], v[134:135]\n"
"v_mfma_f32_16x16x32_bf16 v[108:111], v[80:83], a[184:187], v[108:111]\n"
"v_mfma_f32_16x16x32_bf16 v[108:111], v[84:87], a[188:191], v[108:111]\n"
"s_cbranch_vccnz .LF_1s\n"
".LF_1r:\n"
"v_mfma_f32_16x16x32_bf16 v[112:115], a[192:195], v[56:59], 0\n"
"s_add_u32 s50, s50, 0x180000\n"
"global_load_dwordx4 v[100:103], v5, s[56:57] offset:0 sc1\n"
"v_mfma_f32_16x16x32_bf16 v[112:115], a[196:199], v[60:63], v[112:115]\n"
"ds_write_addtid_b32 v108 offset:1280\n"
"global_load_dwordx4 v[124:127], v10, s[56:57] offset:0 sc1\n"
"v_mfma_f32_16x16x32_bf16 v[112:115], a[200:203], v[64:67], v[112:115]\n"
"ds_write_addtid_b32 v109 offset:5920\n"
"global_load_dwordx4 v[116:119], v10, s[56:57] offset:1024 sc1\n"
"v_mfma_f32_16x16x32_bf16 v[112:115], a[204:207], v[68:71], v[112:115]\n"
"ds_write_addtid_b32 v110 offset:10624\n"
"global_load_dwordx4 v[120:123], v10, s[56:57] offset:2048 sc1\n"
"v_mfma_f32_16x16x32_bf16 v[112:115], a[208:211], v[72:75], v[112:115]\n"
"ds_write_addtid_b32 v111 offset:15264\n"
"global_load_dwordx4 v[128:131], v10, s[56:57] offset:3072 sc1\n"
"v_mfma_f32_16x16x32_bf16 v[112:115], a[212:215], v[76:79], v[112:115]\n"
"global_load_dwordx4 v[104:107], v5, s[56:57] offset:1024 sc1\n"
"v_mfma_f32_16x16x32_bf16 v[112:115], a[216:219], v[80:83], v[112:115]\n"
"v_mfma_f32_16x16x32_bf16 v[112:115], a[220:223], v[84:87], v[112:115]\n"
"v_mfma_f32_16x16x32_bf16 v[132:135], a[224:227], v[56:59], 0\n"
"s_waitcnt lgkmcnt(0)\n"
"v_mfma_f32_16x16x32_bf16 v[132:135], a[228:231], v[60:63], v[132:135]\n"
"s_barrier\n"
"v_mfma_f32_16x16x32_bf16 v[132:135], a[232:235], v[64:67], v[132:135]\n"
"global_load_dwordx4 v[108:111], v6, s[56:57] offset:0 sc1\n"
"ds_read2st64_b32 v[152:153], v13 offset0:0 offset1:1\n"
"v_mfma_f32_16x16x32_bf16 v[132:135], a[236:239], v[68:71], v[132:135]\n"
"ds_read_b128 v[56:59], v2 offset:1536\n"
"ds_read_b128 v[60:63], v3 offset:512\n"
"v_mfma_f32_16x16x32_bf16 v[132:135], a[240:243], v[72:75], v[132:135]\n"
"ds_read_b128 v[136:139], v4 offset:2560\n"
"ds_read_b128 v[140:143], v2 offset:1792\n"
"v_mfma_f32_16x16x32_bf16 v[132:135], a[244:247], v[76:79], v[132:135]\n"
"buffer_load_dwordx2 v[154:155], v9, s[40:43], s50 offen sc1\n"
"v_mfma_f32_16x16x32_bf16 v[132:135], a[248:251], v[80:83], v[132:135]\n"
"global_load_dwordx4 v[64:67], v6, s[56:57] offset:1024 sc1\n"
"ds_read_b128 v[144:147], v3 offset:768\n"
"v_mfma_f32_16x16x32_bf16 v[132:135], a[252:255], v[84:87], v[132:135]\n"
"ds_read_b128 v[148:151], v4 offset:2816\n"
"v_mfma_f32_16x16x32_bf16 v[76:79], v[88:91], a[0:3], 0\n"
"v_mfma_f32_16x16x32_bf16 v[76:79], v[92:95], a[4:7], v[76:79]\n"
"s_waitcnt vmcnt(15)\n"
"v_mfma_f32_16x16x32_bf16 v[76:79], v[36:39], a[8:11], v[76:79]\n"
"s_waitcnt lgkmcnt(3)\n"
"v_add_f32 v56, v56, v60\n"
"v_add_f32 v57, v57, v61\n"
"v_mfma_f32_16x16x32_bf16 v[76:79], v[40:43], a[12:15], v[76:79]\n"
"s_waitcnt vmcnt(13)\n"
"v_add_f32 v58, v58, v62\n"
"v_add_f32 v59, v59, v63\n"
"v_mfma_f32_16x16x32_bf16 v[76:79], v[44:47], a[16:19], v[76:79]\n"
"global_load_dwordx4 v[68:71], v7, s[56:57] offset:0 sc1\n"
"v_add_f32 v56, v56, v136\n"
"v_mfma_f32_16x16x32_bf16 v[76:79], v[48:51], a[20:23], v[76:79]\n"
"s_waitcnt vmcnt(12)\n"
"v_add_f32 v57, v57, v137\n"
"v_add_f32 v58, v58, v138\n"
"v_mfma_f32_16x16x32_bf16 v[76:79], v[52:55], a[24:27], v[76:79]\n"
"v_add_f32 v59, v59, v139\n"
"v_add_f32 v56, v56, v112\n"
"v_mfma_f32_16x16x32_bf16 v[76:79], v[96:99], a[28:31], v[76:79]\n"
"v_add_f32 v57, v57, v113\n"
"v_add_f32 v58, v58, v114\n"
"v_mfma_f32_16x16x32_bf16 v[60:63], v[88:91], a[32:35], 0\n"
"v_add_f32 v59, v59, v115\n"
"v_mfma_f32_16x16x32_bf16 v[60:63], v[92:95], a[36:39], v[60:63]\n"
"ds_write_addtid_b32 v76 offset:1536\n"
"global_load_dwordx4 v[72:75], v7, s[56:57] offset:1024 sc1\n"
"v_mfma_f32_16x16x32_bf16 v[60:63], v[36:39], a[40:43], v[60:63]\n"
"s_add_u32 s93, s50, 0xffd00000\n"
"s_and_b32 s94, s93, 0xfbffffff\n"
"ds_write_addtid_b32 v77 offset:6176\n"
"v_mfma_f32_16x16x32_bf16 v[60:63], v[40:43], a[44:47], v[60:63]\n"
"ds_write_addtid_b32 v78 offset:10880\n"
"v_fma_f32 v80, v18, v153, v26\n"
"v_mfma_f32_16x16x32_bf16 v[60:63], v[44:47], a[48:51], v[60:63]\n"
"ds_write_addtid_b32 v79 offset:15520\n"
"v_fma_f32 v81, v19, v153, v27\n"
"v_mfma_f32_16x16x32_bf16 v[60:63], v[48:51], a[52:55], v[60:63]\n"
"v_fma_f32 v82, v20, v153, v28\n"
"v_fma_f32 v83, v21, v153, v29\n"
"v_mfma_f32_16x16x32_bf16 v[60:63], v[52:55], a[56:59], v[60:63]\n"
"v_fma_f32 v56, v56, v152, v80\n"
"v_fma_f32 v57, v57, v152, v81\n"
"v_mfma_f32_16x16x32_bf16 v[60:63], v[96:99], a[60:63], v[60:63]\n"
"global_load_dwordx4 v[76:79], v8, s[56:57] offset:0 sc1\n"
"v_fma_f32 v58, v58, v152, v82\n"
"v_mfma_f32_16x16x32_bf16 v[84:87], v[88:91], a[64:67], 0\n"
"v_fma_f32 v59, v59, v152, v83\n"
"v_mul_f32_e64 v56, v56, |v56|\n"
"v_mfma_f32_16x16x32_bf16 v[84:87], v[92:95], a[68:71], v[84:87]\n"
"ds_write_addtid_b32 v60 offset:1792\n"
"v_mul_f32_e64 v57, v57, |v57|\n"
"v_mfma_f32_16x16x32_bf16 v[84:87], v[36:39], a[72:75], v[84:87]\n"
"ds_write_addtid_b32 v61 offset:6432\n"
"v_mul_f32_e64 v58, v58, |v58|\n"
"v_mfma_f32_16x16x32_bf16 v[84:87], v[40:43], a[76:79], v[84:87]\n"
"ds_write_addtid_b32 v62 offset:11136\n"
"global_load_dwordx4 v[80:83], v8, s[56:57] offset:1024 sc1\n"
"v_mfma_f32_16x16x32_bf16 v[84:87], v[44:47], a[80:83], v[84:87]\n"
"ds_write_addtid_b32 v63 offset:15776\n"
"v_mul_f32_e64 v59, v59, |v59|\n"
"v_mfma_f32_16x16x32_bf16 v[84:87], v[48:51], a[84:87], v[84:87]\n"
"v_cvt_pk_bf16_f32 v136, v56, v57\n"
"v_cvt_pk_bf16_f32 v137, v58, v59\n"
"v_mfma_f32_16x16x32_bf16 v[84:87], v[52:55], a[88:91], v[84:87]\n"
"v_pk_max_i16 v136, v136, 0\n"
"v_pk_max_i16 v137, v137, 0\n"
"v_mfma_f32_16x16x32_bf16 v[84:87], v[96:99], a[92:95], v[84:87]\n"
"s_waitcnt lgkmcnt(8)\n"
"v_add_f32 v140, v140, v144\n"
"v_add_f32 v141, v141, v145\n"
"v_mfma_f32_16x16x32_bf16 v[56:59], v[88:91], a[96:99], 0\n"
"v_add_f32 v142, v142, v146\n"
"v_add_f32 v143, v143, v147\n"
"v_mfma_f32_16x16x32_bf16 v[56:59], v[92:95], a[100:103], v[56:59]\n"
"ds_write_addtid_b32 v84 offset:2048\n"
"v_add_f32 v140, v140, v148\n"
"v_mfma_f32_16x16x32_bf16 v[56:59], v[36:39], a[104:107], v[56:59]\n"
"ds_write_addtid_b32 v85 offset:6688\n"
"v_add_f32 v141, v141, v149\n"
"v_mfma_f32_16x16x32_bf16 v[56:59], v[40:43], a[108:111], v[56:59]\n"
"ds_write_addtid_b32 v86 offset:11392\n"
"v_add_f32 v142, v142, v150\n"
"v_mfma_f32_16x16x32_bf16 v[56:59], v[44:47], a[112:115], v[56:59]\n"
"ds_write_addtid_b32 v87 offset:16032\n"
"v_add_f32 v143, v143, v151\n"
"v_mfma_f32_16x16x32_bf16 v[56:59], v[48:51], a[116:119], v[56:59]\n"
"s_waitcnt vmcnt(13)\n"
"buffer_store_dwordx2 v[0:1], v16, s[40:43], s95 offen offset:0 sc1\n"
"v_add_f32 v140, v140, v132\n"
"v_mfma_f32_16x16x32_bf16 v[56:59], v[52:55], a[120:123], v[56:59]\n"
"v_add_f32 v141, v141, v133\n"
"v_add_f32 v142, v142, v134\n"
"v_mfma_f32_16x16x32_bf16 v[56:59], v[96:99], a[124:127], v[56:59]\n"
"v_add_f32 v143, v143, v135\n"
"v_fma_f32 v60, v22, v153, v30\n"
"v_mfma_f32_16x16x32_bf16 v[84:87], v[88:91], a[128:131], 0\n"
"v_fma_f32 v61, v23, v153, v31\n"
"v_fma_f32 v62, v24, v153, v32\n"
"v_mfma_f32_16x16x32_bf16 v[84:87], v[92:95], a[132:135], v[84:87]\n"
"ds_write_addtid_b32 v56 offset:2304\n"
"v_fma_f32 v63, v25, v153, v33\n"
"v_mfma_f32_16x16x32_bf16 v[84:87], v[36:39], a[136:139], v[84:87]\n"
"ds_write_addtid_b32 v57 offset:6944\n"
"v_fma_f32 v140, v140, v152, v60\n"
"v_mfma_f32_16x16x32_bf16 v[84:87], v[40:43], a[140:143], v[84:87]\n"
"ds_write_addtid_b32 v58 offset:11648\n"
"v_fma_f32 v141, v141, v152, v61\n"
"v_mfma_f32_16x16x32_bf16 v[84:87], v[44:47], a[144:147], v[84:87]\n"
"s_waitcnt vmcnt(9)\n"
"ds_write_addtid_b32 v59 offset:16288\n"
"v_add_f32 v124, v124, v116\n"
"v_mfma_f32_16x16x32_bf16 v[84:87], v[48:51], a[148:151], v[84:87]\n"
"v_add_f32 v125, v125, v117\n"
"v_add_f32 v126, v126, v118\n"
"v_mfma_f32_16x16x32_bf16 v[84:87], v[52:55], a[152:155], v[84:87]\n"
"v_add_f32 v127, v127, v119\n"
"v_add_f32 v120, v120, v128\n"
"v_mfma_f32_16x16x32_bf16 v[84:87], v[96:99], a[156:159], v[84:87]\n"
"v_add_f32 v121, v121, v129\n"
"v_add_f32 v122, v122, v130\n"
"v_mfma_f32_16x16x32_bf16 v[56:59], v[88:91], a[160:163], 0\n"
"v_add_f32 v123, v123, v131\n"
"v_add_f32 v124, v124, v120\n"
"v_mfma_f32_16x16x32_bf16 v[56:59], v[92:95], a[164:167], v[56:59]\n"
"ds_write_addtid_b32 v84 offset:2560\n"
"v_add_f32 v125, v125, v121\n"
"v_mfma_f32_16x16x32_bf16 v[56:59], v[36:39], a[168:171], v[56:59]\n"
"s_add_u32 s58, s50, 0x180000\n"
"ds_write_addtid_b32 v85 offset:7200\n"
"v_add_f32 v126, v126, v122\n"
"v_mfma_f32_16x16x32_bf16 v[56:59], v[40:43], a[172:175], v[56:59]\n"
"s_and_b32 s58, s58, 0x3ffffff\n"
"ds_write_addtid_b32 v86 offset:11904\n"
"v_add_f32 v127, v127, v123\n"
"v_mfma_f32_16x16x32_bf16 v[56:59], v[44:47], a[176:179], v[56:59]\n"
"s_add_u32 s56, s40, s58\n"
"ds_write_addtid_b32 v87 offset:16544\n"
"v_fma_f32 v142, v142, v152, v62\n"
"v_mfma_f32_16x16x32_bf16 v[56:59], v[48:51], a[180:183], v[56:59]\n"
"s_addc_u32 s57, s41, 0\n"
"v_permlane32_swap_b32 v124, v126\n"
"v_mfma_f32_16x16x32_bf16 v[56:59], v[52:55], a[184:187], v[56:59]\n"
"s_waitcnt vmcnt(6)\n"
"v_cmp_ne_u64 vcc, s[48:49], v[154:155]\n"
"v_fma_f32 v143, v143, v152, v63\n"
"v_mfma_f32_16x16x32_bf16 v[56:59], v[96:99], a[188:191], v[56:59]\n"
"v_permlane32_swap_b32 v125, v127\n"
"v_mfma_f32_16x16x32_bf16 v[128:131], a[192:195], v[88:91], 0\n"
"s_cbranch_vccnz .LF_2s\n"
".LF_2r:\n"
"v_add_f32 v124, v124, v126\n"
"v_mfma_f32_16x16x32_bf16 v[128:131], a[196:199], v[92:95], v[128:131]\n"
"s_add_u32 s50, s50, 0x180000\n"
"ds_write_addtid_b32 v56 offset:2816\n"
"v_add_f32 v125, v125, v127\n"
"v_mfma_f32_16x16x32_bf16 v[128:131], a[200:203], v[36:39], v[128:131]\n"
"ds_write_addtid_b32 v57 offset:7456\n"
"global_load_dwordx4 v[112:115], v5, s[56:57] offset:0 sc1\n"
"v_mfma_f32_16x16x32_bf16 v[128:131], a[204:207], v[40:43], v[128:131]\n"
"ds_write_addtid_b32 v58 offset:12160\n"
"v_mul_f32_e64 v140, v140, |v140|\n"
"v_mfma_f32_16x16x32_bf16 v[128:131], a[208:211], v[44:47], v[128:131]\n"
"ds_write_addtid_b32 v59 offset:16800\n"
"v_mul_f32_e64 v141, v141, |v141|\n"
"v_mfma_f32_16x16x32_bf16 v[128:131], a[212:215], v[48:51], v[128:131]\n"
"v_permlane16_swap_b32 v124, v125\n"
"v_mfma_f32_16x16x32_bf16 v[128:131], a[216:219], v[52:55], v[128:131]\n"
"v_add_f32 v124, v124, v125\n"
"v_mul_f32_e64 v142, v142, |v142|\n"
"v_mfma_f32_16x16x32_bf16 v[128:131], a[220:223], v[96:99], v[128:131]\n"
"global_load_dwordx4 v[116:119], v5, s[56:57] offset:1024 sc1\n"
"v_add_f32_dpp v125, v124, v124 row_ror:8 row_mask:0xf bank_mask:0xf\n"
"v_mfma_f32_16x16x32_bf16 v[132:135], a[224:227], v[88:91], 0\n"
"s_waitcnt lgkmcnt(0)\n"
"v_mov_b32 v124, v125\n"
"v_mul_f32_e64 v143, v143, |v143|\n"
"v_mfma_f32_16x16x32_bf16 v[132:135], a[228:231], v[92:95], v[132:135]\n"
"s_barrier\n"
"v_mfma_f32_16x16x32_bf16 v[132:135], a[232:235], v[36:39], v[132:135]\n"
"v_permlane16_swap_b32 v125, v124\n"
"v_mfma_f32_16x16x32_bf16 v[132:135], a[236:239], v[40:43], v[132:135]\n"
"global_load_dwordx4 v[120:123], v6, s[56:57] offset:0 sc1\n"
"v_mul_f32 v125, 0xba800000, v125\n"
"v_mfma_f32_16x16x32_bf16 v[132:135], a[240:243], v[44:47], v[132:135]\n"
"buffer_load_dwordx2 v[144:145], v9, s[40:43], s50 offen sc1\n"
"v_mfma_f32_16x16x32_bf16 v[132:135], a[244:247], v[48:51], v[132:135]\n"
"v_fma_f32 v124, v124, s62, v17\n"
"v_fma_f32 v124, -v125, v125, v124\n"
"v_mfma_f32_16x16x32_bf16 v[132:135], a[248:251], v[52:55], v[132:135]\n"
"v_max_f32 v124, v17, v124\n"
"v_cvt_pk_bf16_f32 v138, v140, v141\n"
"v_mfma_f32_16x16x32_bf16 v[132:135], a[252:255], v[96:99], v[132:135]\n"
"v_rsq_f32 v124, v124\n"
"s_branch .LF_top\n"
".LF_1s:\n"
"s_cmp_ge_u32 s50, s53\n"
"s_cbranch_scc1 .LF_t0\n"
"s_cmp_ge_u32 s50, s52\n"
"s_cbranch_scc1 .LF_1r\n"
"s_sub_u32 s60, s50, 0x0\n"
"s_mov_b32 s61, 200000\n"
"s_nop 7\n"
".LF_1p:\n"
"buffer_load_dwordx2 v[134:135], v9, s[40:43], s60 offen sc1\n"
"s_waitcnt vmcnt(0)\n"
"v_cmp_ne_u64 vcc, s[48:49], v[134:135]\n"
"s_cbranch_vccz .LF_1px\n"
"s_sleep 1\n"
"s_sub_u32 s61, s61, 1\n"
"s_cbranch_scc0 .LF_1p\n"
"s_branch .LF_end\n"
".LF_1px:\n"
"s_branch .LF_1r\n"
".LF_2s:\n"
"s_cmp_ge_u32 s50, s53\n"
"s_cbranch_scc1 .LF_t1\n"
"s_cmp_ge_u32 s50, s52\n"
"s_cbranch_scc1 .LF_2r\n"
"s_sub_u32 s60, s50, 0x0\n"
"s_mov_b32 s61, 200000\n"
"s_nop 7\n"
".LF_2p:\n"
"buffer_load_dwordx2 v[154:155], v9, s[40:43], s60 offen sc1\n"
"s_waitcnt vmcnt(0)\n"
"v_cmp_ne_u64 vcc, s[48:49], v[154:155]\n"
"s_cbranch_vccz .LF_2px\n"
"s_sleep 1\n"
"s_sub_u32 s61, s61, 1\n"
"s_cbranch_scc0 .LF_2p\n"
"s_branch .LF_end\n"
".LF_2px:\n"
"s_branch .LF_2r\n"
".LF_3s:\n"
"s_cmp_ge_u32 s50, s53\n"
"s_cbranch_scc1 .LF_t2\n"
"s_cmp_ge_u32 s50, s52\n"
"s_cbranch_scc1 .LF_3r\n"
"s_sub_u32 s60, s50, 0x0\n"
"s_mov_b32 s61, 200000\n"
"s_nop 7\n"
".LF_3p:\n"
"buffer_load_dwordx2 v[144:145], v9, s[40:43], s60 offen sc1\n"
"s_waitcnt vmcnt(0)\n"
"v_cmp_ne_u64 vcc, s[48:49], v[144:145]\n"
"s_cbranch_vccz .LF_3px\n"
"s_sleep 1\n"
"s_sub_u32 s61, s61, 1\n"
"s_cbranch_scc0 .LF_3p\n"
"s_branch .LF_end\n"
".LF_3px:\n"
"s_branch .LF_3r\n"
".LF_4s:\n"
"s_cmp_ge_u32 s50, s53\n"
"s_cbranch_scc1 .LF_t3\n"
"s_cmp_ge_u32 s50, s52\n"
"s_cbranch_scc1 .LF_4r\n"
"s_sub_u32 s60, s50, 0x0\n"
"s_mov_b32 s61, 200000\n"
"s_nop 7\n"
".LF_4p:\n"
"buffer_load_dwordx2 v[138:139], v9, s[40:43], s60 offen sc1\n"
"s_waitcnt vmcnt(0)\n"
"v_cmp_ne_u64 vcc, s[48:49], v[138:139]\n"
"s_cbranch_vccz .LF_4px\n"
"s_sleep 1\n"
"s_sub_u32 s61, s61, 1\n"
"s_cbranch_scc0 .LF_4p\n"
"s_branch .LF_end\n"
".LF_4px:\n"
"s_branch .LF_4r\n"
".LF_t0:\n"
"v_mfma_f32_16x16x32_bf16 v[112:115], a[192:195], v[56:59], 0\n"
"v_mfma_f32_16x16x32_bf16 v[112:115], a[196:199], v[60:63], v[112:115]\n"
"ds_write_addtid_b32 v108 offset:1280\n"
"v_mfma_f32_16x16x32_bf16 v[112:115], a[200:203], v[64:67], v[112:115]\n"
"ds_write_addtid_b32 v109 offset:5920\n"
"v_mfma_f32_16x16x32_bf16 v[112:115], a[204:207], v[68:71], v[112:115]\n"
"ds_write_addtid_b32 v110 offset:10624\n"
"v_mfma_f32_16x16x32_bf16 v[112:115], a[208:211], v[72:75], v[112:115]\n"
"ds_write_addtid_b32 v111 offset:15264\n"
"v_mfma_f32_16x16x32_bf16 v[112:115], a[212:215], v[76:79], v[112:115]\n"
"v_mfma_f32_16x16x32_bf16 v[112:115], a[216:219], v[80:83], v[112:115]\n"
"v_mfma_f32_16x16x32_bf16 v[112:115], a[220:223], v[84:87], v[112:115]\n"
"v_mfma_f32_16x16x32_bf16 v[132:135], a[224:227], v[56:59], 0\n"
"s_waitcnt lgkmcnt(0)\n"
"v_mfma_f32_16x16x32_bf16 v[132:135], a[228:231], v[60:63], v[132:135]\n"
"s_barrier\n"
"v_mfma_f32_16x16x32_bf16 v[132:135], a[232:235], v[64:67], v[132:135]\n"
"ds_read2st64_b32 v[152:153], v13 offset0:0 offset1:1\n"
"v_mfma_f32_16x16x32_bf16 v[132:135], a[236:239], v[68:71], v[132:135]\n"
"ds_read_b128 v[56:59], v2 offset:1536\n"
"ds_read_b128 v[60:63], v3 offset:512\n"
"v_mfma_f32_16x16x32_bf16 v[132:135], a[240:243], v[72:75], v[132:135]\n"
"ds_read_b128 v[136:139], v4 offset:2560\n"
"ds_read_b128 v[140:143], v2 offset:1792\n"
"v_mfma_f32_16x16x32_bf16 v[132:135], a[244:247], v[76:79], v[132:135]\n"
"v_mfma_f32_16x16x32_bf16 v[132:135], a[248:251], v[80:83], v[132:135]\n"
"ds_read_b128 v[144:147], v3 offset:768\n"
"v_mfma_f32_16x16x32_bf16 v[132:135], a[252:255], v[84:87], v[132:135]\n"
"s_waitcnt vmcnt(9)\n"
"ds_read_b128 v[148:151], v4 offset:2816\n"
"v_mfma_f32_16x16x32_bf16 v[76:79], v[88:91], a[0:3], 0\n"
"v_mfma_f32_16x16x32_bf16 v[76:79], v[92:95], a[4:7], v[76:79]\n"
"s_waitcnt vmcnt(6)\n"
"v_mfma_f32_16x16x32_bf16 v[76:79], v[36:39], a[8:11], v[76:79]\n"
"s_waitcnt lgkmcnt(3)\n"
"v_add_f32 v56, v56, v60\n"
"v_add_f32 v57, v57, v61\n"
"v_mfma_f32_16x16x32_bf16 v[76:79], v[40:43], a[12:15], v[76:79]\n"
"s_waitcnt vmcnt(4)\n"
"v_add_f32 v58, v58, v62\n"
"v_add_f32 v59, v59, v63\n"
"v_mfma_f32_16x16x32_bf16 v[76:79], v[44:47], a[16:19], v[76:79]\n"
"v_add_f32 v56, v56, v136\n"
"v_mfma_f32_16x16x32_bf16 v[76:79], v[48:51], a[20:23], v[76:79]\n"
"s_waitcnt vmcnt(2)\n"
"v_add_f32 v57, v57, v137\n"
"v_add_f32 v58, v58, v138\n"
"v_mfma_f32_16x16x32_bf16 v[76:79], v[52:55], a[24:27], v[76:79]\n"
"v_add_f32 v59, v59, v139\n"
"v_add_f32 v56, v56, v112\n"
"v_mfma_f32_16x16x32_bf16 v[76:79], v[96:99], a[28:31], v[76:79]\n"
"v_add_f32 v57, v57, v113\n"
"v_add_f32 v58, v58, v114\n"
"v_mfma_f32_16x16x32_bf16 v[60:63], v[88:91], a[32:35], 0\n"
"v_add_f32 v59, v59, v115\n"
"v_mfma_f32_16x16x32_bf16 v[60:63], v[92:95], a[36:39], v[60:63]\n"
"ds_write_addtid_b32 v76 offset:1536\n"
"v_mfma_f32_16x16x32_bf16 v[60:63], v[36:39], a[40:43], v[60:63]\n"
"s_add_u32 s93, s50, 0xffe80000\n"
"s_and_b32 s94, s93, 0xfbffffff\n"
"ds_write_addtid_b32 v77 offset:6176\n"
"v_mfma_f32_16x16x32_bf16 v[60:63], v[40:43], a[44:47], v[60:63]\n"
"ds_write_addtid_b32 v78 offset:10880\n"
"v_fma_f32 v80, v18, v153, v26\n"
"v_mfma_f32_16x16x32_bf16 v[60:63], v[44:47], a[48:51], v[60:63]\n"
"ds_write_addtid_b32 v79 offset:15520\n"
"v_fma_f32 v81, v19, v153, v27\n"
"v_mfma_f32_16x16x32_bf16 v[60:63], v[48:51], a[52:55], v[60:63]\n"
"v_fma_f32 v82, v20, v153, v28\n"
"v_fma_f32 v83, v21, v153, v29\n"
"v_mfma_f32_16x16x32_bf16 v[60:63], v[52:55], a[56:59], v[60:63]\n"
"v_fma_f32 v56, v56, v152, v80\n"
"v_fma_f32 v57, v57, v152, v81\n"
"v_mfma_f32_16x16x32_bf16 v[60:63], v[96:99], a[60:63], v[60:63]\n"
"v_fma_f32 v58, v58, v152, v82\n"
"v_mfma_f32_16x16x32_bf16 v[84:87], v[88:91], a[64:67], 0\n"
"v_fma_f32 v59, v59, v152, v83\n"
"v_mul_f32_e64 v56, v56, |v56|\n"
"v_mfma_f32_16x16x32_bf16 v[84:87], v[92:95], a[68:71], v[84:87]\n"
"ds_write_addtid_b32 v60 offset:1792\n"
"v_mul_f32_e64 v57, v57, |v57|\n"
"v_mfma_f32_16x16x32_bf16 v[84:87], v[36:39], a[72:75], v[84:87]\n"
"ds_write_addtid_b32 v61 offset:6432\n"
"v_mul_f32_e64 v58, v58, |v58|\n"
"v_mfma_f32_16x16x32_bf16 v[84:87], v[40:43], a[76:79], v[84:87]\n"
"ds_write_addtid_b32 v62 offset:11136\n"
"v_mfma_f32_16x16x32_bf16 v[84:87], v[44:47], a[80:83], v[84:87]\n"
"ds_write_addtid_b32 v63 offset:15776\n"
"v_mul_f32_e64 v59, v59, |v59|\n"
"v_mfma_f32_16x16x32_bf16 v[84:87], v[48:51], a[84:87], v[84:87]\n"
"v_cvt_pk_bf16_f32 v136, v56, v57\n"
"v_cvt_pk_bf16_f32 v137, v58, v59\n"
"v_mfma_f32_16x16x32_bf16 v[84:87], v[52:55], a[88:91], v[84:87]\n"
"v_pk_max_i16 v136, v136, 0\n"
"v_pk_max_i16 v137, v137, 0\n"
"v_mfma_f32_16x16x32_bf16 v[84:87], v[96:99], a[92:95], v[84:87]\n"
"s_waitcnt lgkmcnt(8)\n"
"v_add_f32 v140, v140, v144\n"
"v_add_f32 v141, v141, v145\n"
"v_mfma_f32_16x16x32_bf16 v[56:59], v[88:91], a[96:99], 0\n"
"v_add_f32 v142, v142, v146\n"
"v_add_f32 v143, v143, v147\n"
"v_mfma_f32_16x16x32_bf16 v[56:59], v[92:95], a[100:103], v[56:59]\n"
"ds_write_addtid_b32 v84 offset:2048\n"
"v_add_f32 v140, v140, v148\n"
"v_mfma_f32_16x16x32_bf16 v[56:59], v[36:39], a[104:107], v[56:59]\n"
"ds_write_addtid_b32 v85 offset:6688\n"
"v_add_f32 v141, v141, v149\n"
"v_mfma_f32_16x16x32_bf16 v[56:59], v[40:43], a[108:111], v[56:59]\n"
"ds_write_addtid_b32 v86 offset:11392\n"
"v_add_f32 v142, v142, v150\n"
"v_mfma_f32_16x16x32_bf16 v[56:59], v[44:47], a[112:115], v[56:59]\n"
"ds_write_addtid_b32 v87 offset:16032\n"
"v_add_f32 v143, v143, v151\n"
"v_mfma_f32_16x16x32_bf16 v[56:59], v[48:51], a[116:119], v[56:59]\n"
"s_waitcnt vmcnt(0)\n"
"buffer_store_dwordx2 v[0:1], v16, s[40:43], s95 offen offset:0 sc1\n"
"v_add_f32 v140, v140, v132\n"
"v_mfma_f32_16x16x32_bf16 v[56:59], v[52:55], a[120:123], v[56:59]\n"
"v_add_f32 v141, v141, v133\n"
"v_add_f32 v142, v142, v134\n"
"v_mfma_f32_16x16x32_bf16 v[56:59], v[96:99], a[124:127], v[56:59]\n"
"v_add_f32 v143, v143, v135\n"
"v_fma_f32 v60, v22, v153, v30\n"
"v_mfma_f32_16x16x32_bf16 v[84:87], v[88:91], a[128:131], 0\n"
"v_fma_f32 v61, v23, v153, v31\n"
"v_fma_f32 v62, v24, v153, v32\n"
"v_mfma_f32_16x16x32_bf16 v[84:87], v[92:95], a[132:135], v[84:87]\n"
"ds_write_addtid_b32 v56 offset:2304\n"
"v_fma_f32 v63, v25, v153, v33\n"
"v_mfma_f32_16x16x32_bf16 v[84:87], v[36:39], a[136:139], v[84:87]\n"
"ds_write_addtid_b32 v57 offset:6944\n"
"v_fma_f32 v140, v140, v152, v60\n"
"v_mfma_f32_16x16x32_bf16 v[84:87], v[40:43], a[140:143], v[84:87]\n"
"ds_write_addtid_b32 v58 offset:11648\n"
"v_fma_f32 v141, v141, v152, v61\n"
"v_mfma_f32_16x16x32_bf16 v[84:87], v[44:47], a[144:147], v[84:87]\n"
"ds_write_addtid_b32 v59 offset:16288\n"
"v_mfma_f32_16x16x32_bf16 v[84:87], v[48:51], a[148:151], v[84:87]\n"
"v_mfma_f32_16x16x32_bf16 v[84:87], v[52:55], a[152:155], v[84:87]\n"
"v_mfma_f32_16x16x32_bf16 v[84:87], v[96:99], a[156:159], v[84:87]\n"
"v_mfma_f32_16x16x32_bf16 v[56:59], v[88:91], a[160:163], 0\n"
"v_mfma_f32_16x16x32_bf16 v[56:59], v[92:95], a[164:167], v[56:59]\n"
"ds_write_addtid_b32 v84 offset:2560\n"
"v_mfma_f32_16x16x32_bf16 v[56:59], v[36:39], a[168:171], v[56:59]\n"
"ds_write_addtid_b32 v85 offset:7200\n"
"v_mfma_f32_16x16x32_bf16 v[56:59], v[40:43], a[172:175], v[56:59]\n"
"ds_write_addtid_b32 v86 offset:11904\n"
"v_mfma_f32_16x16x32_bf16 v[56:59], v[44:47], a[176:179], v[56:59]\n"
"ds_write_addtid_b32 v87 offset:16544\n"
"v_fma_f32 v142, v142, v152, v62\n"
"v_mfma_f32_16x16x32_bf16 v[56:59], v[48:51], a[180:183], v[56:59]\n"
"v_mfma_f32_16x16x32_bf16 v[56:59], v[52:55], a[184:187], v[56:59]\n"
"v_fma_f32 v143, v143, v152, v63\n"
"v_mfma_f32_16x16x32_bf16 v[56:59], v[96:99], a[188:191], v[56:59]\n"
"v_mfma_f32_16x16x32_bf16 v[128:131], a[192:195], v[88:91], 0\n"
"v_mfma_f32_16x16x32_bf16 v[128:131], a[196:199], v[92:95], v[128:131]\n"
"ds_write_addtid_b32 v56 offset:2816\n"
"v_mfma_f32_16x16x32_bf16 v[128:131], a[200:203], v[36:39], v[128:131]\n"
"ds_write_addtid_b32 v57 offset:7456\n"
"v_mfma_f32_16x16x32_bf16 v[128:131], a[204:207], v[40:43], v[128:131]\n"
"ds_write_addtid_b32 v58 offset:12160\n"
"v_mul_f32_e64 v140, v140, |v140|\n"
"v_mfma_f32_16x16x32_bf16 v[128:131], a[208:211], v[44:47], v[128:131]\n"
"ds_write_addtid_b32 v59 offset:16800\n"
"v_mul_f32_e64 v141, v141, |v141|\n"
"v_mfma_f32_16x16x32_bf16 v[128:131], a[212:215], v[48:51], v[128:131]\n"
"v_mfma_f32_16x16x32_bf16 v[128:131], a[216:219], v[52:55], v[128:131]\n"
"v_mul_f32_e64 v142, v142, |v142|\n"
"v_mfma_f32_16x16x32_bf16 v[128:131], a[220:223], v[96:99], v[128:131]\n"
"v_mfma_f32_16x16x32_bf16 v[132:135], a[224:227], v[88:91], 0\n"
"s_waitcnt lgkmcnt(0)\n"
"v_mul_f32_e64 v143, v143, |v143|\n"
"v_mfma_f32_16x16x32_bf16 v[132:135], a[228:231], v[92:95], v[132:135]\n"
"s_barrier\n"
"v_mfma_f32_16x16x32_bf16 v[132:135], a[232:235], v[36:39], v[132:135]\n"
"v_mfma_f32_16x16x32_bf16 v[132:135], a[236:239], v[40:43], v[132:135]\n"
"v_mfma_f32_16x16x32_bf16 v[132:135], a[240:243], v[44:47], v[132:135]\n"
"v_mfma_f32_16x16x32_bf16 v[132:135], a[244:247], v[48:51], v[132:135]\n"
"v_mfma_f32_16x16x32_bf16 v[132:135], a[248:251], v[52:55], v[132:135]\n"
"v_cvt_pk_bf16_f32 v138, v140, v141\n"
"v_mfma_f32_16x16x32_bf16 v[132:135], a[252:255], v[96:99], v[132:135]\n"
"v_cvt_pk_bf16_f32 v139, v142, v143\n"
"v_pk_max_i16 v138, v138, 0\n"
"ds_read2st64_b32 v[140:141], v14 offset0:0 offset1:1\n"
"v_pk_max_i16 v139, v139, 0\n"
"ds_read_b128 v[56:59], v2 offset:0\n"
"ds_read_b128 v[52:55], v3 offset:2048\n"
"ds_read_b128 v[60:63], v4 offset:1024\n"
"ds_read_b128 v[84:87], v2 offset:256\n"
"ds_read_b128 v[88:91], v3 offset:2304\n"
"ds_read_b128 v[92:95], v4 offset:1280\n"
"s_waitcnt lgkmcnt(3)\n"
"buffer_store_dwordx4 v[136:139], v15, s[40:43], s94 offen sc1\n"
"v_add_f32 v56, v56, v52\n"
"v_add_f32 v57, v57, v53\n"
"v_add_f32 v58, v58, v54\n"
"v_add_f32 v59, v59, v55\n"
"v_add_f32 v56, v56, v60\n"
"v_add_f32 v57, v57, v61\n"
"v_add_f32 v58, v58, v62\n"
"v_add_f32 v59, v59, v63\n"
"v_add_f32 v56, v56, v128\n"
"v_add_f32 v57, v57, v129\n"
"v_add_f32 v58, v58, v130\n"
"v_add_f32 v59, v59, v131\n"
"s_add_u32 s95, s50, 0x0\n"
"s_and_b32 s94, s95, 0xfbffffff\n"
"v_fma_f32 v60, v18, v141, v26\n"
"v_fma_f32 v61, v19, v141, v27\n"
"v_fma_f32 v62, v20, v141, v28\n"
"v_fma_f32 v63, v21, v141, v29\n"
"v_fma_f32 v56, v56, v140, v60\n"
"v_fma_f32 v57, v57, v140, v61\n"
"v_fma_f32 v58, v58, v140, v62\n"
"v_fma_f32 v59, v59, v140, v63\n"
"v_mul_f32_e64 v56, v56, |v56|\n"
"v_mul_f32_e64 v57, v57, |v57|\n"
"v_mul_f32_e64 v58, v58, |v58|\n"
"v_mul_f32_e64 v59, v59, |v59|\n"
"v_cvt_pk_bf16_f32 v128, v56, v57\n"
"v_cvt_pk_bf16_f32 v129, v58, v59\n"
"v_pk_max_i16 v128, v128, 0\n"
"v_pk_max_i16 v129, v129, 0\n"
"s_waitcnt lgkmcnt(0)\n"
"v_add_f32 v84, v84, v88\n"
"v_add_f32 v85, v85, v89\n"
"v_add_f32 v86, v86, v90\n"
"v_add_f32 v87, v87, v91\n"
"v_add_f32 v84, v84, v92\n"
"v_add_f32 v85, v85, v93\n"
"v_add_f32 v86, v86, v94\n"
"v_add_f32 v87, v87, v95\n"
"v_add_f32 v84, v84, v132\n"
"v_add_f32 v85, v85, v133\n"
"v_add_f32 v86, v86, v134\n"
"v_add_f32 v87, v87, v135\n"
"v_fma_f32 v60, v22, v141, v30\n"
"v_fma_f32 v61, v23, v141, v31\n"
"v_fma_f32 v62, v24, v141, v32\n"
"v_fma_f32 v63, v25, v141, v33\n"
"v_fma_f32 v84, v84, v140, v60\n"
"v_fma_f32 v85, v85, v140, v61\n"
"v_fma_f32 v86, v86, v140, v62\n"
"v_fma_f32 v87, v87, v140, v63\n"
"v_mul_f32_e64 v84, v84, |v84|\n"
"v_mul_f32_e64 v85, v85, |v85|\n"
"v_mul_f32_e64 v86, v86, |v86|\n"
"v_mul_f32_e64 v87, v87, |v87|\n"
"v_cvt_pk_bf16_f32 v130, v84, v85\n"
"v_cvt_pk_bf16_f32 v131, v86, v87\n"
"v_pk_max_i16 v130, v130, 0\n"
"v_pk_max_i16 v131, v131, 0\n"
"s_waitcnt vmcnt(0)\n"
"buffer_store_dwordx2 v[0:1], v16, s[40:43], s93 offen offset:0 sc1\n"
"buffer_store_dwordx4 v[128:131], v15, s[40:43], s94 offen sc1\n"
"s_waitcnt vmcnt(0)\n"
"buffer_store_dwordx2 v[0:1], v16, s[40:43], s95 offen offset:0 sc1\n"
"s_branch .LF_end\n"
".LF_t1:\n"
"v_add_f32 v124, v124, v126\n"
"v_mfma_f32_16x16x32_bf16 v[128:131], a[196:199], v[92:95], v[128:131]\n"
"ds_write_addtid_b32 v56 offset:2816\n"
"v_add_f32 v125, v125, v127\n"
"v_mfma_f32_16x16x32_bf16 v[128:131], a[200:203], v[36:39], v[128:131]\n"
"ds_write_addtid_b32 v57 offset:7456\n"
"v_mfma_f32_16x16x32_bf16 v[128:131], a[204:207], v[40:43], v[128:131]\n"
"ds_write_addtid_b32 v58 offset:12160\n"
"v_mul_f32_e64 v140, v140, |v140|\n"
"v_mfma_f32_16x16x32_bf16 v[128:131], a[208:211], v[44:47], v[128:131]\n"
"ds_write_addtid_b32 v59 offset:16800\n"
"v_mul_f32_e64 v141, v141, |v141|\n"
"v_mfma_f32_16x16x32_bf16 v[128:131], a[212:215], v[48:51], v[128:131]\n"
"v_permlane16_swap_b32 v124, v125\n"
"v_mfma_f32_16x16x32_bf16 v[128:131], a[216:219], v[52:55], v[128:131]\n"
"v_add_f32 v124, v124, v125\n"
"v_mul_f32_e64 v142, v142, |v142|\n"
"v_mfma_f32_16x16x32_bf16 v[128:131], a[220:223], v[96:99], v[128:131]\n"
"v_add_f32_dpp v125, v124, v124 row_ror:8 row_mask:0xf bank_mask:0xf\n"
"v_mfma_f32_16x16x32_bf16 v[132:135], a[224:227], v[88:91], 0\n"
"s_waitcnt lgkmcnt(0)\n"
"v_mov_b32 v124, v125\n"
"v_mul_f32_e64 v143, v143, |v143|\n"
"v_mfma_f32_16x16x32_bf16 v[132:135], a[228:231], v[92:95], v[132:135]\n"
"s_barrier\n"
"v_mfma_f32_16x16x32_bf16 v[132:135], a[232:235], v[36:39], v[132:135]\n"
"v_permlane16_swap_b32 v125, v124\n"
"v_mfma_f32_16x16x32_bf16 v[132:135], a[236:239], v[40:43], v[132:135]\n"
"v_mul_f32 v125, 0xba800000, v125\n"
"v_mfma_f32_16x16x32_bf16 v[132:135], a[240:243], v[44:47], v[132:135]\n"
"v_mfma_f32_16x16x32_bf16 v[132:135], a[244:247], v[48:51], v[132:135]\n"
"v_fma_f32 v124, v124, s62, v17\n"
"v_fma_f32 v124, -v125, v125, v124\n"
"v_mfma_f32_16x16x32_bf16 v[132:135], a[248:251], v[52:55], v[132:135]\n"
"v_max_f32 v124, v17, v124\n"
"v_cvt_pk_bf16_f32 v138, v140, v141\n"
"v_mfma_f32_16x16x32_bf16 v[132:135], a[252:255], v[96:99], v[132:135]\n"
"s_waitcnt vmcnt(8)\n"
"v_rsq_f32 v124, v124\n"
"v_mfma_f32_16x16x32_bf16 v[48:51], v[100:103], a[0:3], 0\n"
"v_mul_f32 v125, v125, v124\n"
"v_mfma_f32_16x16x32_bf16 v[48:51], v[104:107], a[4:7], v[48:51]\n"
"s_waitcnt vmcnt(5)\n"
"ds_write_addtid_b32 v124 offset:18592\n"
"ds_write_addtid_b32 v125 offset:18848\n"
"v_mfma_f32_16x16x32_bf16 v[48:51], v[108:111], a[8:11], v[48:51]\n"
"v_cvt_pk_bf16_f32 v139, v142, v143\n"
"v_pk_max_i16 v138, v138, 0\n"
"v_mfma_f32_16x16x32_bf16 v[48:51], v[64:67], a[12:15], v[48:51]\n"
"s_waitcnt vmcnt(3)\n"
"ds_read2st64_b32 v[140:141], v14 offset0:0 offset1:1\n"
"v_pk_max_i16 v139, v139, 0\n"
"v_mfma_f32_16x16x32_bf16 v[48:51], v[68:71], a[16:19], v[48:51]\n"
"ds_read_b128 v[56:59], v2 offset:0\n"
"ds_read_b128 v[52:55], v3 offset:2048\n"
"v_mfma_f32_16x16x32_bf16 v[48:51], v[72:75], a[20:23], v[48:51]\n"
"s_waitcnt vmcnt(1)\n"
"ds_read_b128 v[60:63], v4 offset:1024\n"
"v_mfma_f32_16x16x32_bf16 v[48:51], v[76:79], a[24:27], v[48:51]\n"
"ds_read_b128 v[84:87], v2 offset:256\n"
"ds_read_b128 v[88:91], v3 offset:2304\n"
"v_mfma_f32_16x16x32_bf16 v[48:51], v[80:83], a[28:31], v[48:51]\n"
"ds_read_b128 v[92:95], v4 offset:1280\n"
"v_mfma_f32_16x16x32_bf16 v[96:99], v[100:103], a[32:35], 0\n"
"v_mfma_f32_16x16x32_bf16 v[96:99], v[104:107], a[36:39], v[96:99]\n"
"ds_write_addtid_b32 v48 offset:0\n"
"v_mfma_f32_16x16x32_bf16 v[96:99], v[108:111], a[40:43], v[96:99]\n"
"ds_write_addtid_b32 v49 offset:4640\n"
"v_mfma_f32_16x16x32_bf16 v[96:99], v[64:67], a[44:47], v[96:99]\n"
"s_waitcnt lgkmcnt(5)\n"
"buffer_store_dwordx4 v[136:139], v15, s[40:43], s94 offen sc1\n"
"ds_write_addtid_b32 v50 offset:9344\n"
"v_add_f32 v56, v56, v52\n"
"v_add_f32 v57, v57, v53\n"
"v_mfma_f32_16x16x32_bf16 v[96:99], v[68:71], a[48:51], v[96:99]\n"
"ds_write_addtid_b32 v51 offset:13984\n"
"v_add_f32 v58, v58, v54\n"
"v_mfma_f32_16x16x32_bf16 v[96:99], v[72:75], a[52:55], v[96:99]\n"
"v_add_f32 v59, v59, v55\n"
"v_add_f32 v56, v56, v60\n"
"v_mfma_f32_16x16x32_bf16 v[96:99], v[76:79], a[56:59], v[96:99]\n"
"v_add_f32 v57, v57, v61\n"
"v_add_f32 v58, v58, v62\n"
"v_mfma_f32_16x16x32_bf16 v[96:99], v[80:83], a[60:63], v[96:99]\n"
"v_add_f32 v59, v59, v63\n"
"v_add_f32 v56, v56, v128\n"
"v_mfma_f32_16x16x32_bf16 v[124:127], v[100:103], a[64:67], 0\n"
"v_add_f32 v57, v57, v129\n"
"v_mfma_f32_16x16x32_bf16 v[124:127], v[104:107], a[68:71], v[124:127]\n"
"ds_write_addtid_b32 v96 offset:256\n"
"v_add_f32 v58, v58, v130\n"
"v_mfma_f32_16x16x32_bf16 v[124:127], v[108:111], a[72:75], v[124:127]\n"
"ds_write_addtid_b32 v97 offset:4896\n"
"v_add_f32 v59, v59, v131\n"
"v_mfma_f32_16x16x32_bf16 v[124:127], v[64:67], a[76:79], v[124:127]\n"
"s_add_u32 s95, s50, 0xffe80000\n"
"s_and_b32 s94, s95, 0xfbffffff\n"
"ds_write_addtid_b32 v98 offset:9600\n"
"v_mfma_f32_16x16x32_bf16 v[124:127], v[68:71], a[80:83], v[124:127]\n"
"ds_write_addtid_b32 v99 offset:14240\n"
"v_mfma_f32_16x16x32_bf16 v[124:127], v[72:75], a[84:87], v[124:127]\n"
"v_fma_f32 v60, v18, v141, v26\n"
"v_fma_f32 v61, v19, v141, v27\n"
"v_mfma_f32_16x16x32_bf16 v[124:127], v[76:79], a[88:91], v[124:127]\n"
"v_fma_f32 v62, v20, v141, v28\n"
"v_fma_f32 v63, v21, v141, v29\n"
"v_mfma_f32_16x16x32_bf16 v[124:127], v[80:83], a[92:95], v[124:127]\n"
"v_fma_f32 v56, v56, v140, v60\n"
"v_fma_f32 v57, v57, v140, v61\n"
"v_mfma_f32_16x16x32_bf16 v[96:99], v[100:103], a[96:99], 0\n"
"v_fma_f32 v58, v58, v140, v62\n"
"v_fma_f32 v59, v59, v140, v63\n"
"v_mfma_f32_16x16x32_bf16 v[96:99], v[104:107], a[100:103], v[96:99]\n"
"ds_write_addtid_b32 v124 offset:512\n"
"v_mul_f32_e64 v56, v56, |v56|\n"
"v_mfma_f32_16x16x32_bf16 v[96:99], v[108:111], a[104:107], v[96:99]\n"
"ds_write_addtid_b32 v125 offset:5152\n"
"v_mul_f32_e64 v57, v57, |v57|\n"
"v_mfma_f32_16x16x32_bf16 v[96:99], v[64:67], a[108:111], v[96:99]\n"
"ds_write_addtid_b32 v126 offset:9856\n"
"v_mul_f32_e64 v58, v58, |v58|\n"
"v_mfma_f32_16x16x32_bf16 v[96:99], v[68:71], a[112:115], v[96:99]\n"
"ds_write_addtid_b32 v127 offset:14496\n"
"v_mul_f32_e64 v59, v59, |v59|\n"
"v_mfma_f32_16x16x32_bf16 v[96:99], v[72:75], a[116:119], v[96:99]\n"
"v_cvt_pk_bf16_f32 v128, v56, v57\n"
"v_cvt_pk_bf16_f32 v129, v58, v59\n"
"v_mfma_f32_16x16x32_bf16 v[96:99], v[76:79], a[120:123], v[96:99]\n"
"v_pk_max_i16 v128, v128, 0\n"
"v_pk_max_i16 v129, v129, 0\n"
"v_mfma_f32_16x16x32_bf16 v[96:99], v[80:83], a[124:127], v[96:99]\n"
"s_waitcnt lgkmcnt(12)\n"
"v_add_f32 v84, v84, v88\n"
"v_add_f32 v85, v85, v89\n"
"v_mfma_f32_16x16x32_bf16 v[56:59], v[100:103], a[128:131], 0\n"
"v_add_f32 v86, v86, v90\n"
"v_add_f32 v87, v87, v91\n"
"v_mfma_f32_16x16x32_bf16 v[56:59], v[104:107], a[132:135], v[56:59]\n"
"ds_write_addtid_b32 v96 offset:768\n"
"v_add_f32 v84, v84, v92\n"
"v_mfma_f32_16x16x32_bf16 v[56:59], v[108:111], a[136:139], v[56:59]\n"
"ds_write_addtid_b32 v97 offset:5408\n"
"v_add_f32 v85, v85, v93\n"
"v_mfma_f32_16x16x32_bf16 v[56:59], v[64:67], a[140:143], v[56:59]\n"
"ds_write_addtid_b32 v98 offset:10112\n"
"v_add_f32 v86, v86, v94\n"
"v_mfma_f32_16x16x32_bf16 v[56:59], v[68:71], a[144:147], v[56:59]\n"
"ds_write_addtid_b32 v99 offset:14752\n"
"v_add_f32 v87, v87, v95\n"
"v_mfma_f32_16x16x32_bf16 v[56:59], v[72:75], a[148:151], v[56:59]\n"
"v_add_f32 v84, v84, v132\n"
"v_add_f32 v85, v85, v133\n"
"v_mfma_f32_16x16x32_bf16 v[56:59], v[76:79], a[152:155], v[56:59]\n"
"v_add_f32 v86, v86, v134\n"
"v_add_f32 v87, v87, v135\n"
"v_mfma_f32_16x16x32_bf16 v[56:59], v[80:83], a[156:159], v[56:59]\n"
"v_fma_f32 v60, v22, v141, v30\n"
"v_fma_f32 v61, v23, v141, v31\n"
"v_mfma_f32_16x16x32_bf16 v[88:91], v[100:103], a[160:163], 0\n"
"v_fma_f32 v62, v24, v141, v32\n"
"v_fma_f32 v63, v25, v141, v33\n"
"v_mfma_f32_16x16x32_bf16 v[88:91], v[104:107], a[164:167], v[88:91]\n"
"ds_write_addtid_b32 v56 offset:1024\n"
"v_fma_f32 v84, v84, v140, v60\n"
"v_mfma_f32_16x16x32_bf16 v[88:91], v[108:111], a[168:171], v[88:91]\n"
"ds_write_addtid_b32 v57 offset:5664\n"
"v_fma_f32 v85, v85, v140, v61\n"
"v_mfma_f32_16x16x32_bf16 v[88:91], v[64:67], a[172:175], v[88:91]\n"
"ds_write_addtid_b32 v58 offset:10368\n"
"v_fma_f32 v86, v86, v140, v62\n"
"v_mfma_f32_16x16x32_bf16 v[88:91], v[68:71], a[176:179], v[88:91]\n"
"ds_write_addtid_b32 v59 offset:15008\n"
"v_fma_f32 v87, v87, v140, v63\n"
"v_mfma_f32_16x16x32_bf16 v[88:91], v[72:75], a[180:183], v[88:91]\n"
"v_mul_f32_e64 v84, v84, |v84|\n"
"v_mul_f32_e64 v85, v85, |v85|\n"
"v_mfma_f32_16x16x32_bf16 v[88:91], v[76:79], a[184:187], v[88:91]\n"
"v_mul_f32_e64 v86, v86, |v86|\n"
"v_mfma_f32_16x16x32_bf16 v[88:91], v[80:83], a[188:191], v[88:91]\n"
"v_mul_f32_e64 v87, v87, |v87|\n"
"v_cvt_pk_bf16_f32 v130, v84, v85\n"
"v_mfma_f32_16x16x32_bf16 v[92:95], a[192:195], v[100:103], 0\n"
"v_cvt_pk_bf16_f32 v131, v86, v87\n"
"v_mfma_f32_16x16x32_bf16 v[92:95], a[196:199], v[104:107], v[92:95]\n"
"ds_write_addtid_b32 v88 offset:1280\n"
"v_mfma_f32_16x16x32_bf16 v[92:95], a[200:203], v[108:111], v[92:95]\n"
"ds_write_addtid_b32 v89 offset:5920\n"
"v_pk_max_i16 v130, v130, 0\n"
"v_mfma_f32_16x16x32_bf16 v[92:95], a[204:207], v[64:67], v[92:95]\n"
"ds_write_addtid_b32 v90 offset:10624\n"
"v_pk_max_i16 v131, v131, 0\n"
"v_mfma_f32_16x16x32_bf16 v[92:95], a[208:211], v[68:71], v[92:95]\n"
"ds_write_addtid_b32 v91 offset:15264\n"
"v_mfma_f32_16x16x32_bf16 v[92:95], a[212:215], v[72:75], v[92:95]\n"
"v_mfma_f32_16x16x32_bf16 v[92:95], a[216:219], v[76:79], v[92:95]\n"
"v_mfma_f32_16x16x32_bf16 v[92:95], a[220:223], v[80:83], v[92:95]\n"
"v_mfma_f32_16x16x32_bf16 v[88:91], a[224:227], v[100:103], 0\n"
"s_waitcnt lgkmcnt(0)\n"
"v_mfma_f32_16x16x32_bf16 v[88:91], a[228:231], v[104:107], v[88:91]\n"
"s_barrier\n"
"v_mfma_f32_16x16x32_bf16 v[88:91], a[232:235], v[108:111], v[88:91]\n"
"ds_read2st64_b32 v[136:137], v11 offset0:0 offset1:1\n"
"ds_read_b128 v[96:99], v2 offset:1536\n"
"v_mfma_f32_16x16x32_bf16 v[88:91], a[236:239], v[64:67], v[88:91]\n"
"s_waitcnt vmcnt(0)\n"
"v_mfma_f32_16x16x32_bf16 v[88:91], a[240:243], v[68:71], v[88:91]\n"
"ds_read_b128 v[84:87], v3 offset:512\n"
"v_mfma_f32_16x16x32_bf16 v[88:91], a[244:247], v[72:75], v[88:91]\n"
"ds_read_b128 v[100:103], v4 offset:2560\n"
"ds_read_b128 v[104:107], v2 offset:1792\n"
"v_mfma_f32_16x16x32_bf16 v[88:91], a[248:251], v[76:79], v[88:91]\n"
"ds_read_b128 v[108:111], v3 offset:768\n"
"ds_read_b128 v[124:127], v4 offset:2816\n"
"v_mfma_f32_16x16x32_bf16 v[88:91], a[252:255], v[80:83], v[88:91]\n"
"buffer_store_dwordx2 v[0:1], v16, s[40:43], s93 offen offset:0 sc1\n"
"buffer_store_dwordx4 v[128:131], v15, s[40:43], s94 offen sc1\n"
"s_waitcnt lgkmcnt(3)\n"
"v_add_f32 v96, v96, v84\n"
"v_add_f32 v97, v97, v85\n"
"v_add_f32 v98, v98, v86\n"
"v_add_f32 v99, v99, v87\n"
"v_add_f32 v96, v96, v100\n"
"v_add_f32 v97, v97, v101\n"
"v_add_f32 v98, v98, v102\n"
"v_add_f32 v99, v99, v103\n"
"v_add_f32 v96, v96, v92\n"
"v_add_f32 v97, v97, v93\n"
"v_add_f32 v98, v98, v94\n"
"v_add_f32 v99, v99, v95\n"
"s_add_u32 s93, s50, 0x0\n"
"s_and_b32 s94, s93, 0xfbffffff\n"
"v_fma_f32 v84, v18, v137, v26\n"
"v_fma_f32 v85, v19, v137, v27\n"
"v_fma_f32 v86, v20, v137, v28\n"
"v_fma_f32 v87, v21, v137, v29\n"
"v_fma_f32 v96, v96, v136, v84\n"
"v_fma_f32 v97, v97, v136, v85\n"
"v_fma_f32 v98, v98, v136, v86\n"
"v_fma_f32 v99, v99, v136, v87\n"
"v_mul_f32_e64 v96, v96, |v96|\n"
"v_mul_f32_e64 v97, v97, |v97|\n"
"v_mul_f32_e64 v98, v98, |v98|\n"
"v_mul_f32_e64 v99, v99, |v99|\n"
"v_cvt_pk_bf16_f32 v100, v96, v97\n"
"v_cvt_pk_bf16_f32 v101, v98, v99\n"
"v_pk_max_i16 v100, v100, 0\n"
"v_pk_max_i16 v101, v101, 0\n"
"s_waitcnt lgkmcnt(0)\n"
"v_add_f32 v104, v104, v108\n"
"v_add_f32 v105, v105, v109\n"
"v_add_f32 v106, v106, v110\n"
"v_add_f32 v107, v107, v111\n"
"v_add_f32 v104, v104, v124\n"
"v_add_f32 v105, v105, v125\n"
"v_add_f32 v106, v106, v126\n"
"v_add_f32 v107, v107, v127\n"
"v_add_f32 v104, v104, v88\n"
"v_add_f32 v105, v105, v89\n"
"v_add_f32 v106, v106, v90\n"
"v_add_f32 v107, v107, v91\n"
"v_fma_f32 v96, v22, v137, v30\n"
"v_fma_f32 v97, v23, v137, v31\n"
"v_fma_f32 v98, v24, v137, v32\n"
"v_fma_f32 v99, v25, v137, v33\n"
"v_fma_f32 v104, v104, v136, v96\n"
"v_fma_f32 v105, v105, v136, v97\n"
"v_fma_f32 v106, v106, v136, v98\n"
"v_fma_f32 v107, v107, v136, v99\n"
"v_mul_f32_e64 v104, v104, |v104|\n"
"v_mul_f32_e64 v105, v105, |v105|\n"
"v_mul_f32_e64 v106, v106, |v106|\n"
"v_mul_f32_e64 v107, v107, |v107|\n"
"v_cvt_pk_bf16_f32 v102, v104, v105\n"
"v_cvt_pk_bf16_f32 v103, v106, v107\n"
"v_pk_max_i16 v102, v102, 0\n"
"v_pk_max_i16 v103, v103, 0\n"
"buffer_store_dwordx4 v[100:103], v15, s[40:43], s94 offen sc1\n"
"s_waitcnt vmcnt(1)\n"
"buffer_store_dwordx2 v[0:1], v16, s[40:43], s95 offen offset:0 sc1\n"
"s_waitcnt vmcnt(1)\n"
"buffer_store_dwordx2 v[0:1], v16, s[40:43], s93 offen offset:0 sc1\n"
"s_branch .LF_end\n"
".LF_t2:\n"
"v_cvt_pk_bf16_f32 v131, v86, v87\n"
"v_mfma_f32_16x16x32_bf16 v[92:95], a[196:199], v[104:107], v[92:95]\n"
"ds_write_addtid_b32 v88 offset:1280\n"
"v_mfma_f32_16x16x32_bf16 v[92:95], a[200:203], v[108:111], v[92:95]\n"
"ds_write_addtid_b32 v89 offset:5920\n"
"v_pk_max_i16 v130, v130, 0\n"
"v_mfma_f32_16x16x32_bf16 v[92:95], a[204:207], v[64:67], v[92:95]\n"
"ds_write_addtid_b32 v90 offset:10624\n"
"v_pk_max_i16 v131, v131, 0\n"
"v_mfma_f32_16x16x32_bf16 v[92:95], a[208:211], v[68:71], v[92:95]\n"
"ds_write_addtid_b32 v91 offset:15264\n"
"v_mfma_f32_16x16x32_bf16 v[92:95], a[212:215], v[72:75], v[92:95]\n"
"v_mfma_f32_16x16x32_bf16 v[92:95], a[216:219], v[76:79], v[92:95]\n"
"v_mfma_f32_16x16x32_bf16 v[92:95], a[220:223], v[80:83], v[92:95]\n"
"v_mfma_f32_16x16x32_bf16 v[88:91], a[224:227], v[100:103], 0\n"
"s_waitcnt lgkmcnt(0)\n"
"v_mfma_f32_16x16x32_bf16 v[88:91], a[228:231], v[104:107], v[88:91]\n"
"s_barrier\n"
"v_mfma_f32_16x16x32_bf16 v[88:91], a[232:235], v[108:111], v[88:91]\n"
"ds_read2st64_b32 v[136:137], v11 offset0:0 offset1:1\n"
"ds_read_b128 v[96:99], v2 offset:1536\n"
"v_mfma_f32_16x16x32_bf16 v[88:91], a[236:239], v[64:67], v[88:91]\n"
"s_waitcnt vmcnt(2)\n"
"v_mfma_f32_16x16x32_bf16 v[88:91], a[240:243], v[68:71], v[88:91]\n"
"ds_read_b128 v[84:87], v3 offset:512\n"
"v_mfma_f32_16x16x32_bf16 v[88:91], a[244:247], v[72:75], v[88:91]\n"
"ds_read_b128 v[100:103], v4 offset:2560\n"
"ds_read_b128 v[104:107], v2 offset:1792\n"
"v_mfma_f32_16x16x32_bf16 v[88:91], a[248:251], v[76:79], v[88:91]\n"
"ds_read_b128 v[108:111], v3 offset:768\n"
"ds_read_b128 v[124:127], v4 offset:2816\n"
"v_mfma_f32_16x16x32_bf16 v[88:91], a[252:255], v[80:83], v[88:91]\n"
"s_waitcnt vmcnt(8)\n"
"buffer_store_dwordx2 v[0:1], v16, s[40:43], s93 offen offset:0 sc1\n"
"v_mfma_f32_16x16x32_bf16 v[132:135], v[112:115], a[0:3], 0\n"
"buffer_store_dwordx4 v[128:131], v15, s[40:43], s94 offen sc1\n"
"v_mfma_f32_16x16x32_bf16 v[132:135], v[116:119], a[4:7], v[132:135]\n"
"s_waitcnt vmcnt(7)\n"
"v_mfma_f32_16x16x32_bf16 v[132:135], v[120:123], a[8:11], v[132:135]\n"
"v_mfma_f32_16x16x32_bf16 v[132:135], v[36:39], a[12:15], v[132:135]\n"
"s_waitcnt vmcnt(5)\n"
"v_mfma_f32_16x16x32_bf16 v[132:135], v[40:43], a[16:19], v[132:135]\n"
"s_waitcnt lgkmcnt(3)\n"
"v_add_f32 v96, v96, v84\n"
"v_add_f32 v97, v97, v85\n"
"v_mfma_f32_16x16x32_bf16 v[132:135], v[44:47], a[20:23], v[132:135]\n"
"s_waitcnt vmcnt(2)\n"
"v_add_f32 v98, v98, v86\n"
"v_mfma_f32_16x16x32_bf16 v[132:135], v[48:51], a[24:27], v[132:135]\n"
"v_add_f32 v99, v99, v87\n"
"v_add_f32 v96, v96, v100\n"
"v_mfma_f32_16x16x32_bf16 v[132:135], v[52:55], a[28:31], v[132:135]\n"
"v_add_f32 v97, v97, v101\n"
"v_mfma_f32_16x16x32_bf16 v[128:131], v[112:115], a[32:35], 0\n"
"v_add_f32 v98, v98, v102\n"
"v_add_f32 v99, v99, v103\n"
"v_mfma_f32_16x16x32_bf16 v[128:131], v[116:119], a[36:39], v[128:131]\n"
"ds_write_addtid_b32 v132 offset:1536\n"
"v_add_f32 v96, v96, v92\n"
"v_mfma_f32_16x16x32_bf16 v[128:131], v[120:123], a[40:43], v[128:131]\n"
"ds_write_addtid_b32 v133 offset:6176\n"
"v_add_f32 v97, v97, v93\n"
"v_mfma_f32_16x16x32_bf16 v[128:131], v[36:39], a[44:47], v[128:131]\n"
"ds_write_addtid_b32 v134 offset:10880\n"
"v_add_f32 v98, v98, v94\n"
"v_mfma_f32_16x16x32_bf16 v[128:131], v[40:43], a[48:51], v[128:131]\n"
"ds_write_addtid_b32 v135 offset:15520\n"
"v_add_f32 v99, v99, v95\n"
"v_mfma_f32_16x16x32_bf16 v[128:131], v[44:47], a[52:55], v[128:131]\n"
"s_add_u32 s93, s50, 0xffe80000\n"
"s_and_b32 s94, s93, 0xfbffffff\n"
"v_fma_f32 v84, v18, v137, v26\n"
"v_mfma_f32_16x16x32_bf16 v[128:131], v[48:51], a[56:59], v[128:131]\n"
"v_fma_f32 v85, v19, v137, v27\n"
"v_fma_f32 v86, v20, v137, v28\n"
"v_mfma_f32_16x16x32_bf16 v[128:131], v[52:55], a[60:63], v[128:131]\n"
"v_fma_f32 v87, v21, v137, v29\n"
"v_fma_f32 v96, v96, v136, v84\n"
"v_mfma_f32_16x16x32_bf16 v[92:95], v[112:115], a[64:67], 0\n"
"v_fma_f32 v97, v97, v136, v85\n"
"v_fma_f32 v98, v98, v136, v86\n"
"v_mfma_f32_16x16x32_bf16 v[92:95], v[116:119], a[68:71], v[92:95]\n"
"ds_write_addtid_b32 v128 offset:1792\n"
"v_mfma_f32_16x16x32_bf16 v[92:95], v[120:123], a[72:75], v[92:95]\n"
"ds_write_addtid_b32 v129 offset:6432\n"
"v_fma_f32 v99, v99, v136, v87\n"
"v_mfma_f32_16x16x32_bf16 v[92:95], v[36:39], a[76:79], v[92:95]\n"
"ds_write_addtid_b32 v130 offset:11136\n"
"v_mul_f32_e64 v96, v96, |v96|\n"
"v_mfma_f32_16x16x32_bf16 v[92:95], v[40:43], a[80:83], v[92:95]\n"
"ds_write_addtid_b32 v131 offset:15776\n"
"v_mul_f32_e64 v97, v97, |v97|\n"
"v_mfma_f32_16x16x32_bf16 v[92:95], v[44:47], a[84:87], v[92:95]\n"
"v_mul_f32_e64 v98, v98, |v98|\n"
"v_mfma_f32_16x16x32_bf16 v[92:95], v[48:51], a[88:91], v[92:95]\n"
"v_mul_f32_e64 v99, v99, |v99|\n"
"v_cvt_pk_bf16_f32 v100, v96, v97\n"
"v_mfma_f32_16x16x32_bf16 v[92:95], v[52:55], a[92:95], v[92:95]\n"
"v_cvt_pk_bf16_f32 v101, v98, v99\n"
"v_pk_max_i16 v100, v100, 0\n"
"v_mfma_f32_16x16x32_bf16 v[128:131], v[112:115], a[96:99], 0\n"
"v_pk_max_i16 v101, v101, 0\n"
"v_mfma_f32_16x16x32_bf16 v[128:131], v[116:119], a[100:103], v[128:131]\n"
"s_waitcnt lgkmcnt(8)\n"
"ds_write_addtid_b32 v92 offset:2048\n"
"v_add_f32 v104, v104, v108\n"
"v_mfma_f32_16x16x32_bf16 v[128:131], v[120:123], a[104:107], v[128:131]\n"
"ds_write_addtid_b32 v93 offset:6688\n"
"v_add_f32 v105, v105, v109\n"
"v_mfma_f32_16x16x32_bf16 v[128:131], v[36:39], a[108:111], v[128:131]\n"
"ds_write_addtid_b32 v94 offset:11392\n"
"v_add_f32 v106, v106, v110\n"
"v_mfma_f32_16x16x32_bf16 v[128:131], v[40:43], a[112:115], v[128:131]\n"
"ds_write_addtid_b32 v95 offset:16032\n"
"v_add_f32 v107, v107, v111\n"
"v_mfma_f32_16x16x32_bf16 v[128:131], v[44:47], a[116:119], v[128:131]\n"
"v_add_f32 v104, v104, v124\n"
"v_add_f32 v105, v105, v125\n"
"v_mfma_f32_16x16x32_bf16 v[128:131], v[48:51], a[120:123], v[128:131]\n"
"v_add_f32 v106, v106, v126\n"
"v_add_f32 v107, v107, v127\n"
"v_mfma_f32_16x16x32_bf16 v[128:131], v[52:55], a[124:127], v[128:131]\n"
"v_add_f32 v104, v104, v88\n"
"v_add_f32 v105, v105, v89\n"
"v_mfma_f32_16x16x32_bf16 v[92:95], v[112:115], a[128:131], 0\n"
"v_add_f32 v106, v106, v90\n"
"v_add_f32 v107, v107, v91\n"
"v_mfma_f32_16x16x32_bf16 v[92:95], v[116:119], a[132:135], v[92:95]\n"
"ds_write_addtid_b32 v128 offset:2304\n"
"v_fma_f32 v96, v22, v137, v30\n"
"v_mfma_f32_16x16x32_bf16 v[92:95], v[120:123], a[136:139], v[92:95]\n"
"ds_write_addtid_b32 v129 offset:6944\n"
"v_fma_f32 v97, v23, v137, v31\n"
"v_mfma_f32_16x16x32_bf16 v[92:95], v[36:39], a[140:143], v[92:95]\n"
"ds_write_addtid_b32 v130 offset:11648\n"
"v_fma_f32 v98, v24, v137, v32\n"
"v_mfma_f32_16x16x32_bf16 v[92:95], v[40:43], a[144:147], v[92:95]\n"
"ds_write_addtid_b32 v131 offset:16288\n"
"v_fma_f32 v99, v25, v137, v33\n"
"v_mfma_f32_16x16x32_bf16 v[92:95], v[44:47], a[148:151], v[92:95]\n"
"v_fma_f32 v104, v104, v136, v96\n"
"v_fma_f32 v105, v105, v136, v97\n"
"v_mfma_f32_16x16x32_bf16 v[92:95], v[48:51], a[152:155], v[92:95]\n"
"v_fma_f32 v106, v106, v136, v98\n"
"v_fma_f32 v107, v107, v136, v99\n"
"v_mfma_f32_16x16x32_bf16 v[92:95], v[52:55], a[156:159], v[92:95]\n"
"v_mul_f32_e64 v104, v104, |v104|\n"
"v_mul_f32_e64 v105, v105, |v105|\n"
"v_mfma_f32_16x16x32_bf16 v[96:99], v[112:115], a[160:163], 0\n"
"v_mul_f32_e64 v106, v106, |v106|\n"
"v_mul_f32_e64 v107, v107, |v107|\n"
"v_mfma_f32_16x16x32_bf16 v[96:99], v[116:119], a[164:167], v[96:99]\n"
"ds_write_addtid_b32 v92 offset:2560\n"
"v_cvt_pk_bf16_f32 v102, v104, v105\n"
"v_mfma_f32_16x16x32_bf16 v[96:99], v[120:123], a[168:171], v[96:99]\n"
"ds_write_addtid_b32 v93 offset:7200\n"
"v_cvt_pk_bf16_f32 v103, v106, v107\n"
"v_mfma_f32_16x16x32_bf16 v[96:99], v[36:39], a[172:175], v[96:99]\n"
"ds_write_addtid_b32 v94 offset:11904\n"
"v_pk_max_i16 v102, v102, 0\n"
"v_mfma_f32_16x16x32_bf16 v[96:99], v[40:43], a[176:179], v[96:99]\n"
"ds_write_addtid_b32 v95 offset:16544\n"
"v_pk_max_i16 v103, v103, 0\n"
"v_mfma_f32_16x16x32_bf16 v[96:99], v[44:47], a[180:183], v[96:99]\n"
"buffer_store_dwordx4 v[100:103], v15, s[40:43], s94 offen sc1\n"
"v_mfma_f32_16x16x32_bf16 v[96:99], v[48:51], a[184:187], v[96:99]\n"
"v_mfma_f32_16x16x32_bf16 v[96:99], v[52:55], a[188:191], v[96:99]\n"
"v_mfma_f32_16x16x32_bf16 v[100:103], a[192:195], v[112:115], 0\n"
"v_mfma_f32_16x16x32_bf16 v[100:103], a[196:199], v[116:119], v[100:103]\n"
"ds_write_addtid_b32 v96 offset:2816\n"
"v_mfma_f32_16x16x32_bf16 v[100:103], a[200:203], v[120:123], v[100:103]\n"
"s_waitcnt vmcnt(1)\n"
"buffer_store_dwordx2 v[0:1], v16, s[40:43], s95 offen offset:0 sc1\n"
"ds_write_addtid_b32 v97 offset:7456\n"
"v_mfma_f32_16x16x32_bf16 v[100:103], a[204:207], v[36:39], v[100:103]\n"
"ds_write_addtid_b32 v98 offset:12160\n"
"v_mfma_f32_16x16x32_bf16 v[100:103], a[208:211], v[40:43], v[100:103]\n"
"ds_write_addtid_b32 v99 offset:16800\n"
"v_mfma_f32_16x16x32_bf16 v[100:103], a[212:215], v[44:47], v[100:103]\n"
"v_mfma_f32_16x16x32_bf16 v[100:103], a[216:219], v[48:51], v[100:103]\n"
"v_mfma_f32_16x16x32_bf16 v[100:103], a[220:223], v[52:55], v[100:103]\n"
"v_mfma_f32_16x16x32_bf16 v[104:107], a[224:227], v[112:115], 0\n"
"s_waitcnt lgkmcnt(0)\n"
"v_mfma_f32_16x16x32_bf16 v[104:107], a[228:231], v[116:119], v[104:107]\n"
"s_barrier\n"
"v_mfma_f32_16x16x32_bf16 v[104:107], a[232:235], v[120:123], v[104:107]\n"
"ds_read2st64_b32 v[132:133], v12 offset0:0 offset1:1\n"
"ds_read_b128 v[108:111], v2 offset:0\n"
"v_mfma_f32_16x16x32_bf16 v[104:107], a[236:239], v[36:39], v[104:107]\n"
"ds_read_b128 v[96:99], v3 offset:2048\n"
"ds_read_b128 v[112:115], v4 offset:1024\n"
"v_mfma_f32_16x16x32_bf16 v[104:107], a[240:243], v[40:43], v[104:107]\n"
"v_mfma_f32_16x16x32_bf16 v[104:107], a[244:247], v[44:47], v[104:107]\n"
"ds_read_b128 v[116:119], v2 offset:256\n"
"v_mfma_f32_16x16x32_bf16 v[104:107], a[248:251], v[48:51], v[104:107]\n"
"ds_read_b128 v[120:123], v3 offset:2304\n"
"ds_read_b128 v[124:127], v4 offset:1280\n"
"v_mfma_f32_16x16x32_bf16 v[104:107], a[252:255], v[52:55], v[104:107]\n"
"s_waitcnt lgkmcnt(3)\n"
"v_add_f32 v108, v108, v96\n"
"v_add_f32 v109, v109, v97\n"
"v_add_f32 v110, v110, v98\n"
"v_add_f32 v111, v111, v99\n"
"v_add_f32 v108, v108, v112\n"
"v_add_f32 v109, v109, v113\n"
"v_add_f32 v110, v110, v114\n"
"v_add_f32 v111, v111, v115\n"
"v_add_f32 v108, v108, v100\n"
"v_add_f32 v109, v109, v101\n"
"v_add_f32 v110, v110, v102\n"
"v_add_f32 v111, v111, v103\n"
"s_add_u32 s95, s50, 0x0\n"
"s_and_b32 s94, s95, 0xfbffffff\n"
"v_fma_f32 v96, v18, v133, v26\n"
"v_fma_f32 v97, v19, v133, v27\n"
"v_fma_f32 v98, v20, v133, v28\n"
"v_fma_f32 v99, v21, v133, v29\n"
"v_fma_f32 v108, v108, v132, v96\n"
"v_fma_f32 v109, v109, v132, v97\n"
"v_fma_f32 v110, v110, v132, v98\n"
"v_fma_f32 v111, v111, v132, v99\n"
"v_mul_f32_e64 v108, v108, |v108|\n"
"v_mul_f32_e64 v109, v109, |v109|\n"
"v_mul_f32_e64 v110, v110, |v110|\n"
"v_mul_f32_e64 v111, v111, |v111|\n"
"v_cvt_pk_bf16_f32 v128, v108, v109\n"
"v_cvt_pk_bf16_f32 v129, v110, v111\n"
"v_pk_max_i16 v128, v128, 0\n"
"v_pk_max_i16 v129, v129, 0\n"
"s_waitcnt lgkmcnt(0)\n"
"v_add_f32 v116, v116, v120\n"
"v_add_f32 v117, v117, v121\n"
"v_add_f32 v118, v118, v122\n"
"v_add_f32 v119, v119, v123\n"
"v_add_f32 v116, v116, v124\n"
"v_add_f32 v117, v117, v125\n"
"v_add_f32 v118, v118, v126\n"
"v_add_f32 v119, v119, v127\n"
"s_waitcnt vmcnt(1)\n"
"buffer_store_dwordx2 v[0:1], v16, s[40:43], s93 offen offset:0 sc1\n"
"v_add_f32 v116, v116, v104\n"
"v_add_f32 v117, v117, v105\n"
"v_add_f32 v118, v118, v106\n"
"v_add_f32 v119, v119, v107\n"
"v_fma_f32 v100, v22, v133, v30\n"
"v_fma_f32 v101, v23, v133, v31\n"
"v_fma_f32 v102, v24, v133, v32\n"
"v_fma_f32 v103, v25, v133, v33\n"
"v_fma_f32 v116, v116, v132, v100\n"
"v_fma_f32 v117, v117, v132, v101\n"
"v_fma_f32 v118, v118, v132, v102\n"
"v_fma_f32 v119, v119, v132, v103\n"
"v_mul_f32_e64 v116, v116, |v116|\n"
"v_mul_f32_e64 v117, v117, |v117|\n"
"v_mul_f32_e64 v118, v118, |v118|\n"
"v_mul_f32_e64 v119, v119, |v119|\n"
"v_cvt_pk_bf16_f32 v130, v116, v117\n"
"v_cvt_pk_bf16_f32 v131, v118, v119\n"
"v_pk_max_i16 v130, v130, 0\n"
"v_pk_max_i16 v131, v131, 0\n"
"buffer_store_dwordx4 v[128:131], v15, s[40:43], s94 offen sc1\n"
"s_waitcnt vmcnt(0)\n"
"buffer_store_dwordx2 v[0:1], v16, s[40:43], s95 offen offset:0 sc1\n"
"s_branch .LF_end\n"
".LF_t3:\n"
"v_mfma_f32_16x16x32_bf16 v[100:103], a[196:199], v[116:119], v[100:103]\n"
"ds_write_addtid_b32 v96 offset:2816\n"
"v_mfma_f32_16x16x32_bf16 v[100:103], a[200:203], v[120:123], v[100:103]\n"
"s_waitcnt vmcnt(6)\n"
"buffer_store_dwordx2 v[0:1], v16, s[40:43], s95 offen offset:0 sc1\n"
"ds_write_addtid_b32 v97 offset:7456\n"
"v_mfma_f32_16x16x32_bf16 v[100:103], a[204:207], v[36:39], v[100:103]\n"
"ds_write_addtid_b32 v98 offset:12160\n"
"v_mfma_f32_16x16x32_bf16 v[100:103], a[208:211], v[40:43], v[100:103]\n"
"ds_write_addtid_b32 v99 offset:16800\n"
"v_mfma_f32_16x16x32_bf16 v[100:103], a[212:215], v[44:47], v[100:103]\n"
"v_mfma_f32_16x16x32_bf16 v[100:103], a[216:219], v[48:51], v[100:103]\n"
"v_mfma_f32_16x16x32_bf16 v[100:103], a[220:223], v[52:55], v[100:103]\n"
"v_mfma_f32_16x16x32_bf16 v[104:107], a[224:227], v[112:115], 0\n"
"s_waitcnt lgkmcnt(0)\n"
"v_mfma_f32_16x16x32_bf16 v[104:107], a[228:231], v[116:119], v[104:107]\n"
"s_barrier\n"
"v_mfma_f32_16x16x32_bf16 v[104:107], a[232:235], v[120:123], v[104:107]\n"
"ds_read2st64_b32 v[132:133], v12 offset0:0 offset1:1\n"
"ds_read_b128 v[108:111], v2 offset:0\n"
"v_mfma_f32_16x16x32_bf16 v[104:107], a[236:239], v[36:39], v[104:107]\n"
"ds_read_b128 v[96:99], v3 offset:2048\n"
"ds_read_b128 v[112:115], v4 offset:1024\n"
"v_mfma_f32_16x16x32_bf16 v[104:107], a[240:243], v[40:43], v[104:107]\n"
"v_mfma_f32_16x16x32_bf16 v[104:107], a[244:247], v[44:47], v[104:107]\n"
"ds_read_b128 v[116:119], v2 offset:256\n"
"v_mfma_f32_16x16x32_bf16 v[104:107], a[248:251], v[48:51], v[104:107]\n"
"ds_read_b128 v[120:123], v3 offset:2304\n"
"ds_read_b128 v[124:127], v4 offset:1280\n"
"v_mfma_f32_16x16x32_bf16 v[104:107], a[252:255], v[52:55], v[104:107]\n"
"s_waitcnt vmcnt(11)\n"
"v_mfma_f32_16x16x32_bf16 v[128:131], v[56:59], a[0:3], 0\n"
"v_mfma_f32_16x16x32_bf16 v[128:131], v[60:63], a[4:7], v[128:131]\n"
"s_waitcnt vmcnt(6)\n"
"v_mfma_f32_16x16x32_bf16 v[128:131], v[64:67], a[8:11], v[128:131]\n"
"s_waitcnt lgkmcnt(3)\n"
"v_add_f32 v108, v108, v96\n"
"v_add_f32 v109, v109, v97\n"
"v_mfma_f32_16x16x32_bf16 v[128:131], v[68:71], a[12:15], v[128:131]\n"
"s_waitcnt vmcnt(4)\n"
"v_add_f32 v110, v110, v98\n"
"v_add_f32 v111, v111, v99\n"
"v_mfma_f32_16x16x32_bf16 v[128:131], v[72:75], a[16:19], v[128:131]\n"
"v_add_f32 v108, v108, v112\n"
"v_add_f32 v109, v109, v113\n"
"v_mfma_f32_16x16x32_bf16 v[128:131], v[76:79], a[20:23], v[128:131]\n"
"s_waitcnt vmcnt(2)\n"
"v_add_f32 v110, v110, v114\n"
"v_add_f32 v111, v111, v115\n"
"v_mfma_f32_16x16x32_bf16 v[128:131], v[80:83], a[24:27], v[128:131]\n"
"v_add_f32 v108, v108, v100\n"
"v_add_f32 v109, v109, v101\n"
"v_mfma_f32_16x16x32_bf16 v[128:131], v[84:87], a[28:31], v[128:131]\n"
"v_add_f32 v110, v110, v102\n"
"v_mfma_f32_16x16x32_bf16 v[112:115], v[56:59], a[32:35], 0\n"
"v_add_f32 v111, v111, v103\n"
"v_mfma_f32_16x16x32_bf16 v[112:115], v[60:63], a[36:39], v[112:115]\n"
"ds_write_addtid_b32 v128 offset:0\n"
"v_mfma_f32_16x16x32_bf16 v[112:115], v[64:67], a[40:43], v[112:115]\n"
"s_add_u32 s95, s50, 0xffe80000\n"
"s_and_b32 s94, s95, 0xfbffffff\n"
"ds_write_addtid_b32 v129 offset:4640\n"
"v_mfma_f32_16x16x32_bf16 v[112:115], v[68:71], a[44:47], v[112:115]\n"
"ds_write_addtid_b32 v130 offset:9344\n"
"v_fma_f32 v96, v18, v133, v26\n"
"v_mfma_f32_16x16x32_bf16 v[112:115], v[72:75], a[48:51], v[112:115]\n"
"ds_write_addtid_b32 v131 offset:13984\n"
"v_fma_f32 v97, v19, v133, v27\n"
"v_mfma_f32_16x16x32_bf16 v[112:115], v[76:79], a[52:55], v[112:115]\n"
"v_fma_f32 v98, v20, v133, v28\n"
"v_fma_f32 v99, v21, v133, v29\n"
"v_mfma_f32_16x16x32_bf16 v[112:115], v[80:83], a[56:59], v[112:115]\n"
"v_fma_f32 v108, v108, v132, v96\n"
"v_fma_f32 v109, v109, v132, v97\n"
"v_mfma_f32_16x16x32_bf16 v[112:115], v[84:87], a[60:63], v[112:115]\n"
"v_fma_f32 v110, v110, v132, v98\n"
"v_fma_f32 v111, v111, v132, v99\n"
"v_mfma_f32_16x16x32_bf16 v[100:103], v[56:59], a[64:67], 0\n"
"v_mul_f32_e64 v108, v108, |v108|\n"
"v_mfma_f32_16x16x32_bf16 v[100:103], v[60:63], a[68:71], v[100:103]\n"
"ds_write_addtid_b32 v112 offset:256\n"
"v_mul_f32_e64 v109, v109, |v109|\n"
"v_mfma_f32_16x16x32_bf16 v[100:103], v[64:67], a[72:75], v[100:103]\n"
"ds_write_addtid_b32 v113 offset:4896\n"
"v_mul_f32_e64 v110, v110, |v110|\n"
"v_mfma_f32_16x16x32_bf16 v[100:103], v[68:71], a[76:79], v[100:103]\n"
"ds_write_addtid_b32 v114 offset:9600\n"
"v_mul_f32_e64 v111, v111, |v111|\n"
"v_mfma_f32_16x16x32_bf16 v[100:103], v[72:75], a[80:83], v[100:103]\n"
"ds_write_addtid_b32 v115 offset:14240\n"
"v_mfma_f32_16x16x32_bf16 v[100:103], v[76:79], a[84:87], v[100:103]\n"
"v_cvt_pk_bf16_f32 v128, v108, v109\n"
"v_cvt_pk_bf16_f32 v129, v110, v111\n"
"v_mfma_f32_16x16x32_bf16 v[100:103], v[80:83], a[88:91], v[100:103]\n"
"v_pk_max_i16 v128, v128, 0\n"
"v_pk_max_i16 v129, v129, 0\n"
"v_mfma_f32_16x16x32_bf16 v[100:103], v[84:87], a[92:95], v[100:103]\n"
"s_waitcnt lgkmcnt(8)\n"
"v_add_f32 v116, v116, v120\n"
"v_add_f32 v117, v117, v121\n"
"v_mfma_f32_16x16x32_bf16 v[108:111], v[56:59], a[96:99], 0\n"
"v_add_f32 v118, v118, v122\n"
"v_add_f32 v119, v119, v123\n"
"v_mfma_f32_16x16x32_bf16 v[108:111], v[60:63], a[100:103], v[108:111]\n"
"ds_write_addtid_b32 v100 offset:512\n"
"v_add_f32 v116, v116, v124\n"
"v_mfma_f32_16x16x32_bf16 v[108:111], v[64:67], a[104:107], v[108:111]\n"
"ds_write_addtid_b32 v101 offset:5152\n"
"v_add_f32 v117, v117, v125\n"
"v_mfma_f32_16x16x32_bf16 v[108:111], v[68:71], a[108:111], v[108:111]\n"
"ds_write_addtid_b32 v102 offset:9856\n"
"v_add_f32 v118, v118, v126\n"
"v_mfma_f32_16x16x32_bf16 v[108:111], v[72:75], a[112:115], v[108:111]\n"
"ds_write_addtid_b32 v103 offset:14496\n"
"v_add_f32 v119, v119, v127\n"
"v_mfma_f32_16x16x32_bf16 v[108:111], v[76:79], a[116:119], v[108:111]\n"
"s_waitcnt vmcnt(1)\n"
"buffer_store_dwordx2 v[0:1], v16, s[40:43], s93 offen offset:0 sc1\n"
"v_add_f32 v116, v116, v104\n"
"v_mfma_f32_16x16x32_bf16 v[108:111], v[80:83], a[120:123], v[108:111]\n"
"v_add_f32 v117, v117, v105\n"
"v_add_f32 v118, v118, v106\n"
"v_mfma_f32_16x16x32_bf16 v[108:111], v[84:87], a[124:127], v[108:111]\n"
"v_add_f32 v119, v119, v107\n"
"v_fma_f32 v100, v22, v133, v30\n"
"v_mfma_f32_16x16x32_bf16 v[112:115], v[56:59], a[128:131], 0\n"
"v_fma_f32 v101, v23, v133, v31\n"
"v_fma_f32 v102, v24, v133, v32\n"
"v_mfma_f32_16x16x32_bf16 v[112:115], v[60:63], a[132:135], v[112:115]\n"
"ds_write_addtid_b32 v108 offset:768\n"
"v_fma_f32 v103, v25, v133, v33\n"
"v_mfma_f32_16x16x32_bf16 v[112:115], v[64:67], a[136:139], v[112:115]\n"
"ds_write_addtid_b32 v109 offset:5408\n"
"v_fma_f32 v116, v116, v132, v100\n"
"v_mfma_f32_16x16x32_bf16 v[112:115], v[68:71], a[140:143], v[112:115]\n"
"ds_write_addtid_b32 v110 offset:10112\n"
"v_fma_f32 v117, v117, v132, v101\n"
"v_mfma_f32_16x16x32_bf16 v[112:115], v[72:75], a[144:147], v[112:115]\n"
"ds_write_addtid_b32 v111 offset:14752\n"
"v_fma_f32 v118, v118, v132, v102\n"
"v_mfma_f32_16x16x32_bf16 v[112:115], v[76:79], a[148:151], v[112:115]\n"
"v_fma_f32 v119, v119, v132, v103\n"
"v_mul_f32_e64 v116, v116, |v116|\n"
"v_mfma_f32_16x16x32_bf16 v[112:115], v[80:83], a[152:155], v[112:115]\n"
"v_mul_f32_e64 v117, v117, |v117|\n"
"v_mul_f32_e64 v118, v118, |v118|\n"
"v_mfma_f32_16x16x32_bf16 v[112:115], v[84:87], a[156:159], v[112:115]\n"
"v_mul_f32_e64 v119, v119, |v119|\n"
"v_cvt_pk_bf16_f32 v130, v116, v117\n"
"v_mfma_f32_16x16x32_bf16 v[108:111], v[56:59], a[160:163], 0\n"
"v_cvt_pk_bf16_f32 v131, v118, v119\n"
"v_pk_max_i16 v130, v130, 0\n"
"v_mfma_f32_16x16x32_bf16 v[108:111], v[60:63], a[164:167], v[108:111]\n"
"ds_write_addtid_b32 v112 offset:1024\n"
"v_pk_max_i16 v131, v131, 0\n"
"v_mfma_f32_16x16x32_bf16 v[108:111], v[64:67], a[168:171], v[108:111]\n"
"ds_write_addtid_b32 v113 offset:5664\n"
"v_mfma_f32_16x16x32_bf16 v[108:111], v[68:71], a[172:175], v[108:111]\n"
"ds_write_addtid_b32 v114 offset:10368\n"
"v_mfma_f32_16x16x32_bf16 v[108:111], v[72:75], a[176:179], v[108:111]\n"
"ds_write_addtid_b32 v115 offset:15008\n"
"v_mfma_f32_16x16x32_bf16 v[108:111], v[76:79], a[180:183], v[108:111]\n"
"buffer_store_dwordx4 v[128:131], v15, s[40:43], s94 offen sc1\n"
"v_mfma_f32_16x16x32_bf16 v[108:111], v[80:83], a[184:187], v[108:111]\n"
"v_mfma_f32_16x16x32_bf16 v[108:111], v[84:87], a[188:191], v[108:111]\n"
"v_mfma_f32_16x16x32_bf16 v[112:115], a[192:195], v[56:59], 0\n"
"v_mfma_f32_16x16x32_bf16 v[112:115], a[196:199], v[60:63], v[112:115]\n"
"ds_write_addtid_b32 v108 offset:1280\n"
"v_mfma_f32_16x16x32_bf16 v[112:115], a[200:203], v[64:67], v[112:115]\n"
"ds_write_addtid_b32 v109 offset:5920\n"
"v_mfma_f32_16x16x32_bf16 v[112:115], a[204:207], v[68:71], v[112:115]\n"
"ds_write_addtid_b32 v110 offset:10624\n"
"v_mfma_f32_16x16x32_bf16 v[112:115], a[208:211], v[72:75], v[112:115]\n"
"ds_write_addtid_b32 v111 offset:15264\n"
"v_mfma_f32_16x16x32_bf16 v[112:115], a[212:215], v[76:79], v[112:115]\n"
"v_mfma_f32_16x16x32_bf16 v[112:115], a[216:219], v[80:83], v[112:115]\n"
"v_mfma_f32_16x16x32_bf16 v[112:115], a[220:223], v[84:87], v[112:115]\n"
"v_mfma_f32_16x16x32_bf16 v[132:135], a[224:227], v[56:59], 0\n"
"s_waitcnt lgkmcnt(0)\n"
"v_mfma_f32_16x16x32_bf16 v[132:135], a[228:231], v[60:63], v[132:135]\n"
"s_barrier\n"
"v_mfma_f32_16x16x32_bf16 v[132:135], a[232:235], v[64:67], v[132:135]\n"
"ds_read2st64_b32 v[152:153], v13 offset0:0 offset1:1\n"
"v_mfma_f32_16x16x32_bf16 v[132:135], a[236:239], v[68:71], v[132:135]\n"
"ds_read_b128 v[56:59], v2 offset:1536\n"
"ds_read_b128 v[60:63], v3 offset:512\n"
"v_mfma_f32_16x16x32_bf16 v[132:135], a[240:243], v[72:75], v[132:135]\n"
"ds_read_b128 v[136:139], v4 offset:2560\n"
"ds_read_b128 v[140:143], v2 offset:1792\n"
"v_mfma_f32_16x16x32_bf16 v[132:135], a[244:247], v[76:79], v[132:135]\n"
"v_mfma_f32_16x16x32_bf16 v[132:135], a[248:251], v[80:83], v[132:135]\n"
"ds_read_b128 v[144:147], v3 offset:768\n"
"v_mfma_f32_16x16x32_bf16 v[132:135], a[252:255], v[84:87], v[132:135]\n"
"ds_read_b128 v[148:151], v4 offset:2816\n"
"s_waitcnt lgkmcnt(3)\n"
"v_add_f32 v56, v56, v60\n"
"v_add_f32 v57, v57, v61\n"
"v_add_f32 v58, v58, v62\n"
"v_add_f32 v59, v59, v63\n"
"v_add_f32 v56, v56, v136\n"
"v_add_f32 v57, v57, v137\n"
"v_add_f32 v58, v58, v138\n"
"v_add_f32 v59, v59, v139\n"
"v_add_f32 v56, v56, v112\n"
"v_add_f32 v57, v57, v113\n"
"v_add_f32 v58, v58, v114\n"
"v_add_f32 v59, v59, v115\n"
"s_add_u32 s93, s50, 0x0\n"
"s_and_b32 s94, s93, 0xfbffffff\n"
"v_fma_f32 v80, v18, v153, v26\n"
"v_fma_f32 v81, v19, v153, v27\n"
"v_fma_f32 v82, v20, v153, v28\n"
"v_fma_f32 v83, v21, v153, v29\n"
"v_fma_f32 v56, v56, v152, v80\n"
"v_fma_f32 v57, v57, v152, v81\n"
"v_fma_f32 v58, v58, v152, v82\n"
"v_fma_f32 v59, v59, v152, v83\n"
"v_mul_f32_e64 v56, v56, |v56|\n"
"v_mul_f32_e64 v57, v57, |v57|\n"
"v_mul_f32_e64 v58, v58, |v58|\n"
"v_mul_f32_e64 v59, v59, |v59|\n"
"v_cvt_pk_bf16_f32 v136, v56, v57\n"
"v_cvt_pk_bf16_f32 v137, v58, v59\n"
"v_pk_max_i16 v136, v136, 0\n"
"v_pk_max_i16 v137, v137, 0\n"
"s_waitcnt lgkmcnt(0)\n"
"v_add_f32 v140, v140, v144\n"
"v_add_f32 v141, v141, v145\n"
"v_add_f32 v142, v142, v146\n"
"v_add_f32 v143, v143, v147\n"
"v_add_f32 v140, v140, v148\n"
"v_add_f32 v141, v141, v149\n"
"v_add_f32 v142, v142, v150\n"
"v_add_f32 v143, v143, v151\n"
"s_waitcnt vmcnt(0)\n"
"buffer_store_dwordx2 v[0:1], v16, s[40:43], s95 offen offset:0 sc1\n"
"v_add_f32 v140, v140, v132\n"
"v_add_f32 v141, v141, v133\n"
"v_add_f32 v142, v142, v134\n"
"v_add_f32 v143, v143, v135\n"
"v_fma_f32 v60, v22, v153, v30\n"
"v_fma_f32 v61, v23, v153, v31\n"
"v_fma_f32 v62, v24, v153, v32\n"
"v_fma_f32 v63, v25, v153, v33\n"
"v_fma_f32 v140, v140, v152, v60\n"
"v_fma_f32 v141, v141, v152, v61\n"
"v_fma_f32 v142, v142, v152, v62\n"
"v_fma_f32 v143, v143, v152, v63\n"
"v_mul_f32_e64 v140, v140, |v140|\n"
"v_mul_f32_e64 v141, v141, |v141|\n"
"v_mul_f32_e64 v142, v142, |v142|\n"
"v_mul_f32_e64 v143, v143, |v143|\n"
"v_cvt_pk_bf16_f32 v138, v140, v141\n"
"v_cvt_pk_bf16_f32 v139, v142, v143\n"
"v_pk_max_i16 v138, v138, 0\n"
"v_pk_max_i16 v139, v139, 0\n"
"buffer_store_dwordx4 v[136:139], v15, s[40:43], s94 offen sc1\n"
"s_waitcnt vmcnt(0)\n"
"buffer_store_dwordx2 v[0:1], v16, s[40:43], s93 offen offset:0 sc1\n"
"s_branch .LF_end\n"
".LF_end:\n"
"s_waitcnt vmcnt(0) lgkmcnt(0)\n"
"s_endpgm\n"
      :: "{v2}"((unsigned)(lds0 + ((wid + 3) & 3) * 19104 + trp)), "{v3}"((unsigned)(lds0 + ((wid + 2) & 3) * 19104 + trp)), "{v4}"((unsigned)(lds0 + ((wid + 1) & 3) * 19104 + trp)), "{v5}"((unsigned)(lane * 16 + wid * 8192 + ((0 + rot) % 4) * 2048 + 134283264)), "{v6}"((unsigned)(lane * 16 + wid * 8192 + ((1 + rot) % 4) * 2048 + 134283264)), "{v7}"((unsigned)(lane * 16 + wid * 8192 + ((2 + rot) % 4) * 2048 + 134283264)), "{v8}"((unsigned)(lane * 16 + wid * 8192 + ((3 + rot) % 4) * 2048 + 134283264)), "{v9}"((unsigned)((lane & 31) * 8 + 352768 + F_GL * 1572864)), "{v10}"((unsigned)(lane * 16 + 134561792)), "{v11}"((unsigned)(lds0 + ((wid + 0) & 3) * 19104 + 18592 + ((li >> 1) + 32 * (li & 1)) * 4)), "{v12}"((unsigned)(lds0 + ((wid + 1) & 3) * 19104 + 18592 + ((li >> 1) + 32 * (li & 1)) * 4)), "{v13}"((unsigned)(lds0 + ((wid + 2) & 3) * 19104 + 18592 + ((li >> 1) + 32 * (li & 1)) * 4)), "{v14}"((unsigned)(lds0 + ((wid + 3) & 3) * 19104 + 18592 + ((li >> 1) + 32 * (li & 1)) * 4)), "{v15}"((unsigned)(lane * 16 + pair * 1024 + 98304)), "{v16}"((unsigned)(lane ? DROP : pair * 8 + idx / 7 * 32 + 353024)), "{v17}"((unsigned)(0x3727c5ac)), "{v36}"((unsigned)(vT + tsw)), "{v37}"((unsigned)(vT + (tsw ^ 8))), "{v38}"((unsigned)(pair * 64 + lg * 16)), "{v39}"((unsigned)(vD + ((0 + rot) % 4) * 458752)), "{v40}"((unsigned)(vD + ((1 + rot) % 4) * 458752)), "{v41}"((unsigned)(vD + ((2 + rot) % 4) * 458752)), "{v42}"((unsigned)(vD + ((3 + rot) % 4) * 458752)), "{v43}"((unsigned)(lds0 + wid * 19104 + trp)), "{s[40:43]}"(rS), "{s[44:47]}"(rW), "{s[48:49]}"(wsp), "{s50}"((unsigned)(sA)), "{s51}"((unsigned)(sLo)), "{s52}"((unsigned)(sHi)), "{s53}"((unsigned)(sEx)), "{s54}"((unsigned)(sEnt)), "{s55}"((unsigned)(sLd)), "{s[56:57]}"(wsb + (sLo & 0x3ffffff)), "{s59}"((unsigned)(lds0 + wid * 19104)), "{s62}"((unsigned)(0x3a800000)), "{s[64:65]}"((u64)a.b1), "{s66}"((unsigned)(sW0 + 64 * ((wid + 1) & 3))), "{s67}"((unsigned)(sW0 + 64 * ((wid + 2) & 3))), "{s68}"((unsigned)(sW0 + 64 * ((wid + 3) & 3))), "{s69}"((unsigned)(sW0 + 64 * ((wid + 4) & 3)))
      : "v255", "a255", "s95", "vcc", "scc", "memory");

  } else {
    const int kq = idx / 7, grp = idx % 7, f0 = kq * 28 + wid * 7, pg0 = grp < 4 ? grp * 5 : 4 + grp * 4, pair = pg0 + wid;
    const unsigned rot = SLP & 8 ? 0 : (set * 5) >> 1, tsw = (kq & 1) * 8, sW0 = f0 * 32 * 2048 + pg0 * 64, vD = (lane >> 2) * 2048 + (lane & 3) * 16;
    const u32x4 rW = desc(a.w2, 3584 * 2048);
    if (idx == 0) {
      const unsigned sEx = sHi + H_EX * 3 * RS, sEnt = -wid & 3;
    asm volatile(
"s_nop 4\n"
"v_mov_b32 v186, 0\n"
"global_load_dwordx2 v[0:1], v186, s[48:49] sc1\n"
"v_mov_b32 v188, 0x3f803f80\n"
"v_mov_b32 v189, 0x3f803f80\n"
"v_mov_b32 v190, 0x3f803f80\n"
"v_mov_b32 v191, 0x3f803f80\n"
"s_add_u32 m0, s55, 0\n"
"s_add_u32 s60, s66, 0\n"
"buffer_load_dwordx4 v50, s[44:47], s60 offen lds\n"
"s_add_u32 m0, s55, 1024\n"
"s_add_u32 s60, s66, 32768\n"
"buffer_load_dwordx4 v50, s[44:47], s60 offen lds\n"
"s_add_u32 m0, s55, 2048\n"
"s_add_u32 s60, s67, 0\n"
"buffer_load_dwordx4 v50, s[44:47], s60 offen lds\n"
"s_add_u32 m0, s55, 3072\n"
"s_add_u32 s60, s67, 32768\n"
"buffer_load_dwordx4 v50, s[44:47], s60 offen lds\n"
"s_add_u32 m0, s55, 4096\n"
"s_add_u32 s60, s68, 0\n"
"buffer_load_dwordx4 v50, s[44:47], s60 offen lds\n"
"s_add_u32 m0, s55, 5120\n"
"s_add_u32 s60, s68, 32768\n"
"buffer_load_dwordx4 v50, s[44:47], s60 offen lds\n"
"s_add_u32 m0, s55, 6144\n"
"s_add_u32 s60, s69, 0\n"
"buffer_load_dwordx4 v50, s[44:47], s60 offen lds\n"
"s_add_u32 m0, s55, 7168\n"
"s_add_u32 s60, s69, 32768\n"
"buffer_load_dwordx4 v50, s[44:47], s60 offen lds\n"
"s_add_u32 m0, s55, 8192\n"
"s_add_u32 s60, s70, 0\n"
"buffer_load_dwordx4 v50, s[44:47], s60 offen lds\n"
"s_add_u32 m0, s55, 9216\n"
"s_add_u32 s60, s70, 32768\n"
"buffer_load_dwordx4 v50, s[44:47], s60 offen lds\n"
"s_add_u32 m0, s55, 10240\n"
"s_add_u32 s60, s66, 0\n"
"buffer_load_dwordx4 v51, s[44:47], s60 offen lds\n"
"s_add_u32 m0, s55, 11264\n"
"s_add_u32 s60, s66, 32768\n"
"buffer_load_dwordx4 v51, s[44:47], s60 offen lds\n"
"s_add_u32 m0, s55, 12288\n"
"s_add_u32 s60, s67, 0\n"
"buffer_load_dwordx4 v51, s[44:47], s60 offen lds\n"
"s_add_u32 m0, s55, 13312\n"
"s_add_u32 s60, s67, 32768\n"
"buffer_load_dwordx4 v51, s[44:47], s60 offen lds\n"
"s_add_u32 m0, s55, 14336\n"
"s_add_u32 s60, s68, 0\n"
"buffer_load_dwordx4 v51, s[44:47], s60 offen lds\n"
"s_add_u32 m0, s55, 15360\n"
"s_add_u32 s60, s68, 32768\n"
"buffer_load_dwordx4 v51, s[44:47], s60 offen lds\n"
"s_add_u32 m0, s55, 16384\n"
"s_add_u32 s60, s69, 0\n"
"buffer_load_dwordx4 v51, s[44:47], s60 offen lds\n"
"s_add_u32 m0, s55, 17408\n"
"s_add_u32 s60, s69, 32768\n"
"buffer_load_dwordx4 v51, s[44:47], s60 offen lds\n"
"s_add_u32 m0, s55, 18432\n"
"s_add_u32 s60, s70, 0\n"
"buffer_load_dwordx4 v51, s[44:47], s60 offen lds\n"
"s_add_u32 m0, s55, 19456\n"
"s_add_u32 s60, s70, 32768\n"
"buffer_load_dwordx4 v51, s[44:47], s60 offen lds\n"
"s_add_u32 m0, s55, 20480\n"
"s_add_u32 s60, s66, 0\n"
"buffer_load_dwordx4 v52, s[44:47], s60 offen lds\n"
"s_add_u32 m0, s55, 21504\n"
"s_add_u32 s60, s66, 32768\n"
"buffer_load_dwordx4 v52, s[44:47], s60 offen lds\n"
"s_add_u32 m0, s55, 22528\n"
"s_add_u32 s60, s67, 0\n"
"buffer_load_dwordx4 v52, s[44:47], s60 offen lds\n"
"s_add_u32 m0, s55, 23552\n"
"s_add_u32 s60, s67, 32768\n"
"buffer_load_dwordx4 v52, s[44:47], s60 offen lds\n"
"s_add_u32 m0, s55, 24576\n"
"s_add_u32 s60, s68, 0\n"
"buffer_load_dwordx4 v52, s[44:47], s60 offen lds\n"
"s_add_u32 m0, s55, 25600\n"
"s_add_u32 s60, s68, 32768\n"
"buffer_load_dwordx4 v52, s[44:47], s60 offen lds\n"
"s_add_u32 m0, s55, 26624\n"
"s_add_u32 s60, s69, 0\n"
"buffer_load_dwordx4 v52, s[44:47], s60 offen lds\n"
"s_add_u32 m0, s55, 27648\n"
"s_add_u32 s60, s69, 32768\n"
"buffer_load_dwordx4 v52, s[44:47], s60 offen lds\n"
"s_add_u32 m0, s55, 28672\n"
"s_add_u32 s60, s70, 0\n"
"buffer_load_dwordx4 v52, s[44:47], s60 offen lds\n"
"s_add_u32 m0, s55, 29696\n"
"s_add_u32 s60, s70, 32768\n"
"buffer_load_dwordx4 v52, s[44:47], s60 offen lds\n"
"s_add_u32 m0, s55, 30720\n"
"s_add_u32 s60, s66, 0\n"
"buffer_load_dwordx4 v53, s[44:47], s60 offen lds\n"
"s_add_u32 m0, s55, 31744\n"
"s_add_u32 s60, s66, 32768\n"
"buffer_load_dwordx4 v53, s[44:47], s60 offen lds\n"
"s_add_u32 m0, s55, 32768\n"
"s_add_u32 s60, s67, 0\n"
"buffer_load_dwordx4 v53, s[44:47], s60 offen lds\n"
"s_add_u32 m0, s55, 33792\n"
"s_add_u32 s60, s67, 32768\n"
"buffer_load_dwordx4 v53, s[44:47], s60 offen lds\n"
"s_add_u32 m0, s55, 34816\n"
"s_add_u32 s60, s68, 0\n"
"buffer_load_dwordx4 v53, s[44:47], s60 offen lds\n"
"s_add_u32 m0, s55, 35840\n"
"s_add_u32 s60, s68, 32768\n"
"buffer_load_dwordx4 v53, s[44:47], s60 offen lds\n"
"s_add_u32 m0, s55, 36864\n"
"s_add_u32 s60, s69, 0\n"
"buffer_load_dwordx4 v53, s[44:47], s60 offen lds\n"
"s_add_u32 m0, s55, 37888\n"
"s_add_u32 s60, s69, 32768\n"
"buffer_load_dwordx4 v53, s[44:47], s60 offen lds\n"
"s_add_u32 m0, s55, 38912\n"
"s_add_u32 s60, s70, 0\n"
"buffer_load_dwordx4 v53, s[44:47], s60 offen lds\n"
"s_add_u32 m0, s55, 39936\n"
"s_add_u32 s60, s70, 32768\n"
"buffer_load_dwordx4 v53, s[44:47], s60 offen lds\n"
"s_waitcnt vmcnt(38)\n"
"ds_read_b64_tr_b16 a[0:1], v48 offset:0\n"
"ds_read_b64_tr_b16 a[2:3], v48 offset:256\n"
"ds_read_b64_tr_b16 a[28:29], v49 offset:0\n"
"ds_read_b64_tr_b16 a[30:31], v49 offset:256\n"
"v_add_u32 v0, 2, v0\n"
"v_add_u32 v1, 0x9e3779b9, v1\n"
"v_or_b32 v0, 1, v0\n"
"v_readfirstlane_b32 s49, v1\n"
"v_readfirstlane_b32 s48, v0\n"
"s_waitcnt vmcnt(36)\n"
"ds_read_b64_tr_b16 a[56:57], v48 offset:2048\n"
"ds_read_b64_tr_b16 a[58:59], v48 offset:2304\n"
"ds_read_b64_tr_b16 a[84:85], v49 offset:2048\n"
"ds_read_b64_tr_b16 a[86:87], v49 offset:2304\n"
"s_waitcnt lgkmcnt(4)\n"
"s_add_u32 m0, s55, 0\n"
"s_add_u32 s60, s66, 0\n"
"buffer_load_dwordx4 v54, s[44:47], s60 offen lds\n"
"s_add_u32 m0, s55, 1024\n"
"s_add_u32 s60, s66, 32768\n"
"buffer_load_dwordx4 v54, s[44:47], s60 offen lds\n"
"s_waitcnt vmcnt(36)\n"
"ds_read_b64_tr_b16 a[112:113], v48 offset:4096\n"
"ds_read_b64_tr_b16 a[114:115], v48 offset:4352\n"
"ds_read_b64_tr_b16 a[140:141], v49 offset:4096\n"
"ds_read_b64_tr_b16 a[142:143], v49 offset:4352\n"
"s_waitcnt lgkmcnt(4)\n"
"s_add_u32 m0, s55, 2048\n"
"s_add_u32 s60, s67, 0\n"
"buffer_load_dwordx4 v54, s[44:47], s60 offen lds\n"
"s_add_u32 m0, s55, 3072\n"
"s_add_u32 s60, s67, 32768\n"
"buffer_load_dwordx4 v54, s[44:47], s60 offen lds\n"
"s_waitcnt vmcnt(36)\n"
"ds_read_b64_tr_b16 a[168:169], v48 offset:6144\n"
"ds_read_b64_tr_b16 a[170:171], v48 offset:6400\n"
"ds_read_b64_tr_b16 a[196:197], v49 offset:6144\n"
"ds_read_b64_tr_b16 a[198:199], v49 offset:6400\n"
"s_waitcnt lgkmcnt(4)\n"
"s_add_u32 m0, s55, 4096\n"
"s_add_u32 s60, s68, 0\n"
"buffer_load_dwordx4 v54, s[44:47], s60 offen lds\n"
"s_add_u32 m0, s55, 5120\n"
"s_add_u32 s60, s68, 32768\n"
"buffer_load_dwordx4 v54, s[44:47], s60 offen lds\n"
"s_waitcnt vmcnt(36)\n"
"ds_read_b64_tr_b16 a[224:225], v48 offset:8192\n"
"ds_read_b64_tr_b16 a[226:227], v48 offset:8448\n"
"ds_read_b64_tr_b16 a[252:253], v49 offset:8192\n"
"ds_read_b64_tr_b16 a[254:255], v49 offset:8448\n"
"s_waitcnt lgkmcnt(4)\n"
"s_add_u32 m0, s55, 6144\n"
"s_add_u32 s60, s69, 0\n"
"buffer_load_dwordx4 v54, s[44:47], s60 offen lds\n"
"s_add_u32 m0, s55, 7168\n"
"s_add_u32 s60, s69, 32768\n"
"buffer_load_dwordx4 v54, s[44:47], s60 offen lds\n"
"s_waitcnt vmcnt(36)\n"
"ds_read_b64_tr_b16 a[4:5], v48 offset:10240\n"
"ds_read_b64_tr_b16 a[6:7], v48 offset:10496\n"
"ds_read_b64_tr_b16 a[32:33], v49 offset:10240\n"
"ds_read_b64_tr_b16 a[34:35], v49 offset:10496\n"
"s_waitcnt lgkmcnt(4)\n"
"s_add_u32 m0, s55, 8192\n"
"s_add_u32 s60, s70, 0\n"
"buffer_load_dwordx4 v54, s[44:47], s60 offen lds\n"
"s_add_u32 m0, s55, 9216\n"
"s_add_u32 s60, s70, 32768\n"
"buffer_load_dwordx4 v54, s[44:47], s60 offen lds\n"
"s_waitcnt vmcnt(36)\n"
"ds_read_b64_tr_b16 a[60:61], v48 offset:12288\n"
"ds_read_b64_tr_b16 a[62:63], v48 offset:12544\n"
"ds_read_b64_tr_b16 a[88:89], v49 offset:12288\n"
"ds_read_b64_tr_b16 a[90:91], v49 offset:12544\n"
"s_waitcnt lgkmcnt(4)\n"
"s_add_u32 m0, s55, 10240\n"
"s_add_u32 s60, s66, 0\n"
"buffer_load_dwordx4 v55, s[44:47], s60 offen lds\n"
"s_add_u32 m0, s55, 11264\n"
"s_add_u32 s60, s66, 32768\n"
"buffer_load_dwordx4 v55, s[44:47], s60 offen lds\n"
"s_waitcnt vmcnt(36)\n"
"ds_read_b64_tr_b16 a[116:117], v48 offset:14336\n"
"ds_read_b64_tr_b16 a[118:119], v48 offset:14592\n"
"ds_read_b64_tr_b16 a[144:145], v49 offset:14336\n"
"ds_read_b64_tr_b16 a[146:147], v49 offset:14592\n"
"s_waitcnt lgkmcnt(4)\n"
"s_add_u32 m0, s55, 12288\n"
"s_add_u32 s60, s67, 0\n"
"buffer_load_dwordx4 v55, s[44:47], s60 offen lds\n"
"s_add_u32 m0, s55, 13312\n"
"s_add_u32 s60, s67, 32768\n"
"buffer_load_dwordx4 v55, s[44:47], s60 offen lds\n"
"s_waitcnt vmcnt(36)\n"
"ds_read_b64_tr_b16 a[172:173], v48 offset:16384\n"
"ds_read_b64_tr_b16 a[174:175], v48 offset:16640\n"
"ds_read_b64_tr_b16 a[200:201], v49 offset:16384\n"
"ds_read_b64_tr_b16 a[202:203], v49 offset:16640\n"
"s_waitcnt lgkmcnt(4)\n"
"s_add_u32 m0, s55, 14336\n"
"s_add_u32 s60, s68, 0\n"
"buffer_load_dwordx4 v55, s[44:47], s60 offen lds\n"
"s_add_u32 m0, s55, 15360\n"
"s_add_u32 s60, s68, 32768\n"
"buffer_load_dwordx4 v55, s[44:47], s60 offen lds\n"
"s_waitcnt vmcnt(36)\n"
"ds_read_b64_tr_b16 a[228:229], v48 offset:18432\n"
"ds_read_b64_tr_b16 a[230:231], v48 offset:18688\n"
"ds_read_b64_tr_b16 v[24:25], v49 offset:18432\n"
"ds_read_b64_tr_b16 v[26:27], v49 offset:18688\n"
"s_waitcnt lgkmcnt(4)\n"
"s_add_u32 m0, s55, 16384\n"
"s_add_u32 s60, s69, 0\n"
"buffer_load_dwordx4 v55, s[44:47], s60 offen lds\n"
"s_add_u32 m0, s55, 17408\n"
"s_add_u32 s60, s69, 32768\n"
"buffer_load_dwordx4 v55, s[44:47], s60 offen lds\n"
"s_waitcnt vmcnt(36)\n"
"ds_read_b64_tr_b16 a[8:9], v48 offset:20480\n"
"ds_read_b64_tr_b16 a[10:11], v48 offset:20736\n"
"ds_read_b64_tr_b16 a[36:37], v49 offset:20480\n"
"ds_read_b64_tr_b16 a[38:39], v49 offset:20736\n"
"s_waitcnt lgkmcnt(4)\n"
"s_add_u32 m0, s55, 18432\n"
"s_add_u32 s60, s70, 0\n"
"buffer_load_dwordx4 v55, s[44:47], s60 offen lds\n"
"s_add_u32 m0, s55, 19456\n"
"s_add_u32 s60, s70, 32768\n"
"buffer_load_dwordx4 v55, s[44:47], s60 offen lds\n"
"s_waitcnt vmcnt(36)\n"
"ds_read_b64_tr_b16 a[64:65], v48 offset:22528\n"
"ds_read_b64_tr_b16 a[66:67], v48 offset:22784\n"
"ds_read_b64_tr_b16 a[92:93], v49 offset:22528\n"
"ds_read_b64_tr_b16 a[94:95], v49 offset:22784\n"
"s_waitcnt lgkmcnt(4)\n"
"s_add_u32 m0, s55, 20480\n"
"s_add_u32 s60, s66, 0\n"
"buffer_load_dwordx4 v56, s[44:47], s60 offen lds\n"
"s_add_u32 m0, s55, 21504\n"
"s_add_u32 s60, s66, 32768\n"
"buffer_load_dwordx4 v56, s[44:47], s60 offen lds\n"
"s_waitcnt vmcnt(36)\n"
"ds_read_b64_tr_b16 a[120:121], v48 offset:24576\n"
"ds_read_b64_tr_b16 a[122:123], v48 offset:24832\n"
"ds_read_b64_tr_b16 a[148:149], v49 offset:24576\n"
"ds_read_b64_tr_b16 a[150:151], v49 offset:24832\n"
"s_waitcnt lgkmcnt(4)\n"
"s_add_u32 m0, s55, 22528\n"
"s_add_u32 s60, s67, 0\n"
"buffer_load_dwordx4 v56, s[44:47], s60 offen lds\n"
"s_add_u32 m0, s55, 23552\n"
"s_add_u32 s60, s67, 32768\n"
"buffer_load_dwordx4 v56, s[44:47], s60 offen lds\n"
"s_waitcnt vmcnt(36)\n"
"ds_read_b64_tr_b16 a[176:177], v48 offset:26624\n"
"ds_read_b64_tr_b16 a[178:179], v48 offset:26880\n"
"ds_read_b64_tr_b16 a[204:205], v49 offset:26624\n"
"ds_read_b64_tr_b16 a[206:207], v49 offset:26880\n"
"s_waitcnt lgkmcnt(4)\n"
"s_add_u32 m0, s55, 24576\n"
"s_add_u32 s60, s68, 0\n"
"buffer_load_dwordx4 v56, s[44:47], s60 offen lds\n"
"s_add_u32 m0, s55, 25600\n"
"s_add_u32 s60, s68, 32768\n"
"buffer_load_dwordx4 v56, s[44:47], s60 offen lds\n"
"s_waitcnt vmcnt(36)\n"
"ds_read_b64_tr_b16 a[232:233], v48 offset:28672\n"
"ds_read_b64_tr_b16 a[234:235], v48 offset:28928\n"
"ds_read_b64_tr_b16 v[28:29], v49 offset:28672\n"
"ds_read_b64_tr_b16 v[30:31], v49 offset:28928\n"
"s_waitcnt lgkmcnt(4)\n"
"s_add_u32 m0, s55, 26624\n"
"s_add_u32 s60, s69, 0\n"
"buffer_load_dwordx4 v56, s[44:47], s60 offen lds\n"
"s_add_u32 m0, s55, 27648\n"
"s_add_u32 s60, s69, 32768\n"
"buffer_load_dwordx4 v56, s[44:47], s60 offen lds\n"
"s_waitcnt vmcnt(36)\n"
"ds_read_b64_tr_b16 a[12:13], v48 offset:30720\n"
"ds_read_b64_tr_b16 a[14:15], v48 offset:30976\n"
"ds_read_b64_tr_b16 a[40:41], v49 offset:30720\n"
"ds_read_b64_tr_b16 a[42:43], v49 offset:30976\n"
"s_waitcnt lgkmcnt(4)\n"
"s_add_u32 m0, s55, 28672\n"
"s_add_u32 s60, s70, 0\n"
"buffer_load_dwordx4 v56, s[44:47], s60 offen lds\n"
"s_add_u32 m0, s55, 29696\n"
"s_add_u32 s60, s70, 32768\n"
"buffer_load_dwordx4 v56, s[44:47], s60 offen lds\n"
"s_waitcnt vmcnt(36)\n"
"ds_read_b64_tr_b16 a[68:69], v48 offset:32768\n"
"ds_read_b64_tr_b16 a[70:71], v48 offset:33024\n"
"ds_read_b64_tr_b16 a[96:97], v49 offset:32768\n"
"ds_read_b64_tr_b16 a[98:99], v49 offset:33024\n"
"s_waitcnt lgkmcnt(4)\n"
"s_waitcnt vmcnt(34)\n"
"ds_read_b64_tr_b16 a[124:125], v48 offset:34816\n"
"ds_read_b64_tr_b16 a[126:127], v48 offset:35072\n"
"ds_read_b64_tr_b16 a[152:153], v49 offset:34816\n"
"ds_read_b64_tr_b16 a[154:155], v49 offset:35072\n"
"s_waitcnt lgkmcnt(4)\n"
"s_waitcnt vmcnt(32)\n"
"ds_read_b64_tr_b16 a[180:181], v48 offset:36864\n"
"ds_read_b64_tr_b16 a[182:183], v48 offset:37120\n"
"ds_read_b64_tr_b16 a[208:209], v49 offset:36864\n"
"ds_read_b64_tr_b16 a[210:211], v49 offset:37120\n"
"s_waitcnt lgkmcnt(4)\n"
"s_waitcnt vmcnt(30)\n"
"ds_read_b64_tr_b16 a[236:237], v48 offset:38912\n"
"ds_read_b64_tr_b16 a[238:239], v48 offset:39168\n"
"ds_read_b64_tr_b16 v[32:33], v49 offset:38912\n"
"ds_read_b64_tr_b16 v[34:35], v49 offset:39168\n"
"s_waitcnt lgkmcnt(4)\n"
"s_waitcnt vmcnt(28)\n"
"ds_read_b64_tr_b16 a[16:17], v48 offset:0\n"
"ds_read_b64_tr_b16 a[18:19], v48 offset:256\n"
"ds_read_b64_tr_b16 a[44:45], v49 offset:0\n"
"ds_read_b64_tr_b16 a[46:47], v49 offset:256\n"
"s_waitcnt lgkmcnt(4)\n"
"s_waitcnt vmcnt(26)\n"
"ds_read_b64_tr_b16 a[72:73], v48 offset:2048\n"
"ds_read_b64_tr_b16 a[74:75], v48 offset:2304\n"
"ds_read_b64_tr_b16 a[100:101], v49 offset:2048\n"
"ds_read_b64_tr_b16 a[102:103], v49 offset:2304\n"
"s_waitcnt lgkmcnt(4)\n"
"s_waitcnt vmcnt(24)\n"
"ds_read_b64_tr_b16 a[128:129], v48 offset:4096\n"
"ds_read_b64_tr_b16 a[130:131], v48 offset:4352\n"
"ds_read_b64_tr_b16 a[156:157], v49 offset:4096\n"
"ds_read_b64_tr_b16 a[158:159], v49 offset:4352\n"
"s_waitcnt lgkmcnt(4)\n"
"s_waitcnt vmcnt(22)\n"
"ds_read_b64_tr_b16 a[184:185], v48 offset:6144\n"
"ds_read_b64_tr_b16 a[186:187], v48 offset:6400\n"
"ds_read_b64_tr_b16 a[212:213], v49 offset:6144\n"
"ds_read_b64_tr_b16 a[214:215], v49 offset:6400\n"
"s_waitcnt lgkmcnt(4)\n"
"s_waitcnt vmcnt(20)\n"
"ds_read_b64_tr_b16 a[240:241], v48 offset:8192\n"
"ds_read_b64_tr_b16 a[242:243], v48 offset:8448\n"
"ds_read_b64_tr_b16 v[36:37], v49 offset:8192\n"
"ds_read_b64_tr_b16 v[38:39], v49 offset:8448\n"
"s_waitcnt lgkmcnt(4)\n"
"s_waitcnt vmcnt(18)\n"
"ds_read_b64_tr_b16 a[20:21], v48 offset:10240\n"
"ds_read_b64_tr_b16 a[22:23], v48 offset:10496\n"
"ds_read_b64_tr_b16 a[48:49], v49 offset:10240\n"
"ds_read_b64_tr_b16 a[50:51], v49 offset:10496\n"
"s_waitcnt lgkmcnt(4)\n"
"s_waitcnt vmcnt(16)\n"
"ds_read_b64_tr_b16 a[76:77], v48 offset:12288\n"
"ds_read_b64_tr_b16 a[78:79], v48 offset:12544\n"
"ds_read_b64_tr_b16 a[104:105], v49 offset:12288\n"
"ds_read_b64_tr_b16 a[106:107], v49 offset:12544\n"
"s_waitcnt lgkmcnt(4)\n"
"s_waitcnt vmcnt(14)\n"
"ds_read_b64_tr_b16 a[132:133], v48 offset:14336\n"
"ds_read_b64_tr_b16 a[134:135], v48 offset:14592\n"
"ds_read_b64_tr_b16 a[160:161], v49 offset:14336\n"
"ds_read_b64_tr_b16 a[162:163], v49 offset:14592\n"
"s_waitcnt lgkmcnt(4)\n"
"s_waitcnt vmcnt(12)\n"
"ds_read_b64_tr_b16 a[188:189], v48 offset:16384\n"
"ds_read_b64_tr_b16 a[190:191], v48 offset:16640\n"
"ds_read_b64_tr_b16 a[216:217], v49 offset:16384\n"
"ds_read_b64_tr_b16 a[218:219], v49 offset:16640\n"
"s_waitcnt lgkmcnt(4)\n"
"s_waitcnt vmcnt(10)\n"
"ds_read_b64_tr_b16 a[244:245], v48 offset:18432\n"
"ds_read_b64_tr_b16 a[246:247], v48 offset:18688\n"
"ds_read_b64_tr_b16 v[40:41], v49 offset:18432\n"
"ds_read_b64_tr_b16 v[42:43], v49 offset:18688\n"
"s_waitcnt lgkmcnt(4)\n"
"s_waitcnt vmcnt(8)\n"
"ds_read_b64_tr_b16 a[24:25], v48 offset:20480\n"
"ds_read_b64_tr_b16 a[26:27], v48 offset:20736\n"
"ds_read_b64_tr_b16 a[52:53], v49 offset:20480\n"
"ds_read_b64_tr_b16 a[54:55], v49 offset:20736\n"
"s_waitcnt lgkmcnt(4)\n"
"s_waitcnt vmcnt(6)\n"
"ds_read_b64_tr_b16 a[80:81], v48 offset:22528\n"
"ds_read_b64_tr_b16 a[82:83], v48 offset:22784\n"
"ds_read_b64_tr_b16 a[108:109], v49 offset:22528\n"
"ds_read_b64_tr_b16 a[110:111], v49 offset:22784\n"
"s_waitcnt lgkmcnt(4)\n"
"s_waitcnt vmcnt(4)\n"
"ds_read_b64_tr_b16 a[136:137], v48 offset:24576\n"
"ds_read_b64_tr_b16 a[138:139], v48 offset:24832\n"
"ds_read_b64_tr_b16 a[164:165], v49 offset:24576\n"
"ds_read_b64_tr_b16 a[166:167], v49 offset:24832\n"
"s_waitcnt lgkmcnt(4)\n"
"s_waitcnt vmcnt(2)\n"
"ds_read_b64_tr_b16 a[192:193], v48 offset:26624\n"
"ds_read_b64_tr_b16 a[194:195], v48 offset:26880\n"
"ds_read_b64_tr_b16 a[220:221], v49 offset:26624\n"
"ds_read_b64_tr_b16 a[222:223], v49 offset:26880\n"
"s_waitcnt lgkmcnt(4)\n"
"s_waitcnt vmcnt(0)\n"
"ds_read_b64_tr_b16 a[248:249], v48 offset:28672\n"
"ds_read_b64_tr_b16 a[250:251], v48 offset:28928\n"
"ds_read_b64_tr_b16 v[44:45], v49 offset:28672\n"
"ds_read_b64_tr_b16 v[46:47], v49 offset:28928\n"
"s_waitcnt lgkmcnt(4)\n"
"s_waitcnt vmcnt(0)\n"
"s_waitcnt lgkmcnt(0)\n"
"s_mov_b32 m0, s59\n"
"s_barrier\n"
"s_mov_b64 s[64:65], s[48:49]\n"
"s_mov_b32 s92, 0x80000000\n"
"s_mov_b32 s93, 0x80000000\n"
"s_mov_b32 s95, 0x80000000\n"
"s_mov_b32 s93, 0x80000000\n"
"s_mov_b32 s93, 0x80000000\n"
"s_mov_b32 s95, 0x80000000\n"
"s_mov_b32 s95, 0x80000000\n"
"s_mov_b32 s94, 0x80000000\n"
"s_mov_b32 s94, 0x80000000\n"
"v_mov_b32 v138, 0\n"
"v_mov_b32 v139, 0\n"
"v_mov_b32 v144, 0\n"
"v_mov_b32 v145, 0\n"
"v_mov_b32 v136, 0\n"
"v_mov_b32 v137, 0\n"
"v_mov_b32 v172, 0\n"
"v_mov_b32 v173, 0\n"
"v_mov_b32 v136, 0\n"
"v_mov_b32 v137, 0\n"
"s_cmp_eq_u32 s54, 0\n"
"s_cbranch_scc1 .LH_1s\n"
"s_cmp_eq_u32 s54, 1\n"
"s_cbranch_scc1 .LH_2s\n"
"s_cmp_eq_u32 s54, 2\n"
"s_cbranch_scc1 .LH_3s\n"
"s_cmp_eq_u32 s54, 3\n"
"s_cbranch_scc1 .LH_4s\n"
".p2align 6\n"
".LH_top:\n"
"v_mfma_f32_16x16x32_bf16 v[60:63], v[104:107], a[0:3], 0\n"
"v_mfma_f32_16x16x32_bf16 v[60:63], v[72:75], a[4:7], v[60:63]\n"
"global_load_dwordx4 v[48:51], v7, s[56:57] offset:0 sc1\n"
"v_mfma_f32_16x16x32_bf16 v[60:63], v[76:79], a[8:11], v[60:63]\n"
"v_mfma_f32_16x16x32_bf16 v[60:63], v[80:83], a[12:15], v[60:63]\n"
"s_waitcnt vmcnt(6)\n"
"v_mfma_f32_16x16x32_bf16 v[60:63], v[84:87], a[16:19], v[60:63]\n"
"s_waitcnt lgkmcnt(3)\n"
"v_add_f32 v100, v100, v68\n"
"v_add_f32 v101, v101, v69\n"
"v_mfma_f32_16x16x32_bf16 v[60:63], v[88:91], a[20:23], v[60:63]\n"
"s_waitcnt vmcnt(5)\n"
"v_add_f32 v102, v102, v70\n"
"v_add_f32 v103, v103, v71\n"
"v_mfma_f32_16x16x32_bf16 v[60:63], v[92:95], a[24:27], v[60:63]\n"
"global_load_dwordx4 v[52:55], v8, s[56:57] offset:0 sc1\n"
"v_add_f32 v100, v100, v96\n"
"v_mfma_f32_16x16x32_bf16 v[64:67], v[104:107], a[28:31], 0\n"
"buffer_load_dwordx2 v[136:137], v12, s[40:43], s50 offen sc1\n"
"v_mfma_f32_16x16x32_bf16 v[64:67], v[72:75], a[32:35], v[64:67]\n"
"ds_write_addtid_b32 v60 offset:0\n"
"v_add_f32 v101, v101, v97\n"
"v_mfma_f32_16x16x32_bf16 v[64:67], v[76:79], a[36:39], v[64:67]\n"
"ds_write_addtid_b32 v61 offset:4640\n"
"global_load_dwordx4 v[56:59], v9, s[56:57] offset:0 sc1\n"
"v_mfma_f32_16x16x32_bf16 v[64:67], v[80:83], a[40:43], v[64:67]\n"
"ds_write_addtid_b32 v62 offset:9344\n"
"v_add_f32 v102, v102, v98\n"
"v_mfma_f32_16x16x32_bf16 v[64:67], v[84:87], a[44:47], v[64:67]\n"
"ds_write_addtid_b32 v63 offset:13984\n"
"v_add_f32 v103, v103, v99\n"
"v_mfma_f32_16x16x32_bf16 v[64:67], v[88:91], a[48:51], v[64:67]\n"
"v_add_f32 v100, v100, v116\n"
"v_add_f32 v101, v101, v117\n"
"v_mfma_f32_16x16x32_bf16 v[64:67], v[92:95], a[52:55], v[64:67]\n"
"v_add_f32 v102, v102, v118\n"
"v_add_f32 v103, v103, v119\n"
"v_mfma_f32_16x16x32_bf16 v[68:71], v[104:107], a[56:59], 0\n"
"s_waitcnt vmcnt(7)\n"
"buffer_store_dwordx2 v[0:1], v14, s[40:43], s93 offen offset:0 sc1\n"
"v_mfma_f32_16x16x32_bf16 v[68:71], v[72:75], a[60:63], v[68:71]\n"
"s_add_u32 s95, s50, 0xffd00000\n"
"s_and_b32 s94, s95, 0xfbffffff\n"
"ds_write_addtid_b32 v64 offset:256\n"
"v_mfma_f32_16x16x32_bf16 v[68:71], v[76:79], a[64:67], v[68:71]\n"
"ds_write_addtid_b32 v65 offset:4896\n"
"global_load_dwordx4 v[60:63], v10, s[56:57] offset:0 sc1\n"
"v_mfma_f32_16x16x32_bf16 v[68:71], v[80:83], a[68:71], v[68:71]\n"
"ds_write_addtid_b32 v66 offset:9600\n"
"v_cvt_pk_bf16_f32 v96, v100, v101\n"
"v_mfma_f32_16x16x32_bf16 v[68:71], v[84:87], a[72:75], v[68:71]\n"
"ds_write_addtid_b32 v67 offset:14240\n"
"v_cvt_pk_bf16_f32 v97, v102, v103\n"
"v_mfma_f32_16x16x32_bf16 v[68:71], v[88:91], a[76:79], v[68:71]\n"
"s_waitcnt lgkmcnt(8)\n"
"v_add_f32 v124, v124, v128\n"
"v_add_f32 v125, v125, v129\n"
"v_mfma_f32_16x16x32_bf16 v[68:71], v[92:95], a[80:83], v[68:71]\n"
"v_add_f32 v126, v126, v130\n"
"v_add_f32 v127, v127, v131\n"
"v_mfma_f32_16x16x32_bf16 v[100:103], v[104:107], a[84:87], 0\n"
"global_load_dwordx4 v[64:67], v11, s[56:57] offset:0 sc1\n"
"v_add_f32 v124, v124, v132\n"
"v_mfma_f32_16x16x32_bf16 v[100:103], v[72:75], a[88:91], v[100:103]\n"
"ds_write_addtid_b32 v68 offset:512\n"
"v_add_f32 v125, v125, v133\n"
"v_mfma_f32_16x16x32_bf16 v[100:103], v[76:79], a[92:95], v[100:103]\n"
"ds_write_addtid_b32 v69 offset:5152\n"
"v_add_f32 v126, v126, v134\n"
"v_mfma_f32_16x16x32_bf16 v[100:103], v[80:83], a[96:99], v[100:103]\n"
"ds_write_addtid_b32 v70 offset:9856\n"
"v_add_f32 v127, v127, v135\n"
"v_mfma_f32_16x16x32_bf16 v[100:103], v[84:87], a[100:103], v[100:103]\n"
"ds_write_addtid_b32 v71 offset:14496\n"
"v_add_f32 v124, v124, v120\n"
"v_mfma_f32_16x16x32_bf16 v[100:103], v[88:91], a[104:107], v[100:103]\n"
"v_add_f32 v125, v125, v121\n"
"v_add_f32 v126, v126, v122\n"
"v_mfma_f32_16x16x32_bf16 v[100:103], v[92:95], a[108:111], v[100:103]\n"
"v_add_f32 v127, v127, v123\n"
"v_cvt_pk_bf16_f32 v98, v124, v125\n"
"v_mfma_f32_16x16x32_bf16 v[68:71], v[104:107], a[112:115], 0\n"
"v_cvt_pk_bf16_f32 v99, v126, v127\n"
"v_mfma_f32_16x16x32_bf16 v[68:71], v[72:75], a[116:119], v[68:71]\n"
"ds_write_addtid_b32 v100 offset:768\n"
"v_mfma_f32_16x16x32_bf16 v[68:71], v[76:79], a[120:123], v[68:71]\n"
"ds_write_addtid_b32 v101 offset:5408\n"
"v_mfma_f32_16x16x32_bf16 v[68:71], v[80:83], a[124:127], v[68:71]\n"
"ds_write_addtid_b32 v102 offset:10112\n"
"v_mfma_f32_16x16x32_bf16 v[68:71], v[84:87], a[128:131], v[68:71]\n"
"ds_write_addtid_b32 v103 offset:14752\n"
"v_mfma_f32_16x16x32_bf16 v[68:71], v[88:91], a[132:135], v[68:71]\n"
"v_mfma_f32_16x16x32_bf16 v[68:71], v[92:95], a[136:139], v[68:71]\n"
"v_mfma_f32_16x16x32_bf16 v[116:119], v[104:107], a[140:143], 0\n"
"v_mfma_f32_16x16x32_bf16 v[116:119], v[72:75], a[144:147], v[116:119]\n"
"ds_write_addtid_b32 v68 offset:1024\n"
"v_mfma_f32_16x16x32_bf16 v[116:119], v[76:79], a[148:151], v[116:119]\n"
"ds_write_addtid_b32 v69 offset:5664\n"
"v_mfma_f32_16x16x32_bf16 v[116:119], v[80:83], a[152:155], v[116:119]\n"
"ds_write_addtid_b32 v70 offset:10368\n"
"v_mfma_f32_16x16x32_bf16 v[116:119], v[84:87], a[156:159], v[116:119]\n"
"ds_write_addtid_b32 v71 offset:15008\n"
"v_mfma_f32_16x16x32_bf16 v[116:119], v[88:91], a[160:163], v[116:119]\n"
"buffer_store_dwordx4 v[96:99], v13, s[40:43], s94 offen sc1\n"
"v_mfma_f32_16x16x32_bf16 v[116:119], v[92:95], a[164:167], v[116:119]\n"
"v_mfma_f32_16x16x32_bf16 v[100:103], a[168:171], v[104:107], 0\n"
"v_mfma_f32_16x16x32_bf16 v[100:103], a[172:175], v[72:75], v[100:103]\n"
"ds_write_addtid_b32 v116 offset:1280\n"
"v_mfma_f32_16x16x32_bf16 v[100:103], a[176:179], v[76:79], v[100:103]\n"
"ds_write_addtid_b32 v117 offset:5920\n"
"v_mfma_f32_16x16x32_bf16 v[100:103], a[180:183], v[80:83], v[100:103]\n"
"ds_write_addtid_b32 v118 offset:10624\n"
"v_mfma_f32_16x16x32_bf16 v[100:103], a[184:187], v[84:87], v[100:103]\n"
"ds_write_addtid_b32 v119 offset:15264\n"
"v_mfma_f32_16x16x32_bf16 v[100:103], a[188:191], v[88:91], v[100:103]\n"
"v_mfma_f32_16x16x32_bf16 v[100:103], a[192:195], v[92:95], v[100:103]\n"
"v_mfma_f32_16x16x32_bf16 v[96:99], a[196:199], v[104:107], 0\n"
"v_mfma_f32_16x16x32_bf16 v[96:99], a[200:203], v[72:75], v[96:99]\n"
"v_mfma_f32_16x16x32_bf16 v[96:99], a[204:207], v[76:79], v[96:99]\n"
"v_mfma_f32_16x16x32_bf16 v[96:99], a[208:211], v[80:83], v[96:99]\n"
"s_add_u32 s58, s50, 0x180000\n"
"v_mfma_f32_16x16x32_bf16 v[96:99], a[212:215], v[84:87], v[96:99]\n"
"s_and_b32 s58, s58, 0x3ffffff\n"
"v_mfma_f32_16x16x32_bf16 v[96:99], a[216:219], v[88:91], v[96:99]\n"
"s_add_u32 s56, s40, s58\n"
"v_mfma_f32_16x16x32_bf16 v[96:99], a[220:223], v[92:95], v[96:99]\n"
"s_addc_u32 s57, s41, 0\n"
"v_mfma_f32_16x16x32_bf16 v[116:119], a[224:227], v[104:107], 0\n"
"s_waitcnt vmcnt(5)\n"
"v_cmp_ne_u64 vcc, s[48:49], v[136:137]\n"
"v_mfma_f32_16x16x32_bf16 v[116:119], a[228:231], v[72:75], v[116:119]\n"
"v_mfma_f32_16x16x32_bf16 v[116:119], a[232:235], v[76:79], v[116:119]\n"
"s_cbranch_vccnz .LH_3s\n"
".LH_3r:\n"
"v_mfma_f32_16x16x32_bf16 v[116:119], a[236:239], v[80:83], v[116:119]\n"
"s_add_u32 s50, s50, 0x180000\n"
"global_load_dwordx4 v[68:71], v5, s[56:57] offset:0 sc1\n"
"v_mfma_f32_16x16x32_bf16 v[116:119], a[240:243], v[84:87], v[116:119]\n"
"v_mfma_f32_16x16x32_bf16 v[116:119], a[244:247], v[88:91], v[116:119]\n"
"v_mfma_f32_16x16x32_bf16 v[116:119], a[248:251], v[92:95], v[116:119]\n"
"v_mfma_f32_16x16x32_bf16 v[120:123], a[252:255], v[104:107], 0\n"
"v_mfma_f32_16x16x32_bf16 v[120:123], v[24:27], v[72:75], v[120:123]\n"
"s_waitcnt lgkmcnt(0)\n"
"v_mfma_f32_16x16x32_bf16 v[120:123], v[28:31], v[76:79], v[120:123]\n"
"s_barrier\n"
"v_mfma_f32_16x16x32_bf16 v[120:123], v[32:35], v[80:83], v[120:123]\n"
"ds_read_b128 v[104:107], v2 offset:2048\n"
"ds_read_b128 v[124:127], v3 offset:512\n"
"v_mfma_f32_16x16x32_bf16 v[120:123], v[36:39], v[84:87], v[120:123]\n"
"global_load_dwordx4 v[72:75], v6, s[56:57] offset:0 sc1\n"
"ds_read_b128 v[128:131], v4 offset:3072\n"
"v_mfma_f32_16x16x32_bf16 v[120:123], v[40:43], v[88:91], v[120:123]\n"
"ds_read_b128 v[132:135], v2 offset:2304\n"
"ds_read_b128 v[136:139], v3 offset:768\n"
"v_mfma_f32_16x16x32_bf16 v[120:123], v[44:47], v[92:95], v[120:123]\n"
"global_load_dwordx4 v[76:79], v7, s[56:57] offset:0 sc1\n"
"ds_read_b128 v[140:143], v4 offset:3328\n"
"v_mfma_f32_16x16x32_bf16 v[88:91], v[108:111], a[0:3], 0\n"
"ds_read_b128 v[144:147], v2 offset:3584\n"
"ds_read_b128 v[148:151], v3 offset:1536\n"
"v_mfma_f32_16x16x32_bf16 v[88:91], v[112:115], a[4:7], v[88:91]\n"
"ds_read_b128 v[152:155], v4 offset:3584\n"
"ds_read_b128 v[156:159], v2 offset:3840\n"
"v_mfma_f32_16x16x32_bf16 v[88:91], v[48:51], a[8:11], v[88:91]\n"
"ds_read_b128 v[160:163], v3 offset:1792\n"
"ds_read_b128 v[164:167], v4 offset:3840\n"
"v_mfma_f32_16x16x32_bf16 v[88:91], v[52:55], a[12:15], v[88:91]\n"
"s_waitcnt vmcnt(5)\n"
"v_mfma_f32_16x16x32_bf16 v[88:91], v[56:59], a[16:19], v[88:91]\n"
"s_waitcnt lgkmcnt(9)\n"
"global_load_dwordx4 v[80:83], v8, s[56:57] offset:0 sc1\n"
"v_add_f32 v104, v104, v124\n"
"v_mfma_f32_16x16x32_bf16 v[88:91], v[60:63], a[20:23], v[88:91]\n"
"s_waitcnt vmcnt(5)\n"
"v_add_f32 v105, v105, v125\n"
"v_add_f32 v106, v106, v126\n"
"v_mfma_f32_16x16x32_bf16 v[88:91], v[64:67], a[24:27], v[88:91]\n"
"s_add_u32 s94, s50, 0xff000000\n"
"v_add_f32 v107, v107, v127\n"
"v_add_f32 v104, v104, v128\n"
"v_mfma_f32_16x16x32_bf16 v[168:171], v[108:111], a[28:31], 0\n"
"buffer_load_dwordx2 v[172:173], v12, s[40:43], s50 offen sc1\n"
"v_mfma_f32_16x16x32_bf16 v[168:171], v[112:115], a[32:35], v[168:171]\n"
"ds_write_addtid_b32 v88 offset:2048\n"
"v_add_f32 v105, v105, v129\n"
"v_mfma_f32_16x16x32_bf16 v[168:171], v[48:51], a[36:39], v[168:171]\n"
"ds_write_addtid_b32 v89 offset:6688\n"
"v_add_f32 v106, v106, v130\n"
"v_mfma_f32_16x16x32_bf16 v[168:171], v[52:55], a[40:43], v[168:171]\n"
"ds_write_addtid_b32 v90 offset:11392\n"
"v_add_f32 v107, v107, v131\n"
"v_mfma_f32_16x16x32_bf16 v[168:171], v[56:59], a[44:47], v[168:171]\n"
"ds_write_addtid_b32 v91 offset:16032\n"
"v_add_f32 v104, v104, v100\n"
"v_mfma_f32_16x16x32_bf16 v[168:171], v[60:63], a[48:51], v[168:171]\n"
"global_load_dwordx4 v[84:87], v9, s[56:57] offset:0 sc1\n"
"v_add_f32 v105, v105, v101\n"
"v_mfma_f32_16x16x32_bf16 v[168:171], v[64:67], a[52:55], v[168:171]\n"
"v_add_f32 v106, v106, v102\n"
"v_add_f32 v107, v107, v103\n"
"v_mfma_f32_16x16x32_bf16 v[124:127], v[108:111], a[56:59], 0\n"
"s_add_u32 s92, s50, 0xffd00000\n"
"s_and_b32 s93, s92, 0xfbffffff\n"
"v_cvt_pk_bf16_f32 v128, v104, v105\n"
"v_mfma_f32_16x16x32_bf16 v[124:127], v[112:115], a[60:63], v[124:127]\n"
"ds_write_addtid_b32 v168 offset:2304\n"
"v_cvt_pk_bf16_f32 v129, v106, v107\n"
"v_mfma_f32_16x16x32_bf16 v[124:127], v[48:51], a[64:67], v[124:127]\n"
"s_waitcnt lgkmcnt(11)\n"
"ds_write_addtid_b32 v169 offset:6944\n"
"v_add_f32 v132, v132, v136\n"
"v_mfma_f32_16x16x32_bf16 v[124:127], v[52:55], a[68:71], v[124:127]\n"
"ds_write_addtid_b32 v170 offset:11648\n"
"v_add_f32 v133, v133, v137\n"
"v_mfma_f32_16x16x32_bf16 v[124:127], v[56:59], a[72:75], v[124:127]\n"
"s_waitcnt vmcnt(6)\n"
"ds_write_addtid_b32 v171 offset:16288\n"
"global_load_dwordx4 v[88:91], v10, s[56:57] offset:0 sc1\n"
"v_mfma_f32_16x16x32_bf16 v[124:127], v[60:63], a[76:79], v[124:127]\n"
"global_load_dwordx4 v[92:95], v11, s[56:57] offset:0 sc1\n"
"v_add_f32 v134, v134, v138\n"
"v_mfma_f32_16x16x32_bf16 v[124:127], v[64:67], a[80:83], v[124:127]\n"
"v_add_f32 v135, v135, v139\n"
"v_add_f32 v132, v132, v140\n"
"v_mfma_f32_16x16x32_bf16 v[100:103], v[108:111], a[84:87], 0\n"
"v_add_f32 v133, v133, v141\n"
"v_add_f32 v134, v134, v142\n"
"v_mfma_f32_16x16x32_bf16 v[100:103], v[112:115], a[88:91], v[100:103]\n"
"ds_write_addtid_b32 v124 offset:2560\n"
"v_add_f32 v135, v135, v143\n"
"v_mfma_f32_16x16x32_bf16 v[100:103], v[48:51], a[92:95], v[100:103]\n"
"ds_write_addtid_b32 v125 offset:7200\n"
"v_add_f32 v132, v132, v96\n"
"v_mfma_f32_16x16x32_bf16 v[100:103], v[52:55], a[96:99], v[100:103]\n"
"ds_write_addtid_b32 v126 offset:11904\n"
"v_add_f32 v133, v133, v97\n"
"v_mfma_f32_16x16x32_bf16 v[100:103], v[56:59], a[100:103], v[100:103]\n"
"ds_write_addtid_b32 v127 offset:16544\n"
"v_add_f32 v134, v134, v98\n"
"v_mfma_f32_16x16x32_bf16 v[100:103], v[60:63], a[104:107], v[100:103]\n"
"v_add_f32 v135, v135, v99\n"
"v_cvt_pk_bf16_f32 v130, v132, v133\n"
"v_mfma_f32_16x16x32_bf16 v[100:103], v[64:67], a[108:111], v[100:103]\n"
"buffer_store_dwordx2 v[0:1], v14, s[40:43], s95 offen offset:0 sc1\n"
"v_cvt_pk_bf16_f32 v131, v134, v135\n"
"v_mfma_f32_16x16x32_bf16 v[96:99], v[108:111], a[112:115], 0\n"
"buffer_store_dwordx4 v[128:131], v13, s[40:43], s93 offen sc1\n"
"v_mfma_f32_16x16x32_bf16 v[96:99], v[112:115], a[116:119], v[96:99]\n"
"s_waitcnt lgkmcnt(15)\n"
"ds_write_addtid_b32 v100 offset:2816\n"
"v_add_f32 v144, v144, v148\n"
"v_mfma_f32_16x16x32_bf16 v[96:99], v[48:51], a[120:123], v[96:99]\n"
"ds_write_addtid_b32 v101 offset:7456\n"
"v_add_f32 v145, v145, v149\n"
"v_mfma_f32_16x16x32_bf16 v[96:99], v[52:55], a[124:127], v[96:99]\n"
"ds_write_addtid_b32 v102 offset:12160\n"
"v_add_f32 v146, v146, v150\n"
"v_mfma_f32_16x16x32_bf16 v[96:99], v[56:59], a[128:131], v[96:99]\n"
"ds_write_addtid_b32 v103 offset:16800\n"
"v_add_f32 v147, v147, v151\n"
"v_mfma_f32_16x16x32_bf16 v[96:99], v[60:63], a[132:135], v[96:99]\n"
"v_add_f32 v144, v144, v152\n"
"v_add_f32 v145, v145, v153\n"
"v_mfma_f32_16x16x32_bf16 v[96:99], v[64:67], a[136:139], v[96:99]\n"
"v_add_f32 v146, v146, v154\n"
"v_add_f32 v147, v147, v155\n"
"v_mfma_f32_16x16x32_bf16 v[100:103], v[108:111], a[140:143], 0\n"
"v_add_f32 v144, v144, v116\n"
"v_add_f32 v145, v145, v117\n"
"v_mfma_f32_16x16x32_bf16 v[100:103], v[112:115], a[144:147], v[100:103]\n"
"ds_write_addtid_b32 v96 offset:3072\n"
"v_add_f32 v146, v146, v118\n"
"v_mfma_f32_16x16x32_bf16 v[100:103], v[48:51], a[148:151], v[100:103]\n"
"ds_write_addtid_b32 v97 offset:7712\n"
"v_add_f32 v147, v147, v119\n"
"v_mfma_f32_16x16x32_bf16 v[100:103], v[52:55], a[152:155], v[100:103]\n"
"ds_write_addtid_b32 v98 offset:12416\n"
"v_cvt_pk_bf16_f32 v104, v144, v145\n"
"v_mfma_f32_16x16x32_bf16 v[100:103], v[56:59], a[156:159], v[100:103]\n"
"ds_write_addtid_b32 v99 offset:17056\n"
"v_cvt_pk_bf16_f32 v105, v146, v147\n"
"v_mfma_f32_16x16x32_bf16 v[100:103], v[60:63], a[160:163], v[100:103]\n"
"s_waitcnt lgkmcnt(15)\n"
"v_add_f32 v156, v156, v160\n"
"v_add_f32 v157, v157, v161\n"
"v_mfma_f32_16x16x32_bf16 v[100:103], v[64:67], a[164:167], v[100:103]\n"
"v_add_f32 v158, v158, v162\n"
"v_add_f32 v159, v159, v163\n"
"v_mfma_f32_16x16x32_bf16 v[96:99], v[108:111], a[224:227], 0\n"
"v_add_f32 v156, v156, v164\n"
"v_add_f32 v157, v157, v165\n"
"v_mfma_f32_16x16x32_bf16 v[96:99], v[112:115], a[228:231], v[96:99]\n"
"ds_write_addtid_b32 v100 offset:3328\n"
"v_add_f32 v158, v158, v166\n"
"v_mfma_f32_16x16x32_bf16 v[96:99], v[48:51], a[232:235], v[96:99]\n"
"ds_write_addtid_b32 v101 offset:7968\n"
"v_add_f32 v159, v159, v167\n"
"v_mfma_f32_16x16x32_bf16 v[96:99], v[52:55], a[236:239], v[96:99]\n"
"ds_write_addtid_b32 v102 offset:12672\n"
"v_add_f32 v156, v156, v120\n"
"v_mfma_f32_16x16x32_bf16 v[96:99], v[56:59], a[240:243], v[96:99]\n"
"ds_write_addtid_b32 v103 offset:17312\n"
"v_add_f32 v157, v157, v121\n"
"v_mfma_f32_16x16x32_bf16 v[96:99], v[60:63], a[244:247], v[96:99]\n"
"v_add_f32 v158, v158, v122\n"
"v_add_f32 v159, v159, v123\n"
"v_mfma_f32_16x16x32_bf16 v[96:99], v[64:67], a[248:251], v[96:99]\n"
"v_cvt_pk_bf16_f32 v106, v156, v157\n"
"v_cvt_pk_bf16_f32 v107, v158, v159\n"
"v_mfma_f32_16x16x32_bf16 v[100:103], v[108:111], a[252:255], 0\n"
"buffer_store_dwordx4 v[104:107], v15, s[40:43], s93 offen sc1\n"
"v_mfma_f32_16x16x32_bf16 v[100:103], v[112:115], v[24:27], v[100:103]\n"
"s_cmp_lt_u32 s94, 0x8000000\n"
"s_cselect_b32 s94, 0x80000000, s94\n"
"ds_write_addtid_b32 v96 offset:3584\n"
"v_mfma_f32_16x16x32_bf16 v[100:103], v[48:51], v[28:31], v[100:103]\n"
"ds_write_addtid_b32 v97 offset:8224\n"
"v_mfma_f32_16x16x32_bf16 v[100:103], v[52:55], v[32:35], v[100:103]\n"
"ds_write_addtid_b32 v98 offset:12928\n"
"v_mfma_f32_16x16x32_bf16 v[100:103], v[56:59], v[36:39], v[100:103]\n"
"ds_write_addtid_b32 v99 offset:17568\n"
"v_mfma_f32_16x16x32_bf16 v[100:103], v[60:63], v[40:43], v[100:103]\n"
"buffer_load_dwordx2 v[136:137], v17, s[40:43], s94 offen offset:0 sc1\n"
"v_mfma_f32_16x16x32_bf16 v[100:103], v[64:67], v[44:47], v[100:103]\n"
"s_add_u32 s58, s50, 0x180000\n"
"v_mfma_f32_16x16x32_bf16 v[104:107], a[168:171], v[108:111], 0\n"
"s_and_b32 s58, s58, 0x3ffffff\n"
"v_mfma_f32_16x16x32_bf16 v[104:107], a[172:175], v[112:115], v[104:107]\n"
"s_add_u32 s56, s40, s58\n"
"ds_write_addtid_b32 v100 offset:3840\n"
"v_mfma_f32_16x16x32_bf16 v[104:107], a[176:179], v[48:51], v[104:107]\n"
"s_addc_u32 s57, s41, 0\n"
"ds_write_addtid_b32 v101 offset:8480\n"
"v_mfma_f32_16x16x32_bf16 v[104:107], a[180:183], v[52:55], v[104:107]\n"
"s_waitcnt vmcnt(7)\n"
"ds_write_addtid_b32 v102 offset:13184\n"
"v_cmp_ne_u64 vcc, s[48:49], v[172:173]\n"
"v_mfma_f32_16x16x32_bf16 v[104:107], a[184:187], v[56:59], v[104:107]\n"
"ds_write_addtid_b32 v103 offset:17824\n"
"v_mfma_f32_16x16x32_bf16 v[104:107], a[188:191], v[60:63], v[104:107]\n"
"s_cbranch_vccnz .LH_4s\n"
".LH_4r:\n"
"v_mfma_f32_16x16x32_bf16 v[104:107], a[192:195], v[64:67], v[104:107]\n"
"s_add_u32 s50, s50, 0x180000\n"
"global_load_dwordx4 v[96:99], v5, s[56:57] offset:0 sc1\n"
"v_mfma_f32_16x16x32_bf16 v[116:119], a[196:199], v[108:111], 0\n"
"global_load_dwordx4 v[100:103], v6, s[56:57] offset:0 sc1\n"
"v_mfma_f32_16x16x32_bf16 v[116:119], a[200:203], v[112:115], v[116:119]\n"
"s_waitcnt lgkmcnt(0)\n"
"v_mfma_f32_16x16x32_bf16 v[116:119], a[204:207], v[48:51], v[116:119]\n"
"s_barrier\n"
"v_mfma_f32_16x16x32_bf16 v[116:119], a[208:211], v[52:55], v[116:119]\n"
"ds_read_b128 v[108:111], v2 offset:0\n"
"ds_read_b128 v[112:115], v3 offset:2560\n"
"v_mfma_f32_16x16x32_bf16 v[116:119], a[212:215], v[56:59], v[116:119]\n"
"ds_read_b128 v[120:123], v4 offset:1024\n"
"ds_read_b128 v[124:127], v2 offset:256\n"
"v_mfma_f32_16x16x32_bf16 v[116:119], a[216:219], v[60:63], v[116:119]\n"
"ds_read_b128 v[128:131], v3 offset:2816\n"
"ds_read_b128 v[132:135], v4 offset:1280\n"
"v_mfma_f32_16x16x32_bf16 v[116:119], a[220:223], v[64:67], v[116:119]\n"
"v_mfma_f32_16x16x32_bf16 v[56:59], v[68:71], a[0:3], 0\n"
"v_mfma_f32_16x16x32_bf16 v[56:59], v[72:75], a[4:7], v[56:59]\n"
"v_mfma_f32_16x16x32_bf16 v[56:59], v[76:79], a[8:11], v[56:59]\n"
"global_load_dwordx4 v[48:51], v7, s[56:57] offset:0 sc1\n"
"v_mfma_f32_16x16x32_bf16 v[56:59], v[80:83], a[12:15], v[56:59]\n"
"s_waitcnt vmcnt(8)\n"
"buffer_load_dwordx2 v[138:139], v12, s[40:43], s50 offen sc1\n"
"v_mfma_f32_16x16x32_bf16 v[56:59], v[84:87], a[16:19], v[56:59]\n"
"s_waitcnt lgkmcnt(3)\n"
"v_add_f32 v108, v108, v112\n"
"v_add_f32 v109, v109, v113\n"
"v_mfma_f32_16x16x32_bf16 v[56:59], v[88:91], a[20:23], v[56:59]\n"
"s_waitcnt vmcnt(8)\n"
"v_add_f32 v110, v110, v114\n"
"v_add_f32 v111, v111, v115\n"
"v_mfma_f32_16x16x32_bf16 v[56:59], v[92:95], a[24:27], v[56:59]\n"
"v_add_f32 v108, v108, v120\n"
"v_add_f32 v109, v109, v121\n"
"v_mfma_f32_16x16x32_bf16 v[64:67], v[68:71], a[28:31], 0\n"
"global_load_dwordx4 v[52:55], v8, s[56:57] offset:0 sc1\n"
"v_add_f32 v110, v110, v122\n"
"v_mfma_f32_16x16x32_bf16 v[64:67], v[72:75], a[32:35], v[64:67]\n"
"ds_write_addtid_b32 v56 offset:0\n"
"v_add_f32 v111, v111, v123\n"
"v_mfma_f32_16x16x32_bf16 v[64:67], v[76:79], a[36:39], v[64:67]\n"
"ds_write_addtid_b32 v57 offset:4640\n"
"v_add_f32 v108, v108, v104\n"
"v_mfma_f32_16x16x32_bf16 v[64:67], v[80:83], a[40:43], v[64:67]\n"
"ds_write_addtid_b32 v58 offset:9344\n"
"v_add_f32 v109, v109, v105\n"
"v_mfma_f32_16x16x32_bf16 v[64:67], v[84:87], a[44:47], v[64:67]\n"
"ds_write_addtid_b32 v59 offset:13984\n"
"v_add_f32 v110, v110, v106\n"
"v_mfma_f32_16x16x32_bf16 v[64:67], v[88:91], a[48:51], v[64:67]\n"
"v_add_f32 v111, v111, v107\n"
"v_mfma_f32_16x16x32_bf16 v[64:67], v[92:95], a[52:55], v[64:67]\n"
"s_add_u32 s95, s50, 0xffd00000\n"
"s_and_b32 s93, s95, 0xfbffffff\n"
"global_load_dwordx4 v[56:59], v9, s[56:57] offset:0 sc1\n"
"v_mfma_f32_16x16x32_bf16 v[104:107], v[68:71], a[56:59], 0\n"
"v_cvt_pk_bf16_f32 v112, v108, v109\n"
"v_cvt_pk_bf16_f32 v113, v110, v111\n"
"v_mfma_f32_16x16x32_bf16 v[104:107], v[72:75], a[60:63], v[104:107]\n"
"s_waitcnt lgkmcnt(4)\n"
"ds_write_addtid_b32 v64 offset:256\n"
"v_add_f32 v124, v124, v128\n"
"v_mfma_f32_16x16x32_bf16 v[104:107], v[76:79], a[64:67], v[104:107]\n"
"ds_write_addtid_b32 v65 offset:4896\n"
"v_add_f32 v125, v125, v129\n"
"v_mfma_f32_16x16x32_bf16 v[104:107], v[80:83], a[68:71], v[104:107]\n"
"ds_write_addtid_b32 v66 offset:9600\n"
"global_load_dwordx4 v[60:63], v10, s[56:57] offset:0 sc1\n"
"v_mfma_f32_16x16x32_bf16 v[104:107], v[84:87], a[72:75], v[104:107]\n"
"ds_write_addtid_b32 v67 offset:14240\n"
"v_add_f32 v126, v126, v130\n"
"v_mfma_f32_16x16x32_bf16 v[104:107], v[88:91], a[76:79], v[104:107]\n"
"v_add_f32 v127, v127, v131\n"
"v_add_f32 v124, v124, v132\n"
"v_mfma_f32_16x16x32_bf16 v[104:107], v[92:95], a[80:83], v[104:107]\n"
"v_add_f32 v125, v125, v133\n"
"v_add_f32 v126, v126, v134\n"
"v_mfma_f32_16x16x32_bf16 v[108:111], v[68:71], a[84:87], 0\n"
"v_add_f32 v127, v127, v135\n"
"v_add_f32 v124, v124, v116\n"
"v_mfma_f32_16x16x32_bf16 v[108:111], v[72:75], a[88:91], v[108:111]\n"
"ds_write_addtid_b32 v104 offset:512\n"
"v_add_f32 v125, v125, v117\n"
"v_mfma_f32_16x16x32_bf16 v[108:111], v[76:79], a[92:95], v[108:111]\n"
"ds_write_addtid_b32 v105 offset:5152\n"
"global_load_dwordx4 v[64:67], v11, s[56:57] offset:0 sc1\n"
"v_mfma_f32_16x16x32_bf16 v[108:111], v[80:83], a[96:99], v[108:111]\n"
"ds_write_addtid_b32 v106 offset:9856\n"
"v_add_f32 v126, v126, v118\n"
"v_mfma_f32_16x16x32_bf16 v[108:111], v[84:87], a[100:103], v[108:111]\n"
"ds_write_addtid_b32 v107 offset:14496\n"
"v_add_f32 v127, v127, v119\n"
"v_mfma_f32_16x16x32_bf16 v[108:111], v[88:91], a[104:107], v[108:111]\n"
"v_cvt_pk_bf16_f32 v114, v124, v125\n"
"v_cvt_pk_bf16_f32 v115, v126, v127\n"
"v_mfma_f32_16x16x32_bf16 v[108:111], v[92:95], a[108:111], v[108:111]\n"
"s_waitcnt vmcnt(9)\n"
"buffer_store_dwordx2 v[0:1], v16, s[40:43], s92 offen offset:0 sc1\n"
"v_mfma_f32_16x16x32_bf16 v[104:107], v[68:71], a[112:115], 0\n"
"v_mfma_f32_16x16x32_bf16 v[104:107], v[72:75], a[116:119], v[104:107]\n"
"ds_write_addtid_b32 v108 offset:768\n"
"v_mfma_f32_16x16x32_bf16 v[104:107], v[76:79], a[120:123], v[104:107]\n"
"buffer_store_dwordx4 v[112:115], v13, s[40:43], s93 offen sc1\n"
"ds_write_addtid_b32 v109 offset:5408\n"
"v_mfma_f32_16x16x32_bf16 v[104:107], v[80:83], a[124:127], v[104:107]\n"
"ds_write_addtid_b32 v110 offset:10112\n"
"v_mfma_f32_16x16x32_bf16 v[104:107], v[84:87], a[128:131], v[104:107]\n"
"s_waitcnt vmcnt(10)\n"
"ds_write_addtid_b32 v111 offset:14752\n"
"v_cmp_ne_u64 vcc, s[64:65], v[136:137]\n"
"v_mfma_f32_16x16x32_bf16 v[104:107], v[88:91], a[132:135], v[104:107]\n"
"v_mfma_f32_16x16x32_bf16 v[104:107], v[92:95], a[136:139], v[104:107]\n"
"s_cbranch_vccnz .LH_5s\n"
".LH_5r:\n"
"v_mfma_f32_16x16x32_bf16 v[108:111], v[68:71], a[140:143], 0\n"
"buffer_load_dwordx4 v[112:115], v18, s[40:43], s94 offen offset:0 sc1\n"
"v_mfma_f32_16x16x32_bf16 v[108:111], v[72:75], a[144:147], v[108:111]\n"
"ds_write_addtid_b32 v104 offset:1024\n"
"v_mfma_f32_16x16x32_bf16 v[108:111], v[76:79], a[148:151], v[108:111]\n"
"ds_write_addtid_b32 v105 offset:5664\n"
"v_mfma_f32_16x16x32_bf16 v[108:111], v[80:83], a[152:155], v[108:111]\n"
"ds_write_addtid_b32 v106 offset:10368\n"
"v_mfma_f32_16x16x32_bf16 v[108:111], v[84:87], a[156:159], v[108:111]\n"
"ds_write_addtid_b32 v107 offset:15008\n"
"v_mfma_f32_16x16x32_bf16 v[108:111], v[88:91], a[160:163], v[108:111]\n"
"buffer_load_dwordx4 v[116:119], v18, s[40:43], s94 offen offset:1024 sc1\n"
"v_mfma_f32_16x16x32_bf16 v[108:111], v[92:95], a[164:167], v[108:111]\n"
"v_mfma_f32_16x16x32_bf16 v[104:107], v[68:71], a[224:227], 0\n"
"v_mfma_f32_16x16x32_bf16 v[104:107], v[72:75], a[228:231], v[104:107]\n"
"ds_write_addtid_b32 v108 offset:1280\n"
"v_mfma_f32_16x16x32_bf16 v[104:107], v[76:79], a[232:235], v[104:107]\n"
"ds_write_addtid_b32 v109 offset:5920\n"
"v_mfma_f32_16x16x32_bf16 v[104:107], v[80:83], a[236:239], v[104:107]\n"
"ds_write_addtid_b32 v110 offset:10624\n"
"v_mfma_f32_16x16x32_bf16 v[104:107], v[84:87], a[240:243], v[104:107]\n"
"ds_write_addtid_b32 v111 offset:15264\n"
"v_mfma_f32_16x16x32_bf16 v[104:107], v[88:91], a[244:247], v[104:107]\n"
"v_mfma_f32_16x16x32_bf16 v[104:107], v[92:95], a[248:251], v[104:107]\n"
"v_mfma_f32_16x16x32_bf16 v[120:123], v[68:71], a[252:255], 0\n"
"v_mfma_f32_16x16x32_bf16 v[120:123], v[72:75], v[24:27], v[120:123]\n"
"s_add_u32 s58, s50, 0x180000\n"
"ds_write_addtid_b32 v104 offset:1536\n"
"v_mfma_f32_16x16x32_bf16 v[120:123], v[76:79], v[28:31], v[120:123]\n"
"s_and_b32 s58, s58, 0x3ffffff\n"
"ds_write_addtid_b32 v105 offset:6176\n"
"v_mfma_f32_16x16x32_bf16 v[120:123], v[80:83], v[32:35], v[120:123]\n"
"s_add_u32 s56, s40, s58\n"
"ds_write_addtid_b32 v106 offset:10880\n"
"v_mfma_f32_16x16x32_bf16 v[120:123], v[84:87], v[36:39], v[120:123]\n"
"s_addc_u32 s57, s41, 0\n"
"ds_write_addtid_b32 v107 offset:15520\n"
"v_mfma_f32_16x16x32_bf16 v[120:123], v[88:91], v[40:43], v[120:123]\n"
"s_waitcnt vmcnt(8)\n"
"v_cmp_ne_u64 vcc, s[48:49], v[138:139]\n"
"v_mfma_f32_16x16x32_bf16 v[120:123], v[92:95], v[44:47], v[120:123]\n"
"v_mfma_f32_16x16x32_bf16 v[108:111], a[168:171], v[68:71], 0\n"
"s_cbranch_vccnz .LH_1s\n"
".LH_1r:\n"
"v_mfma_f32_16x16x32_bf16 v[108:111], a[172:175], v[72:75], v[108:111]\n"
"s_add_u32 s50, s50, 0x180000\n"
"ds_write_addtid_b32 v120 offset:1792\n"
"v_mfma_f32_16x16x32_bf16 v[108:111], a[176:179], v[76:79], v[108:111]\n"
"ds_write_addtid_b32 v121 offset:6432\n"
"v_mfma_f32_16x16x32_bf16 v[108:111], a[180:183], v[80:83], v[108:111]\n"
"ds_write_addtid_b32 v122 offset:11136\n"
"v_mfma_f32_16x16x32_bf16 v[108:111], a[184:187], v[84:87], v[108:111]\n"
"ds_write_addtid_b32 v123 offset:15776\n"
"global_load_dwordx4 v[104:107], v5, s[56:57] offset:0 sc1\n"
"v_mfma_f32_16x16x32_bf16 v[108:111], a[188:191], v[88:91], v[108:111]\n"
"v_mfma_f32_16x16x32_bf16 v[108:111], a[192:195], v[92:95], v[108:111]\n"
"v_mfma_f32_16x16x32_bf16 v[120:123], a[196:199], v[68:71], 0\n"
"v_mfma_f32_16x16x32_bf16 v[120:123], a[200:203], v[72:75], v[120:123]\n"
"s_waitcnt lgkmcnt(0)\n"
"v_mfma_f32_16x16x32_bf16 v[120:123], a[204:207], v[76:79], v[120:123]\n"
"s_barrier\n"
"v_mfma_f32_16x16x32_bf16 v[120:123], a[208:211], v[80:83], v[120:123]\n"
"global_load_dwordx4 v[72:75], v6, s[56:57] offset:0 sc1\n"
"ds_read_b128 v[68:71], v2 offset:2048\n"
"v_mfma_f32_16x16x32_bf16 v[120:123], a[212:215], v[84:87], v[120:123]\n"
"ds_read_b128 v[124:127], v3 offset:512\n"
"ds_read_b128 v[128:131], v4 offset:3072\n"
"v_mfma_f32_16x16x32_bf16 v[120:123], a[216:219], v[88:91], v[120:123]\n"
"ds_read_b128 v[132:135], v2 offset:2304\n"
"ds_read_b128 v[136:139], v3 offset:768\n"
"v_mfma_f32_16x16x32_bf16 v[120:123], a[220:223], v[92:95], v[120:123]\n"
"ds_read_b128 v[140:143], v4 offset:3328\n"
"v_mfma_f32_16x16x32_bf16 v[88:91], v[96:99], a[0:3], 0\n"
"global_load_dwordx4 v[76:79], v7, s[56:57] offset:0 sc1\n"
"v_mfma_f32_16x16x32_bf16 v[88:91], v[100:103], a[4:7], v[88:91]\n"
"s_waitcnt vmcnt(10)\n"
"v_mfma_f32_16x16x32_bf16 v[88:91], v[48:51], a[8:11], v[88:91]\n"
"v_mfma_f32_16x16x32_bf16 v[88:91], v[52:55], a[12:15], v[88:91]\n"
"s_waitcnt vmcnt(8)\n"
"v_mfma_f32_16x16x32_bf16 v[88:91], v[56:59], a[16:19], v[88:91]\n"
"s_waitcnt lgkmcnt(3)\n"
"v_add_f32 v68, v68, v124\n"
"v_add_f32 v69, v69, v125\n"
"v_mfma_f32_16x16x32_bf16 v[88:91], v[60:63], a[20:23], v[88:91]\n"
"s_waitcnt vmcnt(7)\n"
"global_load_dwordx4 v[80:83], v8, s[56:57] offset:0 sc1\n"
"v_add_f32 v70, v70, v126\n"
"v_mfma_f32_16x16x32_bf16 v[88:91], v[64:67], a[24:27], v[88:91]\n"
"buffer_load_dwordx2 v[144:145], v12, s[40:43], s50 offen sc1\n"
"v_mfma_f32_16x16x32_bf16 v[92:95], v[96:99], a[28:31], 0\n"
"v_add_f32 v71, v71, v127\n"
"v_add_f32 v68, v68, v128\n"
"v_mfma_f32_16x16x32_bf16 v[92:95], v[100:103], a[32:35], v[92:95]\n"
"s_waitcnt vmcnt(7)\n"
"buffer_store_dwordx2 v[0:1], v14, s[40:43], s95 offen offset:0 sc1\n"
"ds_write_addtid_b32 v88 offset:2048\n"
"v_mfma_f32_16x16x32_bf16 v[92:95], v[48:51], a[36:39], v[92:95]\n"
"ds_write_addtid_b32 v89 offset:6688\n"
"v_add_f32 v69, v69, v129\n"
"v_mfma_f32_16x16x32_bf16 v[92:95], v[52:55], a[40:43], v[92:95]\n"
"ds_write_addtid_b32 v90 offset:11392\n"
"global_load_dwordx4 v[84:87], v9, s[56:57] offset:0 sc1\n"
"v_mfma_f32_16x16x32_bf16 v[92:95], v[56:59], a[44:47], v[92:95]\n"
"ds_write_addtid_b32 v91 offset:16032\n"
"v_add_f32 v70, v70, v130\n"
"v_mfma_f32_16x16x32_bf16 v[92:95], v[60:63], a[48:51], v[92:95]\n"
"v_add_f32 v71, v71, v131\n"
"v_add_f32 v68, v68, v108\n"
"v_mfma_f32_16x16x32_bf16 v[92:95], v[64:67], a[52:55], v[92:95]\n"
"v_add_f32 v69, v69, v109\n"
"v_add_f32 v70, v70, v110\n"
"v_mfma_f32_16x16x32_bf16 v[124:127], v[96:99], a[56:59], 0\n"
"v_add_f32 v71, v71, v111\n"
"v_mfma_f32_16x16x32_bf16 v[124:127], v[100:103], a[60:63], v[124:127]\n"
"ds_write_addtid_b32 v92 offset:2304\n"
"global_load_dwordx4 v[88:91], v10, s[56:57] offset:0 sc1\n"
"v_mfma_f32_16x16x32_bf16 v[124:127], v[48:51], a[64:67], v[124:127]\n"
"s_add_u32 s93, s50, 0xffd00000\n"
"s_and_b32 s95, s93, 0xfbffffff\n"
"ds_write_addtid_b32 v93 offset:6944\n"
"v_mfma_f32_16x16x32_bf16 v[124:127], v[52:55], a[68:71], v[124:127]\n"
"ds_write_addtid_b32 v94 offset:11648\n"
"v_cvt_pk_bf16_f32 v108, v68, v69\n"
"v_mfma_f32_16x16x32_bf16 v[124:127], v[56:59], a[72:75], v[124:127]\n"
"s_waitcnt vmcnt(8)\n"
"ds_write_addtid_b32 v95 offset:16288\n"
"v_cvt_pk_bf16_f32 v109, v70, v71\n"
"v_mfma_f32_16x16x32_bf16 v[124:127], v[60:63], a[76:79], v[124:127]\n"
"s_waitcnt lgkmcnt(8)\n"
"v_add_f32 v132, v132, v136\n"
"v_add_f32 v133, v133, v137\n"
"v_mfma_f32_16x16x32_bf16 v[124:127], v[64:67], a[80:83], v[124:127]\n"
"global_load_dwordx4 v[92:95], v11, s[56:57] offset:0 sc1\n"
"v_add_f32 v134, v134, v138\n"
"v_mfma_f32_16x16x32_bf16 v[68:71], v[96:99], a[84:87], 0\n"
"v_add_f32 v135, v135, v139\n"
"v_add_f32 v132, v132, v140\n"
"v_mfma_f32_16x16x32_bf16 v[68:71], v[100:103], a[88:91], v[68:71]\n"
"ds_write_addtid_b32 v124 offset:2560\n"
"v_add_f32 v133, v133, v141\n"
"v_mfma_f32_16x16x32_bf16 v[68:71], v[48:51], a[92:95], v[68:71]\n"
"ds_write_addtid_b32 v125 offset:7200\n"
"v_add_f32 v134, v134, v142\n"
"v_mfma_f32_16x16x32_bf16 v[68:71], v[52:55], a[96:99], v[68:71]\n"
"ds_write_addtid_b32 v126 offset:11904\n"
"v_add_f32 v135, v135, v143\n"
"v_mfma_f32_16x16x32_bf16 v[68:71], v[56:59], a[100:103], v[68:71]\n"
"ds_write_addtid_b32 v127 offset:16544\n"
"v_add_f32 v132, v132, v120\n"
"v_mfma_f32_16x16x32_bf16 v[68:71], v[60:63], a[104:107], v[68:71]\n"
"v_add_f32 v133, v133, v121\n"
"v_add_f32 v134, v134, v122\n"
"v_mfma_f32_16x16x32_bf16 v[68:71], v[64:67], a[108:111], v[68:71]\n"
"v_add_f32 v135, v135, v123\n"
"v_cvt_pk_bf16_f32 v110, v132, v133\n"
"v_mfma_f32_16x16x32_bf16 v[124:127], v[96:99], a[112:115], 0\n"
"v_cvt_pk_bf16_f32 v111, v134, v135\n"
"v_add_f32 v112, v112, v116\n"
"v_mfma_f32_16x16x32_bf16 v[124:127], v[100:103], a[116:119], v[124:127]\n"
"ds_write_addtid_b32 v68 offset:2816\n"
"v_add_f32 v113, v113, v117\n"
"v_mfma_f32_16x16x32_bf16 v[124:127], v[48:51], a[120:123], v[124:127]\n"
"ds_write_addtid_b32 v69 offset:7456\n"
"v_add_f32 v114, v114, v118\n"
"v_mfma_f32_16x16x32_bf16 v[124:127], v[52:55], a[124:127], v[124:127]\n"
"ds_write_addtid_b32 v70 offset:12160\n"
"v_add_f32 v115, v115, v119\n"
"v_mfma_f32_16x16x32_bf16 v[124:127], v[56:59], a[128:131], v[124:127]\n"
"ds_write_addtid_b32 v71 offset:16800\n"
"v_mfma_f32_16x16x32_bf16 v[124:127], v[60:63], a[132:135], v[124:127]\n"
"v_permlane32_swap_b32 v112, v114\n"
"v_mfma_f32_16x16x32_bf16 v[124:127], v[64:67], a[136:139], v[124:127]\n"
"v_permlane32_swap_b32 v113, v115\n"
"v_mfma_f32_16x16x32_bf16 v[68:71], v[96:99], a[140:143], 0\n"
"v_add_f32 v112, v112, v114\n"
"v_add_f32 v113, v113, v115\n"
"v_mfma_f32_16x16x32_bf16 v[68:71], v[100:103], a[144:147], v[68:71]\n"
"buffer_store_dwordx4 v[108:111], v13, s[40:43], s95 offen sc1\n"
"ds_write_addtid_b32 v124 offset:3072\n"
"v_permlane16_swap_b32 v112, v113\n"
"v_mfma_f32_16x16x32_bf16 v[68:71], v[48:51], a[148:151], v[68:71]\n"
"ds_write_addtid_b32 v125 offset:7712\n"
"v_add_f32 v112, v112, v113\n"
"v_mfma_f32_16x16x32_bf16 v[68:71], v[52:55], a[152:155], v[68:71]\n"
"ds_write_addtid_b32 v126 offset:12416\n"
"v_add_f32_dpp v113, v112, v112 row_ror:8 row_mask:0xf bank_mask:0xf\n"
"v_mfma_f32_16x16x32_bf16 v[68:71], v[56:59], a[156:159], v[68:71]\n"
"ds_write_addtid_b32 v127 offset:17056\n"
"v_mov_b32 v112, v113\n"
"v_mfma_f32_16x16x32_bf16 v[68:71], v[60:63], a[160:163], v[68:71]\n"
"v_permlane16_swap_b32 v113, v112\n"
"v_mfma_f32_16x16x32_bf16 v[68:71], v[64:67], a[164:167], v[68:71]\n"
"v_mul_f32 v113, 0xba800000, v113\n"
"v_fma_f32 v112, v112, s62, v20\n"
"v_mfma_f32_16x16x32_bf16 v[108:111], v[96:99], a[224:227], 0\n"
"v_fma_f32 v112, -v113, v113, v112\n"
"v_max_f32 v112, v20, v112\n"
"v_mfma_f32_16x16x32_bf16 v[108:111], v[100:103], a[228:231], v[108:111]\n"
"ds_write_addtid_b32 v68 offset:3328\n"
"v_mfma_f32_16x16x32_bf16 v[108:111], v[48:51], a[232:235], v[108:111]\n"
"ds_write_addtid_b32 v69 offset:7968\n"
"v_mfma_f32_16x16x32_bf16 v[108:111], v[52:55], a[236:239], v[108:111]\n"
"ds_write_addtid_b32 v70 offset:12672\n"
"v_mfma_f32_16x16x32_bf16 v[108:111], v[56:59], a[240:243], v[108:111]\n"
"ds_write_addtid_b32 v71 offset:17312\n"
"v_mfma_f32_16x16x32_bf16 v[108:111], v[60:63], a[244:247], v[108:111]\n"
"v_rsq_f32 v112, v112\n"
"v_mfma_f32_16x16x32_bf16 v[108:111], v[64:67], a[248:251], v[108:111]\n"
"v_mov_b32 v114, v0\n"
"v_mov_b32 v115, v1\n"
"v_mfma_f32_16x16x32_bf16 v[68:71], v[96:99], a[252:255], 0\n"
"buffer_store_dwordx4 v[112:115], v19, s[40:43], s94 offen sc1\n"
"v_mfma_f32_16x16x32_bf16 v[68:71], v[100:103], v[24:27], v[68:71]\n"
"s_add_u32 s58, s50, 0x180000\n"
"ds_write_addtid_b32 v108 offset:3584\n"
"v_mfma_f32_16x16x32_bf16 v[68:71], v[48:51], v[28:31], v[68:71]\n"
"s_and_b32 s58, s58, 0x3ffffff\n"
"ds_write_addtid_b32 v109 offset:8224\n"
"v_mfma_f32_16x16x32_bf16 v[68:71], v[52:55], v[32:35], v[68:71]\n"
"s_add_u32 s56, s40, s58\n"
"ds_write_addtid_b32 v110 offset:12928\n"
"v_mfma_f32_16x16x32_bf16 v[68:71], v[56:59], v[36:39], v[68:71]\n"
"s_addc_u32 s57, s41, 0\n"
"ds_write_addtid_b32 v111 offset:17568\n"
"v_mfma_f32_16x16x32_bf16 v[68:71], v[60:63], v[40:43], v[68:71]\n"
"s_waitcnt vmcnt(6)\n"
"v_cmp_ne_u64 vcc, s[48:49], v[144:145]\n"
"v_mfma_f32_16x16x32_bf16 v[68:71], v[64:67], v[44:47], v[68:71]\n"
"v_mfma_f32_16x16x32_bf16 v[116:119], a[168:171], v[96:99], 0\n"
"s_cbranch_vccnz .LH_2s\n"
".LH_2r:\n"
"v_mfma_f32_16x16x32_bf16 v[116:119], a[172:175], v[100:103], v[116:119]\n"
"s_add_u32 s50, s50, 0x180000\n"
"ds_write_addtid_b32 v68 offset:3840\n"
"v_mfma_f32_16x16x32_bf16 v[116:119], a[176:179], v[48:51], v[116:119]\n"
"ds_write_addtid_b32 v69 offset:8480\n"
"v_mfma_f32_16x16x32_bf16 v[116:119], a[180:183], v[52:55], v[116:119]\n"
"ds_write_addtid_b32 v70 offset:13184\n"
"v_mfma_f32_16x16x32_bf16 v[116:119], a[184:187], v[56:59], v[116:119]\n"
"ds_write_addtid_b32 v71 offset:17824\n"
"v_mfma_f32_16x16x32_bf16 v[116:119], a[188:191], v[60:63], v[116:119]\n"
"global_load_dwordx4 v[108:111], v5, s[56:57] offset:0 sc1\n"
"v_mfma_f32_16x16x32_bf16 v[116:119], a[192:195], v[64:67], v[116:119]\n"
"v_mfma_f32_16x16x32_bf16 v[120:123], a[196:199], v[96:99], 0\n"
"v_mfma_f32_16x16x32_bf16 v[120:123], a[200:203], v[100:103], v[120:123]\n"
"s_waitcnt lgkmcnt(0)\n"
"global_load_dwordx4 v[112:115], v6, s[56:57] offset:0 sc1\n"
"v_mfma_f32_16x16x32_bf16 v[120:123], a[204:207], v[48:51], v[120:123]\n"
"s_barrier\n"
"v_mfma_f32_16x16x32_bf16 v[120:123], a[208:211], v[52:55], v[120:123]\n"
"ds_read_b128 v[100:103], v2 offset:0\n"
"ds_read_b128 v[68:71], v3 offset:2560\n"
"v_mfma_f32_16x16x32_bf16 v[120:123], a[212:215], v[56:59], v[120:123]\n"
"ds_read_b128 v[96:99], v4 offset:1024\n"
"ds_read_b128 v[124:127], v2 offset:256\n"
"v_mfma_f32_16x16x32_bf16 v[120:123], a[216:219], v[60:63], v[120:123]\n"
"ds_read_b128 v[128:131], v3 offset:2816\n"
"ds_read_b128 v[132:135], v4 offset:1280\n"
"v_mfma_f32_16x16x32_bf16 v[120:123], a[220:223], v[64:67], v[120:123]\n"
"s_branch .LH_top\n"
".LH_1s:\n"
"s_cmp_ge_u32 s50, s53\n"
"s_cbranch_scc1 .LH_t0\n"
"s_cmp_ge_u32 s50, s52\n"
"s_cbranch_scc1 .LH_1r\n"
"s_sub_u32 s60, s50, 0x0\n"
"s_mov_b32 s61, 200000\n"
"s_nop 7\n"
".LH_1p:\n"
"buffer_load_dwordx2 v[138:139], v12, s[40:43], s60 offen sc1\n"
"s_waitcnt vmcnt(0)\n"
"v_cmp_ne_u64 vcc, s[48:49], v[138:139]\n"
"s_cbranch_vccz .LH_1px\n"
"s_sleep 1\n"
"s_sub_u32 s61, s61, 1\n"
"s_cbranch_scc0 .LH_1p\n"
"s_branch .LH_end\n"
".LH_1px:\n"
"s_branch .LH_1r\n"
".LH_2s:\n"
"s_cmp_ge_u32 s50, s53\n"
"s_cbranch_scc1 .LH_t1\n"
"s_cmp_ge_u32 s50, s52\n"
"s_cbranch_scc1 .LH_2r\n"
"s_sub_u32 s60, s50, 0x0\n"
"s_mov_b32 s61, 200000\n"
"s_nop 7\n"
".LH_2p:\n"
"buffer_load_dwordx2 v[144:145], v12, s[40:43], s60 offen sc1\n"
"s_waitcnt vmcnt(0)\n"
"v_cmp_ne_u64 vcc, s[48:49], v[144:145]\n"
"s_cbranch_vccz .LH_2px\n"
"s_sleep 1\n"
"s_sub_u32 s61, s61, 1\n"
"s_cbranch_scc0 .LH_2p\n"
"s_branch .LH_end\n"
".LH_2px:\n"
"s_branch .LH_2r\n"
".LH_3s:\n"
"s_cmp_ge_u32 s50, s53\n"
"s_cbranch_scc1 .LH_t2\n"
"s_cmp_ge_u32 s50, s52\n"
"s_cbranch_scc1 .LH_3r\n"
"s_sub_u32 s60, s50, 0x0\n"
"s_mov_b32 s61, 200000\n"
"s_nop 7\n"
".LH_3p:\n"
"buffer_load_dwordx2 v[136:137], v12, s[40:43], s60 offen sc1\n"
"s_waitcnt vmcnt(0)\n"
"v_cmp_ne_u64 vcc, s[48:49], v[136:137]\n"
"s_cbranch_vccz .LH_3px\n"
"s_sleep 1\n"
"s_sub_u32 s61, s61, 1\n"
"s_cbranch_scc0 .LH_3p\n"
"s_branch .LH_end\n"
".LH_3px:\n"
"s_branch .LH_3r\n"
".LH_4s:\n"
"s_cmp_ge_u32 s50, s53\n"
"s_cbranch_scc1 .LH_t3\n"
"s_cmp_ge_u32 s50, s52\n"
"s_cbranch_scc1 .LH_4r\n"
"s_sub_u32 s60, s50, 0x0\n"
"s_mov_b32 s61, 200000\n"
"s_nop 7\n"
".LH_4p:\n"
"buffer_load_dwordx2 v[172:173], v12, s[40:43], s60 offen sc1\n"
"s_waitcnt vmcnt(0)\n"
"v_cmp_ne_u64 vcc, s[48:49], v[172:173]\n"
"s_cbranch_vccz .LH_4px\n"
"s_sleep 1\n"
"s_sub_u32 s61, s61, 1\n"
"s_cbranch_scc0 .LH_4p\n"
"s_branch .LH_end\n"
".LH_4px:\n"
"s_branch .LH_4r\n"
".LH_5s:\n"
"s_cmp_ge_i32 s94, 0xc000000\n"
"s_cselect_b64 s[64:65], 0, s[64:65]\n"
"s_cmp_ge_u32 s94, 0xc000000\n"
"s_cbranch_scc1 .LH_5r\n"
"s_mov_b32 s61, 200000\n"
"s_nop 7\n"
".LH_5p:\n"
"buffer_load_dwordx2 v[136:137], v17, s[40:43], s94 offen offset:0 sc1\n"
"s_waitcnt vmcnt(0)\n"
"v_cmp_ne_u64 vcc, s[64:65], v[136:137]\n"
"s_cbranch_vccz .LH_5px\n"
"s_sleep 1\n"
"s_sub_u32 s61, s61, 1\n"
"s_cbranch_scc0 .LH_5p\n"
"s_branch .LH_end\n"
".LH_5px:\n"
"s_branch .LH_5r\n"
".LH_t0:\n"
"v_mfma_f32_16x16x32_bf16 v[108:111], a[172:175], v[72:75], v[108:111]\n"
"ds_write_addtid_b32 v120 offset:1792\n"
"v_mfma_f32_16x16x32_bf16 v[108:111], a[176:179], v[76:79], v[108:111]\n"
"ds_write_addtid_b32 v121 offset:6432\n"
"v_mfma_f32_16x16x32_bf16 v[108:111], a[180:183], v[80:83], v[108:111]\n"
"ds_write_addtid_b32 v122 offset:11136\n"
"v_mfma_f32_16x16x32_bf16 v[108:111], a[184:187], v[84:87], v[108:111]\n"
"ds_write_addtid_b32 v123 offset:15776\n"
"v_mfma_f32_16x16x32_bf16 v[108:111], a[188:191], v[88:91], v[108:111]\n"
"v_mfma_f32_16x16x32_bf16 v[108:111], a[192:195], v[92:95], v[108:111]\n"
"v_mfma_f32_16x16x32_bf16 v[120:123], a[196:199], v[68:71], 0\n"
"v_mfma_f32_16x16x32_bf16 v[120:123], a[200:203], v[72:75], v[120:123]\n"
"s_waitcnt lgkmcnt(0)\n"
"v_mfma_f32_16x16x32_bf16 v[120:123], a[204:207], v[76:79], v[120:123]\n"
"s_barrier\n"
"v_mfma_f32_16x16x32_bf16 v[120:123], a[208:211], v[80:83], v[120:123]\n"
"ds_read_b128 v[68:71], v2 offset:2048\n"
"v_mfma_f32_16x16x32_bf16 v[120:123], a[212:215], v[84:87], v[120:123]\n"
"ds_read_b128 v[124:127], v3 offset:512\n"
"ds_read_b128 v[128:131], v4 offset:3072\n"
"v_mfma_f32_16x16x32_bf16 v[120:123], a[216:219], v[88:91], v[120:123]\n"
"ds_read_b128 v[132:135], v2 offset:2304\n"
"ds_read_b128 v[136:139], v3 offset:768\n"
"v_mfma_f32_16x16x32_bf16 v[120:123], a[220:223], v[92:95], v[120:123]\n"
"s_waitcnt vmcnt(10)\n"
"ds_read_b128 v[140:143], v4 offset:3328\n"
"v_mfma_f32_16x16x32_bf16 v[88:91], v[96:99], a[0:3], 0\n"
"v_mfma_f32_16x16x32_bf16 v[88:91], v[100:103], a[4:7], v[88:91]\n"
"s_waitcnt vmcnt(7)\n"
"v_mfma_f32_16x16x32_bf16 v[88:91], v[48:51], a[8:11], v[88:91]\n"
"v_mfma_f32_16x16x32_bf16 v[88:91], v[52:55], a[12:15], v[88:91]\n"
"s_waitcnt vmcnt(5)\n"
"v_mfma_f32_16x16x32_bf16 v[88:91], v[56:59], a[16:19], v[88:91]\n"
"s_waitcnt lgkmcnt(3)\n"
"v_add_f32 v68, v68, v124\n"
"v_add_f32 v69, v69, v125\n"
"v_mfma_f32_16x16x32_bf16 v[88:91], v[60:63], a[20:23], v[88:91]\n"
"s_waitcnt vmcnt(4)\n"
"v_add_f32 v70, v70, v126\n"
"v_mfma_f32_16x16x32_bf16 v[88:91], v[64:67], a[24:27], v[88:91]\n"
"v_mfma_f32_16x16x32_bf16 v[92:95], v[96:99], a[28:31], 0\n"
"v_add_f32 v71, v71, v127\n"
"v_add_f32 v68, v68, v128\n"
"v_mfma_f32_16x16x32_bf16 v[92:95], v[100:103], a[32:35], v[92:95]\n"
"s_waitcnt vmcnt(2)\n"
"buffer_store_dwordx2 v[0:1], v14, s[40:43], s95 offen offset:0 sc1\n"
"ds_write_addtid_b32 v88 offset:2048\n"
"v_mfma_f32_16x16x32_bf16 v[92:95], v[48:51], a[36:39], v[92:95]\n"
"ds_write_addtid_b32 v89 offset:6688\n"
"v_add_f32 v69, v69, v129\n"
"v_mfma_f32_16x16x32_bf16 v[92:95], v[52:55], a[40:43], v[92:95]\n"
"ds_write_addtid_b32 v90 offset:11392\n"
"v_mfma_f32_16x16x32_bf16 v[92:95], v[56:59], a[44:47], v[92:95]\n"
"ds_write_addtid_b32 v91 offset:16032\n"
"v_add_f32 v70, v70, v130\n"
"v_mfma_f32_16x16x32_bf16 v[92:95], v[60:63], a[48:51], v[92:95]\n"
"v_add_f32 v71, v71, v131\n"
"v_add_f32 v68, v68, v108\n"
"v_mfma_f32_16x16x32_bf16 v[92:95], v[64:67], a[52:55], v[92:95]\n"
"v_add_f32 v69, v69, v109\n"
"v_add_f32 v70, v70, v110\n"
"v_mfma_f32_16x16x32_bf16 v[124:127], v[96:99], a[56:59], 0\n"
"v_add_f32 v71, v71, v111\n"
"v_mfma_f32_16x16x32_bf16 v[124:127], v[100:103], a[60:63], v[124:127]\n"
"ds_write_addtid_b32 v92 offset:2304\n"
"v_mfma_f32_16x16x32_bf16 v[124:127], v[48:51], a[64:67], v[124:127]\n"
"s_add_u32 s93, s50, 0xffe80000\n"
"s_and_b32 s95, s93, 0xfbffffff\n"
"ds_write_addtid_b32 v93 offset:6944\n"
"v_mfma_f32_16x16x32_bf16 v[124:127], v[52:55], a[68:71], v[124:127]\n"
"ds_write_addtid_b32 v94 offset:11648\n"
"v_cvt_pk_bf16_f32 v108, v68, v69\n"
"v_mfma_f32_16x16x32_bf16 v[124:127], v[56:59], a[72:75], v[124:127]\n"
"s_waitcnt vmcnt(1)\n"
"ds_write_addtid_b32 v95 offset:16288\n"
"v_cvt_pk_bf16_f32 v109, v70, v71\n"
"v_mfma_f32_16x16x32_bf16 v[124:127], v[60:63], a[76:79], v[124:127]\n"
"s_waitcnt lgkmcnt(8)\n"
"v_add_f32 v132, v132, v136\n"
"v_add_f32 v133, v133, v137\n"
"v_mfma_f32_16x16x32_bf16 v[124:127], v[64:67], a[80:83], v[124:127]\n"
"v_add_f32 v134, v134, v138\n"
"v_mfma_f32_16x16x32_bf16 v[68:71], v[96:99], a[84:87], 0\n"
"v_add_f32 v135, v135, v139\n"
"v_add_f32 v132, v132, v140\n"
"v_mfma_f32_16x16x32_bf16 v[68:71], v[100:103], a[88:91], v[68:71]\n"
"ds_write_addtid_b32 v124 offset:2560\n"
"v_add_f32 v133, v133, v141\n"
"v_mfma_f32_16x16x32_bf16 v[68:71], v[48:51], a[92:95], v[68:71]\n"
"ds_write_addtid_b32 v125 offset:7200\n"
"v_add_f32 v134, v134, v142\n"
"v_mfma_f32_16x16x32_bf16 v[68:71], v[52:55], a[96:99], v[68:71]\n"
"ds_write_addtid_b32 v126 offset:11904\n"
"v_add_f32 v135, v135, v143\n"
"v_mfma_f32_16x16x32_bf16 v[68:71], v[56:59], a[100:103], v[68:71]\n"
"ds_write_addtid_b32 v127 offset:16544\n"
"v_add_f32 v132, v132, v120\n"
"v_mfma_f32_16x16x32_bf16 v[68:71], v[60:63], a[104:107], v[68:71]\n"
"v_add_f32 v133, v133, v121\n"
"v_add_f32 v134, v134, v122\n"
"v_mfma_f32_16x16x32_bf16 v[68:71], v[64:67], a[108:111], v[68:71]\n"
"v_add_f32 v135, v135, v123\n"
"v_cvt_pk_bf16_f32 v110, v132, v133\n"
"v_mfma_f32_16x16x32_bf16 v[124:127], v[96:99], a[112:115], 0\n"
"v_cvt_pk_bf16_f32 v111, v134, v135\n"
"v_mfma_f32_16x16x32_bf16 v[124:127], v[100:103], a[116:119], v[124:127]\n"
"ds_write_addtid_b32 v68 offset:2816\n"
"v_mfma_f32_16x16x32_bf16 v[124:127], v[48:51], a[120:123], v[124:127]\n"
"ds_write_addtid_b32 v69 offset:7456\n"
"v_mfma_f32_16x16x32_bf16 v[124:127], v[52:55], a[124:127], v[124:127]\n"
"ds_write_addtid_b32 v70 offset:12160\n"
"v_mfma_f32_16x16x32_bf16 v[124:127], v[56:59], a[128:131], v[124:127]\n"
"ds_write_addtid_b32 v71 offset:16800\n"
"v_mfma_f32_16x16x32_bf16 v[124:127], v[60:63], a[132:135], v[124:127]\n"
"v_mfma_f32_16x16x32_bf16 v[124:127], v[64:67], a[136:139], v[124:127]\n"
"v_mfma_f32_16x16x32_bf16 v[68:71], v[96:99], a[140:143], 0\n"
"v_mfma_f32_16x16x32_bf16 v[68:71], v[100:103], a[144:147], v[68:71]\n"
"buffer_store_dwordx4 v[108:111], v13, s[40:43], s95 offen sc1\n"
"ds_write_addtid_b32 v124 offset:3072\n"
"v_mfma_f32_16x16x32_bf16 v[68:71], v[48:51], a[148:151], v[68:71]\n"
"ds_write_addtid_b32 v125 offset:7712\n"
"v_mfma_f32_16x16x32_bf16 v[68:71], v[52:55], a[152:155], v[68:71]\n"
"ds_write_addtid_b32 v126 offset:12416\n"
"v_mfma_f32_16x16x32_bf16 v[68:71], v[56:59], a[156:159], v[68:71]\n"
"ds_write_addtid_b32 v127 offset:17056\n"
"v_mfma_f32_16x16x32_bf16 v[68:71], v[60:63], a[160:163], v[68:71]\n"
"v_mfma_f32_16x16x32_bf16 v[68:71], v[64:67], a[164:167], v[68:71]\n"
"v_mfma_f32_16x16x32_bf16 v[108:111], v[96:99], a[224:227], 0\n"
"v_mfma_f32_16x16x32_bf16 v[108:111], v[100:103], a[228:231], v[108:111]\n"
"ds_write_addtid_b32 v68 offset:3328\n"
"v_mfma_f32_16x16x32_bf16 v[108:111], v[48:51], a[232:235], v[108:111]\n"
"ds_write_addtid_b32 v69 offset:7968\n"
"v_mfma_f32_16x16x32_bf16 v[108:111], v[52:55], a[236:239], v[108:111]\n"
"ds_write_addtid_b32 v70 offset:12672\n"
"v_mfma_f32_16x16x32_bf16 v[108:111], v[56:59], a[240:243], v[108:111]\n"
"ds_write_addtid_b32 v71 offset:17312\n"
"v_mfma_f32_16x16x32_bf16 v[108:111], v[60:63], a[244:247], v[108:111]\n"
"v_mfma_f32_16x16x32_bf16 v[108:111], v[64:67], a[248:251], v[108:111]\n"
"v_mfma_f32_16x16x32_bf16 v[68:71], v[96:99], a[252:255], 0\n"
"v_mfma_f32_16x16x32_bf16 v[68:71], v[100:103], v[24:27], v[68:71]\n"
"ds_write_addtid_b32 v108 offset:3584\n"
"v_mfma_f32_16x16x32_bf16 v[68:71], v[48:51], v[28:31], v[68:71]\n"
"ds_write_addtid_b32 v109 offset:8224\n"
"v_mfma_f32_16x16x32_bf16 v[68:71], v[52:55], v[32:35], v[68:71]\n"
"ds_write_addtid_b32 v110 offset:12928\n"
"v_mfma_f32_16x16x32_bf16 v[68:71], v[56:59], v[36:39], v[68:71]\n"
"ds_write_addtid_b32 v111 offset:17568\n"
"v_mfma_f32_16x16x32_bf16 v[68:71], v[60:63], v[40:43], v[68:71]\n"
"v_mfma_f32_16x16x32_bf16 v[68:71], v[64:67], v[44:47], v[68:71]\n"
"v_mfma_f32_16x16x32_bf16 v[116:119], a[168:171], v[96:99], 0\n"
"v_mfma_f32_16x16x32_bf16 v[116:119], a[172:175], v[100:103], v[116:119]\n"
"ds_write_addtid_b32 v68 offset:3840\n"
"v_mfma_f32_16x16x32_bf16 v[116:119], a[176:179], v[48:51], v[116:119]\n"
"ds_write_addtid_b32 v69 offset:8480\n"
"v_mfma_f32_16x16x32_bf16 v[116:119], a[180:183], v[52:55], v[116:119]\n"
"ds_write_addtid_b32 v70 offset:13184\n"
"v_mfma_f32_16x16x32_bf16 v[116:119], a[184:187], v[56:59], v[116:119]\n"
"ds_write_addtid_b32 v71 offset:17824\n"
"v_mfma_f32_16x16x32_bf16 v[116:119], a[188:191], v[60:63], v[116:119]\n"
"v_mfma_f32_16x16x32_bf16 v[116:119], a[192:195], v[64:67], v[116:119]\n"
"v_mfma_f32_16x16x32_bf16 v[120:123], a[196:199], v[96:99], 0\n"
"v_mfma_f32_16x16x32_bf16 v[120:123], a[200:203], v[100:103], v[120:123]\n"
"s_waitcnt lgkmcnt(0)\n"
"v_mfma_f32_16x16x32_bf16 v[120:123], a[204:207], v[48:51], v[120:123]\n"
"s_barrier\n"
"v_mfma_f32_16x16x32_bf16 v[120:123], a[208:211], v[52:55], v[120:123]\n"
"ds_read_b128 v[100:103], v2 offset:0\n"
"ds_read_b128 v[68:71], v3 offset:2560\n"
"v_mfma_f32_16x16x32_bf16 v[120:123], a[212:215], v[56:59], v[120:123]\n"
"ds_read_b128 v[96:99], v4 offset:1024\n"
"ds_read_b128 v[124:127], v2 offset:256\n"
"v_mfma_f32_16x16x32_bf16 v[120:123], a[216:219], v[60:63], v[120:123]\n"
"ds_read_b128 v[128:131], v3 offset:2816\n"
"ds_read_b128 v[132:135], v4 offset:1280\n"
"v_mfma_f32_16x16x32_bf16 v[120:123], a[220:223], v[64:67], v[120:123]\n"
"s_waitcnt lgkmcnt(3)\n"
"v_add_f32 v100, v100, v68\n"
"v_add_f32 v101, v101, v69\n"
"v_add_f32 v102, v102, v70\n"
"v_add_f32 v103, v103, v71\n"
"v_add_f32 v100, v100, v96\n"
"v_add_f32 v101, v101, v97\n"
"v_add_f32 v102, v102, v98\n"
"v_add_f32 v103, v103, v99\n"
"v_add_f32 v100, v100, v116\n"
"v_add_f32 v101, v101, v117\n"
"v_add_f32 v102, v102, v118\n"
"v_add_f32 v103, v103, v119\n"
"s_waitcnt vmcnt(0)\n"
"buffer_store_dwordx2 v[0:1], v14, s[40:43], s93 offen offset:0 sc1\n"
"s_add_u32 s95, s50, 0x0\n"
"s_and_b32 s94, s95, 0xfbffffff\n"
"v_cvt_pk_bf16_f32 v96, v100, v101\n"
"v_cvt_pk_bf16_f32 v97, v102, v103\n"
"s_waitcnt lgkmcnt(0)\n"
"v_add_f32 v124, v124, v128\n"
"v_add_f32 v125, v125, v129\n"
"v_add_f32 v126, v126, v130\n"
"v_add_f32 v127, v127, v131\n"
"v_add_f32 v124, v124, v132\n"
"v_add_f32 v125, v125, v133\n"
"v_add_f32 v126, v126, v134\n"
"v_add_f32 v127, v127, v135\n"
"v_add_f32 v124, v124, v120\n"
"v_add_f32 v125, v125, v121\n"
"v_add_f32 v126, v126, v122\n"
"v_add_f32 v127, v127, v123\n"
"v_cvt_pk_bf16_f32 v98, v124, v125\n"
"v_cvt_pk_bf16_f32 v99, v126, v127\n"
"buffer_store_dwordx4 v[96:99], v13, s[40:43], s94 offen sc1\n"
"s_waitcnt vmcnt(0)\n"
"buffer_store_dwordx2 v[0:1], v14, s[40:43], s95 offen offset:0 sc1\n"
"s_branch .LH_end\n"
".LH_t1:\n"
"v_mfma_f32_16x16x32_bf16 v[116:119], a[172:175], v[100:103], v[116:119]\n"
"ds_write_addtid_b32 v68 offset:3840\n"
"v_mfma_f32_16x16x32_bf16 v[116:119], a[176:179], v[48:51], v[116:119]\n"
"ds_write_addtid_b32 v69 offset:8480\n"
"v_mfma_f32_16x16x32_bf16 v[116:119], a[180:183], v[52:55], v[116:119]\n"
"ds_write_addtid_b32 v70 offset:13184\n"
"v_mfma_f32_16x16x32_bf16 v[116:119], a[184:187], v[56:59], v[116:119]\n"
"ds_write_addtid_b32 v71 offset:17824\n"
"v_mfma_f32_16x16x32_bf16 v[116:119], a[188:191], v[60:63], v[116:119]\n"
"v_mfma_f32_16x16x32_bf16 v[116:119], a[192:195], v[64:67], v[116:119]\n"
"v_mfma_f32_16x16x32_bf16 v[120:123], a[196:199], v[96:99], 0\n"
"v_mfma_f32_16x16x32_bf16 v[120:123], a[200:203], v[100:103], v[120:123]\n"
"s_waitcnt lgkmcnt(0)\n"
"v_mfma_f32_16x16x32_bf16 v[120:123], a[204:207], v[48:51], v[120:123]\n"
"s_barrier\n"
"v_mfma_f32_16x16x32_bf16 v[120:123], a[208:211], v[52:55], v[120:123]\n"
"ds_read_b128 v[100:103], v2 offset:0\n"
"ds_read_b128 v[68:71], v3 offset:2560\n"
"v_mfma_f32_16x16x32_bf16 v[120:123], a[212:215], v[56:59], v[120:123]\n"
"ds_read_b128 v[96:99], v4 offset:1024\n"
"ds_read_b128 v[124:127], v2 offset:256\n"
"v_mfma_f32_16x16x32_bf16 v[120:123], a[216:219], v[60:63], v[120:123]\n"
"ds_read_b128 v[128:131], v3 offset:2816\n"
"ds_read_b128 v[132:135], v4 offset:1280\n"
"v_mfma_f32_16x16x32_bf16 v[120:123], a[220:223], v[64:67], v[120:123]\n"
"s_waitcnt vmcnt(9)\n"
"v_mfma_f32_16x16x32_bf16 v[60:63], v[104:107], a[0:3], 0\n"
"v_mfma_f32_16x16x32_bf16 v[60:63], v[72:75], a[4:7], v[60:63]\n"
"s_waitcnt vmcnt(7)\n"
"v_mfma_f32_16x16x32_bf16 v[60:63], v[76:79], a[8:11], v[60:63]\n"
"v_mfma_f32_16x16x32_bf16 v[60:63], v[80:83], a[12:15], v[60:63]\n"
"s_waitcnt vmcnt(3)\n"
"v_mfma_f32_16x16x32_bf16 v[60:63], v[84:87], a[16:19], v[60:63]\n"
"s_waitcnt lgkmcnt(3)\n"
"v_add_f32 v100, v100, v68\n"
"v_add_f32 v101, v101, v69\n"
"v_mfma_f32_16x16x32_bf16 v[60:63], v[88:91], a[20:23], v[60:63]\n"
"s_waitcnt vmcnt(2)\n"
"v_add_f32 v102, v102, v70\n"
"v_add_f32 v103, v103, v71\n"
"v_mfma_f32_16x16x32_bf16 v[60:63], v[92:95], a[24:27], v[60:63]\n"
"v_add_f32 v100, v100, v96\n"
"v_mfma_f32_16x16x32_bf16 v[64:67], v[104:107], a[28:31], 0\n"
"v_mfma_f32_16x16x32_bf16 v[64:67], v[72:75], a[32:35], v[64:67]\n"
"ds_write_addtid_b32 v60 offset:0\n"
"v_add_f32 v101, v101, v97\n"
"v_mfma_f32_16x16x32_bf16 v[64:67], v[76:79], a[36:39], v[64:67]\n"
"ds_write_addtid_b32 v61 offset:4640\n"
"v_mfma_f32_16x16x32_bf16 v[64:67], v[80:83], a[40:43], v[64:67]\n"
"ds_write_addtid_b32 v62 offset:9344\n"
"v_add_f32 v102, v102, v98\n"
"v_mfma_f32_16x16x32_bf16 v[64:67], v[84:87], a[44:47], v[64:67]\n"
"ds_write_addtid_b32 v63 offset:13984\n"
"v_add_f32 v103, v103, v99\n"
"v_mfma_f32_16x16x32_bf16 v[64:67], v[88:91], a[48:51], v[64:67]\n"
"v_add_f32 v100, v100, v116\n"
"v_add_f32 v101, v101, v117\n"
"v_mfma_f32_16x16x32_bf16 v[64:67], v[92:95], a[52:55], v[64:67]\n"
"v_add_f32 v102, v102, v118\n"
"v_add_f32 v103, v103, v119\n"
"v_mfma_f32_16x16x32_bf16 v[68:71], v[104:107], a[56:59], 0\n"
"s_waitcnt vmcnt(1)\n"
"buffer_store_dwordx2 v[0:1], v14, s[40:43], s93 offen offset:0 sc1\n"
"v_mfma_f32_16x16x32_bf16 v[68:71], v[72:75], a[60:63], v[68:71]\n"
"s_add_u32 s95, s50, 0xffe80000\n"
"s_and_b32 s94, s95, 0xfbffffff\n"
"ds_write_addtid_b32 v64 offset:256\n"
"v_mfma_f32_16x16x32_bf16 v[68:71], v[76:79], a[64:67], v[68:71]\n"
"ds_write_addtid_b32 v65 offset:4896\n"
"v_mfma_f32_16x16x32_bf16 v[68:71], v[80:83], a[68:71], v[68:71]\n"
"ds_write_addtid_b32 v66 offset:9600\n"
"v_cvt_pk_bf16_f32 v96, v100, v101\n"
"v_mfma_f32_16x16x32_bf16 v[68:71], v[84:87], a[72:75], v[68:71]\n"
"ds_write_addtid_b32 v67 offset:14240\n"
"v_cvt_pk_bf16_f32 v97, v102, v103\n"
"v_mfma_f32_16x16x32_bf16 v[68:71], v[88:91], a[76:79], v[68:71]\n"
"s_waitcnt lgkmcnt(8)\n"
"v_add_f32 v124, v124, v128\n"
"v_add_f32 v125, v125, v129\n"
"v_mfma_f32_16x16x32_bf16 v[68:71], v[92:95], a[80:83], v[68:71]\n"
"v_add_f32 v126, v126, v130\n"
"v_add_f32 v127, v127, v131\n"
"v_mfma_f32_16x16x32_bf16 v[100:103], v[104:107], a[84:87], 0\n"
"v_add_f32 v124, v124, v132\n"
"v_mfma_f32_16x16x32_bf16 v[100:103], v[72:75], a[88:91], v[100:103]\n"
"ds_write_addtid_b32 v68 offset:512\n"
"v_add_f32 v125, v125, v133\n"
"v_mfma_f32_16x16x32_bf16 v[100:103], v[76:79], a[92:95], v[100:103]\n"
"ds_write_addtid_b32 v69 offset:5152\n"
"v_add_f32 v126, v126, v134\n"
"v_mfma_f32_16x16x32_bf16 v[100:103], v[80:83], a[96:99], v[100:103]\n"
"ds_write_addtid_b32 v70 offset:9856\n"
"v_add_f32 v127, v127, v135\n"
"v_mfma_f32_16x16x32_bf16 v[100:103], v[84:87], a[100:103], v[100:103]\n"
"ds_write_addtid_b32 v71 offset:14496\n"
"v_add_f32 v124, v124, v120\n"
"v_mfma_f32_16x16x32_bf16 v[100:103], v[88:91], a[104:107], v[100:103]\n"
"v_add_f32 v125, v125, v121\n"
"v_add_f32 v126, v126, v122\n"
"v_mfma_f32_16x16x32_bf16 v[100:103], v[92:95], a[108:111], v[100:103]\n"
"v_add_f32 v127, v127, v123\n"
"v_cvt_pk_bf16_f32 v98, v124, v125\n"
"v_mfma_f32_16x16x32_bf16 v[68:71], v[104:107], a[112:115], 0\n"
"v_cvt_pk_bf16_f32 v99, v126, v127\n"
"v_mfma_f32_16x16x32_bf16 v[68:71], v[72:75], a[116:119], v[68:71]\n"
"ds_write_addtid_b32 v100 offset:768\n"
"v_mfma_f32_16x16x32_bf16 v[68:71], v[76:79], a[120:123], v[68:71]\n"
"ds_write_addtid_b32 v101 offset:5408\n"
"v_mfma_f32_16x16x32_bf16 v[68:71], v[80:83], a[124:127], v[68:71]\n"
"ds_write_addtid_b32 v102 offset:10112\n"
"v_mfma_f32_16x16x32_bf16 v[68:71], v[84:87], a[128:131], v[68:71]\n"
"ds_write_addtid_b32 v103 offset:14752\n"
"v_mfma_f32_16x16x32_bf16 v[68:71], v[88:91], a[132:135], v[68:71]\n"
"v_mfma_f32_16x16x32_bf16 v[68:71], v[92:95], a[136:139], v[68:71]\n"
"v_mfma_f32_16x16x32_bf16 v[116:119], v[104:107], a[140:143], 0\n"
"v_mfma_f32_16x16x32_bf16 v[116:119], v[72:75], a[144:147], v[116:119]\n"
"ds_write_addtid_b32 v68 offset:1024\n"
"v_mfma_f32_16x16x32_bf16 v[116:119], v[76:79], a[148:151], v[116:119]\n"
"ds_write_addtid_b32 v69 offset:5664\n"
"v_mfma_f32_16x16x32_bf16 v[116:119], v[80:83], a[152:155], v[116:119]\n"
"ds_write_addtid_b32 v70 offset:10368\n"
"v_mfma_f32_16x16x32_bf16 v[116:119], v[84:87], a[156:159], v[116:119]\n"
"ds_write_addtid_b32 v71 offset:15008\n"
"v_mfma_f32_16x16x32_bf16 v[116:119], v[88:91], a[160:163], v[116:119]\n"
"buffer_store_dwordx4 v[96:99], v13, s[40:43], s94 offen sc1\n"
"v_mfma_f32_16x16x32_bf16 v[116:119], v[92:95], a[164:167], v[116:119]\n"
"v_mfma_f32_16x16x32_bf16 v[100:103], a[168:171], v[104:107], 0\n"
"v_mfma_f32_16x16x32_bf16 v[100:103], a[172:175], v[72:75], v[100:103]\n"
"ds_write_addtid_b32 v116 offset:1280\n"
"v_mfma_f32_16x16x32_bf16 v[100:103], a[176:179], v[76:79], v[100:103]\n"
"ds_write_addtid_b32 v117 offset:5920\n"
"v_mfma_f32_16x16x32_bf16 v[100:103], a[180:183], v[80:83], v[100:103]\n"
"ds_write_addtid_b32 v118 offset:10624\n"
"v_mfma_f32_16x16x32_bf16 v[100:103], a[184:187], v[84:87], v[100:103]\n"
"ds_write_addtid_b32 v119 offset:15264\n"
"v_mfma_f32_16x16x32_bf16 v[100:103], a[188:191], v[88:91], v[100:103]\n"
"v_mfma_f32_16x16x32_bf16 v[100:103], a[192:195], v[92:95], v[100:103]\n"
"v_mfma_f32_16x16x32_bf16 v[96:99], a[196:199], v[104:107], 0\n"
"v_mfma_f32_16x16x32_bf16 v[96:99], a[200:203], v[72:75], v[96:99]\n"
"v_mfma_f32_16x16x32_bf16 v[96:99], a[204:207], v[76:79], v[96:99]\n"
"v_mfma_f32_16x16x32_bf16 v[96:99], a[208:211], v[80:83], v[96:99]\n"
"v_mfma_f32_16x16x32_bf16 v[96:99], a[212:215], v[84:87], v[96:99]\n"
"v_mfma_f32_16x16x32_bf16 v[96:99], a[216:219], v[88:91], v[96:99]\n"
"v_mfma_f32_16x16x32_bf16 v[96:99], a[220:223], v[92:95], v[96:99]\n"
"v_mfma_f32_16x16x32_bf16 v[116:119], a[224:227], v[104:107], 0\n"
"v_mfma_f32_16x16x32_bf16 v[116:119], a[228:231], v[72:75], v[116:119]\n"
"v_mfma_f32_16x16x32_bf16 v[116:119], a[232:235], v[76:79], v[116:119]\n"
"v_mfma_f32_16x16x32_bf16 v[116:119], a[236:239], v[80:83], v[116:119]\n"
"v_mfma_f32_16x16x32_bf16 v[116:119], a[240:243], v[84:87], v[116:119]\n"
"v_mfma_f32_16x16x32_bf16 v[116:119], a[244:247], v[88:91], v[116:119]\n"
"v_mfma_f32_16x16x32_bf16 v[116:119], a[248:251], v[92:95], v[116:119]\n"
"v_mfma_f32_16x16x32_bf16 v[120:123], a[252:255], v[104:107], 0\n"
"v_mfma_f32_16x16x32_bf16 v[120:123], v[24:27], v[72:75], v[120:123]\n"
"s_waitcnt lgkmcnt(0)\n"
"v_mfma_f32_16x16x32_bf16 v[120:123], v[28:31], v[76:79], v[120:123]\n"
"s_barrier\n"
"v_mfma_f32_16x16x32_bf16 v[120:123], v[32:35], v[80:83], v[120:123]\n"
"ds_read_b128 v[104:107], v2 offset:2048\n"
"ds_read_b128 v[124:127], v3 offset:512\n"
"v_mfma_f32_16x16x32_bf16 v[120:123], v[36:39], v[84:87], v[120:123]\n"
"ds_read_b128 v[128:131], v4 offset:3072\n"
"v_mfma_f32_16x16x32_bf16 v[120:123], v[40:43], v[88:91], v[120:123]\n"
"ds_read_b128 v[132:135], v2 offset:2304\n"
"ds_read_b128 v[136:139], v3 offset:768\n"
"v_mfma_f32_16x16x32_bf16 v[120:123], v[44:47], v[92:95], v[120:123]\n"
"ds_read_b128 v[140:143], v4 offset:3328\n"
"ds_read_b128 v[144:147], v2 offset:3584\n"
"ds_read_b128 v[148:151], v3 offset:1536\n"
"ds_read_b128 v[152:155], v4 offset:3584\n"
"ds_read_b128 v[156:159], v2 offset:3840\n"
"ds_read_b128 v[160:163], v3 offset:1792\n"
"ds_read_b128 v[164:167], v4 offset:3840\n"
"s_waitcnt lgkmcnt(9)\n"
"v_add_f32 v104, v104, v124\n"
"v_add_f32 v105, v105, v125\n"
"v_add_f32 v106, v106, v126\n"
"v_add_f32 v107, v107, v127\n"
"v_add_f32 v104, v104, v128\n"
"v_add_f32 v105, v105, v129\n"
"v_add_f32 v106, v106, v130\n"
"v_add_f32 v107, v107, v131\n"
"v_add_f32 v104, v104, v100\n"
"v_add_f32 v105, v105, v101\n"
"v_add_f32 v106, v106, v102\n"
"v_add_f32 v107, v107, v103\n"
"s_add_u32 s92, s50, 0x0\n"
"s_and_b32 s93, s92, 0xfbffffff\n"
"v_cvt_pk_bf16_f32 v128, v104, v105\n"
"v_cvt_pk_bf16_f32 v129, v106, v107\n"
"s_waitcnt lgkmcnt(6)\n"
"v_add_f32 v132, v132, v136\n"
"v_add_f32 v133, v133, v137\n"
"s_waitcnt vmcnt(0)\n"
"v_add_f32 v134, v134, v138\n"
"v_add_f32 v135, v135, v139\n"
"v_add_f32 v132, v132, v140\n"
"v_add_f32 v133, v133, v141\n"
"v_add_f32 v134, v134, v142\n"
"v_add_f32 v135, v135, v143\n"
"v_add_f32 v132, v132, v96\n"
"v_add_f32 v133, v133, v97\n"
"v_add_f32 v134, v134, v98\n"
"v_add_f32 v135, v135, v99\n"
"v_cvt_pk_bf16_f32 v130, v132, v133\n"
"buffer_store_dwordx2 v[0:1], v14, s[40:43], s95 offen offset:0 sc1\n"
"v_cvt_pk_bf16_f32 v131, v134, v135\n"
"buffer_store_dwordx4 v[128:131], v13, s[40:43], s93 offen sc1\n"
"s_waitcnt lgkmcnt(3)\n"
"v_add_f32 v144, v144, v148\n"
"v_add_f32 v145, v145, v149\n"
"v_add_f32 v146, v146, v150\n"
"v_add_f32 v147, v147, v151\n"
"v_add_f32 v144, v144, v152\n"
"v_add_f32 v145, v145, v153\n"
"v_add_f32 v146, v146, v154\n"
"v_add_f32 v147, v147, v155\n"
"v_add_f32 v144, v144, v116\n"
"v_add_f32 v145, v145, v117\n"
"v_add_f32 v146, v146, v118\n"
"v_add_f32 v147, v147, v119\n"
"v_cvt_pk_bf16_f32 v104, v144, v145\n"
"v_cvt_pk_bf16_f32 v105, v146, v147\n"
"s_waitcnt lgkmcnt(0)\n"
"v_add_f32 v156, v156, v160\n"
"v_add_f32 v157, v157, v161\n"
"v_add_f32 v158, v158, v162\n"
"v_add_f32 v159, v159, v163\n"
"v_add_f32 v156, v156, v164\n"
"v_add_f32 v157, v157, v165\n"
"v_add_f32 v158, v158, v166\n"
"v_add_f32 v159, v159, v167\n"
"v_add_f32 v156, v156, v120\n"
"v_add_f32 v157, v157, v121\n"
"v_add_f32 v158, v158, v122\n"
"v_add_f32 v159, v159, v123\n"
"v_cvt_pk_bf16_f32 v106, v156, v157\n"
"v_cvt_pk_bf16_f32 v107, v158, v159\n"
"buffer_store_dwordx4 v[104:107], v15, s[40:43], s93 offen sc1\n"
"s_waitcnt vmcnt(0)\n"
"buffer_store_dwordx2 v[0:1], v16, s[40:43], s92 offen offset:0 sc1\n"
"s_branch .LH_end\n"
".LH_t2:\n"
"v_mfma_f32_16x16x32_bf16 v[116:119], a[236:239], v[80:83], v[116:119]\n"
"v_mfma_f32_16x16x32_bf16 v[116:119], a[240:243], v[84:87], v[116:119]\n"
"v_mfma_f32_16x16x32_bf16 v[116:119], a[244:247], v[88:91], v[116:119]\n"
"v_mfma_f32_16x16x32_bf16 v[116:119], a[248:251], v[92:95], v[116:119]\n"
"v_mfma_f32_16x16x32_bf16 v[120:123], a[252:255], v[104:107], 0\n"
"v_mfma_f32_16x16x32_bf16 v[120:123], v[24:27], v[72:75], v[120:123]\n"
"s_waitcnt lgkmcnt(0)\n"
"v_mfma_f32_16x16x32_bf16 v[120:123], v[28:31], v[76:79], v[120:123]\n"
"s_barrier\n"
"v_mfma_f32_16x16x32_bf16 v[120:123], v[32:35], v[80:83], v[120:123]\n"
"ds_read_b128 v[104:107], v2 offset:2048\n"
"ds_read_b128 v[124:127], v3 offset:512\n"
"v_mfma_f32_16x16x32_bf16 v[120:123], v[36:39], v[84:87], v[120:123]\n"
"ds_read_b128 v[128:131], v4 offset:3072\n"
"v_mfma_f32_16x16x32_bf16 v[120:123], v[40:43], v[88:91], v[120:123]\n"
"ds_read_b128 v[132:135], v2 offset:2304\n"
"ds_read_b128 v[136:139], v3 offset:768\n"
"v_mfma_f32_16x16x32_bf16 v[120:123], v[44:47], v[92:95], v[120:123]\n"
"s_waitcnt vmcnt(8)\n"
"ds_read_b128 v[140:143], v4 offset:3328\n"
"v_mfma_f32_16x16x32_bf16 v[88:91], v[108:111], a[0:3], 0\n"
"ds_read_b128 v[144:147], v2 offset:3584\n"
"ds_read_b128 v[148:151], v3 offset:1536\n"
"v_mfma_f32_16x16x32_bf16 v[88:91], v[112:115], a[4:7], v[88:91]\n"
"s_waitcnt vmcnt(6)\n"
"ds_read_b128 v[152:155], v4 offset:3584\n"
"ds_read_b128 v[156:159], v2 offset:3840\n"
"v_mfma_f32_16x16x32_bf16 v[88:91], v[48:51], a[8:11], v[88:91]\n"
"ds_read_b128 v[160:163], v3 offset:1792\n"
"ds_read_b128 v[164:167], v4 offset:3840\n"
"v_mfma_f32_16x16x32_bf16 v[88:91], v[52:55], a[12:15], v[88:91]\n"
"s_waitcnt vmcnt(2)\n"
"v_mfma_f32_16x16x32_bf16 v[88:91], v[56:59], a[16:19], v[88:91]\n"
"s_waitcnt lgkmcnt(9)\n"
"v_add_f32 v104, v104, v124\n"
"v_mfma_f32_16x16x32_bf16 v[88:91], v[60:63], a[20:23], v[88:91]\n"
"s_waitcnt vmcnt(1)\n"
"v_add_f32 v105, v105, v125\n"
"v_add_f32 v106, v106, v126\n"
"v_mfma_f32_16x16x32_bf16 v[88:91], v[64:67], a[24:27], v[88:91]\n"
"v_add_f32 v107, v107, v127\n"
"v_add_f32 v104, v104, v128\n"
"v_mfma_f32_16x16x32_bf16 v[168:171], v[108:111], a[28:31], 0\n"
"v_mfma_f32_16x16x32_bf16 v[168:171], v[112:115], a[32:35], v[168:171]\n"
"ds_write_addtid_b32 v88 offset:2048\n"
"v_add_f32 v105, v105, v129\n"
"v_mfma_f32_16x16x32_bf16 v[168:171], v[48:51], a[36:39], v[168:171]\n"
"ds_write_addtid_b32 v89 offset:6688\n"
"v_add_f32 v106, v106, v130\n"
"v_mfma_f32_16x16x32_bf16 v[168:171], v[52:55], a[40:43], v[168:171]\n"
"ds_write_addtid_b32 v90 offset:11392\n"
"v_add_f32 v107, v107, v131\n"
"v_mfma_f32_16x16x32_bf16 v[168:171], v[56:59], a[44:47], v[168:171]\n"
"ds_write_addtid_b32 v91 offset:16032\n"
"v_add_f32 v104, v104, v100\n"
"v_mfma_f32_16x16x32_bf16 v[168:171], v[60:63], a[48:51], v[168:171]\n"
"v_add_f32 v105, v105, v101\n"
"v_mfma_f32_16x16x32_bf16 v[168:171], v[64:67], a[52:55], v[168:171]\n"
"v_add_f32 v106, v106, v102\n"
"v_add_f32 v107, v107, v103\n"
"v_mfma_f32_16x16x32_bf16 v[124:127], v[108:111], a[56:59], 0\n"
"s_add_u32 s92, s50, 0xffe80000\n"
"s_and_b32 s93, s92, 0xfbffffff\n"
"v_cvt_pk_bf16_f32 v128, v104, v105\n"
"v_mfma_f32_16x16x32_bf16 v[124:127], v[112:115], a[60:63], v[124:127]\n"
"ds_write_addtid_b32 v168 offset:2304\n"
"v_cvt_pk_bf16_f32 v129, v106, v107\n"
"v_mfma_f32_16x16x32_bf16 v[124:127], v[48:51], a[64:67], v[124:127]\n"
"s_waitcnt lgkmcnt(11)\n"
"ds_write_addtid_b32 v169 offset:6944\n"
"v_add_f32 v132, v132, v136\n"
"v_mfma_f32_16x16x32_bf16 v[124:127], v[52:55], a[68:71], v[124:127]\n"
"ds_write_addtid_b32 v170 offset:11648\n"
"v_add_f32 v133, v133, v137\n"
"v_mfma_f32_16x16x32_bf16 v[124:127], v[56:59], a[72:75], v[124:127]\n"
"s_waitcnt vmcnt(0)\n"
"ds_write_addtid_b32 v171 offset:16288\n"
"v_mfma_f32_16x16x32_bf16 v[124:127], v[60:63], a[76:79], v[124:127]\n"
"v_add_f32 v134, v134, v138\n"
"v_mfma_f32_16x16x32_bf16 v[124:127], v[64:67], a[80:83], v[124:127]\n"
"v_add_f32 v135, v135, v139\n"
"v_add_f32 v132, v132, v140\n"
"v_mfma_f32_16x16x32_bf16 v[100:103], v[108:111], a[84:87], 0\n"
"v_add_f32 v133, v133, v141\n"
"v_add_f32 v134, v134, v142\n"
"v_mfma_f32_16x16x32_bf16 v[100:103], v[112:115], a[88:91], v[100:103]\n"
"ds_write_addtid_b32 v124 offset:2560\n"
"v_add_f32 v135, v135, v143\n"
"v_mfma_f32_16x16x32_bf16 v[100:103], v[48:51], a[92:95], v[100:103]\n"
"ds_write_addtid_b32 v125 offset:7200\n"
"v_add_f32 v132, v132, v96\n"
"v_mfma_f32_16x16x32_bf16 v[100:103], v[52:55], a[96:99], v[100:103]\n"
"ds_write_addtid_b32 v126 offset:11904\n"
"v_add_f32 v133, v133, v97\n"
"v_mfma_f32_16x16x32_bf16 v[100:103], v[56:59], a[100:103], v[100:103]\n"
"ds_write_addtid_b32 v127 offset:16544\n"
"v_add_f32 v134, v134, v98\n"
"v_mfma_f32_16x16x32_bf16 v[100:103], v[60:63], a[104:107], v[100:103]\n"
"v_add_f32 v135, v135, v99\n"
"v_cvt_pk_bf16_f32 v130, v132, v133\n"
"v_mfma_f32_16x16x32_bf16 v[100:103], v[64:67], a[108:111], v[100:103]\n"
"buffer_store_dwordx2 v[0:1], v14, s[40:43], s95 offen offset:0 sc1\n"
"v_cvt_pk_bf16_f32 v131, v134, v135\n"
"v_mfma_f32_16x16x32_bf16 v[96:99], v[108:111], a[112:115], 0\n"
"buffer_store_dwordx4 v[128:131], v13, s[40:43], s93 offen sc1\n"
"v_mfma_f32_16x16x32_bf16 v[96:99], v[112:115], a[116:119], v[96:99]\n"
"s_waitcnt lgkmcnt(15)\n"
"ds_write_addtid_b32 v100 offset:2816\n"
"v_add_f32 v144, v144, v148\n"
"v_mfma_f32_16x16x32_bf16 v[96:99], v[48:51], a[120:123], v[96:99]\n"
"ds_write_addtid_b32 v101 offset:7456\n"
"v_add_f32 v145, v145, v149\n"
"v_mfma_f32_16x16x32_bf16 v[96:99], v[52:55], a[124:127], v[96:99]\n"
"ds_write_addtid_b32 v102 offset:12160\n"
"v_add_f32 v146, v146, v150\n"
"v_mfma_f32_16x16x32_bf16 v[96:99], v[56:59], a[128:131], v[96:99]\n"
"ds_write_addtid_b32 v103 offset:16800\n"
"v_add_f32 v147, v147, v151\n"
"v_mfma_f32_16x16x32_bf16 v[96:99], v[60:63], a[132:135], v[96:99]\n"
"v_add_f32 v144, v144, v152\n"
"v_add_f32 v145, v145, v153\n"
"v_mfma_f32_16x16x32_bf16 v[96:99], v[64:67], a[136:139], v[96:99]\n"
"v_add_f32 v146, v146, v154\n"
"v_add_f32 v147, v147, v155\n"
"v_mfma_f32_16x16x32_bf16 v[100:103], v[108:111], a[140:143], 0\n"
"v_add_f32 v144, v144, v116\n"
"v_add_f32 v145, v145, v117\n"
"v_mfma_f32_16x16x32_bf16 v[100:103], v[112:115], a[144:147], v[100:103]\n"
"ds_write_addtid_b32 v96 offset:3072\n"
"v_add_f32 v146, v146, v118\n"
"v_mfma_f32_16x16x32_bf16 v[100:103], v[48:51], a[148:151], v[100:103]\n"
"ds_write_addtid_b32 v97 offset:7712\n"
"v_add_f32 v147, v147, v119\n"
"v_mfma_f32_16x16x32_bf16 v[100:103], v[52:55], a[152:155], v[100:103]\n"
"ds_write_addtid_b32 v98 offset:12416\n"
"v_cvt_pk_bf16_f32 v104, v144, v145\n"
"v_mfma_f32_16x16x32_bf16 v[100:103], v[56:59], a[156:159], v[100:103]\n"
"ds_write_addtid_b32 v99 offset:17056\n"
"v_cvt_pk_bf16_f32 v105, v146, v147\n"
"v_mfma_f32_16x16x32_bf16 v[100:103], v[60:63], a[160:163], v[100:103]\n"
"s_waitcnt lgkmcnt(15)\n"
"v_add_f32 v156, v156, v160\n"
"v_add_f32 v157, v157, v161\n"
"v_mfma_f32_16x16x32_bf16 v[100:103], v[64:67], a[164:167], v[100:103]\n"
"v_add_f32 v158, v158, v162\n"
"v_add_f32 v159, v159, v163\n"
"v_mfma_f32_16x16x32_bf16 v[96:99], v[108:111], a[224:227], 0\n"
"v_add_f32 v156, v156, v164\n"
"v_add_f32 v157, v157, v165\n"
"v_mfma_f32_16x16x32_bf16 v[96:99], v[112:115], a[228:231], v[96:99]\n"
"ds_write_addtid_b32 v100 offset:3328\n"
"v_add_f32 v158, v158, v166\n"
"v_mfma_f32_16x16x32_bf16 v[96:99], v[48:51], a[232:235], v[96:99]\n"
"ds_write_addtid_b32 v101 offset:7968\n"
"v_add_f32 v159, v159, v167\n"
"v_mfma_f32_16x16x32_bf16 v[96:99], v[52:55], a[236:239], v[96:99]\n"
"ds_write_addtid_b32 v102 offset:12672\n"
"v_add_f32 v156, v156, v120\n"
"v_mfma_f32_16x16x32_bf16 v[96:99], v[56:59], a[240:243], v[96:99]\n"
"ds_write_addtid_b32 v103 offset:17312\n"
"v_add_f32 v157, v157, v121\n"
"v_mfma_f32_16x16x32_bf16 v[96:99], v[60:63], a[244:247], v[96:99]\n"
"v_add_f32 v158, v158, v122\n"
"v_add_f32 v159, v159, v123\n"
"v_mfma_f32_16x16x32_bf16 v[96:99], v[64:67], a[248:251], v[96:99]\n"
"v_cvt_pk_bf16_f32 v106, v156, v157\n"
"v_cvt_pk_bf16_f32 v107, v158, v159\n"
"v_mfma_f32_16x16x32_bf16 v[100:103], v[108:111], a[252:255], 0\n"
"buffer_store_dwordx4 v[104:107], v15, s[40:43], s93 offen sc1\n"
"v_mfma_f32_16x16x32_bf16 v[100:103], v[112:115], v[24:27], v[100:103]\n"
"ds_write_addtid_b32 v96 offset:3584\n"
"v_mfma_f32_16x16x32_bf16 v[100:103], v[48:51], v[28:31], v[100:103]\n"
"ds_write_addtid_b32 v97 offset:8224\n"
"v_mfma_f32_16x16x32_bf16 v[100:103], v[52:55], v[32:35], v[100:103]\n"
"ds_write_addtid_b32 v98 offset:12928\n"
"v_mfma_f32_16x16x32_bf16 v[100:103], v[56:59], v[36:39], v[100:103]\n"
"ds_write_addtid_b32 v99 offset:17568\n"
"v_mfma_f32_16x16x32_bf16 v[100:103], v[60:63], v[40:43], v[100:103]\n"
"v_mfma_f32_16x16x32_bf16 v[100:103], v[64:67], v[44:47], v[100:103]\n"
"v_mfma_f32_16x16x32_bf16 v[104:107], a[168:171], v[108:111], 0\n"
"v_mfma_f32_16x16x32_bf16 v[104:107], a[172:175], v[112:115], v[104:107]\n"
"ds_write_addtid_b32 v100 offset:3840\n"
"v_mfma_f32_16x16x32_bf16 v[104:107], a[176:179], v[48:51], v[104:107]\n"
"ds_write_addtid_b32 v101 offset:8480\n"
"v_mfma_f32_16x16x32_bf16 v[104:107], a[180:183], v[52:55], v[104:107]\n"
"ds_write_addtid_b32 v102 offset:13184\n"
"v_mfma_f32_16x16x32_bf16 v[104:107], a[184:187], v[56:59], v[104:107]\n"
"ds_write_addtid_b32 v103 offset:17824\n"
"v_mfma_f32_16x16x32_bf16 v[104:107], a[188:191], v[60:63], v[104:107]\n"
"v_mfma_f32_16x16x32_bf16 v[104:107], a[192:195], v[64:67], v[104:107]\n"
"v_mfma_f32_16x16x32_bf16 v[116:119], a[196:199], v[108:111], 0\n"
"v_mfma_f32_16x16x32_bf16 v[116:119], a[200:203], v[112:115], v[116:119]\n"
"s_waitcnt lgkmcnt(0)\n"
"v_mfma_f32_16x16x32_bf16 v[116:119], a[204:207], v[48:51], v[116:119]\n"
"s_barrier\n"
"v_mfma_f32_16x16x32_bf16 v[116:119], a[208:211], v[52:55], v[116:119]\n"
"ds_read_b128 v[108:111], v2 offset:0\n"
"ds_read_b128 v[112:115], v3 offset:2560\n"
"v_mfma_f32_16x16x32_bf16 v[116:119], a[212:215], v[56:59], v[116:119]\n"
"ds_read_b128 v[120:123], v4 offset:1024\n"
"ds_read_b128 v[124:127], v2 offset:256\n"
"v_mfma_f32_16x16x32_bf16 v[116:119], a[216:219], v[60:63], v[116:119]\n"
"ds_read_b128 v[128:131], v3 offset:2816\n"
"ds_read_b128 v[132:135], v4 offset:1280\n"
"v_mfma_f32_16x16x32_bf16 v[116:119], a[220:223], v[64:67], v[116:119]\n"
"s_waitcnt lgkmcnt(3)\n"
"v_add_f32 v108, v108, v112\n"
"v_add_f32 v109, v109, v113\n"
"v_add_f32 v110, v110, v114\n"
"v_add_f32 v111, v111, v115\n"
"v_add_f32 v108, v108, v120\n"
"v_add_f32 v109, v109, v121\n"
"v_add_f32 v110, v110, v122\n"
"v_add_f32 v111, v111, v123\n"
"v_add_f32 v108, v108, v104\n"
"v_add_f32 v109, v109, v105\n"
"v_add_f32 v110, v110, v106\n"
"v_add_f32 v111, v111, v107\n"
"s_add_u32 s95, s50, 0x0\n"
"s_and_b32 s93, s95, 0xfbffffff\n"
"v_cvt_pk_bf16_f32 v112, v108, v109\n"
"v_cvt_pk_bf16_f32 v113, v110, v111\n"
"s_waitcnt lgkmcnt(0)\n"
"v_add_f32 v124, v124, v128\n"
"v_add_f32 v125, v125, v129\n"
"v_add_f32 v126, v126, v130\n"
"v_add_f32 v127, v127, v131\n"
"v_add_f32 v124, v124, v132\n"
"v_add_f32 v125, v125, v133\n"
"v_add_f32 v126, v126, v134\n"
"v_add_f32 v127, v127, v135\n"
"v_add_f32 v124, v124, v116\n"
"v_add_f32 v125, v125, v117\n"
"v_add_f32 v126, v126, v118\n"
"v_add_f32 v127, v127, v119\n"
"v_cvt_pk_bf16_f32 v114, v124, v125\n"
"v_cvt_pk_bf16_f32 v115, v126, v127\n"
"s_waitcnt vmcnt(0)\n"
"buffer_store_dwordx2 v[0:1], v16, s[40:43], s92 offen offset:0 sc1\n"
"buffer_store_dwordx4 v[112:115], v13, s[40:43], s93 offen sc1\n"
"s_waitcnt vmcnt(0)\n"
"buffer_store_dwordx2 v[0:1], v14, s[40:43], s95 offen offset:0 sc1\n"
"s_branch .LH_end\n"
".LH_t3:\n"
"v_mfma_f32_16x16x32_bf16 v[104:107], a[192:195], v[64:67], v[104:107]\n"
"v_mfma_f32_16x16x32_bf16 v[116:119], a[196:199], v[108:111], 0\n"
"v_mfma_f32_16x16x32_bf16 v[116:119], a[200:203], v[112:115], v[116:119]\n"
"s_waitcnt lgkmcnt(0)\n"
"v_mfma_f32_16x16x32_bf16 v[116:119], a[204:207], v[48:51], v[116:119]\n"
"s_barrier\n"
"v_mfma_f32_16x16x32_bf16 v[116:119], a[208:211], v[52:55], v[116:119]\n"
"ds_read_b128 v[108:111], v2 offset:0\n"
"ds_read_b128 v[112:115], v3 offset:2560\n"
"v_mfma_f32_16x16x32_bf16 v[116:119], a[212:215], v[56:59], v[116:119]\n"
"ds_read_b128 v[120:123], v4 offset:1024\n"
"ds_read_b128 v[124:127], v2 offset:256\n"
"v_mfma_f32_16x16x32_bf16 v[116:119], a[216:219], v[60:63], v[116:119]\n"
"ds_read_b128 v[128:131], v3 offset:2816\n"
"ds_read_b128 v[132:135], v4 offset:1280\n"
"v_mfma_f32_16x16x32_bf16 v[116:119], a[220:223], v[64:67], v[116:119]\n"
"s_waitcnt vmcnt(10)\n"
"v_mfma_f32_16x16x32_bf16 v[56:59], v[68:71], a[0:3], 0\n"
"v_mfma_f32_16x16x32_bf16 v[56:59], v[72:75], a[4:7], v[56:59]\n"
"s_waitcnt vmcnt(8)\n"
"v_mfma_f32_16x16x32_bf16 v[56:59], v[76:79], a[8:11], v[56:59]\n"
"v_mfma_f32_16x16x32_bf16 v[56:59], v[80:83], a[12:15], v[56:59]\n"
"s_waitcnt vmcnt(5)\n"
"v_mfma_f32_16x16x32_bf16 v[56:59], v[84:87], a[16:19], v[56:59]\n"
"s_waitcnt lgkmcnt(3)\n"
"v_add_f32 v108, v108, v112\n"
"v_add_f32 v109, v109, v113\n"
"v_mfma_f32_16x16x32_bf16 v[56:59], v[88:91], a[20:23], v[56:59]\n"
"s_waitcnt vmcnt(4)\n"
"v_add_f32 v110, v110, v114\n"
"v_add_f32 v111, v111, v115\n"
"v_mfma_f32_16x16x32_bf16 v[56:59], v[92:95], a[24:27], v[56:59]\n"
"v_add_f32 v108, v108, v120\n"
"v_add_f32 v109, v109, v121\n"
"v_mfma_f32_16x16x32_bf16 v[64:67], v[68:71], a[28:31], 0\n"
"v_add_f32 v110, v110, v122\n"
"v_mfma_f32_16x16x32_bf16 v[64:67], v[72:75], a[32:35], v[64:67]\n"
"ds_write_addtid_b32 v56 offset:0\n"
"v_add_f32 v111, v111, v123\n"
"v_mfma_f32_16x16x32_bf16 v[64:67], v[76:79], a[36:39], v[64:67]\n"
"ds_write_addtid_b32 v57 offset:4640\n"
"v_add_f32 v108, v108, v104\n"
"v_mfma_f32_16x16x32_bf16 v[64:67], v[80:83], a[40:43], v[64:67]\n"
"ds_write_addtid_b32 v58 offset:9344\n"
"v_add_f32 v109, v109, v105\n"
"v_mfma_f32_16x16x32_bf16 v[64:67], v[84:87], a[44:47], v[64:67]\n"
"ds_write_addtid_b32 v59 offset:13984\n"
"v_add_f32 v110, v110, v106\n"
"v_mfma_f32_16x16x32_bf16 v[64:67], v[88:91], a[48:51], v[64:67]\n"
"v_add_f32 v111, v111, v107\n"
"v_mfma_f32_16x16x32_bf16 v[64:67], v[92:95], a[52:55], v[64:67]\n"
"s_add_u32 s95, s50, 0xffe80000\n"
"s_and_b32 s93, s95, 0xfbffffff\n"
"v_mfma_f32_16x16x32_bf16 v[104:107], v[68:71], a[56:59], 0\n"
"v_cvt_pk_bf16_f32 v112, v108, v109\n"
"v_cvt_pk_bf16_f32 v113, v110, v111\n"
"v_mfma_f32_16x16x32_bf16 v[104:107], v[72:75], a[60:63], v[104:107]\n"
"s_waitcnt lgkmcnt(4)\n"
"ds_write_addtid_b32 v64 offset:256\n"
"v_add_f32 v124, v124, v128\n"
"v_mfma_f32_16x16x32_bf16 v[104:107], v[76:79], a[64:67], v[104:107]\n"
"ds_write_addtid_b32 v65 offset:4896\n"
"v_add_f32 v125, v125, v129\n"
"v_mfma_f32_16x16x32_bf16 v[104:107], v[80:83], a[68:71], v[104:107]\n"
"ds_write_addtid_b32 v66 offset:9600\n"
"v_mfma_f32_16x16x32_bf16 v[104:107], v[84:87], a[72:75], v[104:107]\n"
"ds_write_addtid_b32 v67 offset:14240\n"
"v_add_f32 v126, v126, v130\n"
"v_mfma_f32_16x16x32_bf16 v[104:107], v[88:91], a[76:79], v[104:107]\n"
"v_add_f32 v127, v127, v131\n"
"v_add_f32 v124, v124, v132\n"
"v_mfma_f32_16x16x32_bf16 v[104:107], v[92:95], a[80:83], v[104:107]\n"
"v_add_f32 v125, v125, v133\n"
"v_add_f32 v126, v126, v134\n"
"v_mfma_f32_16x16x32_bf16 v[108:111], v[68:71], a[84:87], 0\n"
"v_add_f32 v127, v127, v135\n"
"v_add_f32 v124, v124, v116\n"
"v_mfma_f32_16x16x32_bf16 v[108:111], v[72:75], a[88:91], v[108:111]\n"
"ds_write_addtid_b32 v104 offset:512\n"
"v_add_f32 v125, v125, v117\n"
"v_mfma_f32_16x16x32_bf16 v[108:111], v[76:79], a[92:95], v[108:111]\n"
"ds_write_addtid_b32 v105 offset:5152\n"
"v_mfma_f32_16x16x32_bf16 v[108:111], v[80:83], a[96:99], v[108:111]\n"
"ds_write_addtid_b32 v106 offset:9856\n"
"v_add_f32 v126, v126, v118\n"
"v_mfma_f32_16x16x32_bf16 v[108:111], v[84:87], a[100:103], v[108:111]\n"
"ds_write_addtid_b32 v107 offset:14496\n"
"v_add_f32 v127, v127, v119\n"
"v_mfma_f32_16x16x32_bf16 v[108:111], v[88:91], a[104:107], v[108:111]\n"
"v_cvt_pk_bf16_f32 v114, v124, v125\n"
"v_cvt_pk_bf16_f32 v115, v126, v127\n"
"v_mfma_f32_16x16x32_bf16 v[108:111], v[92:95], a[108:111], v[108:111]\n"
"s_waitcnt vmcnt(1)\n"
"buffer_store_dwordx2 v[0:1], v16, s[40:43], s92 offen offset:0 sc1\n"
"v_mfma_f32_16x16x32_bf16 v[104:107], v[68:71], a[112:115], 0\n"
"v_mfma_f32_16x16x32_bf16 v[104:107], v[72:75], a[116:119], v[104:107]\n"
"ds_write_addtid_b32 v108 offset:768\n"
"v_mfma_f32_16x16x32_bf16 v[104:107], v[76:79], a[120:123], v[104:107]\n"
"buffer_store_dwordx4 v[112:115], v13, s[40:43], s93 offen sc1\n"
"ds_write_addtid_b32 v109 offset:5408\n"
"v_mfma_f32_16x16x32_bf16 v[104:107], v[80:83], a[124:127], v[104:107]\n"
"ds_write_addtid_b32 v110 offset:10112\n"
"v_mfma_f32_16x16x32_bf16 v[104:107], v[84:87], a[128:131], v[104:107]\n"
"s_waitcnt vmcnt(2)\n"
"ds_write_addtid_b32 v111 offset:14752\n"
"v_mfma_f32_16x16x32_bf16 v[104:107], v[88:91], a[132:135], v[104:107]\n"
"v_mfma_f32_16x16x32_bf16 v[104:107], v[92:95], a[136:139], v[104:107]\n"
"v_mfma_f32_16x16x32_bf16 v[108:111], v[68:71], a[140:143], 0\n"
"v_mfma_f32_16x16x32_bf16 v[108:111], v[72:75], a[144:147], v[108:111]\n"
"ds_write_addtid_b32 v104 offset:1024\n"
"v_mfma_f32_16x16x32_bf16 v[108:111], v[76:79], a[148:151], v[108:111]\n"
"ds_write_addtid_b32 v105 offset:5664\n"
"v_mfma_f32_16x16x32_bf16 v[108:111], v[80:83], a[152:155], v[108:111]\n"
"ds_write_addtid_b32 v106 offset:10368\n"
"v_mfma_f32_16x16x32_bf16 v[108:111], v[84:87], a[156:159], v[108:111]\n"
"ds_write_addtid_b32 v107 offset:15008\n"
"v_mfma_f32_16x16x32_bf16 v[108:111], v[88:91], a[160:163], v[108:111]\n"
"v_mfma_f32_16x16x32_bf16 v[108:111], v[92:95], a[164:167], v[108:111]\n"
"v_mfma_f32_16x16x32_bf16 v[104:107], v[68:71], a[224:227], 0\n"
"v_mfma_f32_16x16x32_bf16 v[104:107], v[72:75], a[228:231], v[104:107]\n"
"ds_write_addtid_b32 v108 offset:1280\n"
"v_mfma_f32_16x16x32_bf16 v[104:107], v[76:79], a[232:235], v[104:107]\n"
"ds_write_addtid_b32 v109 offset:5920\n"
"v_mfma_f32_16x16x32_bf16 v[104:107], v[80:83], a[236:239], v[104:107]\n"
"ds_write_addtid_b32 v110 offset:10624\n"
"v_mfma_f32_16x16x32_bf16 v[104:107], v[84:87], a[240:243], v[104:107]\n"
"ds_write_addtid_b32 v111 offset:15264\n"
"v_mfma_f32_16x16x32_bf16 v[104:107], v[88:91], a[244:247], v[104:107]\n"
"v_mfma_f32_16x16x32_bf16 v[104:107], v[92:95], a[248:251], v[104:107]\n"
"v_mfma_f32_16x16x32_bf16 v[120:123], v[68:71], a[252:255], 0\n"
"v_mfma_f32_16x16x32_bf16 v[120:123], v[72:75], v[24:27], v[120:123]\n"
"ds_write_addtid_b32 v104 offset:1536\n"
"v_mfma_f32_16x16x32_bf16 v[120:123], v[76:79], v[28:31], v[120:123]\n"
"ds_write_addtid_b32 v105 offset:6176\n"
"v_mfma_f32_16x16x32_bf16 v[120:123], v[80:83], v[32:35], v[120:123]\n"
"ds_write_addtid_b32 v106 offset:10880\n"
"v_mfma_f32_16x16x32_bf16 v[120:123], v[84:87], v[36:39], v[120:123]\n"
"ds_write_addtid_b32 v107 offset:15520\n"
"v_mfma_f32_16x16x32_bf16 v[120:123], v[88:91], v[40:43], v[120:123]\n"
"v_mfma_f32_16x16x32_bf16 v[120:123], v[92:95], v[44:47], v[120:123]\n"
"v_mfma_f32_16x16x32_bf16 v[108:111], a[168:171], v[68:71], 0\n"
"v_mfma_f32_16x16x32_bf16 v[108:111], a[172:175], v[72:75], v[108:111]\n"
"ds_write_addtid_b32 v120 offset:1792\n"
"v_mfma_f32_16x16x32_bf16 v[108:111], a[176:179], v[76:79], v[108:111]\n"
"ds_write_addtid_b32 v121 offset:6432\n"
"v_mfma_f32_16x16x32_bf16 v[108:111], a[180:183], v[80:83], v[108:111]\n"
"ds_write_addtid_b32 v122 offset:11136\n"
"v_mfma_f32_16x16x32_bf16 v[108:111], a[184:187], v[84:87], v[108:111]\n"
"ds_write_addtid_b32 v123 offset:15776\n"
"v_mfma_f32_16x16x32_bf16 v[108:111], a[188:191], v[88:91], v[108:111]\n"
"v_mfma_f32_16x16x32_bf16 v[108:111], a[192:195], v[92:95], v[108:111]\n"
"v_mfma_f32_16x16x32_bf16 v[120:123], a[196:199], v[68:71], 0\n"
"v_mfma_f32_16x16x32_bf16 v[120:123], a[200:203], v[72:75], v[120:123]\n"
"s_waitcnt lgkmcnt(0)\n"
"v_mfma_f32_16x16x32_bf16 v[120:123], a[204:207], v[76:79], v[120:123]\n"
"s_barrier\n"
"v_mfma_f32_16x16x32_bf16 v[120:123], a[208:211], v[80:83], v[120:123]\n"
"ds_read_b128 v[68:71], v2 offset:2048\n"
"v_mfma_f32_16x16x32_bf16 v[120:123], a[212:215], v[84:87], v[120:123]\n"
"ds_read_b128 v[124:127], v3 offset:512\n"
"ds_read_b128 v[128:131], v4 offset:3072\n"
"v_mfma_f32_16x16x32_bf16 v[120:123], a[216:219], v[88:91], v[120:123]\n"
"ds_read_b128 v[132:135], v2 offset:2304\n"
"ds_read_b128 v[136:139], v3 offset:768\n"
"v_mfma_f32_16x16x32_bf16 v[120:123], a[220:223], v[92:95], v[120:123]\n"
"ds_read_b128 v[140:143], v4 offset:3328\n"
"s_waitcnt lgkmcnt(3)\n"
"v_add_f32 v68, v68, v124\n"
"v_add_f32 v69, v69, v125\n"
"v_add_f32 v70, v70, v126\n"
"v_add_f32 v71, v71, v127\n"
"v_add_f32 v68, v68, v128\n"
"s_waitcnt vmcnt(0)\n"
"buffer_store_dwordx2 v[0:1], v14, s[40:43], s95 offen offset:0 sc1\n"
"v_add_f32 v69, v69, v129\n"
"v_add_f32 v70, v70, v130\n"
"v_add_f32 v71, v71, v131\n"
"v_add_f32 v68, v68, v108\n"
"v_add_f32 v69, v69, v109\n"
"v_add_f32 v70, v70, v110\n"
"v_add_f32 v71, v71, v111\n"
"s_add_u32 s93, s50, 0x0\n"
"s_and_b32 s95, s93, 0xfbffffff\n"
"v_cvt_pk_bf16_f32 v108, v68, v69\n"
"v_cvt_pk_bf16_f32 v109, v70, v71\n"
"s_waitcnt lgkmcnt(0)\n"
"v_add_f32 v132, v132, v136\n"
"v_add_f32 v133, v133, v137\n"
"v_add_f32 v134, v134, v138\n"
"v_add_f32 v135, v135, v139\n"
"v_add_f32 v132, v132, v140\n"
"v_add_f32 v133, v133, v141\n"
"v_add_f32 v134, v134, v142\n"
"v_add_f32 v135, v135, v143\n"
"v_add_f32 v132, v132, v120\n"
"v_add_f32 v133, v133, v121\n"
"v_add_f32 v134, v134, v122\n"
"v_add_f32 v135, v135, v123\n"
"v_cvt_pk_bf16_f32 v110, v132, v133\n"
"v_cvt_pk_bf16_f32 v111, v134, v135\n"
"buffer_store_dwordx4 v[108:111], v13, s[40:43], s95 offen sc1\n"
"s_waitcnt vmcnt(0)\n"
"buffer_store_dwordx2 v[0:1], v14, s[40:43], s93 offen offset:0 sc1\n"
"s_branch .LH_end\n"
".LH_end:\n"
"s_waitcnt vmcnt(0) lgkmcnt(0)\n"
"s_endpgm\n"
      :: "{v2}"((unsigned)(lds0 + ((wid + 3) & 3) * 18592 + trp)), "{v3}"((unsigned)(lds0 + ((wid + 2) & 3) * 18592 + trp)), "{v4}"((unsigned)(lds0 + ((wid + 1) & 3) * 18592 + trp)), "{v5}"((unsigned)(lane * 16 + f0 * 1024 + ((0 + rot) % 7) * 1024 + 134316032)), "{v6}"((unsigned)(lane * 16 + f0 * 1024 + ((1 + rot) % 7) * 1024 + 134316032)), "{v7}"((unsigned)(lane * 16 + f0 * 1024 + ((2 + rot) % 7) * 1024 + 134316032)), "{v8}"((unsigned)(lane * 16 + f0 * 1024 + ((3 + rot) % 7) * 1024 + 134316032)), "{v9}"((unsigned)(lane * 16 + f0 * 1024 + ((4 + rot) % 7) * 1024 + 134316032)), "{v10}"((unsigned)(lane * 16 + f0 * 1024 + ((5 + rot) % 7) * 1024 + 134316032)), "{v11}"((unsigned)(lane * 16 + f0 * 1024 + ((6 + rot) % 7) * 1024 + 134316032)), "{v12}"((unsigned)((f0 + lane % 7) * 8 + kq * 32 + 353024 + H_GL * 1572864)), "{v13}"((unsigned)(lane * 16 + pair * 4096 + kq * 1024 + 212992)), "{v14}"((unsigned)(lane ? DROP : pair * 64 + kq * 8 + 354304)), "{v15}"((unsigned)(lane * 16 + (pg0 + 4) * 4096 + kq * 1024 + 212992)), "{v16}"((unsigned)(lane > 1 ? DROP : (lane ? pg0 + 4 : pair) * 64 + kq * 8 + 354304)), "{v17}"((unsigned)((lane & 15) * 8 + 67462912)), "{v18}"((unsigned)(lane * 16 + 67457024)), "{v19}"((unsigned)((lane & 24) ? DROP : (2 * (lane & 7) + (lane >> 5)) * 16 + 67461120)), "{v20}"((unsigned)(0x3727c5ac)), "{v48}"((unsigned)(vT + tsw)), "{v49}"((unsigned)(vT + (tsw ^ 8))), "{v50}"((unsigned)(vD + ((0 + rot) % 7) * 65536)), "{v51}"((unsigned)(vD + ((1 + rot) % 7) * 65536)), "{v52}"((unsigned)(vD + ((2 + rot) % 7) * 65536)), "{v53}"((unsigned)(vD + ((3 + rot) % 7) * 65536)), "{v54}"((unsigned)(vD + ((4 + rot) % 7) * 65536)), "{v55}"((unsigned)(vD + ((5 + rot) % 7) * 65536)), "{v56}"((unsigned)(vD + ((6 + rot) % 7) * 65536)), "{s[40:43]}"(rS), "{s[44:47]}"(rW), "{s[48:49]}"(wsp), "{s50}"((unsigned)(sA)), "{s51}"((unsigned)(sLo)), "{s52}"((unsigned)(sHi)), "{s53}"((unsigned)(sEx)), "{s54}"((unsigned)(sEnt)), "{s55}"((unsigned)(sLd)), "{s[56:57]}"(wsb + (sLo & 0x3ffffff)), "{s59}"((unsigned)(lds0 + wid * 18592)), "{s62}"((unsigned)(0x3a800000)), "{s66}"((unsigned)(sW0 + 64 * ((wid + 1) & 3))), "{s67}"((unsigned)(sW0 + 64 * ((wid + 2) & 3))), "{s68}"((unsigned)(sW0 + 64 * ((wid + 3) & 3))), "{s69}"((unsigned)(sW0 + 64 * ((wid + 4) & 3))), "{s70}"((unsigned)(sW0 + 256))
      : "v255", "a255", "s95", "vcc", "scc", "memory");

    } else if (grp < 4) {
      const unsigned sEx = sHi + G_EX * 3 * RS, sEnt = -wid & 3;
    asm volatile(
"s_nop 4\n"
"v_mov_b32 v186, 0\n"
"global_load_dwordx2 v[0:1], v186, s[48:49] sc1\n"
"v_mov_b32 v188, 0x3f803f80\n"
"v_mov_b32 v189, 0x3f803f80\n"
"v_mov_b32 v190, 0x3f803f80\n"
"v_mov_b32 v191, 0x3f803f80\n"
"s_add_u32 m0, s55, 0\n"
"s_add_u32 s60, s62, 0\n"
"buffer_load_dwordx4 v46, s[44:47], s60 offen lds\n"
"s_add_u32 m0, s55, 1024\n"
"s_add_u32 s60, s62, 32768\n"
"buffer_load_dwordx4 v46, s[44:47], s60 offen lds\n"
"s_add_u32 m0, s55, 2048\n"
"s_add_u32 s60, s63, 0\n"
"buffer_load_dwordx4 v46, s[44:47], s60 offen lds\n"
"s_add_u32 m0, s55, 3072\n"
"s_add_u32 s60, s63, 32768\n"
"buffer_load_dwordx4 v46, s[44:47], s60 offen lds\n"
"s_add_u32 m0, s55, 4096\n"
"s_add_u32 s60, s64, 0\n"
"buffer_load_dwordx4 v46, s[44:47], s60 offen lds\n"
"s_add_u32 m0, s55, 5120\n"
"s_add_u32 s60, s64, 32768\n"
"buffer_load_dwordx4 v46, s[44:47], s60 offen lds\n"
"s_add_u32 m0, s55, 6144\n"
"s_add_u32 s60, s65, 0\n"
"buffer_load_dwordx4 v46, s[44:47], s60 offen lds\n"
"s_add_u32 m0, s55, 7168\n"
"s_add_u32 s60, s65, 32768\n"
"buffer_load_dwordx4 v46, s[44:47], s60 offen lds\n"
"s_add_u32 m0, s55, 8192\n"
"s_add_u32 s60, s66, 0\n"
"buffer_load_dwordx4 v46, s[44:47], s60 offen lds\n"
"s_add_u32 m0, s55, 9216\n"
"s_add_u32 s60, s66, 32768\n"
"buffer_load_dwordx4 v46, s[44:47], s60 offen lds\n"
"s_add_u32 m0, s55, 10240\n"
"s_add_u32 s60, s62, 0\n"
"buffer_load_dwordx4 v47, s[44:47], s60 offen lds\n"
"s_add_u32 m0, s55, 11264\n"
"s_add_u32 s60, s62, 32768\n"
"buffer_load_dwordx4 v47, s[44:47], s60 offen lds\n"
"s_add_u32 m0, s55, 12288\n"
"s_add_u32 s60, s63, 0\n"
"buffer_load_dwordx4 v47, s[44:47], s60 offen lds\n"
"s_add_u32 m0, s55, 13312\n"
"s_add_u32 s60, s63, 32768\n"
"buffer_load_dwordx4 v47, s[44:47], s60 offen lds\n"
"s_add_u32 m0, s55, 14336\n"
"s_add_u32 s60, s64, 0\n"
"buffer_load_dwordx4 v47, s[44:47], s60 offen lds\n"
"s_add_u32 m0, s55, 15360\n"
"s_add_u32 s60, s64, 32768\n"
"buffer_load_dwordx4 v47, s[44:47], s60 offen lds\n"
"s_add_u32 m0, s55, 16384\n"
"s_add_u32 s60, s65, 0\n"
"buffer_load_dwordx4 v47, s[44:47], s60 offen lds\n"
"s_add_u32 m0, s55, 17408\n"
"s_add_u32 s60, s65, 32768\n"
"buffer_load_dwordx4 v47, s[44:47], s60 offen lds\n"
"s_add_u32 m0, s55, 18432\n"
"s_add_u32 s60, s66, 0\n"
"buffer_load_dwordx4 v47, s[44:47], s60 offen lds\n"
"s_add_u32 m0, s55, 19456\n"
"s_add_u32 s60, s66, 32768\n"
"buffer_load_dwordx4 v47, s[44:47], s60 offen lds\n"
"s_add_u32 m0, s55, 20480\n"
"s_add_u32 s60, s62, 0\n"
"buffer_load_dwordx4 v48, s[44:47], s60 offen lds\n"
"s_add_u32 m0, s55, 21504\n"
"s_add_u32 s60, s62, 32768\n"
"buffer_load_dwordx4 v48, s[44:47], s60 offen lds\n"
"s_add_u32 m0, s55, 22528\n"
"s_add_u32 s60, s63, 0\n"
"buffer_load_dwordx4 v48, s[44:47], s60 offen lds\n"
"s_add_u32 m0, s55, 23552\n"
"s_add_u32 s60, s63, 32768\n"
"buffer_load_dwordx4 v48, s[44:47], s60 offen lds\n"
"s_add_u32 m0, s55, 24576\n"
"s_add_u32 s60, s64, 0\n"
"buffer_load_dwordx4 v48, s[44:47], s60 offen lds\n"
"s_add_u32 m0, s55, 25600\n"
"s_add_u32 s60, s64, 32768\n"
"buffer_load_dwordx4 v48, s[44:47], s60 offen lds\n"
"s_add_u32 m0, s55, 26624\n"
"s_add_u32 s60, s65, 0\n"
"buffer_load_dwordx4 v48, s[44:47], s60 offen lds\n"
"s_add_u32 m0, s55, 27648\n"
"s_add_u32 s60, s65, 32768\n"
"buffer_load_dwordx4 v48, s[44:47], s60 offen lds\n"
"s_add_u32 m0, s55, 28672\n"
"s_add_u32 s60, s66, 0\n"
"buffer_load_dwordx4 v48, s[44:47], s60 offen lds\n"
"s_add_u32 m0, s55, 29696\n"
"s_add_u32 s60, s66, 32768\n"
"buffer_load_dwordx4 v48, s[44:47], s60 offen lds\n"
"s_add_u32 m0, s55, 30720\n"
"s_add_u32 s60, s62, 0\n"
"buffer_load_dwordx4 v49, s[44:47], s60 offen lds\n"
"s_add_u32 m0, s55, 31744\n"
"s_add_u32 s60, s62, 32768\n"
"buffer_load_dwordx4 v49, s[44:47], s60 offen lds\n"
"s_add_u32 m0, s55, 32768\n"
"s_add_u32 s60, s63, 0\n"
"buffer_load_dwordx4 v49, s[44:47], s60 offen lds\n"
"s_add_u32 m0, s55, 33792\n"
"s_add_u32 s60, s63, 32768\n"
"buffer_load_dwordx4 v49, s[44:47], s60 offen lds\n"
"s_add_u32 m0, s55, 34816\n"
"s_add_u32 s60, s64, 0\n"
"buffer_load_dwordx4 v49, s[44:47], s60 offen lds\n"
"s_add_u32 m0, s55, 35840\n"
"s_add_u32 s60, s64, 32768\n"
"buffer_load_dwordx4 v49, s[44:47], s60 offen lds\n"
"s_add_u32 m0, s55, 36864\n"
"s_add_u32 s60, s65, 0\n"
"buffer_load_dwordx4 v49, s[44:47], s60 offen lds\n"
"s_add_u32 m0, s55, 37888\n"
"s_add_u32 s60, s65, 32768\n"
"buffer_load_dwordx4 v49, s[44:47], s60 offen lds\n"
"s_add_u32 m0, s55, 38912\n"
"s_add_u32 s60, s66, 0\n"
"buffer_load_dwordx4 v49, s[44:47], s60 offen lds\n"
"s_add_u32 m0, s55, 39936\n"
"s_add_u32 s60, s66, 32768\n"
"buffer_load_dwordx4 v49, s[44:47], s60 offen lds\n"
"s_waitcnt vmcnt(38)\n"
"ds_read_b64_tr_b16 a[0:1], v44 offset:0\n"
"ds_read_b64_tr_b16 a[2:3], v44 offset:256\n"
"ds_read_b64_tr_b16 a[28:29], v45 offset:0\n"
"ds_read_b64_tr_b16 a[30:31], v45 offset:256\n"
"v_add_u32 v0, 2, v0\n"
"v_add_u32 v1, 0x9e3779b9, v1\n"
"v_or_b32 v0, 1, v0\n"
"v_readfirstlane_b32 s49, v1\n"
"v_readfirstlane_b32 s48, v0\n"
"s_waitcnt vmcnt(36)\n"
"ds_read_b64_tr_b16 a[56:57], v44 offset:2048\n"
"ds_read_b64_tr_b16 a[58:59], v44 offset:2304\n"
"ds_read_b64_tr_b16 a[84:85], v45 offset:2048\n"
"ds_read_b64_tr_b16 a[86:87], v45 offset:2304\n"
"s_waitcnt lgkmcnt(4)\n"
"s_add_u32 m0, s55, 0\n"
"s_add_u32 s60, s62, 0\n"
"buffer_load_dwordx4 v50, s[44:47], s60 offen lds\n"
"s_add_u32 m0, s55, 1024\n"
"s_add_u32 s60, s62, 32768\n"
"buffer_load_dwordx4 v50, s[44:47], s60 offen lds\n"
"s_waitcnt vmcnt(36)\n"
"ds_read_b64_tr_b16 a[112:113], v44 offset:4096\n"
"ds_read_b64_tr_b16 a[114:115], v44 offset:4352\n"
"ds_read_b64_tr_b16 a[140:141], v45 offset:4096\n"
"ds_read_b64_tr_b16 a[142:143], v45 offset:4352\n"
"s_waitcnt lgkmcnt(4)\n"
"s_add_u32 m0, s55, 2048\n"
"s_add_u32 s60, s63, 0\n"
"buffer_load_dwordx4 v50, s[44:47], s60 offen lds\n"
"s_add_u32 m0, s55, 3072\n"
"s_add_u32 s60, s63, 32768\n"
"buffer_load_dwordx4 v50, s[44:47], s60 offen lds\n"
"s_waitcnt vmcnt(36)\n"
"ds_read_b64_tr_b16 a[168:169], v44 offset:6144\n"
"ds_read_b64_tr_b16 a[170:171], v44 offset:6400\n"
"ds_read_b64_tr_b16 a[196:197], v45 offset:6144\n"
"ds_read_b64_tr_b16 a[198:199], v45 offset:6400\n"
"s_waitcnt lgkmcnt(4)\n"
"s_add_u32 m0, s55, 4096\n"
"s_add_u32 s60, s64, 0\n"
"buffer_load_dwordx4 v50, s[44:47], s60 offen lds\n"
"s_add_u32 m0, s55, 5120\n"
"s_add_u32 s60, s64, 32768\n"
"buffer_load_dwordx4 v50, s[44:47], s60 offen lds\n"
"s_waitcnt vmcnt(36)\n"
"ds_read_b64_tr_b16 a[224:225], v44 offset:8192\n"
"ds_read_b64_tr_b16 a[226:227], v44 offset:8448\n"
"ds_read_b64_tr_b16 a[252:253], v45 offset:8192\n"
"ds_read_b64_tr_b16 a[254:255], v45 offset:8448\n"
"s_waitcnt lgkmcnt(4)\n"
"s_add_u32 m0, s55, 6144\n"
"s_add_u32 s60, s65, 0\n"
"buffer_load_dwordx4 v50, s[44:47], s60 offen lds\n"
"s_add_u32 m0, s55, 7168\n"
"s_add_u32 s60, s65, 32768\n"
"buffer_load_dwordx4 v50, s[44:47], s60 offen lds\n"
"s_waitcnt vmcnt(36)\n"
"ds_read_b64_tr_b16 a[4:5], v44 offset:10240\n"
"ds_read_b64_tr_b16 a[6:7], v44 offset:10496\n"
"ds_read_b64_tr_b16 a[32:33], v45 offset:10240\n"
"ds_read_b64_tr_b16 a[34:35], v45 offset:10496\n"
"s_waitcnt lgkmcnt(4)\n"
"s_add_u32 m0, s55, 8192\n"
"s_add_u32 s60, s66, 0\n"
"buffer_load_dwordx4 v50, s[44:47], s60 offen lds\n"
"s_add_u32 m0, s55, 9216\n"
"s_add_u32 s60, s66, 32768\n"
"buffer_load_dwordx4 v50, s[44:47], s60 offen lds\n"
"s_waitcnt vmcnt(36)\n"
"ds_read_b64_tr_b16 a[60:61], v44 offset:12288\n"
"ds_read_b64_tr_b16 a[62:63], v44 offset:12544\n"
"ds_read_b64_tr_b16 a[88:89], v45 offset:12288\n"
"ds_read_b64_tr_b16 a[90:91], v45 offset:12544\n"
"s_waitcnt lgkmcnt(4)\n"
"s_add_u32 m0, s55, 10240\n"
"s_add_u32 s60, s62, 0\n"
"buffer_load_dwordx4 v51, s[44:47], s60 offen lds\n"
"s_add_u32 m0, s55, 11264\n"
"s_add_u32 s60, s62, 32768\n"
"buffer_load_dwordx4 v51, s[44:47], s60 offen lds\n"
"s_waitcnt vmcnt(36)\n"
"ds_read_b64_tr_b16 a[116:117], v44 offset:14336\n"
"ds_read_b64_tr_b16 a[118:119], v44 offset:14592\n"
"ds_read_b64_tr_b16 a[144:145], v45 offset:14336\n"
"ds_read_b64_tr_b16 a[146:147], v45 offset:14592\n"
"s_waitcnt lgkmcnt(4)\n"
"s_add_u32 m0, s55, 12288\n"
"s_add_u32 s60, s63, 0\n"
"buffer_load_dwordx4 v51, s[44:47], s60 offen lds\n"
"s_add_u32 m0, s55, 13312\n"
"s_add_u32 s60, s63, 32768\n"
"buffer_load_dwordx4 v51, s[44:47], s60 offen lds\n"
"s_waitcnt vmcnt(36)\n"
"ds_read_b64_tr_b16 a[172:173], v44 offset:16384\n"
"ds_read_b64_tr_b16 a[174:175], v44 offset:16640\n"
"ds_read_b64_tr_b16 a[200:201], v45 offset:16384\n"
"ds_read_b64_tr_b16 a[202:203], v45 offset:16640\n"
"s_waitcnt lgkmcnt(4)\n"
"s_add_u32 m0, s55, 14336\n"
"s_add_u32 s60, s64, 0\n"
"buffer_load_dwordx4 v51, s[44:47], s60 offen lds\n"
"s_add_u32 m0, s55, 15360\n"
"s_add_u32 s60, s64, 32768\n"
"buffer_load_dwordx4 v51, s[44:47], s60 offen lds\n"
"s_waitcnt vmcnt(36)\n"
"ds_read_b64_tr_b16 a[228:229], v44 offset:18432\n"
"ds_read_b64_tr_b16 a[230:231], v44 offset:18688\n"
"ds_read_b64_tr_b16 v[20:21], v45 offset:18432\n"
"ds_read_b64_tr_b16 v[22:23], v45 offset:18688\n"
"s_waitcnt lgkmcnt(4)\n"
"s_add_u32 m0, s55, 16384\n"
"s_add_u32 s60, s65, 0\n"
"buffer_load_dwordx4 v51, s[44:47], s60 offen lds\n"
"s_add_u32 m0, s55, 17408\n"
"s_add_u32 s60, s65, 32768\n"
"buffer_load_dwordx4 v51, s[44:47], s60 offen lds\n"
"s_waitcnt vmcnt(36)\n"
"ds_read_b64_tr_b16 a[8:9], v44 offset:20480\n"
"ds_read_b64_tr_b16 a[10:11], v44 offset:20736\n"
"ds_read_b64_tr_b16 a[36:37], v45 offset:20480\n"
"ds_read_b64_tr_b16 a[38:39], v45 offset:20736\n"
"s_waitcnt lgkmcnt(4)\n"
"s_add_u32 m0, s55, 18432\n"
"s_add_u32 s60, s66, 0\n"
"buffer_load_dwordx4 v51, s[44:47], s60 offen lds\n"
"s_add_u32 m0, s55, 19456\n"
"s_add_u32 s60, s66, 32768\n"
"buffer_load_dwordx4 v51, s[44:47], s60 offen lds\n"
"s_waitcnt vmcnt(36)\n"
"ds_read_b64_tr_b16 a[64:65], v44 offset:22528\n"
"ds_read_b64_tr_b16 a[66:67], v44 offset:22784\n"
"ds_read_b64_tr_b16 a[92:93], v45 offset:22528\n"
"ds_read_b64_tr_b16 a[94:95], v45 offset:22784\n"
"s_waitcnt lgkmcnt(4)\n"
"s_add_u32 m0, s55, 20480\n"
"s_add_u32 s60, s62, 0\n"
"buffer_load_dwordx4 v52, s[44:47], s60 offen lds\n"
"s_add_u32 m0, s55, 21504\n"
"s_add_u32 s60, s62, 32768\n"
"buffer_load_dwordx4 v52, s[44:47], s60 offen lds\n"
"s_waitcnt vmcnt(36)\n"
"ds_read_b64_tr_b16 a[120:121], v44 offset:24576\n"
"ds_read_b64_tr_b16 a[122:123], v44 offset:24832\n"
"ds_read_b64_tr_b16 a[148:149], v45 offset:24576\n"
"ds_read_b64_tr_b16 a[150:151], v45 offset:24832\n"
"s_waitcnt lgkmcnt(4)\n"
"s_add_u32 m0, s55, 22528\n"
"s_add_u32 s60, s63, 0\n"
"buffer_load_dwordx4 v52, s[44:47], s60 offen lds\n"
"s_add_u32 m0, s55, 23552\n"
"s_add_u32 s60, s63, 32768\n"
"buffer_load_dwordx4 v52, s[44:47], s60 offen lds\n"
"s_waitcnt vmcnt(36)\n"
"ds_read_b64_tr_b16 a[176:177], v44 offset:26624\n"
"ds_read_b64_tr_b16 a[178:179], v44 offset:26880\n"
"ds_read_b64_tr_b16 a[204:205], v45 offset:26624\n"
"ds_read_b64_tr_b16 a[206:207], v45 offset:26880\n"
"s_waitcnt lgkmcnt(4)\n"
"s_add_u32 m0, s55, 24576\n"
"s_add_u32 s60, s64, 0\n"
"buffer_load_dwordx4 v52, s[44:47], s60 offen lds\n"
"s_add_u32 m0, s55, 25600\n"
"s_add_u32 s60, s64, 32768\n"
"buffer_load_dwordx4 v52, s[44:47], s60 offen lds\n"
"s_waitcnt vmcnt(36)\n"
"ds_read_b64_tr_b16 a[232:233], v44 offset:28672\n"
"ds_read_b64_tr_b16 a[234:235], v44 offset:28928\n"
"ds_read_b64_tr_b16 v[24:25], v45 offset:28672\n"
"ds_read_b64_tr_b16 v[26:27], v45 offset:28928\n"
"s_waitcnt lgkmcnt(4)\n"
"s_add_u32 m0, s55, 26624\n"
"s_add_u32 s60, s65, 0\n"
"buffer_load_dwordx4 v52, s[44:47], s60 offen lds\n"
"s_add_u32 m0, s55, 27648\n"
"s_add_u32 s60, s65, 32768\n"
"buffer_load_dwordx4 v52, s[44:47], s60 offen lds\n"
"s_waitcnt vmcnt(36)\n"
"ds_read_b64_tr_b16 a[12:13], v44 offset:30720\n"
"ds_read_b64_tr_b16 a[14:15], v44 offset:30976\n"
"ds_read_b64_tr_b16 a[40:41], v45 offset:30720\n"
"ds_read_b64_tr_b16 a[42:43], v45 offset:30976\n"
"s_waitcnt lgkmcnt(4)\n"
"s_add_u32 m0, s55, 28672\n"
"s_add_u32 s60, s66, 0\n"
"buffer_load_dwordx4 v52, s[44:47], s60 offen lds\n"
"s_add_u32 m0, s55, 29696\n"
"s_add_u32 s60, s66, 32768\n"
"buffer_load_dwordx4 v52, s[44:47], s60 offen lds\n"
"s_waitcnt vmcnt(36)\n"
"ds_read_b64_tr_b16 a[68:69], v44 offset:32768\n"
"ds_read_b64_tr_b16 a[70:71], v44 offset:33024\n"
"ds_read_b64_tr_b16 a[96:97], v45 offset:32768\n"
"ds_read_b64_tr_b16 a[98:99], v45 offset:33024\n"
"s_waitcnt lgkmcnt(4)\n"
"s_waitcnt vmcnt(34)\n"
"ds_read_b64_tr_b16 a[124:125], v44 offset:34816\n"
"ds_read_b64_tr_b16 a[126:127], v44 offset:35072\n"
"ds_read_b64_tr_b16 a[152:153], v45 offset:34816\n"
"ds_read_b64_tr_b16 a[154:155], v45 offset:35072\n"
"s_waitcnt lgkmcnt(4)\n"
"s_waitcnt vmcnt(32)\n"
"ds_read_b64_tr_b16 a[180:181], v44 offset:36864\n"
"ds_read_b64_tr_b16 a[182:183], v44 offset:37120\n"
"ds_read_b64_tr_b16 a[208:209], v45 offset:36864\n"
"ds_read_b64_tr_b16 a[210:211], v45 offset:37120\n"
"s_waitcnt lgkmcnt(4)\n"
"s_waitcnt vmcnt(30)\n"
"ds_read_b64_tr_b16 a[236:237], v44 offset:38912\n"
"ds_read_b64_tr_b16 a[238:239], v44 offset:39168\n"
"ds_read_b64_tr_b16 v[28:29], v45 offset:38912\n"
"ds_read_b64_tr_b16 v[30:31], v45 offset:39168\n"
"s_waitcnt lgkmcnt(4)\n"
"s_waitcnt vmcnt(28)\n"
"ds_read_b64_tr_b16 a[16:17], v44 offset:0\n"
"ds_read_b64_tr_b16 a[18:19], v44 offset:256\n"
"ds_read_b64_tr_b16 a[44:45], v45 offset:0\n"
"ds_read_b64_tr_b16 a[46:47], v45 offset:256\n"
"s_waitcnt lgkmcnt(4)\n"
"s_waitcnt vmcnt(26)\n"
"ds_read_b64_tr_b16 a[72:73], v44 offset:2048\n"
"ds_read_b64_tr_b16 a[74:75], v44 offset:2304\n"
"ds_read_b64_tr_b16 a[100:101], v45 offset:2048\n"
"ds_read_b64_tr_b16 a[102:103], v45 offset:2304\n"
"s_waitcnt lgkmcnt(4)\n"
"s_waitcnt vmcnt(24)\n"
"ds_read_b64_tr_b16 a[128:129], v44 offset:4096\n"
"ds_read_b64_tr_b16 a[130:131], v44 offset:4352\n"
"ds_read_b64_tr_b16 a[156:157], v45 offset:4096\n"
"ds_read_b64_tr_b16 a[158:159], v45 offset:4352\n"
"s_waitcnt lgkmcnt(4)\n"
"s_waitcnt vmcnt(22)\n"
"ds_read_b64_tr_b16 a[184:185], v44 offset:6144\n"
"ds_read_b64_tr_b16 a[186:187], v44 offset:6400\n"
"ds_read_b64_tr_b16 a[212:213], v45 offset:6144\n"
"ds_read_b64_tr_b16 a[214:215], v45 offset:6400\n"
"s_waitcnt lgkmcnt(4)\n"
"s_waitcnt vmcnt(20)\n"
"ds_read_b64_tr_b16 a[240:241], v44 offset:8192\n"
"ds_read_b64_tr_b16 a[242:243], v44 offset:8448\n"
"ds_read_b64_tr_b16 v[32:33], v45 offset:8192\n"
"ds_read_b64_tr_b16 v[34:35], v45 offset:8448\n"
"s_waitcnt lgkmcnt(4)\n"
"s_waitcnt vmcnt(18)\n"
"ds_read_b64_tr_b16 a[20:21], v44 offset:10240\n"
"ds_read_b64_tr_b16 a[22:23], v44 offset:10496\n"
"ds_read_b64_tr_b16 a[48:49], v45 offset:10240\n"
"ds_read_b64_tr_b16 a[50:51], v45 offset:10496\n"
"s_waitcnt lgkmcnt(4)\n"
"s_waitcnt vmcnt(16)\n"
"ds_read_b64_tr_b16 a[76:77], v44 offset:12288\n"
"ds_read_b64_tr_b16 a[78:79], v44 offset:12544\n"
"ds_read_b64_tr_b16 a[104:105], v45 offset:12288\n"
"ds_read_b64_tr_b16 a[106:107], v45 offset:12544\n"
"s_waitcnt lgkmcnt(4)\n"
"s_waitcnt vmcnt(14)\n"
"ds_read_b64_tr_b16 a[132:133], v44 offset:14336\n"
"ds_read_b64_tr_b16 a[134:135], v44 offset:14592\n"
"ds_read_b64_tr_b16 a[160:161], v45 offset:14336\n"
"ds_read_b64_tr_b16 a[162:163], v45 offset:14592\n"
"s_waitcnt lgkmcnt(4)\n"
"s_waitcnt vmcnt(12)\n"
"ds_read_b64_tr_b16 a[188:189], v44 offset:16384\n"
"ds_read_b64_tr_b16 a[190:191], v44 offset:16640\n"
"ds_read_b64_tr_b16 a[216:217], v45 offset:16384\n"
"ds_read_b64_tr_b16 a[218:219], v45 offset:16640\n"
"s_waitcnt lgkmcnt(4)\n"
"s_waitcnt vmcnt(10)\n"
"ds_read_b64_tr_b16 a[244:245], v44 offset:18432\n"
"ds_read_b64_tr_b16 a[246:247], v44 offset:18688\n"
"ds_read_b64_tr_b16 v[36:37], v45 offset:18432\n"
"ds_read_b64_tr_b16 v[38:39], v45 offset:18688\n"
"s_waitcnt lgkmcnt(4)\n"
"s_waitcnt vmcnt(8)\n"
"ds_read_b64_tr_b16 a[24:25], v44 offset:20480\n"
"ds_read_b64_tr_b16 a[26:27], v44 offset:20736\n"
"ds_read_b64_tr_b16 a[52:53], v45 offset:20480\n"
"ds_read_b64_tr_b16 a[54:55], v45 offset:20736\n"
"s_waitcnt lgkmcnt(4)\n"
"s_waitcnt vmcnt(6)\n"
"ds_read_b64_tr_b16 a[80:81], v44 offset:22528\n"
"ds_read_b64_tr_b16 a[82:83], v44 offset:22784\n"
"ds_read_b64_tr_b16 a[108:109], v45 offset:22528\n"
"ds_read_b64_tr_b16 a[110:111], v45 offset:22784\n"
"s_waitcnt lgkmcnt(4)\n"
"s_waitcnt vmcnt(4)\n"
"ds_read_b64_tr_b16 a[136:137], v44 offset:24576\n"
"ds_read_b64_tr_b16 a[138:139], v44 offset:24832\n"
"ds_read_b64_tr_b16 a[164:165], v45 offset:24576\n"
"ds_read_b64_tr_b16 a[166:167], v45 offset:24832\n"
"s_waitcnt lgkmcnt(4)\n"
"s_waitcnt vmcnt(2)\n"
"ds_read_b64_tr_b16 a[192:193], v44 offset:26624\n"
"ds_read_b64_tr_b16 a[194:195], v44 offset:26880\n"
"ds_read_b64_tr_b16 a[220:221], v45 offset:26624\n"
"ds_read_b64_tr_b16 a[222:223], v45 offset:26880\n"
"s_waitcnt lgkmcnt(4)\n"
"s_waitcnt vmcnt(0)\n"
"ds_read_b64_tr_b16 a[248:249], v44 offset:28672\n"
"ds_read_b64_tr_b16 a[250:251], v44 offset:28928\n"
"ds_read_b64_tr_b16 v[40:41], v45 offset:28672\n"
"ds_read_b64_tr_b16 v[42:43], v45 offset:28928\n"
"s_waitcnt lgkmcnt(4)\n"
"s_waitcnt vmcnt(0)\n"
"s_waitcnt lgkmcnt(0)\n"
"s_mov_b32 m0, s59\n"
"s_barrier\n"
"s_mov_b32 s93, 0x80000000\n"
"s_mov_b32 s94, 0x80000000\n"
"s_mov_b32 s95, 0x80000000\n"
"s_mov_b32 s94, 0x80000000\n"
"s_mov_b32 s93, 0x80000000\n"
"s_mov_b32 s95, 0x80000000\n"
"s_mov_b32 s95, 0x80000000\n"
"s_mov_b32 s94, 0x80000000\n"
"v_mov_b32 v128, 0\n"
"v_mov_b32 v129, 0\n"
"v_mov_b32 v132, 0\n"
"v_mov_b32 v133, 0\n"
"v_mov_b32 v136, 0\n"
"v_mov_b32 v137, 0\n"
"v_mov_b32 v168, 0\n"
"v_mov_b32 v169, 0\n"
"s_cmp_eq_u32 s54, 0\n"
"s_cbranch_scc1 .LG_1s\n"
"s_cmp_eq_u32 s54, 1\n"
"s_cbranch_scc1 .LG_2s\n"
"s_cmp_eq_u32 s54, 2\n"
"s_cbranch_scc1 .LG_3s\n"
"s_cmp_eq_u32 s54, 3\n"
"s_cbranch_scc1 .LG_4s\n"
".p2align 6\n"
".LG_top:\n"
"v_mfma_f32_16x16x32_bf16 v[56:59], v[100:103], a[0:3], 0\n"
"v_mfma_f32_16x16x32_bf16 v[56:59], v[68:71], a[4:7], v[56:59]\n"
"global_load_dwordx4 v[44:47], v7, s[56:57] offset:0 sc1\n"
"v_mfma_f32_16x16x32_bf16 v[56:59], v[72:75], a[8:11], v[56:59]\n"
"v_mfma_f32_16x16x32_bf16 v[56:59], v[76:79], a[12:15], v[56:59]\n"
"s_waitcnt vmcnt(5)\n"
"v_mfma_f32_16x16x32_bf16 v[56:59], v[80:83], a[16:19], v[56:59]\n"
"s_waitcnt lgkmcnt(3)\n"
"v_add_f32 v120, v120, v64\n"
"v_add_f32 v121, v121, v65\n"
"v_mfma_f32_16x16x32_bf16 v[56:59], v[84:87], a[20:23], v[56:59]\n"
"s_waitcnt vmcnt(4)\n"
"v_add_f32 v122, v122, v66\n"
"v_add_f32 v123, v123, v67\n"
"v_mfma_f32_16x16x32_bf16 v[56:59], v[88:91], a[24:27], v[56:59]\n"
"global_load_dwordx4 v[48:51], v8, s[56:57] offset:0 sc1\n"
"v_add_f32 v120, v120, v92\n"
"v_mfma_f32_16x16x32_bf16 v[60:63], v[100:103], a[28:31], 0\n"
"buffer_load_dwordx2 v[136:137], v12, s[40:43], s50 offen sc1\n"
"v_mfma_f32_16x16x32_bf16 v[60:63], v[68:71], a[32:35], v[60:63]\n"
"ds_write_addtid_b32 v56 offset:0\n"
"v_add_f32 v121, v121, v93\n"
"v_mfma_f32_16x16x32_bf16 v[60:63], v[72:75], a[36:39], v[60:63]\n"
"ds_write_addtid_b32 v57 offset:4640\n"
"global_load_dwordx4 v[52:55], v9, s[56:57] offset:0 sc1\n"
"v_mfma_f32_16x16x32_bf16 v[60:63], v[76:79], a[40:43], v[60:63]\n"
"ds_write_addtid_b32 v58 offset:9344\n"
"v_add_f32 v122, v122, v94\n"
"v_mfma_f32_16x16x32_bf16 v[60:63], v[80:83], a[44:47], v[60:63]\n"
"ds_write_addtid_b32 v59 offset:13984\n"
"v_add_f32 v123, v123, v95\n"
"v_mfma_f32_16x16x32_bf16 v[60:63], v[84:87], a[48:51], v[60:63]\n"
"v_add_f32 v120, v120, v116\n"
"v_add_f32 v121, v121, v117\n"
"v_mfma_f32_16x16x32_bf16 v[60:63], v[88:91], a[52:55], v[60:63]\n"
"v_add_f32 v122, v122, v118\n"
"v_add_f32 v123, v123, v119\n"
"v_mfma_f32_16x16x32_bf16 v[64:67], v[100:103], a[56:59], 0\n"
"s_waitcnt vmcnt(6)\n"
"buffer_store_dwordx2 v[0:1], v14, s[40:43], s93 offen offset:0 sc1\n"
"v_mfma_f32_16x16x32_bf16 v[64:67], v[68:71], a[60:63], v[64:67]\n"
"s_add_u32 s95, s50, 0xffd00000\n"
"s_and_b32 s94, s95, 0xfbffffff\n"
"ds_write_addtid_b32 v60 offset:256\n"
"v_mfma_f32_16x16x32_bf16 v[64:67], v[72:75], a[64:67], v[64:67]\n"
"ds_write_addtid_b32 v61 offset:4896\n"
"global_load_dwordx4 v[56:59], v10, s[56:57] offset:0 sc1\n"
"v_mfma_f32_16x16x32_bf16 v[64:67], v[76:79], a[68:71], v[64:67]\n"
"ds_write_addtid_b32 v62 offset:9600\n"
"v_cvt_pk_bf16_f32 v92, v120, v121\n"
"v_mfma_f32_16x16x32_bf16 v[64:67], v[80:83], a[72:75], v[64:67]\n"
"ds_write_addtid_b32 v63 offset:14240\n"
"v_cvt_pk_bf16_f32 v93, v122, v123\n"
"v_mfma_f32_16x16x32_bf16 v[64:67], v[84:87], a[76:79], v[64:67]\n"
"s_waitcnt lgkmcnt(8)\n"
"v_add_f32 v124, v124, v128\n"
"v_add_f32 v125, v125, v129\n"
"v_mfma_f32_16x16x32_bf16 v[64:67], v[88:91], a[80:83], v[64:67]\n"
"v_add_f32 v126, v126, v130\n"
"v_add_f32 v127, v127, v131\n"
"v_mfma_f32_16x16x32_bf16 v[96:99], v[100:103], a[84:87], 0\n"
"global_load_dwordx4 v[60:63], v11, s[56:57] offset:0 sc1\n"
"v_add_f32 v124, v124, v132\n"
"v_mfma_f32_16x16x32_bf16 v[96:99], v[68:71], a[88:91], v[96:99]\n"
"ds_write_addtid_b32 v64 offset:512\n"
"v_add_f32 v125, v125, v133\n"
"v_mfma_f32_16x16x32_bf16 v[96:99], v[72:75], a[92:95], v[96:99]\n"
"ds_write_addtid_b32 v65 offset:5152\n"
"v_add_f32 v126, v126, v134\n"
"v_mfma_f32_16x16x32_bf16 v[96:99], v[76:79], a[96:99], v[96:99]\n"
"ds_write_addtid_b32 v66 offset:9856\n"
"v_add_f32 v127, v127, v135\n"
"v_mfma_f32_16x16x32_bf16 v[96:99], v[80:83], a[100:103], v[96:99]\n"
"ds_write_addtid_b32 v67 offset:14496\n"
"v_add_f32 v124, v124, v112\n"
"v_mfma_f32_16x16x32_bf16 v[96:99], v[84:87], a[104:107], v[96:99]\n"
"v_add_f32 v125, v125, v113\n"
"v_add_f32 v126, v126, v114\n"
"v_mfma_f32_16x16x32_bf16 v[96:99], v[88:91], a[108:111], v[96:99]\n"
"v_add_f32 v127, v127, v115\n"
"v_cvt_pk_bf16_f32 v94, v124, v125\n"
"v_mfma_f32_16x16x32_bf16 v[64:67], v[100:103], a[112:115], 0\n"
"v_cvt_pk_bf16_f32 v95, v126, v127\n"
"v_mfma_f32_16x16x32_bf16 v[64:67], v[68:71], a[116:119], v[64:67]\n"
"ds_write_addtid_b32 v96 offset:768\n"
"v_mfma_f32_16x16x32_bf16 v[64:67], v[72:75], a[120:123], v[64:67]\n"
"ds_write_addtid_b32 v97 offset:5408\n"
"v_mfma_f32_16x16x32_bf16 v[64:67], v[76:79], a[124:127], v[64:67]\n"
"ds_write_addtid_b32 v98 offset:10112\n"
"v_mfma_f32_16x16x32_bf16 v[64:67], v[80:83], a[128:131], v[64:67]\n"
"ds_write_addtid_b32 v99 offset:14752\n"
"v_mfma_f32_16x16x32_bf16 v[64:67], v[84:87], a[132:135], v[64:67]\n"
"v_mfma_f32_16x16x32_bf16 v[64:67], v[88:91], a[136:139], v[64:67]\n"
"v_mfma_f32_16x16x32_bf16 v[112:115], v[100:103], a[140:143], 0\n"
"v_mfma_f32_16x16x32_bf16 v[112:115], v[68:71], a[144:147], v[112:115]\n"
"ds_write_addtid_b32 v64 offset:1024\n"
"v_mfma_f32_16x16x32_bf16 v[112:115], v[72:75], a[148:151], v[112:115]\n"
"ds_write_addtid_b32 v65 offset:5664\n"
"v_mfma_f32_16x16x32_bf16 v[112:115], v[76:79], a[152:155], v[112:115]\n"
"ds_write_addtid_b32 v66 offset:10368\n"
"v_mfma_f32_16x16x32_bf16 v[112:115], v[80:83], a[156:159], v[112:115]\n"
"ds_write_addtid_b32 v67 offset:15008\n"
"v_mfma_f32_16x16x32_bf16 v[112:115], v[84:87], a[160:163], v[112:115]\n"
"buffer_store_dwordx4 v[92:95], v13, s[40:43], s94 offen sc1\n"
"v_mfma_f32_16x16x32_bf16 v[112:115], v[88:91], a[164:167], v[112:115]\n"
"v_mfma_f32_16x16x32_bf16 v[96:99], a[168:171], v[100:103], 0\n"
"v_mfma_f32_16x16x32_bf16 v[96:99], a[172:175], v[68:71], v[96:99]\n"
"ds_write_addtid_b32 v112 offset:1280\n"
"v_mfma_f32_16x16x32_bf16 v[96:99], a[176:179], v[72:75], v[96:99]\n"
"ds_write_addtid_b32 v113 offset:5920\n"
"v_mfma_f32_16x16x32_bf16 v[96:99], a[180:183], v[76:79], v[96:99]\n"
"ds_write_addtid_b32 v114 offset:10624\n"
"v_mfma_f32_16x16x32_bf16 v[96:99], a[184:187], v[80:83], v[96:99]\n"
"ds_write_addtid_b32 v115 offset:15264\n"
"v_mfma_f32_16x16x32_bf16 v[96:99], a[188:191], v[84:87], v[96:99]\n"
"v_mfma_f32_16x16x32_bf16 v[96:99], a[192:195], v[88:91], v[96:99]\n"
"v_mfma_f32_16x16x32_bf16 v[92:95], a[196:199], v[100:103], 0\n"
"v_mfma_f32_16x16x32_bf16 v[92:95], a[200:203], v[68:71], v[92:95]\n"
"v_mfma_f32_16x16x32_bf16 v[92:95], a[204:207], v[72:75], v[92:95]\n"
"v_mfma_f32_16x16x32_bf16 v[92:95], a[208:211], v[76:79], v[92:95]\n"
"s_add_u32 s58, s50, 0x180000\n"
"v_mfma_f32_16x16x32_bf16 v[92:95], a[212:215], v[80:83], v[92:95]\n"
"s_and_b32 s58, s58, 0x3ffffff\n"
"v_mfma_f32_16x16x32_bf16 v[92:95], a[216:219], v[84:87], v[92:95]\n"
"s_add_u32 s56, s40, s58\n"
"v_mfma_f32_16x16x32_bf16 v[92:95], a[220:223], v[88:91], v[92:95]\n"
"s_addc_u32 s57, s41, 0\n"
"v_mfma_f32_16x16x32_bf16 v[112:115], a[224:227], v[100:103], 0\n"
"s_waitcnt vmcnt(5)\n"
"v_cmp_ne_u64 vcc, s[48:49], v[136:137]\n"
"v_mfma_f32_16x16x32_bf16 v[112:115], a[228:231], v[68:71], v[112:115]\n"
"v_mfma_f32_16x16x32_bf16 v[112:115], a[232:235], v[72:75], v[112:115]\n"
"s_cbranch_vccnz .LG_3s\n"
".LG_3r:\n"
"v_mfma_f32_16x16x32_bf16 v[112:115], a[236:239], v[76:79], v[112:115]\n"
"s_add_u32 s50, s50, 0x180000\n"
"global_load_dwordx4 v[64:67], v5, s[56:57] offset:0 sc1\n"
"v_mfma_f32_16x16x32_bf16 v[112:115], a[240:243], v[80:83], v[112:115]\n"
"v_mfma_f32_16x16x32_bf16 v[112:115], a[244:247], v[84:87], v[112:115]\n"
"v_mfma_f32_16x16x32_bf16 v[112:115], a[248:251], v[88:91], v[112:115]\n"
"v_mfma_f32_16x16x32_bf16 v[116:119], a[252:255], v[100:103], 0\n"
"v_mfma_f32_16x16x32_bf16 v[116:119], v[20:23], v[68:71], v[116:119]\n"
"s_waitcnt lgkmcnt(0)\n"
"v_mfma_f32_16x16x32_bf16 v[116:119], v[24:27], v[72:75], v[116:119]\n"
"s_barrier\n"
"v_mfma_f32_16x16x32_bf16 v[116:119], v[28:31], v[76:79], v[116:119]\n"
"ds_read_b128 v[100:103], v2 offset:2048\n"
"ds_read_b128 v[120:123], v3 offset:512\n"
"v_mfma_f32_16x16x32_bf16 v[116:119], v[32:35], v[80:83], v[116:119]\n"
"global_load_dwordx4 v[68:71], v6, s[56:57] offset:0 sc1\n"
"ds_read_b128 v[124:127], v4 offset:3072\n"
"v_mfma_f32_16x16x32_bf16 v[116:119], v[36:39], v[84:87], v[116:119]\n"
"ds_read_b128 v[128:131], v2 offset:2304\n"
"ds_read_b128 v[132:135], v3 offset:768\n"
"v_mfma_f32_16x16x32_bf16 v[116:119], v[40:43], v[88:91], v[116:119]\n"
"global_load_dwordx4 v[72:75], v7, s[56:57] offset:0 sc1\n"
"ds_read_b128 v[136:139], v4 offset:3328\n"
"v_mfma_f32_16x16x32_bf16 v[84:87], v[104:107], a[0:3], 0\n"
"ds_read_b128 v[140:143], v2 offset:3584\n"
"ds_read_b128 v[144:147], v3 offset:1536\n"
"v_mfma_f32_16x16x32_bf16 v[84:87], v[108:111], a[4:7], v[84:87]\n"
"ds_read_b128 v[148:151], v4 offset:3584\n"
"ds_read_b128 v[152:155], v2 offset:3840\n"
"v_mfma_f32_16x16x32_bf16 v[84:87], v[44:47], a[8:11], v[84:87]\n"
"ds_read_b128 v[156:159], v3 offset:1792\n"
"ds_read_b128 v[160:163], v4 offset:3840\n"
"v_mfma_f32_16x16x32_bf16 v[84:87], v[48:51], a[12:15], v[84:87]\n"
"s_waitcnt vmcnt(5)\n"
"v_mfma_f32_16x16x32_bf16 v[84:87], v[52:55], a[16:19], v[84:87]\n"
"s_waitcnt lgkmcnt(9)\n"
"global_load_dwordx4 v[76:79], v8, s[56:57] offset:0 sc1\n"
"v_add_f32 v100, v100, v120\n"
"v_mfma_f32_16x16x32_bf16 v[84:87], v[56:59], a[20:23], v[84:87]\n"
"s_waitcnt vmcnt(5)\n"
"v_add_f32 v101, v101, v121\n"
"v_add_f32 v102, v102, v122\n"
"v_mfma_f32_16x16x32_bf16 v[84:87], v[60:63], a[24:27], v[84:87]\n"
"v_add_f32 v103, v103, v123\n"
"v_add_f32 v100, v100, v124\n"
"v_mfma_f32_16x16x32_bf16 v[164:167], v[104:107], a[28:31], 0\n"
"buffer_load_dwordx2 v[168:169], v12, s[40:43], s50 offen sc1\n"
"v_mfma_f32_16x16x32_bf16 v[164:167], v[108:111], a[32:35], v[164:167]\n"
"ds_write_addtid_b32 v84 offset:2048\n"
"v_add_f32 v101, v101, v125\n"
"v_mfma_f32_16x16x32_bf16 v[164:167], v[44:47], a[36:39], v[164:167]\n"
"ds_write_addtid_b32 v85 offset:6688\n"
"v_add_f32 v102, v102, v126\n"
"v_mfma_f32_16x16x32_bf16 v[164:167], v[48:51], a[40:43], v[164:167]\n"
"ds_write_addtid_b32 v86 offset:11392\n"
"v_add_f32 v103, v103, v127\n"
"v_mfma_f32_16x16x32_bf16 v[164:167], v[52:55], a[44:47], v[164:167]\n"
"ds_write_addtid_b32 v87 offset:16032\n"
"v_add_f32 v100, v100, v96\n"
"v_mfma_f32_16x16x32_bf16 v[164:167], v[56:59], a[48:51], v[164:167]\n"
"global_load_dwordx4 v[80:83], v9, s[56:57] offset:0 sc1\n"
"v_add_f32 v101, v101, v97\n"
"v_mfma_f32_16x16x32_bf16 v[164:167], v[60:63], a[52:55], v[164:167]\n"
"v_add_f32 v102, v102, v98\n"
"v_add_f32 v103, v103, v99\n"
"v_mfma_f32_16x16x32_bf16 v[120:123], v[104:107], a[56:59], 0\n"
"s_add_u32 s93, s50, 0xffd00000\n"
"s_and_b32 s94, s93, 0xfbffffff\n"
"v_cvt_pk_bf16_f32 v124, v100, v101\n"
"v_mfma_f32_16x16x32_bf16 v[120:123], v[108:111], a[60:63], v[120:123]\n"
"ds_write_addtid_b32 v164 offset:2304\n"
"v_cvt_pk_bf16_f32 v125, v102, v103\n"
"v_mfma_f32_16x16x32_bf16 v[120:123], v[44:47], a[64:67], v[120:123]\n"
"s_waitcnt lgkmcnt(11)\n"
"ds_write_addtid_b32 v165 offset:6944\n"
"v_add_f32 v128, v128, v132\n"
"v_mfma_f32_16x16x32_bf16 v[120:123], v[48:51], a[68:71], v[120:123]\n"
"ds_write_addtid_b32 v166 offset:11648\n"
"v_add_f32 v129, v129, v133\n"
"v_mfma_f32_16x16x32_bf16 v[120:123], v[52:55], a[72:75], v[120:123]\n"
"s_waitcnt vmcnt(6)\n"
"ds_write_addtid_b32 v167 offset:16288\n"
"global_load_dwordx4 v[84:87], v10, s[56:57] offset:0 sc1\n"
"v_mfma_f32_16x16x32_bf16 v[120:123], v[56:59], a[76:79], v[120:123]\n"
"global_load_dwordx4 v[88:91], v11, s[56:57] offset:0 sc1\n"
"v_add_f32 v130, v130, v134\n"
"v_mfma_f32_16x16x32_bf16 v[120:123], v[60:63], a[80:83], v[120:123]\n"
"v_add_f32 v131, v131, v135\n"
"v_add_f32 v128, v128, v136\n"
"v_mfma_f32_16x16x32_bf16 v[96:99], v[104:107], a[84:87], 0\n"
"v_add_f32 v129, v129, v137\n"
"v_add_f32 v130, v130, v138\n"
"v_mfma_f32_16x16x32_bf16 v[96:99], v[108:111], a[88:91], v[96:99]\n"
"ds_write_addtid_b32 v120 offset:2560\n"
"v_add_f32 v131, v131, v139\n"
"v_mfma_f32_16x16x32_bf16 v[96:99], v[44:47], a[92:95], v[96:99]\n"
"ds_write_addtid_b32 v121 offset:7200\n"
"v_add_f32 v128, v128, v92\n"
"v_mfma_f32_16x16x32_bf16 v[96:99], v[48:51], a[96:99], v[96:99]\n"
"ds_write_addtid_b32 v122 offset:11904\n"
"v_add_f32 v129, v129, v93\n"
"v_mfma_f32_16x16x32_bf16 v[96:99], v[52:55], a[100:103], v[96:99]\n"
"ds_write_addtid_b32 v123 offset:16544\n"
"v_add_f32 v130, v130, v94\n"
"v_mfma_f32_16x16x32_bf16 v[96:99], v[56:59], a[104:107], v[96:99]\n"
"v_add_f32 v131, v131, v95\n"
"v_cvt_pk_bf16_f32 v126, v128, v129\n"
"v_mfma_f32_16x16x32_bf16 v[96:99], v[60:63], a[108:111], v[96:99]\n"
"buffer_store_dwordx2 v[0:1], v14, s[40:43], s95 offen offset:0 sc1\n"
"v_cvt_pk_bf16_f32 v127, v130, v131\n"
"v_mfma_f32_16x16x32_bf16 v[92:95], v[104:107], a[112:115], 0\n"
"buffer_store_dwordx4 v[124:127], v13, s[40:43], s94 offen sc1\n"
"v_mfma_f32_16x16x32_bf16 v[92:95], v[108:111], a[116:119], v[92:95]\n"
"s_waitcnt lgkmcnt(15)\n"
"ds_write_addtid_b32 v96 offset:2816\n"
"v_add_f32 v140, v140, v144\n"
"v_mfma_f32_16x16x32_bf16 v[92:95], v[44:47], a[120:123], v[92:95]\n"
"ds_write_addtid_b32 v97 offset:7456\n"
"v_add_f32 v141, v141, v145\n"
"v_mfma_f32_16x16x32_bf16 v[92:95], v[48:51], a[124:127], v[92:95]\n"
"ds_write_addtid_b32 v98 offset:12160\n"
"v_add_f32 v142, v142, v146\n"
"v_mfma_f32_16x16x32_bf16 v[92:95], v[52:55], a[128:131], v[92:95]\n"
"ds_write_addtid_b32 v99 offset:16800\n"
"v_add_f32 v143, v143, v147\n"
"v_mfma_f32_16x16x32_bf16 v[92:95], v[56:59], a[132:135], v[92:95]\n"
"v_add_f32 v140, v140, v148\n"
"v_add_f32 v141, v141, v149\n"
"v_mfma_f32_16x16x32_bf16 v[92:95], v[60:63], a[136:139], v[92:95]\n"
"v_add_f32 v142, v142, v150\n"
"v_add_f32 v143, v143, v151\n"
"v_mfma_f32_16x16x32_bf16 v[96:99], v[104:107], a[140:143], 0\n"
"v_add_f32 v140, v140, v112\n"
"v_add_f32 v141, v141, v113\n"
"v_mfma_f32_16x16x32_bf16 v[96:99], v[108:111], a[144:147], v[96:99]\n"
"ds_write_addtid_b32 v92 offset:3072\n"
"v_add_f32 v142, v142, v114\n"
"v_mfma_f32_16x16x32_bf16 v[96:99], v[44:47], a[148:151], v[96:99]\n"
"ds_write_addtid_b32 v93 offset:7712\n"
"v_add_f32 v143, v143, v115\n"
"v_mfma_f32_16x16x32_bf16 v[96:99], v[48:51], a[152:155], v[96:99]\n"
"ds_write_addtid_b32 v94 offset:12416\n"
"v_cvt_pk_bf16_f32 v100, v140, v141\n"
"v_mfma_f32_16x16x32_bf16 v[96:99], v[52:55], a[156:159], v[96:99]\n"
"ds_write_addtid_b32 v95 offset:17056\n"
"v_cvt_pk_bf16_f32 v101, v142, v143\n"
"v_mfma_f32_16x16x32_bf16 v[96:99], v[56:59], a[160:163], v[96:99]\n"
"s_waitcnt lgkmcnt(15)\n"
"v_add_f32 v152, v152, v156\n"
"v_add_f32 v153, v153, v157\n"
"v_mfma_f32_16x16x32_bf16 v[96:99], v[60:63], a[164:167], v[96:99]\n"
"v_add_f32 v154, v154, v158\n"
"v_add_f32 v155, v155, v159\n"
"v_mfma_f32_16x16x32_bf16 v[92:95], v[104:107], a[224:227], 0\n"
"v_add_f32 v152, v152, v160\n"
"v_add_f32 v153, v153, v161\n"
"v_mfma_f32_16x16x32_bf16 v[92:95], v[108:111], a[228:231], v[92:95]\n"
"ds_write_addtid_b32 v96 offset:3328\n"
"v_add_f32 v154, v154, v162\n"
"v_mfma_f32_16x16x32_bf16 v[92:95], v[44:47], a[232:235], v[92:95]\n"
"ds_write_addtid_b32 v97 offset:7968\n"
"v_add_f32 v155, v155, v163\n"
"v_mfma_f32_16x16x32_bf16 v[92:95], v[48:51], a[236:239], v[92:95]\n"
"ds_write_addtid_b32 v98 offset:12672\n"
"v_add_f32 v152, v152, v116\n"
"v_mfma_f32_16x16x32_bf16 v[92:95], v[52:55], a[240:243], v[92:95]\n"
"ds_write_addtid_b32 v99 offset:17312\n"
"v_add_f32 v153, v153, v117\n"
"v_mfma_f32_16x16x32_bf16 v[92:95], v[56:59], a[244:247], v[92:95]\n"
"v_add_f32 v154, v154, v118\n"
"v_add_f32 v155, v155, v119\n"
"v_mfma_f32_16x16x32_bf16 v[92:95], v[60:63], a[248:251], v[92:95]\n"
"v_cvt_pk_bf16_f32 v102, v152, v153\n"
"v_cvt_pk_bf16_f32 v103, v154, v155\n"
"v_mfma_f32_16x16x32_bf16 v[96:99], v[104:107], a[252:255], 0\n"
"buffer_store_dwordx4 v[100:103], v15, s[40:43], s94 offen sc1\n"
"v_mfma_f32_16x16x32_bf16 v[96:99], v[108:111], v[20:23], v[96:99]\n"
"ds_write_addtid_b32 v92 offset:3584\n"
"v_mfma_f32_16x16x32_bf16 v[96:99], v[44:47], v[24:27], v[96:99]\n"
"ds_write_addtid_b32 v93 offset:8224\n"
"v_mfma_f32_16x16x32_bf16 v[96:99], v[48:51], v[28:31], v[96:99]\n"
"ds_write_addtid_b32 v94 offset:12928\n"
"v_mfma_f32_16x16x32_bf16 v[96:99], v[52:55], v[32:35], v[96:99]\n"
"ds_write_addtid_b32 v95 offset:17568\n"
"v_mfma_f32_16x16x32_bf16 v[96:99], v[56:59], v[36:39], v[96:99]\n"
"v_mfma_f32_16x16x32_bf16 v[96:99], v[60:63], v[40:43], v[96:99]\n"
"s_add_u32 s58, s50, 0x180000\n"
"v_mfma_f32_16x16x32_bf16 v[100:103], a[168:171], v[104:107], 0\n"
"s_and_b32 s58, s58, 0x3ffffff\n"
"v_mfma_f32_16x16x32_bf16 v[100:103], a[172:175], v[108:111], v[100:103]\n"
"s_add_u32 s56, s40, s58\n"
"ds_write_addtid_b32 v96 offset:3840\n"
"v_mfma_f32_16x16x32_bf16 v[100:103], a[176:179], v[44:47], v[100:103]\n"
"s_addc_u32 s57, s41, 0\n"
"ds_write_addtid_b32 v97 offset:8480\n"
"v_mfma_f32_16x16x32_bf16 v[100:103], a[180:183], v[48:51], v[100:103]\n"
"s_waitcnt vmcnt(6)\n"
"ds_write_addtid_b32 v98 offset:13184\n"
"v_cmp_ne_u64 vcc, s[48:49], v[168:169]\n"
"v_mfma_f32_16x16x32_bf16 v[100:103], a[184:187], v[52:55], v[100:103]\n"
"ds_write_addtid_b32 v99 offset:17824\n"
"v_mfma_f32_16x16x32_bf16 v[100:103], a[188:191], v[56:59], v[100:103]\n"
"s_cbranch_vccnz .LG_4s\n"
".LG_4r:\n"
"v_mfma_f32_16x16x32_bf16 v[100:103], a[192:195], v[60:63], v[100:103]\n"
"s_add_u32 s50, s50, 0x180000\n"
"global_load_dwordx4 v[92:95], v5, s[56:57] offset:0 sc1\n"
"v_mfma_f32_16x16x32_bf16 v[112:115], a[196:199], v[104:107], 0\n"
"v_mfma_f32_16x16x32_bf16 v[112:115], a[200:203], v[108:111], v[112:115]\n"
"s_waitcnt lgkmcnt(0)\n"
"v_mfma_f32_16x16x32_bf16 v[112:115], a[204:207], v[44:47], v[112:115]\n"
"s_barrier\n"
"v_mfma_f32_16x16x32_bf16 v[112:115], a[208:211], v[48:51], v[112:115]\n"
"ds_read_b128 v[96:99], v2 offset:0\n"
"ds_read_b128 v[104:107], v3 offset:2560\n"
"v_mfma_f32_16x16x32_bf16 v[112:115], a[212:215], v[52:55], v[112:115]\n"
"ds_read_b128 v[108:111], v4 offset:1024\n"
"ds_read_b128 v[116:119], v2 offset:256\n"
"v_mfma_f32_16x16x32_bf16 v[112:115], a[216:219], v[56:59], v[112:115]\n"
"global_load_dwordx4 v[44:47], v6, s[56:57] offset:0 sc1\n"
"ds_read_b128 v[120:123], v3 offset:2816\n"
"v_mfma_f32_16x16x32_bf16 v[112:115], a[220:223], v[60:63], v[112:115]\n"
"ds_read_b128 v[124:127], v4 offset:1280\n"
"v_mfma_f32_16x16x32_bf16 v[56:59], v[64:67], a[0:3], 0\n"
"v_mfma_f32_16x16x32_bf16 v[56:59], v[68:71], a[4:7], v[56:59]\n"
"v_mfma_f32_16x16x32_bf16 v[56:59], v[72:75], a[8:11], v[56:59]\n"
"global_load_dwordx4 v[48:51], v7, s[56:57] offset:0 sc1\n"
"v_mfma_f32_16x16x32_bf16 v[56:59], v[76:79], a[12:15], v[56:59]\n"
"s_waitcnt vmcnt(7)\n"
"buffer_load_dwordx2 v[128:129], v12, s[40:43], s50 offen sc1\n"
"v_mfma_f32_16x16x32_bf16 v[56:59], v[80:83], a[16:19], v[56:59]\n"
"s_waitcnt lgkmcnt(3)\n"
"v_add_f32 v96, v96, v104\n"
"v_add_f32 v97, v97, v105\n"
"v_mfma_f32_16x16x32_bf16 v[56:59], v[84:87], a[20:23], v[56:59]\n"
"s_waitcnt vmcnt(7)\n"
"v_add_f32 v98, v98, v106\n"
"v_add_f32 v99, v99, v107\n"
"v_mfma_f32_16x16x32_bf16 v[56:59], v[88:91], a[24:27], v[56:59]\n"
"v_add_f32 v96, v96, v108\n"
"v_add_f32 v97, v97, v109\n"
"v_mfma_f32_16x16x32_bf16 v[104:107], v[64:67], a[28:31], 0\n"
"global_load_dwordx4 v[52:55], v8, s[56:57] offset:0 sc1\n"
"v_add_f32 v98, v98, v110\n"
"v_mfma_f32_16x16x32_bf16 v[104:107], v[68:71], a[32:35], v[104:107]\n"
"ds_write_addtid_b32 v56 offset:0\n"
"v_add_f32 v99, v99, v111\n"
"v_mfma_f32_16x16x32_bf16 v[104:107], v[72:75], a[36:39], v[104:107]\n"
"ds_write_addtid_b32 v57 offset:4640\n"
"v_add_f32 v96, v96, v100\n"
"v_mfma_f32_16x16x32_bf16 v[104:107], v[76:79], a[40:43], v[104:107]\n"
"ds_write_addtid_b32 v58 offset:9344\n"
"v_add_f32 v97, v97, v101\n"
"v_mfma_f32_16x16x32_bf16 v[104:107], v[80:83], a[44:47], v[104:107]\n"
"ds_write_addtid_b32 v59 offset:13984\n"
"v_add_f32 v98, v98, v102\n"
"v_mfma_f32_16x16x32_bf16 v[104:107], v[84:87], a[48:51], v[104:107]\n"
"v_add_f32 v99, v99, v103\n"
"v_mfma_f32_16x16x32_bf16 v[104:107], v[88:91], a[52:55], v[104:107]\n"
"s_add_u32 s95, s50, 0xffd00000\n"
"s_and_b32 s94, s95, 0xfbffffff\n"
"global_load_dwordx4 v[56:59], v9, s[56:57] offset:0 sc1\n"
"v_mfma_f32_16x16x32_bf16 v[100:103], v[64:67], a[56:59], 0\n"
"v_cvt_pk_bf16_f32 v108, v96, v97\n"
"v_cvt_pk_bf16_f32 v109, v98, v99\n"
"v_mfma_f32_16x16x32_bf16 v[100:103], v[68:71], a[60:63], v[100:103]\n"
"s_waitcnt lgkmcnt(4)\n"
"ds_write_addtid_b32 v104 offset:256\n"
"v_add_f32 v116, v116, v120\n"
"v_mfma_f32_16x16x32_bf16 v[100:103], v[72:75], a[64:67], v[100:103]\n"
"ds_write_addtid_b32 v105 offset:4896\n"
"v_add_f32 v117, v117, v121\n"
"v_mfma_f32_16x16x32_bf16 v[100:103], v[76:79], a[68:71], v[100:103]\n"
"ds_write_addtid_b32 v106 offset:9600\n"
"global_load_dwordx4 v[60:63], v10, s[56:57] offset:0 sc1\n"
"v_mfma_f32_16x16x32_bf16 v[100:103], v[80:83], a[72:75], v[100:103]\n"
"ds_write_addtid_b32 v107 offset:14240\n"
"v_add_f32 v118, v118, v122\n"
"v_mfma_f32_16x16x32_bf16 v[100:103], v[84:87], a[76:79], v[100:103]\n"
"v_add_f32 v119, v119, v123\n"
"v_add_f32 v116, v116, v124\n"
"v_mfma_f32_16x16x32_bf16 v[100:103], v[88:91], a[80:83], v[100:103]\n"
"v_add_f32 v117, v117, v125\n"
"v_add_f32 v118, v118, v126\n"
"v_mfma_f32_16x16x32_bf16 v[104:107], v[64:67], a[84:87], 0\n"
"v_add_f32 v119, v119, v127\n"
"v_add_f32 v116, v116, v112\n"
"v_mfma_f32_16x16x32_bf16 v[104:107], v[68:71], a[88:91], v[104:107]\n"
"ds_write_addtid_b32 v100 offset:512\n"
"v_add_f32 v117, v117, v113\n"
"v_mfma_f32_16x16x32_bf16 v[104:107], v[72:75], a[92:95], v[104:107]\n"
"ds_write_addtid_b32 v101 offset:5152\n"
"global_load_dwordx4 v[96:99], v11, s[56:57] offset:0 sc1\n"
"v_mfma_f32_16x16x32_bf16 v[104:107], v[76:79], a[96:99], v[104:107]\n"
"ds_write_addtid_b32 v102 offset:9856\n"
"v_add_f32 v118, v118, v114\n"
"v_mfma_f32_16x16x32_bf16 v[104:107], v[80:83], a[100:103], v[104:107]\n"
"ds_write_addtid_b32 v103 offset:14496\n"
"v_add_f32 v119, v119, v115\n"
"v_mfma_f32_16x16x32_bf16 v[104:107], v[84:87], a[104:107], v[104:107]\n"
"v_cvt_pk_bf16_f32 v110, v116, v117\n"
"v_cvt_pk_bf16_f32 v111, v118, v119\n"
"v_mfma_f32_16x16x32_bf16 v[104:107], v[88:91], a[108:111], v[104:107]\n"
"s_waitcnt vmcnt(8)\n"
"buffer_store_dwordx2 v[0:1], v16, s[40:43], s93 offen offset:0 sc1\n"
"v_mfma_f32_16x16x32_bf16 v[100:103], v[64:67], a[112:115], 0\n"
"v_mfma_f32_16x16x32_bf16 v[100:103], v[68:71], a[116:119], v[100:103]\n"
"ds_write_addtid_b32 v104 offset:768\n"
"v_mfma_f32_16x16x32_bf16 v[100:103], v[72:75], a[120:123], v[100:103]\n"
"buffer_store_dwordx4 v[108:111], v13, s[40:43], s94 offen sc1\n"
"ds_write_addtid_b32 v105 offset:5408\n"
"v_mfma_f32_16x16x32_bf16 v[100:103], v[76:79], a[124:127], v[100:103]\n"
"ds_write_addtid_b32 v106 offset:10112\n"
"v_mfma_f32_16x16x32_bf16 v[100:103], v[80:83], a[128:131], v[100:103]\n"
"ds_write_addtid_b32 v107 offset:14752\n"
"v_mfma_f32_16x16x32_bf16 v[100:103], v[84:87], a[132:135], v[100:103]\n"
"v_mfma_f32_16x16x32_bf16 v[100:103], v[88:91], a[136:139], v[100:103]\n"
"v_mfma_f32_16x16x32_bf16 v[108:111], v[64:67], a[140:143], 0\n"
"v_mfma_f32_16x16x32_bf16 v[108:111], v[68:71], a[144:147], v[108:111]\n"
"ds_write_addtid_b32 v100 offset:1024\n"
"v_mfma_f32_16x16x32_bf16 v[108:111], v[72:75], a[148:151], v[108:111]\n"
"ds_write_addtid_b32 v101 offset:5664\n"
"v_mfma_f32_16x16x32_bf16 v[108:111], v[76:79], a[152:155], v[108:111]\n"
"ds_write_addtid_b32 v102 offset:10368\n"
"v_mfma_f32_16x16x32_bf16 v[108:111], v[80:83], a[156:159], v[108:111]\n"
"ds_write_addtid_b32 v103 offset:15008\n"
"v_mfma_f32_16x16x32_bf16 v[108:111], v[84:87], a[160:163], v[108:111]\n"
"v_mfma_f32_16x16x32_bf16 v[108:111], v[88:91], a[164:167], v[108:111]\n"
"v_mfma_f32_16x16x32_bf16 v[104:107], v[64:67], a[224:227], 0\n"
"v_mfma_f32_16x16x32_bf16 v[104:107], v[68:71], a[228:231], v[104:107]\n"
"ds_write_addtid_b32 v108 offset:1280\n"
"v_mfma_f32_16x16x32_bf16 v[104:107], v[72:75], a[232:235], v[104:107]\n"
"ds_write_addtid_b32 v109 offset:5920\n"
"v_mfma_f32_16x16x32_bf16 v[104:107], v[76:79], a[236:239], v[104:107]\n"
"ds_write_addtid_b32 v110 offset:10624\n"
"v_mfma_f32_16x16x32_bf16 v[104:107], v[80:83], a[240:243], v[104:107]\n"
"ds_write_addtid_b32 v111 offset:15264\n"
"v_mfma_f32_16x16x32_bf16 v[104:107], v[84:87], a[244:247], v[104:107]\n"
"v_mfma_f32_16x16x32_bf16 v[104:107], v[88:91], a[248:251], v[104:107]\n"
"v_mfma_f32_16x16x32_bf16 v[112:115], v[64:67], a[252:255], 0\n"
"v_mfma_f32_16x16x32_bf16 v[112:115], v[68:71], v[20:23], v[112:115]\n"
"s_add_u32 s58, s50, 0x180000\n"
"ds_write_addtid_b32 v104 offset:1536\n"
"v_mfma_f32_16x16x32_bf16 v[112:115], v[72:75], v[24:27], v[112:115]\n"
"s_and_b32 s58, s58, 0x3ffffff\n"
"ds_write_addtid_b32 v105 offset:6176\n"
"v_mfma_f32_16x16x32_bf16 v[112:115], v[76:79], v[28:31], v[112:115]\n"
"s_add_u32 s56, s40, s58\n"
"ds_write_addtid_b32 v106 offset:10880\n"
"v_mfma_f32_16x16x32_bf16 v[112:115], v[80:83], v[32:35], v[112:115]\n"
"s_addc_u32 s57, s41, 0\n"
"ds_write_addtid_b32 v107 offset:15520\n"
"v_mfma_f32_16x16x32_bf16 v[112:115], v[84:87], v[36:39], v[112:115]\n"
"s_waitcnt vmcnt(6)\n"
"v_cmp_ne_u64 vcc, s[48:49], v[128:129]\n"
"v_mfma_f32_16x16x32_bf16 v[112:115], v[88:91], v[40:43], v[112:115]\n"
"v_mfma_f32_16x16x32_bf16 v[104:107], a[168:171], v[64:67], 0\n"
"s_cbranch_vccnz .LG_1s\n"
".LG_1r:\n"
"v_mfma_f32_16x16x32_bf16 v[104:107], a[172:175], v[68:71], v[104:107]\n"
"s_add_u32 s50, s50, 0x180000\n"
"ds_write_addtid_b32 v112 offset:1792\n"
"v_mfma_f32_16x16x32_bf16 v[104:107], a[176:179], v[72:75], v[104:107]\n"
"ds_write_addtid_b32 v113 offset:6432\n"
"v_mfma_f32_16x16x32_bf16 v[104:107], a[180:183], v[76:79], v[104:107]\n"
"ds_write_addtid_b32 v114 offset:11136\n"
"v_mfma_f32_16x16x32_bf16 v[104:107], a[184:187], v[80:83], v[104:107]\n"
"ds_write_addtid_b32 v115 offset:15776\n"
"global_load_dwordx4 v[100:103], v5, s[56:57] offset:0 sc1\n"
"v_mfma_f32_16x16x32_bf16 v[104:107], a[188:191], v[84:87], v[104:107]\n"
"v_mfma_f32_16x16x32_bf16 v[104:107], a[192:195], v[88:91], v[104:107]\n"
"v_mfma_f32_16x16x32_bf16 v[108:111], a[196:199], v[64:67], 0\n"
"v_mfma_f32_16x16x32_bf16 v[108:111], a[200:203], v[68:71], v[108:111]\n"
"s_waitcnt lgkmcnt(0)\n"
"v_mfma_f32_16x16x32_bf16 v[108:111], a[204:207], v[72:75], v[108:111]\n"
"s_barrier\n"
"v_mfma_f32_16x16x32_bf16 v[108:111], a[208:211], v[76:79], v[108:111]\n"
"global_load_dwordx4 v[68:71], v6, s[56:57] offset:0 sc1\n"
"ds_read_b128 v[64:67], v2 offset:2048\n"
"v_mfma_f32_16x16x32_bf16 v[108:111], a[212:215], v[80:83], v[108:111]\n"
"ds_read_b128 v[112:115], v3 offset:512\n"
"ds_read_b128 v[116:119], v4 offset:3072\n"
"v_mfma_f32_16x16x32_bf16 v[108:111], a[216:219], v[84:87], v[108:111]\n"
"ds_read_b128 v[120:123], v2 offset:2304\n"
"ds_read_b128 v[124:127], v3 offset:768\n"
"v_mfma_f32_16x16x32_bf16 v[108:111], a[220:223], v[88:91], v[108:111]\n"
"ds_read_b128 v[128:131], v4 offset:3328\n"
"v_mfma_f32_16x16x32_bf16 v[84:87], v[92:95], a[0:3], 0\n"
"global_load_dwordx4 v[72:75], v7, s[56:57] offset:0 sc1\n"
"v_mfma_f32_16x16x32_bf16 v[84:87], v[44:47], a[4:7], v[84:87]\n"
"s_waitcnt vmcnt(8)\n"
"v_mfma_f32_16x16x32_bf16 v[84:87], v[48:51], a[8:11], v[84:87]\n"
"v_mfma_f32_16x16x32_bf16 v[84:87], v[52:55], a[12:15], v[84:87]\n"
"s_waitcnt vmcnt(6)\n"
"v_mfma_f32_16x16x32_bf16 v[84:87], v[56:59], a[16:19], v[84:87]\n"
"s_waitcnt lgkmcnt(3)\n"
"v_add_f32 v64, v64, v112\n"
"v_add_f32 v65, v65, v113\n"
"v_mfma_f32_16x16x32_bf16 v[84:87], v[60:63], a[20:23], v[84:87]\n"
"s_waitcnt vmcnt(5)\n"
"global_load_dwordx4 v[76:79], v8, s[56:57] offset:0 sc1\n"
"v_add_f32 v66, v66, v114\n"
"v_mfma_f32_16x16x32_bf16 v[84:87], v[96:99], a[24:27], v[84:87]\n"
"buffer_load_dwordx2 v[132:133], v12, s[40:43], s50 offen sc1\n"
"v_mfma_f32_16x16x32_bf16 v[88:91], v[92:95], a[28:31], 0\n"
"v_add_f32 v67, v67, v115\n"
"v_add_f32 v64, v64, v116\n"
"v_mfma_f32_16x16x32_bf16 v[88:91], v[44:47], a[32:35], v[88:91]\n"
"s_waitcnt vmcnt(5)\n"
"buffer_store_dwordx2 v[0:1], v14, s[40:43], s95 offen offset:0 sc1\n"
"ds_write_addtid_b32 v84 offset:2048\n"
"v_mfma_f32_16x16x32_bf16 v[88:91], v[48:51], a[36:39], v[88:91]\n"
"ds_write_addtid_b32 v85 offset:6688\n"
"v_add_f32 v65, v65, v117\n"
"v_mfma_f32_16x16x32_bf16 v[88:91], v[52:55], a[40:43], v[88:91]\n"
"ds_write_addtid_b32 v86 offset:11392\n"
"global_load_dwordx4 v[80:83], v9, s[56:57] offset:0 sc1\n"
"v_mfma_f32_16x16x32_bf16 v[88:91], v[56:59], a[44:47], v[88:91]\n"
"ds_write_addtid_b32 v87 offset:16032\n"
"v_add_f32 v66, v66, v118\n"
"v_mfma_f32_16x16x32_bf16 v[88:91], v[60:63], a[48:51], v[88:91]\n"
"v_add_f32 v67, v67, v119\n"
"v_add_f32 v64, v64, v104\n"
"v_mfma_f32_16x16x32_bf16 v[88:91], v[96:99], a[52:55], v[88:91]\n"
"v_add_f32 v65, v65, v105\n"
"v_add_f32 v66, v66, v106\n"
"v_mfma_f32_16x16x32_bf16 v[112:115], v[92:95], a[56:59], 0\n"
"v_add_f32 v67, v67, v107\n"
"v_mfma_f32_16x16x32_bf16 v[112:115], v[44:47], a[60:63], v[112:115]\n"
"ds_write_addtid_b32 v88 offset:2304\n"
"global_load_dwordx4 v[84:87], v10, s[56:57] offset:0 sc1\n"
"v_mfma_f32_16x16x32_bf16 v[112:115], v[48:51], a[64:67], v[112:115]\n"
"s_add_u32 s93, s50, 0xffd00000\n"
"s_and_b32 s95, s93, 0xfbffffff\n"
"ds_write_addtid_b32 v89 offset:6944\n"
"v_mfma_f32_16x16x32_bf16 v[112:115], v[52:55], a[68:71], v[112:115]\n"
"ds_write_addtid_b32 v90 offset:11648\n"
"v_cvt_pk_bf16_f32 v104, v64, v65\n"
"v_mfma_f32_16x16x32_bf16 v[112:115], v[56:59], a[72:75], v[112:115]\n"
"ds_write_addtid_b32 v91 offset:16288\n"
"v_cvt_pk_bf16_f32 v105, v66, v67\n"
"v_mfma_f32_16x16x32_bf16 v[112:115], v[60:63], a[76:79], v[112:115]\n"
"s_waitcnt lgkmcnt(8)\n"
"v_add_f32 v120, v120, v124\n"
"v_add_f32 v121, v121, v125\n"
"v_mfma_f32_16x16x32_bf16 v[112:115], v[96:99], a[80:83], v[112:115]\n"
"global_load_dwordx4 v[88:91], v11, s[56:57] offset:0 sc1\n"
"v_add_f32 v122, v122, v126\n"
"v_mfma_f32_16x16x32_bf16 v[64:67], v[92:95], a[84:87], 0\n"
"v_add_f32 v123, v123, v127\n"
"v_add_f32 v120, v120, v128\n"
"v_mfma_f32_16x16x32_bf16 v[64:67], v[44:47], a[88:91], v[64:67]\n"
"ds_write_addtid_b32 v112 offset:2560\n"
"v_add_f32 v121, v121, v129\n"
"v_mfma_f32_16x16x32_bf16 v[64:67], v[48:51], a[92:95], v[64:67]\n"
"ds_write_addtid_b32 v113 offset:7200\n"
"v_add_f32 v122, v122, v130\n"
"v_mfma_f32_16x16x32_bf16 v[64:67], v[52:55], a[96:99], v[64:67]\n"
"ds_write_addtid_b32 v114 offset:11904\n"
"v_add_f32 v123, v123, v131\n"
"v_mfma_f32_16x16x32_bf16 v[64:67], v[56:59], a[100:103], v[64:67]\n"
"ds_write_addtid_b32 v115 offset:16544\n"
"v_add_f32 v120, v120, v108\n"
"v_mfma_f32_16x16x32_bf16 v[64:67], v[60:63], a[104:107], v[64:67]\n"
"v_add_f32 v121, v121, v109\n"
"v_add_f32 v122, v122, v110\n"
"v_mfma_f32_16x16x32_bf16 v[64:67], v[96:99], a[108:111], v[64:67]\n"
"v_add_f32 v123, v123, v111\n"
"v_cvt_pk_bf16_f32 v106, v120, v121\n"
"v_mfma_f32_16x16x32_bf16 v[112:115], v[92:95], a[112:115], 0\n"
"v_cvt_pk_bf16_f32 v107, v122, v123\n"
"v_mfma_f32_16x16x32_bf16 v[112:115], v[44:47], a[116:119], v[112:115]\n"
"ds_write_addtid_b32 v64 offset:2816\n"
"v_mfma_f32_16x16x32_bf16 v[112:115], v[48:51], a[120:123], v[112:115]\n"
"ds_write_addtid_b32 v65 offset:7456\n"
"v_mfma_f32_16x16x32_bf16 v[112:115], v[52:55], a[124:127], v[112:115]\n"
"ds_write_addtid_b32 v66 offset:12160\n"
"v_mfma_f32_16x16x32_bf16 v[112:115], v[56:59], a[128:131], v[112:115]\n"
"ds_write_addtid_b32 v67 offset:16800\n"
"v_mfma_f32_16x16x32_bf16 v[112:115], v[60:63], a[132:135], v[112:115]\n"
"v_mfma_f32_16x16x32_bf16 v[112:115], v[96:99], a[136:139], v[112:115]\n"
"v_mfma_f32_16x16x32_bf16 v[108:111], v[92:95], a[140:143], 0\n"
"v_mfma_f32_16x16x32_bf16 v[108:111], v[44:47], a[144:147], v[108:111]\n"
"buffer_store_dwordx4 v[104:107], v13, s[40:43], s95 offen sc1\n"
"ds_write_addtid_b32 v112 offset:3072\n"
"v_mfma_f32_16x16x32_bf16 v[108:111], v[48:51], a[148:151], v[108:111]\n"
"ds_write_addtid_b32 v113 offset:7712\n"
"v_mfma_f32_16x16x32_bf16 v[108:111], v[52:55], a[152:155], v[108:111]\n"
"ds_write_addtid_b32 v114 offset:12416\n"
"v_mfma_f32_16x16x32_bf16 v[108:111], v[56:59], a[156:159], v[108:111]\n"
"ds_write_addtid_b32 v115 offset:17056\n"
"v_mfma_f32_16x16x32_bf16 v[108:111], v[60:63], a[160:163], v[108:111]\n"
"v_mfma_f32_16x16x32_bf16 v[108:111], v[96:99], a[164:167], v[108:111]\n"
"v_mfma_f32_16x16x32_bf16 v[64:67], v[92:95], a[224:227], 0\n"
"v_mfma_f32_16x16x32_bf16 v[64:67], v[44:47], a[228:231], v[64:67]\n"
"ds_write_addtid_b32 v108 offset:3328\n"
"v_mfma_f32_16x16x32_bf16 v[64:67], v[48:51], a[232:235], v[64:67]\n"
"ds_write_addtid_b32 v109 offset:7968\n"
"v_mfma_f32_16x16x32_bf16 v[64:67], v[52:55], a[236:239], v[64:67]\n"
"ds_write_addtid_b32 v110 offset:12672\n"
"v_mfma_f32_16x16x32_bf16 v[64:67], v[56:59], a[240:243], v[64:67]\n"
"ds_write_addtid_b32 v111 offset:17312\n"
"v_mfma_f32_16x16x32_bf16 v[64:67], v[60:63], a[244:247], v[64:67]\n"
"v_mfma_f32_16x16x32_bf16 v[64:67], v[96:99], a[248:251], v[64:67]\n"
"v_mfma_f32_16x16x32_bf16 v[112:115], v[92:95], a[252:255], 0\n"
"v_mfma_f32_16x16x32_bf16 v[112:115], v[44:47], v[20:23], v[112:115]\n"
"s_add_u32 s58, s50, 0x180000\n"
"ds_write_addtid_b32 v64 offset:3584\n"
"v_mfma_f32_16x16x32_bf16 v[112:115], v[48:51], v[24:27], v[112:115]\n"
"s_and_b32 s58, s58, 0x3ffffff\n"
"ds_write_addtid_b32 v65 offset:8224\n"
"v_mfma_f32_16x16x32_bf16 v[112:115], v[52:55], v[28:31], v[112:115]\n"
"s_add_u32 s56, s40, s58\n"
"ds_write_addtid_b32 v66 offset:12928\n"
"v_mfma_f32_16x16x32_bf16 v[112:115], v[56:59], v[32:35], v[112:115]\n"
"s_addc_u32 s57, s41, 0\n"
"ds_write_addtid_b32 v67 offset:17568\n"
"v_mfma_f32_16x16x32_bf16 v[112:115], v[60:63], v[36:39], v[112:115]\n"
"s_waitcnt vmcnt(5)\n"
"v_cmp_ne_u64 vcc, s[48:49], v[132:133]\n"
"v_mfma_f32_16x16x32_bf16 v[112:115], v[96:99], v[40:43], v[112:115]\n"
"v_mfma_f32_16x16x32_bf16 v[116:119], a[168:171], v[92:95], 0\n"
"s_cbranch_vccnz .LG_2s\n"
".LG_2r:\n"
"v_mfma_f32_16x16x32_bf16 v[116:119], a[172:175], v[44:47], v[116:119]\n"
"s_add_u32 s50, s50, 0x180000\n"
"ds_write_addtid_b32 v112 offset:3840\n"
"v_mfma_f32_16x16x32_bf16 v[116:119], a[176:179], v[48:51], v[116:119]\n"
"ds_write_addtid_b32 v113 offset:8480\n"
"v_mfma_f32_16x16x32_bf16 v[116:119], a[180:183], v[52:55], v[116:119]\n"
"ds_write_addtid_b32 v114 offset:13184\n"
"v_mfma_f32_16x16x32_bf16 v[116:119], a[184:187], v[56:59], v[116:119]\n"
"ds_write_addtid_b32 v115 offset:17824\n"
"v_mfma_f32_16x16x32_bf16 v[116:119], a[188:191], v[60:63], v[116:119]\n"
"global_load_dwordx4 v[104:107], v5, s[56:57] offset:0 sc1\n"
"v_mfma_f32_16x16x32_bf16 v[116:119], a[192:195], v[96:99], v[116:119]\n"
"v_mfma_f32_16x16x32_bf16 v[112:115], a[196:199], v[92:95], 0\n"
"v_mfma_f32_16x16x32_bf16 v[112:115], a[200:203], v[44:47], v[112:115]\n"
"s_waitcnt lgkmcnt(0)\n"
"global_load_dwordx4 v[108:111], v6, s[56:57] offset:0 sc1\n"
"v_mfma_f32_16x16x32_bf16 v[112:115], a[204:207], v[48:51], v[112:115]\n"
"s_barrier\n"
"v_mfma_f32_16x16x32_bf16 v[112:115], a[208:211], v[52:55], v[112:115]\n"
"ds_read_b128 v[120:123], v2 offset:0\n"
"ds_read_b128 v[64:67], v3 offset:2560\n"
"v_mfma_f32_16x16x32_bf16 v[112:115], a[212:215], v[56:59], v[112:115]\n"
"ds_read_b128 v[92:95], v4 offset:1024\n"
"ds_read_b128 v[124:127], v2 offset:256\n"
"v_mfma_f32_16x16x32_bf16 v[112:115], a[216:219], v[60:63], v[112:115]\n"
"ds_read_b128 v[128:131], v3 offset:2816\n"
"ds_read_b128 v[132:135], v4 offset:1280\n"
"v_mfma_f32_16x16x32_bf16 v[112:115], a[220:223], v[96:99], v[112:115]\n"
"s_branch .LG_top\n"
".LG_1s:\n"
"s_cmp_ge_u32 s50, s53\n"
"s_cbranch_scc1 .LG_t0\n"
"s_cmp_ge_u32 s50, s52\n"
"s_cbranch_scc1 .LG_1r\n"
"s_sub_u32 s60, s50, 0x0\n"
"s_mov_b32 s61, 200000\n"
"s_nop 7\n"
".LG_1p:\n"
"buffer_load_dwordx2 v[128:129], v12, s[40:43], s60 offen sc1\n"
"s_waitcnt vmcnt(0)\n"
"v_cmp_ne_u64 vcc, s[48:49], v[128:129]\n"
"s_cbranch_vccz .LG_1px\n"
"s_sleep 1\n"
"s_sub_u32 s61, s61, 1\n"
"s_cbranch_scc0 .LG_1p\n"
"s_branch .LG_end\n"
".LG_1px:\n"
"s_branch .LG_1r\n"
".LG_2s:\n"
"s_cmp_ge_u32 s50, s53\n"
"s_cbranch_scc1 .LG_t1\n"
"s_cmp_ge_u32 s50, s52\n"
"s_cbranch_scc1 .LG_2r\n"
"s_sub_u32 s60, s50, 0x0\n"
"s_mov_b32 s61, 200000\n"
"s_nop 7\n"
".LG_2p:\n"
"buffer_load_dwordx2 v[132:133], v12, s[40:43], s60 offen sc1\n"
"s_waitcnt vmcnt(0)\n"
"v_cmp_ne_u64 vcc, s[48:49], v[132:133]\n"
"s_cbranch_vccz .LG_2px\n"
"s_sleep 1\n"
"s_sub_u32 s61, s61, 1\n"
"s_cbranch_scc0 .LG_2p\n"
"s_branch .LG_end\n"
".LG_2px:\n"
"s_branch .LG_2r\n"
".LG_3s:\n"
"s_cmp_ge_u32 s50, s53\n"
"s_cbranch_scc1 .LG_t2\n"
"s_cmp_ge_u32 s50, s52\n"
"s_cbranch_scc1 .LG_3r\n"
"s_sub_u32 s60, s50, 0x0\n"
"s_mov_b32 s61, 200000\n"
"s_nop 7\n"
".LG_3p:\n"
"buffer_load_dwordx2 v[136:137], v12, s[40:43], s60 offen sc1\n"
"s_waitcnt vmcnt(0)\n"
"v_cmp_ne_u64 vcc, s[48:49], v[136:137]\n"
"s_cbranch_vccz .LG_3px\n"
"s_sleep 1\n"
"s_sub_u32 s61, s61, 1\n"
"s_cbranch_scc0 .LG_3p\n"
"s_branch .LG_end\n"
".LG_3px:\n"
"s_branch .LG_3r\n"
".LG_4s:\n"
"s_cmp_ge_u32 s50, s53\n"
"s_cbranch_scc1 .LG_t3\n"
"s_cmp_ge_u32 s50, s52\n"
"s_cbranch_scc1 .LG_4r\n"
"s_sub_u32 s60, s50, 0x0\n"
"s_mov_b32 s61, 200000\n"
"s_nop 7\n"
".LG_4p:\n"
"buffer_load_dwordx2 v[168:169], v12, s[40:43], s60 offen sc1\n"
"s_waitcnt vmcnt(0)\n"
"v_cmp_ne_u64 vcc, s[48:49], v[168:169]\n"
"s_cbranch_vccz .LG_4px\n"
"s_sleep 1\n"
"s_sub_u32 s61, s61, 1\n"
"s_cbranch_scc0 .LG_4p\n"
"s_branch .LG_end\n"
".LG_4px:\n"
"s_branch .LG_4r\n"
".LG_t0:\n"
"v_mfma_f32_16x16x32_bf16 v[104:107], a[172:175], v[68:71], v[104:107]\n"
"ds_write_addtid_b32 v112 offset:1792\n"
"v_mfma_f32_16x16x32_bf16 v[104:107], a[176:179], v[72:75], v[104:107]\n"
"ds_write_addtid_b32 v113 offset:6432\n"
"v_mfma_f32_16x16x32_bf16 v[104:107], a[180:183], v[76:79], v[104:107]\n"
"ds_write_addtid_b32 v114 offset:11136\n"
"v_mfma_f32_16x16x32_bf16 v[104:107], a[184:187], v[80:83], v[104:107]\n"
"ds_write_addtid_b32 v115 offset:15776\n"
"v_mfma_f32_16x16x32_bf16 v[104:107], a[188:191], v[84:87], v[104:107]\n"
"v_mfma_f32_16x16x32_bf16 v[104:107], a[192:195], v[88:91], v[104:107]\n"
"v_mfma_f32_16x16x32_bf16 v[108:111], a[196:199], v[64:67], 0\n"
"v_mfma_f32_16x16x32_bf16 v[108:111], a[200:203], v[68:71], v[108:111]\n"
"s_waitcnt lgkmcnt(0)\n"
"v_mfma_f32_16x16x32_bf16 v[108:111], a[204:207], v[72:75], v[108:111]\n"
"s_barrier\n"
"v_mfma_f32_16x16x32_bf16 v[108:111], a[208:211], v[76:79], v[108:111]\n"
"ds_read_b128 v[64:67], v2 offset:2048\n"
"v_mfma_f32_16x16x32_bf16 v[108:111], a[212:215], v[80:83], v[108:111]\n"
"ds_read_b128 v[112:115], v3 offset:512\n"
"ds_read_b128 v[116:119], v4 offset:3072\n"
"v_mfma_f32_16x16x32_bf16 v[108:111], a[216:219], v[84:87], v[108:111]\n"
"ds_read_b128 v[120:123], v2 offset:2304\n"
"ds_read_b128 v[124:127], v3 offset:768\n"
"v_mfma_f32_16x16x32_bf16 v[108:111], a[220:223], v[88:91], v[108:111]\n"
"s_waitcnt vmcnt(8)\n"
"ds_read_b128 v[128:131], v4 offset:3328\n"
"v_mfma_f32_16x16x32_bf16 v[84:87], v[92:95], a[0:3], 0\n"
"v_mfma_f32_16x16x32_bf16 v[84:87], v[44:47], a[4:7], v[84:87]\n"
"s_waitcnt vmcnt(5)\n"
"v_mfma_f32_16x16x32_bf16 v[84:87], v[48:51], a[8:11], v[84:87]\n"
"v_mfma_f32_16x16x32_bf16 v[84:87], v[52:55], a[12:15], v[84:87]\n"
"s_waitcnt vmcnt(3)\n"
"v_mfma_f32_16x16x32_bf16 v[84:87], v[56:59], a[16:19], v[84:87]\n"
"s_waitcnt lgkmcnt(3)\n"
"v_add_f32 v64, v64, v112\n"
"v_add_f32 v65, v65, v113\n"
"v_mfma_f32_16x16x32_bf16 v[84:87], v[60:63], a[20:23], v[84:87]\n"
"s_waitcnt vmcnt(2)\n"
"v_add_f32 v66, v66, v114\n"
"v_mfma_f32_16x16x32_bf16 v[84:87], v[96:99], a[24:27], v[84:87]\n"
"v_mfma_f32_16x16x32_bf16 v[88:91], v[92:95], a[28:31], 0\n"
"v_add_f32 v67, v67, v115\n"
"v_add_f32 v64, v64, v116\n"
"v_mfma_f32_16x16x32_bf16 v[88:91], v[44:47], a[32:35], v[88:91]\n"
"s_waitcnt vmcnt(0)\n"
"buffer_store_dwordx2 v[0:1], v14, s[40:43], s95 offen offset:0 sc1\n"
"ds_write_addtid_b32 v84 offset:2048\n"
"v_mfma_f32_16x16x32_bf16 v[88:91], v[48:51], a[36:39], v[88:91]\n"
"ds_write_addtid_b32 v85 offset:6688\n"
"v_add_f32 v65, v65, v117\n"
"v_mfma_f32_16x16x32_bf16 v[88:91], v[52:55], a[40:43], v[88:91]\n"
"ds_write_addtid_b32 v86 offset:11392\n"
"v_mfma_f32_16x16x32_bf16 v[88:91], v[56:59], a[44:47], v[88:91]\n"
"ds_write_addtid_b32 v87 offset:16032\n"
"v_add_f32 v66, v66, v118\n"
"v_mfma_f32_16x16x32_bf16 v[88:91], v[60:63], a[48:51], v[88:91]\n"
"v_add_f32 v67, v67, v119\n"
"v_add_f32 v64, v64, v104\n"
"v_mfma_f32_16x16x32_bf16 v[88:91], v[96:99], a[52:55], v[88:91]\n"
"v_add_f32 v65, v65, v105\n"
"v_add_f32 v66, v66, v106\n"
"v_mfma_f32_16x16x32_bf16 v[112:115], v[92:95], a[56:59], 0\n"
"v_add_f32 v67, v67, v107\n"
"v_mfma_f32_16x16x32_bf16 v[112:115], v[44:47], a[60:63], v[112:115]\n"
"ds_write_addtid_b32 v88 offset:2304\n"
"v_mfma_f32_16x16x32_bf16 v[112:115], v[48:51], a[64:67], v[112:115]\n"
"s_add_u32 s93, s50, 0xffe80000\n"
"s_and_b32 s95, s93, 0xfbffffff\n"
"ds_write_addtid_b32 v89 offset:6944\n"
"v_mfma_f32_16x16x32_bf16 v[112:115], v[52:55], a[68:71], v[112:115]\n"
"ds_write_addtid_b32 v90 offset:11648\n"
"v_cvt_pk_bf16_f32 v104, v64, v65\n"
"v_mfma_f32_16x16x32_bf16 v[112:115], v[56:59], a[72:75], v[112:115]\n"
"ds_write_addtid_b32 v91 offset:16288\n"
"v_cvt_pk_bf16_f32 v105, v66, v67\n"
"v_mfma_f32_16x16x32_bf16 v[112:115], v[60:63], a[76:79], v[112:115]\n"
"s_waitcnt lgkmcnt(8)\n"
"v_add_f32 v120, v120, v124\n"
"v_add_f32 v121, v121, v125\n"
"v_mfma_f32_16x16x32_bf16 v[112:115], v[96:99], a[80:83], v[112:115]\n"
"v_add_f32 v122, v122, v126\n"
"v_mfma_f32_16x16x32_bf16 v[64:67], v[92:95], a[84:87], 0\n"
"v_add_f32 v123, v123, v127\n"
"v_add_f32 v120, v120, v128\n"
"v_mfma_f32_16x16x32_bf16 v[64:67], v[44:47], a[88:91], v[64:67]\n"
"ds_write_addtid_b32 v112 offset:2560\n"
"v_add_f32 v121, v121, v129\n"
"v_mfma_f32_16x16x32_bf16 v[64:67], v[48:51], a[92:95], v[64:67]\n"
"ds_write_addtid_b32 v113 offset:7200\n"
"v_add_f32 v122, v122, v130\n"
"v_mfma_f32_16x16x32_bf16 v[64:67], v[52:55], a[96:99], v[64:67]\n"
"ds_write_addtid_b32 v114 offset:11904\n"
"v_add_f32 v123, v123, v131\n"
"v_mfma_f32_16x16x32_bf16 v[64:67], v[56:59], a[100:103], v[64:67]\n"
"ds_write_addtid_b32 v115 offset:16544\n"
"v_add_f32 v120, v120, v108\n"
"v_mfma_f32_16x16x32_bf16 v[64:67], v[60:63], a[104:107], v[64:67]\n"
"v_add_f32 v121, v121, v109\n"
"v_add_f32 v122, v122, v110\n"
"v_mfma_f32_16x16x32_bf16 v[64:67], v[96:99], a[108:111], v[64:67]\n"
"v_add_f32 v123, v123, v111\n"
"v_cvt_pk_bf16_f32 v106, v120, v121\n"
"v_mfma_f32_16x16x32_bf16 v[112:115], v[92:95], a[112:115], 0\n"
"v_cvt_pk_bf16_f32 v107, v122, v123\n"
"v_mfma_f32_16x16x32_bf16 v[112:115], v[44:47], a[116:119], v[112:115]\n"
"ds_write_addtid_b32 v64 offset:2816\n"
"v_mfma_f32_16x16x32_bf16 v[112:115], v[48:51], a[120:123], v[112:115]\n"
"ds_write_addtid_b32 v65 offset:7456\n"
"v_mfma_f32_16x16x32_bf16 v[112:115], v[52:55], a[124:127], v[112:115]\n"
"ds_write_addtid_b32 v66 offset:12160\n"
"v_mfma_f32_16x16x32_bf16 v[112:115], v[56:59], a[128:131], v[112:115]\n"
"ds_write_addtid_b32 v67 offset:16800\n"
"v_mfma_f32_16x16x32_bf16 v[112:115], v[60:63], a[132:135], v[112:115]\n"
"v_mfma_f32_16x16x32_bf16 v[112:115], v[96:99], a[136:139], v[112:115]\n"
"v_mfma_f32_16x16x32_bf16 v[108:111], v[92:95], a[140:143], 0\n"
"v_mfma_f32_16x16x32_bf16 v[108:111], v[44:47], a[144:147], v[108:111]\n"
"buffer_store_dwordx4 v[104:107], v13, s[40:43], s95 offen sc1\n"
"ds_write_addtid_b32 v112 offset:3072\n"
"v_mfma_f32_16x16x32_bf16 v[108:111], v[48:51], a[148:151], v[108:111]\n"
"ds_write_addtid_b32 v113 offset:7712\n"
"v_mfma_f32_16x16x32_bf16 v[108:111], v[52:55], a[152:155], v[108:111]\n"
"ds_write_addtid_b32 v114 offset:12416\n"
"v_mfma_f32_16x16x32_bf16 v[108:111], v[56:59], a[156:159], v[108:111]\n"
"ds_write_addtid_b32 v115 offset:17056\n"
"v_mfma_f32_16x16x32_bf16 v[108:111], v[60:63], a[160:163], v[108:111]\n"
"v_mfma_f32_16x16x32_bf16 v[108:111], v[96:99], a[164:167], v[108:111]\n"
"v_mfma_f32_16x16x32_bf16 v[64:67], v[92:95], a[224:227], 0\n"
"v_mfma_f32_16x16x32_bf16 v[64:67], v[44:47], a[228:231], v[64:67]\n"
"ds_write_addtid_b32 v108 offset:3328\n"
"v_mfma_f32_16x16x32_bf16 v[64:67], v[48:51], a[232:235], v[64:67]\n"
"ds_write_addtid_b32 v109 offset:7968\n"
"v_mfma_f32_16x16x32_bf16 v[64:67], v[52:55], a[236:239], v[64:67]\n"
"ds_write_addtid_b32 v110 offset:12672\n"
"v_mfma_f32_16x16x32_bf16 v[64:67], v[56:59], a[240:243], v[64:67]\n"
"ds_write_addtid_b32 v111 offset:17312\n"
"v_mfma_f32_16x16x32_bf16 v[64:67], v[60:63], a[244:247], v[64:67]\n"
"v_mfma_f32_16x16x32_bf16 v[64:67], v[96:99], a[248:251], v[64:67]\n"
"v_mfma_f32_16x16x32_bf16 v[112:115], v[92:95], a[252:255], 0\n"
"v_mfma_f32_16x16x32_bf16 v[112:115], v[44:47], v[20:23], v[112:115]\n"
"ds_write_addtid_b32 v64 offset:3584\n"
"v_mfma_f32_16x16x32_bf16 v[112:115], v[48:51], v[24:27], v[112:115]\n"
"ds_write_addtid_b32 v65 offset:8224\n"
"v_mfma_f32_16x16x32_bf16 v[112:115], v[52:55], v[28:31], v[112:115]\n"
"ds_write_addtid_b32 v66 offset:12928\n"
"v_mfma_f32_16x16x32_bf16 v[112:115], v[56:59], v[32:35], v[112:115]\n"
"ds_write_addtid_b32 v67 offset:17568\n"
"v_mfma_f32_16x16x32_bf16 v[112:115], v[60:63], v[36:39], v[112:115]\n"
"v_mfma_f32_16x16x32_bf16 v[112:115], v[96:99], v[40:43], v[112:115]\n"
"v_mfma_f32_16x16x32_bf16 v[116:119], a[168:171], v[92:95], 0\n"
"v_mfma_f32_16x16x32_bf16 v[116:119], a[172:175], v[44:47], v[116:119]\n"
"ds_write_addtid_b32 v112 offset:3840\n"
"v_mfma_f32_16x16x32_bf16 v[116:119], a[176:179], v[48:51], v[116:119]\n"
"ds_write_addtid_b32 v113 offset:8480\n"
"v_mfma_f32_16x16x32_bf16 v[116:119], a[180:183], v[52:55], v[116:119]\n"
"ds_write_addtid_b32 v114 offset:13184\n"
"v_mfma_f32_16x16x32_bf16 v[116:119], a[184:187], v[56:59], v[116:119]\n"
"ds_write_addtid_b32 v115 offset:17824\n"
"v_mfma_f32_16x16x32_bf16 v[116:119], a[188:191], v[60:63], v[116:119]\n"
"v_mfma_f32_16x16x32_bf16 v[116:119], a[192:195], v[96:99], v[116:119]\n"
"v_mfma_f32_16x16x32_bf16 v[112:115], a[196:199], v[92:95], 0\n"
"v_mfma_f32_16x16x32_bf16 v[112:115], a[200:203], v[44:47], v[112:115]\n"
"s_waitcnt lgkmcnt(0)\n"
"v_mfma_f32_16x16x32_bf16 v[112:115], a[204:207], v[48:51], v[112:115]\n"
"s_barrier\n"
"v_mfma_f32_16x16x32_bf16 v[112:115], a[208:211], v[52:55], v[112:115]\n"
"ds_read_b128 v[120:123], v2 offset:0\n"
"ds_read_b128 v[64:67], v3 offset:2560\n"
"v_mfma_f32_16x16x32_bf16 v[112:115], a[212:215], v[56:59], v[112:115]\n"
"ds_read_b128 v[92:95], v4 offset:1024\n"
"ds_read_b128 v[124:127], v2 offset:256\n"
"v_mfma_f32_16x16x32_bf16 v[112:115], a[216:219], v[60:63], v[112:115]\n"
"ds_read_b128 v[128:131], v3 offset:2816\n"
"ds_read_b128 v[132:135], v4 offset:1280\n"
"v_mfma_f32_16x16x32_bf16 v[112:115], a[220:223], v[96:99], v[112:115]\n"
"s_waitcnt lgkmcnt(3)\n"
"v_add_f32 v120, v120, v64\n"
"v_add_f32 v121, v121, v65\n"
"v_add_f32 v122, v122, v66\n"
"v_add_f32 v123, v123, v67\n"
"v_add_f32 v120, v120, v92\n"
"v_add_f32 v121, v121, v93\n"
"v_add_f32 v122, v122, v94\n"
"v_add_f32 v123, v123, v95\n"
"v_add_f32 v120, v120, v116\n"
"v_add_f32 v121, v121, v117\n"
"v_add_f32 v122, v122, v118\n"
"v_add_f32 v123, v123, v119\n"
"s_waitcnt vmcnt(0)\n"
"buffer_store_dwordx2 v[0:1], v14, s[40:43], s93 offen offset:0 sc1\n"
"s_add_u32 s95, s50, 0x0\n"
"s_and_b32 s94, s95, 0xfbffffff\n"
"v_cvt_pk_bf16_f32 v92, v120, v121\n"
"v_cvt_pk_bf16_f32 v93, v122, v123\n"
"s_waitcnt lgkmcnt(0)\n"
"v_add_f32 v124, v124, v128\n"
"v_add_f32 v125, v125, v129\n"
"v_add_f32 v126, v126, v130\n"
"v_add_f32 v127, v127, v131\n"
"v_add_f32 v124, v124, v132\n"
"v_add_f32 v125, v125, v133\n"
"v_add_f32 v126, v126, v134\n"
"v_add_f32 v127, v127, v135\n"
"v_add_f32 v124, v124, v112\n"
"v_add_f32 v125, v125, v113\n"
"v_add_f32 v126, v126, v114\n"
"v_add_f32 v127, v127, v115\n"
"v_cvt_pk_bf16_f32 v94, v124, v125\n"
"v_cvt_pk_bf16_f32 v95, v126, v127\n"
"buffer_store_dwordx4 v[92:95], v13, s[40:43], s94 offen sc1\n"
"s_waitcnt vmcnt(0)\n"
"buffer_store_dwordx2 v[0:1], v14, s[40:43], s95 offen offset:0 sc1\n"
"s_branch .LG_end\n"
".LG_t1:\n"
"v_mfma_f32_16x16x32_bf16 v[116:119], a[172:175], v[44:47], v[116:119]\n"
"ds_write_addtid_b32 v112 offset:3840\n"
"v_mfma_f32_16x16x32_bf16 v[116:119], a[176:179], v[48:51], v[116:119]\n"
"ds_write_addtid_b32 v113 offset:8480\n"
"v_mfma_f32_16x16x32_bf16 v[116:119], a[180:183], v[52:55], v[116:119]\n"
"ds_write_addtid_b32 v114 offset:13184\n"
"v_mfma_f32_16x16x32_bf16 v[116:119], a[184:187], v[56:59], v[116:119]\n"
"ds_write_addtid_b32 v115 offset:17824\n"
"v_mfma_f32_16x16x32_bf16 v[116:119], a[188:191], v[60:63], v[116:119]\n"
"v_mfma_f32_16x16x32_bf16 v[116:119], a[192:195], v[96:99], v[116:119]\n"
"v_mfma_f32_16x16x32_bf16 v[112:115], a[196:199], v[92:95], 0\n"
"v_mfma_f32_16x16x32_bf16 v[112:115], a[200:203], v[44:47], v[112:115]\n"
"s_waitcnt lgkmcnt(0)\n"
"v_mfma_f32_16x16x32_bf16 v[112:115], a[204:207], v[48:51], v[112:115]\n"
"s_barrier\n"
"v_mfma_f32_16x16x32_bf16 v[112:115], a[208:211], v[52:55], v[112:115]\n"
"ds_read_b128 v[120:123], v2 offset:0\n"
"ds_read_b128 v[64:67], v3 offset:2560\n"
"v_mfma_f32_16x16x32_bf16 v[112:115], a[212:215], v[56:59], v[112:115]\n"
"ds_read_b128 v[92:95], v4 offset:1024\n"
"ds_read_b128 v[124:127], v2 offset:256\n"
"v_mfma_f32_16x16x32_bf16 v[112:115], a[216:219], v[60:63], v[112:115]\n"
"ds_read_b128 v[128:131], v3 offset:2816\n"
"ds_read_b128 v[132:135], v4 offset:1280\n"
"v_mfma_f32_16x16x32_bf16 v[112:115], a[220:223], v[96:99], v[112:115]\n"
"s_waitcnt vmcnt(8)\n"
"v_mfma_f32_16x16x32_bf16 v[56:59], v[100:103], a[0:3], 0\n"
"v_mfma_f32_16x16x32_bf16 v[56:59], v[68:71], a[4:7], v[56:59]\n"
"s_waitcnt vmcnt(6)\n"
"v_mfma_f32_16x16x32_bf16 v[56:59], v[72:75], a[8:11], v[56:59]\n"
"v_mfma_f32_16x16x32_bf16 v[56:59], v[76:79], a[12:15], v[56:59]\n"
"s_waitcnt vmcnt(2)\n"
"v_mfma_f32_16x16x32_bf16 v[56:59], v[80:83], a[16:19], v[56:59]\n"
"s_waitcnt lgkmcnt(3)\n"
"v_add_f32 v120, v120, v64\n"
"v_add_f32 v121, v121, v65\n"
"v_mfma_f32_16x16x32_bf16 v[56:59], v[84:87], a[20:23], v[56:59]\n"
"s_waitcnt vmcnt(1)\n"
"v_add_f32 v122, v122, v66\n"
"v_add_f32 v123, v123, v67\n"
"v_mfma_f32_16x16x32_bf16 v[56:59], v[88:91], a[24:27], v[56:59]\n"
"v_add_f32 v120, v120, v92\n"
"v_mfma_f32_16x16x32_bf16 v[60:63], v[100:103], a[28:31], 0\n"
"v_mfma_f32_16x16x32_bf16 v[60:63], v[68:71], a[32:35], v[60:63]\n"
"ds_write_addtid_b32 v56 offset:0\n"
"v_add_f32 v121, v121, v93\n"
"v_mfma_f32_16x16x32_bf16 v[60:63], v[72:75], a[36:39], v[60:63]\n"
"ds_write_addtid_b32 v57 offset:4640\n"
"v_mfma_f32_16x16x32_bf16 v[60:63], v[76:79], a[40:43], v[60:63]\n"
"ds_write_addtid_b32 v58 offset:9344\n"
"v_add_f32 v122, v122, v94\n"
"v_mfma_f32_16x16x32_bf16 v[60:63], v[80:83], a[44:47], v[60:63]\n"
"ds_write_addtid_b32 v59 offset:13984\n"
"v_add_f32 v123, v123, v95\n"
"v_mfma_f32_16x16x32_bf16 v[60:63], v[84:87], a[48:51], v[60:63]\n"
"v_add_f32 v120, v120, v116\n"
"v_add_f32 v121, v121, v117\n"
"v_mfma_f32_16x16x32_bf16 v[60:63], v[88:91], a[52:55], v[60:63]\n"
"v_add_f32 v122, v122, v118\n"
"v_add_f32 v123, v123, v119\n"
"v_mfma_f32_16x16x32_bf16 v[64:67], v[100:103], a[56:59], 0\n"
"s_waitcnt vmcnt(0)\n"
"buffer_store_dwordx2 v[0:1], v14, s[40:43], s93 offen offset:0 sc1\n"
"v_mfma_f32_16x16x32_bf16 v[64:67], v[68:71], a[60:63], v[64:67]\n"
"s_add_u32 s95, s50, 0xffe80000\n"
"s_and_b32 s94, s95, 0xfbffffff\n"
"ds_write_addtid_b32 v60 offset:256\n"
"v_mfma_f32_16x16x32_bf16 v[64:67], v[72:75], a[64:67], v[64:67]\n"
"ds_write_addtid_b32 v61 offset:4896\n"
"v_mfma_f32_16x16x32_bf16 v[64:67], v[76:79], a[68:71], v[64:67]\n"
"ds_write_addtid_b32 v62 offset:9600\n"
"v_cvt_pk_bf16_f32 v92, v120, v121\n"
"v_mfma_f32_16x16x32_bf16 v[64:67], v[80:83], a[72:75], v[64:67]\n"
"ds_write_addtid_b32 v63 offset:14240\n"
"v_cvt_pk_bf16_f32 v93, v122, v123\n"
"v_mfma_f32_16x16x32_bf16 v[64:67], v[84:87], a[76:79], v[64:67]\n"
"s_waitcnt lgkmcnt(8)\n"
"v_add_f32 v124, v124, v128\n"
"v_add_f32 v125, v125, v129\n"
"v_mfma_f32_16x16x32_bf16 v[64:67], v[88:91], a[80:83], v[64:67]\n"
"v_add_f32 v126, v126, v130\n"
"v_add_f32 v127, v127, v131\n"
"v_mfma_f32_16x16x32_bf16 v[96:99], v[100:103], a[84:87], 0\n"
"v_add_f32 v124, v124, v132\n"
"v_mfma_f32_16x16x32_bf16 v[96:99], v[68:71], a[88:91], v[96:99]\n"
"ds_write_addtid_b32 v64 offset:512\n"
"v_add_f32 v125, v125, v133\n"
"v_mfma_f32_16x16x32_bf16 v[96:99], v[72:75], a[92:95], v[96:99]\n"
"ds_write_addtid_b32 v65 offset:5152\n"
"v_add_f32 v126, v126, v134\n"
"v_mfma_f32_16x16x32_bf16 v[96:99], v[76:79], a[96:99], v[96:99]\n"
"ds_write_addtid_b32 v66 offset:9856\n"
"v_add_f32 v127, v127, v135\n"
"v_mfma_f32_16x16x32_bf16 v[96:99], v[80:83], a[100:103], v[96:99]\n"
"ds_write_addtid_b32 v67 offset:14496\n"
"v_add_f32 v124, v124, v112\n"
"v_mfma_f32_16x16x32_bf16 v[96:99], v[84:87], a[104:107], v[96:99]\n"
"v_add_f32 v125, v125, v113\n"
"v_add_f32 v126, v126, v114\n"
"v_mfma_f32_16x16x32_bf16 v[96:99], v[88:91], a[108:111], v[96:99]\n"
"v_add_f32 v127, v127, v115\n"
"v_cvt_pk_bf16_f32 v94, v124, v125\n"
"v_mfma_f32_16x16x32_bf16 v[64:67], v[100:103], a[112:115], 0\n"
"v_cvt_pk_bf16_f32 v95, v126, v127\n"
"v_mfma_f32_16x16x32_bf16 v[64:67], v[68:71], a[116:119], v[64:67]\n"
"ds_write_addtid_b32 v96 offset:768\n"
"v_mfma_f32_16x16x32_bf16 v[64:67], v[72:75], a[120:123], v[64:67]\n"
"ds_write_addtid_b32 v97 offset:5408\n"
"v_mfma_f32_16x16x32_bf16 v[64:67], v[76:79], a[124:127], v[64:67]\n"
"ds_write_addtid_b32 v98 offset:10112\n"
"v_mfma_f32_16x16x32_bf16 v[64:67], v[80:83], a[128:131], v[64:67]\n"
"ds_write_addtid_b32 v99 offset:14752\n"
"v_mfma_f32_16x16x32_bf16 v[64:67], v[84:87], a[132:135], v[64:67]\n"
"v_mfma_f32_16x16x32_bf16 v[64:67], v[88:91], a[136:139], v[64:67]\n"
"v_mfma_f32_16x16x32_bf16 v[112:115], v[100:103], a[140:143], 0\n"
"v_mfma_f32_16x16x32_bf16 v[112:115], v[68:71], a[144:147], v[112:115]\n"
"ds_write_addtid_b32 v64 offset:1024\n"
"v_mfma_f32_16x16x32_bf16 v[112:115], v[72:75], a[148:151], v[112:115]\n"
"ds_write_addtid_b32 v65 offset:5664\n"
"v_mfma_f32_16x16x32_bf16 v[112:115], v[76:79], a[152:155], v[112:115]\n"
"ds_write_addtid_b32 v66 offset:10368\n"
"v_mfma_f32_16x16x32_bf16 v[112:115], v[80:83], a[156:159], v[112:115]\n"
"ds_write_addtid_b32 v67 offset:15008\n"
"v_mfma_f32_16x16x32_bf16 v[112:115], v[84:87], a[160:163], v[112:115]\n"
"buffer_store_dwordx4 v[92:95], v13, s[40:43], s94 offen sc1\n"
"v_mfma_f32_16x16x32_bf16 v[112:115], v[88:91], a[164:167], v[112:115]\n"
"v_mfma_f32_16x16x32_bf16 v[96:99], a[168:171], v[100:103], 0\n"
"v_mfma_f32_16x16x32_bf16 v[96:99], a[172:175], v[68:71], v[96:99]\n"
"ds_write_addtid_b32 v112 offset:1280\n"
"v_mfma_f32_16x16x32_bf16 v[96:99], a[176:179], v[72:75], v[96:99]\n"
"ds_write_addtid_b32 v113 offset:5920\n"
"v_mfma_f32_16x16x32_bf16 v[96:99], a[180:183], v[76:79], v[96:99]\n"
"ds_write_addtid_b32 v114 offset:10624\n"
"v_mfma_f32_16x16x32_bf16 v[96:99], a[184:187], v[80:83], v[96:99]\n"
"ds_write_addtid_b32 v115 offset:15264\n"
"v_mfma_f32_16x16x32_bf16 v[96:99], a[188:191], v[84:87], v[96:99]\n"
"v_mfma_f32_16x16x32_bf16 v[96:99], a[192:195], v[88:91], v[96:99]\n"
"v_mfma_f32_16x16x32_bf16 v[92:95], a[196:199], v[100:103], 0\n"
"v_mfma_f32_16x16x32_bf16 v[92:95], a[200:203], v[68:71], v[92:95]\n"
"v_mfma_f32_16x16x32_bf16 v[92:95], a[204:207], v[72:75], v[92:95]\n"
"v_mfma_f32_16x16x32_bf16 v[92:95], a[208:211], v[76:79], v[92:95]\n"
"v_mfma_f32_16x16x32_bf16 v[92:95], a[212:215], v[80:83], v[92:95]\n"
"v_mfma_f32_16x16x32_bf16 v[92:95], a[216:219], v[84:87], v[92:95]\n"
"v_mfma_f32_16x16x32_bf16 v[92:95], a[220:223], v[88:91], v[92:95]\n"
"v_mfma_f32_16x16x32_bf16 v[112:115], a[224:227], v[100:103], 0\n"
"v_mfma_f32_16x16x32_bf16 v[112:115], a[228:231], v[68:71], v[112:115]\n"
"v_mfma_f32_16x16x32_bf16 v[112:115], a[232:235], v[72:75], v[112:115]\n"
"v_mfma_f32_16x16x32_bf16 v[112:115], a[236:239], v[76:79], v[112:115]\n"
"v_mfma_f32_16x16x32_bf16 v[112:115], a[240:243], v[80:83], v[112:115]\n"
"v_mfma_f32_16x16x32_bf16 v[112:115], a[244:247], v[84:87], v[112:115]\n"
"v_mfma_f32_16x16x32_bf16 v[112:115], a[248:251], v[88:91], v[112:115]\n"
"v_mfma_f32_16x16x32_bf16 v[116:119], a[252:255], v[100:103], 0\n"
"v_mfma_f32_16x16x32_bf16 v[116:119], v[20:23], v[68:71], v[116:119]\n"
"s_waitcnt lgkmcnt(0)\n"
"v_mfma_f32_16x16x32_bf16 v[116:119], v[24:27], v[72:75], v[116:119]\n"
"s_barrier\n"
"v_mfma_f32_16x16x32_bf16 v[116:119], v[28:31], v[76:79], v[116:119]\n"
"ds_read_b128 v[100:103], v2 offset:2048\n"
"ds_read_b128 v[120:123], v3 offset:512\n"
"v_mfma_f32_16x16x32_bf16 v[116:119], v[32:35], v[80:83], v[116:119]\n"
"ds_read_b128 v[124:127], v4 offset:3072\n"
"v_mfma_f32_16x16x32_bf16 v[116:119], v[36:39], v[84:87], v[116:119]\n"
"ds_read_b128 v[128:131], v2 offset:2304\n"
"ds_read_b128 v[132:135], v3 offset:768\n"
"v_mfma_f32_16x16x32_bf16 v[116:119], v[40:43], v[88:91], v[116:119]\n"
"ds_read_b128 v[136:139], v4 offset:3328\n"
"ds_read_b128 v[140:143], v2 offset:3584\n"
"ds_read_b128 v[144:147], v3 offset:1536\n"
"ds_read_b128 v[148:151], v4 offset:3584\n"
"ds_read_b128 v[152:155], v2 offset:3840\n"
"ds_read_b128 v[156:159], v3 offset:1792\n"
"ds_read_b128 v[160:163], v4 offset:3840\n"
"s_waitcnt lgkmcnt(9)\n"
"v_add_f32 v100, v100, v120\n"
"v_add_f32 v101, v101, v121\n"
"v_add_f32 v102, v102, v122\n"
"v_add_f32 v103, v103, v123\n"
"v_add_f32 v100, v100, v124\n"
"v_add_f32 v101, v101, v125\n"
"v_add_f32 v102, v102, v126\n"
"v_add_f32 v103, v103, v127\n"
"v_add_f32 v100, v100, v96\n"
"v_add_f32 v101, v101, v97\n"
"v_add_f32 v102, v102, v98\n"
"v_add_f32 v103, v103, v99\n"
"s_add_u32 s93, s50, 0x0\n"
"s_and_b32 s94, s93, 0xfbffffff\n"
"v_cvt_pk_bf16_f32 v124, v100, v101\n"
"v_cvt_pk_bf16_f32 v125, v102, v103\n"
"s_waitcnt lgkmcnt(6)\n"
"v_add_f32 v128, v128, v132\n"
"v_add_f32 v129, v129, v133\n"
"s_waitcnt vmcnt(0)\n"
"v_add_f32 v130, v130, v134\n"
"v_add_f32 v131, v131, v135\n"
"v_add_f32 v128, v128, v136\n"
"v_add_f32 v129, v129, v137\n"
"v_add_f32 v130, v130, v138\n"
"v_add_f32 v131, v131, v139\n"
"v_add_f32 v128, v128, v92\n"
"v_add_f32 v129, v129, v93\n"
"v_add_f32 v130, v130, v94\n"
"v_add_f32 v131, v131, v95\n"
"v_cvt_pk_bf16_f32 v126, v128, v129\n"
"buffer_store_dwordx2 v[0:1], v14, s[40:43], s95 offen offset:0 sc1\n"
"v_cvt_pk_bf16_f32 v127, v130, v131\n"
"buffer_store_dwordx4 v[124:127], v13, s[40:43], s94 offen sc1\n"
"s_waitcnt lgkmcnt(3)\n"
"v_add_f32 v140, v140, v144\n"
"v_add_f32 v141, v141, v145\n"
"v_add_f32 v142, v142, v146\n"
"v_add_f32 v143, v143, v147\n"
"v_add_f32 v140, v140, v148\n"
"v_add_f32 v141, v141, v149\n"
"v_add_f32 v142, v142, v150\n"
"v_add_f32 v143, v143, v151\n"
"v_add_f32 v140, v140, v112\n"
"v_add_f32 v141, v141, v113\n"
"v_add_f32 v142, v142, v114\n"
"v_add_f32 v143, v143, v115\n"
"v_cvt_pk_bf16_f32 v100, v140, v141\n"
"v_cvt_pk_bf16_f32 v101, v142, v143\n"
"s_waitcnt lgkmcnt(0)\n"
"v_add_f32 v152, v152, v156\n"
"v_add_f32 v153, v153, v157\n"
"v_add_f32 v154, v154, v158\n"
"v_add_f32 v155, v155, v159\n"
"v_add_f32 v152, v152, v160\n"
"v_add_f32 v153, v153, v161\n"
"v_add_f32 v154, v154, v162\n"
"v_add_f32 v155, v155, v163\n"
"v_add_f32 v152, v152, v116\n"
"v_add_f32 v153, v153, v117\n"
"v_add_f32 v154, v154, v118\n"
"v_add_f32 v155, v155, v119\n"
"v_cvt_pk_bf16_f32 v102, v152, v153\n"
"v_cvt_pk_bf16_f32 v103, v154, v155\n"
"buffer_store_dwordx4 v[100:103], v15, s[40:43], s94 offen sc1\n"
"s_waitcnt vmcnt(0)\n"
"buffer_store_dwordx2 v[0:1], v16, s[40:43], s93 offen offset:0 sc1\n"
"s_branch .LG_end\n"
".LG_t2:\n"
"v_mfma_f32_16x16x32_bf16 v[112:115], a[236:239], v[76:79], v[112:115]\n"
"v_mfma_f32_16x16x32_bf16 v[112:115], a[240:243], v[80:83], v[112:115]\n"
"v_mfma_f32_16x16x32_bf16 v[112:115], a[244:247], v[84:87], v[112:115]\n"
"v_mfma_f32_16x16x32_bf16 v[112:115], a[248:251], v[88:91], v[112:115]\n"
"v_mfma_f32_16x16x32_bf16 v[116:119], a[252:255], v[100:103], 0\n"
"v_mfma_f32_16x16x32_bf16 v[116:119], v[20:23], v[68:71], v[116:119]\n"
"s_waitcnt lgkmcnt(0)\n"
"v_mfma_f32_16x16x32_bf16 v[116:119], v[24:27], v[72:75], v[116:119]\n"
"s_barrier\n"
"v_mfma_f32_16x16x32_bf16 v[116:119], v[28:31], v[76:79], v[116:119]\n"
"ds_read_b128 v[100:103], v2 offset:2048\n"
"ds_read_b128 v[120:123], v3 offset:512\n"
"v_mfma_f32_16x16x32_bf16 v[116:119], v[32:35], v[80:83], v[116:119]\n"
"ds_read_b128 v[124:127], v4 offset:3072\n"
"v_mfma_f32_16x16x32_bf16 v[116:119], v[36:39], v[84:87], v[116:119]\n"
"ds_read_b128 v[128:131], v2 offset:2304\n"
"ds_read_b128 v[132:135], v3 offset:768\n"
"v_mfma_f32_16x16x32_bf16 v[116:119], v[40:43], v[88:91], v[116:119]\n"
"s_waitcnt vmcnt(8)\n"
"ds_read_b128 v[136:139], v4 offset:3328\n"
"v_mfma_f32_16x16x32_bf16 v[84:87], v[104:107], a[0:3], 0\n"
"ds_read_b128 v[140:143], v2 offset:3584\n"
"ds_read_b128 v[144:147], v3 offset:1536\n"
"v_mfma_f32_16x16x32_bf16 v[84:87], v[108:111], a[4:7], v[84:87]\n"
"s_waitcnt vmcnt(6)\n"
"ds_read_b128 v[148:151], v4 offset:3584\n"
"ds_read_b128 v[152:155], v2 offset:3840\n"
"v_mfma_f32_16x16x32_bf16 v[84:87], v[44:47], a[8:11], v[84:87]\n"
"ds_read_b128 v[156:159], v3 offset:1792\n"
"ds_read_b128 v[160:163], v4 offset:3840\n"
"v_mfma_f32_16x16x32_bf16 v[84:87], v[48:51], a[12:15], v[84:87]\n"
"s_waitcnt vmcnt(2)\n"
"v_mfma_f32_16x16x32_bf16 v[84:87], v[52:55], a[16:19], v[84:87]\n"
"s_waitcnt lgkmcnt(9)\n"
"v_add_f32 v100, v100, v120\n"
"v_mfma_f32_16x16x32_bf16 v[84:87], v[56:59], a[20:23], v[84:87]\n"
"s_waitcnt vmcnt(1)\n"
"v_add_f32 v101, v101, v121\n"
"v_add_f32 v102, v102, v122\n"
"v_mfma_f32_16x16x32_bf16 v[84:87], v[60:63], a[24:27], v[84:87]\n"
"v_add_f32 v103, v103, v123\n"
"v_add_f32 v100, v100, v124\n"
"v_mfma_f32_16x16x32_bf16 v[164:167], v[104:107], a[28:31], 0\n"
"v_mfma_f32_16x16x32_bf16 v[164:167], v[108:111], a[32:35], v[164:167]\n"
"ds_write_addtid_b32 v84 offset:2048\n"
"v_add_f32 v101, v101, v125\n"
"v_mfma_f32_16x16x32_bf16 v[164:167], v[44:47], a[36:39], v[164:167]\n"
"ds_write_addtid_b32 v85 offset:6688\n"
"v_add_f32 v102, v102, v126\n"
"v_mfma_f32_16x16x32_bf16 v[164:167], v[48:51], a[40:43], v[164:167]\n"
"ds_write_addtid_b32 v86 offset:11392\n"
"v_add_f32 v103, v103, v127\n"
"v_mfma_f32_16x16x32_bf16 v[164:167], v[52:55], a[44:47], v[164:167]\n"
"ds_write_addtid_b32 v87 offset:16032\n"
"v_add_f32 v100, v100, v96\n"
"v_mfma_f32_16x16x32_bf16 v[164:167], v[56:59], a[48:51], v[164:167]\n"
"v_add_f32 v101, v101, v97\n"
"v_mfma_f32_16x16x32_bf16 v[164:167], v[60:63], a[52:55], v[164:167]\n"
"v_add_f32 v102, v102, v98\n"
"v_add_f32 v103, v103, v99\n"
"v_mfma_f32_16x16x32_bf16 v[120:123], v[104:107], a[56:59], 0\n"
"s_add_u32 s93, s50, 0xffe80000\n"
"s_and_b32 s94, s93, 0xfbffffff\n"
"v_cvt_pk_bf16_f32 v124, v100, v101\n"
"v_mfma_f32_16x16x32_bf16 v[120:123], v[108:111], a[60:63], v[120:123]\n"
"ds_write_addtid_b32 v164 offset:2304\n"
"v_cvt_pk_bf16_f32 v125, v102, v103\n"
"v_mfma_f32_16x16x32_bf16 v[120:123], v[44:47], a[64:67], v[120:123]\n"
"s_waitcnt lgkmcnt(11)\n"
"ds_write_addtid_b32 v165 offset:6944\n"
"v_add_f32 v128, v128, v132\n"
"v_mfma_f32_16x16x32_bf16 v[120:123], v[48:51], a[68:71], v[120:123]\n"
"ds_write_addtid_b32 v166 offset:11648\n"
"v_add_f32 v129, v129, v133\n"
"v_mfma_f32_16x16x32_bf16 v[120:123], v[52:55], a[72:75], v[120:123]\n"
"s_waitcnt vmcnt(0)\n"
"ds_write_addtid_b32 v167 offset:16288\n"
"v_mfma_f32_16x16x32_bf16 v[120:123], v[56:59], a[76:79], v[120:123]\n"
"v_add_f32 v130, v130, v134\n"
"v_mfma_f32_16x16x32_bf16 v[120:123], v[60:63], a[80:83], v[120:123]\n"
"v_add_f32 v131, v131, v135\n"
"v_add_f32 v128, v128, v136\n"
"v_mfma_f32_16x16x32_bf16 v[96:99], v[104:107], a[84:87], 0\n"
"v_add_f32 v129, v129, v137\n"
"v_add_f32 v130, v130, v138\n"
"v_mfma_f32_16x16x32_bf16 v[96:99], v[108:111], a[88:91], v[96:99]\n"
"ds_write_addtid_b32 v120 offset:2560\n"
"v_add_f32 v131, v131, v139\n"
"v_mfma_f32_16x16x32_bf16 v[96:99], v[44:47], a[92:95], v[96:99]\n"
"ds_write_addtid_b32 v121 offset:7200\n"
"v_add_f32 v128, v128, v92\n"
"v_mfma_f32_16x16x32_bf16 v[96:99], v[48:51], a[96:99], v[96:99]\n"
"ds_write_addtid_b32 v122 offset:11904\n"
"v_add_f32 v129, v129, v93\n"
"v_mfma_f32_16x16x32_bf16 v[96:99], v[52:55], a[100:103], v[96:99]\n"
"ds_write_addtid_b32 v123 offset:16544\n"
"v_add_f32 v130, v130, v94\n"
"v_mfma_f32_16x16x32_bf16 v[96:99], v[56:59], a[104:107], v[96:99]\n"
"v_add_f32 v131, v131, v95\n"
"v_cvt_pk_bf16_f32 v126, v128, v129\n"
"v_mfma_f32_16x16x32_bf16 v[96:99], v[60:63], a[108:111], v[96:99]\n"
"buffer_store_dwordx2 v[0:1], v14, s[40:43], s95 offen offset:0 sc1\n"
"v_cvt_pk_bf16_f32 v127, v130, v131\n"
"v_mfma_f32_16x16x32_bf16 v[92:95], v[104:107], a[112:115], 0\n"
"buffer_store_dwordx4 v[124:127], v13, s[40:43], s94 offen sc1\n"
"v_mfma_f32_16x16x32_bf16 v[92:95], v[108:111], a[116:119], v[92:95]\n"
"s_waitcnt lgkmcnt(15)\n"
"ds_write_addtid_b32 v96 offset:2816\n"
"v_add_f32 v140, v140, v144\n"
"v_mfma_f32_16x16x32_bf16 v[92:95], v[44:47], a[120:123], v[92:95]\n"
"ds_write_addtid_b32 v97 offset:7456\n"
"v_add_f32 v141, v141, v145\n"
"v_mfma_f32_16x16x32_bf16 v[92:95], v[48:51], a[124:127], v[92:95]\n"
"ds_write_addtid_b32 v98 offset:12160\n"
"v_add_f32 v142, v142, v146\n"
"v_mfma_f32_16x16x32_bf16 v[92:95], v[52:55], a[128:131], v[92:95]\n"
"ds_write_addtid_b32 v99 offset:16800\n"
"v_add_f32 v143, v143, v147\n"
"v_mfma_f32_16x16x32_bf16 v[92:95], v[56:59], a[132:135], v[92:95]\n"
"v_add_f32 v140, v140, v148\n"
"v_add_f32 v141, v141, v149\n"
"v_mfma_f32_16x16x32_bf16 v[92:95], v[60:63], a[136:139], v[92:95]\n"
"v_add_f32 v142, v142, v150\n"
"v_add_f32 v143, v143, v151\n"
"v_mfma_f32_16x16x32_bf16 v[96:99], v[104:107], a[140:143], 0\n"
"v_add_f32 v140, v140, v112\n"
"v_add_f32 v141, v141, v113\n"
"v_mfma_f32_16x16x32_bf16 v[96:99], v[108:111], a[144:147], v[96:99]\n"
"ds_write_addtid_b32 v92 offset:3072\n"
"v_add_f32 v142, v142, v114\n"
"v_mfma_f32_16x16x32_bf16 v[96:99], v[44:47], a[148:151], v[96:99]\n"
"ds_write_addtid_b32 v93 offset:7712\n"
"v_add_f32 v143, v143, v115\n"
"v_mfma_f32_16x16x32_bf16 v[96:99], v[48:51], a[152:155], v[96:99]\n"
"ds_write_addtid_b32 v94 offset:12416\n"
"v_cvt_pk_bf16_f32 v100, v140, v141\n"
"v_mfma_f32_16x16x32_bf16 v[96:99], v[52:55], a[156:159], v[96:99]\n"
"ds_write_addtid_b32 v95 offset:17056\n"
"v_cvt_pk_bf16_f32 v101, v142, v143\n"
"v_mfma_f32_16x16x32_bf16 v[96:99], v[56:59], a[160:163], v[96:99]\n"
"s_waitcnt lgkmcnt(15)\n"
"v_add_f32 v152, v152, v156\n"
"v_add_f32 v153, v153, v157\n"
"v_mfma_f32_16x16x32_bf16 v[96:99], v[60:63], a[164:167], v[96:99]\n"
"v_add_f32 v154, v154, v158\n"
"v_add_f32 v155, v155, v159\n"
"v_mfma_f32_16x16x32_bf16 v[92:95], v[104:107], a[224:227], 0\n"
"v_add_f32 v152, v152, v160\n"
"v_add_f32 v153, v153, v161\n"
"v_mfma_f32_16x16x32_bf16 v[92:95], v[108:111], a[228:231], v[92:95]\n"
"ds_write_addtid_b32 v96 offset:3328\n"
"v_add_f32 v154, v154, v162\n"
"v_mfma_f32_16x16x32_bf16 v[92:95], v[44:47], a[232:235], v[92:95]\n"
"ds_write_addtid_b32 v97 offset:7968\n"
"v_add_f32 v155, v155, v163\n"
"v_mfma_f32_16x16x32_bf16 v[92:95], v[48:51], a[236:239], v[92:95]\n"
"ds_write_addtid_b32 v98 offset:12672\n"
"v_add_f32 v152, v152, v116\n"
"v_mfma_f32_16x16x32_bf16 v[92:95], v[52:55], a[240:243], v[92:95]\n"
"ds_write_addtid_b32 v99 offset:17312\n"
"v_add_f32 v153, v153, v117\n"
"v_mfma_f32_16x16x32_bf16 v[92:95], v[56:59], a[244:247], v[92:95]\n"
"v_add_f32 v154, v154, v118\n"
"v_add_f32 v155, v155, v119\n"
"v_mfma_f32_16x16x32_bf16 v[92:95], v[60:63], a[248:251], v[92:95]\n"
"v_cvt_pk_bf16_f32 v102, v152, v153\n"
"v_cvt_pk_bf16_f32 v103, v154, v155\n"
"v_mfma_f32_16x16x32_bf16 v[96:99], v[104:107], a[252:255], 0\n"
"buffer_store_dwordx4 v[100:103], v15, s[40:43], s94 offen sc1\n"
"v_mfma_f32_16x16x32_bf16 v[96:99], v[108:111], v[20:23], v[96:99]\n"
"ds_write_addtid_b32 v92 offset:3584\n"
"v_mfma_f32_16x16x32_bf16 v[96:99], v[44:47], v[24:27], v[96:99]\n"
"ds_write_addtid_b32 v93 offset:8224\n"
"v_mfma_f32_16x16x32_bf16 v[96:99], v[48:51], v[28:31], v[96:99]\n"
"ds_write_addtid_b32 v94 offset:12928\n"
"v_mfma_f32_16x16x32_bf16 v[96:99], v[52:55], v[32:35], v[96:99]\n"
"ds_write_addtid_b32 v95 offset:17568\n"
"v_mfma_f32_16x16x32_bf16 v[96:99], v[56:59], v[36:39], v[96:99]\n"
"v_mfma_f32_16x16x32_bf16 v[96:99], v[60:63], v[40:43], v[96:99]\n"
"v_mfma_f32_16x16x32_bf16 v[100:103], a[168:171], v[104:107], 0\n"
"v_mfma_f32_16x16x32_bf16 v[100:103], a[172:175], v[108:111], v[100:103]\n"
"ds_write_addtid_b32 v96 offset:3840\n"
"v_mfma_f32_16x16x32_bf16 v[100:103], a[176:179], v[44:47], v[100:103]\n"
"ds_write_addtid_b32 v97 offset:8480\n"
"v_mfma_f32_16x16x32_bf16 v[100:103], a[180:183], v[48:51], v[100:103]\n"
"ds_write_addtid_b32 v98 offset:13184\n"
"v_mfma_f32_16x16x32_bf16 v[100:103], a[184:187], v[52:55], v[100:103]\n"
"ds_write_addtid_b32 v99 offset:17824\n"
"v_mfma_f32_16x16x32_bf16 v[100:103], a[188:191], v[56:59], v[100:103]\n"
"v_mfma_f32_16x16x32_bf16 v[100:103], a[192:195], v[60:63], v[100:103]\n"
"v_mfma_f32_16x16x32_bf16 v[112:115], a[196:199], v[104:107], 0\n"
"v_mfma_f32_16x16x32_bf16 v[112:115], a[200:203], v[108:111], v[112:115]\n"
"s_waitcnt lgkmcnt(0)\n"
"v_mfma_f32_16x16x32_bf16 v[112:115], a[204:207], v[44:47], v[112:115]\n"
"s_barrier\n"
"v_mfma_f32_16x16x32_bf16 v[112:115], a[208:211], v[48:51], v[112:115]\n"
"ds_read_b128 v[96:99], v2 offset:0\n"
"ds_read_b128 v[104:107], v3 offset:2560\n"
"v_mfma_f32_16x16x32_bf16 v[112:115], a[212:215], v[52:55], v[112:115]\n"
"ds_read_b128 v[108:111], v4 offset:1024\n"
"ds_read_b128 v[116:119], v2 offset:256\n"
"v_mfma_f32_16x16x32_bf16 v[112:115], a[216:219], v[56:59], v[112:115]\n"
"ds_read_b128 v[120:123], v3 offset:2816\n"
"v_mfma_f32_16x16x32_bf16 v[112:115], a[220:223], v[60:63], v[112:115]\n"
"ds_read_b128 v[124:127], v4 offset:1280\n"
"s_waitcnt lgkmcnt(3)\n"
"v_add_f32 v96, v96, v104\n"
"v_add_f32 v97, v97, v105\n"
"v_add_f32 v98, v98, v106\n"
"v_add_f32 v99, v99, v107\n"
"v_add_f32 v96, v96, v108\n"
"v_add_f32 v97, v97, v109\n"
"v_add_f32 v98, v98, v110\n"
"v_add_f32 v99, v99, v111\n"
"v_add_f32 v96, v96, v100\n"
"v_add_f32 v97, v97, v101\n"
"v_add_f32 v98, v98, v102\n"
"v_add_f32 v99, v99, v103\n"
"s_add_u32 s95, s50, 0x0\n"
"s_and_b32 s94, s95, 0xfbffffff\n"
"v_cvt_pk_bf16_f32 v108, v96, v97\n"
"v_cvt_pk_bf16_f32 v109, v98, v99\n"
"s_waitcnt lgkmcnt(0)\n"
"v_add_f32 v116, v116, v120\n"
"v_add_f32 v117, v117, v121\n"
"v_add_f32 v118, v118, v122\n"
"v_add_f32 v119, v119, v123\n"
"v_add_f32 v116, v116, v124\n"
"v_add_f32 v117, v117, v125\n"
"v_add_f32 v118, v118, v126\n"
"v_add_f32 v119, v119, v127\n"
"v_add_f32 v116, v116, v112\n"
"v_add_f32 v117, v117, v113\n"
"v_add_f32 v118, v118, v114\n"
"v_add_f32 v119, v119, v115\n"
"v_cvt_pk_bf16_f32 v110, v116, v117\n"
"v_cvt_pk_bf16_f32 v111, v118, v119\n"
"s_waitcnt vmcnt(0)\n"
"buffer_store_dwordx2 v[0:1], v16, s[40:43], s93 offen offset:0 sc1\n"
"buffer_store_dwordx4 v[108:111], v13, s[40:43], s94 offen sc1\n"
"s_waitcnt vmcnt(0)\n"
"buffer_store_dwordx2 v[0:1], v14, s[40:43], s95 offen offset:0 sc1\n"
"s_branch .LG_end\n"
".LG_t3:\n"
"v_mfma_f32_16x16x32_bf16 v[100:103], a[192:195], v[60:63], v[100:103]\n"
"v_mfma_f32_16x16x32_bf16 v[112:115], a[196:199], v[104:107], 0\n"
"v_mfma_f32_16x16x32_bf16 v[112:115], a[200:203], v[108:111], v[112:115]\n"
"s_waitcnt lgkmcnt(0)\n"
"v_mfma_f32_16x16x32_bf16 v[112:115], a[204:207], v[44:47], v[112:115]\n"
"s_barrier\n"
"v_mfma_f32_16x16x32_bf16 v[112:115], a[208:211], v[48:51], v[112:115]\n"
"ds_read_b128 v[96:99], v2 offset:0\n"
"ds_read_b128 v[104:107], v3 offset:2560\n"
"v_mfma_f32_16x16x32_bf16 v[112:115], a[212:215], v[52:55], v[112:115]\n"
"ds_read_b128 v[108:111], v4 offset:1024\n"
"ds_read_b128 v[116:119], v2 offset:256\n"
"v_mfma_f32_16x16x32_bf16 v[112:115], a[216:219], v[56:59], v[112:115]\n"
"ds_read_b128 v[120:123], v3 offset:2816\n"
"v_mfma_f32_16x16x32_bf16 v[112:115], a[220:223], v[60:63], v[112:115]\n"
"s_waitcnt vmcnt(9)\n"
"ds_read_b128 v[124:127], v4 offset:1280\n"
"v_mfma_f32_16x16x32_bf16 v[56:59], v[64:67], a[0:3], 0\n"
"v_mfma_f32_16x16x32_bf16 v[56:59], v[68:71], a[4:7], v[56:59]\n"
"s_waitcnt vmcnt(7)\n"
"v_mfma_f32_16x16x32_bf16 v[56:59], v[72:75], a[8:11], v[56:59]\n"
"v_mfma_f32_16x16x32_bf16 v[56:59], v[76:79], a[12:15], v[56:59]\n"
"s_waitcnt vmcnt(4)\n"
"v_mfma_f32_16x16x32_bf16 v[56:59], v[80:83], a[16:19], v[56:59]\n"
"s_waitcnt lgkmcnt(3)\n"
"v_add_f32 v96, v96, v104\n"
"v_add_f32 v97, v97, v105\n"
"v_mfma_f32_16x16x32_bf16 v[56:59], v[84:87], a[20:23], v[56:59]\n"
"s_waitcnt vmcnt(3)\n"
"v_add_f32 v98, v98, v106\n"
"v_add_f32 v99, v99, v107\n"
"v_mfma_f32_16x16x32_bf16 v[56:59], v[88:91], a[24:27], v[56:59]\n"
"v_add_f32 v96, v96, v108\n"
"v_add_f32 v97, v97, v109\n"
"v_mfma_f32_16x16x32_bf16 v[104:107], v[64:67], a[28:31], 0\n"
"v_add_f32 v98, v98, v110\n"
"v_mfma_f32_16x16x32_bf16 v[104:107], v[68:71], a[32:35], v[104:107]\n"
"ds_write_addtid_b32 v56 offset:0\n"
"v_add_f32 v99, v99, v111\n"
"v_mfma_f32_16x16x32_bf16 v[104:107], v[72:75], a[36:39], v[104:107]\n"
"ds_write_addtid_b32 v57 offset:4640\n"
"v_add_f32 v96, v96, v100\n"
"v_mfma_f32_16x16x32_bf16 v[104:107], v[76:79], a[40:43], v[104:107]\n"
"ds_write_addtid_b32 v58 offset:9344\n"
"v_add_f32 v97, v97, v101\n"
"v_mfma_f32_16x16x32_bf16 v[104:107], v[80:83], a[44:47], v[104:107]\n"
"ds_write_addtid_b32 v59 offset:13984\n"
"v_add_f32 v98, v98, v102\n"
"v_mfma_f32_16x16x32_bf16 v[104:107], v[84:87], a[48:51], v[104:107]\n"
"v_add_f32 v99, v99, v103\n"
"v_mfma_f32_16x16x32_bf16 v[104:107], v[88:91], a[52:55], v[104:107]\n"
"s_add_u32 s95, s50, 0xffe80000\n"
"s_and_b32 s94, s95, 0xfbffffff\n"
"v_mfma_f32_16x16x32_bf16 v[100:103], v[64:67], a[56:59], 0\n"
"v_cvt_pk_bf16_f32 v108, v96, v97\n"
"v_cvt_pk_bf16_f32 v109, v98, v99\n"
"v_mfma_f32_16x16x32_bf16 v[100:103], v[68:71], a[60:63], v[100:103]\n"
"s_waitcnt lgkmcnt(4)\n"
"ds_write_addtid_b32 v104 offset:256\n"
"v_add_f32 v116, v116, v120\n"
"v_mfma_f32_16x16x32_bf16 v[100:103], v[72:75], a[64:67], v[100:103]\n"
"ds_write_addtid_b32 v105 offset:4896\n"
"v_add_f32 v117, v117, v121\n"
"v_mfma_f32_16x16x32_bf16 v[100:103], v[76:79], a[68:71], v[100:103]\n"
"ds_write_addtid_b32 v106 offset:9600\n"
"v_mfma_f32_16x16x32_bf16 v[100:103], v[80:83], a[72:75], v[100:103]\n"
"ds_write_addtid_b32 v107 offset:14240\n"
"v_add_f32 v118, v118, v122\n"
"v_mfma_f32_16x16x32_bf16 v[100:103], v[84:87], a[76:79], v[100:103]\n"
"v_add_f32 v119, v119, v123\n"
"v_add_f32 v116, v116, v124\n"
"v_mfma_f32_16x16x32_bf16 v[100:103], v[88:91], a[80:83], v[100:103]\n"
"v_add_f32 v117, v117, v125\n"
"v_add_f32 v118, v118, v126\n"
"v_mfma_f32_16x16x32_bf16 v[104:107], v[64:67], a[84:87], 0\n"
"v_add_f32 v119, v119, v127\n"
"v_add_f32 v116, v116, v112\n"
"v_mfma_f32_16x16x32_bf16 v[104:107], v[68:71], a[88:91], v[104:107]\n"
"ds_write_addtid_b32 v100 offset:512\n"
"v_add_f32 v117, v117, v113\n"
"v_mfma_f32_16x16x32_bf16 v[104:107], v[72:75], a[92:95], v[104:107]\n"
"ds_write_addtid_b32 v101 offset:5152\n"
"v_mfma_f32_16x16x32_bf16 v[104:107], v[76:79], a[96:99], v[104:107]\n"
"ds_write_addtid_b32 v102 offset:9856\n"
"v_add_f32 v118, v118, v114\n"
"v_mfma_f32_16x16x32_bf16 v[104:107], v[80:83], a[100:103], v[104:107]\n"
"ds_write_addtid_b32 v103 offset:14496\n"
"v_add_f32 v119, v119, v115\n"
"v_mfma_f32_16x16x32_bf16 v[104:107], v[84:87], a[104:107], v[104:107]\n"
"v_cvt_pk_bf16_f32 v110, v116, v117\n"
"v_cvt_pk_bf16_f32 v111, v118, v119\n"
"v_mfma_f32_16x16x32_bf16 v[104:107], v[88:91], a[108:111], v[104:107]\n"
"s_waitcnt vmcnt(0)\n"
"buffer_store_dwordx2 v[0:1], v16, s[40:43], s93 offen offset:0 sc1\n"
"v_mfma_f32_16x16x32_bf16 v[100:103], v[64:67], a[112:115], 0\n"
"v_mfma_f32_16x16x32_bf16 v[100:103], v[68:71], a[116:119], v[100:103]\n"
"ds_write_addtid_b32 v104 offset:768\n"
"v_mfma_f32_16x16x32_bf16 v[100:103], v[72:75], a[120:123], v[100:103]\n"
"buffer_store_dwordx4 v[108:111], v13, s[40:43], s94 offen sc1\n"
"ds_write_addtid_b32 v105 offset:5408\n"
"v_mfma_f32_16x16x32_bf16 v[100:103], v[76:79], a[124:127], v[100:103]\n"
"ds_write_addtid_b32 v106 offset:10112\n"
"v_mfma_f32_16x16x32_bf16 v[100:103], v[80:83], a[128:131], v[100:103]\n"
"ds_write_addtid_b32 v107 offset:14752\n"
"v_mfma_f32_16x16x32_bf16 v[100:103], v[84:87], a[132:135], v[100:103]\n"
"v_mfma_f32_16x16x32_bf16 v[100:103], v[88:91], a[136:139], v[100:103]\n"
"v_mfma_f32_16x16x32_bf16 v[108:111], v[64:67], a[140:143], 0\n"
"v_mfma_f32_16x16x32_bf16 v[108:111], v[68:71], a[144:147], v[108:111]\n"
"ds_write_addtid_b32 v100 offset:1024\n"
"v_mfma_f32_16x16x32_bf16 v[108:111], v[72:75], a[148:151], v[108:111]\n"
"ds_write_addtid_b32 v101 offset:5664\n"
"v_mfma_f32_16x16x32_bf16 v[108:111], v[76:79], a[152:155], v[108:111]\n"
"ds_write_addtid_b32 v102 offset:10368\n"
"v_mfma_f32_16x16x32_bf16 v[108:111], v[80:83], a[156:159], v[108:111]\n"
"ds_write_addtid_b32 v103 offset:15008\n"
"v_mfma_f32_16x16x32_bf16 v[108:111], v[84:87], a[160:163], v[108:111]\n"
"v_mfma_f32_16x16x32_bf16 v[108:111], v[88:91], a[164:167], v[108:111]\n"
"v_mfma_f32_16x16x32_bf16 v[104:107], v[64:67], a[224:227], 0\n"
"v_mfma_f32_16x16x32_bf16 v[104:107], v[68:71], a[228:231], v[104:107]\n"
"ds_write_addtid_b32 v108 offset:1280\n"
"v_mfma_f32_16x16x32_bf16 v[104:107], v[72:75], a[232:235], v[104:107]\n"
"ds_write_addtid_b32 v109 offset:5920\n"
"v_mfma_f32_16x16x32_bf16 v[104:107], v[76:79], a[236:239], v[104:107]\n"
"ds_write_addtid_b32 v110 offset:10624\n"
"v_mfma_f32_16x16x32_bf16 v[104:107], v[80:83], a[240:243], v[104:107]\n"
"ds_write_addtid_b32 v111 offset:15264\n"
"v_mfma_f32_16x16x32_bf16 v[104:107], v[84:87], a[244:247], v[104:107]\n"
"v_mfma_f32_16x16x32_bf16 v[104:107], v[88:91], a[248:251], v[104:107]\n"
"v_mfma_f32_16x16x32_bf16 v[112:115], v[64:67], a[252:255], 0\n"
"v_mfma_f32_16x16x32_bf16 v[112:115], v[68:71], v[20:23], v[112:115]\n"
"ds_write_addtid_b32 v104 offset:1536\n"
"v_mfma_f32_16x16x32_bf16 v[112:115], v[72:75], v[24:27], v[112:115]\n"
"ds_write_addtid_b32 v105 offset:6176\n"
"v_mfma_f32_16x16x32_bf16 v[112:115], v[76:79], v[28:31], v[112:115]\n"
"ds_write_addtid_b32 v106 offset:10880\n"
"v_mfma_f32_16x16x32_bf16 v[112:115], v[80:83], v[32:35], v[112:115]\n"
"ds_write_addtid_b32 v107 offset:15520\n"
"v_mfma_f32_16x16x32_bf16 v[112:115], v[84:87], v[36:39], v[112:115]\n"
"v_mfma_f32_16x16x32_bf16 v[112:115], v[88:91], v[40:43], v[112:115]\n"
"v_mfma_f32_16x16x32_bf16 v[104:107], a[168:171], v[64:67], 0\n"
"v_mfma_f32_16x16x32_bf16 v[104:107], a[172:175], v[68:71], v[104:107]\n"
"ds_write_addtid_b32 v112 offset:1792\n"
"v_mfma_f32_16x16x32_bf16 v[104:107], a[176:179], v[72:75], v[104:107]\n"
"ds_write_addtid_b32 v113 offset:6432\n"
"v_mfma_f32_16x16x32_bf16 v[104:107], a[180:183], v[76:79], v[104:107]\n"
"ds_write_addtid_b32 v114 offset:11136\n"
"v_mfma_f32_16x16x32_bf16 v[104:107], a[184:187], v[80:83], v[104:107]\n"
"ds_write_addtid_b32 v115 offset:15776\n"
"v_mfma_f32_16x16x32_bf16 v[104:107], a[188:191], v[84:87], v[104:107]\n"
"v_mfma_f32_16x16x32_bf16 v[104:107], a[192:195], v[88:91], v[104:107]\n"
"v_mfma_f32_16x16x32_bf16 v[108:111], a[196:199], v[64:67], 0\n"
"v_mfma_f32_16x16x32_bf16 v[108:111], a[200:203], v[68:71], v[108:111]\n"
"s_waitcnt lgkmcnt(0)\n"
"v_mfma_f32_16x16x32_bf16 v[108:111], a[204:207], v[72:75], v[108:111]\n"
"s_barrier\n"
"v_mfma_f32_16x16x32_bf16 v[108:111], a[208:211], v[76:79], v[108:111]\n"
"ds_read_b128 v[64:67], v2 offset:2048\n"
"v_mfma_f32_16x16x32_bf16 v[108:111], a[212:215], v[80:83], v[108:111]\n"
"ds_read_b128 v[112:115], v3 offset:512\n"
"ds_read_b128 v[116:119], v4 offset:3072\n"
"v_mfma_f32_16x16x32_bf16 v[108:111], a[216:219], v[84:87], v[108:111]\n"
"ds_read_b128 v[120:123], v2 offset:2304\n"
"ds_read_b128 v[124:127], v3 offset:768\n"
"v_mfma_f32_16x16x32_bf16 v[108:111], a[220:223], v[88:91], v[108:111]\n"
"ds_read_b128 v[128:131], v4 offset:3328\n"
"s_waitcnt lgkmcnt(3)\n"
"v_add_f32 v64, v64, v112\n"
"v_add_f32 v65, v65, v113\n"
"v_add_f32 v66, v66, v114\n"
"v_add_f32 v67, v67, v115\n"
"v_add_f32 v64, v64, v116\n"
"s_waitcnt vmcnt(0)\n"
"buffer_store_dwordx2 v[0:1], v14, s[40:43], s95 offen offset:0 sc1\n"
"v_add_f32 v65, v65, v117\n"
"v_add_f32 v66, v66, v118\n"
"v_add_f32 v67, v67, v119\n"
"v_add_f32 v64, v64, v104\n"
"v_add_f32 v65, v65, v105\n"
"v_add_f32 v66, v66, v106\n"
"v_add_f32 v67, v67, v107\n"
"s_add_u32 s93, s50, 0x0\n"
"s_and_b32 s95, s93, 0xfbffffff\n"
"v_cvt_pk_bf16_f32 v104, v64, v65\n"
"v_cvt_pk_bf16_f32 v105, v66, v67\n"
"s_waitcnt lgkmcnt(0)\n"
"v_add_f32 v120, v120, v124\n"
"v_add_f32 v121, v121, v125\n"
"v_add_f32 v122, v122, v126\n"
"v_add_f32 v123, v123, v127\n"
"v_add_f32 v120, v120, v128\n"
"v_add_f32 v121, v121, v129\n"
"v_add_f32 v122, v122, v130\n"
"v_add_f32 v123, v123, v131\n"
"v_add_f32 v120, v120, v108\n"
"v_add_f32 v121, v121, v109\n"
"v_add_f32 v122, v122, v110\n"
"v_add_f32 v123, v123, v111\n"
"v_cvt_pk_bf16_f32 v106, v120, v121\n"
"v_cvt_pk_bf16_f32 v107, v122, v123\n"
"buffer_store_dwordx4 v[104:107], v13, s[40:43], s95 offen sc1\n"
"s_waitcnt vmcnt(0)\n"
"buffer_store_dwordx2 v[0:1], v14, s[40:43], s93 offen offset:0 sc1\n"
"s_branch .LG_end\n"
".LG_end:\n"
"s_waitcnt vmcnt(0) lgkmcnt(0)\n"
"s_endpgm\n"
      :: "{v2}"((unsigned)(lds0 + ((wid + 3) & 3) * 18592 + trp)), "{v3}"((unsigned)(lds0 + ((wid + 2) & 3) * 18592 + trp)), "{v4}"((unsigned)(lds0 + ((wid + 1) & 3) * 18592 + trp)), "{v5}"((unsigned)(lane * 16 + f0 * 1024 + ((0 + rot) % 7) * 1024 + 134316032)), "{v6}"((unsigned)(lane * 16 + f0 * 1024 + ((1 + rot) % 7) * 1024 + 134316032)), "{v7}"((unsigned)(lane * 16 + f0 * 1024 + ((2 + rot) % 7) * 1024 + 134316032)), "{v8}"((unsigned)(lane * 16 + f0 * 1024 + ((3 + rot) % 7) * 1024 + 134316032)), "{v9}"((unsigned)(lane * 16 + f0 * 1024 + ((4 + rot) % 7) * 1024 + 134316032)), "{v10}"((unsigned)(lane * 16 + f0 * 1024 + ((5 + rot) % 7) * 1024 + 134316032)), "{v11}"((unsigned)(lane * 16 + f0 * 1024 + ((6 + rot) % 7) * 1024 + 134316032)), "{v12}"((unsigned)((f0 + lane % 7) * 8 + kq * 32 + 353024 + G_GL * 1572864)), "{v13}"((unsigned)(lane * 16 + pair * 4096 + kq * 1024 + 212992)), "{v14}"((unsigned)(lane ? DROP : pair * 64 + kq * 8 + 354304)), "{v15}"((unsigned)(lane * 16 + (pg0 + 4) * 4096 + kq * 1024 + 212992)), "{v16}"((unsigned)(lane > 1 ? DROP : (lane ? pg0 + 4 : pair) * 64 + kq * 8 + 354304)), "{v44}"((unsigned)(vT + tsw)), "{v45}"((unsigned)(vT + (tsw ^ 8))), "{v46}"((unsigned)(vD + ((0 + rot) % 7) * 65536)), "{v47}"((unsigned)(vD + ((1 + rot) % 7) * 65536)), "{v48}"((unsigned)(vD + ((2 + rot) % 7) * 65536)), "{v49}"((unsigned)(vD + ((3 + rot) % 7) * 65536)), "{v50}"((unsigned)(vD + ((4 + rot) % 7) * 65536)), "{v51}"((unsigned)(vD + ((5 + rot) % 7) * 65536)), "{v52}"((unsigned)(vD + ((6 + rot) % 7) * 65536)), "{s[40:43]}"(rS), "{s[44:47]}"(rW), "{s[48:49]}"(wsp), "{s50}"((unsigned)(sA)), "{s51}"((unsigned)(sLo)), "{s52}"((unsigned)(sHi)), "{s53}"((unsigned)(sEx)), "{s54}"((unsigned)(sEnt)), "{s55}"((unsigned)(sLd)), "{s[56:57]}"(wsb + (sLo & 0x3ffffff)), "{s59}"((unsigned)(lds0 + wid * 18592)), "{s62}"((unsigned)(sW0 + 64 * ((wid + 1) & 3))), "{s63}"((unsigned)(sW0 + 64 * ((wid + 2) & 3))), "{s64}"((unsigned)(sW0 + 64 * ((wid + 3) & 3))), "{s65}"((unsigned)(sW0 + 64 * ((wid + 4) & 3))), "{s66}"((unsigned)(sW0 + 256))
      : "v255", "a255", "s95", "vcc", "scc", "memory");

    } else {
      const int v16 = kq * 4 + wid;
      const unsigned sEx = sHi + (J_EX + (set == 1 && !J_TL)) * 3 * RS, sEnt = grp - 4;
      const u32x4 rO = desc(a.out, 2048 * 2048), bia0 = *(const u32x4*)(a.b2 + v16 * (32 << JJ) + lg * 8), bia1 = *(const u32x4*)(a.b2 + (JJ ? 32 : 512) + v16 * (32 << JJ) + lg * 8);
      const unsigned se = lg == (li >> 2) ? 0x3f80u << (16 * (li & 1)) : 0u, sel0 = (li & 2) ? 0u : se, sel1 = (li & 2) ? se : 0u;
      const u64 ep = (set == 2 && idx == 27 && wid == 0) ? (u64)a.ws : 0ull;
    asm volatile(
"s_nop 4\n"
"v_mov_b32 v186, 0\n"
"global_load_dwordx2 v[0:1], v186, s[48:49] sc1\n"
"v_mov_b32 v188, 0x3f803f80\n"
"v_mov_b32 v189, 0x3f803f80\n"
"v_mov_b32 v190, 0x3f803f80\n"
"v_mov_b32 v191, 0x3f803f80\n"
"s_add_u32 m0, s55, 0\n"
"s_add_u32 s60, s86, 0\n"
"buffer_load_dwordx4 v46, s[44:47], s60 offen lds\n"
"s_add_u32 m0, s55, 1024\n"
"s_add_u32 s60, s86, 32768\n"
"buffer_load_dwordx4 v46, s[44:47], s60 offen lds\n"
"s_add_u32 m0, s55, 2048\n"
"s_add_u32 s60, s87, 0\n"
"buffer_load_dwordx4 v46, s[44:47], s60 offen lds\n"
"s_add_u32 m0, s55, 3072\n"
"s_add_u32 s60, s87, 32768\n"
"buffer_load_dwordx4 v46, s[44:47], s60 offen lds\n"
"s_add_u32 m0, s55, 4096\n"
"s_add_u32 s60, s88, 0\n"
"buffer_load_dwordx4 v46, s[44:47], s60 offen lds\n"
"s_add_u32 m0, s55, 5120\n"
"s_add_u32 s60, s88, 32768\n"
"buffer_load_dwordx4 v46, s[44:47], s60 offen lds\n"
"s_add_u32 m0, s55, 6144\n"
"s_add_u32 s60, s89, 0\n"
"buffer_load_dwordx4 v46, s[44:47], s60 offen lds\n"
"s_add_u32 m0, s55, 7168\n"
"s_add_u32 s60, s89, 32768\n"
"buffer_load_dwordx4 v46, s[44:47], s60 offen lds\n"
"s_add_u32 m0, s55, 8192\n"
"s_add_u32 s60, s86, 0\n"
"buffer_load_dwordx4 v47, s[44:47], s60 offen lds\n"
"s_add_u32 m0, s55, 9216\n"
"s_add_u32 s60, s86, 32768\n"
"buffer_load_dwordx4 v47, s[44:47], s60 offen lds\n"
"s_add_u32 m0, s55, 10240\n"
"s_add_u32 s60, s87, 0\n"
"buffer_load_dwordx4 v47, s[44:47], s60 offen lds\n"
"s_add_u32 m0, s55, 11264\n"
"s_add_u32 s60, s87, 32768\n"
"buffer_load_dwordx4 v47, s[44:47], s60 offen lds\n"
"s_add_u32 m0, s55, 12288\n"
"s_add_u32 s60, s88, 0\n"
"buffer_load_dwordx4 v47, s[44:47], s60 offen lds\n"
"s_add_u32 m0, s55, 13312\n"
"s_add_u32 s60, s88, 32768\n"
"buffer_load_dwordx4 v47, s[44:47], s60 offen lds\n"
"s_add_u32 m0, s55, 14336\n"
"s_add_u32 s60, s89, 0\n"
"buffer_load_dwordx4 v47, s[44:47], s60 offen lds\n"
"s_add_u32 m0, s55, 15360\n"
"s_add_u32 s60, s89, 32768\n"
"buffer_load_dwordx4 v47, s[44:47], s60 offen lds\n"
"s_add_u32 m0, s55, 16384\n"
"s_add_u32 s60, s86, 0\n"
"buffer_load_dwordx4 v48, s[44:47], s60 offen lds\n"
"s_add_u32 m0, s55, 17408\n"
"s_add_u32 s60, s86, 32768\n"
"buffer_load_dwordx4 v48, s[44:47], s60 offen lds\n"
"s_add_u32 m0, s55, 18432\n"
"s_add_u32 s60, s87, 0\n"
"buffer_load_dwordx4 v48, s[44:47], s60 offen lds\n"
"s_add_u32 m0, s55, 19456\n"
"s_add_u32 s60, s87, 32768\n"
"buffer_load_dwordx4 v48, s[44:47], s60 offen lds\n"
"s_add_u32 m0, s55, 20480\n"
"s_add_u32 s60, s88, 0\n"
"buffer_load_dwordx4 v48, s[44:47], s60 offen lds\n"
"s_add_u32 m0, s55, 21504\n"
"s_add_u32 s60, s88, 32768\n"
"buffer_load_dwordx4 v48, s[44:47], s60 offen lds\n"
"s_add_u32 m0, s55, 22528\n"
"s_add_u32 s60, s89, 0\n"
"buffer_load_dwordx4 v48, s[44:47], s60 offen lds\n"
"s_add_u32 m0, s55, 23552\n"
"s_add_u32 s60, s89, 32768\n"
"buffer_load_dwordx4 v48, s[44:47], s60 offen lds\n"
"s_add_u32 m0, s55, 24576\n"
"s_add_u32 s60, s86, 0\n"
"buffer_load_dwordx4 v49, s[44:47], s60 offen lds\n"
"s_add_u32 m0, s55, 25600\n"
"s_add_u32 s60, s86, 32768\n"
"buffer_load_dwordx4 v49, s[44:47], s60 offen lds\n"
"s_add_u32 m0, s55, 26624\n"
"s_add_u32 s60, s87, 0\n"
"buffer_load_dwordx4 v49, s[44:47], s60 offen lds\n"
"s_add_u32 m0, s55, 27648\n"
"s_add_u32 s60, s87, 32768\n"
"buffer_load_dwordx4 v49, s[44:47], s60 offen lds\n"
"s_add_u32 m0, s55, 28672\n"
"s_add_u32 s60, s88, 0\n"
"buffer_load_dwordx4 v49, s[44:47], s60 offen lds\n"
"s_add_u32 m0, s55, 29696\n"
"s_add_u32 s60, s88, 32768\n"
"buffer_load_dwordx4 v49, s[44:47], s60 offen lds\n"
"s_add_u32 m0, s55, 30720\n"
"s_add_u32 s60, s89, 0\n"
"buffer_load_dwordx4 v49, s[44:47], s60 offen lds\n"
"s_add_u32 m0, s55, 31744\n"
"s_add_u32 s60, s89, 32768\n"
"buffer_load_dwordx4 v49, s[44:47], s60 offen lds\n"
"s_add_u32 m0, s55, 32768\n"
"s_add_u32 s60, s86, 0\n"
"buffer_load_dwordx4 v50, s[44:47], s60 offen lds\n"
"s_add_u32 m0, s55, 33792\n"
"s_add_u32 s60, s86, 32768\n"
"buffer_load_dwordx4 v50, s[44:47], s60 offen lds\n"
"s_add_u32 m0, s55, 34816\n"
"s_add_u32 s60, s87, 0\n"
"buffer_load_dwordx4 v50, s[44:47], s60 offen lds\n"
"s_add_u32 m0, s55, 35840\n"
"s_add_u32 s60, s87, 32768\n"
"buffer_load_dwordx4 v50, s[44:47], s60 offen lds\n"
"s_add_u32 m0, s55, 36864\n"
"s_add_u32 s60, s88, 0\n"
"buffer_load_dwordx4 v50, s[44:47], s60 offen lds\n"
"s_add_u32 m0, s55, 37888\n"
"s_add_u32 s60, s88, 32768\n"
"buffer_load_dwordx4 v50, s[44:47], s60 offen lds\n"
"s_add_u32 m0, s55, 38912\n"
"s_add_u32 s60, s89, 0\n"
"buffer_load_dwordx4 v50, s[44:47], s60 offen lds\n"
"s_add_u32 m0, s55, 39936\n"
"s_add_u32 s60, s89, 32768\n"
"buffer_load_dwordx4 v50, s[44:47], s60 offen lds\n"
"s_waitcnt vmcnt(38)\n"
"ds_read_b64_tr_b16 a[0:1], v44 offset:0\n"
"ds_read_b64_tr_b16 a[2:3], v44 offset:256\n"
"ds_read_b64_tr_b16 a[28:29], v45 offset:0\n"
"ds_read_b64_tr_b16 a[30:31], v45 offset:256\n"
"v_add_u32 v0, 2, v0\n"
"v_add_u32 v1, 0x9e3779b9, v1\n"
"v_or_b32 v0, 1, v0\n"
"v_readfirstlane_b32 s49, v1\n"
"v_readfirstlane_b32 s48, v0\n"
"s_waitcnt vmcnt(36)\n"
"ds_read_b64_tr_b16 a[56:57], v44 offset:2048\n"
"ds_read_b64_tr_b16 a[58:59], v44 offset:2304\n"
"ds_read_b64_tr_b16 a[84:85], v45 offset:2048\n"
"ds_read_b64_tr_b16 a[86:87], v45 offset:2304\n"
"s_waitcnt lgkmcnt(4)\n"
"s_add_u32 m0, s55, 0\n"
"s_add_u32 s60, s86, 0\n"
"buffer_load_dwordx4 v51, s[44:47], s60 offen lds\n"
"s_add_u32 m0, s55, 1024\n"
"s_add_u32 s60, s86, 32768\n"
"buffer_load_dwordx4 v51, s[44:47], s60 offen lds\n"
"s_waitcnt vmcnt(36)\n"
"ds_read_b64_tr_b16 a[112:113], v44 offset:4096\n"
"ds_read_b64_tr_b16 a[114:115], v44 offset:4352\n"
"ds_read_b64_tr_b16 a[140:141], v45 offset:4096\n"
"ds_read_b64_tr_b16 a[142:143], v45 offset:4352\n"
"s_waitcnt lgkmcnt(4)\n"
"s_add_u32 m0, s55, 2048\n"
"s_add_u32 s60, s87, 0\n"
"buffer_load_dwordx4 v51, s[44:47], s60 offen lds\n"
"s_add_u32 m0, s55, 3072\n"
"s_add_u32 s60, s87, 32768\n"
"buffer_load_dwordx4 v51, s[44:47], s60 offen lds\n"
"s_waitcnt vmcnt(36)\n"
"ds_read_b64_tr_b16 a[168:169], v44 offset:6144\n"
"ds_read_b64_tr_b16 a[170:171], v44 offset:6400\n"
"ds_read_b64_tr_b16 a[196:197], v45 offset:6144\n"
"ds_read_b64_tr_b16 a[198:199], v45 offset:6400\n"
"s_waitcnt lgkmcnt(4)\n"
"s_add_u32 m0, s55, 4096\n"
"s_add_u32 s60, s88, 0\n"
"buffer_load_dwordx4 v51, s[44:47], s60 offen lds\n"
"s_add_u32 m0, s55, 5120\n"
"s_add_u32 s60, s88, 32768\n"
"buffer_load_dwordx4 v51, s[44:47], s60 offen lds\n"
"s_waitcnt vmcnt(36)\n"
"ds_read_b64_tr_b16 a[4:5], v44 offset:8192\n"
"ds_read_b64_tr_b16 a[6:7], v44 offset:8448\n"
"ds_read_b64_tr_b16 a[32:33], v45 offset:8192\n"
"ds_read_b64_tr_b16 a[34:35], v45 offset:8448\n"
"s_waitcnt lgkmcnt(4)\n"
"s_add_u32 m0, s55, 6144\n"
"s_add_u32 s60, s89, 0\n"
"buffer_load_dwordx4 v51, s[44:47], s60 offen lds\n"
"s_add_u32 m0, s55, 7168\n"
"s_add_u32 s60, s89, 32768\n"
"buffer_load_dwordx4 v51, s[44:47], s60 offen lds\n"
"s_waitcnt vmcnt(36)\n"
"ds_read_b64_tr_b16 a[60:61], v44 offset:10240\n"
"ds_read_b64_tr_b16 a[62:63], v44 offset:10496\n"
"ds_read_b64_tr_b16 a[88:89], v45 offset:10240\n"
"ds_read_b64_tr_b16 a[90:91], v45 offset:10496\n"
"s_waitcnt lgkmcnt(4)\n"
"s_add_u32 m0, s55, 8192\n"
"s_add_u32 s60, s86, 0\n"
"buffer_load_dwordx4 v52, s[44:47], s60 offen lds\n"
"s_add_u32 m0, s55, 9216\n"
"s_add_u32 s60, s86, 32768\n"
"buffer_load_dwordx4 v52, s[44:47], s60 offen lds\n"
"s_waitcnt vmcnt(36)\n"
"ds_read_b64_tr_b16 a[116:117], v44 offset:12288\n"
"ds_read_b64_tr_b16 a[118:119], v44 offset:12544\n"
"ds_read_b64_tr_b16 a[144:145], v45 offset:12288\n"
"ds_read_b64_tr_b16 a[146:147], v45 offset:12544\n"
"s_waitcnt lgkmcnt(4)\n"
"s_add_u32 m0, s55, 10240\n"
"s_add_u32 s60, s87, 0\n"
"buffer_load_dwordx4 v52, s[44:47], s60 offen lds\n"
"s_add_u32 m0, s55, 11264\n"
"s_add_u32 s60, s87, 32768\n"
"buffer_load_dwordx4 v52, s[44:47], s60 offen lds\n"
"s_waitcnt vmcnt(36)\n"
"ds_read_b64_tr_b16 a[172:173], v44 offset:14336\n"
"ds_read_b64_tr_b16 a[174:175], v44 offset:14592\n"
"ds_read_b64_tr_b16 a[200:201], v45 offset:14336\n"
"ds_read_b64_tr_b16 a[202:203], v45 offset:14592\n"
"s_waitcnt lgkmcnt(4)\n"
"s_add_u32 m0, s55, 12288\n"
"s_add_u32 s60, s88, 0\n"
"buffer_load_dwordx4 v52, s[44:47], s60 offen lds\n"
"s_add_u32 m0, s55, 13312\n"
"s_add_u32 s60, s88, 32768\n"
"buffer_load_dwordx4 v52, s[44:47], s60 offen lds\n"
"s_waitcnt vmcnt(36)\n"
"ds_read_b64_tr_b16 a[8:9], v44 offset:16384\n"
"ds_read_b64_tr_b16 a[10:11], v44 offset:16640\n"
"ds_read_b64_tr_b16 a[36:37], v45 offset:16384\n"
"ds_read_b64_tr_b16 a[38:39], v45 offset:16640\n"
"s_waitcnt lgkmcnt(4)\n"
"s_add_u32 m0, s55, 14336\n"
"s_add_u32 s60, s89, 0\n"
"buffer_load_dwordx4 v52, s[44:47], s60 offen lds\n"
"s_add_u32 m0, s55, 15360\n"
"s_add_u32 s60, s89, 32768\n"
"buffer_load_dwordx4 v52, s[44:47], s60 offen lds\n"
"s_waitcnt vmcnt(36)\n"
"ds_read_b64_tr_b16 a[64:65], v44 offset:18432\n"
"ds_read_b64_tr_b16 a[66:67], v44 offset:18688\n"
"ds_read_b64_tr_b16 a[92:93], v45 offset:18432\n"
"ds_read_b64_tr_b16 a[94:95], v45 offset:18688\n"
"s_waitcnt lgkmcnt(4)\n"
"s_waitcnt vmcnt(34)\n"
"ds_read_b64_tr_b16 a[120:121], v44 offset:20480\n"
"ds_read_b64_tr_b16 a[122:123], v44 offset:20736\n"
"ds_read_b64_tr_b16 a[148:149], v45 offset:20480\n"
"ds_read_b64_tr_b16 a[150:151], v45 offset:20736\n"
"s_waitcnt lgkmcnt(4)\n"
"s_waitcnt vmcnt(32)\n"
"ds_read_b64_tr_b16 a[176:177], v44 offset:22528\n"
"ds_read_b64_tr_b16 a[178:179], v44 offset:22784\n"
"ds_read_b64_tr_b16 a[204:205], v45 offset:22528\n"
"ds_read_b64_tr_b16 a[206:207], v45 offset:22784\n"
"s_waitcnt lgkmcnt(4)\n"
"s_waitcnt vmcnt(30)\n"
"ds_read_b64_tr_b16 a[12:13], v44 offset:24576\n"
"ds_read_b64_tr_b16 a[14:15], v44 offset:24832\n"
"ds_read_b64_tr_b16 a[40:41], v45 offset:24576\n"
"ds_read_b64_tr_b16 a[42:43], v45 offset:24832\n"
"s_waitcnt lgkmcnt(4)\n"
"s_waitcnt vmcnt(28)\n"
"ds_read_b64_tr_b16 a[68:69], v44 offset:26624\n"
"ds_read_b64_tr_b16 a[70:71], v44 offset:26880\n"
"ds_read_b64_tr_b16 a[96:97], v45 offset:26624\n"
"ds_read_b64_tr_b16 a[98:99], v45 offset:26880\n"
"s_waitcnt lgkmcnt(4)\n"
"s_waitcnt vmcnt(26)\n"
"ds_read_b64_tr_b16 a[124:125], v44 offset:28672\n"
"ds_read_b64_tr_b16 a[126:127], v44 offset:28928\n"
"ds_read_b64_tr_b16 a[152:153], v45 offset:28672\n"
"ds_read_b64_tr_b16 a[154:155], v45 offset:28928\n"
"s_waitcnt lgkmcnt(4)\n"
"s_waitcnt vmcnt(24)\n"
"ds_read_b64_tr_b16 a[180:181], v44 offset:30720\n"
"ds_read_b64_tr_b16 a[182:183], v44 offset:30976\n"
"ds_read_b64_tr_b16 a[208:209], v45 offset:30720\n"
"ds_read_b64_tr_b16 a[210:211], v45 offset:30976\n"
"s_waitcnt lgkmcnt(4)\n"
"s_waitcnt vmcnt(22)\n"
"ds_read_b64_tr_b16 a[16:17], v44 offset:32768\n"
"ds_read_b64_tr_b16 a[18:19], v44 offset:33024\n"
"ds_read_b64_tr_b16 a[44:45], v45 offset:32768\n"
"ds_read_b64_tr_b16 a[46:47], v45 offset:33024\n"
"s_waitcnt lgkmcnt(4)\n"
"s_waitcnt vmcnt(20)\n"
"ds_read_b64_tr_b16 a[72:73], v44 offset:34816\n"
"ds_read_b64_tr_b16 a[74:75], v44 offset:35072\n"
"ds_read_b64_tr_b16 a[100:101], v45 offset:34816\n"
"ds_read_b64_tr_b16 a[102:103], v45 offset:35072\n"
"s_waitcnt lgkmcnt(4)\n"
"s_waitcnt vmcnt(18)\n"
"ds_read_b64_tr_b16 a[128:129], v44 offset:36864\n"
"ds_read_b64_tr_b16 a[130:131], v44 offset:37120\n"
"ds_read_b64_tr_b16 a[156:157], v45 offset:36864\n"
"ds_read_b64_tr_b16 a[158:159], v45 offset:37120\n"
"s_waitcnt lgkmcnt(4)\n"
"s_waitcnt vmcnt(16)\n"
"ds_read_b64_tr_b16 a[184:185], v44 offset:38912\n"
"ds_read_b64_tr_b16 a[186:187], v44 offset:39168\n"
"ds_read_b64_tr_b16 a[212:213], v45 offset:38912\n"
"ds_read_b64_tr_b16 a[214:215], v45 offset:39168\n"
"s_waitcnt lgkmcnt(4)\n"
"s_waitcnt vmcnt(14)\n"
"ds_read_b64_tr_b16 a[20:21], v44 offset:0\n"
"ds_read_b64_tr_b16 a[22:23], v44 offset:256\n"
"ds_read_b64_tr_b16 a[48:49], v45 offset:0\n"
"ds_read_b64_tr_b16 a[50:51], v45 offset:256\n"
"s_waitcnt lgkmcnt(4)\n"
"s_waitcnt vmcnt(12)\n"
"ds_read_b64_tr_b16 a[76:77], v44 offset:2048\n"
"ds_read_b64_tr_b16 a[78:79], v44 offset:2304\n"
"ds_read_b64_tr_b16 a[104:105], v45 offset:2048\n"
"ds_read_b64_tr_b16 a[106:107], v45 offset:2304\n"
"s_waitcnt lgkmcnt(4)\n"
"s_waitcnt vmcnt(10)\n"
"ds_read_b64_tr_b16 a[132:133], v44 offset:4096\n"
"ds_read_b64_tr_b16 a[134:135], v44 offset:4352\n"
"ds_read_b64_tr_b16 a[160:161], v45 offset:4096\n"
"ds_read_b64_tr_b16 a[162:163], v45 offset:4352\n"
"s_waitcnt lgkmcnt(4)\n"
"s_waitcnt vmcnt(8)\n"
"ds_read_b64_tr_b16 a[188:189], v44 offset:6144\n"
"ds_read_b64_tr_b16 a[190:191], v44 offset:6400\n"
"ds_read_b64_tr_b16 a[216:217], v45 offset:6144\n"
"ds_read_b64_tr_b16 a[218:219], v45 offset:6400\n"
"s_waitcnt lgkmcnt(4)\n"
"s_waitcnt vmcnt(6)\n"
"ds_read_b64_tr_b16 a[24:25], v44 offset:8192\n"
"ds_read_b64_tr_b16 a[26:27], v44 offset:8448\n"
"ds_read_b64_tr_b16 a[52:53], v45 offset:8192\n"
"ds_read_b64_tr_b16 a[54:55], v45 offset:8448\n"
"s_waitcnt lgkmcnt(4)\n"
"s_waitcnt vmcnt(4)\n"
"ds_read_b64_tr_b16 a[80:81], v44 offset:10240\n"
"ds_read_b64_tr_b16 a[82:83], v44 offset:10496\n"
"ds_read_b64_tr_b16 a[108:109], v45 offset:10240\n"
"ds_read_b64_tr_b16 a[110:111], v45 offset:10496\n"
"s_waitcnt lgkmcnt(4)\n"
"s_waitcnt vmcnt(2)\n"
"ds_read_b64_tr_b16 a[136:137], v44 offset:12288\n"
"ds_read_b64_tr_b16 a[138:139], v44 offset:12544\n"
"ds_read_b64_tr_b16 a[164:165], v45 offset:12288\n"
"ds_read_b64_tr_b16 a[166:167], v45 offset:12544\n"
"s_waitcnt lgkmcnt(4)\n"
"s_waitcnt vmcnt(0)\n"
"ds_read_b64_tr_b16 a[192:193], v44 offset:14336\n"
"ds_read_b64_tr_b16 a[194:195], v44 offset:14592\n"
"ds_read_b64_tr_b16 a[220:221], v45 offset:14336\n"
"ds_read_b64_tr_b16 a[222:223], v45 offset:14592\n"
"s_waitcnt lgkmcnt(4)\n"
"s_waitcnt vmcnt(0)\n"
"s_waitcnt lgkmcnt(0)\n"
"s_mov_b32 m0, s59\n"
"s_barrier\n"
"s_mov_b64 s[84:85], s[48:49]\n"
"s_mov_b32 s29, 0x80000000\n"
"s_mov_b32 s30, 0x80000000\n"
"s_mov_b32 s28, 0x80000000\n"
"s_mov_b32 s31, 0x80000000\n"
"s_mov_b32 s31, 0x80000000\n"
"s_mov_b32 s30, 0x80000000\n"
"s_mov_b32 s90, 0x80000000\n"
"s_mov_b32 s29, 0x80000000\n"
"s_mov_b32 s31, 0x80000000\n"
"s_mov_b32 s30, 0x80000000\n"
"s_mov_b32 s91, 0x80000000\n"
"v_mov_b32 v144, 0\n"
"v_mov_b32 v145, 0\n"
"v_mov_b32 v160, 0\n"
"v_mov_b32 v161, 0\n"
"v_mov_b32 v164, 0\n"
"v_mov_b32 v165, 0\n"
"v_mov_b32 v92, 0\n"
"v_mov_b32 v93, 0\n"
"s_cmp_eq_u32 s54, 0\n"
"s_cbranch_scc1 .LJ_1s\n"
"s_cmp_eq_u32 s54, 1\n"
"s_cbranch_scc1 .LJ_2s\n"
"s_cmp_eq_u32 s54, 2\n"
"s_cbranch_scc1 .LJ_3s\n"
".p2align 6\n"
".LJ_top:\n"
"v_mfma_f32_16x16x32_bf16 v[44:47], v[96:99], a[0:3], 0\n"
"s_cmp_lt_u32 s90, 0x8000000\n"
"s_cselect_b32 s90, 0x80000000, s90\n"
"v_mfma_f32_16x16x32_bf16 v[44:47], v[100:103], a[4:7], v[44:47]\n"
"s_waitcnt vmcnt(15)\n"
"global_load_dwordx4 a[224:227], v10, s[56:57] offset:0 sc1\n"
"v_mfma_f32_16x16x32_bf16 v[44:47], v[104:107], a[8:11], v[44:47]\n"
"s_waitcnt lgkmcnt(6)\n"
"buffer_load_dwordx2 v[92:93], v15, s[40:43], s90 offen offset:0 sc1\n"
"v_mfma_f32_16x16x32_bf16 v[44:47], v[108:111], a[12:15], v[44:47]\n"
"s_waitcnt vmcnt(13)\n"
"buffer_store_dwordx4 v[48:51], v18, s[76:79], s29 offen sc1\n"
"v_mfma_f32_16x16x32_bf16 v[44:47], v[112:115], a[16:19], v[44:47]\n"
"s_waitcnt lgkmcnt(3)\n"
"v_add_f32 v60, v60, v56\n"
"v_add_f32 v61, v61, v57\n"
"v_mfma_f32_16x16x32_bf16 v[44:47], v[116:119], a[20:23], v[44:47]\n"
"s_waitcnt vmcnt(13)\n"
"v_add_f32 v62, v62, v58\n"
"v_add_f32 v63, v63, v59\n"
"v_mfma_f32_16x16x32_bf16 v[44:47], v[120:123], a[24:27], v[44:47]\n"
"global_load_dwordx4 a[228:231], v11, s[56:57] offset:0 sc1\n"
"v_add_f32 v60, v60, v64\n"
"v_mfma_f32_16x16x32_bf16 v[48:51], v[96:99], a[28:31], 0\n"
"v_add_f32 v61, v61, v65\n"
"v_add_f32 v62, v62, v66\n"
"v_mfma_f32_16x16x32_bf16 v[48:51], v[100:103], a[32:35], v[48:51]\n"
"ds_write_addtid_b32 v44 offset:0\n"
"v_add_f32 v63, v63, v67\n"
"v_mfma_f32_16x16x32_bf16 v[48:51], v[104:107], a[36:39], v[48:51]\n"
"ds_write_addtid_b32 v45 offset:4640\n"
"v_add_f32 v60, v60, v52\n"
"v_mfma_f32_16x16x32_bf16 v[48:51], v[108:111], a[40:43], v[48:51]\n"
"ds_write_addtid_b32 v46 offset:9344\n"
"v_add_f32 v61, v61, v53\n"
"v_mfma_f32_16x16x32_bf16 v[48:51], v[112:115], a[44:47], v[48:51]\n"
"ds_write_addtid_b32 v47 offset:13984\n"
"v_add_f32 v62, v62, v54\n"
"v_mfma_f32_16x16x32_bf16 v[48:51], v[116:119], a[48:51], v[48:51]\n"
"v_add_f32 v63, v63, v55\n"
"v_mfma_f32_16x16x32_bf16 v[48:51], v[120:123], a[52:55], v[48:51]\n"
"s_add_u32 s31, s50, 0xffd00000\n"
"s_and_b32 s30, s31, 0xfbffffff\n"
"v_cvt_pk_bf16_f32 v44, v60, v61\n"
"v_mfma_f32_16x16x32_bf16 v[52:55], v[96:99], a[56:59], 0\n"
"s_waitcnt vmcnt(10)\n"
"v_cvt_pk_bf16_f32 v45, v62, v63\n"
"v_mfma_f32_16x16x32_bf16 v[56:59], v[38:41], v[68:71], 0\n"
"v_mfma_f32_16x16x32_bf16 v[56:59], v[38:41], v[76:79], v[56:59]\n"
"v_mfma_f32_16x16x32_bf16 v[56:59], v[38:41], v[84:87], v[56:59]\n"
"v_mfma_f32_16x16x32_bf16 v[60:63], v[38:41], v[72:75], 0\n"
"v_mfma_f32_16x16x32_bf16 v[60:63], v[38:41], v[80:83], v[60:63]\n"
"v_mfma_f32_16x16x32_bf16 v[60:63], v[38:41], v[88:91], v[60:63]\n"
"v_cvt_pk_bf16_f32 v64, v56, v57\n"
"v_cvt_pk_bf16_f32 v65, v58, v59\n"
"v_mfma_f32_16x16x32_bf16 v[52:55], v[100:103], a[60:63], v[52:55]\n"
"s_waitcnt lgkmcnt(4)\n"
"ds_write_addtid_b32 v48 offset:256\n"
"v_add_f32 v152, v152, v156\n"
"v_mfma_f32_16x16x32_bf16 v[52:55], v[104:107], a[64:67], v[52:55]\n"
"ds_write_addtid_b32 v49 offset:4896\n"
"v_add_f32 v153, v153, v157\n"
"v_mfma_f32_16x16x32_bf16 v[52:55], v[108:111], a[68:71], v[52:55]\n"
"ds_write_addtid_b32 v50 offset:9600\n"
"v_add_f32 v154, v154, v158\n"
"v_mfma_f32_16x16x32_bf16 v[52:55], v[112:115], a[72:75], v[52:55]\n"
"ds_write_addtid_b32 v51 offset:14240\n"
"v_add_f32 v155, v155, v159\n"
"v_mfma_f32_16x16x32_bf16 v[52:55], v[116:119], a[76:79], v[52:55]\n"
"buffer_store_dwordx2 v[0:1], v14, s[40:43], s28 offen offset:0 sc1\n"
"v_add_f32 v152, v152, v160\n"
"v_mfma_f32_16x16x32_bf16 v[52:55], v[120:123], a[80:83], v[52:55]\n"
"v_add_f32 v153, v153, v161\n"
"v_add_f32 v154, v154, v162\n"
"v_mfma_f32_16x16x32_bf16 v[48:51], v[96:99], a[84:87], 0\n"
"v_add_f32 v155, v155, v163\n"
"v_add_f32 v152, v152, v148\n"
"v_mfma_f32_16x16x32_bf16 v[48:51], v[100:103], a[88:91], v[48:51]\n"
"s_add_u32 s58, s50, 0x180000\n"
"ds_write_addtid_b32 v52 offset:512\n"
"v_add_f32 v153, v153, v149\n"
"v_mfma_f32_16x16x32_bf16 v[48:51], v[104:107], a[92:95], v[48:51]\n"
"s_and_b32 s58, s58, 0x3ffffff\n"
"ds_write_addtid_b32 v53 offset:5152\n"
"v_add_f32 v154, v154, v150\n"
"v_mfma_f32_16x16x32_bf16 v[48:51], v[108:111], a[96:99], v[48:51]\n"
"s_add_u32 s56, s40, s58\n"
"ds_write_addtid_b32 v54 offset:9856\n"
"v_add_f32 v155, v155, v151\n"
"v_mfma_f32_16x16x32_bf16 v[48:51], v[112:115], a[100:103], v[48:51]\n"
"s_addc_u32 s57, s41, 0\n"
"ds_write_addtid_b32 v55 offset:14496\n"
"v_cvt_pk_bf16_f32 v46, v152, v153\n"
"v_mfma_f32_16x16x32_bf16 v[48:51], v[116:119], a[104:107], v[48:51]\n"
"s_waitcnt vmcnt(9)\n"
"v_cmp_ne_u64 vcc, s[48:49], v[164:165]\n"
"v_cvt_pk_bf16_f32 v47, v154, v155\n"
"v_mfma_f32_16x16x32_bf16 v[48:51], v[120:123], a[108:111], v[48:51]\n"
"buffer_store_dwordx4 v[44:47], v13, s[40:43], s30 offen sc1\n"
"v_cvt_pk_bf16_f32 v66, v60, v61\n"
"v_cvt_pk_bf16_f32 v67, v62, v63\n"
"v_mfma_f32_16x16x32_bf16 v[52:55], v[144:147], v[144:147], 0\n"
"v_mfma_f32_16x16x32_bf16 v[44:47], v[34:37], v[144:147], 0\n"
"v_mfma_f32_16x16x32_bf16 v[52:55], v[64:67], v[64:67], v[52:55]\n"
"v_mfma_f32_16x16x32_bf16 v[44:47], v[34:37], v[64:67], v[44:47]\n"
"v_mfma_f32_16x16x32_bf16 v[56:59], v[96:99], a[112:115], 0\n"
"s_cbranch_vccnz .LJ_3s\n"
".LJ_3r:\n"
"v_cndmask_b32_e64 v52, v52, v53, s[64:65]\n"
"v_mfma_f32_16x16x32_bf16 v[56:59], v[100:103], a[116:119], v[56:59]\n"
"s_add_u32 s50, s50, 0x180000\n"
"ds_write_addtid_b32 v48 offset:768\n"
"v_cndmask_b32_e64 v54, v54, v55, s[64:65]\n"
"v_mfma_f32_16x16x32_bf16 v[56:59], v[104:107], a[120:123], v[56:59]\n"
"ds_write_addtid_b32 v49 offset:5408\n"
"v_cndmask_b32_e64 v45, v52, v54, s[66:67]\n"
"v_mfma_f32_16x16x32_bf16 v[56:59], v[108:111], a[124:127], v[56:59]\n"
"buffer_store_dwordx2 v[44:45], v23, s[40:43], s91 offen offset:0 sc1\n"
"ds_write_addtid_b32 v50 offset:10112\n"
"v_mfma_f32_16x16x32_bf16 v[56:59], v[112:115], a[128:131], v[56:59]\n"
"ds_write_addtid_b32 v51 offset:14752\n"
"global_load_dwordx4 a[232:235], v5, s[56:57] offset:0 sc1\n"
"v_mfma_f32_16x16x32_bf16 v[56:59], v[116:119], a[132:135], v[56:59]\n"
"ds_bpermute_b32 v44, v21, v64\n"
"v_mfma_f32_16x16x32_bf16 v[56:59], v[120:123], a[136:139], v[56:59]\n"
"buffer_load_dwordx2 v[144:145], v12, s[40:43], s50 offen sc1\n"
"v_mfma_f32_16x16x32_bf16 v[48:51], v[96:99], a[140:143], 0\n"
"ds_bpermute_b32 v45, v21, v65\n"
"v_mfma_f32_16x16x32_bf16 v[48:51], v[100:103], a[144:147], v[48:51]\n"
"ds_write_addtid_b32 v56 offset:1024\n"
"v_mfma_f32_16x16x32_bf16 v[48:51], v[104:107], a[148:151], v[48:51]\n"
"ds_write_addtid_b32 v57 offset:5664\n"
"global_load_dwordx4 a[236:239], v6, s[56:57] offset:0 sc1\n"
"v_mfma_f32_16x16x32_bf16 v[48:51], v[108:111], a[152:155], v[48:51]\n"
"ds_write_addtid_b32 v58 offset:10368\n"
"v_mfma_f32_16x16x32_bf16 v[48:51], v[112:115], a[156:159], v[48:51]\n"
"ds_write_addtid_b32 v59 offset:15008\n"
"v_mfma_f32_16x16x32_bf16 v[48:51], v[116:119], a[160:163], v[48:51]\n"
"ds_bpermute_b32 v46, v21, v66\n"
"v_mfma_f32_16x16x32_bf16 v[48:51], v[120:123], a[164:167], v[48:51]\n"
"ds_bpermute_b32 v47, v21, v67\n"
"v_mfma_f32_16x16x32_bf16 v[68:71], a[168:171], v[96:99], 0\n"
"global_load_dwordx4 a[240:243], v7, s[56:57] offset:0 sc1\n"
"v_mfma_f32_16x16x32_bf16 v[68:71], a[172:175], v[100:103], v[68:71]\n"
"ds_write_addtid_b32 v48 offset:1280\n"
"v_mfma_f32_16x16x32_bf16 v[68:71], a[176:179], v[104:107], v[68:71]\n"
"ds_write_addtid_b32 v49 offset:5920\n"
"v_mfma_f32_16x16x32_bf16 v[68:71], a[180:183], v[108:111], v[68:71]\n"
"ds_write_addtid_b32 v50 offset:10624\n"
"v_mfma_f32_16x16x32_bf16 v[68:71], a[184:187], v[112:115], v[68:71]\n"
"ds_write_addtid_b32 v51 offset:15264\n"
"v_mfma_f32_16x16x32_bf16 v[68:71], a[188:191], v[116:119], v[68:71]\n"
"s_waitcnt lgkmcnt(4)\n"
"buffer_store_dwordx4 v[44:47], v19, s[76:79], s29 offen sc1\n"
"global_load_dwordx4 a[244:247], v8, s[56:57] offset:0 sc1\n"
"v_mfma_f32_16x16x32_bf16 v[68:71], a[192:195], v[120:123], v[68:71]\n"
"v_mfma_f32_16x16x32_bf16 v[72:75], a[196:199], v[96:99], 0\n"
"s_and_b32 s30, s90, 0x3ffffff\n"
"v_mfma_f32_16x16x32_bf16 v[72:75], a[200:203], v[100:103], v[72:75]\n"
"s_waitcnt lgkmcnt(0)\n"
"v_mfma_f32_16x16x32_bf16 v[72:75], a[204:207], v[104:107], v[72:75]\n"
"s_barrier\n"
"v_mfma_f32_16x16x32_bf16 v[72:75], a[208:211], v[108:111], v[72:75]\n"
"s_waitcnt vmcnt(11)\n"
"global_load_dwordx4 a[248:251], v9, s[56:57] offset:0 sc1\n"
"ds_read_b128 v[76:79], v2 offset:0\n"
"v_mfma_f32_16x16x32_bf16 v[72:75], a[212:215], v[112:115], v[72:75]\n"
"s_add_u32 s62, s40, s30\n"
"ds_read_b128 v[80:83], v3 offset:512\n"
"ds_read_b128 v[84:87], v4 offset:1024\n"
"v_mfma_f32_16x16x32_bf16 v[72:75], a[216:219], v[116:119], v[72:75]\n"
"s_addc_u32 s63, s41, 0\n"
"ds_read_b128 v[88:91], v2 offset:256\n"
"ds_read_b128 v[96:99], v3 offset:768\n"
"v_mfma_f32_16x16x32_bf16 v[72:75], a[220:223], v[120:123], v[72:75]\n"
"ds_read_b128 v[100:103], v4 offset:1280\n"
"v_mfma_f32_16x16x32_bf16 v[104:107], v[124:127], a[0:3], 0\n"
"v_cmp_ne_u64 vcc, s[84:85], v[92:93]\n"
"v_mfma_f32_16x16x32_bf16 v[104:107], v[128:131], a[4:7], v[104:107]\n"
"global_load_dwordx4 a[252:255], v10, s[56:57] offset:0 sc1\n"
"v_mfma_f32_16x16x32_bf16 v[104:107], v[132:135], a[8:11], v[104:107]\n"
"s_cbranch_vccnz .LJ_4s\n"
".LJ_4r:\n"
"v_mfma_f32_16x16x32_bf16 v[104:107], v[136:139], a[12:15], v[104:107]\n"
"global_load_dwordx4 v[46:49], v16, s[62:63] offset:-4096 sc1\n"
"v_mfma_f32_16x16x32_bf16 v[104:107], v[140:143], a[16:19], v[104:107]\n"
"s_waitcnt lgkmcnt(3)\n"
"v_add_f32 v76, v76, v80\n"
"v_add_f32 v77, v77, v81\n"
"v_mfma_f32_16x16x32_bf16 v[104:107], a[224:227], a[20:23], v[104:107]\n"
"s_waitcnt vmcnt(12)\n"
"v_add_f32 v78, v78, v82\n"
"v_add_f32 v79, v79, v83\n"
"v_mfma_f32_16x16x32_bf16 v[104:107], a[228:231], a[24:27], v[104:107]\n"
"global_load_dwordx4 v[92:95], v11, s[56:57] offset:0 sc1\n"
"v_add_f32 v76, v76, v84\n"
"v_mfma_f32_16x16x32_bf16 v[80:83], v[124:127], a[28:31], 0\n"
"v_add_f32 v77, v77, v85\n"
"v_add_f32 v78, v78, v86\n"
"v_mfma_f32_16x16x32_bf16 v[80:83], v[128:131], a[32:35], v[80:83]\n"
"ds_write_addtid_b32 v104 offset:1536\n"
"v_add_f32 v79, v79, v87\n"
"v_mfma_f32_16x16x32_bf16 v[80:83], v[132:135], a[36:39], v[80:83]\n"
"ds_write_addtid_b32 v105 offset:6176\n"
"v_add_f32 v76, v76, v68\n"
"v_mfma_f32_16x16x32_bf16 v[80:83], v[136:139], a[40:43], v[80:83]\n"
"ds_write_addtid_b32 v106 offset:10880\n"
"v_add_f32 v77, v77, v69\n"
"v_mfma_f32_16x16x32_bf16 v[80:83], v[140:143], a[44:47], v[80:83]\n"
"ds_write_addtid_b32 v107 offset:15520\n"
"v_add_f32 v78, v78, v70\n"
"v_mfma_f32_16x16x32_bf16 v[80:83], a[224:227], a[48:51], v[80:83]\n"
"global_load_dwordx4 v[50:53], v16, s[62:63] offset:-3072 sc1\n"
"v_add_f32 v79, v79, v71\n"
"v_mfma_f32_16x16x32_bf16 v[80:83], a[228:231], a[52:55], v[80:83]\n"
"s_add_u32 s29, s50, 0xffd00000\n"
"s_and_b32 s30, s29, 0xfbffffff\n"
"v_cvt_pk_bf16_f32 v84, v76, v77\n"
"v_mfma_f32_16x16x32_bf16 v[68:71], v[124:127], a[56:59], 0\n"
"v_cvt_pk_bf16_f32 v85, v78, v79\n"
"v_mfma_f32_16x16x32_bf16 v[68:71], v[128:131], a[60:63], v[68:71]\n"
"s_waitcnt lgkmcnt(4)\n"
"ds_write_addtid_b32 v80 offset:1792\n"
"v_add_f32 v88, v88, v96\n"
"v_mfma_f32_16x16x32_bf16 v[68:71], v[132:135], a[64:67], v[68:71]\n"
"ds_write_addtid_b32 v81 offset:6432\n"
"v_add_f32 v89, v89, v97\n"
"v_mfma_f32_16x16x32_bf16 v[68:71], v[136:139], a[68:71], v[68:71]\n"
"ds_write_addtid_b32 v82 offset:11136\n"
"v_add_f32 v90, v90, v98\n"
"v_mfma_f32_16x16x32_bf16 v[68:71], v[140:143], a[72:75], v[68:71]\n"
"ds_write_addtid_b32 v83 offset:15776\n"
"v_add_f32 v91, v91, v99\n"
"v_mfma_f32_16x16x32_bf16 v[68:71], a[224:227], a[76:79], v[68:71]\n"
"s_waitcnt vmcnt(12)\n"
"v_add_f32 v88, v88, v100\n"
"v_add_f32 v89, v89, v101\n"
"v_mfma_f32_16x16x32_bf16 v[68:71], a[228:231], a[80:83], v[68:71]\n"
"v_add_f32 v90, v90, v102\n"
"v_add_f32 v91, v91, v103\n"
"v_mfma_f32_16x16x32_bf16 v[76:79], v[124:127], a[84:87], 0\n"
"v_add_f32 v88, v88, v72\n"
"v_add_f32 v89, v89, v73\n"
"v_mfma_f32_16x16x32_bf16 v[76:79], v[128:131], a[88:91], v[76:79]\n"
"s_add_u32 s58, s50, 0x180000\n"
"ds_write_addtid_b32 v68 offset:2048\n"
"v_add_f32 v90, v90, v74\n"
"v_mfma_f32_16x16x32_bf16 v[76:79], v[132:135], a[92:95], v[76:79]\n"
"s_and_b32 s58, s58, 0x3ffffff\n"
"ds_write_addtid_b32 v69 offset:6688\n"
"v_add_f32 v91, v91, v75\n"
"v_mfma_f32_16x16x32_bf16 v[76:79], v[136:139], a[96:99], v[76:79]\n"
"s_add_u32 s56, s40, s58\n"
"ds_write_addtid_b32 v70 offset:11392\n"
"v_cvt_pk_bf16_f32 v86, v88, v89\n"
"v_mfma_f32_16x16x32_bf16 v[76:79], v[140:143], a[100:103], v[76:79]\n"
"s_addc_u32 s57, s41, 0\n"
"ds_write_addtid_b32 v71 offset:16032\n"
"v_cvt_pk_bf16_f32 v87, v90, v91\n"
"v_mfma_f32_16x16x32_bf16 v[76:79], a[224:227], a[104:107], v[76:79]\n"
"s_waitcnt vmcnt(9)\n"
"buffer_store_dwordx4 v[84:87], v13, s[40:43], s30 offen sc1\n"
"v_cmp_ne_u64 vcc, s[48:49], v[144:145]\n"
"v_mfma_f32_16x16x32_bf16 v[76:79], a[228:231], a[108:111], v[76:79]\n"
"buffer_store_dwordx2 v[0:1], v14, s[40:43], s31 offen offset:0 sc1\n"
"v_mfma_f32_16x16x32_bf16 v[68:71], v[124:127], a[112:115], 0\n"
"s_cbranch_vccnz .LJ_1s\n"
".LJ_1r:\n"
"v_mfma_f32_16x16x32_bf16 v[68:71], v[128:131], a[116:119], v[68:71]\n"
"s_add_u32 s50, s50, 0x180000\n"
"ds_write_addtid_b32 v76 offset:2304\n"
"global_load_dwordx4 v[54:57], v16, s[62:63] offset:-2048 sc1\n"
"v_mfma_f32_16x16x32_bf16 v[68:71], v[132:135], a[120:123], v[68:71]\n"
"ds_write_addtid_b32 v77 offset:6944\n"
"v_mfma_f32_16x16x32_bf16 v[68:71], v[136:139], a[124:127], v[68:71]\n"
"ds_write_addtid_b32 v78 offset:11648\n"
"v_mfma_f32_16x16x32_bf16 v[68:71], v[140:143], a[128:131], v[68:71]\n"
"ds_write_addtid_b32 v79 offset:16288\n"
"global_load_dwordx4 v[96:99], v5, s[56:57] offset:0 sc1\n"
"v_mfma_f32_16x16x32_bf16 v[68:71], a[224:227], a[132:135], v[68:71]\n"
"v_mfma_f32_16x16x32_bf16 v[68:71], a[228:231], a[136:139], v[68:71]\n"
"buffer_load_dwordx2 v[160:161], v12, s[40:43], s50 offen sc1\n"
"v_mfma_f32_16x16x32_bf16 v[112:115], v[124:127], a[140:143], 0\n"
"global_load_dwordx4 v[58:61], v16, s[62:63] offset:-1024 sc1\n"
"v_mfma_f32_16x16x32_bf16 v[112:115], v[128:131], a[144:147], v[112:115]\n"
"ds_write_addtid_b32 v68 offset:2560\n"
"v_mfma_f32_16x16x32_bf16 v[112:115], v[132:135], a[148:151], v[112:115]\n"
"ds_write_addtid_b32 v69 offset:7200\n"
"global_load_dwordx4 v[100:103], v6, s[56:57] offset:0 sc1\n"
"v_mfma_f32_16x16x32_bf16 v[112:115], v[136:139], a[152:155], v[112:115]\n"
"ds_write_addtid_b32 v70 offset:11904\n"
"v_mfma_f32_16x16x32_bf16 v[112:115], v[140:143], a[156:159], v[112:115]\n"
"s_waitcnt vmcnt(13)\n"
"buffer_store_dwordx2 v[0:1], v24, s[40:43], s91 offen offset:0 sc1\n"
"ds_write_addtid_b32 v71 offset:16544\n"
"v_mfma_f32_16x16x32_bf16 v[112:115], a[224:227], a[160:163], v[112:115]\n"
"global_load_dwordx4 v[62:65], v17, s[62:63] offset:0 sc1\n"
"v_mfma_f32_16x16x32_bf16 v[112:115], a[228:231], a[164:167], v[112:115]\n"
"v_mfma_f32_16x16x32_bf16 v[144:147], a[168:171], v[124:127], 0\n"
"global_load_dwordx4 v[104:107], v7, s[56:57] offset:0 sc1\n"
"v_mfma_f32_16x16x32_bf16 v[144:147], a[172:175], v[128:131], v[144:147]\n"
"ds_write_addtid_b32 v112 offset:2816\n"
"v_mfma_f32_16x16x32_bf16 v[144:147], a[176:179], v[132:135], v[144:147]\n"
"ds_write_addtid_b32 v113 offset:7456\n"
"v_mfma_f32_16x16x32_bf16 v[144:147], a[180:183], v[136:139], v[144:147]\n"
"ds_write_addtid_b32 v114 offset:12160\n"
"global_load_dwordx4 v[70:73], v16, s[62:63] offset:0 sc1\n"
"v_mfma_f32_16x16x32_bf16 v[144:147], a[184:187], v[140:143], v[144:147]\n"
"ds_write_addtid_b32 v115 offset:16800\n"
"v_mfma_f32_16x16x32_bf16 v[144:147], a[188:191], a[224:227], v[144:147]\n"
"global_load_dwordx4 v[108:111], v8, s[56:57] offset:0 sc1\n"
"v_mfma_f32_16x16x32_bf16 v[144:147], a[192:195], a[228:231], v[144:147]\n"
"v_mfma_f32_16x16x32_bf16 v[148:151], a[196:199], v[124:127], 0\n"
"v_mfma_f32_16x16x32_bf16 v[148:151], a[200:203], v[128:131], v[148:151]\n"
"s_waitcnt lgkmcnt(0)\n"
"global_load_dwordx4 v[74:77], v16, s[62:63] offset:1024 sc1\n"
"v_mfma_f32_16x16x32_bf16 v[148:151], a[204:207], v[132:135], v[148:151]\n"
"s_barrier\n"
"v_mfma_f32_16x16x32_bf16 v[148:151], a[208:211], v[136:139], v[148:151]\n"
"global_load_dwordx4 v[112:115], v9, s[56:57] offset:0 sc1\n"
"ds_read_b128 v[124:127], v2 offset:1536\n"
"v_mfma_f32_16x16x32_bf16 v[148:151], a[212:215], v[140:143], v[148:151]\n"
"ds_read_b128 v[128:131], v3 offset:2048\n"
"ds_read_b128 v[132:135], v4 offset:2560\n"
"v_mfma_f32_16x16x32_bf16 v[148:151], a[216:219], a[224:227], v[148:151]\n"
"ds_read_b128 v[136:139], v2 offset:1792\n"
"ds_read_b128 v[140:143], v3 offset:2304\n"
"v_mfma_f32_16x16x32_bf16 v[148:151], a[220:223], a[228:231], v[148:151]\n"
"ds_read_b128 v[152:155], v4 offset:2816\n"
"global_load_dwordx4 v[78:81], v16, s[62:63] offset:2048 sc1\n"
"v_mfma_f32_16x16x32_bf16 v[156:159], a[232:235], a[0:3], 0\n"
"v_mfma_f32_16x16x32_bf16 v[156:159], a[236:239], a[4:7], v[156:159]\n"
"s_waitcnt vmcnt(20)\n"
"global_load_dwordx4 v[116:119], v10, s[56:57] offset:0 sc1\n"
"v_mfma_f32_16x16x32_bf16 v[156:159], a[240:243], a[8:11], v[156:159]\n"
"v_mfma_f32_16x16x32_bf16 v[156:159], a[244:247], a[12:15], v[156:159]\n"
"s_waitcnt vmcnt(19)\n"
"v_mfma_f32_16x16x32_bf16 v[156:159], a[248:251], a[16:19], v[156:159]\n"
"s_waitcnt lgkmcnt(3)\n"
"v_add_f32 v124, v124, v128\n"
"v_add_f32 v125, v125, v129\n"
"v_mfma_f32_16x16x32_bf16 v[156:159], a[252:255], a[20:23], v[156:159]\n"
"s_waitcnt vmcnt(17)\n"
"v_add_f32 v126, v126, v130\n"
"v_add_f32 v127, v127, v131\n"
"v_mfma_f32_16x16x32_bf16 v[156:159], v[92:95], a[24:27], v[156:159]\n"
"global_load_dwordx4 v[120:123], v11, s[56:57] offset:0 sc1\n"
"v_add_f32 v124, v124, v132\n"
"v_mfma_f32_16x16x32_bf16 v[128:131], a[232:235], a[28:31], 0\n"
"v_add_f32 v125, v125, v133\n"
"v_add_f32 v126, v126, v134\n"
"v_mfma_f32_16x16x32_bf16 v[128:131], a[236:239], a[32:35], v[128:131]\n"
"ds_write_addtid_b32 v156 offset:3072\n"
"v_add_f32 v127, v127, v135\n"
"v_mfma_f32_16x16x32_bf16 v[128:131], a[240:243], a[36:39], v[128:131]\n"
"ds_write_addtid_b32 v157 offset:7712\n"
"v_add_f32 v124, v124, v144\n"
"v_mfma_f32_16x16x32_bf16 v[128:131], a[244:247], a[40:43], v[128:131]\n"
"ds_write_addtid_b32 v158 offset:12416\n"
"v_add_f32 v125, v125, v145\n"
"v_mfma_f32_16x16x32_bf16 v[128:131], a[248:251], a[44:47], v[128:131]\n"
"ds_write_addtid_b32 v159 offset:17056\n"
"v_add_f32 v126, v126, v146\n"
"v_mfma_f32_16x16x32_bf16 v[128:131], a[252:255], a[48:51], v[128:131]\n"
"global_load_dwordx4 v[82:85], v16, s[62:63] offset:3072 sc1\n"
"v_add_f32 v127, v127, v147\n"
"v_mfma_f32_16x16x32_bf16 v[128:131], v[92:95], a[52:55], v[128:131]\n"
"s_add_u32 s28, s50, 0xffd00000\n"
"s_and_b32 s31, s28, 0xfbffffff\n"
"v_cvt_pk_bf16_f32 v132, v124, v125\n"
"v_mfma_f32_16x16x32_bf16 v[144:147], a[232:235], a[56:59], 0\n"
"v_cvt_pk_bf16_f32 v133, v126, v127\n"
"v_mfma_f32_16x16x32_bf16 v[144:147], a[236:239], a[60:63], v[144:147]\n"
"s_waitcnt lgkmcnt(4)\n"
"ds_write_addtid_b32 v128 offset:3328\n"
"v_add_f32 v136, v136, v140\n"
"v_mfma_f32_16x16x32_bf16 v[144:147], a[240:243], a[64:67], v[144:147]\n"
"s_mov_b32 s91, s90\n"
"ds_write_addtid_b32 v129 offset:7968\n"
"v_add_f32 v137, v137, v141\n"
"v_mfma_f32_16x16x32_bf16 v[144:147], a[244:247], a[68:71], v[144:147]\n"
"ds_write_addtid_b32 v130 offset:12672\n"
"v_add_f32 v138, v138, v142\n"
"v_mfma_f32_16x16x32_bf16 v[144:147], a[248:251], a[72:75], v[144:147]\n"
"s_waitcnt vmcnt(17)\n"
"buffer_store_dwordx2 v[0:1], v14, s[40:43], s29 offen offset:0 sc1\n"
"ds_write_addtid_b32 v131 offset:17312\n"
"v_mfma_f32_16x16x32_bf16 v[144:147], a[252:255], a[76:79], v[144:147]\n"
"v_add_f32 v139, v139, v143\n"
"v_add_f32 v136, v136, v152\n"
"v_mfma_f32_16x16x32_bf16 v[144:147], v[92:95], a[80:83], v[144:147]\n"
"v_add_f32 v137, v137, v153\n"
"v_add_f32 v138, v138, v154\n"
"v_mfma_f32_16x16x32_bf16 v[128:131], a[232:235], a[84:87], 0\n"
"v_add_f32 v139, v139, v155\n"
"v_add_f32 v136, v136, v148\n"
"v_mfma_f32_16x16x32_bf16 v[128:131], a[236:239], a[88:91], v[128:131]\n"
"s_add_u32 s58, s50, 0x180000\n"
"ds_write_addtid_b32 v144 offset:3584\n"
"v_add_f32 v137, v137, v149\n"
"v_mfma_f32_16x16x32_bf16 v[128:131], a[240:243], a[92:95], v[128:131]\n"
"s_and_b32 s58, s58, 0x3ffffff\n"
"ds_write_addtid_b32 v145 offset:8224\n"
"v_add_f32 v138, v138, v150\n"
"v_mfma_f32_16x16x32_bf16 v[128:131], a[244:247], a[96:99], v[128:131]\n"
"s_add_u32 s56, s40, s58\n"
"ds_write_addtid_b32 v146 offset:12928\n"
"v_add_f32 v139, v139, v151\n"
"v_mfma_f32_16x16x32_bf16 v[128:131], a[248:251], a[100:103], v[128:131]\n"
"s_addc_u32 s57, s41, 0\n"
"ds_write_addtid_b32 v147 offset:17568\n"
"v_cvt_pk_bf16_f32 v134, v136, v137\n"
"v_mfma_f32_16x16x32_bf16 v[128:131], a[252:255], a[104:107], v[128:131]\n"
"s_waitcnt vmcnt(14)\n"
"v_cmp_ne_u64 vcc, s[48:49], v[160:161]\n"
"v_cvt_pk_bf16_f32 v135, v138, v139\n"
"v_mfma_f32_16x16x32_bf16 v[128:131], v[92:95], a[108:111], v[128:131]\n"
"buffer_store_dwordx4 v[132:135], v13, s[40:43], s31 offen sc1\n"
"global_load_dwordx4 v[86:89], v17, s[62:63] offset:1024 sc1\n"
"v_mfma_f32_16x16x32_bf16 v[136:139], a[232:235], a[112:115], 0\n"
"s_cbranch_vccnz .LJ_2s\n"
".LJ_2r:\n"
"v_mov_b32 v44, v26\n"
"v_mfma_f32_16x16x32_bf16 v[136:139], a[236:239], a[116:119], v[136:139]\n"
"s_add_u32 s50, s50, 0x180000\n"
"ds_write_addtid_b32 v128 offset:3840\n"
"v_mov_b32 v45, v27\n"
"v_mfma_f32_16x16x32_bf16 v[136:139], a[240:243], a[120:123], v[136:139]\n"
"ds_write_addtid_b32 v129 offset:8480\n"
"v_mov_b32 v66, v28\n"
"v_mfma_f32_16x16x32_bf16 v[136:139], a[244:247], a[124:127], v[136:139]\n"
"ds_write_addtid_b32 v130 offset:13184\n"
"v_mov_b32 v67, v29\n"
"v_mfma_f32_16x16x32_bf16 v[136:139], a[248:251], a[128:131], v[136:139]\n"
"s_waitcnt vmcnt(12)\n"
"ds_write_addtid_b32 v131 offset:17824\n"
"global_load_dwordx4 v[124:127], v5, s[56:57] offset:0 sc1\n"
"v_mfma_f32_16x16x32_bf16 v[132:135], v[38:41], v[44:47], 0\n"
"s_cmp_ge_i32 s91, s80\n"
"s_cbranch_scc1 .LJ_ws\n"
".LJ_wr:\n"
"v_mfma_f32_16x16x32_bf16 v[132:135], v[38:41], v[52:55], v[132:135]\n"
"s_lshr_b32 s29, s91, s81\n"
"v_mfma_f32_16x16x32_bf16 v[132:135], v[38:41], v[60:63], v[132:135]\n"
"s_bitcmp1_b32 s29, 15\n"
"s_cselect_b32 s31, s83, s82\n"
"v_mfma_f32_16x16x32_bf16 v[128:131], v[38:41], v[48:51], 0\n"
"s_add_u32 s29, s29, s31\n"
"v_mfma_f32_16x16x32_bf16 v[128:131], v[38:41], v[56:59], v[128:131]\n"
"v_mfma_f32_16x16x32_bf16 v[128:131], v[38:41], v[64:67], v[128:131]\n"
"v_cvt_pk_bf16_f32 v144, v132, v133\n"
"v_cvt_pk_bf16_f32 v145, v134, v135\n"
"v_mfma_f32_16x16x32_bf16 v[136:139], a[252:255], a[132:135], v[136:139]\n"
"v_mfma_f32_16x16x32_bf16 v[136:139], v[92:95], a[136:139], v[136:139]\n"
"buffer_load_dwordx2 v[164:165], v12, s[40:43], s50 offen sc1\n"
"v_mfma_f32_16x16x32_bf16 v[44:47], a[232:235], a[140:143], 0\n"
"v_cvt_pk_bf16_f32 v146, v128, v129\n"
"v_cvt_pk_bf16_f32 v147, v130, v131\n"
"v_mfma_f32_16x16x32_bf16 v[44:47], a[236:239], a[144:147], v[44:47]\n"
"ds_write_addtid_b32 v136 offset:4096\n"
"v_mov_b32 v68, v30\n"
"v_mfma_f32_16x16x32_bf16 v[44:47], a[240:243], a[148:151], v[44:47]\n"
"ds_write_addtid_b32 v137 offset:8736\n"
"global_load_dwordx4 v[128:131], v6, s[56:57] offset:0 sc1\n"
"v_mfma_f32_16x16x32_bf16 v[44:47], a[244:247], a[152:155], v[44:47]\n"
"ds_write_addtid_b32 v138 offset:13440\n"
"v_mov_b32 v69, v31\n"
"v_mfma_f32_16x16x32_bf16 v[44:47], a[248:251], a[156:159], v[44:47]\n"
"ds_write_addtid_b32 v139 offset:18080\n"
"v_mov_b32 v90, v32\n"
"v_mfma_f32_16x16x32_bf16 v[44:47], a[252:255], a[160:163], v[44:47]\n"
"ds_bpermute_b32 v48, v21, v144\n"
"v_mfma_f32_16x16x32_bf16 v[44:47], v[92:95], a[164:167], v[44:47]\n"
"ds_bpermute_b32 v49, v21, v145\n"
"v_mfma_f32_16x16x32_bf16 v[52:55], a[168:171], a[232:235], 0\n"
"global_load_dwordx4 v[132:135], v7, s[56:57] offset:0 sc1\n"
"v_mov_b32 v91, v33\n"
"v_mfma_f32_16x16x32_bf16 v[52:55], a[172:175], a[236:239], v[52:55]\n"
"ds_write_addtid_b32 v44 offset:4352\n"
"v_mfma_f32_16x16x32_bf16 v[52:55], a[176:179], a[240:243], v[52:55]\n"
"ds_write_addtid_b32 v45 offset:8992\n"
"v_mfma_f32_16x16x32_bf16 v[52:55], a[180:183], a[244:247], v[52:55]\n"
"ds_write_addtid_b32 v46 offset:13696\n"
"v_mfma_f32_16x16x32_bf16 v[52:55], a[184:187], a[248:251], v[52:55]\n"
"ds_write_addtid_b32 v47 offset:18336\n"
"v_mfma_f32_16x16x32_bf16 v[52:55], a[188:191], a[252:255], v[52:55]\n"
"global_load_dwordx4 v[136:139], v8, s[56:57] offset:0 sc1\n"
"v_mfma_f32_16x16x32_bf16 v[52:55], a[192:195], v[92:95], v[52:55]\n"
"ds_bpermute_b32 v50, v21, v146\n"
"v_mfma_f32_16x16x32_bf16 v[148:151], a[196:199], a[232:235], 0\n"
"ds_bpermute_b32 v51, v21, v147\n"
"v_mfma_f32_16x16x32_bf16 v[148:151], a[200:203], a[236:239], v[148:151]\n"
"s_waitcnt lgkmcnt(2)\n"
"v_mfma_f32_16x16x32_bf16 v[148:151], a[204:207], a[240:243], v[148:151]\n"
"s_barrier\n"
"v_mfma_f32_16x16x32_bf16 v[148:151], a[208:211], a[244:247], v[148:151]\n"
"global_load_dwordx4 v[140:143], v9, s[56:57] offset:0 sc1\n"
"ds_read_b128 v[60:63], v2 offset:3072\n"
"v_mfma_f32_16x16x32_bf16 v[148:151], a[212:215], a[248:251], v[148:151]\n"
"ds_read_b128 v[56:59], v3 offset:3584\n"
"ds_read_b128 v[64:67], v4 offset:4096\n"
"v_mfma_f32_16x16x32_bf16 v[148:151], a[216:219], a[252:255], v[148:151]\n"
"s_add_u32 s90, s50, 0xff980000\n"
"ds_read_b128 v[152:155], v2 offset:3328\n"
"ds_read_b128 v[156:159], v3 offset:3840\n"
"v_mfma_f32_16x16x32_bf16 v[148:151], a[220:223], v[92:95], v[148:151]\n"
"ds_read_b128 v[160:163], v4 offset:4352\n"
"s_branch .LJ_top\n"
".LJ_1s:\n"
"s_cmp_ge_u32 s50, s53\n"
"s_cbranch_scc1 .LJ_t0\n"
"s_cmp_ge_u32 s50, s52\n"
"s_cbranch_scc1 .LJ_1r\n"
"s_sub_u32 s60, s50, 0x0\n"
"s_mov_b32 s61, 200000\n"
"s_nop 7\n"
".LJ_1p:\n"
"buffer_load_dwordx2 v[144:145], v12, s[40:43], s60 offen sc1\n"
"s_waitcnt vmcnt(0)\n"
"v_cmp_ne_u64 vcc, s[48:49], v[144:145]\n"
"s_cbranch_vccz .LJ_1px\n"
"s_sleep 1\n"
"s_sub_u32 s61, s61, 1\n"
"s_cbranch_scc0 .LJ_1p\n"
"s_branch .LJ_end\n"
".LJ_1px:\n"
"s_branch .LJ_1r\n"
".LJ_2s:\n"
"s_cmp_ge_u32 s50, s53\n"
"s_cbranch_scc1 .LJ_t1\n"
"s_cmp_ge_u32 s50, s52\n"
"s_cbranch_scc1 .LJ_2r\n"
"s_sub_u32 s60, s50, 0x0\n"
"s_mov_b32 s61, 200000\n"
"s_nop 7\n"
".LJ_2p:\n"
"buffer_load_dwordx2 v[160:161], v12, s[40:43], s60 offen sc1\n"
"s_waitcnt vmcnt(0)\n"
"v_cmp_ne_u64 vcc, s[48:49], v[160:161]\n"
"s_cbranch_vccz .LJ_2px\n"
"s_sleep 1\n"
"s_sub_u32 s61, s61, 1\n"
"s_cbranch_scc0 .LJ_2p\n"
"s_branch .LJ_end\n"
".LJ_2px:\n"
"s_branch .LJ_2r\n"
".LJ_3s:\n"
"s_cmp_ge_u32 s50, s53\n"
"s_cbranch_scc1 .LJ_t2\n"
"s_cmp_ge_u32 s50, s52\n"
"s_cbranch_scc1 .LJ_3r\n"
"s_sub_u32 s60, s50, 0x0\n"
"s_mov_b32 s61, 200000\n"
"s_nop 7\n"
".LJ_3p:\n"
"buffer_load_dwordx2 v[164:165], v12, s[40:43], s60 offen sc1\n"
"s_waitcnt vmcnt(0)\n"
"v_cmp_ne_u64 vcc, s[48:49], v[164:165]\n"
"s_cbranch_vccz .LJ_3px\n"
"s_sleep 1\n"
"s_sub_u32 s61, s61, 1\n"
"s_cbranch_scc0 .LJ_3p\n"
"s_branch .LJ_end\n"
".LJ_3px:\n"
"s_branch .LJ_3r\n"
".LJ_4s:\n"
"s_cmp_ge_i32 s90, 0x10000000\n"
"s_cselect_b64 s[84:85], 0, s[84:85]\n"
"s_cmp_ge_u32 s90, 0x10000000\n"
"s_cbranch_scc1 .LJ_4r\n"
"s_mov_b32 s61, 200000\n"
"s_nop 7\n"
".LJ_4p:\n"
"buffer_load_dwordx2 v[92:93], v15, s[40:43], s90 offen offset:0 sc1\n"
"s_waitcnt vmcnt(0)\n"
"v_cmp_ne_u64 vcc, s[84:85], v[92:93]\n"
"s_cbranch_vccz .LJ_4px\n"
"s_sleep 1\n"
"s_sub_u32 s61, s61, 1\n"
"s_cbranch_scc0 .LJ_4p\n"
"s_branch .LJ_end\n"
".LJ_4px:\n"
"s_branch .LJ_4r\n"
".LJ_ws:\n"
"s_mov_b64 s[76:77], s[68:69]\n"
"s_mov_b32 s78, s70\n"
"s_nop 7\n"
"v_mov_b32 v18, v20\n"
"v_add_u32 v19, 64, v20\n"
"v_mov_b32 v21, v22\n"
"s_mov_b32 s80, 0x7fffffff\n"
"s_mov_b32 s81, 4\n"
"s_mov_b32 s82, 0xff400000\n"
"s_mov_b32 s83, 0xff3f8800\n"
"s_branch .LJ_wr\n"
".LJ_wt0k6s:\n"
"s_mov_b64 s[76:77], s[68:69]\n"
"s_mov_b32 s78, s70\n"
"s_nop 7\n"
"v_mov_b32 v18, v20\n"
"v_add_u32 v19, 64, v20\n"
"v_mov_b32 v21, v22\n"
"s_mov_b32 s80, 0x7fffffff\n"
"s_mov_b32 s81, 4\n"
"s_mov_b32 s82, 0xff400000\n"
"s_mov_b32 s83, 0xff3f8800\n"
"s_branch .LJ_wt0k6r\n"
".LJ_4t0k7s:\n"
"s_cmp_ge_i32 s90, 0x10000000\n"
"s_cselect_b64 s[84:85], 0, s[84:85]\n"
"s_cmp_ge_u32 s90, 0x10000000\n"
"s_cbranch_scc1 .LJ_4t0k7r\n"
"s_mov_b32 s61, 200000\n"
"s_nop 7\n"
".LJ_4t0k7p:\n"
"buffer_load_dwordx2 v[92:93], v15, s[40:43], s90 offen offset:0 sc1\n"
"s_waitcnt vmcnt(0)\n"
"v_cmp_ne_u64 vcc, s[84:85], v[92:93]\n"
"s_cbranch_vccz .LJ_4t0k7px\n"
"s_sleep 1\n"
"s_sub_u32 s61, s61, 1\n"
"s_cbranch_scc0 .LJ_4t0k7p\n"
"s_branch .LJ_end\n"
".LJ_4t0k7px:\n"
"s_branch .LJ_4t0k7r\n"
".LJ_wt0k7s:\n"
"s_mov_b64 s[76:77], s[68:69]\n"
"s_mov_b32 s78, s70\n"
"s_nop 7\n"
"v_mov_b32 v18, v20\n"
"v_add_u32 v19, 64, v20\n"
"v_mov_b32 v21, v22\n"
"s_mov_b32 s80, 0x7fffffff\n"
"s_mov_b32 s81, 4\n"
"s_mov_b32 s82, 0xff400000\n"
"s_mov_b32 s83, 0xff3f8800\n"
"s_branch .LJ_wt0k7r\n"
".LJ_4t0k0s:\n"
"s_cmp_ge_i32 s90, 0x10000000\n"
"s_cbranch_scc1 .LJ_end\n"
"s_cselect_b64 s[84:85], 0, s[84:85]\n"
"s_cmp_ge_u32 s90, 0x10000000\n"
"s_cbranch_scc1 .LJ_4t0k0r\n"
"s_mov_b32 s61, 200000\n"
"s_nop 7\n"
".LJ_4t0k0p:\n"
"buffer_load_dwordx2 v[92:93], v15, s[40:43], s90 offen offset:0 sc1\n"
"s_waitcnt vmcnt(0)\n"
"v_cmp_ne_u64 vcc, s[84:85], v[92:93]\n"
"s_cbranch_vccz .LJ_4t0k0px\n"
"s_sleep 1\n"
"s_sub_u32 s61, s61, 1\n"
"s_cbranch_scc0 .LJ_4t0k0p\n"
"s_branch .LJ_end\n"
".LJ_4t0k0px:\n"
"s_branch .LJ_4t0k0r\n"
".LJ_wt0k0s:\n"
"s_mov_b64 s[76:77], s[68:69]\n"
"s_mov_b32 s78, s70\n"
"s_nop 7\n"
"v_mov_b32 v18, v20\n"
"v_add_u32 v19, 64, v20\n"
"v_mov_b32 v21, v22\n"
"s_mov_b32 s80, 0x7fffffff\n"
"s_mov_b32 s81, 4\n"
"s_mov_b32 s82, 0xff400000\n"
"s_mov_b32 s83, 0xff3f8800\n"
"s_branch .LJ_wt0k0r\n"
".LJ_wt1k6s:\n"
"s_mov_b64 s[76:77], s[68:69]\n"
"s_mov_b32 s78, s70\n"
"s_nop 7\n"
"v_mov_b32 v18, v20\n"
"v_add_u32 v19, 64, v20\n"
"v_mov_b32 v21, v22\n"
"s_mov_b32 s80, 0x7fffffff\n"
"s_mov_b32 s81, 4\n"
"s_mov_b32 s82, 0xff400000\n"
"s_mov_b32 s83, 0xff3f8800\n"
"s_branch .LJ_wt1k6r\n"
".LJ_4t1k7s:\n"
"s_cmp_ge_i32 s90, 0x10000000\n"
"s_cselect_b64 s[84:85], 0, s[84:85]\n"
"s_cmp_ge_u32 s90, 0x10000000\n"
"s_cbranch_scc1 .LJ_4t1k7r\n"
"s_mov_b32 s61, 200000\n"
"s_nop 7\n"
".LJ_4t1k7p:\n"
"buffer_load_dwordx2 v[92:93], v15, s[40:43], s90 offen offset:0 sc1\n"
"s_waitcnt vmcnt(0)\n"
"v_cmp_ne_u64 vcc, s[84:85], v[92:93]\n"
"s_cbranch_vccz .LJ_4t1k7px\n"
"s_sleep 1\n"
"s_sub_u32 s61, s61, 1\n"
"s_cbranch_scc0 .LJ_4t1k7p\n"
"s_branch .LJ_end\n"
".LJ_4t1k7px:\n"
"s_branch .LJ_4t1k7r\n"
".LJ_wt1k7s:\n"
"s_mov_b64 s[76:77], s[68:69]\n"
"s_mov_b32 s78, s70\n"
"s_nop 7\n"
"v_mov_b32 v18, v20\n"
"v_add_u32 v19, 64, v20\n"
"v_mov_b32 v21, v22\n"
"s_mov_b32 s80, 0x7fffffff\n"
"s_mov_b32 s81, 4\n"
"s_mov_b32 s82, 0xff400000\n"
"s_mov_b32 s83, 0xff3f8800\n"
"s_branch .LJ_wt1k7r\n"
".LJ_4t1k0s:\n"
"s_cmp_ge_i32 s90, 0x10000000\n"
"s_cbranch_scc1 .LJ_end\n"
"s_cselect_b64 s[84:85], 0, s[84:85]\n"
"s_cmp_ge_u32 s90, 0x10000000\n"
"s_cbranch_scc1 .LJ_4t1k0r\n"
"s_mov_b32 s61, 200000\n"
"s_nop 7\n"
".LJ_4t1k0p:\n"
"buffer_load_dwordx2 v[92:93], v15, s[40:43], s90 offen offset:0 sc1\n"
"s_waitcnt vmcnt(0)\n"
"v_cmp_ne_u64 vcc, s[84:85], v[92:93]\n"
"s_cbranch_vccz .LJ_4t1k0px\n"
"s_sleep 1\n"
"s_sub_u32 s61, s61, 1\n"
"s_cbranch_scc0 .LJ_4t1k0p\n"
"s_branch .LJ_end\n"
".LJ_4t1k0px:\n"
"s_branch .LJ_4t1k0r\n"
".LJ_wt1k0s:\n"
"s_mov_b64 s[76:77], s[68:69]\n"
"s_mov_b32 s78, s70\n"
"s_nop 7\n"
"v_mov_b32 v18, v20\n"
"v_add_u32 v19, 64, v20\n"
"v_mov_b32 v21, v22\n"
"s_mov_b32 s80, 0x7fffffff\n"
"s_mov_b32 s81, 4\n"
"s_mov_b32 s82, 0xff400000\n"
"s_mov_b32 s83, 0xff3f8800\n"
"s_branch .LJ_wt1k0r\n"
".LJ_4t2k7s:\n"
"s_cmp_ge_i32 s90, 0x10000000\n"
"s_cselect_b64 s[84:85], 0, s[84:85]\n"
"s_cmp_ge_u32 s90, 0x10000000\n"
"s_cbranch_scc1 .LJ_4t2k7r\n"
"s_mov_b32 s61, 200000\n"
"s_nop 7\n"
".LJ_4t2k7p:\n"
"buffer_load_dwordx2 v[92:93], v15, s[40:43], s90 offen offset:0 sc1\n"
"s_waitcnt vmcnt(0)\n"
"v_cmp_ne_u64 vcc, s[84:85], v[92:93]\n"
"s_cbranch_vccz .LJ_4t2k7px\n"
"s_sleep 1\n"
"s_sub_u32 s61, s61, 1\n"
"s_cbranch_scc0 .LJ_4t2k7p\n"
"s_branch .LJ_end\n"
".LJ_4t2k7px:\n"
"s_branch .LJ_4t2k7r\n"
".LJ_wt2k7s:\n"
"s_mov_b64 s[76:77], s[68:69]\n"
"s_mov_b32 s78, s70\n"
"s_nop 7\n"
"v_mov_b32 v18, v20\n"
"v_add_u32 v19, 64, v20\n"
"v_mov_b32 v21, v22\n"
"s_mov_b32 s80, 0x7fffffff\n"
"s_mov_b32 s81, 4\n"
"s_mov_b32 s82, 0xff400000\n"
"s_mov_b32 s83, 0xff3f8800\n"
"s_branch .LJ_wt2k7r\n"
".LJ_4t2k0s:\n"
"s_cmp_ge_i32 s90, 0x10000000\n"
"s_cbranch_scc1 .LJ_end\n"
"s_cselect_b64 s[84:85], 0, s[84:85]\n"
"s_cmp_ge_u32 s90, 0x10000000\n"
"s_cbranch_scc1 .LJ_4t2k0r\n"
"s_mov_b32 s61, 200000\n"
"s_nop 7\n"
".LJ_4t2k0p:\n"
"buffer_load_dwordx2 v[92:93], v15, s[40:43], s90 offen offset:0 sc1\n"
"s_waitcnt vmcnt(0)\n"
"v_cmp_ne_u64 vcc, s[84:85], v[92:93]\n"
"s_cbranch_vccz .LJ_4t2k0px\n"
"s_sleep 1\n"
"s_sub_u32 s61, s61, 1\n"
"s_cbranch_scc0 .LJ_4t2k0p\n"
"s_branch .LJ_end\n"
".LJ_4t2k0px:\n"
"s_branch .LJ_4t2k0r\n"
".LJ_wt2k0s:\n"
"s_mov_b64 s[76:77], s[68:69]\n"
"s_mov_b32 s78, s70\n"
"s_nop 7\n"
"v_mov_b32 v18, v20\n"
"v_add_u32 v19, 64, v20\n"
"v_mov_b32 v21, v22\n"
"s_mov_b32 s80, 0x7fffffff\n"
"s_mov_b32 s81, 4\n"
"s_mov_b32 s82, 0xff400000\n"
"s_mov_b32 s83, 0xff3f8800\n"
"s_branch .LJ_wt2k0r\n"
".LJ_t0:\n"
"v_mfma_f32_16x16x32_bf16 v[68:71], v[128:131], a[116:119], v[68:71]\n"
"ds_write_addtid_b32 v76 offset:2304\n"
"global_load_dwordx4 v[54:57], v16, s[62:63] offset:-2048 sc1\n"
"v_mfma_f32_16x16x32_bf16 v[68:71], v[132:135], a[120:123], v[68:71]\n"
"ds_write_addtid_b32 v77 offset:6944\n"
"v_mfma_f32_16x16x32_bf16 v[68:71], v[136:139], a[124:127], v[68:71]\n"
"ds_write_addtid_b32 v78 offset:11648\n"
"v_mfma_f32_16x16x32_bf16 v[68:71], v[140:143], a[128:131], v[68:71]\n"
"ds_write_addtid_b32 v79 offset:16288\n"
"v_mfma_f32_16x16x32_bf16 v[68:71], a[224:227], a[132:135], v[68:71]\n"
"v_mfma_f32_16x16x32_bf16 v[68:71], a[228:231], a[136:139], v[68:71]\n"
"v_mfma_f32_16x16x32_bf16 v[112:115], v[124:127], a[140:143], 0\n"
"global_load_dwordx4 v[58:61], v16, s[62:63] offset:-1024 sc1\n"
"v_mfma_f32_16x16x32_bf16 v[112:115], v[128:131], a[144:147], v[112:115]\n"
"ds_write_addtid_b32 v68 offset:2560\n"
"v_mfma_f32_16x16x32_bf16 v[112:115], v[132:135], a[148:151], v[112:115]\n"
"ds_write_addtid_b32 v69 offset:7200\n"
"v_mfma_f32_16x16x32_bf16 v[112:115], v[136:139], a[152:155], v[112:115]\n"
"ds_write_addtid_b32 v70 offset:11904\n"
"v_mfma_f32_16x16x32_bf16 v[112:115], v[140:143], a[156:159], v[112:115]\n"
"ds_write_addtid_b32 v71 offset:16544\n"
"v_mfma_f32_16x16x32_bf16 v[112:115], a[224:227], a[160:163], v[112:115]\n"
"global_load_dwordx4 v[62:65], v17, s[62:63] offset:0 sc1\n"
"v_mfma_f32_16x16x32_bf16 v[112:115], a[228:231], a[164:167], v[112:115]\n"
"v_mfma_f32_16x16x32_bf16 v[144:147], a[168:171], v[124:127], 0\n"
"v_mfma_f32_16x16x32_bf16 v[144:147], a[172:175], v[128:131], v[144:147]\n"
"ds_write_addtid_b32 v112 offset:2816\n"
"v_mfma_f32_16x16x32_bf16 v[144:147], a[176:179], v[132:135], v[144:147]\n"
"ds_write_addtid_b32 v113 offset:7456\n"
"v_mfma_f32_16x16x32_bf16 v[144:147], a[180:183], v[136:139], v[144:147]\n"
"ds_write_addtid_b32 v114 offset:12160\n"
"global_load_dwordx4 v[70:73], v16, s[62:63] offset:0 sc1\n"
"v_mfma_f32_16x16x32_bf16 v[144:147], a[184:187], v[140:143], v[144:147]\n"
"ds_write_addtid_b32 v115 offset:16800\n"
"v_mfma_f32_16x16x32_bf16 v[144:147], a[188:191], a[224:227], v[144:147]\n"
"v_mfma_f32_16x16x32_bf16 v[144:147], a[192:195], a[228:231], v[144:147]\n"
"v_mfma_f32_16x16x32_bf16 v[148:151], a[196:199], v[124:127], 0\n"
"v_mfma_f32_16x16x32_bf16 v[148:151], a[200:203], v[128:131], v[148:151]\n"
"s_waitcnt lgkmcnt(0)\n"
"global_load_dwordx4 v[74:77], v16, s[62:63] offset:1024 sc1\n"
"v_mfma_f32_16x16x32_bf16 v[148:151], a[204:207], v[132:135], v[148:151]\n"
"s_barrier\n"
"v_mfma_f32_16x16x32_bf16 v[148:151], a[208:211], v[136:139], v[148:151]\n"
"ds_read_b128 v[124:127], v2 offset:1536\n"
"v_mfma_f32_16x16x32_bf16 v[148:151], a[212:215], v[140:143], v[148:151]\n"
"ds_read_b128 v[128:131], v3 offset:2048\n"
"ds_read_b128 v[132:135], v4 offset:2560\n"
"v_mfma_f32_16x16x32_bf16 v[148:151], a[216:219], a[224:227], v[148:151]\n"
"ds_read_b128 v[136:139], v2 offset:1792\n"
"ds_read_b128 v[140:143], v3 offset:2304\n"
"v_mfma_f32_16x16x32_bf16 v[148:151], a[220:223], a[228:231], v[148:151]\n"
"s_waitcnt vmcnt(15)\n"
"ds_read_b128 v[152:155], v4 offset:2816\n"
"global_load_dwordx4 v[78:81], v16, s[62:63] offset:2048 sc1\n"
"v_mfma_f32_16x16x32_bf16 v[156:159], a[232:235], a[0:3], 0\n"
"v_mfma_f32_16x16x32_bf16 v[156:159], a[236:239], a[4:7], v[156:159]\n"
"s_waitcnt vmcnt(13)\n"
"v_mfma_f32_16x16x32_bf16 v[156:159], a[240:243], a[8:11], v[156:159]\n"
"v_mfma_f32_16x16x32_bf16 v[156:159], a[244:247], a[12:15], v[156:159]\n"
"s_waitcnt vmcnt(11)\n"
"v_mfma_f32_16x16x32_bf16 v[156:159], a[248:251], a[16:19], v[156:159]\n"
"s_waitcnt lgkmcnt(3)\n"
"v_add_f32 v124, v124, v128\n"
"v_add_f32 v125, v125, v129\n"
"v_mfma_f32_16x16x32_bf16 v[156:159], a[252:255], a[20:23], v[156:159]\n"
"s_waitcnt vmcnt(9)\n"
"v_add_f32 v126, v126, v130\n"
"v_add_f32 v127, v127, v131\n"
"v_mfma_f32_16x16x32_bf16 v[156:159], v[92:95], a[24:27], v[156:159]\n"
"v_add_f32 v124, v124, v132\n"
"v_mfma_f32_16x16x32_bf16 v[128:131], a[232:235], a[28:31], 0\n"
"v_add_f32 v125, v125, v133\n"
"v_add_f32 v126, v126, v134\n"
"v_mfma_f32_16x16x32_bf16 v[128:131], a[236:239], a[32:35], v[128:131]\n"
"ds_write_addtid_b32 v156 offset:3072\n"
"v_add_f32 v127, v127, v135\n"
"v_mfma_f32_16x16x32_bf16 v[128:131], a[240:243], a[36:39], v[128:131]\n"
"ds_write_addtid_b32 v157 offset:7712\n"
"v_add_f32 v124, v124, v144\n"
"v_mfma_f32_16x16x32_bf16 v[128:131], a[244:247], a[40:43], v[128:131]\n"
"ds_write_addtid_b32 v158 offset:12416\n"
"v_add_f32 v125, v125, v145\n"
"v_mfma_f32_16x16x32_bf16 v[128:131], a[248:251], a[44:47], v[128:131]\n"
"ds_write_addtid_b32 v159 offset:17056\n"
"v_add_f32 v126, v126, v146\n"
"v_mfma_f32_16x16x32_bf16 v[128:131], a[252:255], a[48:51], v[128:131]\n"
"global_load_dwordx4 v[82:85], v16, s[62:63] offset:3072 sc1\n"
"v_add_f32 v127, v127, v147\n"
"v_mfma_f32_16x16x32_bf16 v[128:131], v[92:95], a[52:55], v[128:131]\n"
"s_add_u32 s28, s50, 0xffe80000\n"
"s_and_b32 s31, s28, 0xfbffffff\n"
"v_cvt_pk_bf16_f32 v132, v124, v125\n"
"v_mfma_f32_16x16x32_bf16 v[144:147], a[232:235], a[56:59], 0\n"
"v_cvt_pk_bf16_f32 v133, v126, v127\n"
"v_mfma_f32_16x16x32_bf16 v[144:147], a[236:239], a[60:63], v[144:147]\n"
"s_waitcnt lgkmcnt(4)\n"
"ds_write_addtid_b32 v128 offset:3328\n"
"v_add_f32 v136, v136, v140\n"
"v_mfma_f32_16x16x32_bf16 v[144:147], a[240:243], a[64:67], v[144:147]\n"
"s_mov_b32 s91, s90\n"
"ds_write_addtid_b32 v129 offset:7968\n"
"v_add_f32 v137, v137, v141\n"
"v_mfma_f32_16x16x32_bf16 v[144:147], a[244:247], a[68:71], v[144:147]\n"
"ds_write_addtid_b32 v130 offset:12672\n"
"v_add_f32 v138, v138, v142\n"
"v_mfma_f32_16x16x32_bf16 v[144:147], a[248:251], a[72:75], v[144:147]\n"
"s_waitcnt vmcnt(8)\n"
"buffer_store_dwordx2 v[0:1], v14, s[40:43], s29 offen offset:0 sc1\n"
"ds_write_addtid_b32 v131 offset:17312\n"
"v_mfma_f32_16x16x32_bf16 v[144:147], a[252:255], a[76:79], v[144:147]\n"
"v_add_f32 v139, v139, v143\n"
"v_add_f32 v136, v136, v152\n"
"v_mfma_f32_16x16x32_bf16 v[144:147], v[92:95], a[80:83], v[144:147]\n"
"v_add_f32 v137, v137, v153\n"
"v_add_f32 v138, v138, v154\n"
"v_mfma_f32_16x16x32_bf16 v[128:131], a[232:235], a[84:87], 0\n"
"v_add_f32 v139, v139, v155\n"
"v_add_f32 v136, v136, v148\n"
"v_mfma_f32_16x16x32_bf16 v[128:131], a[236:239], a[88:91], v[128:131]\n"
"ds_write_addtid_b32 v144 offset:3584\n"
"v_add_f32 v137, v137, v149\n"
"v_mfma_f32_16x16x32_bf16 v[128:131], a[240:243], a[92:95], v[128:131]\n"
"ds_write_addtid_b32 v145 offset:8224\n"
"v_add_f32 v138, v138, v150\n"
"v_mfma_f32_16x16x32_bf16 v[128:131], a[244:247], a[96:99], v[128:131]\n"
"ds_write_addtid_b32 v146 offset:12928\n"
"v_add_f32 v139, v139, v151\n"
"v_mfma_f32_16x16x32_bf16 v[128:131], a[248:251], a[100:103], v[128:131]\n"
"ds_write_addtid_b32 v147 offset:17568\n"
"v_cvt_pk_bf16_f32 v134, v136, v137\n"
"v_mfma_f32_16x16x32_bf16 v[128:131], a[252:255], a[104:107], v[128:131]\n"
"v_cvt_pk_bf16_f32 v135, v138, v139\n"
"v_mfma_f32_16x16x32_bf16 v[128:131], v[92:95], a[108:111], v[128:131]\n"
"buffer_store_dwordx4 v[132:135], v13, s[40:43], s31 offen sc1\n"
"global_load_dwordx4 v[86:89], v17, s[62:63] offset:1024 sc1\n"
"v_mfma_f32_16x16x32_bf16 v[136:139], a[232:235], a[112:115], 0\n"
"v_mov_b32 v44, v26\n"
"v_mfma_f32_16x16x32_bf16 v[136:139], a[236:239], a[116:119], v[136:139]\n"
"ds_write_addtid_b32 v128 offset:3840\n"
"v_mov_b32 v45, v27\n"
"v_mfma_f32_16x16x32_bf16 v[136:139], a[240:243], a[120:123], v[136:139]\n"
"ds_write_addtid_b32 v129 offset:8480\n"
"v_mov_b32 v66, v28\n"
"v_mfma_f32_16x16x32_bf16 v[136:139], a[244:247], a[124:127], v[136:139]\n"
"ds_write_addtid_b32 v130 offset:13184\n"
"v_mov_b32 v67, v29\n"
"v_mfma_f32_16x16x32_bf16 v[136:139], a[248:251], a[128:131], v[136:139]\n"
"s_waitcnt vmcnt(7)\n"
"ds_write_addtid_b32 v131 offset:17824\n"
"v_mfma_f32_16x16x32_bf16 v[132:135], v[38:41], v[44:47], 0\n"
"s_cmp_ge_i32 s91, s80\n"
"s_cbranch_scc1 .LJ_wt0k6s\n"
".LJ_wt0k6r:\n"
"v_mfma_f32_16x16x32_bf16 v[132:135], v[38:41], v[52:55], v[132:135]\n"
"s_lshr_b32 s29, s91, s81\n"
"v_mfma_f32_16x16x32_bf16 v[132:135], v[38:41], v[60:63], v[132:135]\n"
"s_bitcmp1_b32 s29, 15\n"
"s_cselect_b32 s31, s83, s82\n"
"v_mfma_f32_16x16x32_bf16 v[128:131], v[38:41], v[48:51], 0\n"
"s_add_u32 s29, s29, s31\n"
"v_mfma_f32_16x16x32_bf16 v[128:131], v[38:41], v[56:59], v[128:131]\n"
"v_mfma_f32_16x16x32_bf16 v[128:131], v[38:41], v[64:67], v[128:131]\n"
"v_cvt_pk_bf16_f32 v144, v132, v133\n"
"v_cvt_pk_bf16_f32 v145, v134, v135\n"
"v_mfma_f32_16x16x32_bf16 v[136:139], a[252:255], a[132:135], v[136:139]\n"
"v_mfma_f32_16x16x32_bf16 v[136:139], v[92:95], a[136:139], v[136:139]\n"
"v_mfma_f32_16x16x32_bf16 v[44:47], a[232:235], a[140:143], 0\n"
"v_cvt_pk_bf16_f32 v146, v128, v129\n"
"v_cvt_pk_bf16_f32 v147, v130, v131\n"
"v_mfma_f32_16x16x32_bf16 v[44:47], a[236:239], a[144:147], v[44:47]\n"
"ds_write_addtid_b32 v136 offset:4096\n"
"v_mov_b32 v68, v30\n"
"v_mfma_f32_16x16x32_bf16 v[44:47], a[240:243], a[148:151], v[44:47]\n"
"ds_write_addtid_b32 v137 offset:8736\n"
"v_mfma_f32_16x16x32_bf16 v[44:47], a[244:247], a[152:155], v[44:47]\n"
"ds_write_addtid_b32 v138 offset:13440\n"
"v_mov_b32 v69, v31\n"
"v_mfma_f32_16x16x32_bf16 v[44:47], a[248:251], a[156:159], v[44:47]\n"
"ds_write_addtid_b32 v139 offset:18080\n"
"v_mov_b32 v90, v32\n"
"v_mfma_f32_16x16x32_bf16 v[44:47], a[252:255], a[160:163], v[44:47]\n"
"ds_bpermute_b32 v48, v21, v144\n"
"v_mfma_f32_16x16x32_bf16 v[44:47], v[92:95], a[164:167], v[44:47]\n"
"ds_bpermute_b32 v49, v21, v145\n"
"v_mfma_f32_16x16x32_bf16 v[52:55], a[168:171], a[232:235], 0\n"
"v_mov_b32 v91, v33\n"
"v_mfma_f32_16x16x32_bf16 v[52:55], a[172:175], a[236:239], v[52:55]\n"
"ds_write_addtid_b32 v44 offset:4352\n"
"v_mfma_f32_16x16x32_bf16 v[52:55], a[176:179], a[240:243], v[52:55]\n"
"ds_write_addtid_b32 v45 offset:8992\n"
"v_mfma_f32_16x16x32_bf16 v[52:55], a[180:183], a[244:247], v[52:55]\n"
"ds_write_addtid_b32 v46 offset:13696\n"
"v_mfma_f32_16x16x32_bf16 v[52:55], a[184:187], a[248:251], v[52:55]\n"
"ds_write_addtid_b32 v47 offset:18336\n"
"v_mfma_f32_16x16x32_bf16 v[52:55], a[188:191], a[252:255], v[52:55]\n"
"v_mfma_f32_16x16x32_bf16 v[52:55], a[192:195], v[92:95], v[52:55]\n"
"ds_bpermute_b32 v50, v21, v146\n"
"v_mfma_f32_16x16x32_bf16 v[148:151], a[196:199], a[232:235], 0\n"
"ds_bpermute_b32 v51, v21, v147\n"
"v_mfma_f32_16x16x32_bf16 v[148:151], a[200:203], a[236:239], v[148:151]\n"
"s_waitcnt lgkmcnt(2)\n"
"v_mfma_f32_16x16x32_bf16 v[148:151], a[204:207], a[240:243], v[148:151]\n"
"s_barrier\n"
"v_mfma_f32_16x16x32_bf16 v[148:151], a[208:211], a[244:247], v[148:151]\n"
"ds_read_b128 v[60:63], v2 offset:3072\n"
"v_mfma_f32_16x16x32_bf16 v[148:151], a[212:215], a[248:251], v[148:151]\n"
"ds_read_b128 v[56:59], v3 offset:3584\n"
"ds_read_b128 v[64:67], v4 offset:4096\n"
"v_mfma_f32_16x16x32_bf16 v[148:151], a[216:219], a[252:255], v[148:151]\n"
"s_add_u32 s90, s50, 0xffc80000\n"
"ds_read_b128 v[152:155], v2 offset:3328\n"
"ds_read_b128 v[156:159], v3 offset:3840\n"
"v_mfma_f32_16x16x32_bf16 v[148:151], a[220:223], v[92:95], v[148:151]\n"
"ds_read_b128 v[160:163], v4 offset:4352\n"
"s_cmp_lt_u32 s90, 0x8000000\n"
"s_cselect_b32 s90, 0x80000000, s90\n"
"s_waitcnt lgkmcnt(6)\n"
"buffer_load_dwordx2 v[92:93], v15, s[40:43], s90 offen offset:0 sc1\n"
"buffer_store_dwordx4 v[48:51], v18, s[76:79], s29 offen sc1\n"
"s_waitcnt lgkmcnt(3)\n"
"v_add_f32 v60, v60, v56\n"
"v_add_f32 v61, v61, v57\n"
"v_add_f32 v62, v62, v58\n"
"v_add_f32 v63, v63, v59\n"
"v_add_f32 v60, v60, v64\n"
"v_add_f32 v61, v61, v65\n"
"v_add_f32 v62, v62, v66\n"
"v_add_f32 v63, v63, v67\n"
"v_add_f32 v60, v60, v52\n"
"v_add_f32 v61, v61, v53\n"
"v_add_f32 v62, v62, v54\n"
"v_add_f32 v63, v63, v55\n"
"s_add_u32 s31, s50, 0x0\n"
"s_and_b32 s30, s31, 0xfbffffff\n"
"v_cvt_pk_bf16_f32 v44, v60, v61\n"
"s_waitcnt vmcnt(2)\n"
"v_cvt_pk_bf16_f32 v45, v62, v63\n"
"v_mfma_f32_16x16x32_bf16 v[56:59], v[38:41], v[68:71], 0\n"
"v_mfma_f32_16x16x32_bf16 v[56:59], v[38:41], v[76:79], v[56:59]\n"
"v_mfma_f32_16x16x32_bf16 v[56:59], v[38:41], v[84:87], v[56:59]\n"
"v_mfma_f32_16x16x32_bf16 v[60:63], v[38:41], v[72:75], 0\n"
"v_mfma_f32_16x16x32_bf16 v[60:63], v[38:41], v[80:83], v[60:63]\n"
"v_mfma_f32_16x16x32_bf16 v[60:63], v[38:41], v[88:91], v[60:63]\n"
"v_cvt_pk_bf16_f32 v64, v56, v57\n"
"v_cvt_pk_bf16_f32 v65, v58, v59\n"
"s_waitcnt lgkmcnt(0)\n"
"v_add_f32 v152, v152, v156\n"
"v_add_f32 v153, v153, v157\n"
"v_add_f32 v154, v154, v158\n"
"v_add_f32 v155, v155, v159\n"
"s_waitcnt vmcnt(3)\n"
"buffer_store_dwordx2 v[0:1], v14, s[40:43], s28 offen offset:0 sc1\n"
"v_add_f32 v152, v152, v160\n"
"v_add_f32 v153, v153, v161\n"
"v_add_f32 v154, v154, v162\n"
"v_add_f32 v155, v155, v163\n"
"v_add_f32 v152, v152, v148\n"
"v_add_f32 v153, v153, v149\n"
"v_add_f32 v154, v154, v150\n"
"v_add_f32 v155, v155, v151\n"
"v_cvt_pk_bf16_f32 v46, v152, v153\n"
"v_cvt_pk_bf16_f32 v47, v154, v155\n"
"buffer_store_dwordx4 v[44:47], v13, s[40:43], s30 offen sc1\n"
"v_cvt_pk_bf16_f32 v66, v60, v61\n"
"v_cvt_pk_bf16_f32 v67, v62, v63\n"
"ds_bpermute_b32 v44, v21, v64\n"
"ds_bpermute_b32 v45, v21, v65\n"
"ds_bpermute_b32 v46, v21, v66\n"
"ds_bpermute_b32 v47, v21, v67\n"
"s_waitcnt lgkmcnt(0)\n"
"buffer_store_dwordx4 v[44:47], v19, s[76:79], s29 offen sc1\n"
"s_and_b32 s30, s90, 0x3ffffff\n"
"s_waitcnt vmcnt(4)\n"
"s_add_u32 s62, s40, s30\n"
"s_addc_u32 s63, s41, 0\n"
"v_cmp_ne_u64 vcc, s[84:85], v[92:93]\n"
"s_cbranch_vccnz .LJ_4t0k7s\n"
".LJ_4t0k7r:\n"
"global_load_dwordx4 v[46:49], v16, s[62:63] offset:-4096 sc1\n"
"global_load_dwordx4 v[50:53], v16, s[62:63] offset:-3072 sc1\n"
"s_waitcnt vmcnt(3)\n"
"buffer_store_dwordx2 v[0:1], v14, s[40:43], s31 offen offset:0 sc1\n"
"global_load_dwordx4 v[54:57], v16, s[62:63] offset:-2048 sc1\n"
"global_load_dwordx4 v[58:61], v16, s[62:63] offset:-1024 sc1\n"
"global_load_dwordx4 v[62:65], v17, s[62:63] offset:0 sc1\n"
"global_load_dwordx4 v[70:73], v16, s[62:63] offset:0 sc1\n"
"global_load_dwordx4 v[74:77], v16, s[62:63] offset:1024 sc1\n"
"global_load_dwordx4 v[78:81], v16, s[62:63] offset:2048 sc1\n"
"global_load_dwordx4 v[82:85], v16, s[62:63] offset:3072 sc1\n"
"s_mov_b32 s91, s90\n"
"global_load_dwordx4 v[86:89], v17, s[62:63] offset:1024 sc1\n"
"v_mov_b32 v44, v26\n"
"v_mov_b32 v45, v27\n"
"v_mov_b32 v66, v28\n"
"v_mov_b32 v67, v29\n"
"s_waitcnt vmcnt(5)\n"
"v_mfma_f32_16x16x32_bf16 v[132:135], v[38:41], v[44:47], 0\n"
"s_cmp_ge_i32 s91, s80\n"
"s_cbranch_scc1 .LJ_wt0k7s\n"
".LJ_wt0k7r:\n"
"v_mfma_f32_16x16x32_bf16 v[132:135], v[38:41], v[52:55], v[132:135]\n"
"s_lshr_b32 s29, s91, s81\n"
"v_mfma_f32_16x16x32_bf16 v[132:135], v[38:41], v[60:63], v[132:135]\n"
"s_bitcmp1_b32 s29, 15\n"
"s_cselect_b32 s31, s83, s82\n"
"v_mfma_f32_16x16x32_bf16 v[128:131], v[38:41], v[48:51], 0\n"
"s_add_u32 s29, s29, s31\n"
"v_mfma_f32_16x16x32_bf16 v[128:131], v[38:41], v[56:59], v[128:131]\n"
"v_mfma_f32_16x16x32_bf16 v[128:131], v[38:41], v[64:67], v[128:131]\n"
"v_cvt_pk_bf16_f32 v144, v132, v133\n"
"v_cvt_pk_bf16_f32 v145, v134, v135\n"
"s_nop 5\n"
"v_cvt_pk_bf16_f32 v146, v128, v129\n"
"v_cvt_pk_bf16_f32 v147, v130, v131\n"
"v_mov_b32 v68, v30\n"
"v_mov_b32 v69, v31\n"
"v_mov_b32 v90, v32\n"
"ds_bpermute_b32 v48, v21, v144\n"
"ds_bpermute_b32 v49, v21, v145\n"
"v_mov_b32 v91, v33\n"
"ds_bpermute_b32 v50, v21, v146\n"
"ds_bpermute_b32 v51, v21, v147\n"
"s_add_u32 s90, s50, 0x100000\n"
"s_cmp_lt_u32 s90, 0x8000000\n"
"s_cselect_b32 s90, 0x80000000, s90\n"
"s_waitcnt lgkmcnt(0)\n"
"buffer_load_dwordx2 v[92:93], v15, s[40:43], s90 offen offset:0 sc1\n"
"buffer_store_dwordx4 v[48:51], v18, s[76:79], s29 offen sc1\n"
"s_waitcnt vmcnt(2)\n"
"v_mfma_f32_16x16x32_bf16 v[56:59], v[38:41], v[68:71], 0\n"
"v_mfma_f32_16x16x32_bf16 v[56:59], v[38:41], v[76:79], v[56:59]\n"
"v_mfma_f32_16x16x32_bf16 v[56:59], v[38:41], v[84:87], v[56:59]\n"
"v_mfma_f32_16x16x32_bf16 v[60:63], v[38:41], v[72:75], 0\n"
"v_mfma_f32_16x16x32_bf16 v[60:63], v[38:41], v[80:83], v[60:63]\n"
"v_mfma_f32_16x16x32_bf16 v[60:63], v[38:41], v[88:91], v[60:63]\n"
"v_cvt_pk_bf16_f32 v64, v56, v57\n"
"v_cvt_pk_bf16_f32 v65, v58, v59\n"
"s_nop 5\n"
"v_cvt_pk_bf16_f32 v66, v60, v61\n"
"v_cvt_pk_bf16_f32 v67, v62, v63\n"
"ds_bpermute_b32 v44, v21, v64\n"
"ds_bpermute_b32 v45, v21, v65\n"
"ds_bpermute_b32 v46, v21, v66\n"
"ds_bpermute_b32 v47, v21, v67\n"
"s_waitcnt lgkmcnt(0)\n"
"buffer_store_dwordx4 v[44:47], v19, s[76:79], s29 offen sc1\n"
"s_and_b32 s30, s90, 0x3ffffff\n"
"s_waitcnt vmcnt(2)\n"
"s_add_u32 s62, s40, s30\n"
"s_addc_u32 s63, s41, 0\n"
"v_cmp_ne_u64 vcc, s[84:85], v[92:93]\n"
"s_cbranch_vccnz .LJ_4t0k0s\n"
".LJ_4t0k0r:\n"
"global_load_dwordx4 v[46:49], v16, s[62:63] offset:-4096 sc1\n"
"global_load_dwordx4 v[50:53], v16, s[62:63] offset:-3072 sc1\n"
"global_load_dwordx4 v[54:57], v16, s[62:63] offset:-2048 sc1\n"
"global_load_dwordx4 v[58:61], v16, s[62:63] offset:-1024 sc1\n"
"global_load_dwordx4 v[62:65], v17, s[62:63] offset:0 sc1\n"
"global_load_dwordx4 v[70:73], v16, s[62:63] offset:0 sc1\n"
"global_load_dwordx4 v[74:77], v16, s[62:63] offset:1024 sc1\n"
"global_load_dwordx4 v[78:81], v16, s[62:63] offset:2048 sc1\n"
"global_load_dwordx4 v[82:85], v16, s[62:63] offset:3072 sc1\n"
"s_mov_b32 s91, s90\n"
"global_load_dwordx4 v[86:89], v17, s[62:63] offset:1024 sc1\n"
"v_mov_b32 v44, v26\n"
"v_mov_b32 v45, v27\n"
"v_mov_b32 v66, v28\n"
"v_mov_b32 v67, v29\n"
"s_waitcnt vmcnt(5)\n"
"v_mfma_f32_16x16x32_bf16 v[132:135], v[38:41], v[44:47], 0\n"
"s_cmp_ge_i32 s91, s80\n"
"s_cbranch_scc1 .LJ_wt0k0s\n"
".LJ_wt0k0r:\n"
"v_mfma_f32_16x16x32_bf16 v[132:135], v[38:41], v[52:55], v[132:135]\n"
"s_lshr_b32 s29, s91, s81\n"
"v_mfma_f32_16x16x32_bf16 v[132:135], v[38:41], v[60:63], v[132:135]\n"
"s_bitcmp1_b32 s29, 15\n"
"s_cselect_b32 s31, s83, s82\n"
"v_mfma_f32_16x16x32_bf16 v[128:131], v[38:41], v[48:51], 0\n"
"s_add_u32 s29, s29, s31\n"
"v_mfma_f32_16x16x32_bf16 v[128:131], v[38:41], v[56:59], v[128:131]\n"
"v_mfma_f32_16x16x32_bf16 v[128:131], v[38:41], v[64:67], v[128:131]\n"
"v_cvt_pk_bf16_f32 v144, v132, v133\n"
"v_cvt_pk_bf16_f32 v145, v134, v135\n"
"s_nop 5\n"
"v_cvt_pk_bf16_f32 v146, v128, v129\n"
"v_cvt_pk_bf16_f32 v147, v130, v131\n"
"v_mov_b32 v68, v30\n"
"v_mov_b32 v69, v31\n"
"v_mov_b32 v90, v32\n"
"ds_bpermute_b32 v48, v21, v144\n"
"ds_bpermute_b32 v49, v21, v145\n"
"v_mov_b32 v91, v33\n"
"ds_bpermute_b32 v50, v21, v146\n"
"ds_bpermute_b32 v51, v21, v147\n"
"s_waitcnt lgkmcnt(0)\n"
"buffer_store_dwordx4 v[48:51], v18, s[76:79], s29 offen sc1\n"
"s_waitcnt vmcnt(1)\n"
"v_mfma_f32_16x16x32_bf16 v[56:59], v[38:41], v[68:71], 0\n"
"v_mfma_f32_16x16x32_bf16 v[56:59], v[38:41], v[76:79], v[56:59]\n"
"v_mfma_f32_16x16x32_bf16 v[56:59], v[38:41], v[84:87], v[56:59]\n"
"v_mfma_f32_16x16x32_bf16 v[60:63], v[38:41], v[72:75], 0\n"
"v_mfma_f32_16x16x32_bf16 v[60:63], v[38:41], v[80:83], v[60:63]\n"
"v_mfma_f32_16x16x32_bf16 v[60:63], v[38:41], v[88:91], v[60:63]\n"
"v_cvt_pk_bf16_f32 v64, v56, v57\n"
"v_cvt_pk_bf16_f32 v65, v58, v59\n"
"s_nop 5\n"
"v_cvt_pk_bf16_f32 v66, v60, v61\n"
"v_cvt_pk_bf16_f32 v67, v62, v63\n"
"ds_bpermute_b32 v44, v21, v64\n"
"ds_bpermute_b32 v45, v21, v65\n"
"ds_bpermute_b32 v46, v21, v66\n"
"ds_bpermute_b32 v47, v21, v67\n"
"s_waitcnt lgkmcnt(0)\n"
"buffer_store_dwordx4 v[44:47], v19, s[76:79], s29 offen sc1\n"
"s_branch .LJ_end\n"
".LJ_t1:\n"
"v_mov_b32 v44, v26\n"
"v_mfma_f32_16x16x32_bf16 v[136:139], a[236:239], a[116:119], v[136:139]\n"
"ds_write_addtid_b32 v128 offset:3840\n"
"v_mov_b32 v45, v27\n"
"v_mfma_f32_16x16x32_bf16 v[136:139], a[240:243], a[120:123], v[136:139]\n"
"ds_write_addtid_b32 v129 offset:8480\n"
"v_mov_b32 v66, v28\n"
"v_mfma_f32_16x16x32_bf16 v[136:139], a[244:247], a[124:127], v[136:139]\n"
"ds_write_addtid_b32 v130 offset:13184\n"
"v_mov_b32 v67, v29\n"
"v_mfma_f32_16x16x32_bf16 v[136:139], a[248:251], a[128:131], v[136:139]\n"
"s_waitcnt vmcnt(12)\n"
"ds_write_addtid_b32 v131 offset:17824\n"
"v_mfma_f32_16x16x32_bf16 v[132:135], v[38:41], v[44:47], 0\n"
"s_cmp_ge_i32 s91, s80\n"
"s_cbranch_scc1 .LJ_wt1k6s\n"
".LJ_wt1k6r:\n"
"v_mfma_f32_16x16x32_bf16 v[132:135], v[38:41], v[52:55], v[132:135]\n"
"s_lshr_b32 s29, s91, s81\n"
"v_mfma_f32_16x16x32_bf16 v[132:135], v[38:41], v[60:63], v[132:135]\n"
"s_bitcmp1_b32 s29, 15\n"
"s_cselect_b32 s31, s83, s82\n"
"v_mfma_f32_16x16x32_bf16 v[128:131], v[38:41], v[48:51], 0\n"
"s_add_u32 s29, s29, s31\n"
"v_mfma_f32_16x16x32_bf16 v[128:131], v[38:41], v[56:59], v[128:131]\n"
"v_mfma_f32_16x16x32_bf16 v[128:131], v[38:41], v[64:67], v[128:131]\n"
"v_cvt_pk_bf16_f32 v144, v132, v133\n"
"v_cvt_pk_bf16_f32 v145, v134, v135\n"
"v_mfma_f32_16x16x32_bf16 v[136:139], a[252:255], a[132:135], v[136:139]\n"
"v_mfma_f32_16x16x32_bf16 v[136:139], v[92:95], a[136:139], v[136:139]\n"
"v_mfma_f32_16x16x32_bf16 v[44:47], a[232:235], a[140:143], 0\n"
"v_cvt_pk_bf16_f32 v146, v128, v129\n"
"v_cvt_pk_bf16_f32 v147, v130, v131\n"
"v_mfma_f32_16x16x32_bf16 v[44:47], a[236:239], a[144:147], v[44:47]\n"
"ds_write_addtid_b32 v136 offset:4096\n"
"v_mov_b32 v68, v30\n"
"v_mfma_f32_16x16x32_bf16 v[44:47], a[240:243], a[148:151], v[44:47]\n"
"ds_write_addtid_b32 v137 offset:8736\n"
"v_mfma_f32_16x16x32_bf16 v[44:47], a[244:247], a[152:155], v[44:47]\n"
"ds_write_addtid_b32 v138 offset:13440\n"
"v_mov_b32 v69, v31\n"
"v_mfma_f32_16x16x32_bf16 v[44:47], a[248:251], a[156:159], v[44:47]\n"
"ds_write_addtid_b32 v139 offset:18080\n"
"v_mov_b32 v90, v32\n"
"v_mfma_f32_16x16x32_bf16 v[44:47], a[252:255], a[160:163], v[44:47]\n"
"ds_bpermute_b32 v48, v21, v144\n"
"v_mfma_f32_16x16x32_bf16 v[44:47], v[92:95], a[164:167], v[44:47]\n"
"ds_bpermute_b32 v49, v21, v145\n"
"v_mfma_f32_16x16x32_bf16 v[52:55], a[168:171], a[232:235], 0\n"
"v_mov_b32 v91, v33\n"
"v_mfma_f32_16x16x32_bf16 v[52:55], a[172:175], a[236:239], v[52:55]\n"
"ds_write_addtid_b32 v44 offset:4352\n"
"v_mfma_f32_16x16x32_bf16 v[52:55], a[176:179], a[240:243], v[52:55]\n"
"ds_write_addtid_b32 v45 offset:8992\n"
"v_mfma_f32_16x16x32_bf16 v[52:55], a[180:183], a[244:247], v[52:55]\n"
"ds_write_addtid_b32 v46 offset:13696\n"
"v_mfma_f32_16x16x32_bf16 v[52:55], a[184:187], a[248:251], v[52:55]\n"
"ds_write_addtid_b32 v47 offset:18336\n"
"v_mfma_f32_16x16x32_bf16 v[52:55], a[188:191], a[252:255], v[52:55]\n"
"v_mfma_f32_16x16x32_bf16 v[52:55], a[192:195], v[92:95], v[52:55]\n"
"ds_bpermute_b32 v50, v21, v146\n"
"v_mfma_f32_16x16x32_bf16 v[148:151], a[196:199], a[232:235], 0\n"
"ds_bpermute_b32 v51, v21, v147\n"
"v_mfma_f32_16x16x32_bf16 v[148:151], a[200:203], a[236:239], v[148:151]\n"
"s_waitcnt lgkmcnt(2)\n"
"v_mfma_f32_16x16x32_bf16 v[148:151], a[204:207], a[240:243], v[148:151]\n"
"s_barrier\n"
"v_mfma_f32_16x16x32_bf16 v[148:151], a[208:211], a[244:247], v[148:151]\n"
"ds_read_b128 v[60:63], v2 offset:3072\n"
"v_mfma_f32_16x16x32_bf16 v[148:151], a[212:215], a[248:251], v[148:151]\n"
"ds_read_b128 v[56:59], v3 offset:3584\n"
"ds_read_b128 v[64:67], v4 offset:4096\n"
"v_mfma_f32_16x16x32_bf16 v[148:151], a[216:219], a[252:255], v[148:151]\n"
"s_add_u32 s90, s50, 0xffb00000\n"
"ds_read_b128 v[152:155], v2 offset:3328\n"
"ds_read_b128 v[156:159], v3 offset:3840\n"
"v_mfma_f32_16x16x32_bf16 v[148:151], a[220:223], v[92:95], v[148:151]\n"
"s_waitcnt vmcnt(14)\n"
"ds_read_b128 v[160:163], v4 offset:4352\n"
"v_mfma_f32_16x16x32_bf16 v[44:47], v[96:99], a[0:3], 0\n"
"s_cmp_lt_u32 s90, 0x8000000\n"
"s_cselect_b32 s90, 0x80000000, s90\n"
"v_mfma_f32_16x16x32_bf16 v[44:47], v[100:103], a[4:7], v[44:47]\n"
"s_waitcnt vmcnt(9)\n"
"v_mfma_f32_16x16x32_bf16 v[44:47], v[104:107], a[8:11], v[44:47]\n"
"s_waitcnt lgkmcnt(6)\n"
"buffer_load_dwordx2 v[92:93], v15, s[40:43], s90 offen offset:0 sc1\n"
"v_mfma_f32_16x16x32_bf16 v[44:47], v[108:111], a[12:15], v[44:47]\n"
"s_waitcnt vmcnt(6)\n"
"buffer_store_dwordx4 v[48:51], v18, s[76:79], s29 offen sc1\n"
"v_mfma_f32_16x16x32_bf16 v[44:47], v[112:115], a[16:19], v[44:47]\n"
"s_waitcnt lgkmcnt(3)\n"
"v_add_f32 v60, v60, v56\n"
"v_add_f32 v61, v61, v57\n"
"v_mfma_f32_16x16x32_bf16 v[44:47], v[116:119], a[20:23], v[44:47]\n"
"s_waitcnt vmcnt(6)\n"
"v_add_f32 v62, v62, v58\n"
"v_add_f32 v63, v63, v59\n"
"v_mfma_f32_16x16x32_bf16 v[44:47], v[120:123], a[24:27], v[44:47]\n"
"v_add_f32 v60, v60, v64\n"
"v_mfma_f32_16x16x32_bf16 v[48:51], v[96:99], a[28:31], 0\n"
"v_add_f32 v61, v61, v65\n"
"v_add_f32 v62, v62, v66\n"
"v_mfma_f32_16x16x32_bf16 v[48:51], v[100:103], a[32:35], v[48:51]\n"
"ds_write_addtid_b32 v44 offset:0\n"
"v_add_f32 v63, v63, v67\n"
"v_mfma_f32_16x16x32_bf16 v[48:51], v[104:107], a[36:39], v[48:51]\n"
"ds_write_addtid_b32 v45 offset:4640\n"
"v_add_f32 v60, v60, v52\n"
"v_mfma_f32_16x16x32_bf16 v[48:51], v[108:111], a[40:43], v[48:51]\n"
"ds_write_addtid_b32 v46 offset:9344\n"
"v_add_f32 v61, v61, v53\n"
"v_mfma_f32_16x16x32_bf16 v[48:51], v[112:115], a[44:47], v[48:51]\n"
"ds_write_addtid_b32 v47 offset:13984\n"
"v_add_f32 v62, v62, v54\n"
"v_mfma_f32_16x16x32_bf16 v[48:51], v[116:119], a[48:51], v[48:51]\n"
"v_add_f32 v63, v63, v55\n"
"v_mfma_f32_16x16x32_bf16 v[48:51], v[120:123], a[52:55], v[48:51]\n"
"s_add_u32 s31, s50, 0xffe80000\n"
"s_and_b32 s30, s31, 0xfbffffff\n"
"v_cvt_pk_bf16_f32 v44, v60, v61\n"
"v_mfma_f32_16x16x32_bf16 v[52:55], v[96:99], a[56:59], 0\n"
"s_waitcnt vmcnt(2)\n"
"v_cvt_pk_bf16_f32 v45, v62, v63\n"
"v_mfma_f32_16x16x32_bf16 v[56:59], v[38:41], v[68:71], 0\n"
"v_mfma_f32_16x16x32_bf16 v[56:59], v[38:41], v[76:79], v[56:59]\n"
"v_mfma_f32_16x16x32_bf16 v[56:59], v[38:41], v[84:87], v[56:59]\n"
"v_mfma_f32_16x16x32_bf16 v[60:63], v[38:41], v[72:75], 0\n"
"v_mfma_f32_16x16x32_bf16 v[60:63], v[38:41], v[80:83], v[60:63]\n"
"v_mfma_f32_16x16x32_bf16 v[60:63], v[38:41], v[88:91], v[60:63]\n"
"v_cvt_pk_bf16_f32 v64, v56, v57\n"
"v_cvt_pk_bf16_f32 v65, v58, v59\n"
"v_mfma_f32_16x16x32_bf16 v[52:55], v[100:103], a[60:63], v[52:55]\n"
"s_waitcnt lgkmcnt(4)\n"
"ds_write_addtid_b32 v48 offset:256\n"
"v_add_f32 v152, v152, v156\n"
"v_mfma_f32_16x16x32_bf16 v[52:55], v[104:107], a[64:67], v[52:55]\n"
"ds_write_addtid_b32 v49 offset:4896\n"
"v_add_f32 v153, v153, v157\n"
"v_mfma_f32_16x16x32_bf16 v[52:55], v[108:111], a[68:71], v[52:55]\n"
"ds_write_addtid_b32 v50 offset:9600\n"
"v_add_f32 v154, v154, v158\n"
"v_mfma_f32_16x16x32_bf16 v[52:55], v[112:115], a[72:75], v[52:55]\n"
"ds_write_addtid_b32 v51 offset:14240\n"
"v_add_f32 v155, v155, v159\n"
"v_mfma_f32_16x16x32_bf16 v[52:55], v[116:119], a[76:79], v[52:55]\n"
"s_waitcnt vmcnt(3)\n"
"buffer_store_dwordx2 v[0:1], v14, s[40:43], s28 offen offset:0 sc1\n"
"v_add_f32 v152, v152, v160\n"
"v_mfma_f32_16x16x32_bf16 v[52:55], v[120:123], a[80:83], v[52:55]\n"
"v_add_f32 v153, v153, v161\n"
"v_add_f32 v154, v154, v162\n"
"v_mfma_f32_16x16x32_bf16 v[48:51], v[96:99], a[84:87], 0\n"
"v_add_f32 v155, v155, v163\n"
"v_add_f32 v152, v152, v148\n"
"v_mfma_f32_16x16x32_bf16 v[48:51], v[100:103], a[88:91], v[48:51]\n"
"ds_write_addtid_b32 v52 offset:512\n"
"v_add_f32 v153, v153, v149\n"
"v_mfma_f32_16x16x32_bf16 v[48:51], v[104:107], a[92:95], v[48:51]\n"
"ds_write_addtid_b32 v53 offset:5152\n"
"v_add_f32 v154, v154, v150\n"
"v_mfma_f32_16x16x32_bf16 v[48:51], v[108:111], a[96:99], v[48:51]\n"
"ds_write_addtid_b32 v54 offset:9856\n"
"v_add_f32 v155, v155, v151\n"
"v_mfma_f32_16x16x32_bf16 v[48:51], v[112:115], a[100:103], v[48:51]\n"
"ds_write_addtid_b32 v55 offset:14496\n"
"v_cvt_pk_bf16_f32 v46, v152, v153\n"
"v_mfma_f32_16x16x32_bf16 v[48:51], v[116:119], a[104:107], v[48:51]\n"
"v_cvt_pk_bf16_f32 v47, v154, v155\n"
"v_mfma_f32_16x16x32_bf16 v[48:51], v[120:123], a[108:111], v[48:51]\n"
"buffer_store_dwordx4 v[44:47], v13, s[40:43], s30 offen sc1\n"
"v_cvt_pk_bf16_f32 v66, v60, v61\n"
"v_cvt_pk_bf16_f32 v67, v62, v63\n"
"v_mfma_f32_16x16x32_bf16 v[56:59], v[96:99], a[112:115], 0\n"
"v_mfma_f32_16x16x32_bf16 v[56:59], v[100:103], a[116:119], v[56:59]\n"
"ds_write_addtid_b32 v48 offset:768\n"
"v_mfma_f32_16x16x32_bf16 v[56:59], v[104:107], a[120:123], v[56:59]\n"
"ds_write_addtid_b32 v49 offset:5408\n"
"v_mfma_f32_16x16x32_bf16 v[56:59], v[108:111], a[124:127], v[56:59]\n"
"ds_write_addtid_b32 v50 offset:10112\n"
"v_mfma_f32_16x16x32_bf16 v[56:59], v[112:115], a[128:131], v[56:59]\n"
"ds_write_addtid_b32 v51 offset:14752\n"
"v_mfma_f32_16x16x32_bf16 v[56:59], v[116:119], a[132:135], v[56:59]\n"
"ds_bpermute_b32 v44, v21, v64\n"
"v_mfma_f32_16x16x32_bf16 v[56:59], v[120:123], a[136:139], v[56:59]\n"
"v_mfma_f32_16x16x32_bf16 v[48:51], v[96:99], a[140:143], 0\n"
"ds_bpermute_b32 v45, v21, v65\n"
"v_mfma_f32_16x16x32_bf16 v[48:51], v[100:103], a[144:147], v[48:51]\n"
"ds_write_addtid_b32 v56 offset:1024\n"
"v_mfma_f32_16x16x32_bf16 v[48:51], v[104:107], a[148:151], v[48:51]\n"
"ds_write_addtid_b32 v57 offset:5664\n"
"v_mfma_f32_16x16x32_bf16 v[48:51], v[108:111], a[152:155], v[48:51]\n"
"ds_write_addtid_b32 v58 offset:10368\n"
"v_mfma_f32_16x16x32_bf16 v[48:51], v[112:115], a[156:159], v[48:51]\n"
"ds_write_addtid_b32 v59 offset:15008\n"
"v_mfma_f32_16x16x32_bf16 v[48:51], v[116:119], a[160:163], v[48:51]\n"
"ds_bpermute_b32 v46, v21, v66\n"
"v_mfma_f32_16x16x32_bf16 v[48:51], v[120:123], a[164:167], v[48:51]\n"
"ds_bpermute_b32 v47, v21, v67\n"
"v_mfma_f32_16x16x32_bf16 v[68:71], a[168:171], v[96:99], 0\n"
"v_mfma_f32_16x16x32_bf16 v[68:71], a[172:175], v[100:103], v[68:71]\n"
"ds_write_addtid_b32 v48 offset:1280\n"
"v_mfma_f32_16x16x32_bf16 v[68:71], a[176:179], v[104:107], v[68:71]\n"
"ds_write_addtid_b32 v49 offset:5920\n"
"v_mfma_f32_16x16x32_bf16 v[68:71], a[180:183], v[108:111], v[68:71]\n"
"ds_write_addtid_b32 v50 offset:10624\n"
"v_mfma_f32_16x16x32_bf16 v[68:71], a[184:187], v[112:115], v[68:71]\n"
"ds_write_addtid_b32 v51 offset:15264\n"
"v_mfma_f32_16x16x32_bf16 v[68:71], a[188:191], v[116:119], v[68:71]\n"
"s_waitcnt lgkmcnt(4)\n"
"buffer_store_dwordx4 v[44:47], v19, s[76:79], s29 offen sc1\n"
"v_mfma_f32_16x16x32_bf16 v[68:71], a[192:195], v[120:123], v[68:71]\n"
"v_mfma_f32_16x16x32_bf16 v[72:75], a[196:199], v[96:99], 0\n"
"s_and_b32 s30, s90, 0x3ffffff\n"
"v_mfma_f32_16x16x32_bf16 v[72:75], a[200:203], v[100:103], v[72:75]\n"
"s_waitcnt lgkmcnt(0)\n"
"v_mfma_f32_16x16x32_bf16 v[72:75], a[204:207], v[104:107], v[72:75]\n"
"s_barrier\n"
"v_mfma_f32_16x16x32_bf16 v[72:75], a[208:211], v[108:111], v[72:75]\n"
"s_waitcnt vmcnt(4)\n"
"ds_read_b128 v[76:79], v2 offset:0\n"
"v_mfma_f32_16x16x32_bf16 v[72:75], a[212:215], v[112:115], v[72:75]\n"
"s_add_u32 s62, s40, s30\n"
"ds_read_b128 v[80:83], v3 offset:512\n"
"ds_read_b128 v[84:87], v4 offset:1024\n"
"v_mfma_f32_16x16x32_bf16 v[72:75], a[216:219], v[116:119], v[72:75]\n"
"s_addc_u32 s63, s41, 0\n"
"ds_read_b128 v[88:91], v2 offset:256\n"
"ds_read_b128 v[96:99], v3 offset:768\n"
"v_mfma_f32_16x16x32_bf16 v[72:75], a[220:223], v[120:123], v[72:75]\n"
"ds_read_b128 v[100:103], v4 offset:1280\n"
"v_cmp_ne_u64 vcc, s[84:85], v[92:93]\n"
"s_cbranch_vccnz .LJ_4t1k7s\n"
".LJ_4t1k7r:\n"
"global_load_dwordx4 v[46:49], v16, s[62:63] offset:-4096 sc1\n"
"s_waitcnt lgkmcnt(3)\n"
"v_add_f32 v76, v76, v80\n"
"v_add_f32 v77, v77, v81\n"
"v_add_f32 v78, v78, v82\n"
"v_add_f32 v79, v79, v83\n"
"v_add_f32 v76, v76, v84\n"
"v_add_f32 v77, v77, v85\n"
"v_add_f32 v78, v78, v86\n"
"v_add_f32 v79, v79, v87\n"
"v_add_f32 v76, v76, v68\n"
"v_add_f32 v77, v77, v69\n"
"v_add_f32 v78, v78, v70\n"
"global_load_dwordx4 v[50:53], v16, s[62:63] offset:-3072 sc1\n"
"v_add_f32 v79, v79, v71\n"
"s_add_u32 s29, s50, 0x0\n"
"s_and_b32 s30, s29, 0xfbffffff\n"
"v_cvt_pk_bf16_f32 v84, v76, v77\n"
"v_cvt_pk_bf16_f32 v85, v78, v79\n"
"s_waitcnt lgkmcnt(0)\n"
"v_add_f32 v88, v88, v96\n"
"v_add_f32 v89, v89, v97\n"
"v_add_f32 v90, v90, v98\n"
"v_add_f32 v91, v91, v99\n"
"s_waitcnt vmcnt(3)\n"
"v_add_f32 v88, v88, v100\n"
"v_add_f32 v89, v89, v101\n"
"v_add_f32 v90, v90, v102\n"
"v_add_f32 v91, v91, v103\n"
"v_add_f32 v88, v88, v72\n"
"v_add_f32 v89, v89, v73\n"
"v_add_f32 v90, v90, v74\n"
"v_add_f32 v91, v91, v75\n"
"v_cvt_pk_bf16_f32 v86, v88, v89\n"
"v_cvt_pk_bf16_f32 v87, v90, v91\n"
"buffer_store_dwordx4 v[84:87], v13, s[40:43], s30 offen sc1\n"
"buffer_store_dwordx2 v[0:1], v14, s[40:43], s31 offen offset:0 sc1\n"
"global_load_dwordx4 v[54:57], v16, s[62:63] offset:-2048 sc1\n"
"global_load_dwordx4 v[58:61], v16, s[62:63] offset:-1024 sc1\n"
"global_load_dwordx4 v[62:65], v17, s[62:63] offset:0 sc1\n"
"global_load_dwordx4 v[70:73], v16, s[62:63] offset:0 sc1\n"
"global_load_dwordx4 v[74:77], v16, s[62:63] offset:1024 sc1\n"
"global_load_dwordx4 v[78:81], v16, s[62:63] offset:2048 sc1\n"
"global_load_dwordx4 v[82:85], v16, s[62:63] offset:3072 sc1\n"
"s_mov_b32 s91, s90\n"
"s_waitcnt vmcnt(8)\n"
"buffer_store_dwordx2 v[0:1], v14, s[40:43], s29 offen offset:0 sc1\n"
"global_load_dwordx4 v[86:89], v17, s[62:63] offset:1024 sc1\n"
"v_mov_b32 v44, v26\n"
"v_mov_b32 v45, v27\n"
"v_mov_b32 v66, v28\n"
"v_mov_b32 v67, v29\n"
"s_waitcnt vmcnt(6)\n"
"v_mfma_f32_16x16x32_bf16 v[132:135], v[38:41], v[44:47], 0\n"
"s_cmp_ge_i32 s91, s80\n"
"s_cbranch_scc1 .LJ_wt1k7s\n"
".LJ_wt1k7r:\n"
"v_mfma_f32_16x16x32_bf16 v[132:135], v[38:41], v[52:55], v[132:135]\n"
"s_lshr_b32 s29, s91, s81\n"
"v_mfma_f32_16x16x32_bf16 v[132:135], v[38:41], v[60:63], v[132:135]\n"
"s_bitcmp1_b32 s29, 15\n"
"s_cselect_b32 s31, s83, s82\n"
"v_mfma_f32_16x16x32_bf16 v[128:131], v[38:41], v[48:51], 0\n"
"s_add_u32 s29, s29, s31\n"
"v_mfma_f32_16x16x32_bf16 v[128:131], v[38:41], v[56:59], v[128:131]\n"
"v_mfma_f32_16x16x32_bf16 v[128:131], v[38:41], v[64:67], v[128:131]\n"
"v_cvt_pk_bf16_f32 v144, v132, v133\n"
"v_cvt_pk_bf16_f32 v145, v134, v135\n"
"s_nop 5\n"
"v_cvt_pk_bf16_f32 v146, v128, v129\n"
"v_cvt_pk_bf16_f32 v147, v130, v131\n"
"v_mov_b32 v68, v30\n"
"v_mov_b32 v69, v31\n"
"v_mov_b32 v90, v32\n"
"ds_bpermute_b32 v48, v21, v144\n"
"ds_bpermute_b32 v49, v21, v145\n"
"v_mov_b32 v91, v33\n"
"ds_bpermute_b32 v50, v21, v146\n"
"ds_bpermute_b32 v51, v21, v147\n"
"s_add_u32 s90, s50, 0xfff80000\n"
"s_cmp_lt_u32 s90, 0x8000000\n"
"s_cselect_b32 s90, 0x80000000, s90\n"
"s_waitcnt lgkmcnt(0)\n"
"buffer_load_dwordx2 v[92:93], v15, s[40:43], s90 offen offset:0 sc1\n"
"buffer_store_dwordx4 v[48:51], v18, s[76:79], s29 offen sc1\n"
"s_waitcnt vmcnt(2)\n"
"v_mfma_f32_16x16x32_bf16 v[56:59], v[38:41], v[68:71], 0\n"
"v_mfma_f32_16x16x32_bf16 v[56:59], v[38:41], v[76:79], v[56:59]\n"
"v_mfma_f32_16x16x32_bf16 v[56:59], v[38:41], v[84:87], v[56:59]\n"
"v_mfma_f32_16x16x32_bf16 v[60:63], v[38:41], v[72:75], 0\n"
"v_mfma_f32_16x16x32_bf16 v[60:63], v[38:41], v[80:83], v[60:63]\n"
"v_mfma_f32_16x16x32_bf16 v[60:63], v[38:41], v[88:91], v[60:63]\n"
"v_cvt_pk_bf16_f32 v64, v56, v57\n"
"v_cvt_pk_bf16_f32 v65, v58, v59\n"
"s_nop 5\n"
"v_cvt_pk_bf16_f32 v66, v60, v61\n"
"v_cvt_pk_bf16_f32 v67, v62, v63\n"
"ds_bpermute_b32 v44, v21, v64\n"
"ds_bpermute_b32 v45, v21, v65\n"
"ds_bpermute_b32 v46, v21, v66\n"
"ds_bpermute_b32 v47, v21, v67\n"
"s_waitcnt lgkmcnt(0)\n"
"buffer_store_dwordx4 v[44:47], v19, s[76:79], s29 offen sc1\n"
"s_and_b32 s30, s90, 0x3ffffff\n"
"s_waitcnt vmcnt(2)\n"
"s_add_u32 s62, s40, s30\n"
"s_addc_u32 s63, s41, 0\n"
"v_cmp_ne_u64 vcc, s[84:85], v[92:93]\n"
"s_cbranch_vccnz .LJ_4t1k0s\n"
".LJ_4t1k0r:\n"
"global_load_dwordx4 v[46:49], v16, s[62:63] offset:-4096 sc1\n"
"global_load_dwordx4 v[50:53], v16, s[62:63] offset:-3072 sc1\n"
"global_load_dwordx4 v[54:57], v16, s[62:63] offset:-2048 sc1\n"
"global_load_dwordx4 v[58:61], v16, s[62:63] offset:-1024 sc1\n"
"global_load_dwordx4 v[62:65], v17, s[62:63] offset:0 sc1\n"
"global_load_dwordx4 v[70:73], v16, s[62:63] offset:0 sc1\n"
"global_load_dwordx4 v[74:77], v16, s[62:63] offset:1024 sc1\n"
"global_load_dwordx4 v[78:81], v16, s[62:63] offset:2048 sc1\n"
"global_load_dwordx4 v[82:85], v16, s[62:63] offset:3072 sc1\n"
"s_mov_b32 s91, s90\n"
"global_load_dwordx4 v[86:89], v17, s[62:63] offset:1024 sc1\n"
"v_mov_b32 v44, v26\n"
"v_mov_b32 v45, v27\n"
"v_mov_b32 v66, v28\n"
"v_mov_b32 v67, v29\n"
"s_waitcnt vmcnt(5)\n"
"v_mfma_f32_16x16x32_bf16 v[132:135], v[38:41], v[44:47], 0\n"
"s_cmp_ge_i32 s91, s80\n"
"s_cbranch_scc1 .LJ_wt1k0s\n"
".LJ_wt1k0r:\n"
"v_mfma_f32_16x16x32_bf16 v[132:135], v[38:41], v[52:55], v[132:135]\n"
"s_lshr_b32 s29, s91, s81\n"
"v_mfma_f32_16x16x32_bf16 v[132:135], v[38:41], v[60:63], v[132:135]\n"
"s_bitcmp1_b32 s29, 15\n"
"s_cselect_b32 s31, s83, s82\n"
"v_mfma_f32_16x16x32_bf16 v[128:131], v[38:41], v[48:51], 0\n"
"s_add_u32 s29, s29, s31\n"
"v_mfma_f32_16x16x32_bf16 v[128:131], v[38:41], v[56:59], v[128:131]\n"
"v_mfma_f32_16x16x32_bf16 v[128:131], v[38:41], v[64:67], v[128:131]\n"
"v_cvt_pk_bf16_f32 v144, v132, v133\n"
"v_cvt_pk_bf16_f32 v145, v134, v135\n"
"s_nop 5\n"
"v_cvt_pk_bf16_f32 v146, v128, v129\n"
"v_cvt_pk_bf16_f32 v147, v130, v131\n"
"v_mov_b32 v68, v30\n"
"v_mov_b32 v69, v31\n"
"v_mov_b32 v90, v32\n"
"ds_bpermute_b32 v48, v21, v144\n"
"ds_bpermute_b32 v49, v21, v145\n"
"v_mov_b32 v91, v33\n"
"ds_bpermute_b32 v50, v21, v146\n"
"ds_bpermute_b32 v51, v21, v147\n"
"s_waitcnt lgkmcnt(0)\n"
"buffer_store_dwordx4 v[48:51], v18, s[76:79], s29 offen sc1\n"
"s_waitcnt vmcnt(1)\n"
"v_mfma_f32_16x16x32_bf16 v[56:59], v[38:41], v[68:71], 0\n"
"v_mfma_f32_16x16x32_bf16 v[56:59], v[38:41], v[76:79], v[56:59]\n"
"v_mfma_f32_16x16x32_bf16 v[56:59], v[38:41], v[84:87], v[56:59]\n"
"v_mfma_f32_16x16x32_bf16 v[60:63], v[38:41], v[72:75], 0\n"
"v_mfma_f32_16x16x32_bf16 v[60:63], v[38:41], v[80:83], v[60:63]\n"
"v_mfma_f32_16x16x32_bf16 v[60:63], v[38:41], v[88:91], v[60:63]\n"
"v_cvt_pk_bf16_f32 v64, v56, v57\n"
"v_cvt_pk_bf16_f32 v65, v58, v59\n"
"s_nop 5\n"
"v_cvt_pk_bf16_f32 v66, v60, v61\n"
"v_cvt_pk_bf16_f32 v67, v62, v63\n"
"ds_bpermute_b32 v44, v21, v64\n"
"ds_bpermute_b32 v45, v21, v65\n"
"ds_bpermute_b32 v46, v21, v66\n"
"ds_bpermute_b32 v47, v21, v67\n"
"s_waitcnt lgkmcnt(0)\n"
"buffer_store_dwordx4 v[44:47], v19, s[76:79], s29 offen sc1\n"
"s_branch .LJ_end\n"
".LJ_t2:\n"
"v_mfma_f32_16x16x32_bf16 v[56:59], v[100:103], a[116:119], v[56:59]\n"
"ds_write_addtid_b32 v48 offset:768\n"
"v_mfma_f32_16x16x32_bf16 v[56:59], v[104:107], a[120:123], v[56:59]\n"
"ds_write_addtid_b32 v49 offset:5408\n"
"v_mfma_f32_16x16x32_bf16 v[56:59], v[108:111], a[124:127], v[56:59]\n"
"ds_write_addtid_b32 v50 offset:10112\n"
"v_mfma_f32_16x16x32_bf16 v[56:59], v[112:115], a[128:131], v[56:59]\n"
"ds_write_addtid_b32 v51 offset:14752\n"
"v_mfma_f32_16x16x32_bf16 v[56:59], v[116:119], a[132:135], v[56:59]\n"
"ds_bpermute_b32 v44, v21, v64\n"
"v_mfma_f32_16x16x32_bf16 v[56:59], v[120:123], a[136:139], v[56:59]\n"
"v_mfma_f32_16x16x32_bf16 v[48:51], v[96:99], a[140:143], 0\n"
"ds_bpermute_b32 v45, v21, v65\n"
"v_mfma_f32_16x16x32_bf16 v[48:51], v[100:103], a[144:147], v[48:51]\n"
"ds_write_addtid_b32 v56 offset:1024\n"
"v_mfma_f32_16x16x32_bf16 v[48:51], v[104:107], a[148:151], v[48:51]\n"
"ds_write_addtid_b32 v57 offset:5664\n"
"v_mfma_f32_16x16x32_bf16 v[48:51], v[108:111], a[152:155], v[48:51]\n"
"ds_write_addtid_b32 v58 offset:10368\n"
"v_mfma_f32_16x16x32_bf16 v[48:51], v[112:115], a[156:159], v[48:51]\n"
"ds_write_addtid_b32 v59 offset:15008\n"
"v_mfma_f32_16x16x32_bf16 v[48:51], v[116:119], a[160:163], v[48:51]\n"
"ds_bpermute_b32 v46, v21, v66\n"
"v_mfma_f32_16x16x32_bf16 v[48:51], v[120:123], a[164:167], v[48:51]\n"
"ds_bpermute_b32 v47, v21, v67\n"
"v_mfma_f32_16x16x32_bf16 v[68:71], a[168:171], v[96:99], 0\n"
"v_mfma_f32_16x16x32_bf16 v[68:71], a[172:175], v[100:103], v[68:71]\n"
"ds_write_addtid_b32 v48 offset:1280\n"
"v_mfma_f32_16x16x32_bf16 v[68:71], a[176:179], v[104:107], v[68:71]\n"
"ds_write_addtid_b32 v49 offset:5920\n"
"v_mfma_f32_16x16x32_bf16 v[68:71], a[180:183], v[108:111], v[68:71]\n"
"ds_write_addtid_b32 v50 offset:10624\n"
"v_mfma_f32_16x16x32_bf16 v[68:71], a[184:187], v[112:115], v[68:71]\n"
"ds_write_addtid_b32 v51 offset:15264\n"
"v_mfma_f32_16x16x32_bf16 v[68:71], a[188:191], v[116:119], v[68:71]\n"
"s_waitcnt lgkmcnt(4)\n"
"buffer_store_dwordx4 v[44:47], v19, s[76:79], s29 offen sc1\n"
"v_mfma_f32_16x16x32_bf16 v[68:71], a[192:195], v[120:123], v[68:71]\n"
"v_mfma_f32_16x16x32_bf16 v[72:75], a[196:199], v[96:99], 0\n"
"s_and_b32 s30, s90, 0x3ffffff\n"
"v_mfma_f32_16x16x32_bf16 v[72:75], a[200:203], v[100:103], v[72:75]\n"
"s_waitcnt lgkmcnt(0)\n"
"v_mfma_f32_16x16x32_bf16 v[72:75], a[204:207], v[104:107], v[72:75]\n"
"s_barrier\n"
"v_mfma_f32_16x16x32_bf16 v[72:75], a[208:211], v[108:111], v[72:75]\n"
"s_waitcnt vmcnt(5)\n"
"ds_read_b128 v[76:79], v2 offset:0\n"
"v_mfma_f32_16x16x32_bf16 v[72:75], a[212:215], v[112:115], v[72:75]\n"
"s_add_u32 s62, s40, s30\n"
"ds_read_b128 v[80:83], v3 offset:512\n"
"ds_read_b128 v[84:87], v4 offset:1024\n"
"v_mfma_f32_16x16x32_bf16 v[72:75], a[216:219], v[116:119], v[72:75]\n"
"s_addc_u32 s63, s41, 0\n"
"ds_read_b128 v[88:91], v2 offset:256\n"
"ds_read_b128 v[96:99], v3 offset:768\n"
"v_mfma_f32_16x16x32_bf16 v[72:75], a[220:223], v[120:123], v[72:75]\n"
"s_waitcnt vmcnt(10)\n"
"ds_read_b128 v[100:103], v4 offset:1280\n"
"v_mfma_f32_16x16x32_bf16 v[104:107], v[124:127], a[0:3], 0\n"
"v_cmp_ne_u64 vcc, s[84:85], v[92:93]\n"
"v_mfma_f32_16x16x32_bf16 v[104:107], v[128:131], a[4:7], v[104:107]\n"
"s_waitcnt vmcnt(8)\n"
"v_mfma_f32_16x16x32_bf16 v[104:107], v[132:135], a[8:11], v[104:107]\n"
"s_cbranch_vccnz .LJ_4t2k7s\n"
".LJ_4t2k7r:\n"
"v_mfma_f32_16x16x32_bf16 v[104:107], v[136:139], a[12:15], v[104:107]\n"
"s_waitcnt vmcnt(6)\n"
"global_load_dwordx4 v[46:49], v16, s[62:63] offset:-4096 sc1\n"
"v_mfma_f32_16x16x32_bf16 v[104:107], v[140:143], a[16:19], v[104:107]\n"
"s_waitcnt lgkmcnt(3)\n"
"v_add_f32 v76, v76, v80\n"
"v_add_f32 v77, v77, v81\n"
"v_mfma_f32_16x16x32_bf16 v[104:107], a[224:227], a[20:23], v[104:107]\n"
"s_waitcnt vmcnt(4)\n"
"v_add_f32 v78, v78, v82\n"
"v_add_f32 v79, v79, v83\n"
"v_mfma_f32_16x16x32_bf16 v[104:107], a[228:231], a[24:27], v[104:107]\n"
"v_add_f32 v76, v76, v84\n"
"v_mfma_f32_16x16x32_bf16 v[80:83], v[124:127], a[28:31], 0\n"
"v_add_f32 v77, v77, v85\n"
"v_add_f32 v78, v78, v86\n"
"v_mfma_f32_16x16x32_bf16 v[80:83], v[128:131], a[32:35], v[80:83]\n"
"ds_write_addtid_b32 v104 offset:1536\n"
"v_add_f32 v79, v79, v87\n"
"v_mfma_f32_16x16x32_bf16 v[80:83], v[132:135], a[36:39], v[80:83]\n"
"ds_write_addtid_b32 v105 offset:6176\n"
"v_add_f32 v76, v76, v68\n"
"v_mfma_f32_16x16x32_bf16 v[80:83], v[136:139], a[40:43], v[80:83]\n"
"ds_write_addtid_b32 v106 offset:10880\n"
"v_add_f32 v77, v77, v69\n"
"v_mfma_f32_16x16x32_bf16 v[80:83], v[140:143], a[44:47], v[80:83]\n"
"ds_write_addtid_b32 v107 offset:15520\n"
"v_add_f32 v78, v78, v70\n"
"v_mfma_f32_16x16x32_bf16 v[80:83], a[224:227], a[48:51], v[80:83]\n"
"global_load_dwordx4 v[50:53], v16, s[62:63] offset:-3072 sc1\n"
"v_add_f32 v79, v79, v71\n"
"v_mfma_f32_16x16x32_bf16 v[80:83], a[228:231], a[52:55], v[80:83]\n"
"s_add_u32 s29, s50, 0xffe80000\n"
"s_and_b32 s30, s29, 0xfbffffff\n"
"v_cvt_pk_bf16_f32 v84, v76, v77\n"
"v_mfma_f32_16x16x32_bf16 v[68:71], v[124:127], a[56:59], 0\n"
"v_cvt_pk_bf16_f32 v85, v78, v79\n"
"v_mfma_f32_16x16x32_bf16 v[68:71], v[128:131], a[60:63], v[68:71]\n"
"s_waitcnt lgkmcnt(4)\n"
"ds_write_addtid_b32 v80 offset:1792\n"
"v_add_f32 v88, v88, v96\n"
"v_mfma_f32_16x16x32_bf16 v[68:71], v[132:135], a[64:67], v[68:71]\n"
"ds_write_addtid_b32 v81 offset:6432\n"
"v_add_f32 v89, v89, v97\n"
"v_mfma_f32_16x16x32_bf16 v[68:71], v[136:139], a[68:71], v[68:71]\n"
"ds_write_addtid_b32 v82 offset:11136\n"
"v_add_f32 v90, v90, v98\n"
"v_mfma_f32_16x16x32_bf16 v[68:71], v[140:143], a[72:75], v[68:71]\n"
"ds_write_addtid_b32 v83 offset:15776\n"
"v_add_f32 v91, v91, v99\n"
"v_mfma_f32_16x16x32_bf16 v[68:71], a[224:227], a[76:79], v[68:71]\n"
"s_waitcnt vmcnt(3)\n"
"v_add_f32 v88, v88, v100\n"
"v_add_f32 v89, v89, v101\n"
"v_mfma_f32_16x16x32_bf16 v[68:71], a[228:231], a[80:83], v[68:71]\n"
"v_add_f32 v90, v90, v102\n"
"v_add_f32 v91, v91, v103\n"
"v_mfma_f32_16x16x32_bf16 v[76:79], v[124:127], a[84:87], 0\n"
"v_add_f32 v88, v88, v72\n"
"v_add_f32 v89, v89, v73\n"
"v_mfma_f32_16x16x32_bf16 v[76:79], v[128:131], a[88:91], v[76:79]\n"
"ds_write_addtid_b32 v68 offset:2048\n"
"v_add_f32 v90, v90, v74\n"
"v_mfma_f32_16x16x32_bf16 v[76:79], v[132:135], a[92:95], v[76:79]\n"
"ds_write_addtid_b32 v69 offset:6688\n"
"v_add_f32 v91, v91, v75\n"
"v_mfma_f32_16x16x32_bf16 v[76:79], v[136:139], a[96:99], v[76:79]\n"
"ds_write_addtid_b32 v70 offset:11392\n"
"v_cvt_pk_bf16_f32 v86, v88, v89\n"
"v_mfma_f32_16x16x32_bf16 v[76:79], v[140:143], a[100:103], v[76:79]\n"
"ds_write_addtid_b32 v71 offset:16032\n"
"v_cvt_pk_bf16_f32 v87, v90, v91\n"
"v_mfma_f32_16x16x32_bf16 v[76:79], a[224:227], a[104:107], v[76:79]\n"
"buffer_store_dwordx4 v[84:87], v13, s[40:43], s30 offen sc1\n"
"v_mfma_f32_16x16x32_bf16 v[76:79], a[228:231], a[108:111], v[76:79]\n"
"buffer_store_dwordx2 v[0:1], v14, s[40:43], s31 offen offset:0 sc1\n"
"v_mfma_f32_16x16x32_bf16 v[68:71], v[124:127], a[112:115], 0\n"
"v_mfma_f32_16x16x32_bf16 v[68:71], v[128:131], a[116:119], v[68:71]\n"
"ds_write_addtid_b32 v76 offset:2304\n"
"global_load_dwordx4 v[54:57], v16, s[62:63] offset:-2048 sc1\n"
"v_mfma_f32_16x16x32_bf16 v[68:71], v[132:135], a[120:123], v[68:71]\n"
"ds_write_addtid_b32 v77 offset:6944\n"
"v_mfma_f32_16x16x32_bf16 v[68:71], v[136:139], a[124:127], v[68:71]\n"
"ds_write_addtid_b32 v78 offset:11648\n"
"v_mfma_f32_16x16x32_bf16 v[68:71], v[140:143], a[128:131], v[68:71]\n"
"ds_write_addtid_b32 v79 offset:16288\n"
"v_mfma_f32_16x16x32_bf16 v[68:71], a[224:227], a[132:135], v[68:71]\n"
"v_mfma_f32_16x16x32_bf16 v[68:71], a[228:231], a[136:139], v[68:71]\n"
"v_mfma_f32_16x16x32_bf16 v[112:115], v[124:127], a[140:143], 0\n"
"global_load_dwordx4 v[58:61], v16, s[62:63] offset:-1024 sc1\n"
"v_mfma_f32_16x16x32_bf16 v[112:115], v[128:131], a[144:147], v[112:115]\n"
"ds_write_addtid_b32 v68 offset:2560\n"
"v_mfma_f32_16x16x32_bf16 v[112:115], v[132:135], a[148:151], v[112:115]\n"
"ds_write_addtid_b32 v69 offset:7200\n"
"v_mfma_f32_16x16x32_bf16 v[112:115], v[136:139], a[152:155], v[112:115]\n"
"ds_write_addtid_b32 v70 offset:11904\n"
"v_mfma_f32_16x16x32_bf16 v[112:115], v[140:143], a[156:159], v[112:115]\n"
"ds_write_addtid_b32 v71 offset:16544\n"
"v_mfma_f32_16x16x32_bf16 v[112:115], a[224:227], a[160:163], v[112:115]\n"
"global_load_dwordx4 v[62:65], v17, s[62:63] offset:0 sc1\n"
"v_mfma_f32_16x16x32_bf16 v[112:115], a[228:231], a[164:167], v[112:115]\n"
"v_mfma_f32_16x16x32_bf16 v[144:147], a[168:171], v[124:127], 0\n"
"v_mfma_f32_16x16x32_bf16 v[144:147], a[172:175], v[128:131], v[144:147]\n"
"ds_write_addtid_b32 v112 offset:2816\n"
"v_mfma_f32_16x16x32_bf16 v[144:147], a[176:179], v[132:135], v[144:147]\n"
"ds_write_addtid_b32 v113 offset:7456\n"
"v_mfma_f32_16x16x32_bf16 v[144:147], a[180:183], v[136:139], v[144:147]\n"
"ds_write_addtid_b32 v114 offset:12160\n"
"global_load_dwordx4 v[70:73], v16, s[62:63] offset:0 sc1\n"
"v_mfma_f32_16x16x32_bf16 v[144:147], a[184:187], v[140:143], v[144:147]\n"
"ds_write_addtid_b32 v115 offset:16800\n"
"v_mfma_f32_16x16x32_bf16 v[144:147], a[188:191], a[224:227], v[144:147]\n"
"v_mfma_f32_16x16x32_bf16 v[144:147], a[192:195], a[228:231], v[144:147]\n"
"v_mfma_f32_16x16x32_bf16 v[148:151], a[196:199], v[124:127], 0\n"
"v_mfma_f32_16x16x32_bf16 v[148:151], a[200:203], v[128:131], v[148:151]\n"
"s_waitcnt lgkmcnt(0)\n"
"global_load_dwordx4 v[74:77], v16, s[62:63] offset:1024 sc1\n"
"v_mfma_f32_16x16x32_bf16 v[148:151], a[204:207], v[132:135], v[148:151]\n"
"s_barrier\n"
"v_mfma_f32_16x16x32_bf16 v[148:151], a[208:211], v[136:139], v[148:151]\n"
"ds_read_b128 v[124:127], v2 offset:1536\n"
"v_mfma_f32_16x16x32_bf16 v[148:151], a[212:215], v[140:143], v[148:151]\n"
"ds_read_b128 v[128:131], v3 offset:2048\n"
"ds_read_b128 v[132:135], v4 offset:2560\n"
"v_mfma_f32_16x16x32_bf16 v[148:151], a[216:219], a[224:227], v[148:151]\n"
"ds_read_b128 v[136:139], v2 offset:1792\n"
"ds_read_b128 v[140:143], v3 offset:2304\n"
"v_mfma_f32_16x16x32_bf16 v[148:151], a[220:223], a[228:231], v[148:151]\n"
"ds_read_b128 v[152:155], v4 offset:2816\n"
"global_load_dwordx4 v[78:81], v16, s[62:63] offset:2048 sc1\n"
"s_waitcnt lgkmcnt(3)\n"
"v_add_f32 v124, v124, v128\n"
"v_add_f32 v125, v125, v129\n"
"v_add_f32 v126, v126, v130\n"
"v_add_f32 v127, v127, v131\n"
"v_add_f32 v124, v124, v132\n"
"v_add_f32 v125, v125, v133\n"
"v_add_f32 v126, v126, v134\n"
"v_add_f32 v127, v127, v135\n"
"v_add_f32 v124, v124, v144\n"
"v_add_f32 v125, v125, v145\n"
"v_add_f32 v126, v126, v146\n"
"global_load_dwordx4 v[82:85], v16, s[62:63] offset:3072 sc1\n"
"v_add_f32 v127, v127, v147\n"
"s_add_u32 s28, s50, 0x0\n"
"s_and_b32 s31, s28, 0xfbffffff\n"
"v_cvt_pk_bf16_f32 v132, v124, v125\n"
"v_cvt_pk_bf16_f32 v133, v126, v127\n"
"s_waitcnt lgkmcnt(0)\n"
"v_add_f32 v136, v136, v140\n"
"s_mov_b32 s91, s90\n"
"v_add_f32 v137, v137, v141\n"
"v_add_f32 v138, v138, v142\n"
"s_waitcnt vmcnt(8)\n"
"buffer_store_dwordx2 v[0:1], v14, s[40:43], s29 offen offset:0 sc1\n"
"v_add_f32 v139, v139, v143\n"
"v_add_f32 v136, v136, v152\n"
"v_add_f32 v137, v137, v153\n"
"v_add_f32 v138, v138, v154\n"
"v_add_f32 v139, v139, v155\n"
"v_add_f32 v136, v136, v148\n"
"v_add_f32 v137, v137, v149\n"
"v_add_f32 v138, v138, v150\n"
"v_add_f32 v139, v139, v151\n"
"v_cvt_pk_bf16_f32 v134, v136, v137\n"
"v_cvt_pk_bf16_f32 v135, v138, v139\n"
"buffer_store_dwordx4 v[132:135], v13, s[40:43], s31 offen sc1\n"
"global_load_dwordx4 v[86:89], v17, s[62:63] offset:1024 sc1\n"
"v_mov_b32 v44, v26\n"
"v_mov_b32 v45, v27\n"
"v_mov_b32 v66, v28\n"
"v_mov_b32 v67, v29\n"
"s_waitcnt vmcnt(7)\n"
"v_mfma_f32_16x16x32_bf16 v[132:135], v[38:41], v[44:47], 0\n"
"s_cmp_ge_i32 s91, s80\n"
"s_cbranch_scc1 .LJ_wt2k7s\n"
".LJ_wt2k7r:\n"
"v_mfma_f32_16x16x32_bf16 v[132:135], v[38:41], v[52:55], v[132:135]\n"
"s_lshr_b32 s29, s91, s81\n"
"v_mfma_f32_16x16x32_bf16 v[132:135], v[38:41], v[60:63], v[132:135]\n"
"s_bitcmp1_b32 s29, 15\n"
"s_cselect_b32 s31, s83, s82\n"
"v_mfma_f32_16x16x32_bf16 v[128:131], v[38:41], v[48:51], 0\n"
"s_add_u32 s29, s29, s31\n"
"v_mfma_f32_16x16x32_bf16 v[128:131], v[38:41], v[56:59], v[128:131]\n"
"v_mfma_f32_16x16x32_bf16 v[128:131], v[38:41], v[64:67], v[128:131]\n"
"v_cvt_pk_bf16_f32 v144, v132, v133\n"
"v_cvt_pk_bf16_f32 v145, v134, v135\n"
"s_nop 5\n"
"v_cvt_pk_bf16_f32 v146, v128, v129\n"
"v_cvt_pk_bf16_f32 v147, v130, v131\n"
"v_mov_b32 v68, v30\n"
"v_mov_b32 v69, v31\n"
"v_mov_b32 v90, v32\n"
"ds_bpermute_b32 v48, v21, v144\n"
"ds_bpermute_b32 v49, v21, v145\n"
"v_mov_b32 v91, v33\n"
"ds_bpermute_b32 v50, v21, v146\n"
"ds_bpermute_b32 v51, v21, v147\n"
"s_add_u32 s90, s50, 0xffe00000\n"
"s_cmp_lt_u32 s90, 0x8000000\n"
"s_cselect_b32 s90, 0x80000000, s90\n"
"s_waitcnt lgkmcnt(0)\n"
"buffer_load_dwordx2 v[92:93], v15, s[40:43], s90 offen offset:0 sc1\n"
"buffer_store_dwordx4 v[48:51], v18, s[76:79], s29 offen sc1\n"
"s_waitcnt vmcnt(2)\n"
"v_mfma_f32_16x16x32_bf16 v[56:59], v[38:41], v[68:71], 0\n"
"v_mfma_f32_16x16x32_bf16 v[56:59], v[38:41], v[76:79], v[56:59]\n"
"v_mfma_f32_16x16x32_bf16 v[56:59], v[38:41], v[84:87], v[56:59]\n"
"v_mfma_f32_16x16x32_bf16 v[60:63], v[38:41], v[72:75], 0\n"
"v_mfma_f32_16x16x32_bf16 v[60:63], v[38:41], v[80:83], v[60:63]\n"
"v_mfma_f32_16x16x32_bf16 v[60:63], v[38:41], v[88:91], v[60:63]\n"
"v_cvt_pk_bf16_f32 v64, v56, v57\n"
"v_cvt_pk_bf16_f32 v65, v58, v59\n"
"s_waitcnt vmcnt(3)\n"
"buffer_store_dwordx2 v[0:1], v14, s[40:43], s28 offen offset:0 sc1\n"
"s_nop 3\n"
"v_cvt_pk_bf16_f32 v66, v60, v61\n"
"v_cvt_pk_bf16_f32 v67, v62, v63\n"
"ds_bpermute_b32 v44, v21, v64\n"
"ds_bpermute_b32 v45, v21, v65\n"
"ds_bpermute_b32 v46, v21, v66\n"
"ds_bpermute_b32 v47, v21, v67\n"
"s_waitcnt lgkmcnt(0)\n"
"buffer_store_dwordx4 v[44:47], v19, s[76:79], s29 offen sc1\n"
"s_and_b32 s30, s90, 0x3ffffff\n"
"s_waitcnt vmcnt(3)\n"
"s_add_u32 s62, s40, s30\n"
"s_addc_u32 s63, s41, 0\n"
"v_cmp_ne_u64 vcc, s[84:85], v[92:93]\n"
"s_cbranch_vccnz .LJ_4t2k0s\n"
".LJ_4t2k0r:\n"
"global_load_dwordx4 v[46:49], v16, s[62:63] offset:-4096 sc1\n"
"global_load_dwordx4 v[50:53], v16, s[62:63] offset:-3072 sc1\n"
"global_load_dwordx4 v[54:57], v16, s[62:63] offset:-2048 sc1\n"
"global_load_dwordx4 v[58:61], v16, s[62:63] offset:-1024 sc1\n"
"global_load_dwordx4 v[62:65], v17, s[62:63] offset:0 sc1\n"
"global_load_dwordx4 v[70:73], v16, s[62:63] offset:0 sc1\n"
"global_load_dwordx4 v[74:77], v16, s[62:63] offset:1024 sc1\n"
"global_load_dwordx4 v[78:81], v16, s[62:63] offset:2048 sc1\n"
"global_load_dwordx4 v[82:85], v16, s[62:63] offset:3072 sc1\n"
"s_mov_b32 s91, s90\n"
"global_load_dwordx4 v[86:89], v17, s[62:63] offset:1024 sc1\n"
"v_mov_b32 v44, v26\n"
"v_mov_b32 v45, v27\n"
"v_mov_b32 v66, v28\n"
"v_mov_b32 v67, v29\n"
"s_waitcnt vmcnt(5)\n"
"v_mfma_f32_16x16x32_bf16 v[132:135], v[38:41], v[44:47], 0\n"
"s_cmp_ge_i32 s91, s80\n"
"s_cbranch_scc1 .LJ_wt2k0s\n"
".LJ_wt2k0r:\n"
"v_mfma_f32_16x16x32_bf16 v[132:135], v[38:41], v[52:55], v[132:135]\n"
"s_lshr_b32 s29, s91, s81\n"
"v_mfma_f32_16x16x32_bf16 v[132:135], v[38:41], v[60:63], v[132:135]\n"
"s_bitcmp1_b32 s29, 15\n"
"s_cselect_b32 s31, s83, s82\n"
"v_mfma_f32_16x16x32_bf16 v[128:131], v[38:41], v[48:51], 0\n"
"s_add_u32 s29, s29, s31\n"
"v_mfma_f32_16x16x32_bf16 v[128:131], v[38:41], v[56:59], v[128:131]\n"
"v_mfma_f32_16x16x32_bf16 v[128:131], v[38:41], v[64:67], v[128:131]\n"
"v_cvt_pk_bf16_f32 v144, v132, v133\n"
"v_cvt_pk_bf16_f32 v145, v134, v135\n"
"s_nop 5\n"
"v_cvt_pk_bf16_f32 v146, v128, v129\n"
"v_cvt_pk_bf16_f32 v147, v130, v131\n"
"v_mov_b32 v68, v30\n"
"v_mov_b32 v69, v31\n"
"v_mov_b32 v90, v32\n"
"ds_bpermute_b32 v48, v21, v144\n"
"ds_bpermute_b32 v49, v21, v145\n"
"v_mov_b32 v91, v33\n"
"ds_bpermute_b32 v50, v21, v146\n"
"ds_bpermute_b32 v51, v21, v147\n"
"s_waitcnt lgkmcnt(0)\n"
"buffer_store_dwordx4 v[48:51], v18, s[76:79], s29 offen sc1\n"
"s_waitcnt vmcnt(1)\n"
"v_mfma_f32_16x16x32_bf16 v[56:59], v[38:41], v[68:71], 0\n"
"v_mfma_f32_16x16x32_bf16 v[56:59], v[38:41], v[76:79], v[56:59]\n"
"v_mfma_f32_16x16x32_bf16 v[56:59], v[38:41], v[84:87], v[56:59]\n"
"v_mfma_f32_16x16x32_bf16 v[60:63], v[38:41], v[72:75], 0\n"
"v_mfma_f32_16x16x32_bf16 v[60:63], v[38:41], v[80:83], v[60:63]\n"
"v_mfma_f32_16x16x32_bf16 v[60:63], v[38:41], v[88:91], v[60:63]\n"
"v_cvt_pk_bf16_f32 v64, v56, v57\n"
"v_cvt_pk_bf16_f32 v65, v58, v59\n"
"s_nop 5\n"
"v_cvt_pk_bf16_f32 v66, v60, v61\n"
"v_cvt_pk_bf16_f32 v67, v62, v63\n"
"ds_bpermute_b32 v44, v21, v64\n"
"ds_bpermute_b32 v45, v21, v65\n"
"ds_bpermute_b32 v46, v21, v66\n"
"ds_bpermute_b32 v47, v21, v67\n"
"s_waitcnt lgkmcnt(0)\n"
"buffer_store_dwordx4 v[44:47], v19, s[76:79], s29 offen sc1\n"
"s_branch .LJ_end\n"
".LJ_end:\n"
"s_cmp_lt_u32 s50, s53\n"
"s_cbranch_scc1 .LJ_ne\n"
"s_cmp_eq_u64 s[72:73], 0\n"
"s_cbranch_scc1 .LJ_ne\n"
"v_mov_b32 v12, 0\n"
"global_store_dwordx2 v12, v[0:1], s[72:73] sc1\n"
".LJ_ne:\n"
"s_waitcnt vmcnt(0) lgkmcnt(0)\n"
"s_endpgm\n"
      :: "{v2}"((unsigned)(lds0 + ((wid + 3) & 3) * 18592 + trp)), "{v3}"((unsigned)(lds0 + ((wid + 2) & 3) * 18592 + trp)), "{v4}"((unsigned)(lds0 + ((wid + 1) & 3) * 18592 + trp)), "{v5}"((unsigned)(lane * 16 + f0 * 1024 + ((0 + rot) % 7) * 1024 + 134316032)), "{v6}"((unsigned)(lane * 16 + f0 * 1024 + ((1 + rot) % 7) * 1024 + 134316032)), "{v7}"((unsigned)(lane * 16 + f0 * 1024 + ((2 + rot) % 7) * 1024 + 134316032)), "{v8}"((unsigned)(lane * 16 + f0 * 1024 + ((3 + rot) % 7) * 1024 + 134316032)), "{v9}"((unsigned)(lane * 16 + f0 * 1024 + ((4 + rot) % 7) * 1024 + 134316032)), "{v10}"((unsigned)(lane * 16 + f0 * 1024 + ((5 + rot) % 7) * 1024 + 134316032)), "{v11}"((unsigned)(lane * 16 + f0 * 1024 + ((6 + rot) % 7) * 1024 + 134316032)), "{v12}"((unsigned)((f0 + lane % 7) * 8 + kq * 32 + 353024 + J_GL * 1572864)), "{v13}"((unsigned)(lane * 16 + pair * 4096 + kq * 1024 + 212992)), "{v14}"((unsigned)(lane ? DROP : pair * 64 + kq * 8 + 354304)), "{v15}"((unsigned)((v16 * 2 + ((lane >> 2) & 1)) * 64 + (lane & 3) * 8 + 354304)), "{v16}"((unsigned)(lane * 16 + v16 * 8192 + 134434816)), "{v17}"((unsigned)(lane * 16 + v16 * 2048 + 134283264)), "{v18}"((unsigned)(lane * 16 + v16 * 2048 + 0)), "{v19}"((unsigned)(lane * 16 + v16 * 2048 + 1024)), "{v20}"((unsigned)(((lane >> 4) + (lane & 12)) * 4096 + (lane & 3) * 16 + v16 * 128)), "{v21}"((unsigned)(lane * 4)), "{v22}"((unsigned)((lane & 3) * 64 + (lane & 60))), "{v23}"((unsigned)(lg == (li >> 2) ? v16 * 128 + li * 8 + 67457024 : DROP)), "{v24}"((unsigned)(lane ? DROP : v16 * 8 + 67462912)), "{v26}"((unsigned)(bia0[0])), "{v27}"((unsigned)(bia0[1])), "{v28}"((unsigned)(bia0[2])), "{v29}"((unsigned)(bia0[3])), "{v30}"((unsigned)(bia1[0])), "{v31}"((unsigned)(bia1[1])), "{v32}"((unsigned)(bia1[2])), "{v33}"((unsigned)(bia1[3])), "{v34}"((unsigned)(0x3f803f80)), "{v35}"((unsigned)(0x3f803f80)), "{v36}"((unsigned)(0x3f803f80)), "{v37}"((unsigned)(0x3f803f80)), "{v38}"((unsigned)(sel0)), "{v39}"((unsigned)(sel1)), "{v40}"((unsigned)(sel0)), "{v41}"((unsigned)(sel1)), "{v44}"((unsigned)(vT + tsw)), "{v45}"((unsigned)(vT + (tsw ^ 8))), "{v46}"((unsigned)(vD + ((0 + rot) % 7) * 65536)), "{v47}"((unsigned)(vD + ((1 + rot) % 7) * 65536)), "{v48}"((unsigned)(vD + ((2 + rot) % 7) * 65536)), "{v49}"((unsigned)(vD + ((3 + rot) % 7) * 65536)), "{v50}"((unsigned)(vD + ((4 + rot) % 7) * 65536)), "{v51}"((unsigned)(vD + ((5 + rot) % 7) * 65536)), "{v52}"((unsigned)(vD + ((6 + rot) % 7) * 65536)), "{s[40:43]}"(rS), "{s[44:47]}"(rW), "{s[48:49]}"(wsp), "{s50}"((unsigned)(sA)), "{s51}"((unsigned)(sLo)), "{s52}"((unsigned)(sHi)), "{s53}"((unsigned)(sEx)), "{s54}"((unsigned)(sEnt)), "{s55}"((unsigned)(sLd)), "{s[56:57]}"(wsb + (sLo & 0x3ffffff)), "{s59}"((unsigned)(lds0 + wid * 18592)), "{s[62:63]}"(wsb), "{s[64:65]}"(0xAAAAAAAAAAAAAAAAull), "{s[66:67]}"(0xCCCCCCCCCCCCCCCCull), "{s[68:71]}"(rO), "{s[72:73]}"(ep), "{s[76:79]}"(rS), "{s80}"((unsigned)(201326592)), "{s81}"((unsigned)(0)), "{s82}"((unsigned)(0)), "{s83}"((unsigned)(0)), "{s86}"((unsigned)(sW0 + 64 * ((wid + 1) & 3))), "{s87}"((unsigned)(sW0 + 64 * ((wid + 2) & 3))), "{s88}"((unsigned)(sW0 + 64 * ((wid + 3) & 3))), "{s89}"((unsigned)(sW0 + 64 * ((wid + 4) & 3)))
      : "v255", "a255", "s95", "vcc", "scc", "memory");

    }
  }
}

extern "C" void kernel_launch(void* const* d_in, const int* in_sizes, int n_in, void* d_out, int out_size,
                              void* d_ws, size_t ws_size, hipStream_t stream) {
  Args m{};
  m.x = (const u16*)d_in[0]; m.wqkv = (const u16*)d_in[1]; m.wo = (const u16*)d_in[2]; m.w1 = (const u16*)d_in[4];
  m.b1 = (const u16*)d_in[5]; m.w2 = (const u16*)d_in[6]; m.b2 = (const u16*)d_in[7];
  m.out = (u16*)d_out; m.ws = (char*)d_ws;
  (void)hipFuncSetAttribute((const void*)stream_kernel, hipFuncAttributeMaxDynamicSharedMemorySize, LDS_BYTES);
  hipLaunchKernelGGL(stream_kernel, dim3(256), dim3(256), LDS_BYTES, stream, m);
}
```

```cpp
#include <hip/hip_runtime.h>
#include <stdint.h>
typedef unsigned short u16;
typedef unsigned long long u64;
typedef __attribute__((ext_vector_type(4))) unsigned u32x4;
constexpr unsigned F_X = 0;
constexpr unsigned F_ATT = 32768;
constexpr unsigned F_X1 = 65536;
constexpr unsigned F_H = 98304;
constexpr unsigned F_P = 212992;
constexpr unsigned F_ST1 = 344064;
constexpr unsigned F_ST2 = 348160;
constexpr unsigned F_G = 352256;
constexpr unsigned F_FA = 352512;
constexpr unsigned F_FX1 = 352768;
constexpr unsigned F_FH = 353024;
constexpr unsigned F_FX = 354048;
constexpr unsigned F_FP = 354304;
constexpr unsigned RS = 524288;
constexpr unsigned PADF = 256;
constexpr unsigned LOWB = 64;
constexpr unsigned L_TAB = 98304;
constexpr unsigned LDS_BYTES = 163840;
constexpr int Q_GL = 2;
constexpr int Q_EX = 0;
constexpr int Q_TL = 1;
constexpr int O_GL = 1;
constexpr int O_EX = 0;
constexpr int O_TL = 1;
constexpr int F_GL = 1;
constexpr int F_EX = 0;
constexpr int F_TL = 1;
constexpr int G_GL = 1;
constexpr int G_EX = 0;
constexpr int G_TL = 1;
constexpr int H_GL = 1;
constexpr int H_EX = 0;
constexpr int H_TL = 1;
constexpr int J_GL = 1;
constexpr int J_EX = 0;
constexpr int J_TL = 1;
#define MAPV 4
#define FDLY 0
#define PF 42
#define KR 1
#define XP 2
#define OR 1
#define JJ 1
#define DLO 0
#define DLF 450
#define DLG 0
#define SLP 12
#define TW 1
#define DLW 250
#define DLB 300
#define EC 1
#define DLQ 0
#define FR0 0
#define PQ 3
#define QW 6
#define NFL 48
#define PSTX 0
#define CP 1
#define FN 4
#define XF 1
#define P2 0
#define DT 400
#define DU 0
#define DX 1
#define NE 20
#define DLX 300

constexpr unsigned DROP = 0x40000000u, WS0 = 1u << 20;
static_assert(WS0 + (PADF + 256 - LOWB) * RS <= (256u << 20), "workspace");
struct Args { const u16 *x, *wqkv, *wo, *w1, *b1, *w2, *b2; u16* out; char* ws;
};
__device__ __forceinline__ u32x4 desc(const void* p, unsigned n) { const u64 a = (u64)p; return (u32x4){(unsigned)a, (unsigned)(a >> 32) & 0xffffu, n, 0x00020000u}; }
__device__ __forceinline__ float lo16(unsigned v) { return __builtin_bit_cast(float, v << 16); }
__device__ __forceinline__ float hi16(unsigned v) { return __builtin_bit_cast(float, v & 0xffff0000u); }

__host__ __device__ inline void decode(int bid, int& role, int& set, int& idx) {
  const int xg = bid & 7, xj = bid >> 3;
  if (xg < 4) {
    if (xj < 16) { role = 0; set = xj & 1; idx = MAPV == 1 || MAPV == 4 ? 8 * xg + (xj >> 1) : MAPV == 2 ? 4 * (xj >> 1) + xg : 4 * (xj >> 1) + ((xg + 2 * set) & 3); }
    else if (MAPV == 4 && SLP && xj >= 30) { role = 2; set = 0; idx = (int)(0x1915110e0d090501ull >> (16 * xg + 8 * (xj & 1))) & 31; }
    else { role = 2; set = xj < 23 ? 1 : xj < 30 ? 2 : 0; idx = 7 * xg + (xj - 16) % 7; }
  } else if (xj < 21) { role = 3; set = xj / 7; idx = (xg - 4) * 7 + xj % 7; }
  else {
    const int k = xj - 21, no = xg == 4 ? 11 : xg == 5 ? 5 : 4;
    if (k < no) { role = 1; set = xg == 4 ? k >> 3 : xg == 5 ? 1 : 2; idx = xg == 4 ? k & 7 : xg == 5 ? 3 + k : (xg - 6) * 4 + k; }
    else if (SLP) { role = 2; set = 0; idx = (int)((xg == 5 ? 0x181716100f0000ull : xg == 6 ? 0x1b1a0a08070302ull : 0x14131206040c0bull) >> (8 * (k - 4))) & 31; }
    else { role = 2; set = 0; idx = (int)((xg == 5 ? 0x19181711100200ull : xg == 6 ? 0x1b1a0b0a090403ull : 0x14131206050d0cull) >> (8 * (k - 4))) & 31; }
  }
}

__global__ __launch_bounds__(256, 1) void stream_kernel(Args a) {
  extern __shared__ __attribute__((aligned(16))) char smem[];
  const int tid = threadIdx.x, lane = tid & 63, wid = __builtin_amdgcn_readfirstlane(tid >> 6), li = lane & 15, lg = lane >> 4;
  const unsigned lds0 = (unsigned)(size_t)smem;
  const u64 tz = __builtin_amdgcn_s_memrealtime();
  const int bp = (blockIdx.x + 2 * (XP - 1)) & 7, bid = (blockIdx.x & ~7) | ((bp & 1) << 2) | (bp >> 1);
  {
    u64 pc; unsigned len;
    asm volatile("s_getpc_b64 %0\n.Lc0:\n s_mov_b32 %1, .LO_end-.Lc0\n s_max_u32 %1, %1, .LH_end-.Lc0\n s_max_u32 %1, %1, .LJ_end-.Lc0\n s_max_u32 %1, %1, .LG_end-.Lc0" : "=s"(pc), "=s"(len)::"scc");
    asm volatile("global_load_dword v252, %0, %1" ::"v"(min(((blockIdx.x >> 3) * 256 + tid) * 16, len)), "s"(pc) : "v252");
  }
  const int pi = (li >> 2) + 4 * (li & 3), xj = bid >> 3, xo = (bid & 7) - 4;
  auto two = [&]() __attribute__((always_inline)) {
    const int hf = xo > 1;
#pragma unroll
    for (int i = 0; i < 191 / (84 - NE) + 1; ++i) {
      const int r0 = min(((i * (84 - NE) + xj * 4 + wid - NE) * 64 + lane) >> (4 - hf), 767);
      asm volatile("global_load_dword v252, %0, %1" ::"v"(((r0 & 3) * 256 + 64 + (r0 >> 2)) * 2048 + (hf ? (lane & 7) + (xo & 1) * 8 : xo ? max(li, 6) : li) * 128), "s"(a.wo) : "v252", "memory");
    }
  };
  auto tw2 = [&]() __attribute__((always_inline)) {
#pragma unroll
    for (int i = 0; i < 3; ++i)
      if (i * 21 + xj < 56) asm volatile("global_load_dword v252, %0, %1" ::"v"((xo * 896 + wid * 224 + (i * 21 + xj) * 4 + lg) * 2048 + li * 128), "s"(a.w2) : "v252", "memory");
  };
  auto conv = [&](int pr, bool early) __attribute__((always_inline)) {
    const __amdgpu_buffer_rsrc_t rb = __builtin_amdgcn_make_buffer_rsrc(a.ws + WS0 - LOWB * RS, 0, (PADF + 256) * RS, 0x00020000);
    const u32x4* src = (const u32x4*)(a.x + ((pr >> 1) * 32 + 2 * pi + (pr & 1)) * 1024 + wid * 256 + lg * 8);
    const u64 ev = __hip_atomic_load((const u64*)a.ws, __ATOMIC_RELAXED, __HIP_MEMORY_SCOPE_AGENT);
    u32x4 v[8];
#pragma unroll
    for (int k = 0; k < 8; ++k) v[k] = src[k * 4];
    u32x4 pf = {};
    if (early && (EC & 2)) asm volatile("global_load_dword v252, %0, %1 sc1" ::"v"(WS0 + (PADF - LOWB + pr) * RS + (lane & 1) * F_G + wid * 8192 * !(lane & 1)), "s"(a.ws) : "v252");
    if (!early && pr >= NE && pr < (MAPV == 4 ? 84 : 104) && (PF & 50) == 2) {
      const int hf = MAPV == 4 ? xo > 1 : xo, g = xj * 4 + wid - NE, n = MAPV == 4 ? (hf ? 767 / (84 - NE) + 1 : 1535 / (84 - NE) + 1) : hf ? 767 / (112 - NE) + 1 : 1535 / (80 - NE) + 1;
      for (int i = 0; i < n; ++i) {
        const int u = g * n + i, r0 = hf ? u : u >> 1, row = r0 < 768 ? (r0 / 192) * 256 + 64 + r0 % 192 : 0;
        asm volatile("global_load_dwordx4 %0, %1, off" : "+v"(pf) : "v"((const char*)a.wo + row * 2048 + (hf ? xo == (MAPV == 4 ? 3 : 2) : u & 1) * 1024 + lane * 16));
      }
    }
    float s = 0.f, q = 0.f;
#pragma unroll
    for (int k = 0; k < 8; ++k) __builtin_amdgcn_raw_buffer_store_b128(v[k], rb, lane * 16 + k * 1024, (PADF + pr) * RS + F_X + wid * 8192, 16);
    if (!early && pr >= NE && pr < 84 && (PF & 16)) two();
    __builtin_amdgcn_sched_barrier(0);
#pragma unroll
    for (int k = 0; k < 8; ++k) {
#pragma unroll
      for (int d = 0; d < 4; ++d) { const float l = lo16(v[k][d]), h = hi16(v[k][d]); s += l + h; q = fmaf(l, l, fmaf(h, h, q)); }
    }
    s += __shfl_xor(s, 16); q += __shfl_xor(q, 16); s += __shfl_xor(s, 32); q += __shfl_xor(q, 32);
    float2* t = (float2*)smem;
    if (lg == 0) t[wid * 16 + li] = make_float2(s, q);
    if (!early && pr >= NE && pr < 84 && (PF & 16)) {
      asm volatile("s_waitcnt vmcnt(%0)" ::"n"(191 / (84 - NE) + 1) : "memory");
    } else asm volatile("s_waitcnt vmcnt(0)" : "+v"(pf)::"memory");
    __syncthreads();
    if (tid < 16) {
      const float2 t0 = t[li], t1 = t[16 + li], t2 = t[32 + li], t3 = t[48 + li];
      const float nm = ((t0.x + t1.x) + (t2.x + t3.x)) * -0x1p-10f;
      const float var = fmaxf(fmaf(-nm, nm, fmaf((t0.y + t1.y) + (t2.y + t3.y), 0x1p-10f, 1e-5f)), 1e-5f);
      const u32x4 g = {__builtin_bit_cast(unsigned, __builtin_amdgcn_rsqf(var)), __builtin_bit_cast(unsigned, nm), ((unsigned)ev + 2u) | 1u, (unsigned)(ev >> 32) + 0x9E3779B9u};
      __builtin_amdgcn_raw_buffer_store_b128(g, rb, li * 16, (PADF + pr) * RS + F_G, 16);
    }
    __syncthreads();
  };
  if (!(bid & 4)) {
    if (DX == 1 && bid >= 128) asm volatile("global_load_dword v252, %0, %1" ::"v"((((bid >> 3) - 16) * 64 + (tid >> 2)) * 128), "s"(a.x) : "v252");
    const unsigned o = (wid * 256 + (lane >> 2)) * 6144 + (bid & 7) * 512 + (lane & 3) * 128;
#pragma unroll
    for (int i = 0; i < PQ - 1; ++i) {
      const int j = (bid >> 3) + (PQ == 3 ? i * 16 : i * 16 - 16);
      if ((PQ == 3 && !i) || bid >= 128) asm volatile("global_load_dword v252, %0, %1" ::"v"(o + j / 3 * 16 * 6144 + j % 3 * 2048), "s"(a.wqkv) : "v252");
    }
  }
  int xe = xo;
  asm volatile("" : "+s"(xe));
  if (EC && xe >= 0 && xj < NE / 4) conv(xj * 4 + xo, true);
  if (FN == 4) __builtin_amdgcn_s_barrier();
  int role, set, idx;
  decode(bid, role, set, idx);
  const u64 wsp = (u64)a.ws, wsb = wsp + WS0 - LOWB * RS;
  const u32x4 rS = desc(a.ws + WS0 - LOWB * RS, (PADF + 256) * RS);
  if (role == 2 && xo < 0 && (PF & 1)) {
    const int g = (xj - 16) * 4 + wid;
    const char* p = (const char*)a.wqkv + (((g >> 2) * 2 + 1) * 32 + (g & 3) * 8 + (lane >> 5)) * 6144 + (bid & 7) * 512 + (lane & 31) * 16;
    u32x4 t[12];
    unsigned fl = 0;
    if (PF & 4) fl = __hip_atomic_load((const unsigned*)(a.ws + WS0 + (size_t)(PADF - LOWB + g * 4 + (bid & 7)) * RS + F_ST2 + (lane < 56 ? lane : 0) * 128), __ATOMIC_RELAXED, __HIP_MEMORY_SCOPE_AGENT);
#pragma unroll
    for (int i = 0; i < 12; ++i) t[i] = *(const u32x4*)(p + (i & 3) * 12288 + (i >> 2) * 2048);
#pragma unroll
    for (int i = 0; i < 12; ++i) asm volatile("" ::"v"(t[i]));
    asm volatile("" ::"v"(fl));
  }
  if (role == 2 && xo < 0)
    asm volatile("global_load_dword v252, %0, %1 sc1" ::"v"(WS0 + (PADF - LOWB + (xj * 4 + wid - 64) * 4 + (bid & 7)) * RS + F_ST2 + (lane < 16 ? lane : lane < NFL ? lane + 16 : 0) * 128), "s"(a.ws) : "v252");
  if (role == 2 && xo < 0 && (SLP & 1)) {
    const unsigned c0 = 14 * (xo + 4), c = c0 + ((3 - c0 - (unsigned)((u64)a.w1 >> 7)) & 7) + (lane & 2) * 4;
    asm volatile("global_load_dword v252, %0, %1" ::"v"(((xj * 4 + wid - 64) * 16 + (lane >> 2)) * 7168 + (c < c0 + 14 ? c : c0) * 128 + (lane & 1) * 64), "s"(a.w1) : "v252");
  }
  if (role == 3 && xj * 4 + wid < 56 && (SLP & 2))
    asm volatile("global_load_dword v252, %0, %1" ::"v"((xo * 896 + (xj * 4 + wid) * 16 + (lane >> 2)) * 2048 + (((3 - (unsigned)((u64)a.w2 >> 7)) & 7) + (lane & 2) * 4) * 128 + (lane & 1) * 64), "s"(a.w2) : "v252");
  if (role == 0 || FN == 2 || (FN == 3 && role == 2)) asm volatile("s_waitcnt vmcnt(%0)" ::"n"(QW));
  if (role == 0 && DLQ) while (__builtin_amdgcn_s_memrealtime() - tz < DLQ) __builtin_amdgcn_s_sleep(1);
  if (FN == 3) __builtin_amdgcn_s_barrier();
  if (xo < 0 && (PF & 8) && !FN) {
    const int g = xj * 4 + wid;
    const char* p = (const char*)a.wqkv + (g * 8 + (lane >> 5)) * 6144 + (bid & 7) * 512 + (lane & 31) * 16;
    u32x4 t[12];
#pragma unroll
    for (int i = 0; i < 12; ++i) t[i] = *(const u32x4*)(p + (i & 3) * 12288 + (i >> 2) * 2048);
    unsigned fl = 0;
    if (role == 2) fl = __hip_atomic_load((const unsigned*)(a.ws + WS0 + (size_t)(PADF - LOWB + (g - 64) * 4 + (bid & 7)) * RS + F_ST2 + (lane < 56 ? lane : 0) * 128), __ATOMIC_RELAXED, __HIP_MEMORY_SCOPE_AGENT);
#pragma unroll
    for (int i = 0; i < 12; ++i) asm volatile("" ::"v"(t[i]));
    asm volatile("" ::"v"(fl));
  }
  if (role == 2 && xo < 0) for (int i = 0; i < FDLY; ++i) __builtin_amdgcn_s_sleep(16);
  const int p2 = xj < 4 ? 84 + xj * 4 + xo : P2 ? (xj % 7 < 4 ? 84 + (set * 4 + xj % 7) * 4 + xo : 128) : xj % 7 > 3 ? 84 + (set * 3 + xj % 7) * 4 + xo : 128;
  if (role == 3 && xj >= NE / 4 && (PF & 32)) {
    while (__builtin_amdgcn_s_memrealtime() - tz < DLW) __builtin_amdgcn_s_sleep(2);
    two();
  }
  for (int pr = role < 3 ? 128 : EC && xj < NE / 4 ? p2 : xj * 4 + xo; pr < 128; pr = pr < 84 ? p2 : 128) {
    if (pr >= NE) while (__builtin_amdgcn_s_memrealtime() - tz < DLX) __builtin_amdgcn_s_sleep(4);
    if (pr >= 84 && (SLP & 32)) {
      while (__builtin_amdgcn_s_memrealtime() - tz < DLG) __builtin_amdgcn_s_sleep(4);
      tw2();
    }
    conv(pr, false);
  }
  const unsigned sLo = (PADF + set) * RS, sLd = lds0 + wid * 40960, vT = sLd + (8 * lg + (li >> 2)) * 64 + (li & 3) * 16;
  const unsigned trp = (li & 3) * 4608 + (li & 1) * 32 + (li & 2) * 64 + (li >> 2) * 64 + lg * 16;
  if (role == 0) {
    const unsigned sA = sLo - 2 * RS, sHi = sLo + 127 * 2 * RS, sEx = sHi + Q_EX * 2 * RS, sEnt = -wid & 3, tsw = 0, rot = KR * !PQ * (set + 2 * (idx & 1));
    const unsigned sW0 = wid * 256 * 6144 + idx * 64, vD = (lane >> 2) * 6144 + (lane & 3) * 16;
    const u32x4 rW = desc(a.wqkv, 1024 * 6144);
    auto ok = [&](int r, int prev) { const int k = lg + 4 * r; return prev ? k >= pi + 9 : (k <= pi && k >= pi - 7); };
    auto qmask = [&](int r, int prev) { return ok(r, prev) ? 0u : 0xff800000u; };
    auto pmask = [&](int d) { return (ok(2 * (d & 1), d >> 1) ? 0xffffu : 0u) | (ok(2 * (d & 1) + 1, d >> 1) ? 0xffff0000u : 0u); };
    asm volatile(
"s_nop 4\n"
"v_mov_b32 v186, 0\n"
"global_load_dwordx2 v[0:1], v186, s[48:49] sc1\n"
"v_mov_b32 v188, 0x3f803f80\n"
"v_mov_b32 v189, 0x3f803f80\n"
"v_mov_b32 v190, 0x3f803f80\n"
"v_mov_b32 v191, 0x3f803f80\n"
"v_xor_b32 v229, 64, v55\n"
"v_xor_b32 v230, 128, v55\n"
"v_xor_b32 v231, 192, v55\n"
"s_add_u32 m0, s55, 0\n"
"s_add_u32 s60, s68, 0\n"
"buffer_load_dwordx4 v57, s[44:47], s60 offen lds\n"
"s_add_u32 m0, s55, 1024\n"
"s_add_u32 s60, s68, 98304\n"
"buffer_load_dwordx4 v57, s[44:47], s60 offen lds\n"
"s_add_u32 m0, s55, 2048\n"
"s_add_u32 s60, s69, 0\n"
"buffer_load_dwordx4 v57, s[44:47], s60 offen lds\n"
"s_add_u32 m0, s55, 3072\n"
"s_add_u32 s60, s69, 98304\n"
"buffer_load_dwordx4 v57, s[44:47], s60 offen lds\n"
"s_add_u32 m0, s55, 4096\n"
"s_add_u32 s60, s70, 0\n"
"buffer_load_dwordx4 v57, s[44:47], s60 offen lds\n"
"s_add_u32 m0, s55, 5120\n"
"s_add_u32 s60, s70, 98304\n"
"buffer_load_dwordx4 v57, s[44:47], s60 offen lds\n"
"s_add_u32 m0, s55, 6144\n"
"s_add_u32 s60, s68, 196608\n"
"buffer_load_dwordx4 v57, s[44:47], s60 offen lds\n"
"s_add_u32 m0, s55, 7168\n"
"s_add_u32 s60, s68, 294912\n"
"buffer_load_dwordx4 v57, s[44:47], s60 offen lds\n"
"s_add_u32 m0, s55, 8192\n"
"s_add_u32 s60, s69, 196608\n"
"buffer_load_dwordx4 v57, s[44:47], s60 offen lds\n"
"s_add_u32 m0, s55, 9216\n"
"s_add_u32 s60, s69, 294912\n"
"buffer_load_dwordx4 v57, s[44:47], s60 offen lds\n"
"s_add_u32 m0, s55, 10240\n"
"s_add_u32 s60, s70, 196608\n"
"buffer_load_dwordx4 v57, s[44:47], s60 offen lds\n"
"s_add_u32 m0, s55, 11264\n"
"s_add_u32 s60, s70, 294912\n"
"buffer_load_dwordx4 v57, s[44:47], s60 offen lds\n"
"s_add_u32 m0, s55, 12288\n"
"s_add_u32 s60, s68, 393216\n"
"buffer_load_dwordx4 v57, s[44:47], s60 offen lds\n"
"s_add_u32 m0, s55, 13312\n"
"s_add_u32 s60, s68, 491520\n"
"buffer_load_dwordx4 v57, s[44:47], s60 offen lds\n"
"s_add_u32 m0, s55, 14336\n"
"s_add_u32 s60, s69, 393216\n"
"buffer_load_dwordx4 v57, s[44:47], s60 offen lds\n"
"s_add_u32 m0, s55, 15360\n"
"s_add_u32 s60, s69, 491520\n"
"buffer_load_dwordx4 v57, s[44:47], s60 offen lds\n"
"s_add_u32 m0, s55, 16384\n"
"s_add_u32 s60, s70, 393216\n"
"buffer_load_dwordx4 v57, s[44:47], s60 offen lds\n"
"s_add_u32 m0, s55, 17408\n"
"s_add_u32 s60, s70, 491520\n"
"buffer_load_dwordx4 v57, s[44:47], s60 offen lds\n"
"s_add_u32 m0, s55, 18432\n"
"s_add_u32 s60, s68, 589824\n"
"buffer_load_dwordx4 v57, s[44:47], s60 offen lds\n"
"s_add_u32 m0, s55, 19456\n"
"s_add_u32 s60, s68, 688128\n"
"buffer_load_dwordx4 v57, s[44:47], s60 offen lds\n"
"s_add_u32 m0, s55, 20480\n"
"s_add_u32 s60, s69, 589824\n"
"buffer_load_dwordx4 v57, s[44:47], s60 offen lds\n"
"s_add_u32 m0, s55, 21504\n"
"s_add_u32 s60, s69, 688128\n"
"buffer_load_dwordx4 v57, s[44:47], s60 offen lds\n"
"s_add_u32 m0, s55, 22528\n"
"s_add_u32 s60, s70, 589824\n"
"buffer_load_dwordx4 v57, s[44:47], s60 offen lds\n"
"s_add_u32 m0, s55, 23552\n"
"s_add_u32 s60, s70, 688128\n"
"buffer_load_dwordx4 v57, s[44:47], s60 offen lds\n"
"s_add_u32 m0, s55, 24576\n"
"s_add_u32 s60, s68, 786432\n"
"buffer_load_dwordx4 v57, s[44:47], s60 offen lds\n"
"s_add_u32 m0, s55, 25600\n"
"s_add_u32 s60, s68, 884736\n"
"buffer_load_dwordx4 v57, s[44:47], s60 offen lds\n"
"s_add_u32 m0, s55, 26624\n"
"s_add_u32 s60, s69, 786432\n"
"buffer_load_dwordx4 v57, s[44:47], s60 offen lds\n"
"s_add_u32 m0, s55, 27648\n"
"s_add_u32 s60, s69, 884736\n"
"buffer_load_dwordx4 v57, s[44:47], s60 offen lds\n"
"s_add_u32 m0, s55, 28672\n"
"s_add_u32 s60, s70, 786432\n"
"buffer_load_dwordx4 v57, s[44:47], s60 offen lds\n"
"s_add_u32 m0, s55, 29696\n"
"s_add_u32 s60, s70, 884736\n"
"buffer_load_dwordx4 v57, s[44:47], s60 offen lds\n"
"s_add_u32 m0, s55, 30720\n"
"s_add_u32 s60, s68, 983040\n"
"buffer_load_dwordx4 v57, s[44:47], s60 offen lds\n"
"s_add_u32 m0, s55, 31744\n"
"s_add_u32 s60, s68, 1081344\n"
"buffer_load_dwordx4 v57, s[44:47], s60 offen lds\n"
"s_waitcnt vmcnt(30)\n"
"ds_read_b64_tr_b16 a[0:1], v48 offset:0\n"
"ds_read_b64_tr_b16 a[2:3], v48 offset:256\n"
"ds_read_b64_tr_b16 a[32:33], v49 offset:0\n"
"ds_read_b64_tr_b16 a[34:35], v49 offset:256\n"
"v_add_u32 v0, 2, v0\n"
"v_add_u32 v1, 0x9e3779b9, v1\n"
"v_or_b32 v0, 1, v0\n"
"v_readfirstlane_b32 s49, v1\n"
"v_readfirstlane_b32 s48, v0\n"
"s_waitcnt vmcnt(28)\n"
"ds_read_b64_tr_b16 a[64:65], v48 offset:2048\n"
"ds_read_b64_tr_b16 a[66:67], v48 offset:2304\n"
"ds_read_b64_tr_b16 a[96:97], v49 offset:2048\n"
"ds_read_b64_tr_b16 a[98:99], v49 offset:2304\n"
"s_waitcnt lgkmcnt(4)\n"
"s_add_u32 m0, s55, 0\n"
"s_add_u32 s60, s69, 983040\n"
"buffer_load_dwordx4 v57, s[44:47], s60 offen lds\n"
"s_add_u32 m0, s55, 1024\n"
"s_add_u32 s60, s69, 1081344\n"
"buffer_load_dwordx4 v57, s[44:47], s60 offen lds\n"
"v_mfma_f32_16x16x32_bf16 v[192:195], a[0:3], v[188:191], 0\n"
"v_mfma_f32_16x16x32_bf16 v[196:199], a[32:35], v[188:191], 0\n"
"s_waitcnt vmcnt(28)\n"
"ds_read_b64_tr_b16 a[128:129], v48 offset:4096\n"
"ds_read_b64_tr_b16 a[130:131], v48 offset:4352\n"
"ds_read_b64_tr_b16 a[160:161], v49 offset:4096\n"
"ds_read_b64_tr_b16 a[162:163], v49 offset:4352\n"
"s_waitcnt lgkmcnt(4)\n"
"s_add_u32 m0, s55, 2048\n"
"s_add_u32 s60, s70, 983040\n"
"buffer_load_dwordx4 v57, s[44:47], s60 offen lds\n"
"s_add_u32 m0, s55, 3072\n"
"s_add_u32 s60, s70, 1081344\n"
"buffer_load_dwordx4 v57, s[44:47], s60 offen lds\n"
"v_mfma_f32_16x16x32_bf16 v[200:203], a[64:67], v[188:191], 0\n"
"v_mfma_f32_16x16x32_bf16 v[204:207], a[96:99], v[188:191], 0\n"
"s_waitcnt vmcnt(28)\n"
"ds_read_b64_tr_b16 a[4:5], v48 offset:6144\n"
"ds_read_b64_tr_b16 a[6:7], v48 offset:6400\n"
"ds_read_b64_tr_b16 a[36:37], v49 offset:6144\n"
"ds_read_b64_tr_b16 a[38:39], v49 offset:6400\n"
"s_waitcnt lgkmcnt(4)\n"
"s_add_u32 m0, s55, 4096\n"
"s_add_u32 s60, s68, 1179648\n"
"buffer_load_dwordx4 v57, s[44:47], s60 offen lds\n"
"s_add_u32 m0, s55, 5120\n"
"s_add_u32 s60, s68, 1277952\n"
"buffer_load_dwordx4 v57, s[44:47], s60 offen lds\n"
"v_mfma_f32_16x16x32_bf16 v[208:211], v[188:191], a[128:131], 0\n"
"v_mfma_f32_16x16x32_bf16 v[212:215], v[188:191], a[160:163], 0\n"
"s_waitcnt vmcnt(28)\n"
"ds_read_b64_tr_b16 a[68:69], v48 offset:8192\n"
"ds_read_b64_tr_b16 a[70:71], v48 offset:8448\n"
"ds_read_b64_tr_b16 a[100:101], v49 offset:8192\n"
"ds_read_b64_tr_b16 a[102:103], v49 offset:8448\n"
"s_waitcnt lgkmcnt(4)\n"
"s_add_u32 m0, s55, 6144\n"
"s_add_u32 s60, s69, 1179648\n"
"buffer_load_dwordx4 v57, s[44:47], s60 offen lds\n"
"s_add_u32 m0, s55, 7168\n"
"s_add_u32 s60, s69, 1277952\n"
"buffer_load_dwordx4 v57, s[44:47], s60 offen lds\n"
"v_mfma_f32_16x16x32_bf16 v[192:195], a[4:7], v[188:191], v[192:195]\n"
"v_mfma_f32_16x16x32_bf16 v[196:199], a[36:39], v[188:191], v[196:199]\n"
"s_waitcnt vmcnt(28)\n"
"ds_read_b64_tr_b16 a[132:133], v48 offset:10240\n"
"ds_read_b64_tr_b16 a[134:135], v48 offset:10496\n"
"ds_read_b64_tr_b16 a[164:165], v49 offset:10240\n"
"ds_read_b64_tr_b16 a[166:167], v49 offset:10496\n"
"s_waitcnt lgkmcnt(4)\n"
"s_add_u32 m0, s55, 8192\n"
"s_add_u32 s60, s70, 1179648\n"
"buffer_load_dwordx4 v57, s[44:47], s60 offen lds\n"
"s_add_u32 m0, s55, 9216\n"
"s_add_u32 s60, s70, 1277952\n"
"buffer_load_dwordx4 v57, s[44:47], s60 offen lds\n"
"v_mfma_f32_16x16x32_bf16 v[200:203], a[68:71], v[188:191], v[200:203]\n"
"v_mfma_f32_16x16x32_bf16 v[204:207], a[100:103], v[188:191], v[204:207]\n"
"s_waitcnt vmcnt(28)\n"
"ds_read_b64_tr_b16 a[8:9], v48 offset:12288\n"
"ds_read_b64_tr_b16 a[10:11], v48 offset:12544\n"
"ds_read_b64_tr_b16 a[40:41], v49 offset:12288\n"
"ds_read_b64_tr_b16 a[42:43], v49 offset:12544\n"
"s_waitcnt lgkmcnt(4)\n"
"s_add_u32 m0, s55, 10240\n"
"s_add_u32 s60, s68, 1376256\n"
"buffer_load_dwordx4 v57, s[44:47], s60 offen lds\n"
"s_add_u32 m0, s55, 11264\n"
"s_add_u32 s60, s68, 1474560\n"
"buffer_load_dwordx4 v57, s[44:47], s60 offen lds\n"
"v_mfma_f32_16x16x32_bf16 v[208:211], v[188:191], a[132:135], v[208:211]\n"
"v_mfma_f32_16x16x32_bf16 v[212:215], v[188:191], a[164:167], v[212:215]\n"
"s_waitcnt vmcnt(28)\n"
"ds_read_b64_tr_b16 a[72:73], v48 offset:14336\n"
"ds_read_b64_tr_b16 a[74:75], v48 offset:14592\n"
"ds_read_b64_tr_b16 a[104:105], v49 offset:14336\n"
"ds_read_b64_tr_b16 a[106:107], v49 offset:14592\n"
"s_waitcnt lgkmcnt(4)\n"
"s_add_u32 m0, s55, 12288\n"
"s_add_u32 s60, s69, 1376256\n"
"buffer_load_dwordx4 v57, s[44:47], s60 offen lds\n"
"s_add_u32 m0, s55, 13312\n"
"s_add_u32 s60, s69, 1474560\n"
"buffer_load_dwordx4 v57, s[44:47], s60 offen lds\n"
"v_mfma_f32_16x16x32_bf16 v[192:195], a[8:11], v[188:191], v[192:195]\n"
"v_mfma_f32_16x16x32_bf16 v[196:199], a[40:43], v[188:191], v[196:199]\n"
"s_waitcnt vmcnt(28)\n"
"ds_read_b64_tr_b16 a[136:137], v48 offset:16384\n"
"ds_read_b64_tr_b16 a[138:139], v48 offset:16640\n"
"ds_read_b64_tr_b16 a[168:169], v49 offset:16384\n"
"ds_read_b64_tr_b16 a[170:171], v49 offset:16640\n"
"s_waitcnt lgkmcnt(4)\n"
"s_add_u32 m0, s55, 14336\n"
"s_add_u32 s60, s70, 1376256\n"
"buffer_load_dwordx4 v57, s[44:47], s60 offen lds\n"
"s_add_u32 m0, s55, 15360\n"
"s_add_u32 s60, s70, 1474560\n"
"buffer_load_dwordx4 v57, s[44:47], s60 offen lds\n"
"v_mfma_f32_16x16x32_bf16 v[200:203], a[72:75], v[188:191], v[200:203]\n"
"v_mfma_f32_16x16x32_bf16 v[204:207], a[104:107], v[188:191], v[204:207]\n"
"s_waitcnt vmcnt(28)\n"
"ds_read_b64_tr_b16 a[12:13], v48 offset:18432\n"
"ds_read_b64_tr_b16 a[14:15], v48 offset:18688\n"
"ds_read_b64_tr_b16 a[44:45], v49 offset:18432\n"
"ds_read_b64_tr_b16 a[46:47], v49 offset:18688\n"
"s_waitcnt lgkmcnt(4)\n"
"s_add_u32 m0, s55, 16384\n"
"s_mov_b32 s60, 0\n"
"v_xor_b32 v187, 0, v54\n"
"buffer_load_dwordx4 v187, s[64:67], s60 offen lds\n"
"s_add_u32 m0, s55, 17408\n"
"s_mov_b32 s60, 32768\n"
"v_xor_b32 v187, 32, v54\n"
"buffer_load_dwordx4 v187, s[64:67], s60 offen lds\n"
"v_mfma_f32_16x16x32_bf16 v[208:211], v[188:191], a[136:139], v[208:211]\n"
"v_mfma_f32_16x16x32_bf16 v[212:215], v[188:191], a[168:171], v[212:215]\n"
"s_waitcnt vmcnt(28)\n"
"ds_read_b64_tr_b16 a[76:77], v48 offset:20480\n"
"ds_read_b64_tr_b16 a[78:79], v48 offset:20736\n"
"ds_read_b64_tr_b16 a[108:109], v49 offset:20480\n"
"ds_read_b64_tr_b16 a[110:111], v49 offset:20736\n"
"s_waitcnt lgkmcnt(4)\n"
"s_add_u32 m0, s55, 18432\n"
"s_mov_b32 s60, 4096\n"
"v_xor_b32 v187, 64, v54\n"
"buffer_load_dwordx4 v187, s[64:67], s60 offen lds\n"
"s_add_u32 m0, s55, 19456\n"
"s_mov_b32 s60, 36864\n"
"v_xor_b32 v187, 96, v54\n"
"buffer_load_dwordx4 v187, s[64:67], s60 offen lds\n"
"v_mfma_f32_16x16x32_bf16 v[192:195], a[12:15], v[188:191], v[192:195]\n"
"v_mfma_f32_16x16x32_bf16 v[196:199], a[44:47], v[188:191], v[196:199]\n"
"s_waitcnt vmcnt(28)\n"
"ds_read_b64_tr_b16 a[140:141], v48 offset:22528\n"
"ds_read_b64_tr_b16 a[142:143], v48 offset:22784\n"
"ds_read_b64_tr_b16 a[172:173], v49 offset:22528\n"
"ds_read_b64_tr_b16 a[174:175], v49 offset:22784\n"
"s_waitcnt lgkmcnt(4)\n"
"s_add_u32 m0, s55, 20480\n"
"s_mov_b32 s60, 8192\n"
"v_xor_b32 v187, 128, v54\n"
"buffer_load_dwordx4 v187, s[64:67], s60 offen lds\n"
"s_add_u32 m0, s55, 21504\n"
"s_mov_b32 s60, 40960\n"
"v_xor_b32 v187, 160, v54\n"
"buffer_load_dwordx4 v187, s[64:67], s60 offen lds\n"
"v_mfma_f32_16x16x32_bf16 v[200:203], a[76:79], v[188:191], v[200:203]\n"
"v_mfma_f32_16x16x32_bf16 v[204:207], a[108:111], v[188:191], v[204:207]\n"
"s_waitcnt vmcnt(28)\n"
"ds_read_b64_tr_b16 a[16:17], v48 offset:24576\n"
"ds_read_b64_tr_b16 a[18:19], v48 offset:24832\n"
"ds_read_b64_tr_b16 a[48:49], v49 offset:24576\n"
"ds_read_b64_tr_b16 a[50:51], v49 offset:24832\n"
"s_waitcnt lgkmcnt(4)\n"
"s_add_u32 m0, s55, 22528\n"
"s_mov_b32 s60, 12288\n"
"v_xor_b32 v187, 192, v54\n"
"buffer_load_dwordx4 v187, s[64:67], s60 offen lds\n"
"s_add_u32 m0, s55, 23552\n"
"s_mov_b32 s60, 45056\n"
"v_xor_b32 v187, 224, v54\n"
"buffer_load_dwordx4 v187, s[64:67], s60 offen lds\n"
"v_mfma_f32_16x16x32_bf16 v[208:211], v[188:191], a[140:143], v[208:211]\n"
"v_mfma_f32_16x16x32_bf16 v[212:215], v[188:191], a[172:175], v[212:215]\n"
"s_waitcnt vmcnt(28)\n"
"ds_read_b64_tr_b16 a[80:81], v48 offset:26624\n"
"ds_read_b64_tr_b16 a[82:83], v48 offset:26880\n"
"ds_read_b64_tr_b16 a[112:113], v49 offset:26624\n"
"ds_read_b64_tr_b16 a[114:115], v49 offset:26880\n"
"s_waitcnt lgkmcnt(4)\n"
"s_add_u32 m0, s55, 24576\n"
"s_mov_b32 s60, 65536\n"
"v_xor_b32 v187, 0, v54\n"
"buffer_load_dwordx4 v187, s[64:67], s60 offen lds\n"
"s_add_u32 m0, s55, 25600\n"
"s_mov_b32 s60, 98304\n"
"v_xor_b32 v187, 32, v54\n"
"buffer_load_dwordx4 v187, s[64:67], s60 offen lds\n"
"v_mfma_f32_16x16x32_bf16 v[192:195], a[16:19], v[188:191], v[192:195]\n"
"v_mfma_f32_16x16x32_bf16 v[196:199], a[48:51], v[188:191], v[196:199]\n"
"s_waitcnt vmcnt(28)\n"
"ds_read_b64_tr_b16 a[144:145], v48 offset:28672\n"
"ds_read_b64_tr_b16 a[146:147], v48 offset:28928\n"
"ds_read_b64_tr_b16 a[176:177], v49 offset:28672\n"
"ds_read_b64_tr_b16 a[178:179], v49 offset:28928\n"
"s_waitcnt lgkmcnt(4)\n"
"s_add_u32 m0, s55, 26624\n"
"s_mov_b32 s60, 69632\n"
"v_xor_b32 v187, 64, v54\n"
"buffer_load_dwordx4 v187, s[64:67], s60 offen lds\n"
"s_add_u32 m0, s55, 27648\n"
"s_mov_b32 s60, 102400\n"
"v_xor_b32 v187, 96, v54\n"
"buffer_load_dwordx4 v187, s[64:67], s60 offen lds\n"
"v_mfma_f32_16x16x32_bf16 v[200:203], a[80:83], v[188:191], v[200:203]\n"
"v_mfma_f32_16x16x32_bf16 v[204:207], a[112:115], v[188:191], v[204:207]\n"
"s_waitcnt vmcnt(28)\n"
"ds_read_b64_tr_b16 a[20:21], v48 offset:30720\n"
"ds_read_b64_tr_b16 a[22:23], v48 offset:30976\n"
"ds_read_b64_tr_b16 a[52:53], v49 offset:30720\n"
"ds_read_b64_tr_b16 a[54:55], v49 offset:30976\n"
"s_waitcnt lgkmcnt(4)\n"
"s_add_u32 m0, s55, 28672\n"
"s_mov_b32 s60, 73728\n"
"v_xor_b32 v187, 128, v54\n"
"buffer_load_dwordx4 v187, s[64:67], s60 offen lds\n"
"s_add_u32 m0, s55, 29696\n"
"s_mov_b32 s60, 106496\n"
"v_xor_b32 v187, 160, v54\n"
"buffer_load_dwordx4 v187, s[64:67], s60 offen lds\n"
"v_mfma_f32_16x16x32_bf16 v[208:211], v[188:191], a[144:147], v[208:211]\n"
"v_mfma_f32_16x16x32_bf16 v[212:215], v[188:191], a[176:179], v[212:215]\n"
"s_waitcnt vmcnt(28)\n"
"ds_read_b64_tr_b16 a[84:85], v48 offset:0\n"
"ds_read_b64_tr_b16 a[86:87], v48 offset:256\n"
"ds_read_b64_tr_b16 a[116:117], v49 offset:0\n"
"ds_read_b64_tr_b16 a[118:119], v49 offset:256\n"
"s_waitcnt lgkmcnt(4)\n"
"s_add_u32 m0, s55, 30720\n"
"s_mov_b32 s60, 77824\n"
"v_xor_b32 v187, 192, v54\n"
"buffer_load_dwordx4 v187, s[64:67], s60 offen lds\n"
"s_add_u32 m0, s55, 31744\n"
"s_mov_b32 s60, 110592\n"
"v_xor_b32 v187, 224, v54\n"
"buffer_load_dwordx4 v187, s[64:67], s60 offen lds\n"
"v_mfma_f32_16x16x32_bf16 v[192:195], a[20:23], v[188:191], v[192:195]\n"
"v_mfma_f32_16x16x32_bf16 v[196:199], a[52:55], v[188:191], v[196:199]\n"
"buffer_load_dwordx4 v[232:235], v50, s[40:43], s50 offen sc1\n"
"buffer_load_dwordx4 v[236:239], v51, s[40:43], s50 offen sc1\n"
"buffer_load_dwordx4 v[240:243], v52, s[40:43], s50 offen sc1\n"
"buffer_load_dwordx4 v[244:247], v53, s[40:43], s50 offen sc1\n"
"s_waitcnt vmcnt(32)\n"
"ds_read_b64_tr_b16 a[148:149], v48 offset:2048\n"
"ds_read_b64_tr_b16 a[150:151], v48 offset:2304\n"
"ds_read_b64_tr_b16 a[180:181], v49 offset:2048\n"
"ds_read_b64_tr_b16 a[182:183], v49 offset:2304\n"
"s_waitcnt lgkmcnt(4)\n"
"v_mfma_f32_16x16x32_bf16 v[200:203], a[84:87], v[188:191], v[200:203]\n"
"v_mfma_f32_16x16x32_bf16 v[204:207], a[116:119], v[188:191], v[204:207]\n"
"s_waitcnt vmcnt(30)\n"
"ds_read_b64_tr_b16 a[24:25], v48 offset:4096\n"
"ds_read_b64_tr_b16 a[26:27], v48 offset:4352\n"
"ds_read_b64_tr_b16 a[56:57], v49 offset:4096\n"
"ds_read_b64_tr_b16 a[58:59], v49 offset:4352\n"
"s_waitcnt lgkmcnt(4)\n"
"v_mfma_f32_16x16x32_bf16 v[208:211], v[188:191], a[148:151], v[208:211]\n"
"v_mfma_f32_16x16x32_bf16 v[212:215], v[188:191], a[180:183], v[212:215]\n"
"s_waitcnt vmcnt(28)\n"
"ds_read_b64_tr_b16 a[88:89], v48 offset:6144\n"
"ds_read_b64_tr_b16 a[90:91], v48 offset:6400\n"
"ds_read_b64_tr_b16 a[120:121], v49 offset:6144\n"
"ds_read_b64_tr_b16 a[122:123], v49 offset:6400\n"
"s_waitcnt lgkmcnt(4)\n"
"v_mfma_f32_16x16x32_bf16 v[192:195], a[24:27], v[188:191], v[192:195]\n"
"v_mfma_f32_16x16x32_bf16 v[196:199], a[56:59], v[188:191], v[196:199]\n"
"s_waitcnt vmcnt(12)\n"
"s_cmp_lg_u32 s54, 0\n"
"s_cbranch_scc1 .LQ_v385_0\n"
"ds_read_b128 v[64:67], v55 offset:16384\n"
"ds_read_b128 v[68:71], v229 offset:16384\n"
"ds_read_b128 v[72:75], v230 offset:16384\n"
"ds_read_b128 v[76:79], v231 offset:16384\n"
"ds_read_b128 v[80:83], v55 offset:16640\n"
"ds_read_b128 v[84:87], v229 offset:16640\n"
"ds_read_b128 v[88:91], v230 offset:16640\n"
"ds_read_b128 v[92:95], v231 offset:16640\n"
"s_branch .LQ_v385_3\n"
".LQ_v385_0:\n"
"s_cmp_lg_u32 s54, 1\n"
"s_cbranch_scc1 .LQ_v385_1\n"
"ds_read_b128 v[96:99], v55 offset:16384\n"
"ds_read_b128 v[100:103], v229 offset:16384\n"
"ds_read_b128 v[104:107], v230 offset:16384\n"
"ds_read_b128 v[108:111], v231 offset:16384\n"
"ds_read_b128 v[112:115], v55 offset:16640\n"
"ds_read_b128 v[116:119], v229 offset:16640\n"
"ds_read_b128 v[120:123], v230 offset:16640\n"
"ds_read_b128 v[124:127], v231 offset:16640\n"
"s_branch .LQ_v385_3\n"
".LQ_v385_1:\n"
"s_cmp_lg_u32 s54, 2\n"
"s_cbranch_scc1 .LQ_v385_2\n"
"ds_read_b128 a[192:195], v55 offset:16384\n"
"ds_read_b128 a[196:199], v229 offset:16384\n"
"ds_read_b128 a[200:203], v230 offset:16384\n"
"ds_read_b128 a[204:207], v231 offset:16384\n"
"ds_read_b128 a[208:211], v55 offset:16640\n"
"ds_read_b128 a[212:215], v229 offset:16640\n"
"ds_read_b128 a[216:219], v230 offset:16640\n"
"ds_read_b128 a[220:223], v231 offset:16640\n"
"s_branch .LQ_v385_3\n"
".LQ_v385_2:\n"
"ds_read_b128 a[224:227], v55 offset:16384\n"
"ds_read_b128 a[228:231], v229 offset:16384\n"
"ds_read_b128 a[232:235], v230 offset:16384\n"
"ds_read_b128 a[236:239], v231 offset:16384\n"
"ds_read_b128 a[240:243], v55 offset:16640\n"
"ds_read_b128 a[244:247], v229 offset:16640\n"
"ds_read_b128 a[248:251], v230 offset:16640\n"
"ds_read_b128 a[252:255], v231 offset:16640\n"
".LQ_v385_3:\n"
"s_waitcnt vmcnt(26)\n"
"ds_read_b64_tr_b16 a[152:153], v48 offset:8192\n"
"ds_read_b64_tr_b16 a[154:155], v48 offset:8448\n"
"ds_read_b64_tr_b16 a[184:185], v49 offset:8192\n"
"ds_read_b64_tr_b16 a[186:187], v49 offset:8448\n"
"s_waitcnt lgkmcnt(4)\n"
"v_mfma_f32_16x16x32_bf16 v[200:203], a[88:91], v[188:191], v[200:203]\n"
"v_mfma_f32_16x16x32_bf16 v[204:207], a[120:123], v[188:191], v[204:207]\n"
"s_waitcnt vmcnt(24)\n"
"ds_read_b64_tr_b16 a[28:29], v48 offset:10240\n"
"ds_read_b64_tr_b16 a[30:31], v48 offset:10496\n"
"ds_read_b64_tr_b16 a[60:61], v49 offset:10240\n"
"ds_read_b64_tr_b16 a[62:63], v49 offset:10496\n"
"s_waitcnt lgkmcnt(4)\n"
"v_mfma_f32_16x16x32_bf16 v[208:211], v[188:191], a[152:155], v[208:211]\n"
"v_mfma_f32_16x16x32_bf16 v[212:215], v[188:191], a[184:187], v[212:215]\n"
"s_waitcnt vmcnt(22)\n"
"ds_read_b64_tr_b16 a[92:93], v48 offset:12288\n"
"ds_read_b64_tr_b16 a[94:95], v48 offset:12544\n"
"ds_read_b64_tr_b16 a[124:125], v49 offset:12288\n"
"ds_read_b64_tr_b16 a[126:127], v49 offset:12544\n"
"s_waitcnt lgkmcnt(4)\n"
"v_mfma_f32_16x16x32_bf16 v[192:195], a[28:31], v[188:191], v[192:195]\n"
"v_mfma_f32_16x16x32_bf16 v[196:199], a[60:63], v[188:191], v[196:199]\n"
"s_waitcnt vmcnt(4)\n"
"s_cmp_lg_u32 s54, 0\n"
"s_cbranch_scc1 .LQ_v411_0\n"
"ds_read_b128 v[96:99], v55 offset:24576\n"
"ds_read_b128 v[100:103], v229 offset:24576\n"
"ds_read_b128 v[104:107], v230 offset:24576\n"
"ds_read_b128 v[108:111], v231 offset:24576\n"
"ds_read_b128 v[112:115], v55 offset:24832\n"
"ds_read_b128 v[116:119], v229 offset:24832\n"
"ds_read_b128 v[120:123], v230 offset:24832\n"
"ds_read_b128 v[124:127], v231 offset:24832\n"
"s_branch .LQ_v411_3\n"
".LQ_v411_0:\n"
"s_cmp_lg_u32 s54, 1\n"
"s_cbranch_scc1 .LQ_v411_1\n"
"ds_read_b128 a[192:195], v55 offset:24576\n"
"ds_read_b128 a[196:199], v229 offset:24576\n"
"ds_read_b128 a[200:203], v230 offset:24576\n"
"ds_read_b128 a[204:207], v231 offset:24576\n"
"ds_read_b128 a[208:211], v55 offset:24832\n"
"ds_read_b128 a[212:215], v229 offset:24832\n"
"ds_read_b128 a[216:219], v230 offset:24832\n"
"ds_read_b128 a[220:223], v231 offset:24832\n"
"s_branch .LQ_v411_3\n"
".LQ_v411_1:\n"
"s_cmp_lg_u32 s54, 2\n"
"s_cbranch_scc1 .LQ_v411_2\n"
"ds_read_b128 a[224:227], v55 offset:24576\n"
"ds_read_b128 a[228:231], v229 offset:24576\n"
"ds_read_b128 a[232:235], v230 offset:24576\n"
"ds_read_b128 a[236:239], v231 offset:24576\n"
"ds_read_b128 a[240:243], v55 offset:24832\n"
"ds_read_b128 a[244:247], v229 offset:24832\n"
"ds_read_b128 a[248:251], v230 offset:24832\n"
"ds_read_b128 a[252:255], v231 offset:24832\n"
"s_branch .LQ_v411_3\n"
".LQ_v411_2:\n"
"ds_read_b128 v[64:67], v55 offset:24576\n"
"ds_read_b128 v[68:71], v229 offset:24576\n"
"ds_read_b128 v[72:75], v230 offset:24576\n"
"ds_read_b128 v[76:79], v231 offset:24576\n"
"ds_read_b128 v[80:83], v55 offset:24832\n"
"ds_read_b128 v[84:87], v229 offset:24832\n"
"ds_read_b128 v[88:91], v230 offset:24832\n"
"ds_read_b128 v[92:95], v231 offset:24832\n"
".LQ_v411_3:\n"
"s_waitcnt vmcnt(20)\n"
"ds_read_b64_tr_b16 a[156:157], v48 offset:14336\n"
"ds_read_b64_tr_b16 a[158:159], v48 offset:14592\n"
"ds_read_b64_tr_b16 a[188:189], v49 offset:14336\n"
"ds_read_b64_tr_b16 a[190:191], v49 offset:14592\n"
"s_waitcnt lgkmcnt(4)\n"
"v_mfma_f32_16x16x32_bf16 v[200:203], a[92:95], v[188:191], v[200:203]\n"
"v_mfma_f32_16x16x32_bf16 v[204:207], a[124:127], v[188:191], v[204:207]\n"
"s_waitcnt lgkmcnt(0)\n"
"v_mfma_f32_16x16x32_bf16 v[208:211], v[188:191], a[156:159], v[208:211]\n"
"v_mfma_f32_16x16x32_bf16 v[212:215], v[188:191], a[188:191], v[212:215]\n"
"s_mov_b32 m0, s59\n"
"ds_write_b128 v58, v[192:195] offset:0\n"
"ds_write_b128 v58, v[196:199] offset:1024\n"
"ds_write_b128 v58, v[200:203] offset:2048\n"
"ds_write_b128 v58, v[204:207] offset:3072\n"
"s_nop 2\n"
"v_mov_b32 v209, v212\n"
"ds_write_b64 v58, v[208:209] offset:4096\n"
"s_waitcnt lgkmcnt(0)\n"
"s_barrier\n"
"ds_read_b128 v[128:131], v59 offset:0\n"
"ds_read_b128 v[132:135], v60 offset:0\n"
"ds_read_b128 v[136:139], v61 offset:0\n"
"ds_read_b128 v[140:143], v59 offset:1024\n"
"ds_read_b128 v[144:147], v60 offset:1024\n"
"ds_read_b128 v[148:151], v61 offset:1024\n"
"ds_read_b128 v[152:155], v59 offset:2048\n"
"ds_read_b128 v[156:159], v60 offset:2048\n"
"ds_read_b128 v[160:163], v61 offset:2048\n"
"ds_read_b128 v[164:167], v59 offset:3072\n"
"ds_read_b128 v[168:171], v60 offset:3072\n"
"ds_read_b128 v[172:175], v61 offset:3072\n"
"ds_read_b64 v[176:177], v59 offset:4096\n"
"ds_read_b64 v[180:181], v60 offset:4096\n"
"ds_read_b64 v[184:185], v61 offset:4096\n"
"s_waitcnt lgkmcnt(12)\n"
"v_add_f32 v128, v128, v136\n"
"v_add_f32 v129, v129, v137\n"
"v_add_f32 v130, v130, v138\n"
"v_add_f32 v131, v131, v139\n"
"v_add_f32 v132, v132, v192\n"
"v_add_f32 v133, v133, v193\n"
"v_add_f32 v134, v134, v194\n"
"v_add_f32 v135, v135, v195\n"
"v_add_f32 v28, v128, v132\n"
"v_add_f32 v29, v129, v133\n"
"v_add_f32 v30, v130, v134\n"
"v_add_f32 v31, v131, v135\n"
"s_waitcnt lgkmcnt(9)\n"
"v_add_f32 v140, v140, v148\n"
"v_add_f32 v141, v141, v149\n"
"v_add_f32 v142, v142, v150\n"
"v_add_f32 v143, v143, v151\n"
"v_add_f32 v144, v144, v196\n"
"v_add_f32 v145, v145, v197\n"
"v_add_f32 v146, v146, v198\n"
"v_add_f32 v147, v147, v199\n"
"v_add_f32 v32, v140, v144\n"
"v_add_f32 v33, v141, v145\n"
"v_add_f32 v34, v142, v146\n"
"v_add_f32 v35, v143, v147\n"
"s_waitcnt lgkmcnt(6)\n"
"v_add_f32 v152, v152, v160\n"
"v_add_f32 v153, v153, v161\n"
"v_add_f32 v154, v154, v162\n"
"v_add_f32 v155, v155, v163\n"
"v_add_f32 v156, v156, v200\n"
"v_add_f32 v157, v157, v201\n"
"v_add_f32 v158, v158, v202\n"
"v_add_f32 v159, v159, v203\n"
"v_add_f32 v36, v152, v156\n"
"v_add_f32 v37, v153, v157\n"
"v_add_f32 v38, v154, v158\n"
"v_add_f32 v39, v155, v159\n"
"s_waitcnt lgkmcnt(3)\n"
"v_add_f32 v164, v164, v172\n"
"v_add_f32 v165, v165, v173\n"
"v_add_f32 v166, v166, v174\n"
"v_add_f32 v167, v167, v175\n"
"v_add_f32 v168, v168, v204\n"
"v_add_f32 v169, v169, v205\n"
"v_add_f32 v170, v170, v206\n"
"v_add_f32 v171, v171, v207\n"
"v_add_f32 v40, v164, v168\n"
"v_add_f32 v41, v165, v169\n"
"v_add_f32 v42, v166, v170\n"
"v_add_f32 v43, v167, v171\n"
"s_waitcnt lgkmcnt(0)\n"
"v_add_f32 v176, v176, v184\n"
"v_add_f32 v177, v177, v185\n"
"v_add_f32 v180, v180, v208\n"
"v_add_f32 v181, v181, v209\n"
"v_add_f32 v44, v176, v180\n"
"v_add_f32 v45, v177, v181\n"
"s_mov_b32 s94, 0x80000000\n"
"s_mov_b32 s95, 0x80000000\n"
"v_mov_b32 v144, 0\n"
"v_mov_b32 v145, 0\n"
"v_mov_b32 v146, 0\n"
"v_mov_b32 v147, 0\n"
"v_mov_b32 v152, 0\n"
"v_mov_b32 v153, 0\n"
"v_mov_b32 v154, 0\n"
"v_mov_b32 v155, 0\n"
"v_mov_b32 v160, 0\n"
"v_mov_b32 v161, 0\n"
"v_mov_b32 v162, 0\n"
"v_mov_b32 v163, 0\n"
"v_mov_b32 v168, 0\n"
"v_mov_b32 v169, 0\n"
"v_mov_b32 v170, 0\n"
"v_mov_b32 v171, 0\n"
"s_mov_b32 s61, 200000\n"
"s_waitcnt vmcnt(0)\n"
".LQ_pc:\n"
"v_cmp_ne_u64 vcc, s[48:49], v[234:235]\n"
"v_cmp_ne_u64 s[28:29], s[48:49], v[238:239]\n"
"s_or_b64 vcc, vcc, s[28:29]\n"
"s_cbranch_scc1 .LQ_pp\n"
"s_add_u32 s56, s56, 0x200000\n"
"s_addc_u32 s57, s57, 0\n"
"s_add_u32 s50, s50, 0x200000\n"
"v_cmp_ne_u64 vcc, s[48:49], v[242:243]\n"
"s_cmp_lg_u32 s54, 0\n"
"s_cbranch_scc1 .LQ_v535_0\n"
"v_mov_b32 v144, v232\n"
"v_mov_b32 v145, v233\n"
"v_mov_b32 v152, v236\n"
"v_mov_b32 v153, v237\n"
"v_mov_b32 v160, v240\n"
"v_mov_b32 v161, v241\n"
"v_mov_b32 v162, v242\n"
"v_mov_b32 v163, v243\n"
"v_mov_b32 v168, v244\n"
"v_mov_b32 v169, v245\n"
"v_mov_b32 v170, v246\n"
"v_mov_b32 v171, v247\n"
"s_cbranch_vccnz .LQ_3s\n"
"s_branch .LQ_3r\n"
"s_branch .LQ_v535_3\n"
".LQ_v535_0:\n"
"s_cmp_lg_u32 s54, 1\n"
"s_cbranch_scc1 .LQ_v535_1\n"
"v_mov_b32 v152, v232\n"
"v_mov_b32 v153, v233\n"
"v_mov_b32 v160, v236\n"
"v_mov_b32 v161, v237\n"
"v_mov_b32 v168, v240\n"
"v_mov_b32 v169, v241\n"
"v_mov_b32 v170, v242\n"
"v_mov_b32 v171, v243\n"
"v_mov_b32 v144, v244\n"
"v_mov_b32 v145, v245\n"
"v_mov_b32 v146, v246\n"
"v_mov_b32 v147, v247\n"
"s_cbranch_vccnz .LQ_4s\n"
"s_branch .LQ_4r\n"
"s_branch .LQ_v535_3\n"
".LQ_v535_1:\n"
"s_cmp_lg_u32 s54, 2\n"
"s_cbranch_scc1 .LQ_v535_2\n"
"v_mov_b32 v160, v232\n"
"v_mov_b32 v161, v233\n"
"v_mov_b32 v168, v236\n"
"v_mov_b32 v169, v237\n"
"v_mov_b32 v144, v240\n"
"v_mov_b32 v145, v241\n"
"v_mov_b32 v146, v242\n"
"v_mov_b32 v147, v243\n"
"v_mov_b32 v152, v244\n"
"v_mov_b32 v153, v245\n"
"v_mov_b32 v154, v246\n"
"v_mov_b32 v155, v247\n"
"s_cbranch_vccnz .LQ_1s\n"
"s_branch .LQ_1r\n"
"s_branch .LQ_v535_3\n"
".LQ_v535_2:\n"
"v_mov_b32 v168, v232\n"
"v_mov_b32 v169, v233\n"
"v_mov_b32 v144, v236\n"
"v_mov_b32 v145, v237\n"
"v_mov_b32 v152, v240\n"
"v_mov_b32 v153, v241\n"
"v_mov_b32 v154, v242\n"
"v_mov_b32 v155, v243\n"
"v_mov_b32 v160, v244\n"
"v_mov_b32 v161, v245\n"
"v_mov_b32 v162, v246\n"
"v_mov_b32 v163, v247\n"
"v_mfma_f32_16x16x32_bf16 v[108:111], a[224:227], a[64:67], 0\n"
"s_cbranch_vccnz .LQ_2s\n"
"s_branch .LQ_2r\n"
".LQ_v535_3:\n"
".LQ_pp:\n"
"s_sleep 1\n"
"buffer_load_dwordx4 v[232:235], v50, s[40:43], s50 offen sc1\n"
"buffer_load_dwordx4 v[236:239], v51, s[40:43], s50 offen sc1\n"
"buffer_load_dwordx4 v[240:243], v52, s[40:43], s50 offen sc1\n"
"buffer_load_dwordx4 v[244:247], v53, s[40:43], s50 offen sc1\n"
"s_sub_u32 s61, s61, 1\n"
"s_waitcnt vmcnt(0)\n"
"s_cbranch_scc0 .LQ_pc\n"
"s_branch .LQ_end\n"
".p2align 6\n"
".LQ_top:\n"
"v_mfma_f32_16x16x32_bf16 v[128:131], a[128:131], v[64:67], 0\n"
"s_add_u32 s50, s50, 0x100000\n"
"global_load_dwordx4 a[192:195], v5, s[56:57] offset:0 sc1\n"
"v_mfma_f32_16x16x32_bf16 v[128:131], a[132:135], v[68:71], v[128:131]\n"
"ds_write_addtid_b32 v148 offset:4352\n"
"v_mfma_f32_16x16x32_bf16 v[128:131], a[136:139], v[72:75], v[128:131]\n"
"ds_write_addtid_b32 v149 offset:8992\n"
"v_mfma_f32_16x16x32_bf16 v[128:131], a[140:143], v[76:79], v[128:131]\n"
"ds_write_addtid_b32 v150 offset:13696\n"
"v_mfma_f32_16x16x32_bf16 v[128:131], a[144:147], v[80:83], v[128:131]\n"
"ds_write_addtid_b32 v151 offset:18336\n"
"v_mov_b32 v254, v144\n"
"v_mfma_f32_16x16x32_bf16 v[128:131], a[148:151], v[84:87], v[128:131]\n"
"global_load_dwordx4 a[196:199], v5, s[56:57] offset:1024 sc1\n"
"v_mul_f32 v135, v145, v39\n"
"v_mfma_f32_16x16x32_bf16 v[128:131], a[152:155], v[88:91], v[128:131]\n"
"v_mul_f32 v133, v145, v37\n"
"v_mul_f32 v134, v145, v38\n"
"v_mfma_f32_16x16x32_bf16 v[128:131], a[156:159], v[92:95], v[128:131]\n"
"v_mul_f32 v139, v145, v43\n"
"v_mul_f32 v132, v145, v36\n"
"v_mfma_f32_16x16x32_bf16 v[140:143], a[160:163], v[64:67], 0\n"
"v_mul_f32 v137, v145, v41\n"
"v_mul_f32 v138, v145, v42\n"
"v_mfma_f32_16x16x32_bf16 v[140:143], a[164:167], v[68:71], v[140:143]\n"
"ds_write_addtid_b32 v128 offset:0\n"
"v_mul_f32 v136, v145, v40\n"
"v_mfma_f32_16x16x32_bf16 v[140:143], a[168:171], v[72:75], v[140:143]\n"
"s_waitcnt lgkmcnt(1)\n"
"ds_write_addtid_b32 v129 offset:4640\n"
"global_load_dwordx4 a[200:203], v6, s[56:57] offset:0 sc1\n"
"v_mfma_f32_16x16x32_bf16 v[140:143], a[172:175], v[76:79], v[140:143]\n"
"s_barrier\n"
"v_mfma_f32_16x16x32_bf16 v[140:143], a[176:179], v[80:83], v[140:143]\n"
"ds_write_addtid_b32 v130 offset:9344\n"
"ds_read2st64_b32 v[144:145], v12 offset0:0 offset1:1\n"
"v_mfma_f32_16x16x32_bf16 v[140:143], a[180:183], v[84:87], v[140:143]\n"
"ds_write_addtid_b32 v131 offset:13984\n"
"ds_read2st64_b32 v[146:147], v12 offset0:2 offset1:3\n"
"v_mfma_f32_16x16x32_bf16 v[140:143], a[184:187], v[88:91], v[140:143]\n"
"ds_read2st64_b32 v[156:157], v11 offset0:4 offset1:5\n"
"ds_read2st64_b32 v[164:165], v11 offset0:6 offset1:7\n"
"v_mfma_f32_16x16x32_bf16 v[140:143], a[188:191], v[92:95], v[140:143]\n"
"s_add_u32 s94, s50, 0xffc00000\n"
"s_and_b32 s95, s94, 0xfbffffff\n"
"v_mfma_f32_16x16x32_bf16 v[128:131], v[64:67], a[0:3], 0\n"
"global_load_dwordx4 a[204:207], v6, s[56:57] offset:1024 sc1\n"
"v_mfma_f32_16x16x32_bf16 v[128:131], v[68:71], a[4:7], v[128:131]\n"
"ds_write_addtid_b32 v140 offset:256\n"
"v_mfma_f32_16x16x32_bf16 v[128:131], v[72:75], a[8:11], v[128:131]\n"
"s_waitcnt lgkmcnt(3)\n"
"ds_write_addtid_b32 v141 offset:4896\n"
"v_mfma_f32_16x16x32_bf16 v[148:151], v[144:147], v[180:183], v[16:19]\n"
"s_cmp_eq_u32 s94, s62\n"
"s_cbranch_scc1 .LQ_fs\n"
".LQ_fr:\n"
"v_mfma_f32_16x16x32_bf16 v[128:131], v[76:79], a[12:15], v[128:131]\n"
"ds_write_addtid_b32 v142 offset:9600\n"
"v_max_f32 v253, v172, v174\n"
"v_mfma_f32_16x16x32_bf16 v[128:131], v[80:83], a[16:19], v[128:131]\n"
"ds_write_addtid_b32 v143 offset:14240\n"
"global_load_dwordx4 a[208:211], v7, s[56:57] offset:0 sc1\n"
"v_mfma_f32_16x16x32_bf16 v[128:131], v[84:87], a[20:23], v[128:131]\n"
"v_max_f32 v252, v173, v175\n"
"v_max3_f32 v253, v148, v150, v253\n"
"v_mfma_f32_16x16x32_bf16 v[128:131], v[88:91], a[24:27], v[128:131]\n"
"v_max3_f32 v252, v149, v151, v252\n"
"v_max_f32 v251, v253, v252\n"
"v_mfma_f32_16x16x32_bf16 v[128:131], v[92:95], a[28:31], v[128:131]\n"
"v_mov_b32 v250, v251\n"
"v_mfma_f32_16x16x32_bf16 v[140:143], v[64:67], a[32:35], 0\n"
"v_permlane32_swap_b32 v250, v251\n"
"v_mfma_f32_16x16x32_bf16 v[140:143], v[68:71], a[36:39], v[140:143]\n"
"ds_write_addtid_b32 v128 offset:512\n"
"global_load_dwordx4 a[212:215], v7, s[56:57] offset:1024 sc1\n"
"v_mfma_f32_16x16x32_bf16 v[140:143], v[72:75], a[40:43], v[140:143]\n"
"ds_write_addtid_b32 v129 offset:5152\n"
"v_max_f32 v251, v250, v251\n"
"v_mfma_f32_16x16x32_bf16 v[140:143], v[76:79], a[44:47], v[140:143]\n"
"ds_write_addtid_b32 v130 offset:9856\n"
"v_mov_b32 v250, v251\n"
"v_mfma_f32_16x16x32_bf16 v[140:143], v[80:83], a[48:51], v[140:143]\n"
"ds_write_addtid_b32 v131 offset:14496\n"
"v_mfma_f32_16x16x32_bf16 v[140:143], v[84:87], a[52:55], v[140:143]\n"
"v_permlane16_swap_b32 v250, v251\n"
"v_mfma_f32_16x16x32_bf16 v[140:143], v[88:91], a[56:59], v[140:143]\n"
"global_load_dwordx4 a[216:219], v8, s[56:57] offset:0 sc1\n"
"v_max_f32 v251, v250, v251\n"
"v_mfma_f32_16x16x32_bf16 v[140:143], v[92:95], a[60:63], v[140:143]\n"
"v_mul_f32 v251, v251, v249\n"
"v_fma_f32 v253, v253, v249, -v251\n"
"v_mfma_f32_16x16x32_bf16 v[132:135], a[64:67], v[64:67], v[132:135]\n"
"v_fma_f32 v252, v252, v249, -v251\n"
"v_mfma_f32_16x16x32_bf16 v[132:135], a[68:71], v[68:71], v[132:135]\n"
"ds_write_addtid_b32 v140 offset:768\n"
"v_mfma_f32_16x16x32_bf16 v[132:135], a[72:75], v[72:75], v[132:135]\n"
"ds_write_addtid_b32 v141 offset:5408\n"
"v_mfma_f32_16x16x32_bf16 v[132:135], a[76:79], v[76:79], v[132:135]\n"
"ds_write_addtid_b32 v142 offset:10112\n"
"global_load_dwordx4 a[220:223], v8, s[56:57] offset:1024 sc1\n"
"v_mfma_f32_16x16x32_bf16 v[132:135], a[80:83], v[80:83], v[132:135]\n"
"ds_write_addtid_b32 v143 offset:14752\n"
"v_mfma_f32_16x16x32_bf16 v[132:135], a[84:87], v[84:87], v[132:135]\n"
"v_exp_f32 v253, v253\n"
"v_mfma_f32_16x16x32_bf16 v[132:135], a[88:91], v[88:91], v[132:135]\n"
"v_exp_f32 v252, v252\n"
"v_mfma_f32_16x16x32_bf16 v[132:135], a[92:95], v[92:95], v[132:135]\n"
"s_add_u32 s58, s50, 0x100000\n"
"v_add_f32 v251, v253, v252\n"
"v_mov_b32 v250, v251\n"
"v_mfma_f32_16x16x32_bf16 v[136:139], a[96:99], v[64:67], v[136:139]\n"
"s_and_b32 s58, s58, 0x3ffffff\n"
"v_permlane32_swap_b32 v250, v251\n"
"v_mfma_f32_16x16x32_bf16 v[136:139], a[100:103], v[68:71], v[136:139]\n"
"s_add_u32 s56, s40, s58\n"
"v_add_f32 v251, v250, v251\n"
"v_mov_b32 v250, v251\n"
"v_mfma_f32_16x16x32_bf16 v[136:139], a[104:107], v[72:75], v[136:139]\n"
"s_addc_u32 s57, s41, 0\n"
"v_permlane16_swap_b32 v250, v251\n"
"v_mfma_f32_16x16x32_bf16 v[136:139], a[108:111], v[76:79], v[136:139]\n"
"s_waitcnt vmcnt(8)\n"
"v_cmp_ne_u64 vcc, s[48:49], v[170:171]\n"
"v_add_f32 v251, v250, v251\n"
"v_mfma_f32_16x16x32_bf16 v[136:139], a[112:115], v[80:83], v[136:139]\n"
"buffer_load_dwordx4 v[144:147], v9, s[40:43], s50 offen sc1\n"
"v_mfma_f32_16x16x32_bf16 v[136:139], a[116:119], v[84:87], v[136:139]\n"
"s_cbranch_vccnz .LQ_4s\n"
".LQ_4r:\n"
"v_mfma_f32_16x16x32_bf16 v[136:139], a[120:123], v[88:91], v[136:139]\n"
"s_add_u32 s50, s50, 0x100000\n"
"v_rcp_f32 v251, v251\n"
"v_mfma_f32_16x16x32_bf16 v[136:139], a[124:127], v[92:95], v[136:139]\n"
"global_load_dwordx4 a[224:227], v5, s[56:57] offset:0 sc1\n"
"v_mul_f32 v253, v253, v251\n"
"v_mfma_f32_16x16x32_bf16 v[64:67], v[96:99], a[64:67], 0\n"
"v_mul_f32 v252, v252, v251\n"
"v_cvt_pk_bf16_f32 v250, v253, v252\n"
"v_mfma_f32_16x16x32_bf16 v[64:67], v[100:103], a[68:71], v[64:67]\n"
"v_mfma_f32_16x16x32_bf16 v[64:67], v[104:107], a[72:75], v[64:67]\n"
"s_waitcnt lgkmcnt(12)\n"
"v_and_b32 v68, v250, v24\n"
"v_and_b32 v69, v250, v25\n"
"v_mfma_f32_16x16x32_bf16 v[64:67], v[108:111], a[76:79], v[64:67]\n"
"v_and_b32 v70, v250, v26\n"
"v_and_b32 v71, v250, v27\n"
"v_mfma_f32_16x16x32_bf16 v[64:67], v[112:115], a[80:83], v[64:67]\n"
"global_load_dwordx4 a[228:231], v5, s[56:57] offset:1024 sc1\n"
"v_mfma_f32_16x16x32_bf16 v[72:75], v[156:159], v[68:71], 0\n"
"v_mul_f32_dpp v91, v153, v44 row_newbcast:3 row_mask:0xf bank_mask:0xf\n"
"v_mov_b32 v253, v152\n"
"v_mfma_f32_16x16x32_bf16 v[76:79], v[164:167], v[68:71], 0\n"
"v_mul_f32_dpp v89, v153, v44 row_newbcast:1 row_mask:0xf bank_mask:0xf\n"
"v_mul_f32_dpp v90, v153, v44 row_newbcast:2 row_mask:0xf bank_mask:0xf\n"
"v_mfma_f32_16x16x32_bf16 v[64:67], v[116:119], a[84:87], v[64:67]\n"
"v_cvt_pk_bf16_f32 v68, v72, v73\n"
"v_cvt_pk_bf16_f32 v69, v74, v75\n"
"v_mfma_f32_16x16x32_bf16 v[64:67], v[120:123], a[88:91], v[64:67]\n"
"v_cvt_pk_bf16_f32 v70, v76, v77\n"
"v_cvt_pk_bf16_f32 v71, v78, v79\n"
"v_mfma_f32_16x16x32_bf16 v[64:67], v[124:127], a[92:95], v[64:67]\n"
"buffer_store_dwordx4 v[68:71], v13, s[40:43], s95 offen sc1\n"
"v_mul_f32_dpp v130, v153, v45 row_newbcast:2 row_mask:0xf bank_mask:0xf\n"
"v_mul_f32_dpp v131, v153, v45 row_newbcast:3 row_mask:0xf bank_mask:0xf\n"
"v_mul_f32_dpp v88, v153, v44 row_newbcast:0 row_mask:0xf bank_mask:0xf\n"
"v_mfma_f32_16x16x32_bf16 v[68:71], v[96:99], a[96:99], 0\n"
"s_waitcnt lgkmcnt(0)\n"
"v_mul_f32_dpp v128, v153, v45 row_newbcast:0 row_mask:0xf bank_mask:0xf\n"
"v_mul_f32_dpp v129, v153, v45 row_newbcast:1 row_mask:0xf bank_mask:0xf\n"
"v_mfma_f32_16x16x32_bf16 v[68:71], v[100:103], a[100:103], v[68:71]\n"
"s_barrier\n"
"v_mfma_f32_16x16x32_bf16 v[68:71], v[104:107], a[104:107], v[68:71]\n"
"ds_write_addtid_b32 v64 offset:1024\n"
"ds_read_b128 v[72:75], v2 offset:1024\n"
"v_mfma_f32_16x16x32_bf16 v[68:71], v[108:111], a[108:111], v[68:71]\n"
"ds_write_addtid_b32 v65 offset:5664\n"
"ds_read_b128 v[76:79], v3 offset:2048\n"
"v_mfma_f32_16x16x32_bf16 v[68:71], v[112:115], a[112:115], v[68:71]\n"
"ds_write_addtid_b32 v66 offset:10368\n"
"global_load_dwordx4 a[232:235], v6, s[56:57] offset:0 sc1\n"
"v_mfma_f32_16x16x32_bf16 v[68:71], v[116:119], a[116:119], v[68:71]\n"
"ds_write_addtid_b32 v67 offset:15008\n"
"ds_read_b128 v[80:83], v4 offset:3072\n"
"v_mfma_f32_16x16x32_bf16 v[68:71], v[120:123], a[120:123], v[68:71]\n"
"ds_read_b128 v[84:87], v2 offset:1280\n"
"ds_read_b128 v[64:67], v3 offset:2304\n"
"v_mfma_f32_16x16x32_bf16 v[68:71], v[124:127], a[124:127], v[68:71]\n"
"ds_read_b128 v[92:95], v4 offset:3328\n"
"v_mfma_f32_16x16x32_bf16 v[140:143], v[96:99], a[0:3], 0\n"
"global_load_dwordx4 a[236:239], v6, s[56:57] offset:1024 sc1\n"
"v_mfma_f32_16x16x32_bf16 v[140:143], v[100:103], a[4:7], v[140:143]\n"
"ds_write_addtid_b32 v68 offset:1280\n"
"v_mfma_f32_16x16x32_bf16 v[140:143], v[104:107], a[8:11], v[140:143]\n"
"ds_write_addtid_b32 v69 offset:5920\n"
"v_mfma_f32_16x16x32_bf16 v[140:143], v[108:111], a[12:15], v[140:143]\n"
"s_waitcnt lgkmcnt(5)\n"
"ds_write_addtid_b32 v70 offset:10624\n"
"v_add_f32 v72, v72, v76\n"
"v_mfma_f32_16x16x32_bf16 v[140:143], v[112:115], a[16:19], v[140:143]\n"
"ds_write_addtid_b32 v71 offset:15264\n"
"v_add_f32 v73, v73, v77\n"
"v_mfma_f32_16x16x32_bf16 v[140:143], v[116:119], a[20:23], v[140:143]\n"
"global_load_dwordx4 a[240:243], v7, s[56:57] offset:0 sc1\n"
"v_add_f32 v74, v74, v78\n"
"v_mfma_f32_16x16x32_bf16 v[140:143], v[120:123], a[24:27], v[140:143]\n"
"v_add_f32 v75, v75, v79\n"
"v_add_f32 v72, v72, v80\n"
"v_mfma_f32_16x16x32_bf16 v[140:143], v[124:127], a[28:31], v[140:143]\n"
"v_add_f32 v73, v73, v81\n"
"v_add_f32 v74, v74, v82\n"
"v_mfma_f32_16x16x32_bf16 v[68:71], v[96:99], a[32:35], 0\n"
"v_add_f32 v75, v75, v83\n"
"v_add_f32 v72, v72, v132\n"
"v_mfma_f32_16x16x32_bf16 v[68:71], v[100:103], a[36:39], v[68:71]\n"
"ds_write_addtid_b32 v140 offset:1536\n"
"v_add_f32 v73, v73, v133\n"
"v_mfma_f32_16x16x32_bf16 v[68:71], v[104:107], a[40:43], v[68:71]\n"
"ds_write_addtid_b32 v141 offset:6176\n"
"global_load_dwordx4 a[244:247], v7, s[56:57] offset:1024 sc1\n"
"v_mfma_f32_16x16x32_bf16 v[68:71], v[108:111], a[44:47], v[68:71]\n"
"ds_write_addtid_b32 v142 offset:10880\n"
"v_add_f32 v74, v74, v134\n"
"v_mfma_f32_16x16x32_bf16 v[68:71], v[112:115], a[48:51], v[68:71]\n"
"ds_write_addtid_b32 v143 offset:15520\n"
"v_add_f32 v75, v75, v135\n"
"v_mfma_f32_16x16x32_bf16 v[68:71], v[116:119], a[52:55], v[68:71]\n"
"v_mul_f32 v72, v254, v72\n"
"v_mul_f32 v73, v254, v73\n"
"v_mfma_f32_16x16x32_bf16 v[68:71], v[120:123], a[56:59], v[68:71]\n"
"v_mul_f32 v74, v254, v74\n"
"v_mul_f32 v75, v254, v75\n"
"v_mfma_f32_16x16x32_bf16 v[68:71], v[124:127], a[60:63], v[68:71]\n"
"global_load_dwordx4 a[248:251], v8, s[56:57] offset:0 sc1\n"
"v_cvt_pk_bf16_f32 v76, v72, v73\n"
"v_mfma_f32_16x16x32_bf16 v[88:91], v[96:99], a[128:131], v[88:91]\n"
"v_cvt_pk_bf16_f32 v77, v74, v75\n"
"v_mfma_f32_16x16x32_bf16 v[88:91], v[100:103], a[132:135], v[88:91]\n"
"ds_write_addtid_b32 v68 offset:1792\n"
"ds_write_addtid_b32 v76 offset:18592\n"
"v_mfma_f32_16x16x32_bf16 v[88:91], v[104:107], a[136:139], v[88:91]\n"
"ds_write_addtid_b32 v69 offset:6432\n"
"ds_write_addtid_b32 v77 offset:18848\n"
"v_mfma_f32_16x16x32_bf16 v[88:91], v[108:111], a[140:143], v[88:91]\n"
"s_waitcnt lgkmcnt(12)\n"
"ds_write_addtid_b32 v70 offset:11136\n"
"v_add_f32 v84, v84, v64\n"
"v_mfma_f32_16x16x32_bf16 v[88:91], v[112:115], a[144:147], v[88:91]\n"
"ds_write_addtid_b32 v71 offset:15776\n"
"global_load_dwordx4 a[252:255], v8, s[56:57] offset:1024 sc1\n"
"v_mfma_f32_16x16x32_bf16 v[88:91], v[116:119], a[148:151], v[88:91]\n"
"v_add_f32 v85, v85, v65\n"
"v_add_f32 v86, v86, v66\n"
"v_mfma_f32_16x16x32_bf16 v[88:91], v[120:123], a[152:155], v[88:91]\n"
"v_add_f32 v87, v87, v67\n"
"v_add_f32 v84, v84, v92\n"
"v_mfma_f32_16x16x32_bf16 v[88:91], v[124:127], a[156:159], v[88:91]\n"
"v_add_f32 v85, v85, v93\n"
"v_add_f32 v86, v86, v94\n"
"v_mfma_f32_16x16x32_bf16 v[128:131], v[96:99], a[160:163], v[128:131]\n"
"s_add_u32 s58, s50, 0x100000\n"
"v_add_f32 v87, v87, v95\n"
"v_add_f32 v84, v84, v136\n"
"v_mfma_f32_16x16x32_bf16 v[128:131], v[100:103], a[164:167], v[128:131]\n"
"s_and_b32 s58, s58, 0x3ffffff\n"
"v_add_f32 v85, v85, v137\n"
"v_add_f32 v86, v86, v138\n"
"v_mfma_f32_16x16x32_bf16 v[128:131], v[104:107], a[168:171], v[128:131]\n"
"s_add_u32 s56, s40, s58\n"
"v_add_f32 v87, v87, v139\n"
"v_mul_f32 v84, v254, v84\n"
"v_mfma_f32_16x16x32_bf16 v[128:131], v[108:111], a[172:175], v[128:131]\n"
"s_addc_u32 s57, s41, 0\n"
"v_mul_f32 v85, v254, v85\n"
"v_mul_f32 v86, v254, v86\n"
"v_mfma_f32_16x16x32_bf16 v[128:131], v[112:115], a[176:179], v[128:131]\n"
"s_waitcnt vmcnt(9)\n"
"v_cmp_ne_u64 vcc, s[48:49], v[146:147]\n"
"v_mul_f32 v87, v254, v87\n"
"v_mfma_f32_16x16x32_bf16 v[128:131], v[116:119], a[180:183], v[128:131]\n"
"buffer_load_dwordx4 v[152:155], v10, s[40:43], s50 offen sc1\n"
"v_mfma_f32_16x16x32_bf16 v[128:131], v[120:123], a[184:187], v[128:131]\n"
"s_cbranch_vccnz .LQ_1s\n"
".LQ_1r:\n"
"v_cvt_pk_bf16_f32 v78, v84, v85\n"
"v_mfma_f32_16x16x32_bf16 v[128:131], v[124:127], a[188:191], v[128:131]\n"
"v_cvt_pk_bf16_f32 v79, v86, v87\n"
"v_mfma_f32_16x16x32_bf16 v[80:83], a[192:195], a[64:67], 0\n"
"s_add_u32 s50, s50, 0x100000\n"
"global_load_dwordx4 v[64:67], v5, s[56:57] offset:0 sc1\n"
"ds_write_addtid_b32 v78 offset:19104\n"
"v_mfma_f32_16x16x32_bf16 v[80:83], a[196:199], a[68:71], v[80:83]\n"
"ds_write_addtid_b32 v79 offset:19360\n"
"v_mfma_f32_16x16x32_bf16 v[80:83], a[200:203], a[72:75], v[80:83]\n"
"v_mfma_f32_16x16x32_bf16 v[80:83], a[204:207], a[76:79], v[80:83]\n"
"v_mul_f32 v249, 0x3e8293ee, v160\n"
"v_mfma_f32_16x16x32_bf16 v[80:83], a[208:211], a[80:83], v[80:83]\n"
"v_mul_f32 v126, v161, v30\n"
"v_mul_f32 v127, v161, v31\n"
"v_mfma_f32_16x16x32_bf16 v[80:83], a[212:215], a[84:87], v[80:83]\n"
"global_load_dwordx4 v[68:71], v5, s[56:57] offset:1024 sc1\n"
"v_mul_f32 v125, v161, v29\n"
"v_mfma_f32_16x16x32_bf16 v[80:83], a[216:219], a[88:91], v[80:83]\n"
"v_mul_f32 v135, v161, v35\n"
"v_mul_f32 v124, v161, v28\n"
"v_mfma_f32_16x16x32_bf16 v[80:83], a[220:223], a[92:95], v[80:83]\n"
"s_waitcnt vmcnt(9)\n"
"buffer_store_dwordx2 v[0:1], v14, s[40:43], s94 offen offset:0 sc1\n"
"v_mul_f32 v134, v161, v34\n"
"v_mfma_f32_16x16x32_bf16 v[84:87], a[192:195], a[96:99], 0\n"
"s_waitcnt lgkmcnt(0)\n"
"v_mul_f32 v132, v161, v32\n"
"v_mul_f32 v133, v161, v33\n"
"v_mfma_f32_16x16x32_bf16 v[84:87], a[196:199], a[100:103], v[84:87]\n"
"s_barrier\n"
"v_mfma_f32_16x16x32_bf16 v[84:87], a[200:203], a[104:107], v[84:87]\n"
"ds_write_addtid_b32 v80 offset:2048\n"
"global_load_dwordx4 v[72:75], v6, s[56:57] offset:0 sc1\n"
"v_mfma_f32_16x16x32_bf16 v[84:87], a[204:207], a[108:111], v[84:87]\n"
"ds_write_addtid_b32 v81 offset:6688\n"
"ds_read_b128 v[92:95], v2 offset:2560\n"
"v_mfma_f32_16x16x32_bf16 v[84:87], a[208:211], a[112:115], v[84:87]\n"
"ds_write_addtid_b32 v82 offset:11392\n"
"ds_read_b128 v[96:99], v3 offset:3584\n"
"v_mfma_f32_16x16x32_bf16 v[84:87], a[212:215], a[116:119], v[84:87]\n"
"ds_write_addtid_b32 v83 offset:16032\n"
"ds_read_b128 v[100:103], v4 offset:0\n"
"v_mfma_f32_16x16x32_bf16 v[84:87], a[216:219], a[120:123], v[84:87]\n"
"ds_read_b128 v[104:107], v2 offset:2816\n"
"ds_read_b128 v[108:111], v3 offset:3840\n"
"v_mfma_f32_16x16x32_bf16 v[84:87], a[220:223], a[124:127], v[84:87]\n"
"global_load_dwordx4 v[76:79], v6, s[56:57] offset:1024 sc1\n"
"ds_read_b128 v[112:115], v4 offset:256\n"
"v_mfma_f32_16x16x32_bf16 v[116:119], a[128:131], a[192:195], 0\n"
"v_mfma_f32_16x16x32_bf16 v[116:119], a[132:135], a[196:199], v[116:119]\n"
"ds_write_addtid_b32 v84 offset:2304\n"
"v_mfma_f32_16x16x32_bf16 v[116:119], a[136:139], a[200:203], v[116:119]\n"
"ds_write_addtid_b32 v85 offset:6944\n"
"v_mfma_f32_16x16x32_bf16 v[116:119], a[140:143], a[204:207], v[116:119]\n"
"s_waitcnt lgkmcnt(5)\n"
"ds_write_addtid_b32 v86 offset:11648\n"
"v_add_f32 v92, v92, v96\n"
"v_mfma_f32_16x16x32_bf16 v[116:119], a[144:147], a[208:211], v[116:119]\n"
"ds_write_addtid_b32 v87 offset:16288\n"
"global_load_dwordx4 v[80:83], v7, s[56:57] offset:0 sc1\n"
"v_mfma_f32_16x16x32_bf16 v[116:119], a[148:151], a[212:215], v[116:119]\n"
"v_add_f32 v93, v93, v97\n"
"v_add_f32 v94, v94, v98\n"
"v_mfma_f32_16x16x32_bf16 v[116:119], a[152:155], a[216:219], v[116:119]\n"
"v_add_f32 v95, v95, v99\n"
"v_add_f32 v92, v92, v100\n"
"v_mfma_f32_16x16x32_bf16 v[116:119], a[156:159], a[220:223], v[116:119]\n"
"v_add_f32 v93, v93, v101\n"
"v_add_f32 v94, v94, v102\n"
"v_mfma_f32_16x16x32_bf16 v[96:99], a[160:163], a[192:195], 0\n"
"v_add_f32 v95, v95, v103\n"
"v_add_f32 v92, v92, v88\n"
"v_mfma_f32_16x16x32_bf16 v[96:99], a[164:167], a[196:199], v[96:99]\n"
"ds_write_addtid_b32 v116 offset:2560\n"
"global_load_dwordx4 v[84:87], v7, s[56:57] offset:1024 sc1\n"
"v_mfma_f32_16x16x32_bf16 v[96:99], a[168:171], a[200:203], v[96:99]\n"
"ds_write_addtid_b32 v117 offset:7200\n"
"v_add_f32 v93, v93, v89\n"
"v_mfma_f32_16x16x32_bf16 v[96:99], a[172:175], a[204:207], v[96:99]\n"
"ds_write_addtid_b32 v118 offset:11904\n"
"v_add_f32 v94, v94, v90\n"
"v_mfma_f32_16x16x32_bf16 v[96:99], a[176:179], a[208:211], v[96:99]\n"
"ds_write_addtid_b32 v119 offset:16544\n"
"v_add_f32 v95, v95, v91\n"
"v_mfma_f32_16x16x32_bf16 v[96:99], a[180:183], a[212:215], v[96:99]\n"
"v_mul_f32_dpp v92, v253, v92 row_newbcast:0 row_mask:0xf bank_mask:0xf\n"
"v_mul_f32_dpp v93, v253, v93 row_newbcast:1 row_mask:0xf bank_mask:0xf\n"
"v_mfma_f32_16x16x32_bf16 v[96:99], a[184:187], a[216:219], v[96:99]\n"
"global_load_dwordx4 v[88:91], v8, s[56:57] offset:0 sc1\n"
"v_mul_f32_dpp v94, v253, v94 row_newbcast:2 row_mask:0xf bank_mask:0xf\n"
"v_mfma_f32_16x16x32_bf16 v[96:99], a[188:191], a[220:223], v[96:99]\n"
"v_mul_f32_dpp v95, v253, v95 row_newbcast:3 row_mask:0xf bank_mask:0xf\n"
"v_cvt_pk_bf16_f32 v158, v92, v93\n"
"v_mfma_f32_16x16x32_bf16 v[124:127], a[0:3], a[192:195], v[124:127]\n"
"v_cvt_pk_bf16_f32 v159, v94, v95\n"
"v_mfma_f32_16x16x32_bf16 v[124:127], a[4:7], a[196:199], v[124:127]\n"
"ds_write_addtid_b32 v96 offset:2816\n"
"ds_write_addtid_b32 v158 offset:19616\n"
"v_mfma_f32_16x16x32_bf16 v[124:127], a[8:11], a[200:203], v[124:127]\n"
"ds_write_addtid_b32 v97 offset:7456\n"
"ds_write_addtid_b32 v159 offset:19872\n"
"v_mfma_f32_16x16x32_bf16 v[124:127], a[12:15], a[204:207], v[124:127]\n"
"s_waitcnt lgkmcnt(12)\n"
"ds_write_addtid_b32 v98 offset:12160\n"
"global_load_dwordx4 v[92:95], v8, s[56:57] offset:1024 sc1\n"
"v_mfma_f32_16x16x32_bf16 v[124:127], a[16:19], a[208:211], v[124:127]\n"
"ds_write_addtid_b32 v99 offset:16800\n"
"v_add_f32 v104, v104, v108\n"
"v_mfma_f32_16x16x32_bf16 v[124:127], a[20:23], a[212:215], v[124:127]\n"
"v_add_f32 v105, v105, v109\n"
"v_add_f32 v106, v106, v110\n"
"v_mfma_f32_16x16x32_bf16 v[124:127], a[24:27], a[216:219], v[124:127]\n"
"v_add_f32 v107, v107, v111\n"
"v_add_f32 v104, v104, v112\n"
"v_mfma_f32_16x16x32_bf16 v[124:127], a[28:31], a[220:223], v[124:127]\n"
"v_add_f32 v105, v105, v113\n"
"v_add_f32 v106, v106, v114\n"
"v_mfma_f32_16x16x32_bf16 v[132:135], a[32:35], a[192:195], v[132:135]\n"
"v_add_f32 v107, v107, v115\n"
"v_add_f32 v104, v104, v128\n"
"v_mfma_f32_16x16x32_bf16 v[132:135], a[36:39], a[196:199], v[132:135]\n"
"v_add_f32 v105, v105, v129\n"
"v_add_f32 v106, v106, v130\n"
"v_mfma_f32_16x16x32_bf16 v[132:135], a[40:43], a[200:203], v[132:135]\n"
"s_add_u32 s58, s50, 0x100000\n"
"buffer_load_dwordx4 v[160:163], v9, s[40:43], s50 offen sc1\n"
"v_mfma_f32_16x16x32_bf16 v[132:135], a[44:47], a[204:207], v[132:135]\n"
"s_and_b32 s58, s58, 0x3ffffff\n"
"v_add_f32 v107, v107, v131\n"
"v_mul_f32_dpp v104, v253, v104 row_newbcast:0 row_mask:0xf bank_mask:0xf\n"
"v_mfma_f32_16x16x32_bf16 v[132:135], a[48:51], a[208:211], v[132:135]\n"
"s_add_u32 s56, s40, s58\n"
"v_mul_f32_dpp v105, v253, v105 row_newbcast:1 row_mask:0xf bank_mask:0xf\n"
"v_mul_f32_dpp v106, v253, v106 row_newbcast:2 row_mask:0xf bank_mask:0xf\n"
"v_mfma_f32_16x16x32_bf16 v[132:135], a[52:55], a[212:215], v[132:135]\n"
"s_addc_u32 s57, s41, 0\n"
"v_mul_f32_dpp v107, v253, v107 row_newbcast:3 row_mask:0xf bank_mask:0xf\n"
"v_cvt_pk_bf16_f32 v166, v104, v105\n"
"v_mfma_f32_16x16x32_bf16 v[132:135], a[56:59], a[216:219], v[132:135]\n"
"s_waitcnt vmcnt(10)\n"
"v_cmp_ne_u64 vcc, s[48:49], v[154:155]\n"
"v_cvt_pk_bf16_f32 v167, v106, v107\n"
"v_mfma_f32_16x16x32_bf16 v[132:135], a[60:63], a[220:223], v[132:135]\n"
"ds_write_addtid_b32 v166 offset:20128\n"
"ds_write_addtid_b32 v167 offset:20384\n"
"v_mfma_f32_16x16x32_bf16 v[108:111], a[224:227], a[64:67], 0\n"
"s_cbranch_vccnz .LQ_2s\n"
".LQ_2r:\n"
"v_mfma_f32_16x16x32_bf16 v[108:111], a[228:231], a[68:71], v[108:111]\n"
"global_load_dwordx4 v[96:99], v5, s[56:57] offset:0 sc1\n"
"v_mfma_f32_16x16x32_bf16 v[108:111], a[232:235], a[72:75], v[108:111]\n"
"s_add_u32 s50, s50, 0x100000\n"
"v_mfma_f32_16x16x32_bf16 v[108:111], a[236:239], a[76:79], v[108:111]\n"
"v_mfma_f32_16x16x32_bf16 v[108:111], a[240:243], a[80:83], v[108:111]\n"
"v_mfma_f32_16x16x32_bf16 v[108:111], a[244:247], a[84:87], v[108:111]\n"
"v_mfma_f32_16x16x32_bf16 v[108:111], a[248:251], a[88:91], v[108:111]\n"
"global_load_dwordx4 v[100:103], v5, s[56:57] offset:1024 sc1\n"
"v_mfma_f32_16x16x32_bf16 v[108:111], a[252:255], a[92:95], v[108:111]\n"
"v_mfma_f32_16x16x32_bf16 v[116:119], a[224:227], a[96:99], 0\n"
"v_mfma_f32_16x16x32_bf16 v[116:119], a[228:231], a[100:103], v[116:119]\n"
"ds_write_addtid_b32 v108 offset:3072\n"
"v_mfma_f32_16x16x32_bf16 v[116:119], a[232:235], a[104:107], v[116:119]\n"
"s_waitcnt lgkmcnt(1)\n"
"ds_write_addtid_b32 v109 offset:7712\n"
"v_mfma_f32_16x16x32_bf16 v[116:119], a[236:239], a[108:111], v[116:119]\n"
"s_barrier\n"
"v_mfma_f32_16x16x32_bf16 v[116:119], a[240:243], a[112:115], v[116:119]\n"
"ds_write_addtid_b32 v110 offset:12416\n"
"ds_read_b128 v[128:131], v2 offset:4096\n"
"v_mfma_f32_16x16x32_bf16 v[116:119], a[244:247], a[116:119], v[116:119]\n"
"ds_write_addtid_b32 v111 offset:17056\n"
"global_load_dwordx4 v[104:107], v6, s[56:57] offset:0 sc1\n"
"v_mfma_f32_16x16x32_bf16 v[116:119], a[248:251], a[120:123], v[116:119]\n"
"ds_read_b128 v[120:123], v3 offset:512\n"
"ds_read_b128 v[136:139], v4 offset:1536\n"
"v_mfma_f32_16x16x32_bf16 v[116:119], a[252:255], a[124:127], v[116:119]\n"
"global_load_dwordx4 v[108:111], v6, s[56:57] offset:1024 sc1\n"
"ds_read_b128 v[140:143], v2 offset:4352\n"
"v_mfma_f32_16x16x32_bf16 v[148:151], a[128:131], a[224:227], 0\n"
"ds_read_b128 v[168:171], v3 offset:768\n"
"ds_read_b128 v[172:175], v4 offset:1792\n"
"v_mfma_f32_16x16x32_bf16 v[148:151], a[132:135], a[228:231], v[148:151]\n"
"ds_write_addtid_b32 v116 offset:3328\n"
"v_mfma_f32_16x16x32_bf16 v[148:151], a[136:139], a[232:235], v[148:151]\n"
"ds_write_addtid_b32 v117 offset:7968\n"
"v_mfma_f32_16x16x32_bf16 v[148:151], a[140:143], a[236:239], v[148:151]\n"
"ds_write_addtid_b32 v118 offset:12672\n"
"v_mfma_f32_16x16x32_bf16 v[148:151], a[144:147], a[240:243], v[148:151]\n"
"s_waitcnt lgkmcnt(6)\n"
"ds_write_addtid_b32 v119 offset:17312\n"
"global_load_dwordx4 v[112:115], v7, s[56:57] offset:0 sc1\n"
"v_mfma_f32_16x16x32_bf16 v[148:151], a[148:151], a[244:247], v[148:151]\n"
"v_add_f32 v128, v128, v120\n"
"v_add_f32 v129, v129, v121\n"
"v_mfma_f32_16x16x32_bf16 v[148:151], a[152:155], a[248:251], v[148:151]\n"
"v_add_f32 v130, v130, v122\n"
"v_add_f32 v131, v131, v123\n"
"v_mfma_f32_16x16x32_bf16 v[148:151], a[156:159], a[252:255], v[148:151]\n"
"v_add_f32 v128, v128, v136\n"
"v_add_f32 v129, v129, v137\n"
"v_mfma_f32_16x16x32_bf16 v[176:179], a[160:163], a[224:227], 0\n"
"v_add_f32 v130, v130, v138\n"
"v_add_f32 v131, v131, v139\n"
"v_mfma_f32_16x16x32_bf16 v[176:179], a[164:167], a[228:231], v[176:179]\n"
"ds_write_addtid_b32 v148 offset:3584\n"
"global_load_dwordx4 v[116:119], v7, s[56:57] offset:1024 sc1\n"
"v_mfma_f32_16x16x32_bf16 v[176:179], a[168:171], a[232:235], v[176:179]\n"
"ds_write_addtid_b32 v149 offset:8224\n"
"v_add_f32 v128, v128, v124\n"
"v_mfma_f32_16x16x32_bf16 v[176:179], a[172:175], a[236:239], v[176:179]\n"
"ds_write_addtid_b32 v150 offset:12928\n"
"v_add_f32 v129, v129, v125\n"
"v_mfma_f32_16x16x32_bf16 v[176:179], a[176:179], a[240:243], v[176:179]\n"
"ds_write_addtid_b32 v151 offset:17568\n"
"v_add_f32 v130, v130, v126\n"
"v_mfma_f32_16x16x32_bf16 v[176:179], a[180:183], a[244:247], v[176:179]\n"
"v_add_f32 v131, v131, v127\n"
"v_cvt_pk_bf16_f32 v180, v128, v129\n"
"v_mfma_f32_16x16x32_bf16 v[176:179], a[184:187], a[248:251], v[176:179]\n"
"global_load_dwordx4 v[120:123], v8, s[56:57] offset:0 sc1\n"
"v_cvt_pk_bf16_f32 v181, v130, v131\n"
"v_mfma_f32_16x16x32_bf16 v[176:179], a[188:191], a[252:255], v[176:179]\n"
"ds_read2st64_b32 v[136:137], v11 offset0:0 offset1:1\n"
"ds_read2st64_b32 v[138:139], v11 offset0:2 offset1:3\n"
"v_mfma_f32_16x16x32_bf16 v[128:131], a[224:227], a[0:3], 0\n"
"s_waitcnt lgkmcnt(10)\n"
"v_add_f32 v140, v140, v168\n"
"v_add_f32 v141, v141, v169\n"
"v_mfma_f32_16x16x32_bf16 v[128:131], a[228:231], a[4:7], v[128:131]\n"
"ds_write_addtid_b32 v176 offset:3840\n"
"v_add_f32 v142, v142, v170\n"
"v_mfma_f32_16x16x32_bf16 v[128:131], a[232:235], a[8:11], v[128:131]\n"
"ds_write_addtid_b32 v177 offset:8480\n"
"v_add_f32 v143, v143, v171\n"
"v_mfma_f32_16x16x32_bf16 v[128:131], a[236:239], a[12:15], v[128:131]\n"
"ds_write_addtid_b32 v178 offset:13184\n"
"global_load_dwordx4 v[124:127], v8, s[56:57] offset:1024 sc1\n"
"v_mfma_f32_16x16x32_bf16 v[128:131], a[240:243], a[16:19], v[128:131]\n"
"ds_write_addtid_b32 v179 offset:17824\n"
"v_add_f32 v140, v140, v172\n"
"v_mfma_f32_16x16x32_bf16 v[128:131], a[244:247], a[20:23], v[128:131]\n"
"v_add_f32 v141, v141, v173\n"
"v_add_f32 v142, v142, v174\n"
"v_mfma_f32_16x16x32_bf16 v[128:131], a[248:251], a[24:27], v[128:131]\n"
"v_add_f32 v143, v143, v175\n"
"v_add_f32 v140, v140, v132\n"
"v_mfma_f32_16x16x32_bf16 v[128:131], a[252:255], a[28:31], v[128:131]\n"
"v_add_f32 v141, v141, v133\n"
"v_add_f32 v142, v142, v134\n"
"v_mfma_f32_16x16x32_bf16 v[148:151], a[224:227], a[32:35], 0\n"
"s_add_u32 s58, s50, 0x100000\n"
"buffer_load_dwordx4 v[168:171], v9, s[40:43], s50 offen sc1\n"
"v_mfma_f32_16x16x32_bf16 v[148:151], a[228:231], a[36:39], v[148:151]\n"
"s_and_b32 s58, s58, 0x3ffffff\n"
"ds_write_addtid_b32 v128 offset:4096\n"
"v_add_f32 v143, v143, v135\n"
"v_mfma_f32_16x16x32_bf16 v[148:151], a[232:235], a[40:43], v[148:151]\n"
"s_add_u32 s56, s40, s58\n"
"ds_write_addtid_b32 v129 offset:8736\n"
"v_cvt_pk_bf16_f32 v182, v140, v141\n"
"v_mfma_f32_16x16x32_bf16 v[148:151], a[236:239], a[44:47], v[148:151]\n"
"s_addc_u32 s57, s41, 0\n"
"ds_write_addtid_b32 v130 offset:13440\n"
"v_cvt_pk_bf16_f32 v183, v142, v143\n"
"v_mfma_f32_16x16x32_bf16 v[148:151], a[240:243], a[48:51], v[148:151]\n"
"s_waitcnt vmcnt(9)\n"
"ds_write_addtid_b32 v131 offset:18080\n"
"v_cmp_ne_u64 vcc, s[48:49], v[162:163]\n"
"v_mfma_f32_16x16x32_bf16 v[148:151], a[244:247], a[52:55], v[148:151]\n"
"s_waitcnt lgkmcnt(8)\n"
"v_mfma_f32_16x16x32_bf16 v[172:175], v[136:139], v[180:183], v[20:23]\n"
"v_mfma_f32_16x16x32_bf16 v[148:151], a[248:251], a[56:59], v[148:151]\n"
"s_cbranch_vccnz .LQ_3s\n"
".LQ_3r:\n"
"v_mfma_f32_16x16x32_bf16 v[148:151], a[252:255], a[60:63], v[148:151]\n"
"s_branch .LQ_top\n"
".LQ_1s:\n"
"s_cmp_ge_u32 s50, s53\n"
"s_cbranch_scc1 .LQ_t0\n"
"s_cmp_ge_u32 s50, s52\n"
"s_cbranch_scc1 .LQ_1r\n"
"s_sub_u32 s60, s50, 0x100000\n"
"s_mov_b32 s61, 200000\n"
"s_nop 7\n"
".LQ_1p:\n"
"buffer_load_dwordx4 v[144:147], v9, s[40:43], s60 offen sc1\n"
"s_waitcnt vmcnt(0)\n"
"v_cmp_ne_u64 vcc, s[48:49], v[146:147]\n"
"s_cbranch_vccz .LQ_1px\n"
"s_sleep 1\n"
"s_sub_u32 s61, s61, 1\n"
"s_cbranch_scc0 .LQ_1p\n"
"s_branch .LQ_end\n"
".LQ_1px:\n"
"s_branch .LQ_1r\n"
".LQ_2s:\n"
"s_cmp_ge_u32 s50, s53\n"
"s_cbranch_scc1 .LQ_t1\n"
"s_cmp_ge_u32 s50, s52\n"
"s_cbranch_scc1 .LQ_2r\n"
"s_sub_u32 s60, s50, 0x100000\n"
"s_mov_b32 s61, 200000\n"
"s_nop 7\n"
".LQ_2p:\n"
"buffer_load_dwordx4 v[152:155], v10, s[40:43], s60 offen sc1\n"
"s_waitcnt vmcnt(0)\n"
"v_cmp_ne_u64 vcc, s[48:49], v[154:155]\n"
"s_cbranch_vccz .LQ_2px\n"
"s_sleep 1\n"
"s_sub_u32 s61, s61, 1\n"
"s_cbranch_scc0 .LQ_2p\n"
"s_branch .LQ_end\n"
".LQ_2px:\n"
"s_branch .LQ_2r\n"
".LQ_3s:\n"
"s_cmp_ge_u32 s50, s53\n"
"s_cbranch_scc1 .LQ_t2\n"
"s_cmp_ge_u32 s50, s52\n"
"s_cbranch_scc1 .LQ_3r\n"
"s_sub_u32 s60, s50, 0x100000\n"
"s_mov_b32 s61, 200000\n"
"s_nop 7\n"
".LQ_3p:\n"
"buffer_load_dwordx4 v[160:163], v9, s[40:43], s60 offen sc1\n"
"s_waitcnt vmcnt(0)\n"
"v_cmp_ne_u64 vcc, s[48:49], v[162:163]\n"
"s_cbranch_vccz .LQ_3px\n"
"s_sleep 1\n"
"s_sub_u32 s61, s61, 1\n"
"s_cbranch_scc0 .LQ_3p\n"
"s_branch .LQ_end\n"
".LQ_3px:\n"
"s_branch .LQ_3r\n"
".LQ_4s:\n"
"s_cmp_ge_u32 s50, s53\n"
"s_cbranch_scc1 .LQ_t3\n"
"s_cmp_ge_u32 s50, s52\n"
"s_cbranch_scc1 .LQ_4r\n"
"s_sub_u32 s60, s50, 0x100000\n"
"s_mov_b32 s61, 200000\n"
"s_nop 7\n"
".LQ_4p:\n"
"buffer_load_dwordx4 v[168:171], v9, s[40:43], s60 offen sc1\n"
"s_waitcnt vmcnt(0)\n"
"v_cmp_ne_u64 vcc, s[48:49], v[170:171]\n"
"s_cbranch_vccz .LQ_4px\n"
"s_sleep 1\n"
"s_sub_u32 s61, s61, 1\n"
"s_cbranch_scc0 .LQ_4p\n"
"s_branch .LQ_end\n"
".LQ_4px:\n"
"s_branch .LQ_4r\n"
".LQ_fs:\n"
"s_nop 7\n"
"v_mov_b32 v172, 0xff800000\n"
"v_mov_b32 v173, 0xff800000\n"
"v_mov_b32 v174, 0xff800000\n"
"v_mov_b32 v175, 0xff800000\n"
"v_mov_b32 v158, 0\n"
"v_mov_b32 v159, 0\n"
"v_mov_b32 v166, 0\n"
"v_mov_b32 v167, 0\n"
"s_add_u32 s62, s62, 0x4000000\n"
"s_branch .LQ_fr\n"
".LQ_ft0k7s:\n"
"s_nop 7\n"
"v_mov_b32 v172, 0xff800000\n"
"v_mov_b32 v173, 0xff800000\n"
"v_mov_b32 v174, 0xff800000\n"
"v_mov_b32 v175, 0xff800000\n"
"v_mov_b32 v158, 0\n"
"v_mov_b32 v159, 0\n"
"v_mov_b32 v166, 0\n"
"v_mov_b32 v167, 0\n"
"s_add_u32 s62, s62, 0x4000000\n"
"s_branch .LQ_ft0k7r\n"
".LQ_ft1k7s:\n"
"s_nop 7\n"
"v_mov_b32 v172, 0xff800000\n"
"v_mov_b32 v173, 0xff800000\n"
"v_mov_b32 v174, 0xff800000\n"
"v_mov_b32 v175, 0xff800000\n"
"v_mov_b32 v158, 0\n"
"v_mov_b32 v159, 0\n"
"v_mov_b32 v166, 0\n"
"v_mov_b32 v167, 0\n"
"s_add_u32 s62, s62, 0x4000000\n"
"s_branch .LQ_ft1k7r\n"
".LQ_ft2k7s:\n"
"s_nop 7\n"
"v_mov_b32 v172, 0xff800000\n"
"v_mov_b32 v173, 0xff800000\n"
"v_mov_b32 v174, 0xff800000\n"
"v_mov_b32 v175, 0xff800000\n"
"v_mov_b32 v158, 0\n"
"v_mov_b32 v159, 0\n"
"v_mov_b32 v166, 0\n"
"v_mov_b32 v167, 0\n"
"s_add_u32 s62, s62, 0x4000000\n"
"s_branch .LQ_ft2k7r\n"
".LQ_ft3k0s:\n"
"s_nop 7\n"
"v_mov_b32 v172, 0xff800000\n"
"v_mov_b32 v173, 0xff800000\n"
"v_mov_b32 v174, 0xff800000\n"
"v_mov_b32 v175, 0xff800000\n"
"v_mov_b32 v158, 0\n"
"v_mov_b32 v159, 0\n"
"v_mov_b32 v166, 0\n"
"v_mov_b32 v167, 0\n"
"s_add_u32 s62, s62, 0x4000000\n"
"s_branch .LQ_ft3k0r\n"
".LQ_t0:\n"
"v_cvt_pk_bf16_f32 v78, v84, v85\n"
"v_mfma_f32_16x16x32_bf16 v[128:131], v[124:127], a[188:191], v[128:131]\n"
"s_waitcnt vmcnt(17)\n"
"v_cvt_pk_bf16_f32 v79, v86, v87\n"
"v_mfma_f32_16x16x32_bf16 v[80:83], a[192:195], a[64:67], 0\n"
"ds_write_addtid_b32 v78 offset:19104\n"
"v_mfma_f32_16x16x32_bf16 v[80:83], a[196:199], a[68:71], v[80:83]\n"
"s_waitcnt vmcnt(15)\n"
"ds_write_addtid_b32 v79 offset:19360\n"
"v_mfma_f32_16x16x32_bf16 v[80:83], a[200:203], a[72:75], v[80:83]\n"
"v_mfma_f32_16x16x32_bf16 v[80:83], a[204:207], a[76:79], v[80:83]\n"
"s_waitcnt vmcnt(13)\n"
"v_mul_f32 v249, 0x3e8293ee, v160\n"
"v_mfma_f32_16x16x32_bf16 v[80:83], a[208:211], a[80:83], v[80:83]\n"
"v_mul_f32 v126, v161, v30\n"
"v_mul_f32 v127, v161, v31\n"
"v_mfma_f32_16x16x32_bf16 v[80:83], a[212:215], a[84:87], v[80:83]\n"
"s_waitcnt vmcnt(11)\n"
"v_mul_f32 v125, v161, v29\n"
"v_mfma_f32_16x16x32_bf16 v[80:83], a[216:219], a[88:91], v[80:83]\n"
"v_mul_f32 v135, v161, v35\n"
"v_mul_f32 v124, v161, v28\n"
"v_mfma_f32_16x16x32_bf16 v[80:83], a[220:223], a[92:95], v[80:83]\n"
"s_waitcnt vmcnt(7)\n"
"buffer_store_dwordx2 v[0:1], v14, s[40:43], s94 offen offset:0 sc1\n"
"v_mul_f32 v134, v161, v34\n"
"v_mfma_f32_16x16x32_bf16 v[84:87], a[192:195], a[96:99], 0\n"
"s_waitcnt lgkmcnt(0)\n"
"v_mul_f32 v132, v161, v32\n"
"v_mul_f32 v133, v161, v33\n"
"v_mfma_f32_16x16x32_bf16 v[84:87], a[196:199], a[100:103], v[84:87]\n"
"s_barrier\n"
"v_mfma_f32_16x16x32_bf16 v[84:87], a[200:203], a[104:107], v[84:87]\n"
"ds_write_addtid_b32 v80 offset:2048\n"
"v_mfma_f32_16x16x32_bf16 v[84:87], a[204:207], a[108:111], v[84:87]\n"
"ds_write_addtid_b32 v81 offset:6688\n"
"ds_read_b128 v[92:95], v2 offset:2560\n"
"v_mfma_f32_16x16x32_bf16 v[84:87], a[208:211], a[112:115], v[84:87]\n"
"ds_write_addtid_b32 v82 offset:11392\n"
"ds_read_b128 v[96:99], v3 offset:3584\n"
"v_mfma_f32_16x16x32_bf16 v[84:87], a[212:215], a[116:119], v[84:87]\n"
"ds_write_addtid_b32 v83 offset:16032\n"
"ds_read_b128 v[100:103], v4 offset:0\n"
"v_mfma_f32_16x16x32_bf16 v[84:87], a[216:219], a[120:123], v[84:87]\n"
"ds_read_b128 v[104:107], v2 offset:2816\n"
"ds_read_b128 v[108:111], v3 offset:3840\n"
"v_mfma_f32_16x16x32_bf16 v[84:87], a[220:223], a[124:127], v[84:87]\n"
"ds_read_b128 v[112:115], v4 offset:256\n"
"v_mfma_f32_16x16x32_bf16 v[116:119], a[128:131], a[192:195], 0\n"
"v_mfma_f32_16x16x32_bf16 v[116:119], a[132:135], a[196:199], v[116:119]\n"
"ds_write_addtid_b32 v84 offset:2304\n"
"v_mfma_f32_16x16x32_bf16 v[116:119], a[136:139], a[200:203], v[116:119]\n"
"ds_write_addtid_b32 v85 offset:6944\n"
"v_mfma_f32_16x16x32_bf16 v[116:119], a[140:143], a[204:207], v[116:119]\n"
"s_waitcnt lgkmcnt(5)\n"
"ds_write_addtid_b32 v86 offset:11648\n"
"v_add_f32 v92, v92, v96\n"
"v_mfma_f32_16x16x32_bf16 v[116:119], a[144:147], a[208:211], v[116:119]\n"
"ds_write_addtid_b32 v87 offset:16288\n"
"v_mfma_f32_16x16x32_bf16 v[116:119], a[148:151], a[212:215], v[116:119]\n"
"v_add_f32 v93, v93, v97\n"
"v_add_f32 v94, v94, v98\n"
"v_mfma_f32_16x16x32_bf16 v[116:119], a[152:155], a[216:219], v[116:119]\n"
"v_add_f32 v95, v95, v99\n"
"v_add_f32 v92, v92, v100\n"
"v_mfma_f32_16x16x32_bf16 v[116:119], a[156:159], a[220:223], v[116:119]\n"
"v_add_f32 v93, v93, v101\n"
"v_add_f32 v94, v94, v102\n"
"v_mfma_f32_16x16x32_bf16 v[96:99], a[160:163], a[192:195], 0\n"
"v_add_f32 v95, v95, v103\n"
"v_add_f32 v92, v92, v88\n"
"v_mfma_f32_16x16x32_bf16 v[96:99], a[164:167], a[196:199], v[96:99]\n"
"ds_write_addtid_b32 v116 offset:2560\n"
"v_mfma_f32_16x16x32_bf16 v[96:99], a[168:171], a[200:203], v[96:99]\n"
"ds_write_addtid_b32 v117 offset:7200\n"
"v_add_f32 v93, v93, v89\n"
"v_mfma_f32_16x16x32_bf16 v[96:99], a[172:175], a[204:207], v[96:99]\n"
"ds_write_addtid_b32 v118 offset:11904\n"
"v_add_f32 v94, v94, v90\n"
"v_mfma_f32_16x16x32_bf16 v[96:99], a[176:179], a[208:211], v[96:99]\n"
"ds_write_addtid_b32 v119 offset:16544\n"
"v_add_f32 v95, v95, v91\n"
"v_mfma_f32_16x16x32_bf16 v[96:99], a[180:183], a[212:215], v[96:99]\n"
"v_mul_f32_dpp v92, v253, v92 row_newbcast:0 row_mask:0xf bank_mask:0xf\n"
"v_mul_f32_dpp v93, v253, v93 row_newbcast:1 row_mask:0xf bank_mask:0xf\n"
"v_mfma_f32_16x16x32_bf16 v[96:99], a[184:187], a[216:219], v[96:99]\n"
"v_mul_f32_dpp v94, v253, v94 row_newbcast:2 row_mask:0xf bank_mask:0xf\n"
"v_mfma_f32_16x16x32_bf16 v[96:99], a[188:191], a[220:223], v[96:99]\n"
"v_mul_f32_dpp v95, v253, v95 row_newbcast:3 row_mask:0xf bank_mask:0xf\n"
"v_cvt_pk_bf16_f32 v158, v92, v93\n"
"v_mfma_f32_16x16x32_bf16 v[124:127], a[0:3], a[192:195], v[124:127]\n"
"v_cvt_pk_bf16_f32 v159, v94, v95\n"
"v_mfma_f32_16x16x32_bf16 v[124:127], a[4:7], a[196:199], v[124:127]\n"
"ds_write_addtid_b32 v96 offset:2816\n"
"ds_write_addtid_b32 v158 offset:19616\n"
"v_mfma_f32_16x16x32_bf16 v[124:127], a[8:11], a[200:203], v[124:127]\n"
"ds_write_addtid_b32 v97 offset:7456\n"
"ds_write_addtid_b32 v159 offset:19872\n"
"v_mfma_f32_16x16x32_bf16 v[124:127], a[12:15], a[204:207], v[124:127]\n"
"s_waitcnt lgkmcnt(12)\n"
"ds_write_addtid_b32 v98 offset:12160\n"
"v_mfma_f32_16x16x32_bf16 v[124:127], a[16:19], a[208:211], v[124:127]\n"
"ds_write_addtid_b32 v99 offset:16800\n"
"v_add_f32 v104, v104, v108\n"
"v_mfma_f32_16x16x32_bf16 v[124:127], a[20:23], a[212:215], v[124:127]\n"
"v_add_f32 v105, v105, v109\n"
"v_add_f32 v106, v106, v110\n"
"v_mfma_f32_16x16x32_bf16 v[124:127], a[24:27], a[216:219], v[124:127]\n"
"v_add_f32 v107, v107, v111\n"
"v_add_f32 v104, v104, v112\n"
"v_mfma_f32_16x16x32_bf16 v[124:127], a[28:31], a[220:223], v[124:127]\n"
"v_add_f32 v105, v105, v113\n"
"v_add_f32 v106, v106, v114\n"
"v_mfma_f32_16x16x32_bf16 v[132:135], a[32:35], a[192:195], v[132:135]\n"
"v_add_f32 v107, v107, v115\n"
"v_add_f32 v104, v104, v128\n"
"v_mfma_f32_16x16x32_bf16 v[132:135], a[36:39], a[196:199], v[132:135]\n"
"v_add_f32 v105, v105, v129\n"
"v_add_f32 v106, v106, v130\n"
"v_mfma_f32_16x16x32_bf16 v[132:135], a[40:43], a[200:203], v[132:135]\n"
"v_mfma_f32_16x16x32_bf16 v[132:135], a[44:47], a[204:207], v[132:135]\n"
"v_add_f32 v107, v107, v131\n"
"v_mul_f32_dpp v104, v253, v104 row_newbcast:0 row_mask:0xf bank_mask:0xf\n"
"v_mfma_f32_16x16x32_bf16 v[132:135], a[48:51], a[208:211], v[132:135]\n"
"v_mul_f32_dpp v105, v253, v105 row_newbcast:1 row_mask:0xf bank_mask:0xf\n"
"v_mul_f32_dpp v106, v253, v106 row_newbcast:2 row_mask:0xf bank_mask:0xf\n"
"v_mfma_f32_16x16x32_bf16 v[132:135], a[52:55], a[212:215], v[132:135]\n"
"v_mul_f32_dpp v107, v253, v107 row_newbcast:3 row_mask:0xf bank_mask:0xf\n"
"v_cvt_pk_bf16_f32 v166, v104, v105\n"
"v_mfma_f32_16x16x32_bf16 v[132:135], a[56:59], a[216:219], v[132:135]\n"
"s_waitcnt vmcnt(1)\n"
"v_cvt_pk_bf16_f32 v167, v106, v107\n"
"v_mfma_f32_16x16x32_bf16 v[132:135], a[60:63], a[220:223], v[132:135]\n"
"s_waitcnt vmcnt(9)\n"
"ds_write_addtid_b32 v166 offset:20128\n"
"ds_write_addtid_b32 v167 offset:20384\n"
"v_mfma_f32_16x16x32_bf16 v[108:111], a[224:227], a[64:67], 0\n"
"v_mfma_f32_16x16x32_bf16 v[108:111], a[228:231], a[68:71], v[108:111]\n"
"s_waitcnt vmcnt(6)\n"
"v_mfma_f32_16x16x32_bf16 v[108:111], a[232:235], a[72:75], v[108:111]\n"
"v_mfma_f32_16x16x32_bf16 v[108:111], a[236:239], a[76:79], v[108:111]\n"
"s_waitcnt vmcnt(4)\n"
"v_mfma_f32_16x16x32_bf16 v[108:111], a[240:243], a[80:83], v[108:111]\n"
"v_mfma_f32_16x16x32_bf16 v[108:111], a[244:247], a[84:87], v[108:111]\n"
"s_waitcnt vmcnt(2)\n"
"v_mfma_f32_16x16x32_bf16 v[108:111], a[248:251], a[88:91], v[108:111]\n"
"v_mfma_f32_16x16x32_bf16 v[108:111], a[252:255], a[92:95], v[108:111]\n"
"v_mfma_f32_16x16x32_bf16 v[116:119], a[224:227], a[96:99], 0\n"
"v_mfma_f32_16x16x32_bf16 v[116:119], a[228:231], a[100:103], v[116:119]\n"
"ds_write_addtid_b32 v108 offset:3072\n"
"v_mfma_f32_16x16x32_bf16 v[116:119], a[232:235], a[104:107], v[116:119]\n"
"s_waitcnt lgkmcnt(1)\n"
"ds_write_addtid_b32 v109 offset:7712\n"
"v_mfma_f32_16x16x32_bf16 v[116:119], a[236:239], a[108:111], v[116:119]\n"
"s_barrier\n"
"v_mfma_f32_16x16x32_bf16 v[116:119], a[240:243], a[112:115], v[116:119]\n"
"ds_write_addtid_b32 v110 offset:12416\n"
"ds_read_b128 v[128:131], v2 offset:4096\n"
"v_mfma_f32_16x16x32_bf16 v[116:119], a[244:247], a[116:119], v[116:119]\n"
"ds_write_addtid_b32 v111 offset:17056\n"
"v_mfma_f32_16x16x32_bf16 v[116:119], a[248:251], a[120:123], v[116:119]\n"
"ds_read_b128 v[120:123], v3 offset:512\n"
"ds_read_b128 v[136:139], v4 offset:1536\n"
"v_mfma_f32_16x16x32_bf16 v[116:119], a[252:255], a[124:127], v[116:119]\n"
"ds_read_b128 v[140:143], v2 offset:4352\n"
"v_mfma_f32_16x16x32_bf16 v[148:151], a[128:131], a[224:227], 0\n"
"ds_read_b128 v[168:171], v3 offset:768\n"
"ds_read_b128 v[172:175], v4 offset:1792\n"
"v_mfma_f32_16x16x32_bf16 v[148:151], a[132:135], a[228:231], v[148:151]\n"
"ds_write_addtid_b32 v116 offset:3328\n"
"v_mfma_f32_16x16x32_bf16 v[148:151], a[136:139], a[232:235], v[148:151]\n"
"ds_write_addtid_b32 v117 offset:7968\n"
"v_mfma_f32_16x16x32_bf16 v[148:151], a[140:143], a[236:239], v[148:151]\n"
"ds_write_addtid_b32 v118 offset:12672\n"
"v_mfma_f32_16x16x32_bf16 v[148:151], a[144:147], a[240:243], v[148:151]\n"
"s_waitcnt lgkmcnt(6)\n"
"ds_write_addtid_b32 v119 offset:17312\n"
"v_mfma_f32_16x16x32_bf16 v[148:151], a[148:151], a[244:247], v[148:151]\n"
"v_add_f32 v128, v128, v120\n"
"v_add_f32 v129, v129, v121\n"
"v_mfma_f32_16x16x32_bf16 v[148:151], a[152:155], a[248:251], v[148:151]\n"
"v_add_f32 v130, v130, v122\n"
"v_add_f32 v131, v131, v123\n"
"v_mfma_f32_16x16x32_bf16 v[148:151], a[156:159], a[252:255], v[148:151]\n"
"v_add_f32 v128, v128, v136\n"
"v_add_f32 v129, v129, v137\n"
"v_mfma_f32_16x16x32_bf16 v[176:179], a[160:163], a[224:227], 0\n"
"v_add_f32 v130, v130, v138\n"
"v_add_f32 v131, v131, v139\n"
"v_mfma_f32_16x16x32_bf16 v[176:179], a[164:167], a[228:231], v[176:179]\n"
"ds_write_addtid_b32 v148 offset:3584\n"
"v_mfma_f32_16x16x32_bf16 v[176:179], a[168:171], a[232:235], v[176:179]\n"
"ds_write_addtid_b32 v149 offset:8224\n"
"v_add_f32 v128, v128, v124\n"
"v_mfma_f32_16x16x32_bf16 v[176:179], a[172:175], a[236:239], v[176:179]\n"
"ds_write_addtid_b32 v150 offset:12928\n"
"v_add_f32 v129, v129, v125\n"
"v_mfma_f32_16x16x32_bf16 v[176:179], a[176:179], a[240:243], v[176:179]\n"
"ds_write_addtid_b32 v151 offset:17568\n"
"v_add_f32 v130, v130, v126\n"
"v_mfma_f32_16x16x32_bf16 v[176:179], a[180:183], a[244:247], v[176:179]\n"
"v_add_f32 v131, v131, v127\n"
"v_cvt_pk_bf16_f32 v180, v128, v129\n"
"v_mfma_f32_16x16x32_bf16 v[176:179], a[184:187], a[248:251], v[176:179]\n"
"v_cvt_pk_bf16_f32 v181, v130, v131\n"
"v_mfma_f32_16x16x32_bf16 v[176:179], a[188:191], a[252:255], v[176:179]\n"
"ds_read2st64_b32 v[136:137], v11 offset0:0 offset1:1\n"
"ds_read2st64_b32 v[138:139], v11 offset0:2 offset1:3\n"
"v_mfma_f32_16x16x32_bf16 v[128:131], a[224:227], a[0:3], 0\n"
"s_waitcnt lgkmcnt(10)\n"
"v_add_f32 v140, v140, v168\n"
"v_add_f32 v141, v141, v169\n"
"v_mfma_f32_16x16x32_bf16 v[128:131], a[228:231], a[4:7], v[128:131]\n"
"ds_write_addtid_b32 v176 offset:3840\n"
"v_add_f32 v142, v142, v170\n"
"v_mfma_f32_16x16x32_bf16 v[128:131], a[232:235], a[8:11], v[128:131]\n"
"ds_write_addtid_b32 v177 offset:8480\n"
"v_add_f32 v143, v143, v171\n"
"v_mfma_f32_16x16x32_bf16 v[128:131], a[236:239], a[12:15], v[128:131]\n"
"ds_write_addtid_b32 v178 offset:13184\n"
"v_mfma_f32_16x16x32_bf16 v[128:131], a[240:243], a[16:19], v[128:131]\n"
"ds_write_addtid_b32 v179 offset:17824\n"
"v_add_f32 v140, v140, v172\n"
"v_mfma_f32_16x16x32_bf16 v[128:131], a[244:247], a[20:23], v[128:131]\n"
"v_add_f32 v141, v141, v173\n"
"v_add_f32 v142, v142, v174\n"
"v_mfma_f32_16x16x32_bf16 v[128:131], a[248:251], a[24:27], v[128:131]\n"
"v_add_f32 v143, v143, v175\n"
"v_add_f32 v140, v140, v132\n"
"v_mfma_f32_16x16x32_bf16 v[128:131], a[252:255], a[28:31], v[128:131]\n"
"v_add_f32 v141, v141, v133\n"
"v_add_f32 v142, v142, v134\n"
"v_mfma_f32_16x16x32_bf16 v[148:151], a[224:227], a[32:35], 0\n"
"v_mfma_f32_16x16x32_bf16 v[148:151], a[228:231], a[36:39], v[148:151]\n"
"ds_write_addtid_b32 v128 offset:4096\n"
"v_add_f32 v143, v143, v135\n"
"v_mfma_f32_16x16x32_bf16 v[148:151], a[232:235], a[40:43], v[148:151]\n"
"ds_write_addtid_b32 v129 offset:8736\n"
"v_cvt_pk_bf16_f32 v182, v140, v141\n"
"v_mfma_f32_16x16x32_bf16 v[148:151], a[236:239], a[44:47], v[148:151]\n"
"ds_write_addtid_b32 v130 offset:13440\n"
"v_cvt_pk_bf16_f32 v183, v142, v143\n"
"v_mfma_f32_16x16x32_bf16 v[148:151], a[240:243], a[48:51], v[148:151]\n"
"ds_write_addtid_b32 v131 offset:18080\n"
"v_mfma_f32_16x16x32_bf16 v[148:151], a[244:247], a[52:55], v[148:151]\n"
"s_waitcnt lgkmcnt(8)\n"
"v_mfma_f32_16x16x32_bf16 v[172:175], v[136:139], v[180:183], v[20:23]\n"
"v_mfma_f32_16x16x32_bf16 v[148:151], a[248:251], a[56:59], v[148:151]\n"
"v_mfma_f32_16x16x32_bf16 v[148:151], a[252:255], a[60:63], v[148:151]\n"
"s_nop 7\n"
"ds_write_addtid_b32 v148 offset:4352\n"
"ds_write_addtid_b32 v149 offset:8992\n"
"ds_write_addtid_b32 v150 offset:13696\n"
"ds_write_addtid_b32 v151 offset:18336\n"
"s_waitcnt lgkmcnt(0)\n"
"s_barrier\n"
"ds_read2st64_b32 v[144:145], v12 offset0:0 offset1:1\n"
"ds_read2st64_b32 v[146:147], v12 offset0:2 offset1:3\n"
"ds_read2st64_b32 v[156:157], v11 offset0:4 offset1:5\n"
"ds_read2st64_b32 v[164:165], v11 offset0:6 offset1:7\n"
"s_add_u32 s94, s50, 0xfff00000\n"
"s_and_b32 s95, s94, 0xfbffffff\n"
"s_waitcnt lgkmcnt(2)\n"
"v_mfma_f32_16x16x32_bf16 v[148:151], v[144:147], v[180:183], v[16:19]\n"
"s_cmp_eq_u32 s94, s62\n"
"s_cbranch_scc1 .LQ_ft0k7s\n"
".LQ_ft0k7r:\n"
"v_max_f32 v253, v172, v174\n"
"v_max_f32 v252, v173, v175\n"
"s_nop 3\n"
"v_max3_f32 v253, v148, v150, v253\n"
"v_max3_f32 v252, v149, v151, v252\n"
"v_max_f32 v251, v253, v252\n"
"v_mov_b32 v250, v251\n"
"s_nop 1\n"
"v_permlane32_swap_b32 v250, v251\n"
"v_max_f32 v251, v250, v251\n"
"v_mov_b32 v250, v251\n"
"s_nop 1\n"
"v_permlane16_swap_b32 v250, v251\n"
"v_max_f32 v251, v250, v251\n"
"v_mul_f32 v251, v251, v249\n"
"v_fma_f32 v253, v253, v249, -v251\n"
"v_fma_f32 v252, v252, v249, -v251\n"
"v_exp_f32 v253, v253\n"
"v_exp_f32 v252, v252\n"
"s_nop 0\n"
"v_add_f32 v251, v253, v252\n"
"v_mov_b32 v250, v251\n"
"s_nop 1\n"
"v_permlane32_swap_b32 v250, v251\n"
"v_add_f32 v251, v250, v251\n"
"v_mov_b32 v250, v251\n"
"s_nop 1\n"
"v_permlane16_swap_b32 v250, v251\n"
"v_add_f32 v251, v250, v251\n"
"v_rcp_f32 v251, v251\n"
"s_nop 0\n"
"v_mul_f32 v253, v253, v251\n"
"v_mul_f32 v252, v252, v251\n"
"v_cvt_pk_bf16_f32 v250, v253, v252\n"
"s_waitcnt lgkmcnt(0)\n"
"v_and_b32 v68, v250, v24\n"
"v_and_b32 v69, v250, v25\n"
"v_and_b32 v70, v250, v26\n"
"v_and_b32 v71, v250, v27\n"
"s_nop 1\n"
"v_mfma_f32_16x16x32_bf16 v[72:75], v[156:159], v[68:71], 0\n"
"v_mfma_f32_16x16x32_bf16 v[76:79], v[164:167], v[68:71], 0\n"
"s_nop 6\n"
"v_cvt_pk_bf16_f32 v68, v72, v73\n"
"v_cvt_pk_bf16_f32 v69, v74, v75\n"
"v_cvt_pk_bf16_f32 v70, v76, v77\n"
"v_cvt_pk_bf16_f32 v71, v78, v79\n"
"buffer_store_dwordx4 v[68:71], v13, s[40:43], s95 offen sc1\n"
"s_barrier\n"
"s_waitcnt vmcnt(0)\n"
"buffer_store_dwordx2 v[0:1], v14, s[40:43], s94 offen offset:0 sc1\n"
"s_branch .LQ_end\n"
".LQ_t1:\n"
"v_mfma_f32_16x16x32_bf16 v[108:111], a[228:231], a[68:71], v[108:111]\n"
"s_waitcnt vmcnt(15)\n"
"v_mfma_f32_16x16x32_bf16 v[108:111], a[232:235], a[72:75], v[108:111]\n"
"v_mfma_f32_16x16x32_bf16 v[108:111], a[236:239], a[76:79], v[108:111]\n"
"s_waitcnt vmcnt(13)\n"
"v_mfma_f32_16x16x32_bf16 v[108:111], a[240:243], a[80:83], v[108:111]\n"
"v_mfma_f32_16x16x32_bf16 v[108:111], a[244:247], a[84:87], v[108:111]\n"
"s_waitcnt vmcnt(11)\n"
"v_mfma_f32_16x16x32_bf16 v[108:111], a[248:251], a[88:91], v[108:111]\n"
"v_mfma_f32_16x16x32_bf16 v[108:111], a[252:255], a[92:95], v[108:111]\n"
"v_mfma_f32_16x16x32_bf16 v[116:119], a[224:227], a[96:99], 0\n"
"v_mfma_f32_16x16x32_bf16 v[116:119], a[228:231], a[100:103], v[116:119]\n"
"ds_write_addtid_b32 v108 offset:3072\n"
"v_mfma_f32_16x16x32_bf16 v[116:119], a[232:235], a[104:107], v[116:119]\n"
"s_waitcnt lgkmcnt(1)\n"
"ds_write_addtid_b32 v109 offset:7712\n"
"v_mfma_f32_16x16x32_bf16 v[116:119], a[236:239], a[108:111], v[116:119]\n"
"s_barrier\n"
"v_mfma_f32_16x16x32_bf16 v[116:119], a[240:243], a[112:115], v[116:119]\n"
"ds_write_addtid_b32 v110 offset:12416\n"
"ds_read_b128 v[128:131], v2 offset:4096\n"
"v_mfma_f32_16x16x32_bf16 v[116:119], a[244:247], a[116:119], v[116:119]\n"
"ds_write_addtid_b32 v111 offset:17056\n"
"v_mfma_f32_16x16x32_bf16 v[116:119], a[248:251], a[120:123], v[116:119]\n"
"ds_read_b128 v[120:123], v3 offset:512\n"
"ds_read_b128 v[136:139], v4 offset:1536\n"
"v_mfma_f32_16x16x32_bf16 v[116:119], a[252:255], a[124:127], v[116:119]\n"
"ds_read_b128 v[140:143], v2 offset:4352\n"
"v_mfma_f32_16x16x32_bf16 v[148:151], a[128:131], a[224:227], 0\n"
"ds_read_b128 v[168:171], v3 offset:768\n"
"ds_read_b128 v[172:175], v4 offset:1792\n"
"v_mfma_f32_16x16x32_bf16 v[148:151], a[132:135], a[228:231], v[148:151]\n"
"ds_write_addtid_b32 v116 offset:3328\n"
"v_mfma_f32_16x16x32_bf16 v[148:151], a[136:139], a[232:235], v[148:151]\n"
"ds_write_addtid_b32 v117 offset:7968\n"
"v_mfma_f32_16x16x32_bf16 v[148:151], a[140:143], a[236:239], v[148:151]\n"
"ds_write_addtid_b32 v118 offset:12672\n"
"v_mfma_f32_16x16x32_bf16 v[148:151], a[144:147], a[240:243], v[148:151]\n"
"s_waitcnt lgkmcnt(6)\n"
"ds_write_addtid_b32 v119 offset:17312\n"
"v_mfma_f32_16x16x32_bf16 v[148:151], a[148:151], a[244:247], v[148:151]\n"
"v_add_f32 v128, v128, v120\n"
"v_add_f32 v129, v129, v121\n"
"v_mfma_f32_16x16x32_bf16 v[148:151], a[152:155], a[248:251], v[148:151]\n"
"v_add_f32 v130, v130, v122\n"
"v_add_f32 v131, v131, v123\n"
"v_mfma_f32_16x16x32_bf16 v[148:151], a[156:159], a[252:255], v[148:151]\n"
"v_add_f32 v128, v128, v136\n"
"v_add_f32 v129, v129, v137\n"
"v_mfma_f32_16x16x32_bf16 v[176:179], a[160:163], a[224:227], 0\n"
"v_add_f32 v130, v130, v138\n"
"v_add_f32 v131, v131, v139\n"
"v_mfma_f32_16x16x32_bf16 v[176:179], a[164:167], a[228:231], v[176:179]\n"
"ds_write_addtid_b32 v148 offset:3584\n"
"v_mfma_f32_16x16x32_bf16 v[176:179], a[168:171], a[232:235], v[176:179]\n"
"ds_write_addtid_b32 v149 offset:8224\n"
"v_add_f32 v128, v128, v124\n"
"v_mfma_f32_16x16x32_bf16 v[176:179], a[172:175], a[236:239], v[176:179]\n"
"ds_write_addtid_b32 v150 offset:12928\n"
"v_add_f32 v129, v129, v125\n"
"v_mfma_f32_16x16x32_bf16 v[176:179], a[176:179], a[240:243], v[176:179]\n"
"ds_write_addtid_b32 v151 offset:17568\n"
"v_add_f32 v130, v130, v126\n"
"v_mfma_f32_16x16x32_bf16 v[176:179], a[180:183], a[244:247], v[176:179]\n"
"v_add_f32 v131, v131, v127\n"
"v_cvt_pk_bf16_f32 v180, v128, v129\n"
"v_mfma_f32_16x16x32_bf16 v[176:179], a[184:187], a[248:251], v[176:179]\n"
"v_cvt_pk_bf16_f32 v181, v130, v131\n"
"v_mfma_f32_16x16x32_bf16 v[176:179], a[188:191], a[252:255], v[176:179]\n"
"ds_read2st64_b32 v[136:137], v11 offset0:0 offset1:1\n"
"ds_read2st64_b32 v[138:139], v11 offset0:2 offset1:3\n"
"v_mfma_f32_16x16x32_bf16 v[128:131], a[224:227], a[0:3], 0\n"
"s_waitcnt lgkmcnt(10)\n"
"v_add_f32 v140, v140, v168\n"
"v_add_f32 v141, v141, v169\n"
"v_mfma_f32_16x16x32_bf16 v[128:131], a[228:231], a[4:7], v[128:131]\n"
"ds_write_addtid_b32 v176 offset:3840\n"
"v_add_f32 v142, v142, v170\n"
"v_mfma_f32_16x16x32_bf16 v[128:131], a[232:235], a[8:11], v[128:131]\n"
"ds_write_addtid_b32 v177 offset:8480\n"
"v_add_f32 v143, v143, v171\n"
"v_mfma_f32_16x16x32_bf16 v[128:131], a[236:239], a[12:15], v[128:131]\n"
"ds_write_addtid_b32 v178 offset:13184\n"
"v_mfma_f32_16x16x32_bf16 v[128:131], a[240:243], a[16:19], v[128:131]\n"
"ds_write_addtid_b32 v179 offset:17824\n"
"v_add_f32 v140, v140, v172\n"
"v_mfma_f32_16x16x32_bf16 v[128:131], a[244:247], a[20:23], v[128:131]\n"
"v_add_f32 v141, v141, v173\n"
"v_add_f32 v142, v142, v174\n"
"v_mfma_f32_16x16x32_bf16 v[128:131], a[248:251], a[24:27], v[128:131]\n"
"v_add_f32 v143, v143, v175\n"
"v_add_f32 v140, v140, v132\n"
"v_mfma_f32_16x16x32_bf16 v[128:131], a[252:255], a[28:31], v[128:131]\n"
"v_add_f32 v141, v141, v133\n"
"v_add_f32 v142, v142, v134\n"
"v_mfma_f32_16x16x32_bf16 v[148:151], a[224:227], a[32:35], 0\n"
"v_mfma_f32_16x16x32_bf16 v[148:151], a[228:231], a[36:39], v[148:151]\n"
"ds_write_addtid_b32 v128 offset:4096\n"
"v_add_f32 v143, v143, v135\n"
"v_mfma_f32_16x16x32_bf16 v[148:151], a[232:235], a[40:43], v[148:151]\n"
"ds_write_addtid_b32 v129 offset:8736\n"
"v_cvt_pk_bf16_f32 v182, v140, v141\n"
"v_mfma_f32_16x16x32_bf16 v[148:151], a[236:239], a[44:47], v[148:151]\n"
"ds_write_addtid_b32 v130 offset:13440\n"
"v_cvt_pk_bf16_f32 v183, v142, v143\n"
"v_mfma_f32_16x16x32_bf16 v[148:151], a[240:243], a[48:51], v[148:151]\n"
"s_waitcnt vmcnt(0)\n"
"ds_write_addtid_b32 v131 offset:18080\n"
"v_mfma_f32_16x16x32_bf16 v[148:151], a[244:247], a[52:55], v[148:151]\n"
"s_waitcnt lgkmcnt(8)\n"
"v_mfma_f32_16x16x32_bf16 v[172:175], v[136:139], v[180:183], v[20:23]\n"
"v_mfma_f32_16x16x32_bf16 v[148:151], a[248:251], a[56:59], v[148:151]\n"
"v_mfma_f32_16x16x32_bf16 v[148:151], a[252:255], a[60:63], v[148:151]\n"
"s_waitcnt vmcnt(8)\n"
"v_mfma_f32_16x16x32_bf16 v[128:131], a[128:131], v[64:67], 0\n"
"v_mfma_f32_16x16x32_bf16 v[128:131], a[132:135], v[68:71], v[128:131]\n"
"s_waitcnt vmcnt(5)\n"
"ds_write_addtid_b32 v148 offset:4352\n"
"v_mfma_f32_16x16x32_bf16 v[128:131], a[136:139], v[72:75], v[128:131]\n"
"ds_write_addtid_b32 v149 offset:8992\n"
"v_mfma_f32_16x16x32_bf16 v[128:131], a[140:143], v[76:79], v[128:131]\n"
"s_waitcnt vmcnt(3)\n"
"ds_write_addtid_b32 v150 offset:13696\n"
"v_mfma_f32_16x16x32_bf16 v[128:131], a[144:147], v[80:83], v[128:131]\n"
"ds_write_addtid_b32 v151 offset:18336\n"
"v_mov_b32 v254, v144\n"
"v_mfma_f32_16x16x32_bf16 v[128:131], a[148:151], v[84:87], v[128:131]\n"
"s_waitcnt vmcnt(1)\n"
"v_mul_f32 v135, v145, v39\n"
"v_mfma_f32_16x16x32_bf16 v[128:131], a[152:155], v[88:91], v[128:131]\n"
"v_mul_f32 v133, v145, v37\n"
"v_mul_f32 v134, v145, v38\n"
"v_mfma_f32_16x16x32_bf16 v[128:131], a[156:159], v[92:95], v[128:131]\n"
"v_mul_f32 v139, v145, v43\n"
"v_mul_f32 v132, v145, v36\n"
"v_mfma_f32_16x16x32_bf16 v[140:143], a[160:163], v[64:67], 0\n"
"v_mul_f32 v137, v145, v41\n"
"v_mul_f32 v138, v145, v42\n"
"v_mfma_f32_16x16x32_bf16 v[140:143], a[164:167], v[68:71], v[140:143]\n"
"ds_write_addtid_b32 v128 offset:0\n"
"v_mul_f32 v136, v145, v40\n"
"v_mfma_f32_16x16x32_bf16 v[140:143], a[168:171], v[72:75], v[140:143]\n"
"s_waitcnt lgkmcnt(1)\n"
"ds_write_addtid_b32 v129 offset:4640\n"
"v_mfma_f32_16x16x32_bf16 v[140:143], a[172:175], v[76:79], v[140:143]\n"
"s_barrier\n"
"v_mfma_f32_16x16x32_bf16 v[140:143], a[176:179], v[80:83], v[140:143]\n"
"ds_write_addtid_b32 v130 offset:9344\n"
"ds_read2st64_b32 v[144:145], v12 offset0:0 offset1:1\n"
"v_mfma_f32_16x16x32_bf16 v[140:143], a[180:183], v[84:87], v[140:143]\n"
"ds_write_addtid_b32 v131 offset:13984\n"
"ds_read2st64_b32 v[146:147], v12 offset0:2 offset1:3\n"
"v_mfma_f32_16x16x32_bf16 v[140:143], a[184:187], v[88:91], v[140:143]\n"
"ds_read2st64_b32 v[156:157], v11 offset0:4 offset1:5\n"
"ds_read2st64_b32 v[164:165], v11 offset0:6 offset1:7\n"
"v_mfma_f32_16x16x32_bf16 v[140:143], a[188:191], v[92:95], v[140:143]\n"
"s_add_u32 s94, s50, 0xffe00000\n"
"s_and_b32 s95, s94, 0xfbffffff\n"
"v_mfma_f32_16x16x32_bf16 v[128:131], v[64:67], a[0:3], 0\n"
"v_mfma_f32_16x16x32_bf16 v[128:131], v[68:71], a[4:7], v[128:131]\n"
"ds_write_addtid_b32 v140 offset:256\n"
"v_mfma_f32_16x16x32_bf16 v[128:131], v[72:75], a[8:11], v[128:131]\n"
"s_waitcnt lgkmcnt(3)\n"
"ds_write_addtid_b32 v141 offset:4896\n"
"v_mfma_f32_16x16x32_bf16 v[148:151], v[144:147], v[180:183], v[16:19]\n"
"s_cmp_eq_u32 s94, s62\n"
"s_cbranch_scc1 .LQ_ft1k7s\n"
".LQ_ft1k7r:\n"
"v_mfma_f32_16x16x32_bf16 v[128:131], v[76:79], a[12:15], v[128:131]\n"
"ds_write_addtid_b32 v142 offset:9600\n"
"v_max_f32 v253, v172, v174\n"
"v_mfma_f32_16x16x32_bf16 v[128:131], v[80:83], a[16:19], v[128:131]\n"
"ds_write_addtid_b32 v143 offset:14240\n"
"v_mfma_f32_16x16x32_bf16 v[128:131], v[84:87], a[20:23], v[128:131]\n"
"v_max_f32 v252, v173, v175\n"
"v_max3_f32 v253, v148, v150, v253\n"
"v_mfma_f32_16x16x32_bf16 v[128:131], v[88:91], a[24:27], v[128:131]\n"
"v_max3_f32 v252, v149, v151, v252\n"
"v_max_f32 v251, v253, v252\n"
"v_mfma_f32_16x16x32_bf16 v[128:131], v[92:95], a[28:31], v[128:131]\n"
"v_mov_b32 v250, v251\n"
"v_mfma_f32_16x16x32_bf16 v[140:143], v[64:67], a[32:35], 0\n"
"v_permlane32_swap_b32 v250, v251\n"
"v_mfma_f32_16x16x32_bf16 v[140:143], v[68:71], a[36:39], v[140:143]\n"
"ds_write_addtid_b32 v128 offset:512\n"
"v_mfma_f32_16x16x32_bf16 v[140:143], v[72:75], a[40:43], v[140:143]\n"
"ds_write_addtid_b32 v129 offset:5152\n"
"v_max_f32 v251, v250, v251\n"
"v_mfma_f32_16x16x32_bf16 v[140:143], v[76:79], a[44:47], v[140:143]\n"
"ds_write_addtid_b32 v130 offset:9856\n"
"v_mov_b32 v250, v251\n"
"v_mfma_f32_16x16x32_bf16 v[140:143], v[80:83], a[48:51], v[140:143]\n"
"ds_write_addtid_b32 v131 offset:14496\n"
"v_mfma_f32_16x16x32_bf16 v[140:143], v[84:87], a[52:55], v[140:143]\n"
"v_permlane16_swap_b32 v250, v251\n"
"v_mfma_f32_16x16x32_bf16 v[140:143], v[88:91], a[56:59], v[140:143]\n"
"v_max_f32 v251, v250, v251\n"
"v_mfma_f32_16x16x32_bf16 v[140:143], v[92:95], a[60:63], v[140:143]\n"
"v_mul_f32 v251, v251, v249\n"
"v_fma_f32 v253, v253, v249, -v251\n"
"v_mfma_f32_16x16x32_bf16 v[132:135], a[64:67], v[64:67], v[132:135]\n"
"v_fma_f32 v252, v252, v249, -v251\n"
"v_mfma_f32_16x16x32_bf16 v[132:135], a[68:71], v[68:71], v[132:135]\n"
"ds_write_addtid_b32 v140 offset:768\n"
"v_mfma_f32_16x16x32_bf16 v[132:135], a[72:75], v[72:75], v[132:135]\n"
"ds_write_addtid_b32 v141 offset:5408\n"
"v_mfma_f32_16x16x32_bf16 v[132:135], a[76:79], v[76:79], v[132:135]\n"
"ds_write_addtid_b32 v142 offset:10112\n"
"v_mfma_f32_16x16x32_bf16 v[132:135], a[80:83], v[80:83], v[132:135]\n"
"ds_write_addtid_b32 v143 offset:14752\n"
"v_mfma_f32_16x16x32_bf16 v[132:135], a[84:87], v[84:87], v[132:135]\n"
"v_exp_f32 v253, v253\n"
"v_mfma_f32_16x16x32_bf16 v[132:135], a[88:91], v[88:91], v[132:135]\n"
"v_exp_f32 v252, v252\n"
"v_mfma_f32_16x16x32_bf16 v[132:135], a[92:95], v[92:95], v[132:135]\n"
"v_add_f32 v251, v253, v252\n"
"v_mov_b32 v250, v251\n"
"v_mfma_f32_16x16x32_bf16 v[136:139], a[96:99], v[64:67], v[136:139]\n"
"v_permlane32_swap_b32 v250, v251\n"
"v_mfma_f32_16x16x32_bf16 v[136:139], a[100:103], v[68:71], v[136:139]\n"
"v_add_f32 v251, v250, v251\n"
"v_mov_b32 v250, v251\n"
"v_mfma_f32_16x16x32_bf16 v[136:139], a[104:107], v[72:75], v[136:139]\n"
"v_permlane16_swap_b32 v250, v251\n"
"v_mfma_f32_16x16x32_bf16 v[136:139], a[108:111], v[76:79], v[136:139]\n"
"v_add_f32 v251, v250, v251\n"
"v_mfma_f32_16x16x32_bf16 v[136:139], a[112:115], v[80:83], v[136:139]\n"
"v_mfma_f32_16x16x32_bf16 v[136:139], a[116:119], v[84:87], v[136:139]\n"
"v_mfma_f32_16x16x32_bf16 v[136:139], a[120:123], v[88:91], v[136:139]\n"
"v_rcp_f32 v251, v251\n"
"v_mfma_f32_16x16x32_bf16 v[136:139], a[124:127], v[92:95], v[136:139]\n"
"v_mul_f32 v253, v253, v251\n"
"v_mul_f32 v252, v252, v251\n"
"v_cvt_pk_bf16_f32 v250, v253, v252\n"
"s_waitcnt lgkmcnt(12)\n"
"v_and_b32 v68, v250, v24\n"
"v_and_b32 v69, v250, v25\n"
"v_and_b32 v70, v250, v26\n"
"v_and_b32 v71, v250, v27\n"
"s_nop 1\n"
"v_mfma_f32_16x16x32_bf16 v[72:75], v[156:159], v[68:71], 0\n"
"v_mfma_f32_16x16x32_bf16 v[76:79], v[164:167], v[68:71], 0\n"
"s_nop 6\n"
"v_cvt_pk_bf16_f32 v68, v72, v73\n"
"v_cvt_pk_bf16_f32 v69, v74, v75\n"
"v_cvt_pk_bf16_f32 v70, v76, v77\n"
"v_cvt_pk_bf16_f32 v71, v78, v79\n"
"buffer_store_dwordx4 v[68:71], v13, s[40:43], s95 offen sc1\n"
"s_waitcnt lgkmcnt(0)\n"
"s_barrier\n"
"ds_read_b128 v[72:75], v2 offset:1024\n"
"ds_read_b128 v[76:79], v3 offset:2048\n"
"ds_read_b128 v[80:83], v4 offset:3072\n"
"ds_read_b128 v[84:87], v2 offset:1280\n"
"ds_read_b128 v[64:67], v3 offset:2304\n"
"ds_read_b128 v[92:95], v4 offset:3328\n"
"s_waitcnt lgkmcnt(3)\n"
"v_add_f32 v72, v72, v76\n"
"v_add_f32 v73, v73, v77\n"
"v_add_f32 v74, v74, v78\n"
"v_add_f32 v75, v75, v79\n"
"v_add_f32 v72, v72, v80\n"
"v_add_f32 v73, v73, v81\n"
"v_add_f32 v74, v74, v82\n"
"v_add_f32 v75, v75, v83\n"
"v_add_f32 v72, v72, v132\n"
"v_add_f32 v73, v73, v133\n"
"v_add_f32 v74, v74, v134\n"
"v_add_f32 v75, v75, v135\n"
"v_mul_f32 v72, v254, v72\n"
"v_mul_f32 v73, v254, v73\n"
"v_mul_f32 v74, v254, v74\n"
"v_mul_f32 v75, v254, v75\n"
"v_cvt_pk_bf16_f32 v76, v72, v73\n"
"v_cvt_pk_bf16_f32 v77, v74, v75\n"
"ds_write_addtid_b32 v76 offset:18592\n"
"ds_write_addtid_b32 v77 offset:18848\n"
"s_waitcnt lgkmcnt(2)\n"
"v_add_f32 v84, v84, v64\n"
"v_add_f32 v85, v85, v65\n"
"v_add_f32 v86, v86, v66\n"
"v_add_f32 v87, v87, v67\n"
"v_add_f32 v84, v84, v92\n"
"v_add_f32 v85, v85, v93\n"
"v_add_f32 v86, v86, v94\n"
"v_add_f32 v87, v87, v95\n"
"v_add_f32 v84, v84, v136\n"
"v_add_f32 v85, v85, v137\n"
"v_add_f32 v86, v86, v138\n"
"v_add_f32 v87, v87, v139\n"
"v_mul_f32 v84, v254, v84\n"
"v_mul_f32 v85, v254, v85\n"
"v_mul_f32 v86, v254, v86\n"
"v_mul_f32 v87, v254, v87\n"
"v_cvt_pk_bf16_f32 v78, v84, v85\n"
"v_cvt_pk_bf16_f32 v79, v86, v87\n"
"ds_write_addtid_b32 v78 offset:19104\n"
"ds_write_addtid_b32 v79 offset:19360\n"
"s_waitcnt vmcnt(0)\n"
"buffer_store_dwordx2 v[0:1], v14, s[40:43], s94 offen offset:0 sc1\n"
"s_waitcnt lgkmcnt(0)\n"
"s_barrier\n"
"s_branch .LQ_end\n"
".LQ_t2:\n"
"v_mfma_f32_16x16x32_bf16 v[148:151], a[252:255], a[60:63], v[148:151]\n"
"s_waitcnt vmcnt(17)\n"
"v_mfma_f32_16x16x32_bf16 v[128:131], a[128:131], v[64:67], 0\n"
"v_mfma_f32_16x16x32_bf16 v[128:131], a[132:135], v[68:71], v[128:131]\n"
"s_waitcnt vmcnt(14)\n"
"ds_write_addtid_b32 v148 offset:4352\n"
"v_mfma_f32_16x16x32_bf16 v[128:131], a[136:139], v[72:75], v[128:131]\n"
"ds_write_addtid_b32 v149 offset:8992\n"
"v_mfma_f32_16x16x32_bf16 v[128:131], a[140:143], v[76:79], v[128:131]\n"
"s_waitcnt vmcnt(12)\n"
"ds_write_addtid_b32 v150 offset:13696\n"
"v_mfma_f32_16x16x32_bf16 v[128:131], a[144:147], v[80:83], v[128:131]\n"
"ds_write_addtid_b32 v151 offset:18336\n"
"v_mov_b32 v254, v144\n"
"v_mfma_f32_16x16x32_bf16 v[128:131], a[148:151], v[84:87], v[128:131]\n"
"s_waitcnt vmcnt(10)\n"
"v_mul_f32 v135, v145, v39\n"
"v_mfma_f32_16x16x32_bf16 v[128:131], a[152:155], v[88:91], v[128:131]\n"
"v_mul_f32 v133, v145, v37\n"
"v_mul_f32 v134, v145, v38\n"
"v_mfma_f32_16x16x32_bf16 v[128:131], a[156:159], v[92:95], v[128:131]\n"
"v_mul_f32 v139, v145, v43\n"
"v_mul_f32 v132, v145, v36\n"
"v_mfma_f32_16x16x32_bf16 v[140:143], a[160:163], v[64:67], 0\n"
"v_mul_f32 v137, v145, v41\n"
"v_mul_f32 v138, v145, v42\n"
"v_mfma_f32_16x16x32_bf16 v[140:143], a[164:167], v[68:71], v[140:143]\n"
"ds_write_addtid_b32 v128 offset:0\n"
"v_mul_f32 v136, v145, v40\n"
"v_mfma_f32_16x16x32_bf16 v[140:143], a[168:171], v[72:75], v[140:143]\n"
"s_waitcnt lgkmcnt(1)\n"
"ds_write_addtid_b32 v129 offset:4640\n"
"v_mfma_f32_16x16x32_bf16 v[140:143], a[172:175], v[76:79], v[140:143]\n"
"s_barrier\n"
"v_mfma_f32_16x16x32_bf16 v[140:143], a[176:179], v[80:83], v[140:143]\n"
"ds_write_addtid_b32 v130 offset:9344\n"
"ds_read2st64_b32 v[144:145], v12 offset0:0 offset1:1\n"
"v_mfma_f32_16x16x32_bf16 v[140:143], a[180:183], v[84:87], v[140:143]\n"
"ds_write_addtid_b32 v131 offset:13984\n"
"ds_read2st64_b32 v[146:147], v12 offset0:2 offset1:3\n"
"v_mfma_f32_16x16x32_bf16 v[140:143], a[184:187], v[88:91], v[140:143]\n"
"ds_read2st64_b32 v[156:157], v11 offset0:4 offset1:5\n"
"ds_read2st64_b32 v[164:165], v11 offset0:6 offset1:7\n"
"v_mfma_f32_16x16x32_bf16 v[140:143], a[188:191], v[92:95], v[140:143]\n"
"s_add_u32 s94, s50, 0xffd00000\n"
"s_and_b32 s95, s94, 0xfbffffff\n"
"v_mfma_f32_16x16x32_bf16 v[128:131], v[64:67], a[0:3], 0\n"
"v_mfma_f32_16x16x32_bf16 v[128:131], v[68:71], a[4:7], v[128:131]\n"
"ds_write_addtid_b32 v140 offset:256\n"
"v_mfma_f32_16x16x32_bf16 v[128:131], v[72:75], a[8:11], v[128:131]\n"
"s_waitcnt lgkmcnt(3)\n"
"ds_write_addtid_b32 v141 offset:4896\n"
"v_mfma_f32_16x16x32_bf16 v[148:151], v[144:147], v[180:183], v[16:19]\n"
"s_cmp_eq_u32 s94, s62\n"
"s_cbranch_scc1 .LQ_ft2k7s\n"
".LQ_ft2k7r:\n"
"v_mfma_f32_16x16x32_bf16 v[128:131], v[76:79], a[12:15], v[128:131]\n"
"ds_write_addtid_b32 v142 offset:9600\n"
"v_max_f32 v253, v172, v174\n"
"v_mfma_f32_16x16x32_bf16 v[128:131], v[80:83], a[16:19], v[128:131]\n"
"ds_write_addtid_b32 v143 offset:14240\n"
"v_mfma_f32_16x16x32_bf16 v[128:131], v[84:87], a[20:23], v[128:131]\n"
"v_max_f32 v252, v173, v175\n"
"v_max3_f32 v253, v148, v150, v253\n"
"v_mfma_f32_16x16x32_bf16 v[128:131], v[88:91], a[24:27], v[128:131]\n"
"v_max3_f32 v252, v149, v151, v252\n"
"v_max_f32 v251, v253, v252\n"
"v_mfma_f32_16x16x32_bf16 v[128:131], v[92:95], a[28:31], v[128:131]\n"
"v_mov_b32 v250, v251\n"
"v_mfma_f32_16x16x32_bf16 v[140:143], v[64:67], a[32:35], 0\n"
"v_permlane32_swap_b32 v250, v251\n"
"v_mfma_f32_16x16x32_bf16 v[140:143], v[68:71], a[36:39], v[140:143]\n"
"ds_write_addtid_b32 v128 offset:512\n"
"v_mfma_f32_16x16x32_bf16 v[140:143], v[72:75], a[40:43], v[140:143]\n"
"ds_write_addtid_b32 v129 offset:5152\n"
"v_max_f32 v251, v250, v251\n"
"v_mfma_f32_16x16x32_bf16 v[140:143], v[76:79], a[44:47], v[140:143]\n"
"ds_write_addtid_b32 v130 offset:9856\n"
"v_mov_b32 v250, v251\n"
"v_mfma_f32_16x16x32_bf16 v[140:143], v[80:83], a[48:51], v[140:143]\n"
"ds_write_addtid_b32 v131 offset:14496\n"
"v_mfma_f32_16x16x32_bf16 v[140:143], v[84:87], a[52:55], v[140:143]\n"
"v_permlane16_swap_b32 v250, v251\n"
"v_mfma_f32_16x16x32_bf16 v[140:143], v[88:91], a[56:59], v[140:143]\n"
"v_max_f32 v251, v250, v251\n"
"v_mfma_f32_16x16x32_bf16 v[140:143], v[92:95], a[60:63], v[140:143]\n"
"v_mul_f32 v251, v251, v249\n"
"v_fma_f32 v253, v253, v249, -v251\n"
"v_mfma_f32_16x16x32_bf16 v[132:135], a[64:67], v[64:67], v[132:135]\n"
"v_fma_f32 v252, v252, v249, -v251\n"
"v_mfma_f32_16x16x32_bf16 v[132:135], a[68:71], v[68:71], v[132:135]\n"
"ds_write_addtid_b32 v140 offset:768\n"
"v_mfma_f32_16x16x32_bf16 v[132:135], a[72:75], v[72:75], v[132:135]\n"
"ds_write_addtid_b32 v141 offset:5408\n"
"v_mfma_f32_16x16x32_bf16 v[132:135], a[76:79], v[76:79], v[132:135]\n"
"ds_write_addtid_b32 v142 offset:10112\n"
"v_mfma_f32_16x16x32_bf16 v[132:135], a[80:83], v[80:83], v[132:135]\n"
"ds_write_addtid_b32 v143 offset:14752\n"
"v_mfma_f32_16x16x32_bf16 v[132:135], a[84:87], v[84:87], v[132:135]\n"
"v_exp_f32 v253, v253\n"
"v_mfma_f32_16x16x32_bf16 v[132:135], a[88:91], v[88:91], v[132:135]\n"
"v_exp_f32 v252, v252\n"
"v_mfma_f32_16x16x32_bf16 v[132:135], a[92:95], v[92:95], v[132:135]\n"
"v_add_f32 v251, v253, v252\n"
"v_mov_b32 v250, v251\n"
"v_mfma_f32_16x16x32_bf16 v[136:139], a[96:99], v[64:67], v[136:139]\n"
"v_permlane32_swap_b32 v250, v251\n"
"v_mfma_f32_16x16x32_bf16 v[136:139], a[100:103], v[68:71], v[136:139]\n"
"v_add_f32 v251, v250, v251\n"
"v_mov_b32 v250, v251\n"
"v_mfma_f32_16x16x32_bf16 v[136:139], a[104:107], v[72:75], v[136:139]\n"
"v_permlane16_swap_b32 v250, v251\n"
"v_mfma_f32_16x16x32_bf16 v[136:139], a[108:111], v[76:79], v[136:139]\n"
"s_waitcnt vmcnt(0)\n"
"v_add_f32 v251, v250, v251\n"
"v_mfma_f32_16x16x32_bf16 v[136:139], a[112:115], v[80:83], v[136:139]\n"
"v_mfma_f32_16x16x32_bf16 v[136:139], a[116:119], v[84:87], v[136:139]\n"
"v_mfma_f32_16x16x32_bf16 v[136:139], a[120:123], v[88:91], v[136:139]\n"
"v_rcp_f32 v251, v251\n"
"v_mfma_f32_16x16x32_bf16 v[136:139], a[124:127], v[92:95], v[136:139]\n"
"s_waitcnt vmcnt(7)\n"
"v_mul_f32 v253, v253, v251\n"
"v_mfma_f32_16x16x32_bf16 v[64:67], v[96:99], a[64:67], 0\n"
"v_mul_f32 v252, v252, v251\n"
"v_cvt_pk_bf16_f32 v250, v253, v252\n"
"v_mfma_f32_16x16x32_bf16 v[64:67], v[100:103], a[68:71], v[64:67]\n"
"s_waitcnt vmcnt(5)\n"
"v_mfma_f32_16x16x32_bf16 v[64:67], v[104:107], a[72:75], v[64:67]\n"
"s_waitcnt lgkmcnt(12)\n"
"v_and_b32 v68, v250, v24\n"
"v_and_b32 v69, v250, v25\n"
"v_mfma_f32_16x16x32_bf16 v[64:67], v[108:111], a[76:79], v[64:67]\n"
"s_waitcnt vmcnt(3)\n"
"v_and_b32 v70, v250, v26\n"
"v_and_b32 v71, v250, v27\n"
"v_mfma_f32_16x16x32_bf16 v[64:67], v[112:115], a[80:83], v[64:67]\n"
"v_mfma_f32_16x16x32_bf16 v[72:75], v[156:159], v[68:71], 0\n"
"v_mul_f32_dpp v91, v153, v44 row_newbcast:3 row_mask:0xf bank_mask:0xf\n"
"v_mov_b32 v253, v152\n"
"v_mfma_f32_16x16x32_bf16 v[76:79], v[164:167], v[68:71], 0\n"
"v_mul_f32_dpp v89, v153, v44 row_newbcast:1 row_mask:0xf bank_mask:0xf\n"
"v_mul_f32_dpp v90, v153, v44 row_newbcast:2 row_mask:0xf bank_mask:0xf\n"
"v_mfma_f32_16x16x32_bf16 v[64:67], v[116:119], a[84:87], v[64:67]\n"
"s_waitcnt vmcnt(1)\n"
"v_cvt_pk_bf16_f32 v68, v72, v73\n"
"v_cvt_pk_bf16_f32 v69, v74, v75\n"
"v_mfma_f32_16x16x32_bf16 v[64:67], v[120:123], a[88:91], v[64:67]\n"
"v_cvt_pk_bf16_f32 v70, v76, v77\n"
"v_cvt_pk_bf16_f32 v71, v78, v79\n"
"v_mfma_f32_16x16x32_bf16 v[64:67], v[124:127], a[92:95], v[64:67]\n"
"buffer_store_dwordx4 v[68:71], v13, s[40:43], s95 offen sc1\n"
"v_mul_f32_dpp v130, v153, v45 row_newbcast:2 row_mask:0xf bank_mask:0xf\n"
"v_mul_f32_dpp v131, v153, v45 row_newbcast:3 row_mask:0xf bank_mask:0xf\n"
"v_mul_f32_dpp v88, v153, v44 row_newbcast:0 row_mask:0xf bank_mask:0xf\n"
"v_mfma_f32_16x16x32_bf16 v[68:71], v[96:99], a[96:99], 0\n"
"s_waitcnt lgkmcnt(0)\n"
"v_mul_f32_dpp v128, v153, v45 row_newbcast:0 row_mask:0xf bank_mask:0xf\n"
"v_mul_f32_dpp v129, v153, v45 row_newbcast:1 row_mask:0xf bank_mask:0xf\n"
"v_mfma_f32_16x16x32_bf16 v[68:71], v[100:103], a[100:103], v[68:71]\n"
"s_barrier\n"
"v_mfma_f32_16x16x32_bf16 v[68:71], v[104:107], a[104:107], v[68:71]\n"
"ds_write_addtid_b32 v64 offset:1024\n"
"ds_read_b128 v[72:75], v2 offset:1024\n"
"v_mfma_f32_16x16x32_bf16 v[68:71], v[108:111], a[108:111], v[68:71]\n"
"ds_write_addtid_b32 v65 offset:5664\n"
"ds_read_b128 v[76:79], v3 offset:2048\n"
"v_mfma_f32_16x16x32_bf16 v[68:71], v[112:115], a[112:115], v[68:71]\n"
"ds_write_addtid_b32 v66 offset:10368\n"
"v_mfma_f32_16x16x32_bf16 v[68:71], v[116:119], a[116:119], v[68:71]\n"
"ds_write_addtid_b32 v67 offset:15008\n"
"ds_read_b128 v[80:83], v4 offset:3072\n"
"v_mfma_f32_16x16x32_bf16 v[68:71], v[120:123], a[120:123], v[68:71]\n"
"ds_read_b128 v[84:87], v2 offset:1280\n"
"ds_read_b128 v[64:67], v3 offset:2304\n"
"v_mfma_f32_16x16x32_bf16 v[68:71], v[124:127], a[124:127], v[68:71]\n"
"ds_read_b128 v[92:95], v4 offset:3328\n"
"v_mfma_f32_16x16x32_bf16 v[140:143], v[96:99], a[0:3], 0\n"
"v_mfma_f32_16x16x32_bf16 v[140:143], v[100:103], a[4:7], v[140:143]\n"
"ds_write_addtid_b32 v68 offset:1280\n"
"v_mfma_f32_16x16x32_bf16 v[140:143], v[104:107], a[8:11], v[140:143]\n"
"ds_write_addtid_b32 v69 offset:5920\n"
"v_mfma_f32_16x16x32_bf16 v[140:143], v[108:111], a[12:15], v[140:143]\n"
"s_waitcnt lgkmcnt(5)\n"
"ds_write_addtid_b32 v70 offset:10624\n"
"v_add_f32 v72, v72, v76\n"
"v_mfma_f32_16x16x32_bf16 v[140:143], v[112:115], a[16:19], v[140:143]\n"
"ds_write_addtid_b32 v71 offset:15264\n"
"v_add_f32 v73, v73, v77\n"
"v_mfma_f32_16x16x32_bf16 v[140:143], v[116:119], a[20:23], v[140:143]\n"
"v_add_f32 v74, v74, v78\n"
"v_mfma_f32_16x16x32_bf16 v[140:143], v[120:123], a[24:27], v[140:143]\n"
"v_add_f32 v75, v75, v79\n"
"v_add_f32 v72, v72, v80\n"
"v_mfma_f32_16x16x32_bf16 v[140:143], v[124:127], a[28:31], v[140:143]\n"
"v_add_f32 v73, v73, v81\n"
"v_add_f32 v74, v74, v82\n"
"v_mfma_f32_16x16x32_bf16 v[68:71], v[96:99], a[32:35], 0\n"
"v_add_f32 v75, v75, v83\n"
"v_add_f32 v72, v72, v132\n"
"v_mfma_f32_16x16x32_bf16 v[68:71], v[100:103], a[36:39], v[68:71]\n"
"ds_write_addtid_b32 v140 offset:1536\n"
"v_add_f32 v73, v73, v133\n"
"v_mfma_f32_16x16x32_bf16 v[68:71], v[104:107], a[40:43], v[68:71]\n"
"ds_write_addtid_b32 v141 offset:6176\n"
"v_mfma_f32_16x16x32_bf16 v[68:71], v[108:111], a[44:47], v[68:71]\n"
"ds_write_addtid_b32 v142 offset:10880\n"
"v_add_f32 v74, v74, v134\n"
"v_mfma_f32_16x16x32_bf16 v[68:71], v[112:115], a[48:51], v[68:71]\n"
"ds_write_addtid_b32 v143 offset:15520\n"
"v_add_f32 v75, v75, v135\n"
"v_mfma_f32_16x16x32_bf16 v[68:71], v[116:119], a[52:55], v[68:71]\n"
"v_mul_f32 v72, v254, v72\n"
"v_mul_f32 v73, v254, v73\n"
"v_mfma_f32_16x16x32_bf16 v[68:71], v[120:123], a[56:59], v[68:71]\n"
"v_mul_f32 v74, v254, v74\n"
"v_mul_f32 v75, v254, v75\n"
"v_mfma_f32_16x16x32_bf16 v[68:71], v[124:127], a[60:63], v[68:71]\n"
"v_cvt_pk_bf16_f32 v76, v72, v73\n"
"v_mfma_f32_16x16x32_bf16 v[88:91], v[96:99], a[128:131], v[88:91]\n"
"v_cvt_pk_bf16_f32 v77, v74, v75\n"
"v_mfma_f32_16x16x32_bf16 v[88:91], v[100:103], a[132:135], v[88:91]\n"
"ds_write_addtid_b32 v68 offset:1792\n"
"ds_write_addtid_b32 v76 offset:18592\n"
"v_mfma_f32_16x16x32_bf16 v[88:91], v[104:107], a[136:139], v[88:91]\n"
"ds_write_addtid_b32 v69 offset:6432\n"
"ds_write_addtid_b32 v77 offset:18848\n"
"v_mfma_f32_16x16x32_bf16 v[88:91], v[108:111], a[140:143], v[88:91]\n"
"s_waitcnt lgkmcnt(12)\n"
"ds_write_addtid_b32 v70 offset:11136\n"
"v_add_f32 v84, v84, v64\n"
"v_mfma_f32_16x16x32_bf16 v[88:91], v[112:115], a[144:147], v[88:91]\n"
"ds_write_addtid_b32 v71 offset:15776\n"
"v_mfma_f32_16x16x32_bf16 v[88:91], v[116:119], a[148:151], v[88:91]\n"
"v_add_f32 v85, v85, v65\n"
"v_add_f32 v86, v86, v66\n"
"v_mfma_f32_16x16x32_bf16 v[88:91], v[120:123], a[152:155], v[88:91]\n"
"v_add_f32 v87, v87, v67\n"
"v_add_f32 v84, v84, v92\n"
"v_mfma_f32_16x16x32_bf16 v[88:91], v[124:127], a[156:159], v[88:91]\n"
"v_add_f32 v85, v85, v93\n"
"v_add_f32 v86, v86, v94\n"
"v_mfma_f32_16x16x32_bf16 v[128:131], v[96:99], a[160:163], v[128:131]\n"
"v_add_f32 v87, v87, v95\n"
"v_add_f32 v84, v84, v136\n"
"v_mfma_f32_16x16x32_bf16 v[128:131], v[100:103], a[164:167], v[128:131]\n"
"v_add_f32 v85, v85, v137\n"
"v_add_f32 v86, v86, v138\n"
"v_mfma_f32_16x16x32_bf16 v[128:131], v[104:107], a[168:171], v[128:131]\n"
"v_add_f32 v87, v87, v139\n"
"v_mul_f32 v84, v254, v84\n"
"v_mfma_f32_16x16x32_bf16 v[128:131], v[108:111], a[172:175], v[128:131]\n"
"v_mul_f32 v85, v254, v85\n"
"v_mul_f32 v86, v254, v86\n"
"v_mfma_f32_16x16x32_bf16 v[128:131], v[112:115], a[176:179], v[128:131]\n"
"v_mul_f32 v87, v254, v87\n"
"v_mfma_f32_16x16x32_bf16 v[128:131], v[116:119], a[180:183], v[128:131]\n"
"v_mfma_f32_16x16x32_bf16 v[128:131], v[120:123], a[184:187], v[128:131]\n"
"v_cvt_pk_bf16_f32 v78, v84, v85\n"
"v_mfma_f32_16x16x32_bf16 v[128:131], v[124:127], a[188:191], v[128:131]\n"
"v_cvt_pk_bf16_f32 v79, v86, v87\n"
"ds_write_addtid_b32 v78 offset:19104\n"
"ds_write_addtid_b32 v79 offset:19360\n"
"s_waitcnt vmcnt(0)\n"
"buffer_store_dwordx2 v[0:1], v14, s[40:43], s94 offen offset:0 sc1\n"
"s_waitcnt lgkmcnt(0)\n"
"s_barrier\n"
"ds_read_b128 v[92:95], v2 offset:2560\n"
"ds_read_b128 v[96:99], v3 offset:3584\n"
"ds_read_b128 v[100:103], v4 offset:0\n"
"ds_read_b128 v[104:107], v2 offset:2816\n"
"ds_read_b128 v[108:111], v3 offset:3840\n"
"ds_read_b128 v[112:115], v4 offset:256\n"
"s_waitcnt lgkmcnt(3)\n"
"v_add_f32 v92, v92, v96\n"
"v_add_f32 v93, v93, v97\n"
"v_add_f32 v94, v94, v98\n"
"v_add_f32 v95, v95, v99\n"
"v_add_f32 v92, v92, v100\n"
"v_add_f32 v93, v93, v101\n"
"v_add_f32 v94, v94, v102\n"
"v_add_f32 v95, v95, v103\n"
"v_add_f32 v92, v92, v88\n"
"v_add_f32 v93, v93, v89\n"
"v_add_f32 v94, v94, v90\n"
"v_add_f32 v95, v95, v91\n"
"v_mul_f32_dpp v92, v253, v92 row_newbcast:0 row_mask:0xf bank_mask:0xf\n"
"v_mul_f32_dpp v93, v253, v93 row_newbcast:1 row_mask:0xf bank_mask:0xf\n"
"v_mul_f32_dpp v94, v253, v94 row_newbcast:2 row_mask:0xf bank_mask:0xf\n"
"v_mul_f32_dpp v95, v253, v95 row_newbcast:3 row_mask:0xf bank_mask:0xf\n"
"v_cvt_pk_bf16_f32 v158, v92, v93\n"
"v_cvt_pk_bf16_f32 v159, v94, v95\n"
"ds_write_addtid_b32 v158 offset:19616\n"
"ds_write_addtid_b32 v159 offset:19872\n"
"s_waitcnt lgkmcnt(2)\n"
"v_add_f32 v104, v104, v108\n"
"v_add_f32 v105, v105, v109\n"
"v_add_f32 v106, v106, v110\n"
"v_add_f32 v107, v107, v111\n"
"v_add_f32 v104, v104, v112\n"
"v_add_f32 v105, v105, v113\n"
"v_add_f32 v106, v106, v114\n"
"v_add_f32 v107, v107, v115\n"
"v_add_f32 v104, v104, v128\n"
"v_add_f32 v105, v105, v129\n"
"v_add_f32 v106, v106, v130\n"
"v_add_f32 v107, v107, v131\n"
"v_mul_f32_dpp v104, v253, v104 row_newbcast:0 row_mask:0xf bank_mask:0xf\n"
"v_mul_f32_dpp v105, v253, v105 row_newbcast:1 row_mask:0xf bank_mask:0xf\n"
"v_mul_f32_dpp v106, v253, v106 row_newbcast:2 row_mask:0xf bank_mask:0xf\n"
"v_mul_f32_dpp v107, v253, v107 row_newbcast:3 row_mask:0xf bank_mask:0xf\n"
"v_cvt_pk_bf16_f32 v166, v104, v105\n"
"v_cvt_pk_bf16_f32 v167, v106, v107\n"
"ds_write_addtid_b32 v166 offset:20128\n"
"ds_write_addtid_b32 v167 offset:20384\n"
"s_waitcnt lgkmcnt(0)\n"
"s_barrier\n"
"s_branch .LQ_end\n"
".LQ_t3:\n"
"v_mfma_f32_16x16x32_bf16 v[136:139], a[120:123], v[88:91], v[136:139]\n"
"v_rcp_f32 v251, v251\n"
"v_mfma_f32_16x16x32_bf16 v[136:139], a[124:127], v[92:95], v[136:139]\n"
"s_waitcnt vmcnt(16)\n"
"v_mul_f32 v253, v253, v251\n"
"v_mfma_f32_16x16x32_bf16 v[64:67], v[96:99], a[64:67], 0\n"
"v_mul_f32 v252, v252, v251\n"
"v_cvt_pk_bf16_f32 v250, v253, v252\n"
"v_mfma_f32_16x16x32_bf16 v[64:67], v[100:103], a[68:71], v[64:67]\n"
"s_waitcnt vmcnt(14)\n"
"v_mfma_f32_16x16x32_bf16 v[64:67], v[104:107], a[72:75], v[64:67]\n"
"s_waitcnt lgkmcnt(12)\n"
"v_and_b32 v68, v250, v24\n"
"v_and_b32 v69, v250, v25\n"
"v_mfma_f32_16x16x32_bf16 v[64:67], v[108:111], a[76:79], v[64:67]\n"
"s_waitcnt vmcnt(12)\n"
"v_and_b32 v70, v250, v26\n"
"v_and_b32 v71, v250, v27\n"
"v_mfma_f32_16x16x32_bf16 v[64:67], v[112:115], a[80:83], v[64:67]\n"
"v_mfma_f32_16x16x32_bf16 v[72:75], v[156:159], v[68:71], 0\n"
"v_mul_f32_dpp v91, v153, v44 row_newbcast:3 row_mask:0xf bank_mask:0xf\n"
"v_mov_b32 v253, v152\n"
"v_mfma_f32_16x16x32_bf16 v[76:79], v[164:167], v[68:71], 0\n"
"v_mul_f32_dpp v89, v153, v44 row_newbcast:1 row_mask:0xf bank_mask:0xf\n"
"v_mul_f32_dpp v90, v153, v44 row_newbcast:2 row_mask:0xf bank_mask:0xf\n"
"v_mfma_f32_16x16x32_bf16 v[64:67], v[116:119], a[84:87], v[64:67]\n"
"s_waitcnt vmcnt(10)\n"
"v_cvt_pk_bf16_f32 v68, v72, v73\n"
"v_cvt_pk_bf16_f32 v69, v74, v75\n"
"v_mfma_f32_16x16x32_bf16 v[64:67], v[120:123], a[88:91], v[64:67]\n"
"v_cvt_pk_bf16_f32 v70, v76, v77\n"
"v_cvt_pk_bf16_f32 v71, v78, v79\n"
"v_mfma_f32_16x16x32_bf16 v[64:67], v[124:127], a[92:95], v[64:67]\n"
"buffer_store_dwordx4 v[68:71], v13, s[40:43], s95 offen sc1\n"
"v_mul_f32_dpp v130, v153, v45 row_newbcast:2 row_mask:0xf bank_mask:0xf\n"
"v_mul_f32_dpp v131, v153, v45 row_newbcast:3 row_mask:0xf bank_mask:0xf\n"
"v_mul_f32_dpp v88, v153, v44 row_newbcast:0 row_mask:0xf bank_mask:0xf\n"
"v_mfma_f32_16x16x32_bf16 v[68:71], v[96:99], a[96:99], 0\n"
"s_waitcnt lgkmcnt(0)\n"
"v_mul_f32_dpp v128, v153, v45 row_newbcast:0 row_mask:0xf bank_mask:0xf\n"
"v_mul_f32_dpp v129, v153, v45 row_newbcast:1 row_mask:0xf bank_mask:0xf\n"
"v_mfma_f32_16x16x32_bf16 v[68:71], v[100:103], a[100:103], v[68:71]\n"
"s_barrier\n"
"v_mfma_f32_16x16x32_bf16 v[68:71], v[104:107], a[104:107], v[68:71]\n"
"ds_write_addtid_b32 v64 offset:1024\n"
"ds_read_b128 v[72:75], v2 offset:1024\n"
"v_mfma_f32_16x16x32_bf16 v[68:71], v[108:111], a[108:111], v[68:71]\n"
"ds_write_addtid_b32 v65 offset:5664\n"
"ds_read_b128 v[76:79], v3 offset:2048\n"
"v_mfma_f32_16x16x32_bf16 v[68:71], v[112:115], a[112:115], v[68:71]\n"
"ds_write_addtid_b32 v66 offset:10368\n"
"v_mfma_f32_16x16x32_bf16 v[68:71], v[116:119], a[116:119], v[68:71]\n"
"ds_write_addtid_b32 v67 offset:15008\n"
"ds_read_b128 v[80:83], v4 offset:3072\n"
"v_mfma_f32_16x16x32_bf16 v[68:71], v[120:123], a[120:123], v[68:71]\n"
"ds_read_b128 v[84:87], v2 offset:1280\n"
"ds_read_b128 v[64:67], v3 offset:2304\n"
"v_mfma_f32_16x16x32_bf16 v[68:71], v[124:127], a[124:127], v[68:71]\n"
"ds_read_b128 v[92:95], v4 offset:3328\n"
"v_mfma_f32_16x16x32_bf16 v[140:143], v[96:99], a[0:3], 0\n"
"v_mfma_f32_16x16x32_bf16 v[140:143], v[100:103], a[4:7], v[140:143]\n"
"ds_write_addtid_b32 v68 offset:1280\n"
"v_mfma_f32_16x16x32_bf16 v[140:143], v[104:107], a[8:11], v[140:143]\n"
"ds_write_addtid_b32 v69 offset:5920\n"
"v_mfma_f32_16x16x32_bf16 v[140:143], v[108:111], a[12:15], v[140:143]\n"
"s_waitcnt lgkmcnt(5)\n"
"ds_write_addtid_b32 v70 offset:10624\n"
"v_add_f32 v72, v72, v76\n"
"v_mfma_f32_16x16x32_bf16 v[140:143], v[112:115], a[16:19], v[140:143]\n"
"ds_write_addtid_b32 v71 offset:15264\n"
"v_add_f32 v73, v73, v77\n"
"v_mfma_f32_16x16x32_bf16 v[140:143], v[116:119], a[20:23], v[140:143]\n"
"v_add_f32 v74, v74, v78\n"
"v_mfma_f32_16x16x32_bf16 v[140:143], v[120:123], a[24:27], v[140:143]\n"
"v_add_f32 v75, v75, v79\n"
"v_add_f32 v72, v72, v80\n"
"v_mfma_f32_16x16x32_bf16 v[140:143], v[124:127], a[28:31], v[140:143]\n"
"v_add_f32 v73, v73, v81\n"
"v_add_f32 v74, v74, v82\n"
"v_mfma_f32_16x16x32_bf16 v[68:71], v[96:99], a[32:35], 0\n"
"v_add_f32 v75, v75, v83\n"
"v_add_f32 v72, v72, v132\n"
"v_mfma_f32_16x16x32_bf16 v[68:71], v[100:103], a[36:39], v[68:71]\n"
"ds_write_addtid_b32 v140 offset:1536\n"
"v_add_f32 v73, v73, v133\n"
"v_mfma_f32_16x16x32_bf16 v[68:71], v[104:107], a[40:43], v[68:71]\n"
"ds_write_addtid_b32 v141 offset:6176\n"
"v_mfma_f32_16x16x32_bf16 v[68:71], v[108:111], a[44:47], v[68:71]\n"
"ds_write_addtid_b32 v142 offset:10880\n"
"v_add_f32 v74, v74, v134\n"
"v_mfma_f32_16x16x32_bf16 v[68:71], v[112:115], a[48:51], v[68:71]\n"
"ds_write_addtid_b32 v143 offset:15520\n"
"v_add_f32 v75, v75, v135\n"
"v_mfma_f32_16x16x32_bf16 v[68:71], v[116:119], a[52:55], v[68:71]\n"
"v_mul_f32 v72, v254, v72\n"
"v_mul_f32 v73, v254, v73\n"
"v_mfma_f32_16x16x32_bf16 v[68:71], v[120:123], a[56:59], v[68:71]\n"
"v_mul_f32 v74, v254, v74\n"
"v_mul_f32 v75, v254, v75\n"
"v_mfma_f32_16x16x32_bf16 v[68:71], v[124:127], a[60:63], v[68:71]\n"
"v_cvt_pk_bf16_f32 v76, v72, v73\n"
"v_mfma_f32_16x16x32_bf16 v[88:91], v[96:99], a[128:131], v[88:91]\n"
"v_cvt_pk_bf16_f32 v77, v74, v75\n"
"v_mfma_f32_16x16x32_bf16 v[88:91], v[100:103], a[132:135], v[88:91]\n"
"ds_write_addtid_b32 v68 offset:1792\n"
"ds_write_addtid_b32 v76 offset:18592\n"
"v_mfma_f32_16x16x32_bf16 v[88:91], v[104:107], a[136:139], v[88:91]\n"
"ds_write_addtid_b32 v69 offset:6432\n"
"ds_write_addtid_b32 v77 offset:18848\n"
"v_mfma_f32_16x16x32_bf16 v[88:91], v[108:111], a[140:143], v[88:91]\n"
"s_waitcnt lgkmcnt(12)\n"
"ds_write_addtid_b32 v70 offset:11136\n"
"v_add_f32 v84, v84, v64\n"
"v_mfma_f32_16x16x32_bf16 v[88:91], v[112:115], a[144:147], v[88:91]\n"
"ds_write_addtid_b32 v71 offset:15776\n"
"v_mfma_f32_16x16x32_bf16 v[88:91], v[116:119], a[148:151], v[88:91]\n"
"v_add_f32 v85, v85, v65\n"
"v_add_f32 v86, v86, v66\n"
"v_mfma_f32_16x16x32_bf16 v[88:91], v[120:123], a[152:155], v[88:91]\n"
"v_add_f32 v87, v87, v67\n"
"v_add_f32 v84, v84, v92\n"
"v_mfma_f32_16x16x32_bf16 v[88:91], v[124:127], a[156:159], v[88:91]\n"
"v_add_f32 v85, v85, v93\n"
"v_add_f32 v86, v86, v94\n"
"v_mfma_f32_16x16x32_bf16 v[128:131], v[96:99], a[160:163], v[128:131]\n"
"v_add_f32 v87, v87, v95\n"
"v_add_f32 v84, v84, v136\n"
"v_mfma_f32_16x16x32_bf16 v[128:131], v[100:103], a[164:167], v[128:131]\n"
"v_add_f32 v85, v85, v137\n"
"v_add_f32 v86, v86, v138\n"
"v_mfma_f32_16x16x32_bf16 v[128:131], v[104:107], a[168:171], v[128:131]\n"
"v_add_f32 v87, v87, v139\n"
"v_mul_f32 v84, v254, v84\n"
"v_mfma_f32_16x16x32_bf16 v[128:131], v[108:111], a[172:175], v[128:131]\n"
"v_mul_f32 v85, v254, v85\n"
"v_mul_f32 v86, v254, v86\n"
"v_mfma_f32_16x16x32_bf16 v[128:131], v[112:115], a[176:179], v[128:131]\n"
"s_waitcnt vmcnt(1)\n"
"v_mul_f32 v87, v254, v87\n"
"v_mfma_f32_16x16x32_bf16 v[128:131], v[116:119], a[180:183], v[128:131]\n"
"v_mfma_f32_16x16x32_bf16 v[128:131], v[120:123], a[184:187], v[128:131]\n"
"v_cvt_pk_bf16_f32 v78, v84, v85\n"
"v_mfma_f32_16x16x32_bf16 v[128:131], v[124:127], a[188:191], v[128:131]\n"
"s_waitcnt vmcnt(8)\n"
"v_cvt_pk_bf16_f32 v79, v86, v87\n"
"v_mfma_f32_16x16x32_bf16 v[80:83], a[192:195], a[64:67], 0\n"
"ds_write_addtid_b32 v78 offset:19104\n"
"v_mfma_f32_16x16x32_bf16 v[80:83], a[196:199], a[68:71], v[80:83]\n"
"s_waitcnt vmcnt(6)\n"
"ds_write_addtid_b32 v79 offset:19360\n"
"v_mfma_f32_16x16x32_bf16 v[80:83], a[200:203], a[72:75], v[80:83]\n"
"v_mfma_f32_16x16x32_bf16 v[80:83], a[204:207], a[76:79], v[80:83]\n"
"s_waitcnt vmcnt(4)\n"
"v_mul_f32 v249, 0x3e8293ee, v160\n"
"v_mfma_f32_16x16x32_bf16 v[80:83], a[208:211], a[80:83], v[80:83]\n"
"v_mul_f32 v126, v161, v30\n"
"v_mul_f32 v127, v161, v31\n"
"v_mfma_f32_16x16x32_bf16 v[80:83], a[212:215], a[84:87], v[80:83]\n"
"s_waitcnt vmcnt(2)\n"
"v_mul_f32 v125, v161, v29\n"
"v_mfma_f32_16x16x32_bf16 v[80:83], a[216:219], a[88:91], v[80:83]\n"
"v_mul_f32 v135, v161, v35\n"
"v_mul_f32 v124, v161, v28\n"
"v_mfma_f32_16x16x32_bf16 v[80:83], a[220:223], a[92:95], v[80:83]\n"
"s_waitcnt vmcnt(0)\n"
"buffer_store_dwordx2 v[0:1], v14, s[40:43], s94 offen offset:0 sc1\n"
"v_mul_f32 v134, v161, v34\n"
"v_mfma_f32_16x16x32_bf16 v[84:87], a[192:195], a[96:99], 0\n"
"s_waitcnt lgkmcnt(0)\n"
"v_mul_f32 v132, v161, v32\n"
"v_mul_f32 v133, v161, v33\n"
"v_mfma_f32_16x16x32_bf16 v[84:87], a[196:199], a[100:103], v[84:87]\n"
"s_barrier\n"
"v_mfma_f32_16x16x32_bf16 v[84:87], a[200:203], a[104:107], v[84:87]\n"
"ds_write_addtid_b32 v80 offset:2048\n"
"v_mfma_f32_16x16x32_bf16 v[84:87], a[204:207], a[108:111], v[84:87]\n"
"ds_write_addtid_b32 v81 offset:6688\n"
"ds_read_b128 v[92:95], v2 offset:2560\n"
"v_mfma_f32_16x16x32_bf16 v[84:87], a[208:211], a[112:115], v[84:87]\n"
"ds_write_addtid_b32 v82 offset:11392\n"
"ds_read_b128 v[96:99], v3 offset:3584\n"
"v_mfma_f32_16x16x32_bf16 v[84:87], a[212:215], a[116:119], v[84:87]\n"
"ds_write_addtid_b32 v83 offset:16032\n"
"ds_read_b128 v[100:103], v4 offset:0\n"
"v_mfma_f32_16x16x32_bf16 v[84:87], a[216:219], a[120:123], v[84:87]\n"
"ds_read_b128 v[104:107], v2 offset:2816\n"
"ds_read_b128 v[108:111], v3 offset:3840\n"
"v_mfma_f32_16x16x32_bf16 v[84:87], a[220:223], a[124:127], v[84:87]\n"
"ds_read_b128 v[112:115], v4 offset:256\n"
"v_mfma_f32_16x16x32_bf16 v[116:119], a[128:131], a[192:195], 0\n"
"v_mfma_f32_16x16x32_bf16 v[116:119], a[132:135], a[196:199], v[116:119]\n"
"ds_write_addtid_b32 v84 offset:2304\n"
"v_mfma_f32_16x16x32_bf16 v[116:119], a[136:139], a[200:203], v[116:119]\n"
"ds_write_addtid_b32 v85 offset:6944\n"
"v_mfma_f32_16x16x32_bf16 v[116:119], a[140:143], a[204:207], v[116:119]\n"
"s_waitcnt lgkmcnt(5)\n"
"ds_write_addtid_b32 v86 offset:11648\n"
"v_add_f32 v92, v92, v96\n"
"v_mfma_f32_16x16x32_bf16 v[116:119], a[144:147], a[208:211], v[116:119]\n"
"ds_write_addtid_b32 v87 offset:16288\n"
"v_mfma_f32_16x16x32_bf16 v[116:119], a[148:151], a[212:215], v[116:119]\n"
"v_add_f32 v93, v93, v97\n"
"v_add_f32 v94, v94, v98\n"
"v_mfma_f32_16x16x32_bf16 v[116:119], a[152:155], a[216:219], v[116:119]\n"
"v_add_f32 v95, v95, v99\n"
"v_add_f32 v92, v92, v100\n"
"v_mfma_f32_16x16x32_bf16 v[116:119], a[156:159], a[220:223], v[116:119]\n"
"v_add_f32 v93, v93, v101\n"
"v_add_f32 v94, v94, v102\n"
"v_mfma_f32_16x16x32_bf16 v[96:99], a[160:163], a[192:195], 0\n"
"v_add_f32 v95, v95, v103\n"
"v_add_f32 v92, v92, v88\n"
"v_mfma_f32_16x16x32_bf16 v[96:99], a[164:167], a[196:199], v[96:99]\n"
"ds_write_addtid_b32 v116 offset:2560\n"
"v_mfma_f32_16x16x32_bf16 v[96:99], a[168:171], a[200:203], v[96:99]\n"
"ds_write_addtid_b32 v117 offset:7200\n"
"v_add_f32 v93, v93, v89\n"
"v_mfma_f32_16x16x32_bf16 v[96:99], a[172:175], a[204:207], v[96:99]\n"
"ds_write_addtid_b32 v118 offset:11904\n"
"v_add_f32 v94, v94, v90\n"
"v_mfma_f32_16x16x32_bf16 v[96:99], a[176:179], a[208:211], v[96:99]\n"
"ds_write_addtid_b32 v119 offset:16544\n"
"v_add_f32 v95, v95, v91\n"
"v_mfma_f32_16x16x32_bf16 v[96:99], a[180:183], a[212:215], v[96:99]\n"
"v_mul_f32_dpp v92, v253, v92 row_newbcast:0 row_mask:0xf bank_mask:0xf\n"
"v_mul_f32_dpp v93, v253, v93 row_newbcast:1 row_mask:0xf bank_mask:0xf\n"
"v_mfma_f32_16x16x32_bf16 v[96:99], a[184:187], a[216:219], v[96:99]\n"
"v_mul_f32_dpp v94, v253, v94 row_newbcast:2 row_mask:0xf bank_mask:0xf\n"
"v_mfma_f32_16x16x32_bf16 v[96:99], a[188:191], a[220:223], v[96:99]\n"
"v_mul_f32_dpp v95, v253, v95 row_newbcast:3 row_mask:0xf bank_mask:0xf\n"
"v_cvt_pk_bf16_f32 v158, v92, v93\n"
"v_mfma_f32_16x16x32_bf16 v[124:127], a[0:3], a[192:195], v[124:127]\n"
"v_cvt_pk_bf16_f32 v159, v94, v95\n"
"v_mfma_f32_16x16x32_bf16 v[124:127], a[4:7], a[196:199], v[124:127]\n"
"ds_write_addtid_b32 v96 offset:2816\n"
"ds_write_addtid_b32 v158 offset:19616\n"
"v_mfma_f32_16x16x32_bf16 v[124:127], a[8:11], a[200:203], v[124:127]\n"
"ds_write_addtid_b32 v97 offset:7456\n"
"ds_write_addtid_b32 v159 offset:19872\n"
"v_mfma_f32_16x16x32_bf16 v[124:127], a[12:15], a[204:207], v[124:127]\n"
"s_waitcnt lgkmcnt(12)\n"
"ds_write_addtid_b32 v98 offset:12160\n"
"v_mfma_f32_16x16x32_bf16 v[124:127], a[16:19], a[208:211], v[124:127]\n"
"ds_write_addtid_b32 v99 offset:16800\n"
"v_add_f32 v104, v104, v108\n"
"v_mfma_f32_16x16x32_bf16 v[124:127], a[20:23], a[212:215], v[124:127]\n"
"v_add_f32 v105, v105, v109\n"
"v_add_f32 v106, v106, v110\n"
"v_mfma_f32_16x16x32_bf16 v[124:127], a[24:27], a[216:219], v[124:127]\n"
"v_add_f32 v107, v107, v111\n"
"v_add_f32 v104, v104, v112\n"
"v_mfma_f32_16x16x32_bf16 v[124:127], a[28:31], a[220:223], v[124:127]\n"
"v_add_f32 v105, v105, v113\n"
"v_add_f32 v106, v106, v114\n"
"v_mfma_f32_16x16x32_bf16 v[132:135], a[32:35], a[192:195], v[132:135]\n"
"v_add_f32 v107, v107, v115\n"
"v_add_f32 v104, v104, v128\n"
"v_mfma_f32_16x16x32_bf16 v[132:135], a[36:39], a[196:199], v[132:135]\n"
"v_add_f32 v105, v105, v129\n"
"v_add_f32 v106, v106, v130\n"
"v_mfma_f32_16x16x32_bf16 v[132:135], a[40:43], a[200:203], v[132:135]\n"
"v_mfma_f32_16x16x32_bf16 v[132:135], a[44:47], a[204:207], v[132:135]\n"
"v_add_f32 v107, v107, v131\n"
"v_mul_f32_dpp v104, v253, v104 row_newbcast:0 row_mask:0xf bank_mask:0xf\n"
"v_mfma_f32_16x16x32_bf16 v[132:135], a[48:51], a[208:211], v[132:135]\n"
"v_mul_f32_dpp v105, v253, v105 row_newbcast:1 row_mask:0xf bank_mask:0xf\n"
"v_mul_f32_dpp v106, v253, v106 row_newbcast:2 row_mask:0xf bank_mask:0xf\n"
"v_mfma_f32_16x16x32_bf16 v[132:135], a[52:55], a[212:215], v[132:135]\n"
"v_mul_f32_dpp v107, v253, v107 row_newbcast:3 row_mask:0xf bank_mask:0xf\n"
"v_cvt_pk_bf16_f32 v166, v104, v105\n"
"v_mfma_f32_16x16x32_bf16 v[132:135], a[56:59], a[216:219], v[132:135]\n"
"v_cvt_pk_bf16_f32 v167, v106, v107\n"
"v_mfma_f32_16x16x32_bf16 v[132:135], a[60:63], a[220:223], v[132:135]\n"
"ds_write_addtid_b32 v166 offset:20128\n"
"ds_write_addtid_b32 v167 offset:20384\n"
"s_waitcnt lgkmcnt(0)\n"
"s_barrier\n"
"ds_read_b128 v[128:131], v2 offset:4096\n"
"ds_read_b128 v[120:123], v3 offset:512\n"
"ds_read_b128 v[136:139], v4 offset:1536\n"
"ds_read_b128 v[140:143], v2 offset:4352\n"
"ds_read_b128 v[168:171], v3 offset:768\n"
"ds_read_b128 v[172:175], v4 offset:1792\n"
"s_waitcnt lgkmcnt(3)\n"
"v_add_f32 v128, v128, v120\n"
"v_add_f32 v129, v129, v121\n"
"v_add_f32 v130, v130, v122\n"
"v_add_f32 v131, v131, v123\n"
"v_add_f32 v128, v128, v136\n"
"v_add_f32 v129, v129, v137\n"
"v_add_f32 v130, v130, v138\n"
"v_add_f32 v131, v131, v139\n"
"v_add_f32 v128, v128, v124\n"
"v_add_f32 v129, v129, v125\n"
"v_add_f32 v130, v130, v126\n"
"v_add_f32 v131, v131, v127\n"
"v_cvt_pk_bf16_f32 v180, v128, v129\n"
"v_cvt_pk_bf16_f32 v181, v130, v131\n"
"ds_read2st64_b32 v[136:137], v11 offset0:0 offset1:1\n"
"ds_read2st64_b32 v[138:139], v11 offset0:2 offset1:3\n"
"s_waitcnt lgkmcnt(2)\n"
"v_add_f32 v140, v140, v168\n"
"v_add_f32 v141, v141, v169\n"
"v_add_f32 v142, v142, v170\n"
"v_add_f32 v143, v143, v171\n"
"v_add_f32 v140, v140, v172\n"
"v_add_f32 v141, v141, v173\n"
"v_add_f32 v142, v142, v174\n"
"v_add_f32 v143, v143, v175\n"
"v_add_f32 v140, v140, v132\n"
"v_add_f32 v141, v141, v133\n"
"v_add_f32 v142, v142, v134\n"
"v_add_f32 v143, v143, v135\n"
"v_cvt_pk_bf16_f32 v182, v140, v141\n"
"v_cvt_pk_bf16_f32 v183, v142, v143\n"
"s_waitcnt lgkmcnt(0)\n"
"s_nop 0\n"
"v_mfma_f32_16x16x32_bf16 v[172:175], v[136:139], v[180:183], v[20:23]\n"
"s_barrier\n"
"ds_read2st64_b32 v[144:145], v12 offset0:0 offset1:1\n"
"ds_read2st64_b32 v[146:147], v12 offset0:2 offset1:3\n"
"ds_read2st64_b32 v[156:157], v11 offset0:4 offset1:5\n"
"ds_read2st64_b32 v[164:165], v11 offset0:6 offset1:7\n"
"s_add_u32 s94, s50, 0x0\n"
"s_and_b32 s95, s94, 0xfbffffff\n"
"s_waitcnt lgkmcnt(2)\n"
"v_mfma_f32_16x16x32_bf16 v[148:151], v[144:147], v[180:183], v[16:19]\n"
"s_cmp_eq_u32 s94, s62\n"
"s_cbranch_scc1 .LQ_ft3k0s\n"
".LQ_ft3k0r:\n"
"v_max_f32 v253, v172, v174\n"
"v_max_f32 v252, v173, v175\n"
"s_nop 3\n"
"v_max3_f32 v253, v148, v150, v253\n"
"v_max3_f32 v252, v149, v151, v252\n"
"v_max_f32 v251, v253, v252\n"
"v_mov_b32 v250, v251\n"
"s_nop 1\n"
"v_permlane32_swap_b32 v250, v251\n"
"v_max_f32 v251, v250, v251\n"
"v_mov_b32 v250, v251\n"
"s_nop 1\n"
"v_permlane16_swap_b32 v250, v251\n"
"v_max_f32 v251, v250, v251\n"
"v_mul_f32 v251, v251, v249\n"
"v_fma_f32 v253, v253, v249, -v251\n"
"v_fma_f32 v252, v252, v249, -v251\n"
"v_exp_f32 v253, v253\n"
"v_exp_f32 v252, v252\n"
"s_nop 0\n"
"v_add_f32 v251, v253, v252\n"
"v_mov_b32 v250, v251\n"
"s_nop 1\n"
"v_permlane32_swap_b32 v250, v251\n"
"v_add_f32 v251, v250, v251\n"
"v_mov_b32 v250, v251\n"
"s_nop 1\n"
"v_permlane16_swap_b32 v250, v251\n"
"v_add_f32 v251, v250, v251\n"
"v_rcp_f32 v251, v251\n"
"s_nop 0\n"
"v_mul_f32 v253, v253, v251\n"
"v_mul_f32 v252, v252, v251\n"
"v_cvt_pk_bf16_f32 v250, v253, v252\n"
"s_waitcnt lgkmcnt(0)\n"
"v_and_b32 v68, v250, v24\n"
"v_and_b32 v69, v250, v25\n"
"v_and_b32 v70, v250, v26\n"
"v_and_b32 v71, v250, v27\n"
"s_nop 1\n"
"v_mfma_f32_16x16x32_bf16 v[72:75], v[156:159], v[68:71], 0\n"
"v_mfma_f32_16x16x32_bf16 v[76:79], v[164:167], v[68:71], 0\n"
"s_nop 6\n"
"v_cvt_pk_bf16_f32 v68, v72, v73\n"
"v_cvt_pk_bf16_f32 v69, v74, v75\n"
"v_cvt_pk_bf16_f32 v70, v76, v77\n"
"v_cvt_pk_bf16_f32 v71, v78, v79\n"
"buffer_store_dwordx4 v[68:71], v13, s[40:43], s95 offen sc1\n"
"s_waitcnt vmcnt(0)\n"
"buffer_store_dwordx2 v[0:1], v14, s[40:43], s94 offen offset:0 sc1\n"
"s_branch .LQ_end\n"
".LQ_end:\n"
"s_waitcnt vmcnt(0) lgkmcnt(0)\n"
"s_endpgm\n"
      :: "{v2}"((unsigned)(lds0 + ((wid + 3) & 3) * 20640 + trp)), "{v3}"((unsigned)(lds0 + ((wid + 2) & 3) * 20640 + trp)), "{v4}"((unsigned)(lds0 + ((wid + 1) & 3) * 20640 + trp)), "{v5}"((unsigned)(lane * 16 + wid * 8192 + ((0 + rot) % 4) * 2048 + 134217728)), "{v6}"((unsigned)(lane * 16 + wid * 8192 + ((1 + rot) % 4) * 2048 + 134217728)), "{v7}"((unsigned)(lane * 16 + wid * 8192 + ((2 + rot) % 4) * 2048 + 134217728)), "{v8}"((unsigned)(lane * 16 + wid * 8192 + ((3 + rot) % 4) * 2048 + 134217728)), "{v9}"((unsigned)(li * 16 + 352256 + Q_GL * 1048576)), "{v10}"((unsigned)(((li + 4 * lg) & 15) * 16 + 352256 + Q_GL * 1048576)), "{v11}"((unsigned)(lds0 + ((wid + 1) & 3) * 20640 + 18592 + lane * 4)), "{v12}"((unsigned)(lds0 + ((wid + 2) & 3) * 20640 + 18592 + lane * 4)), "{v13}"((unsigned)(lane * 16 + idx * 1024 + 32768)), "{v14}"((unsigned)(lane ? DROP : idx * 8 + 352512)), "{v16}"((unsigned)(qmask(0, 0))), "{v17}"((unsigned)(qmask(1, 0))), "{v18}"((unsigned)(qmask(2, 0))), "{v19}"((unsigned)(qmask(3, 0))), "{v20}"((unsigned)(qmask(0, 1))), "{v21}"((unsigned)(qmask(1, 1))), "{v22}"((unsigned)(qmask(2, 1))), "{v23}"((unsigned)(qmask(3, 1))), "{v24}"((unsigned)(pmask(0))), "{v25}"((unsigned)(pmask(1))), "{v26}"((unsigned)(pmask(2))), "{v27}"((unsigned)(pmask(3))), "{v48}"((unsigned)(vT + tsw - wid * 6144)), "{v49}"((unsigned)(vT + (tsw ^ 8) - wid * 6144)), "{v50}"((unsigned)((sEnt == 1 ? (li + 4 * lg) & 15 : li) * 16 + 1400832)), "{v51}"((unsigned)((sEnt == 0 ? (li + 4 * lg) & 15 : li) * 16 + 2449408)), "{v52}"((unsigned)((sEnt == 3 ? (li + 4 * lg) & 15 : li) * 16 + 3497984)), "{v53}"((unsigned)((sEnt == 2 ? (li + 4 * lg) & 15 : li) * 16 + 4546560)), "{v54}"((unsigned)(((lane >> 5) * 8 + set) * 2048 + wid * 512 + ((lane ^ (lane >> 5)) & 31) * 16)), "{v55}"((unsigned)(sLd - wid * 6144 + li * 512 + (lg ^ li) * 16)), "{v56}"((unsigned)(lane * 128 + wid * 8192 + (2 + (idx & 7) % 6) * 1048576 + 134217728)), "{v57}"((unsigned)(vD + ((0 + rot) % 4) * 393216)), "{v58}"((unsigned)(lds0 + 139264 + ((wid + 0) & 3) * 6144 + lane * 16)), "{v59}"((unsigned)(lds0 + 139264 + ((wid + 1) & 3) * 6144 + lane * 16)), "{v60}"((unsigned)(lds0 + 139264 + ((wid + 2) & 3) * 6144 + lane * 16)), "{v61}"((unsigned)(lds0 + 139264 + ((wid + 3) & 3) * 6144 + lane * 16)), "{s[40:43]}"(rS), "{s[44:47]}"(rW), "{s[48:49]}"(wsp), "{s50}"((unsigned)(sA)), "{s51}"((unsigned)(sLo)), "{s52}"((unsigned)(sHi)), "{s53}"((unsigned)(sEx)), "{s54}"((unsigned)(sEnt)), "{s55}"((unsigned)(sLd - wid * 6144)), "{s[56:57]}"(wsb + (sLo & 0x3ffffff)), "{s59}"((unsigned)(lds0 + wid * 20640)), "{s62}"((unsigned)(sLo)), "{s[64:67]}"(desc(a.x, 1 << 22)), "{s68}"((unsigned)(sW0 + 0)), "{s69}"((unsigned)(sW0 + 2048)), "{s70}"((unsigned)(sW0 + 4096))
      : "v255", "a255", "s95", "vcc", "scc", "memory");

  }
  if (DT && xo < 0 && xj < 12 + NE) {
    while (__builtin_amdgcn_s_memrealtime() - tz < DT + (xj - 16) * DU) __builtin_amdgcn_s_sleep(2);
    asm volatile("global_load_dword v252, %0, %1 sc1" ::"v"(WS0 + (PADF - LOWB + xj - 12) * RS + F_X + tid * 128), "s"(a.ws) : "v252");
  }
  while (__builtin_amdgcn_s_memrealtime() - tz < (role == 1 ? DLO : role == 2 ? (xo < 0 ? DLF : DLB) : DLG)) __builtin_amdgcn_s_sleep(4);
  if (role == 2 && xo < 0 && (SLP & 4)) {
#pragma unroll
    for (int i = 0; i < 4 * TW; ++i)
      asm volatile("global_load_dword v252, %0, %1" ::"v"((wid * 256 + i / TW * 64 + (xj - 16) * 4 + lg) * 7168 + (14 * (xo + 4) + min(li, 13)) * 128 + i % TW * 64), "s"(a.w1) : "v252");
  }
  if (role == 3 && (SLP & 8) && !((SLP & 32) && p2 < 128)) tw2();
  const unsigned sA = sLo - 3 * RS, sHi = sLo + (set ? 84 : 85) * 3 * RS;
  if (role == 1) {
    const unsigned sEx = sHi + O_EX * 3 * RS, sEnt = OR ? -wid & 3 : 0, tsw = 0, pair = idx * 4 + wid, rot = 0;
    const unsigned sW0 = wid * 256 * 2048 + idx * 256, vD = (lane >> 2) * 2048 + (lane & 3) * 16;
    const u32x4 rW = desc(a.wo, 1024 * 2048);
    asm volatile(
"s_nop 4\n"
"v_mov_b32 v186, 0\n"
"global_load_dwordx2 v[0:1], v186, s[48:49] sc1\n"
"v_mov_b32 v188, 0x3f803f80\n"
"v_mov_b32 v189, 0x3f803f80\n"
"v_mov_b32 v190, 0x3f803f80\n"
"v_mov_b32 v191, 0x3f803f80\n"
"s_add_u32 m0, s55, 0\n"
"s_add_u32 s60, s66, 0\n"
"buffer_load_dwordx4 v22, s[44:47], s60 offen lds\n"
"s_add_u32 m0, s55, 1024\n"
"s_add_u32 s60, s66, 32768\n"
"buffer_load_dwordx4 v22, s[44:47], s60 offen lds\n"
"s_add_u32 m0, s55, 2048\n"
"s_add_u32 s60, s67, 0\n"
"buffer_load_dwordx4 v22, s[44:47], s60 offen lds\n"
"s_add_u32 m0, s55, 3072\n"
"s_add_u32 s60, s67, 32768\n"
"buffer_load_dwordx4 v22, s[44:47], s60 offen lds\n"
"s_add_u32 m0, s55, 4096\n"
"s_add_u32 s60, s68, 0\n"
"buffer_load_dwordx4 v22, s[44:47], s60 offen lds\n"
"s_add_u32 m0, s55, 5120\n"
"s_add_u32 s60, s68, 32768\n"
"buffer_load_dwordx4 v22, s[44:47], s60 offen lds\n"
"s_add_u32 m0, s55, 6144\n"
"s_add_u32 s60, s69, 0\n"
"buffer_load_dwordx4 v22, s[44:47], s60 offen lds\n"
"s_add_u32 m0, s55, 7168\n"
"s_add_u32 s60, s69, 32768\n"
"buffer_load_dwordx4 v22, s[44:47], s60 offen lds\n"
"s_add_u32 m0, s55, 8192\n"
"s_add_u32 s60, s66, 65536\n"
"buffer_load_dwordx4 v22, s[44:47], s60 offen lds\n"
"s_add_u32 m0, s55, 9216\n"
"s_add_u32 s60, s66, 98304\n"
"buffer_load_dwordx4 v22, s[44:47], s60 offen lds\n"
"s_add_u32 m0, s55, 10240\n"
"s_add_u32 s60, s67, 65536\n"
"buffer_load_dwordx4 v22, s[44:47], s60 offen lds\n"
"s_add_u32 m0, s55, 11264\n"
"s_add_u32 s60, s67, 98304\n"
"buffer_load_dwordx4 v22, s[44:47], s60 offen lds\n"
"s_add_u32 m0, s55, 12288\n"
"s_add_u32 s60, s68, 65536\n"
"buffer_load_dwordx4 v22, s[44:47], s60 offen lds\n"
"s_add_u32 m0, s55, 13312\n"
"s_add_u32 s60, s68, 98304\n"
"buffer_load_dwordx4 v22, s[44:47], s60 offen lds\n"
"s_add_u32 m0, s55, 14336\n"
"s_add_u32 s60, s69, 65536\n"
"buffer_load_dwordx4 v22, s[44:47], s60 offen lds\n"
"s_add_u32 m0, s55, 15360\n"
"s_add_u32 s60, s69, 98304\n"
"buffer_load_dwordx4 v22, s[44:47], s60 offen lds\n"
"s_add_u32 m0, s55, 16384\n"
"s_add_u32 s60, s66, 0\n"
"buffer_load_dwordx4 v23, s[44:47], s60 offen lds\n"
"s_add_u32 m0, s55, 17408\n"
"s_add_u32 s60, s66, 32768\n"
"buffer_load_dwordx4 v23, s[44:47], s60 offen lds\n"
"s_add_u32 m0, s55, 18432\n"
"s_add_u32 s60, s67, 0\n"
"buffer_load_dwordx4 v23, s[44:47], s60 offen lds\n"
"s_add_u32 m0, s55, 19456\n"
"s_add_u32 s60, s67, 32768\n"
"buffer_load_dwordx4 v23, s[44:47], s60 offen lds\n"
"s_add_u32 m0, s55, 20480\n"
"s_add_u32 s60, s68, 0\n"
"buffer_load_dwordx4 v23, s[44:47], s60 offen lds\n"
"s_add_u32 m0, s55, 21504\n"
"s_add_u32 s60, s68, 32768\n"
"buffer_load_dwordx4 v23, s[44:47], s60 offen lds\n"
"s_add_u32 m0, s55, 22528\n"
"s_add_u32 s60, s69, 0\n"
"buffer_load_dwordx4 v23, s[44:47], s60 offen lds\n"
"s_add_u32 m0, s55, 23552\n"
"s_add_u32 s60, s69, 32768\n"
"buffer_load_dwordx4 v23, s[44:47], s60 offen lds\n"
"s_add_u32 m0, s55, 24576\n"
"s_add_u32 s60, s66, 65536\n"
"buffer_load_dwordx4 v23, s[44:47], s60 offen lds\n"
"s_add_u32 m0, s55, 25600\n"
"s_add_u32 s60, s66, 98304\n"
"buffer_load_dwordx4 v23, s[44:47], s60 offen lds\n"
"s_add_u32 m0, s55, 26624\n"
"s_add_u32 s60, s67, 65536\n"
"buffer_load_dwordx4 v23, s[44:47], s60 offen lds\n"
"s_add_u32 m0, s55, 27648\n"
"s_add_u32 s60, s67, 98304\n"
"buffer_load_dwordx4 v23, s[44:47], s60 offen lds\n"
"s_add_u32 m0, s55, 28672\n"
"s_add_u32 s60, s68, 65536\n"
"buffer_load_dwordx4 v23, s[44:47], s60 offen lds\n"
"s_add_u32 m0, s55, 29696\n"
"s_add_u32 s60, s68, 98304\n"
"buffer_load_dwordx4 v23, s[44:47], s60 offen lds\n"
"s_add_u32 m0, s55, 30720\n"
"s_add_u32 s60, s69, 65536\n"
"buffer_load_dwordx4 v23, s[44:47], s60 offen lds\n"
"s_add_u32 m0, s55, 31744\n"
"s_add_u32 s60, s69, 98304\n"
"buffer_load_dwordx4 v23, s[44:47], s60 offen lds\n"
"s_add_u32 m0, s55, 32768\n"
"s_add_u32 s60, s66, 0\n"
"buffer_load_dwordx4 v24, s[44:47], s60 offen lds\n"
"s_add_u32 m0, s55, 33792\n"
"s_add_u32 s60, s66, 32768\n"
"buffer_load_dwordx4 v24, s[44:47], s60 offen lds\n"
"s_add_u32 m0, s55, 34816\n"
"s_add_u32 s60, s67, 0\n"
"buffer_load_dwordx4 v24, s[44:47], s60 offen lds\n"
"s_add_u32 m0, s55, 35840\n"
"s_add_u32 s60, s67, 32768\n"
"buffer_load_dwordx4 v24, s[44:47], s60 offen lds\n"
"s_add_u32 m0, s55, 36864\n"
"s_add_u32 s60, s68, 0\n"
"buffer_load_dwordx4 v24, s[44:47], s60 offen lds\n"
"s_add_u32 m0, s55, 37888\n"
"s_add_u32 s60, s68, 32768\n"
"buffer_load_dwordx4 v24, s[44:47], s60 offen lds\n"
"s_add_u32 m0, s55, 38912\n"
"s_add_u32 s60, s69, 0\n"
"buffer_load_dwordx4 v24, s[44:47], s60 offen lds\n"
"s_add_u32 m0, s55, 39936\n"
"s_add_u32 s60, s69, 32768\n"
"buffer_load_dwordx4 v24, s[44:47], s60 offen lds\n"
"s_waitcnt vmcnt(38)\n"
"ds_read_b64_tr_b16 a[0:1], v20 offset:0\n"
"ds_read_b64_tr_b16 a[2:3], v20 offset:256\n"
"ds_read_b64_tr_b16 a[32:33], v21 offset:0\n"
"ds_read_b64_tr_b16 a[34:35], v21 offset:256\n"
"v_add_u32 v0, 2, v0\n"
"v_add_u32 v1, 0x9e3779b9, v1\n"
"v_or_b32 v0, 1, v0\n"
"v_readfirstlane_b32 s49, v1\n"
"v_readfirstlane_b32 s48, v0\n"
"s_waitcnt vmcnt(36)\n"
"ds_read_b64_tr_b16 a[64:65], v20 offset:2048\n"
"ds_read_b64_tr_b16 a[66:67], v20 offset:2304\n"
"ds_read_b64_tr_b16 a[96:97], v21 offset:2048\n"
"ds_read_b64_tr_b16 a[98:99], v21 offset:2304\n"
"s_waitcnt lgkmcnt(4)\n"
"s_add_u32 m0, s55, 0\n"
"s_add_u32 s60, s66, 65536\n"
"buffer_load_dwordx4 v24, s[44:47], s60 offen lds\n"
"s_add_u32 m0, s55, 1024\n"
"s_add_u32 s60, s66, 98304\n"
"buffer_load_dwordx4 v24, s[44:47], s60 offen lds\n"
"s_waitcnt vmcnt(36)\n"
"ds_read_b64_tr_b16 a[128:129], v20 offset:4096\n"
"ds_read_b64_tr_b16 a[130:131], v20 offset:4352\n"
"ds_read_b64_tr_b16 a[160:161], v21 offset:4096\n"
"ds_read_b64_tr_b16 a[162:163], v21 offset:4352\n"
"s_waitcnt lgkmcnt(4)\n"
"s_add_u32 m0, s55, 2048\n"
"s_add_u32 s60, s67, 65536\n"
"buffer_load_dwordx4 v24, s[44:47], s60 offen lds\n"
"s_add_u32 m0, s55, 3072\n"
"s_add_u32 s60, s67, 98304\n"
"buffer_load_dwordx4 v24, s[44:47], s60 offen lds\n"
"s_waitcnt vmcnt(36)\n"
"ds_read_b64_tr_b16 a[192:193], v20 offset:6144\n"
"ds_read_b64_tr_b16 a[194:195], v20 offset:6400\n"
"ds_read_b64_tr_b16 a[224:225], v21 offset:6144\n"
"ds_read_b64_tr_b16 a[226:227], v21 offset:6400\n"
"s_waitcnt lgkmcnt(4)\n"
"s_add_u32 m0, s55, 4096\n"
"s_add_u32 s60, s68, 65536\n"
"buffer_load_dwordx4 v24, s[44:47], s60 offen lds\n"
"s_add_u32 m0, s55, 5120\n"
"s_add_u32 s60, s68, 98304\n"
"buffer_load_dwordx4 v24, s[44:47], s60 offen lds\n"
"s_waitcnt vmcnt(36)\n"
"ds_read_b64_tr_b16 a[4:5], v20 offset:8192\n"
"ds_read_b64_tr_b16 a[6:7], v20 offset:8448\n"
"ds_read_b64_tr_b16 a[36:37], v21 offset:8192\n"
"ds_read_b64_tr_b16 a[38:39], v21 offset:8448\n"
"s_waitcnt lgkmcnt(4)\n"
"s_add_u32 m0, s55, 6144\n"
"s_add_u32 s60, s69, 65536\n"
"buffer_load_dwordx4 v24, s[44:47], s60 offen lds\n"
"s_add_u32 m0, s55, 7168\n"
"s_add_u32 s60, s69, 98304\n"
"buffer_load_dwordx4 v24, s[44:47], s60 offen lds\n"
"s_waitcnt vmcnt(36)\n"
"ds_read_b64_tr_b16 a[68:69], v20 offset:10240\n"
"ds_read_b64_tr_b16 a[70:71], v20 offset:10496\n"
"ds_read_b64_tr_b16 a[100:101], v21 offset:10240\n"
"ds_read_b64_tr_b16 a[102:103], v21 offset:10496\n"
"s_waitcnt lgkmcnt(4)\n"
"s_add_u32 m0, s55, 8192\n"
"s_add_u32 s60, s66, 0\n"
"buffer_load_dwordx4 v25, s[44:47], s60 offen lds\n"
"s_add_u32 m0, s55, 9216\n"
"s_add_u32 s60, s66, 32768\n"
"buffer_load_dwordx4 v25, s[44:47], s60 offen lds\n"
"s_waitcnt vmcnt(36)\n"
"ds_read_b64_tr_b16 a[132:133], v20 offset:12288\n"
"ds_read_b64_tr_b16 a[134:135], v20 offset:12544\n"
"ds_read_b64_tr_b16 a[164:165], v21 offset:12288\n"
"ds_read_b64_tr_b16 a[166:167], v21 offset:12544\n"
"s_waitcnt lgkmcnt(4)\n"
"s_add_u32 m0, s55, 10240\n"
"s_add_u32 s60, s67, 0\n"
"buffer_load_dwordx4 v25, s[44:47], s60 offen lds\n"
"s_add_u32 m0, s55, 11264\n"
"s_add_u32 s60, s67, 32768\n"
"buffer_load_dwordx4 v25, s[44:47], s60 offen lds\n"
"s_waitcnt vmcnt(36)\n"
"ds_read_b64_tr_b16 a[196:197], v20 offset:14336\n"
"ds_read_b64_tr_b16 a[198:199], v20 offset:14592\n"
"ds_read_b64_tr_b16 a[228:229], v21 offset:14336\n"
"ds_read_b64_tr_b16 a[230:231], v21 offset:14592\n"
"s_waitcnt lgkmcnt(4)\n"
"s_add_u32 m0, s55, 12288\n"
"s_add_u32 s60, s68, 0\n"
"buffer_load_dwordx4 v25, s[44:47], s60 offen lds\n"
"s_add_u32 m0, s55, 13312\n"
"s_add_u32 s60, s68, 32768\n"
"buffer_load_dwordx4 v25, s[44:47], s60 offen lds\n"
"s_waitcnt vmcnt(36)\n"
"ds_read_b64_tr_b16 a[8:9], v20 offset:16384\n"
"ds_read_b64_tr_b16 a[10:11], v20 offset:16640\n"
"ds_read_b64_tr_b16 a[40:41], v21 offset:16384\n"
"ds_read_b64_tr_b16 a[42:43], v21 offset:16640\n"
"s_waitcnt lgkmcnt(4)\n"
"s_add_u32 m0, s55, 14336\n"
"s_add_u32 s60, s69, 0\n"
"buffer_load_dwordx4 v25, s[44:47], s60 offen lds\n"
"s_add_u32 m0, s55, 15360\n"
"s_add_u32 s60, s69, 32768\n"
"buffer_load_dwordx4 v25, s[44:47], s60 offen lds\n"
"s_waitcnt vmcnt(36)\n"
"ds_read_b64_tr_b16 a[72:73], v20 offset:18432\n"
"ds_read_b64_tr_b16 a[74:75], v20 offset:18688\n"
"ds_read_b64_tr_b16 a[104:105], v21 offset:18432\n"
"ds_read_b64_tr_b16 a[106:107], v21 offset:18688\n"
"s_waitcnt lgkmcnt(4)\n"
"s_add_u32 m0, s55, 16384\n"
"s_add_u32 s60, s66, 65536\n"
"buffer_load_dwordx4 v25, s[44:47], s60 offen lds\n"
"s_add_u32 m0, s55, 17408\n"
"s_add_u32 s60, s66, 98304\n"
"buffer_load_dwordx4 v25, s[44:47], s60 offen lds\n"
"s_waitcnt vmcnt(36)\n"
"ds_read_b64_tr_b16 a[136:137], v20 offset:20480\n"
"ds_read_b64_tr_b16 a[138:139], v20 offset:20736\n"
"ds_read_b64_tr_b16 a[168:169], v21 offset:20480\n"
"ds_read_b64_tr_b16 a[170:171], v21 offset:20736\n"
"s_waitcnt lgkmcnt(4)\n"
"s_add_u32 m0, s55, 18432\n"
"s_add_u32 s60, s67, 65536\n"
"buffer_load_dwordx4 v25, s[44:47], s60 offen lds\n"
"s_add_u32 m0, s55, 19456\n"
"s_add_u32 s60, s67, 98304\n"
"buffer_load_dwordx4 v25, s[44:47], s60 offen lds\n"
"s_waitcnt vmcnt(36)\n"
"ds_read_b64_tr_b16 a[200:201], v20 offset:22528\n"
"ds_read_b64_tr_b16 a[202:203], v20 offset:22784\n"
"ds_read_b64_tr_b16 a[232:233], v21 offset:22528\n"
"ds_read_b64_tr_b16 a[234:235], v21 offset:22784\n"
"s_waitcnt lgkmcnt(4)\n"
"s_add_u32 m0, s55, 20480\n"
"s_add_u32 s60, s68, 65536\n"
"buffer_load_dwordx4 v25, s[44:47], s60 offen lds\n"
"s_add_u32 m0, s55, 21504\n"
"s_add_u32 s60, s68, 98304\n"
"buffer_load_dwordx4 v25, s[44:47], s60 offen lds\n"
"s_waitcnt vmcnt(36)\n"
"ds_read_b64_tr_b16 a[12:13], v20 offset:24576\n"
"ds_read_b64_tr_b16 a[14:15], v20 offset:24832\n"
"ds_read_b64_tr_b16 a[44:45], v21 offset:24576\n"
"ds_read_b64_tr_b16 a[46:47], v21 offset:24832\n"
"s_waitcnt lgkmcnt(4)\n"
"s_add_u32 m0, s55, 22528\n"
"s_add_u32 s60, s69, 65536\n"
"buffer_load_dwordx4 v25, s[44:47], s60 offen lds\n"
"s_add_u32 m0, s55, 23552\n"
"s_add_u32 s60, s69, 98304\n"
"buffer_load_dwordx4 v25, s[44:47], s60 offen lds\n"
"s_waitcnt vmcnt(36)\n"
"ds_read_b64_tr_b16 a[76:77], v20 offset:26624\n"
"ds_read_b64_tr_b16 a[78:79], v20 offset:26880\n"
"ds_read_b64_tr_b16 a[108:109], v21 offset:26624\n"
"ds_read_b64_tr_b16 a[110:111], v21 offset:26880\n"
"s_waitcnt lgkmcnt(4)\n"
"s_waitcnt vmcnt(34)\n"
"ds_read_b64_tr_b16 a[140:141], v20 offset:28672\n"
"ds_read_b64_tr_b16 a[142:143], v20 offset:28928\n"
"ds_read_b64_tr_b16 a[172:173], v21 offset:28672\n"
"ds_read_b64_tr_b16 a[174:175], v21 offset:28928\n"
"s_waitcnt lgkmcnt(4)\n"
"s_waitcnt vmcnt(32)\n"
"ds_read_b64_tr_b16 a[204:205], v20 offset:30720\n"
"ds_read_b64_tr_b16 a[206:207], v20 offset:30976\n"
"ds_read_b64_tr_b16 a[236:237], v21 offset:30720\n"
"ds_read_b64_tr_b16 a[238:239], v21 offset:30976\n"
"s_waitcnt lgkmcnt(4)\n"
"s_waitcnt vmcnt(30)\n"
"ds_read_b64_tr_b16 a[16:17], v20 offset:32768\n"
"ds_read_b64_tr_b16 a[18:19], v20 offset:33024\n"
"ds_read_b64_tr_b16 a[48:49], v21 offset:32768\n"
"ds_read_b64_tr_b16 a[50:51], v21 offset:33024\n"
"s_waitcnt lgkmcnt(4)\n"
"s_waitcnt vmcnt(28)\n"
"ds_read_b64_tr_b16 a[80:81], v20 offset:34816\n"
"ds_read_b64_tr_b16 a[82:83], v20 offset:35072\n"
"ds_read_b64_tr_b16 a[112:113], v21 offset:34816\n"
"ds_read_b64_tr_b16 a[114:115], v21 offset:35072\n"
"s_waitcnt lgkmcnt(4)\n"
"s_waitcnt vmcnt(26)\n"
"ds_read_b64_tr_b16 a[144:145], v20 offset:36864\n"
"ds_read_b64_tr_b16 a[146:147], v20 offset:37120\n"
"ds_read_b64_tr_b16 a[176:177], v21 offset:36864\n"
"ds_read_b64_tr_b16 a[178:179], v21 offset:37120\n"
"s_waitcnt lgkmcnt(4)\n"
"s_waitcnt vmcnt(24)\n"
"ds_read_b64_tr_b16 a[208:209], v20 offset:38912\n"
"ds_read_b64_tr_b16 a[210:211], v20 offset:39168\n"
"ds_read_b64_tr_b16 a[240:241], v21 offset:38912\n"
"ds_read_b64_tr_b16 a[242:243], v21 offset:39168\n"
"s_waitcnt lgkmcnt(4)\n"
"s_waitcnt vmcnt(22)\n"
"ds_read_b64_tr_b16 a[20:21], v20 offset:0\n"
"ds_read_b64_tr_b16 a[22:23], v20 offset:256\n"
"ds_read_b64_tr_b16 a[52:53], v21 offset:0\n"
"ds_read_b64_tr_b16 a[54:55], v21 offset:256\n"
"s_waitcnt lgkmcnt(4)\n"
"s_waitcnt vmcnt(20)\n"
"ds_read_b64_tr_b16 a[84:85], v20 offset:2048\n"
"ds_read_b64_tr_b16 a[86:87], v20 offset:2304\n"
"ds_read_b64_tr_b16 a[116:117], v21 offset:2048\n"
"ds_read_b64_tr_b16 a[118:119], v21 offset:2304\n"
"s_waitcnt lgkmcnt(4)\n"
"s_waitcnt vmcnt(18)\n"
"ds_read_b64_tr_b16 a[148:149], v20 offset:4096\n"
"ds_read_b64_tr_b16 a[150:151], v20 offset:4352\n"
"ds_read_b64_tr_b16 a[180:181], v21 offset:4096\n"
"ds_read_b64_tr_b16 a[182:183], v21 offset:4352\n"
"s_waitcnt lgkmcnt(4)\n"
"s_waitcnt vmcnt(16)\n"
"ds_read_b64_tr_b16 a[212:213], v20 offset:6144\n"
"ds_read_b64_tr_b16 a[214:215], v20 offset:6400\n"
"ds_read_b64_tr_b16 a[244:245], v21 offset:6144\n"
"ds_read_b64_tr_b16 a[246:247], v21 offset:6400\n"
"s_waitcnt lgkmcnt(4)\n"
"s_waitcnt vmcnt(14)\n"
"ds_read_b64_tr_b16 a[24:25], v20 offset:8192\n"
"ds_read_b64_tr_b16 a[26:27], v20 offset:8448\n"
"ds_read_b64_tr_b16 a[56:57], v21 offset:8192\n"
"ds_read_b64_tr_b16 a[58:59], v21 offset:8448\n"
"s_waitcnt lgkmcnt(4)\n"
"s_waitcnt vmcnt(12)\n"
"ds_read_b64_tr_b16 a[88:89], v20 offset:10240\n"
"ds_read_b64_tr_b16 a[90:91], v20 offset:10496\n"
"ds_read_b64_tr_b16 a[120:121], v21 offset:10240\n"
"ds_read_b64_tr_b16 a[122:123], v21 offset:10496\n"
"s_waitcnt lgkmcnt(4)\n"
"s_waitcnt vmcnt(10)\n"
"ds_read_b64_tr_b16 a[152:153], v20 offset:12288\n"
"ds_read_b64_tr_b16 a[154:155], v20 offset:12544\n"
"ds_read_b64_tr_b16 a[184:185], v21 offset:12288\n"
"ds_read_b64_tr_b16 a[186:187], v21 offset:12544\n"
"s_waitcnt lgkmcnt(4)\n"
"s_waitcnt vmcnt(8)\n"
"ds_read_b64_tr_b16 a[216:217], v20 offset:14336\n"
"ds_read_b64_tr_b16 a[218:219], v20 offset:14592\n"
"ds_read_b64_tr_b16 a[248:249], v21 offset:14336\n"
"ds_read_b64_tr_b16 a[250:251], v21 offset:14592\n"
"s_waitcnt lgkmcnt(4)\n"
"s_waitcnt vmcnt(6)\n"
"ds_read_b64_tr_b16 a[28:29], v20 offset:16384\n"
"ds_read_b64_tr_b16 a[30:31], v20 offset:16640\n"
"ds_read_b64_tr_b16 a[60:61], v21 offset:16384\n"
"ds_read_b64_tr_b16 a[62:63], v21 offset:16640\n"
"s_waitcnt lgkmcnt(4)\n"
"s_waitcnt vmcnt(4)\n"
"ds_read_b64_tr_b16 a[92:93], v20 offset:18432\n"
"ds_read_b64_tr_b16 a[94:95], v20 offset:18688\n"
"ds_read_b64_tr_b16 a[124:125], v21 offset:18432\n"
"ds_read_b64_tr_b16 a[126:127], v21 offset:18688\n"
"s_waitcnt lgkmcnt(4)\n"
"s_waitcnt vmcnt(2)\n"
"ds_read_b64_tr_b16 a[156:157], v20 offset:20480\n"
"ds_read_b64_tr_b16 a[158:159], v20 offset:20736\n"
"ds_read_b64_tr_b16 a[188:189], v21 offset:20480\n"
"ds_read_b64_tr_b16 a[190:191], v21 offset:20736\n"
"s_waitcnt lgkmcnt(4)\n"
"s_waitcnt vmcnt(0)\n"
"ds_read_b64_tr_b16 a[220:221], v20 offset:22528\n"
"ds_read_b64_tr_b16 a[222:223], v20 offset:22784\n"
"ds_read_b64_tr_b16 a[252:253], v21 offset:22528\n"
"ds_read_b64_tr_b16 a[254:255], v21 offset:22784\n"
"s_waitcnt lgkmcnt(4)\n"
"s_waitcnt lgkmcnt(0)\n"
"s_mov_b32 m0, s59\n"
"s_barrier\n"
"s_mov_b32 s94, 0x80000000\n"
"s_mov_b32 s93, 0x80000000\n"
"s_mov_b32 s95, 0x80000000\n"
"s_mov_b32 s93, 0x80000000\n"
"s_mov_b32 s93, 0x80000000\n"
"s_mov_b32 s94, 0x80000000\n"
"s_mov_b32 s95, 0x80000000\n"
"s_mov_b32 s94, 0x80000000\n"
"v_mov_b32 v124, 0\n"
"v_mov_b32 v125, 0\n"
"v_mov_b32 v120, 0\n"
"v_mov_b32 v121, 0\n"
"v_mov_b32 v124, 0\n"
"v_mov_b32 v125, 0\n"
"v_mov_b32 v120, 0\n"
"v_mov_b32 v121, 0\n"
"s_cmp_eq_u32 s54, 0\n"
"s_cbranch_scc1 .LO_1s\n"
"s_cmp_eq_u32 s54, 1\n"
"s_cbranch_scc1 .LO_2s\n"
"s_cmp_eq_u32 s54, 2\n"
"s_cbranch_scc1 .LO_3s\n"
"s_cmp_eq_u32 s54, 3\n"
"s_cbranch_scc1 .LO_4s\n"
".p2align 6\n"
".LO_top:\n"
"v_mfma_f32_16x16x32_bf16 v[40:43], v[84:87], a[0:3], 0\n"
"global_load_dwordx4 v[20:23], v6, s[56:57] offset:1024 sc1\n"
"v_mfma_f32_16x16x32_bf16 v[40:43], v[88:91], a[4:7], v[40:43]\n"
"s_waitcnt vmcnt(13)\n"
"v_mfma_f32_16x16x32_bf16 v[40:43], v[92:95], a[8:11], v[40:43]\n"
"s_waitcnt lgkmcnt(3)\n"
"v_add_f32 v72, v72, v44\n"
"v_add_f32 v73, v73, v45\n"
"v_mfma_f32_16x16x32_bf16 v[40:43], v[52:55], a[12:15], v[40:43]\n"
"s_waitcnt vmcnt(11)\n"
"v_add_f32 v74, v74, v46\n"
"v_add_f32 v75, v75, v47\n"
"v_mfma_f32_16x16x32_bf16 v[40:43], v[56:59], a[16:19], v[40:43]\n"
"v_add_f32 v72, v72, v48\n"
"v_add_f32 v73, v73, v49\n"
"v_mfma_f32_16x16x32_bf16 v[40:43], v[60:63], a[20:23], v[40:43]\n"
"s_waitcnt vmcnt(8)\n"
"global_load_dwordx4 v[24:27], v7, s[56:57] offset:0 sc1\n"
"v_add_f32 v74, v74, v50\n"
"v_mfma_f32_16x16x32_bf16 v[40:43], v[64:67], a[24:27], v[40:43]\n"
"v_add_f32 v75, v75, v51\n"
"v_add_f32 v72, v72, v112\n"
"v_mfma_f32_16x16x32_bf16 v[40:43], v[68:71], a[28:31], v[40:43]\n"
"v_add_f32 v73, v73, v113\n"
"v_add_f32 v74, v74, v114\n"
"v_mfma_f32_16x16x32_bf16 v[44:47], v[84:87], a[32:35], 0\n"
"global_load_dwordx4 v[28:31], v7, s[56:57] offset:1024 sc1\n"
"v_add_f32 v75, v75, v115\n"
"v_mfma_f32_16x16x32_bf16 v[44:47], v[88:91], a[36:39], v[44:47]\n"
"s_add_u32 s95, s50, 0xffd00000\n"
"s_and_b32 s94, s95, 0xfbffffff\n"
"ds_write_addtid_b32 v40 offset:0\n"
"v_mfma_f32_16x16x32_bf16 v[44:47], v[92:95], a[40:43], v[44:47]\n"
"ds_write_addtid_b32 v41 offset:4640\n"
"v_cvt_pk_bf16_f32 v48, v72, v73\n"
"v_mfma_f32_16x16x32_bf16 v[44:47], v[52:55], a[44:47], v[44:47]\n"
"ds_write_addtid_b32 v42 offset:9344\n"
"v_cvt_pk_bf16_f32 v49, v74, v75\n"
"v_mfma_f32_16x16x32_bf16 v[44:47], v[56:59], a[48:51], v[44:47]\n"
"s_waitcnt lgkmcnt(3)\n"
"ds_write_addtid_b32 v43 offset:13984\n"
"v_add_f32 v76, v76, v80\n"
"v_mfma_f32_16x16x32_bf16 v[44:47], v[60:63], a[52:55], v[44:47]\n"
"global_load_dwordx4 v[32:35], v8, s[56:57] offset:0 sc1\n"
"v_add_f32 v77, v77, v81\n"
"v_mfma_f32_16x16x32_bf16 v[44:47], v[64:67], a[56:59], v[44:47]\n"
"v_add_f32 v78, v78, v82\n"
"v_add_f32 v79, v79, v83\n"
"v_mfma_f32_16x16x32_bf16 v[44:47], v[68:71], a[60:63], v[44:47]\n"
"v_add_f32 v76, v76, v120\n"
"v_add_f32 v77, v77, v121\n"
"v_mfma_f32_16x16x32_bf16 v[40:43], v[84:87], a[64:67], 0\n"
"v_add_f32 v78, v78, v122\n"
"v_add_f32 v79, v79, v123\n"
"v_mfma_f32_16x16x32_bf16 v[40:43], v[88:91], a[68:71], v[40:43]\n"
"ds_write_addtid_b32 v44 offset:256\n"
"v_add_f32 v76, v76, v108\n"
"v_mfma_f32_16x16x32_bf16 v[40:43], v[92:95], a[72:75], v[40:43]\n"
"s_waitcnt vmcnt(9)\n"
"buffer_store_dwordx2 v[0:1], v12, s[40:43], s93 offen offset:0 sc1\n"
"ds_write_addtid_b32 v45 offset:4896\n"
"v_mfma_f32_16x16x32_bf16 v[40:43], v[52:55], a[76:79], v[40:43]\n"
"ds_write_addtid_b32 v46 offset:9600\n"
"v_add_f32 v77, v77, v109\n"
"v_mfma_f32_16x16x32_bf16 v[40:43], v[56:59], a[80:83], v[40:43]\n"
"s_waitcnt vmcnt(9)\n"
"ds_write_addtid_b32 v47 offset:14240\n"
"global_load_dwordx4 v[36:39], v8, s[56:57] offset:1024 sc1\n"
"v_mfma_f32_16x16x32_bf16 v[40:43], v[60:63], a[84:87], v[40:43]\n"
"v_add_f32 v78, v78, v110\n"
"v_add_f32 v79, v79, v111\n"
"v_mfma_f32_16x16x32_bf16 v[40:43], v[64:67], a[88:91], v[40:43]\n"
"v_cvt_pk_bf16_f32 v50, v76, v77\n"
"v_cvt_pk_bf16_f32 v51, v78, v79\n"
"v_mfma_f32_16x16x32_bf16 v[40:43], v[68:71], a[92:95], v[40:43]\n"
"v_lshlrev_b32 v72, 16, v116\n"
"v_and_b32 v73, 0xffff0000, v116\n"
"v_mfma_f32_16x16x32_bf16 v[44:47], v[84:87], a[96:99], 0\n"
"v_lshlrev_b32 v74, 16, v117\n"
"v_and_b32 v75, 0xffff0000, v117\n"
"v_mfma_f32_16x16x32_bf16 v[44:47], v[88:91], a[100:103], v[44:47]\n"
"ds_write_addtid_b32 v40 offset:512\n"
"v_lshlrev_b32 v76, 16, v118\n"
"v_mfma_f32_16x16x32_bf16 v[44:47], v[92:95], a[104:107], v[44:47]\n"
"ds_write_addtid_b32 v41 offset:5152\n"
"v_and_b32 v77, 0xffff0000, v118\n"
"v_mfma_f32_16x16x32_bf16 v[44:47], v[52:55], a[108:111], v[44:47]\n"
"ds_write_addtid_b32 v42 offset:9856\n"
"v_lshlrev_b32 v78, 16, v119\n"
"v_mfma_f32_16x16x32_bf16 v[44:47], v[56:59], a[112:115], v[44:47]\n"
"ds_write_addtid_b32 v43 offset:14496\n"
"v_and_b32 v79, 0xffff0000, v119\n"
"v_mfma_f32_16x16x32_bf16 v[44:47], v[60:63], a[116:119], v[44:47]\n"
"v_mfma_f32_16x16x32_bf16 v[44:47], v[64:67], a[120:123], v[44:47]\n"
"v_mfma_f32_16x16x32_bf16 v[44:47], v[68:71], a[124:127], v[44:47]\n"
"v_mfma_f32_16x16x32_bf16 v[40:43], v[84:87], a[128:131], 0\n"
"v_mfma_f32_16x16x32_bf16 v[40:43], v[88:91], a[132:135], v[40:43]\n"
"ds_write_addtid_b32 v44 offset:768\n"
"v_mfma_f32_16x16x32_bf16 v[40:43], v[92:95], a[136:139], v[40:43]\n"
"buffer_store_dwordx4 v[48:51], v11, s[40:43], s94 offen sc1\n"
"ds_write_addtid_b32 v45 offset:5408\n"
"v_mfma_f32_16x16x32_bf16 v[80:83], v[48:51], v[48:51], 0\n"
"v_mfma_f32_16x16x32_bf16 v[108:111], v[14:17], v[48:51], 0\n"
"v_mfma_f32_16x16x32_bf16 v[40:43], v[52:55], a[140:143], v[40:43]\n"
"ds_write_addtid_b32 v46 offset:10112\n"
"v_cndmask_b32_e64 v80, v80, v81, s[62:63]\n"
"v_mfma_f32_16x16x32_bf16 v[40:43], v[56:59], a[144:147], v[40:43]\n"
"ds_write_addtid_b32 v47 offset:14752\n"
"v_cndmask_b32_e64 v82, v82, v83, s[62:63]\n"
"v_mfma_f32_16x16x32_bf16 v[40:43], v[60:63], a[148:151], v[40:43]\n"
"global_load_dwordx4 v[112:115], v10, s[56:57] sc1\n"
"v_cndmask_b32_e64 v109, v80, v82, s[64:65]\n"
"v_mfma_f32_16x16x32_bf16 v[40:43], v[64:67], a[152:155], v[40:43]\n"
"buffer_store_dwordx2 v[108:109], v13, s[40:43], s94 offen offset:0 sc1\n"
"v_mfma_f32_16x16x32_bf16 v[40:43], v[68:71], a[156:159], v[40:43]\n"
"v_mfma_f32_16x16x32_bf16 v[48:51], v[84:87], a[160:163], 0\n"
"s_add_u32 s58, s50, 0x180000\n"
"v_mfma_f32_16x16x32_bf16 v[48:51], v[88:91], a[164:167], v[48:51]\n"
"s_and_b32 s58, s58, 0x3ffffff\n"
"ds_write_addtid_b32 v40 offset:1024\n"
"v_mfma_f32_16x16x32_bf16 v[48:51], v[92:95], a[168:171], v[48:51]\n"
"s_add_u32 s56, s40, s58\n"
"ds_write_addtid_b32 v41 offset:5664\n"
"v_mfma_f32_16x16x32_bf16 v[48:51], v[52:55], a[172:175], v[48:51]\n"
"s_addc_u32 s57, s41, 0\n"
"ds_write_addtid_b32 v42 offset:10368\n"
"v_mfma_f32_16x16x32_bf16 v[48:51], v[56:59], a[176:179], v[48:51]\n"
"s_waitcnt vmcnt(10)\n"
"ds_write_addtid_b32 v43 offset:15008\n"
"v_cmp_ne_u64 vcc, s[48:49], v[124:125]\n"
"v_mfma_f32_16x16x32_bf16 v[48:51], v[60:63], a[180:183], v[48:51]\n"
"v_mfma_f32_16x16x32_bf16 v[48:51], v[64:67], a[184:187], v[48:51]\n"
"s_cbranch_vccnz .LO_3s\n"
".LO_3r:\n"
"v_mfma_f32_16x16x32_bf16 v[48:51], v[68:71], a[188:191], v[48:51]\n"
"s_add_u32 s50, s50, 0x180000\n"
"global_load_dwordx4 v[40:43], v5, s[56:57] offset:0 sc1\n"
"v_mfma_f32_16x16x32_bf16 v[72:75], a[192:195], v[84:87], v[72:75]\n"
"v_mfma_f32_16x16x32_bf16 v[72:75], a[196:199], v[88:91], v[72:75]\n"
"ds_write_addtid_b32 v48 offset:1280\n"
"v_mfma_f32_16x16x32_bf16 v[72:75], a[200:203], v[92:95], v[72:75]\n"
"ds_write_addtid_b32 v49 offset:5920\n"
"v_mfma_f32_16x16x32_bf16 v[72:75], a[204:207], v[52:55], v[72:75]\n"
"ds_write_addtid_b32 v50 offset:10624\n"
"v_mfma_f32_16x16x32_bf16 v[72:75], a[208:211], v[56:59], v[72:75]\n"
"ds_write_addtid_b32 v51 offset:15264\n"
"global_load_dwordx4 v[44:47], v5, s[56:57] offset:1024 sc1\n"
"v_mfma_f32_16x16x32_bf16 v[72:75], a[212:215], v[60:63], v[72:75]\n"
"v_mfma_f32_16x16x32_bf16 v[72:75], a[216:219], v[64:67], v[72:75]\n"
"v_mfma_f32_16x16x32_bf16 v[72:75], a[220:223], v[68:71], v[72:75]\n"
"v_mfma_f32_16x16x32_bf16 v[76:79], a[224:227], v[84:87], v[76:79]\n"
"s_waitcnt lgkmcnt(0)\n"
"v_mfma_f32_16x16x32_bf16 v[76:79], a[228:231], v[88:91], v[76:79]\n"
"s_barrier\n"
"v_mfma_f32_16x16x32_bf16 v[76:79], a[232:235], v[92:95], v[76:79]\n"
"ds_read_b128 v[80:83], v2 offset:1536\n"
"ds_read_b128 v[84:87], v3 offset:512\n"
"v_mfma_f32_16x16x32_bf16 v[76:79], a[236:239], v[52:55], v[76:79]\n"
"buffer_load_dwordx2 v[120:121], v9, s[40:43], s50 offen sc1\n"
"v_mfma_f32_16x16x32_bf16 v[76:79], a[240:243], v[56:59], v[76:79]\n"
"global_load_dwordx4 v[48:51], v6, s[56:57] offset:0 sc1\n"
"ds_read_b128 v[88:91], v4 offset:2560\n"
"v_mfma_f32_16x16x32_bf16 v[76:79], a[244:247], v[60:63], v[76:79]\n"
"ds_read_b128 v[92:95], v2 offset:1792\n"
"ds_read_b128 v[108:111], v3 offset:768\n"
"v_mfma_f32_16x16x32_bf16 v[76:79], a[248:251], v[64:67], v[76:79]\n"
"global_load_dwordx4 v[52:55], v6, s[56:57] offset:1024 sc1\n"
"ds_read_b128 v[116:119], v4 offset:2816\n"
"v_mfma_f32_16x16x32_bf16 v[76:79], a[252:255], v[68:71], v[76:79]\n"
"v_mfma_f32_16x16x32_bf16 v[64:67], v[96:99], a[0:3], 0\n"
"v_mfma_f32_16x16x32_bf16 v[64:67], v[100:103], a[4:7], v[64:67]\n"
"s_waitcnt vmcnt(13)\n"
"v_mfma_f32_16x16x32_bf16 v[64:67], v[104:107], a[8:11], v[64:67]\n"
"s_waitcnt lgkmcnt(3)\n"
"v_add_f32 v80, v80, v84\n"
"v_add_f32 v81, v81, v85\n"
"v_mfma_f32_16x16x32_bf16 v[64:67], v[20:23], a[12:15], v[64:67]\n"
"s_waitcnt vmcnt(11)\n"
"global_load_dwordx4 v[56:59], v7, s[56:57] offset:0 sc1\n"
"v_add_f32 v82, v82, v86\n"
"v_mfma_f32_16x16x32_bf16 v[64:67], v[24:27], a[16:19], v[64:67]\n"
"v_add_f32 v83, v83, v87\n"
"v_add_f32 v80, v80, v88\n"
"v_mfma_f32_16x16x32_bf16 v[64:67], v[28:31], a[20:23], v[64:67]\n"
"s_waitcnt vmcnt(9)\n"
"v_add_f32 v81, v81, v89\n"
"v_add_f32 v82, v82, v90\n"
"v_mfma_f32_16x16x32_bf16 v[64:67], v[32:35], a[24:27], v[64:67]\n"
"v_add_f32 v83, v83, v91\n"
"v_add_f32 v80, v80, v72\n"
"v_mfma_f32_16x16x32_bf16 v[64:67], v[36:39], a[28:31], v[64:67]\n"
"v_add_f32 v81, v81, v73\n"
"v_add_f32 v82, v82, v74\n"
"v_mfma_f32_16x16x32_bf16 v[84:87], v[96:99], a[32:35], 0\n"
"v_add_f32 v83, v83, v75\n"
"v_mfma_f32_16x16x32_bf16 v[84:87], v[100:103], a[36:39], v[84:87]\n"
"s_add_u32 s94, s50, 0xffd00000\n"
"s_and_b32 s93, s94, 0xfbffffff\n"
"ds_write_addtid_b32 v64 offset:1536\n"
"v_mfma_f32_16x16x32_bf16 v[84:87], v[104:107], a[40:43], v[84:87]\n"
"ds_write_addtid_b32 v65 offset:6176\n"
"v_cvt_pk_bf16_f32 v72, v80, v81\n"
"v_mfma_f32_16x16x32_bf16 v[84:87], v[20:23], a[44:47], v[84:87]\n"
"ds_write_addtid_b32 v66 offset:10880\n"
"global_load_dwordx4 v[60:63], v7, s[56:57] offset:1024 sc1\n"
"v_mfma_f32_16x16x32_bf16 v[84:87], v[24:27], a[48:51], v[84:87]\n"
"ds_write_addtid_b32 v67 offset:15520\n"
"v_cvt_pk_bf16_f32 v73, v82, v83\n"
"v_mfma_f32_16x16x32_bf16 v[84:87], v[28:31], a[52:55], v[84:87]\n"
"s_waitcnt lgkmcnt(4)\n"
"v_add_f32 v92, v92, v108\n"
"v_add_f32 v93, v93, v109\n"
"v_mfma_f32_16x16x32_bf16 v[84:87], v[32:35], a[56:59], v[84:87]\n"
"v_add_f32 v94, v94, v110\n"
"v_add_f32 v95, v95, v111\n"
"v_mfma_f32_16x16x32_bf16 v[84:87], v[36:39], a[60:63], v[84:87]\n"
"v_add_f32 v92, v92, v116\n"
"v_add_f32 v93, v93, v117\n"
"v_mfma_f32_16x16x32_bf16 v[80:83], v[96:99], a[64:67], 0\n"
"global_load_dwordx4 v[64:67], v8, s[56:57] offset:0 sc1\n"
"v_add_f32 v94, v94, v118\n"
"v_mfma_f32_16x16x32_bf16 v[80:83], v[100:103], a[68:71], v[80:83]\n"
"ds_write_addtid_b32 v84 offset:1792\n"
"global_load_dwordx4 v[68:71], v8, s[56:57] offset:1024 sc1\n"
"v_mfma_f32_16x16x32_bf16 v[80:83], v[104:107], a[72:75], v[80:83]\n"
"ds_write_addtid_b32 v85 offset:6432\n"
"v_add_f32 v95, v95, v119\n"
"v_mfma_f32_16x16x32_bf16 v[80:83], v[20:23], a[76:79], v[80:83]\n"
"ds_write_addtid_b32 v86 offset:11136\n"
"v_add_f32 v92, v92, v76\n"
"v_mfma_f32_16x16x32_bf16 v[80:83], v[24:27], a[80:83], v[80:83]\n"
"ds_write_addtid_b32 v87 offset:15776\n"
"v_add_f32 v93, v93, v77\n"
"v_mfma_f32_16x16x32_bf16 v[80:83], v[28:31], a[84:87], v[80:83]\n"
"s_waitcnt vmcnt(10)\n"
"v_add_f32 v94, v94, v78\n"
"v_add_f32 v95, v95, v79\n"
"v_mfma_f32_16x16x32_bf16 v[80:83], v[32:35], a[88:91], v[80:83]\n"
"s_waitcnt vmcnt(9)\n"
"v_cvt_pk_bf16_f32 v74, v92, v93\n"
"v_cvt_pk_bf16_f32 v75, v94, v95\n"
"v_mfma_f32_16x16x32_bf16 v[80:83], v[36:39], a[92:95], v[80:83]\n"
"buffer_store_dwordx4 v[72:75], v11, s[40:43], s93 offen sc1\n"
"v_lshlrev_b32 v84, 16, v112\n"
"v_and_b32 v85, 0xffff0000, v112\n"
"v_lshlrev_b32 v86, 16, v113\n"
"v_mfma_f32_16x16x32_bf16 v[76:79], v[72:75], v[72:75], 0\n"
"buffer_store_dwordx2 v[0:1], v12, s[40:43], s95 offen offset:0 sc1\n"
"v_and_b32 v87, 0xffff0000, v113\n"
"v_mfma_f32_16x16x32_bf16 v[88:91], v[14:17], v[72:75], 0\n"
"v_lshlrev_b32 v92, 16, v114\n"
"v_and_b32 v93, 0xffff0000, v114\n"
"v_mfma_f32_16x16x32_bf16 v[108:111], v[96:99], a[96:99], 0\n"
"v_cndmask_b32_e64 v76, v76, v77, s[62:63]\n"
"v_cndmask_b32_e64 v78, v78, v79, s[62:63]\n"
"v_mfma_f32_16x16x32_bf16 v[108:111], v[100:103], a[100:103], v[108:111]\n"
"ds_write_addtid_b32 v80 offset:2048\n"
"v_cndmask_b32_e64 v89, v76, v78, s[64:65]\n"
"v_mfma_f32_16x16x32_bf16 v[108:111], v[104:107], a[104:107], v[108:111]\n"
"buffer_store_dwordx2 v[88:89], v13, s[40:43], s93 offen offset:0 sc1\n"
"ds_write_addtid_b32 v81 offset:6688\n"
"v_mfma_f32_16x16x32_bf16 v[108:111], v[20:23], a[108:111], v[108:111]\n"
"ds_write_addtid_b32 v82 offset:11392\n"
"v_lshlrev_b32 v94, 16, v115\n"
"v_mfma_f32_16x16x32_bf16 v[108:111], v[24:27], a[112:115], v[108:111]\n"
"ds_write_addtid_b32 v83 offset:16032\n"
"v_and_b32 v95, 0xffff0000, v115\n"
"v_mfma_f32_16x16x32_bf16 v[108:111], v[28:31], a[116:119], v[108:111]\n"
"v_mfma_f32_16x16x32_bf16 v[108:111], v[32:35], a[120:123], v[108:111]\n"
"v_mfma_f32_16x16x32_bf16 v[108:111], v[36:39], a[124:127], v[108:111]\n"
"v_mfma_f32_16x16x32_bf16 v[72:75], v[96:99], a[128:131], 0\n"
"v_mfma_f32_16x16x32_bf16 v[72:75], v[100:103], a[132:135], v[72:75]\n"
"ds_write_addtid_b32 v108 offset:2304\n"
"v_mfma_f32_16x16x32_bf16 v[72:75], v[104:107], a[136:139], v[72:75]\n"
"ds_write_addtid_b32 v109 offset:6944\n"
"v_mfma_f32_16x16x32_bf16 v[72:75], v[20:23], a[140:143], v[72:75]\n"
"ds_write_addtid_b32 v110 offset:11648\n"
"v_mfma_f32_16x16x32_bf16 v[72:75], v[24:27], a[144:147], v[72:75]\n"
"ds_write_addtid_b32 v111 offset:16288\n"
"v_mfma_f32_16x16x32_bf16 v[72:75], v[28:31], a[148:151], v[72:75]\n"
"v_mfma_f32_16x16x32_bf16 v[72:75], v[32:35], a[152:155], v[72:75]\n"
"global_load_dwordx4 v[88:91], v10, s[56:57] sc1\n"
"v_mfma_f32_16x16x32_bf16 v[72:75], v[36:39], a[156:159], v[72:75]\n"
"v_mfma_f32_16x16x32_bf16 v[76:79], v[96:99], a[160:163], 0\n"
"v_mfma_f32_16x16x32_bf16 v[76:79], v[100:103], a[164:167], v[76:79]\n"
"ds_write_addtid_b32 v72 offset:2560\n"
"v_mfma_f32_16x16x32_bf16 v[76:79], v[104:107], a[168:171], v[76:79]\n"
"s_add_u32 s58, s50, 0x180000\n"
"ds_write_addtid_b32 v73 offset:7200\n"
"v_mfma_f32_16x16x32_bf16 v[76:79], v[20:23], a[172:175], v[76:79]\n"
"s_and_b32 s58, s58, 0x3ffffff\n"
"ds_write_addtid_b32 v74 offset:11904\n"
"v_mfma_f32_16x16x32_bf16 v[76:79], v[24:27], a[176:179], v[76:79]\n"
"s_add_u32 s56, s40, s58\n"
"ds_write_addtid_b32 v75 offset:16544\n"
"v_mfma_f32_16x16x32_bf16 v[76:79], v[28:31], a[180:183], v[76:79]\n"
"s_addc_u32 s57, s41, 0\n"
"v_mfma_f32_16x16x32_bf16 v[76:79], v[32:35], a[184:187], v[76:79]\n"
"s_waitcnt vmcnt(10)\n"
"v_cmp_ne_u64 vcc, s[48:49], v[120:121]\n"
"v_mfma_f32_16x16x32_bf16 v[76:79], v[36:39], a[188:191], v[76:79]\n"
"v_mfma_f32_16x16x32_bf16 v[84:87], a[192:195], v[96:99], v[84:87]\n"
"s_cbranch_vccnz .LO_4s\n"
".LO_4r:\n"
"v_mfma_f32_16x16x32_bf16 v[84:87], a[196:199], v[100:103], v[84:87]\n"
"s_add_u32 s50, s50, 0x180000\n"
"ds_write_addtid_b32 v76 offset:2816\n"
"v_mfma_f32_16x16x32_bf16 v[84:87], a[200:203], v[104:107], v[84:87]\n"
"ds_write_addtid_b32 v77 offset:7456\n"
"global_load_dwordx4 v[72:75], v5, s[56:57] offset:0 sc1\n"
"v_mfma_f32_16x16x32_bf16 v[84:87], a[204:207], v[20:23], v[84:87]\n"
"ds_write_addtid_b32 v78 offset:12160\n"
"v_mfma_f32_16x16x32_bf16 v[84:87], a[208:211], v[24:27], v[84:87]\n"
"ds_write_addtid_b32 v79 offset:16800\n"
"v_mfma_f32_16x16x32_bf16 v[84:87], a[212:215], v[28:31], v[84:87]\n"
"v_mfma_f32_16x16x32_bf16 v[84:87], a[216:219], v[32:35], v[84:87]\n"
"v_mfma_f32_16x16x32_bf16 v[84:87], a[220:223], v[36:39], v[84:87]\n"
"global_load_dwordx4 v[76:79], v5, s[56:57] offset:1024 sc1\n"
"v_mfma_f32_16x16x32_bf16 v[92:95], a[224:227], v[96:99], v[92:95]\n"
"s_waitcnt lgkmcnt(0)\n"
"global_load_dwordx4 v[80:83], v6, s[56:57] offset:0 sc1\n"
"v_mfma_f32_16x16x32_bf16 v[92:95], a[228:231], v[100:103], v[92:95]\n"
"s_barrier\n"
"v_mfma_f32_16x16x32_bf16 v[92:95], a[232:235], v[104:107], v[92:95]\n"
"ds_read_b128 v[96:99], v2 offset:0\n"
"ds_read_b128 v[100:103], v3 offset:2048\n"
"v_mfma_f32_16x16x32_bf16 v[92:95], a[236:239], v[20:23], v[92:95]\n"
"ds_read_b128 v[104:107], v4 offset:1024\n"
"ds_read_b128 v[108:111], v2 offset:256\n"
"v_mfma_f32_16x16x32_bf16 v[92:95], a[240:243], v[24:27], v[92:95]\n"
"ds_read_b128 v[112:115], v3 offset:2304\n"
"ds_read_b128 v[116:119], v4 offset:1280\n"
"v_mfma_f32_16x16x32_bf16 v[92:95], a[244:247], v[28:31], v[92:95]\n"
"buffer_load_dwordx2 v[124:125], v9, s[40:43], s50 offen sc1\n"
"v_mfma_f32_16x16x32_bf16 v[92:95], a[248:251], v[32:35], v[92:95]\n"
"v_mfma_f32_16x16x32_bf16 v[92:95], a[252:255], v[36:39], v[92:95]\n"
"v_mfma_f32_16x16x32_bf16 v[120:123], v[40:43], a[0:3], 0\n"
"v_mfma_f32_16x16x32_bf16 v[120:123], v[44:47], a[4:7], v[120:123]\n"
"s_waitcnt vmcnt(12)\n"
"global_load_dwordx4 v[20:23], v6, s[56:57] offset:1024 sc1\n"
"v_mfma_f32_16x16x32_bf16 v[120:123], v[48:51], a[8:11], v[120:123]\n"
"s_waitcnt lgkmcnt(3)\n"
"v_add_f32 v96, v96, v100\n"
"v_add_f32 v97, v97, v101\n"
"v_mfma_f32_16x16x32_bf16 v[120:123], v[52:55], a[12:15], v[120:123]\n"
"s_waitcnt vmcnt(11)\n"
"v_add_f32 v98, v98, v102\n"
"v_add_f32 v99, v99, v103\n"
"v_mfma_f32_16x16x32_bf16 v[120:123], v[56:59], a[16:19], v[120:123]\n"
"v_add_f32 v96, v96, v104\n"
"v_add_f32 v97, v97, v105\n"
"v_mfma_f32_16x16x32_bf16 v[120:123], v[60:63], a[20:23], v[120:123]\n"
"s_waitcnt vmcnt(9)\n"
"v_add_f32 v98, v98, v106\n"
"v_add_f32 v99, v99, v107\n"
"v_mfma_f32_16x16x32_bf16 v[120:123], v[64:67], a[24:27], v[120:123]\n"
"global_load_dwordx4 v[24:27], v7, s[56:57] offset:0 sc1\n"
"v_add_f32 v96, v96, v84\n"
"v_mfma_f32_16x16x32_bf16 v[120:123], v[68:71], a[28:31], v[120:123]\n"
"global_load_dwordx4 v[28:31], v7, s[56:57] offset:1024 sc1\n"
"v_add_f32 v97, v97, v85\n"
"v_mfma_f32_16x16x32_bf16 v[100:103], v[40:43], a[32:35], 0\n"
"v_add_f32 v98, v98, v86\n"
"v_add_f32 v99, v99, v87\n"
"v_mfma_f32_16x16x32_bf16 v[100:103], v[44:47], a[36:39], v[100:103]\n"
"s_add_u32 s95, s50, 0xffd00000\n"
"s_and_b32 s93, s95, 0xfbffffff\n"
"ds_write_addtid_b32 v120 offset:0\n"
"v_mfma_f32_16x16x32_bf16 v[100:103], v[48:51], a[40:43], v[100:103]\n"
"s_waitcnt vmcnt(8)\n"
"buffer_store_dwordx2 v[0:1], v12, s[40:43], s94 offen offset:0 sc1\n"
"ds_write_addtid_b32 v121 offset:4640\n"
"v_mfma_f32_16x16x32_bf16 v[100:103], v[52:55], a[44:47], v[100:103]\n"
"ds_write_addtid_b32 v122 offset:9344\n"
"v_cvt_pk_bf16_f32 v84, v96, v97\n"
"v_mfma_f32_16x16x32_bf16 v[100:103], v[56:59], a[48:51], v[100:103]\n"
"ds_write_addtid_b32 v123 offset:13984\n"
"global_load_dwordx4 v[32:35], v8, s[56:57] offset:0 sc1\n"
"v_mfma_f32_16x16x32_bf16 v[100:103], v[60:63], a[52:55], v[100:103]\n"
"v_cvt_pk_bf16_f32 v85, v98, v99\n"
"v_mfma_f32_16x16x32_bf16 v[100:103], v[64:67], a[56:59], v[100:103]\n"
"s_waitcnt lgkmcnt(4)\n"
"v_add_f32 v108, v108, v112\n"
"v_add_f32 v109, v109, v113\n"
"v_mfma_f32_16x16x32_bf16 v[100:103], v[68:71], a[60:63], v[100:103]\n"
"v_add_f32 v110, v110, v114\n"
"v_add_f32 v111, v111, v115\n"
"v_mfma_f32_16x16x32_bf16 v[96:99], v[40:43], a[64:67], 0\n"
"v_add_f32 v108, v108, v116\n"
"v_add_f32 v109, v109, v117\n"
"v_mfma_f32_16x16x32_bf16 v[96:99], v[44:47], a[68:71], v[96:99]\n"
"ds_write_addtid_b32 v100 offset:256\n"
"v_add_f32 v110, v110, v118\n"
"v_mfma_f32_16x16x32_bf16 v[96:99], v[48:51], a[72:75], v[96:99]\n"
"ds_write_addtid_b32 v101 offset:4896\n"
"v_add_f32 v111, v111, v119\n"
"v_mfma_f32_16x16x32_bf16 v[96:99], v[52:55], a[76:79], v[96:99]\n"
"ds_write_addtid_b32 v102 offset:9600\n"
"v_add_f32 v108, v108, v92\n"
"v_mfma_f32_16x16x32_bf16 v[96:99], v[56:59], a[80:83], v[96:99]\n"
"ds_write_addtid_b32 v103 offset:14240\n"
"v_add_f32 v109, v109, v93\n"
"v_mfma_f32_16x16x32_bf16 v[96:99], v[60:63], a[84:87], v[96:99]\n"
"global_load_dwordx4 v[36:39], v8, s[56:57] offset:1024 sc1\n"
"v_add_f32 v110, v110, v94\n"
"v_mfma_f32_16x16x32_bf16 v[96:99], v[64:67], a[88:91], v[96:99]\n"
"s_waitcnt vmcnt(10)\n"
"v_add_f32 v111, v111, v95\n"
"v_cvt_pk_bf16_f32 v86, v108, v109\n"
"v_mfma_f32_16x16x32_bf16 v[96:99], v[68:71], a[92:95], v[96:99]\n"
"v_cvt_pk_bf16_f32 v87, v110, v111\n"
"v_lshlrev_b32 v100, 16, v88\n"
"v_mfma_f32_16x16x32_bf16 v[92:95], v[40:43], a[96:99], 0\n"
"v_and_b32 v101, 0xffff0000, v88\n"
"v_lshlrev_b32 v102, 16, v89\n"
"v_mfma_f32_16x16x32_bf16 v[92:95], v[44:47], a[100:103], v[92:95]\n"
"ds_write_addtid_b32 v96 offset:512\n"
"v_and_b32 v103, 0xffff0000, v89\n"
"v_mfma_f32_16x16x32_bf16 v[92:95], v[48:51], a[104:107], v[92:95]\n"
"ds_write_addtid_b32 v97 offset:5152\n"
"v_lshlrev_b32 v104, 16, v90\n"
"v_mfma_f32_16x16x32_bf16 v[92:95], v[52:55], a[108:111], v[92:95]\n"
"buffer_store_dwordx4 v[84:87], v11, s[40:43], s93 offen sc1\n"
"ds_write_addtid_b32 v98 offset:9856\n"
"v_and_b32 v105, 0xffff0000, v90\n"
"v_lshlrev_b32 v106, 16, v91\n"
"v_mfma_f32_16x16x32_bf16 v[108:111], v[84:87], v[84:87], 0\n"
"v_and_b32 v107, 0xffff0000, v91\n"
"v_mfma_f32_16x16x32_bf16 v[112:115], v[14:17], v[84:87], 0\n"
"v_mfma_f32_16x16x32_bf16 v[92:95], v[56:59], a[112:115], v[92:95]\n"
"ds_write_addtid_b32 v99 offset:14496\n"
"v_cndmask_b32_e64 v108, v108, v109, s[62:63]\n"
"v_mfma_f32_16x16x32_bf16 v[92:95], v[60:63], a[116:119], v[92:95]\n"
"v_cndmask_b32_e64 v110, v110, v111, s[62:63]\n"
"v_cndmask_b32_e64 v113, v108, v110, s[64:65]\n"
"v_mfma_f32_16x16x32_bf16 v[92:95], v[64:67], a[120:123], v[92:95]\n"
"buffer_store_dwordx2 v[112:113], v13, s[40:43], s93 offen offset:0 sc1\n"
"v_mfma_f32_16x16x32_bf16 v[92:95], v[68:71], a[124:127], v[92:95]\n"
"v_mfma_f32_16x16x32_bf16 v[84:87], v[40:43], a[128:131], 0\n"
"v_mfma_f32_16x16x32_bf16 v[84:87], v[44:47], a[132:135], v[84:87]\n"
"ds_write_addtid_b32 v92 offset:768\n"
"v_mfma_f32_16x16x32_bf16 v[84:87], v[48:51], a[136:139], v[84:87]\n"
"ds_write_addtid_b32 v93 offset:5408\n"
"v_mfma_f32_16x16x32_bf16 v[84:87], v[52:55], a[140:143], v[84:87]\n"
"ds_write_addtid_b32 v94 offset:10112\n"
"v_mfma_f32_16x16x32_bf16 v[84:87], v[56:59], a[144:147], v[84:87]\n"
"ds_write_addtid_b32 v95 offset:14752\n"
"v_mfma_f32_16x16x32_bf16 v[84:87], v[60:63], a[148:151], v[84:87]\n"
"v_mfma_f32_16x16x32_bf16 v[84:87], v[64:67], a[152:155], v[84:87]\n"
"v_mfma_f32_16x16x32_bf16 v[84:87], v[68:71], a[156:159], v[84:87]\n"
"global_load_dwordx4 v[96:99], v10, s[56:57] sc1\n"
"v_mfma_f32_16x16x32_bf16 v[92:95], v[40:43], a[160:163], 0\n"
"v_mfma_f32_16x16x32_bf16 v[92:95], v[44:47], a[164:167], v[92:95]\n"
"s_add_u32 s58, s50, 0x180000\n"
"ds_write_addtid_b32 v84 offset:1024\n"
"v_mfma_f32_16x16x32_bf16 v[92:95], v[48:51], a[168:171], v[92:95]\n"
"s_and_b32 s58, s58, 0x3ffffff\n"
"ds_write_addtid_b32 v85 offset:5664\n"
"v_mfma_f32_16x16x32_bf16 v[92:95], v[52:55], a[172:175], v[92:95]\n"
"s_add_u32 s56, s40, s58\n"
"ds_write_addtid_b32 v86 offset:10368\n"
"v_mfma_f32_16x16x32_bf16 v[92:95], v[56:59], a[176:179], v[92:95]\n"
"s_addc_u32 s57, s41, 0\n"
"ds_write_addtid_b32 v87 offset:15008\n"
"v_mfma_f32_16x16x32_bf16 v[92:95], v[60:63], a[180:183], v[92:95]\n"
"s_waitcnt vmcnt(9)\n"
"v_cmp_ne_u64 vcc, s[48:49], v[124:125]\n"
"v_mfma_f32_16x16x32_bf16 v[92:95], v[64:67], a[184:187], v[92:95]\n"
"v_mfma_f32_16x16x32_bf16 v[92:95], v[68:71], a[188:191], v[92:95]\n"
"s_cbranch_vccnz .LO_1s\n"
".LO_1r:\n"
"v_mfma_f32_16x16x32_bf16 v[100:103], a[192:195], v[40:43], v[100:103]\n"
"s_add_u32 s50, s50, 0x180000\n"
"global_load_dwordx4 v[84:87], v5, s[56:57] offset:0 sc1\n"
"v_mfma_f32_16x16x32_bf16 v[100:103], a[196:199], v[44:47], v[100:103]\n"
"ds_write_addtid_b32 v92 offset:1280\n"
"v_mfma_f32_16x16x32_bf16 v[100:103], a[200:203], v[48:51], v[100:103]\n"
"ds_write_addtid_b32 v93 offset:5920\n"
"v_mfma_f32_16x16x32_bf16 v[100:103], a[204:207], v[52:55], v[100:103]\n"
"ds_write_addtid_b32 v94 offset:10624\n"
"v_mfma_f32_16x16x32_bf16 v[100:103], a[208:211], v[56:59], v[100:103]\n"
"ds_write_addtid_b32 v95 offset:15264\n"
"v_mfma_f32_16x16x32_bf16 v[100:103], a[212:215], v[60:63], v[100:103]\n"
"global_load_dwordx4 v[88:91], v5, s[56:57] offset:1024 sc1\n"
"v_mfma_f32_16x16x32_bf16 v[100:103], a[216:219], v[64:67], v[100:103]\n"
"v_mfma_f32_16x16x32_bf16 v[100:103], a[220:223], v[68:71], v[100:103]\n"
"v_mfma_f32_16x16x32_bf16 v[104:107], a[224:227], v[40:43], v[104:107]\n"
"s_waitcnt lgkmcnt(0)\n"
"v_mfma_f32_16x16x32_bf16 v[104:107], a[228:231], v[44:47], v[104:107]\n"
"s_barrier\n"
"v_mfma_f32_16x16x32_bf16 v[104:107], a[232:235], v[48:51], v[104:107]\n"
"global_load_dwordx4 v[92:95], v6, s[56:57] offset:0 sc1\n"
"ds_read_b128 v[40:43], v2 offset:1536\n"
"v_mfma_f32_16x16x32_bf16 v[104:107], a[236:239], v[52:55], v[104:107]\n"
"ds_read_b128 v[44:47], v3 offset:512\n"
"ds_read_b128 v[48:51], v4 offset:2560\n"
"v_mfma_f32_16x16x32_bf16 v[104:107], a[240:243], v[56:59], v[104:107]\n"
"ds_read_b128 v[108:111], v2 offset:1792\n"
"ds_read_b128 v[112:115], v3 offset:768\n"
"v_mfma_f32_16x16x32_bf16 v[104:107], a[244:247], v[60:63], v[104:107]\n"
"ds_read_b128 v[116:119], v4 offset:2816\n"
"v_mfma_f32_16x16x32_bf16 v[104:107], a[248:251], v[64:67], v[104:107]\n"
"buffer_load_dwordx2 v[120:121], v9, s[40:43], s50 offen sc1\n"
"v_mfma_f32_16x16x32_bf16 v[104:107], a[252:255], v[68:71], v[104:107]\n"
"global_load_dwordx4 v[52:55], v6, s[56:57] offset:1024 sc1\n"
"v_mfma_f32_16x16x32_bf16 v[64:67], v[72:75], a[0:3], 0\n"
"v_mfma_f32_16x16x32_bf16 v[64:67], v[76:79], a[4:7], v[64:67]\n"
"s_waitcnt vmcnt(13)\n"
"v_mfma_f32_16x16x32_bf16 v[64:67], v[80:83], a[8:11], v[64:67]\n"
"s_waitcnt lgkmcnt(3)\n"
"v_add_f32 v40, v40, v44\n"
"v_add_f32 v41, v41, v45\n"
"v_mfma_f32_16x16x32_bf16 v[64:67], v[20:23], a[12:15], v[64:67]\n"
"s_waitcnt vmcnt(11)\n"
"v_add_f32 v42, v42, v46\n"
"v_add_f32 v43, v43, v47\n"
"v_mfma_f32_16x16x32_bf16 v[64:67], v[24:27], a[16:19], v[64:67]\n"
"global_load_dwordx4 v[56:59], v7, s[56:57] offset:0 sc1\n"
"v_add_f32 v40, v40, v48\n"
"v_mfma_f32_16x16x32_bf16 v[64:67], v[28:31], a[20:23], v[64:67]\n"
"s_waitcnt vmcnt(9)\n"
"v_add_f32 v41, v41, v49\n"
"v_add_f32 v42, v42, v50\n"
"v_mfma_f32_16x16x32_bf16 v[64:67], v[32:35], a[24:27], v[64:67]\n"
"v_add_f32 v43, v43, v51\n"
"v_add_f32 v40, v40, v100\n"
"v_mfma_f32_16x16x32_bf16 v[64:67], v[36:39], a[28:31], v[64:67]\n"
"v_add_f32 v41, v41, v101\n"
"v_add_f32 v42, v42, v102\n"
"v_mfma_f32_16x16x32_bf16 v[44:47], v[72:75], a[32:35], 0\n"
"v_add_f32 v43, v43, v103\n"
"v_mfma_f32_16x16x32_bf16 v[44:47], v[76:79], a[36:39], v[44:47]\n"
"ds_write_addtid_b32 v64 offset:1536\n"
"global_load_dwordx4 v[60:63], v7, s[56:57] offset:1024 sc1\n"
"v_mfma_f32_16x16x32_bf16 v[44:47], v[80:83], a[40:43], v[44:47]\n"
"s_add_u32 s93, s50, 0xffd00000\n"
"s_and_b32 s94, s93, 0xfbffffff\n"
"ds_write_addtid_b32 v65 offset:6176\n"
"v_mfma_f32_16x16x32_bf16 v[44:47], v[20:23], a[44:47], v[44:47]\n"
"ds_write_addtid_b32 v66 offset:10880\n"
"v_cvt_pk_bf16_f32 v48, v40, v41\n"
"v_mfma_f32_16x16x32_bf16 v[44:47], v[24:27], a[48:51], v[44:47]\n"
"ds_write_addtid_b32 v67 offset:15520\n"
"v_cvt_pk_bf16_f32 v49, v42, v43\n"
"v_mfma_f32_16x16x32_bf16 v[44:47], v[28:31], a[52:55], v[44:47]\n"
"s_waitcnt lgkmcnt(4)\n"
"v_add_f32 v108, v108, v112\n"
"v_add_f32 v109, v109, v113\n"
"v_mfma_f32_16x16x32_bf16 v[44:47], v[32:35], a[56:59], v[44:47]\n"
"s_waitcnt vmcnt(8)\n"
"global_load_dwordx4 v[64:67], v8, s[56:57] offset:0 sc1\n"
"v_add_f32 v110, v110, v114\n"
"v_mfma_f32_16x16x32_bf16 v[44:47], v[36:39], a[60:63], v[44:47]\n"
"buffer_store_dwordx2 v[0:1], v12, s[40:43], s95 offen offset:0 sc1\n"
"v_add_f32 v111, v111, v115\n"
"v_mfma_f32_16x16x32_bf16 v[40:43], v[72:75], a[64:67], 0\n"
"v_add_f32 v108, v108, v116\n"
"v_add_f32 v109, v109, v117\n"
"v_mfma_f32_16x16x32_bf16 v[40:43], v[76:79], a[68:71], v[40:43]\n"
"ds_write_addtid_b32 v44 offset:1792\n"
"v_add_f32 v110, v110, v118\n"
"v_mfma_f32_16x16x32_bf16 v[40:43], v[80:83], a[72:75], v[40:43]\n"
"ds_write_addtid_b32 v45 offset:6432\n"
"v_add_f32 v111, v111, v119\n"
"v_mfma_f32_16x16x32_bf16 v[40:43], v[20:23], a[76:79], v[40:43]\n"
"ds_write_addtid_b32 v46 offset:11136\n"
"global_load_dwordx4 v[68:71], v8, s[56:57] offset:1024 sc1\n"
"v_mfma_f32_16x16x32_bf16 v[40:43], v[24:27], a[80:83], v[40:43]\n"
"ds_write_addtid_b32 v47 offset:15776\n"
"v_add_f32 v108, v108, v104\n"
"v_mfma_f32_16x16x32_bf16 v[40:43], v[28:31], a[84:87], v[40:43]\n"
"v_add_f32 v109, v109, v105\n"
"v_add_f32 v110, v110, v106\n"
"v_mfma_f32_16x16x32_bf16 v[40:43], v[32:35], a[88:91], v[40:43]\n"
"v_add_f32 v111, v111, v107\n"
"v_cvt_pk_bf16_f32 v50, v108, v109\n"
"v_mfma_f32_16x16x32_bf16 v[40:43], v[36:39], a[92:95], v[40:43]\n"
"s_waitcnt vmcnt(10)\n"
"v_cvt_pk_bf16_f32 v51, v110, v111\n"
"v_lshlrev_b32 v112, 16, v96\n"
"v_mfma_f32_16x16x32_bf16 v[44:47], v[72:75], a[96:99], 0\n"
"v_and_b32 v113, 0xffff0000, v96\n"
"v_lshlrev_b32 v114, 16, v97\n"
"v_mfma_f32_16x16x32_bf16 v[44:47], v[76:79], a[100:103], v[44:47]\n"
"ds_write_addtid_b32 v40 offset:2048\n"
"v_and_b32 v115, 0xffff0000, v97\n"
"v_mfma_f32_16x16x32_bf16 v[44:47], v[80:83], a[104:107], v[44:47]\n"
"ds_write_addtid_b32 v41 offset:6688\n"
"v_lshlrev_b32 v108, 16, v98\n"
"v_mfma_f32_16x16x32_bf16 v[44:47], v[20:23], a[108:111], v[44:47]\n"
"ds_write_addtid_b32 v42 offset:11392\n"
"v_and_b32 v109, 0xffff0000, v98\n"
"v_mfma_f32_16x16x32_bf16 v[44:47], v[24:27], a[112:115], v[44:47]\n"
"ds_write_addtid_b32 v43 offset:16032\n"
"v_lshlrev_b32 v110, 16, v99\n"
"v_mfma_f32_16x16x32_bf16 v[44:47], v[28:31], a[116:119], v[44:47]\n"
"v_and_b32 v111, 0xffff0000, v99\n"
"v_mfma_f32_16x16x32_bf16 v[44:47], v[32:35], a[120:123], v[44:47]\n"
"v_mfma_f32_16x16x32_bf16 v[44:47], v[36:39], a[124:127], v[44:47]\n"
"buffer_store_dwordx4 v[48:51], v11, s[40:43], s94 offen sc1\n"
"v_mfma_f32_16x16x32_bf16 v[40:43], v[48:51], v[48:51], 0\n"
"v_mfma_f32_16x16x32_bf16 v[96:99], v[14:17], v[48:51], 0\n"
"v_mfma_f32_16x16x32_bf16 v[100:103], v[72:75], a[128:131], 0\n"
"v_cndmask_b32_e64 v40, v40, v41, s[62:63]\n"
"v_cndmask_b32_e64 v42, v42, v43, s[62:63]\n"
"v_mfma_f32_16x16x32_bf16 v[100:103], v[76:79], a[132:135], v[100:103]\n"
"ds_write_addtid_b32 v44 offset:2304\n"
"v_cndmask_b32_e64 v97, v40, v42, s[64:65]\n"
"v_mfma_f32_16x16x32_bf16 v[100:103], v[80:83], a[136:139], v[100:103]\n"
"buffer_store_dwordx2 v[96:97], v13, s[40:43], s94 offen offset:0 sc1\n"
"ds_write_addtid_b32 v45 offset:6944\n"
"v_mfma_f32_16x16x32_bf16 v[100:103], v[20:23], a[140:143], v[100:103]\n"
"ds_write_addtid_b32 v46 offset:11648\n"
"v_mfma_f32_16x16x32_bf16 v[100:103], v[24:27], a[144:147], v[100:103]\n"
"ds_write_addtid_b32 v47 offset:16288\n"
"global_load_dwordx4 v[116:119], v10, s[56:57] sc1\n"
"v_mfma_f32_16x16x32_bf16 v[100:103], v[28:31], a[148:151], v[100:103]\n"
"v_mfma_f32_16x16x32_bf16 v[100:103], v[32:35], a[152:155], v[100:103]\n"
"v_mfma_f32_16x16x32_bf16 v[100:103], v[36:39], a[156:159], v[100:103]\n"
"v_mfma_f32_16x16x32_bf16 v[40:43], v[72:75], a[160:163], 0\n"
"v_mfma_f32_16x16x32_bf16 v[40:43], v[76:79], a[164:167], v[40:43]\n"
"ds_write_addtid_b32 v100 offset:2560\n"
"v_mfma_f32_16x16x32_bf16 v[40:43], v[80:83], a[168:171], v[40:43]\n"
"s_add_u32 s58, s50, 0x180000\n"
"ds_write_addtid_b32 v101 offset:7200\n"
"v_mfma_f32_16x16x32_bf16 v[40:43], v[20:23], a[172:175], v[40:43]\n"
"s_and_b32 s58, s58, 0x3ffffff\n"
"ds_write_addtid_b32 v102 offset:11904\n"
"v_mfma_f32_16x16x32_bf16 v[40:43], v[24:27], a[176:179], v[40:43]\n"
"s_add_u32 s56, s40, s58\n"
"ds_write_addtid_b32 v103 offset:16544\n"
"v_mfma_f32_16x16x32_bf16 v[40:43], v[28:31], a[180:183], v[40:43]\n"
"s_addc_u32 s57, s41, 0\n"
"v_mfma_f32_16x16x32_bf16 v[40:43], v[32:35], a[184:187], v[40:43]\n"
"s_waitcnt vmcnt(9)\n"
"v_cmp_ne_u64 vcc, s[48:49], v[120:121]\n"
"v_mfma_f32_16x16x32_bf16 v[40:43], v[36:39], a[188:191], v[40:43]\n"
"v_mfma_f32_16x16x32_bf16 v[112:115], a[192:195], v[72:75], v[112:115]\n"
"s_cbranch_vccnz .LO_2s\n"
".LO_2r:\n"
"v_mfma_f32_16x16x32_bf16 v[112:115], a[196:199], v[76:79], v[112:115]\n"
"s_add_u32 s50, s50, 0x180000\n"
"ds_write_addtid_b32 v40 offset:2816\n"
"global_load_dwordx4 v[96:99], v5, s[56:57] offset:0 sc1\n"
"v_mfma_f32_16x16x32_bf16 v[112:115], a[200:203], v[80:83], v[112:115]\n"
"ds_write_addtid_b32 v41 offset:7456\n"
"v_mfma_f32_16x16x32_bf16 v[112:115], a[204:207], v[20:23], v[112:115]\n"
"ds_write_addtid_b32 v42 offset:12160\n"
"v_mfma_f32_16x16x32_bf16 v[112:115], a[208:211], v[24:27], v[112:115]\n"
"ds_write_addtid_b32 v43 offset:16800\n"
"v_mfma_f32_16x16x32_bf16 v[112:115], a[212:215], v[28:31], v[112:115]\n"
"v_mfma_f32_16x16x32_bf16 v[112:115], a[216:219], v[32:35], v[112:115]\n"
"global_load_dwordx4 v[100:103], v5, s[56:57] offset:1024 sc1\n"
"v_mfma_f32_16x16x32_bf16 v[112:115], a[220:223], v[36:39], v[112:115]\n"
"v_mfma_f32_16x16x32_bf16 v[108:111], a[224:227], v[72:75], v[108:111]\n"
"s_waitcnt lgkmcnt(0)\n"
"v_mfma_f32_16x16x32_bf16 v[108:111], a[228:231], v[76:79], v[108:111]\n"
"s_barrier\n"
"v_mfma_f32_16x16x32_bf16 v[108:111], a[232:235], v[80:83], v[108:111]\n"
"buffer_load_dwordx2 v[124:125], v9, s[40:43], s50 offen sc1\n"
"v_mfma_f32_16x16x32_bf16 v[108:111], a[236:239], v[20:23], v[108:111]\n"
"global_load_dwordx4 v[104:107], v6, s[56:57] offset:0 sc1\n"
"ds_read_b128 v[72:75], v2 offset:0\n"
"v_mfma_f32_16x16x32_bf16 v[108:111], a[240:243], v[24:27], v[108:111]\n"
"ds_read_b128 v[44:47], v3 offset:2048\n"
"ds_read_b128 v[48:51], v4 offset:1024\n"
"v_mfma_f32_16x16x32_bf16 v[108:111], a[244:247], v[28:31], v[108:111]\n"
"ds_read_b128 v[76:79], v2 offset:256\n"
"ds_read_b128 v[80:83], v3 offset:2304\n"
"v_mfma_f32_16x16x32_bf16 v[108:111], a[248:251], v[32:35], v[108:111]\n"
"ds_read_b128 v[120:123], v4 offset:1280\n"
"v_mfma_f32_16x16x32_bf16 v[108:111], a[252:255], v[36:39], v[108:111]\n"
"s_branch .LO_top\n"
".LO_1s:\n"
"s_cmp_ge_u32 s50, s53\n"
"s_cbranch_scc1 .LO_t0\n"
"s_cmp_ge_u32 s50, s52\n"
"s_cbranch_scc1 .LO_1r\n"
"s_sub_u32 s60, s50, 0x0\n"
"s_mov_b32 s61, 200000\n"
"s_nop 7\n"
".LO_1p:\n"
"buffer_load_dwordx2 v[124:125], v9, s[40:43], s60 offen sc1\n"
"s_waitcnt vmcnt(0)\n"
"v_cmp_ne_u64 vcc, s[48:49], v[124:125]\n"
"s_cbranch_vccz .LO_1px\n"
"s_sleep 1\n"
"s_sub_u32 s61, s61, 1\n"
"s_cbranch_scc0 .LO_1p\n"
"s_branch .LO_end\n"
".LO_1px:\n"
"s_branch .LO_1r\n"
".LO_2s:\n"
"s_cmp_ge_u32 s50, s53\n"
"s_cbranch_scc1 .LO_t1\n"
"s_cmp_ge_u32 s50, s52\n"
"s_cbranch_scc1 .LO_2r\n"
"s_sub_u32 s60, s50, 0x0\n"
"s_mov_b32 s61, 200000\n"
"s_nop 7\n"
".LO_2p:\n"
"buffer_load_dwordx2 v[120:121], v9, s[40:43], s60 offen sc1\n"
"s_waitcnt vmcnt(0)\n"
"v_cmp_ne_u64 vcc, s[48:49], v[120:121]\n"
"s_cbranch_vccz .LO_2px\n"
"s_sleep 1\n"
"s_sub_u32 s61, s61, 1\n"
"s_cbranch_scc0 .LO_2p\n"
"s_branch .LO_end\n"
".LO_2px:\n"
"s_branch .LO_2r\n"
".LO_3s:\n"
"s_cmp_ge_u32 s50, s53\n"
"s_cbranch_scc1 .LO_t2\n"
"s_cmp_ge_u32 s50, s52\n"
"s_cbranch_scc1 .LO_3r\n"
"s_sub_u32 s60, s50, 0x0\n"
"s_mov_b32 s61, 200000\n"
"s_nop 7\n"
".LO_3p:\n"
"buffer_load_dwordx2 v[124:125], v9, s[40:43], s60 offen sc1\n"
"s_waitcnt vmcnt(0)\n"
"v_cmp_ne_u64 vcc, s[48:49], v[124:125]\n"
"s_cbranch_vccz .LO_3px\n"
"s_sleep 1\n"
"s_sub_u32 s61, s61, 1\n"
"s_cbranch_scc0 .LO_3p\n"
"s_branch .LO_end\n"
".LO_3px:\n"
"s_branch .LO_3r\n"
".LO_4s:\n"
"s_cmp_ge_u32 s50, s53\n"
"s_cbranch_scc1 .LO_t3\n"
"s_cmp_ge_u32 s50, s52\n"
"s_cbranch_scc1 .LO_4r\n"
"s_sub_u32 s60, s50, 0x0\n"
"s_mov_b32 s61, 200000\n"
"s_nop 7\n"
".LO_4p:\n"
"buffer_load_dwordx2 v[120:121], v9, s[40:43], s60 offen sc1\n"
"s_waitcnt vmcnt(0)\n"
"v_cmp_ne_u64 vcc, s[48:49], v[120:121]\n"
"s_cbranch_vccz .LO_4px\n"
"s_sleep 1\n"
"s_sub_u32 s61, s61, 1\n"
"s_cbranch_scc0 .LO_4p\n"
"s_branch .LO_end\n"
".LO_4px:\n"
"s_branch .LO_4r\n"
".LO_t0:\n"
"v_mfma_f32_16x16x32_bf16 v[100:103], a[192:195], v[40:43], v[100:103]\n"
"v_mfma_f32_16x16x32_bf16 v[100:103], a[196:199], v[44:47], v[100:103]\n"
"ds_write_addtid_b32 v92 offset:1280\n"
"v_mfma_f32_16x16x32_bf16 v[100:103], a[200:203], v[48:51], v[100:103]\n"
"ds_write_addtid_b32 v93 offset:5920\n"
"v_mfma_f32_16x16x32_bf16 v[100:103], a[204:207], v[52:55], v[100:103]\n"
"ds_write_addtid_b32 v94 offset:10624\n"
"v_mfma_f32_16x16x32_bf16 v[100:103], a[208:211], v[56:59], v[100:103]\n"
"ds_write_addtid_b32 v95 offset:15264\n"
"v_mfma_f32_16x16x32_bf16 v[100:103], a[212:215], v[60:63], v[100:103]\n"
"v_mfma_f32_16x16x32_bf16 v[100:103], a[216:219], v[64:67], v[100:103]\n"
"v_mfma_f32_16x16x32_bf16 v[100:103], a[220:223], v[68:71], v[100:103]\n"
"v_mfma_f32_16x16x32_bf16 v[104:107], a[224:227], v[40:43], v[104:107]\n"
"s_waitcnt lgkmcnt(0)\n"
"v_mfma_f32_16x16x32_bf16 v[104:107], a[228:231], v[44:47], v[104:107]\n"
"s_barrier\n"
"v_mfma_f32_16x16x32_bf16 v[104:107], a[232:235], v[48:51], v[104:107]\n"
"ds_read_b128 v[40:43], v2 offset:1536\n"
"v_mfma_f32_16x16x32_bf16 v[104:107], a[236:239], v[52:55], v[104:107]\n"
"ds_read_b128 v[44:47], v3 offset:512\n"
"ds_read_b128 v[48:51], v4 offset:2560\n"
"v_mfma_f32_16x16x32_bf16 v[104:107], a[240:243], v[56:59], v[104:107]\n"
"ds_read_b128 v[108:111], v2 offset:1792\n"
"ds_read_b128 v[112:115], v3 offset:768\n"
"v_mfma_f32_16x16x32_bf16 v[104:107], a[244:247], v[60:63], v[104:107]\n"
"ds_read_b128 v[116:119], v4 offset:2816\n"
"v_mfma_f32_16x16x32_bf16 v[104:107], a[248:251], v[64:67], v[104:107]\n"
"v_mfma_f32_16x16x32_bf16 v[104:107], a[252:255], v[68:71], v[104:107]\n"
"s_waitcnt vmcnt(11)\n"
"v_mfma_f32_16x16x32_bf16 v[64:67], v[72:75], a[0:3], 0\n"
"v_mfma_f32_16x16x32_bf16 v[64:67], v[76:79], a[4:7], v[64:67]\n"
"s_waitcnt vmcnt(8)\n"
"v_mfma_f32_16x16x32_bf16 v[64:67], v[80:83], a[8:11], v[64:67]\n"
"s_waitcnt lgkmcnt(3)\n"
"v_add_f32 v40, v40, v44\n"
"v_add_f32 v41, v41, v45\n"
"v_mfma_f32_16x16x32_bf16 v[64:67], v[20:23], a[12:15], v[64:67]\n"
"s_waitcnt vmcnt(6)\n"
"v_add_f32 v42, v42, v46\n"
"v_add_f32 v43, v43, v47\n"
"v_mfma_f32_16x16x32_bf16 v[64:67], v[24:27], a[16:19], v[64:67]\n"
"v_add_f32 v40, v40, v48\n"
"v_mfma_f32_16x16x32_bf16 v[64:67], v[28:31], a[20:23], v[64:67]\n"
"s_waitcnt vmcnt(3)\n"
"v_add_f32 v41, v41, v49\n"
"v_add_f32 v42, v42, v50\n"
"v_mfma_f32_16x16x32_bf16 v[64:67], v[32:35], a[24:27], v[64:67]\n"
"v_add_f32 v43, v43, v51\n"
"v_add_f32 v40, v40, v100\n"
"v_mfma_f32_16x16x32_bf16 v[64:67], v[36:39], a[28:31], v[64:67]\n"
"v_add_f32 v41, v41, v101\n"
"v_add_f32 v42, v42, v102\n"
"v_mfma_f32_16x16x32_bf16 v[44:47], v[72:75], a[32:35], 0\n"
"v_add_f32 v43, v43, v103\n"
"v_mfma_f32_16x16x32_bf16 v[44:47], v[76:79], a[36:39], v[44:47]\n"
"ds_write_addtid_b32 v64 offset:1536\n"
"v_mfma_f32_16x16x32_bf16 v[44:47], v[80:83], a[40:43], v[44:47]\n"
"s_add_u32 s93, s50, 0xffe80000\n"
"s_and_b32 s94, s93, 0xfbffffff\n"
"ds_write_addtid_b32 v65 offset:6176\n"
"v_mfma_f32_16x16x32_bf16 v[44:47], v[20:23], a[44:47], v[44:47]\n"
"ds_write_addtid_b32 v66 offset:10880\n"
"v_cvt_pk_bf16_f32 v48, v40, v41\n"
"v_mfma_f32_16x16x32_bf16 v[44:47], v[24:27], a[48:51], v[44:47]\n"
"ds_write_addtid_b32 v67 offset:15520\n"
"v_cvt_pk_bf16_f32 v49, v42, v43\n"
"v_mfma_f32_16x16x32_bf16 v[44:47], v[28:31], a[52:55], v[44:47]\n"
"s_waitcnt lgkmcnt(4)\n"
"v_add_f32 v108, v108, v112\n"
"v_add_f32 v109, v109, v113\n"
"v_mfma_f32_16x16x32_bf16 v[44:47], v[32:35], a[56:59], v[44:47]\n"
"s_waitcnt vmcnt(1)\n"
"v_add_f32 v110, v110, v114\n"
"v_mfma_f32_16x16x32_bf16 v[44:47], v[36:39], a[60:63], v[44:47]\n"
"buffer_store_dwordx2 v[0:1], v12, s[40:43], s95 offen offset:0 sc1\n"
"v_add_f32 v111, v111, v115\n"
"v_mfma_f32_16x16x32_bf16 v[40:43], v[72:75], a[64:67], 0\n"
"v_add_f32 v108, v108, v116\n"
"v_add_f32 v109, v109, v117\n"
"v_mfma_f32_16x16x32_bf16 v[40:43], v[76:79], a[68:71], v[40:43]\n"
"ds_write_addtid_b32 v44 offset:1792\n"
"v_add_f32 v110, v110, v118\n"
"v_mfma_f32_16x16x32_bf16 v[40:43], v[80:83], a[72:75], v[40:43]\n"
"ds_write_addtid_b32 v45 offset:6432\n"
"v_add_f32 v111, v111, v119\n"
"v_mfma_f32_16x16x32_bf16 v[40:43], v[20:23], a[76:79], v[40:43]\n"
"ds_write_addtid_b32 v46 offset:11136\n"
"v_mfma_f32_16x16x32_bf16 v[40:43], v[24:27], a[80:83], v[40:43]\n"
"ds_write_addtid_b32 v47 offset:15776\n"
"v_add_f32 v108, v108, v104\n"
"v_mfma_f32_16x16x32_bf16 v[40:43], v[28:31], a[84:87], v[40:43]\n"
"v_add_f32 v109, v109, v105\n"
"v_add_f32 v110, v110, v106\n"
"v_mfma_f32_16x16x32_bf16 v[40:43], v[32:35], a[88:91], v[40:43]\n"
"v_add_f32 v111, v111, v107\n"
"v_cvt_pk_bf16_f32 v50, v108, v109\n"
"v_mfma_f32_16x16x32_bf16 v[40:43], v[36:39], a[92:95], v[40:43]\n"
"s_waitcnt vmcnt(1)\n"
"v_cvt_pk_bf16_f32 v51, v110, v111\n"
"v_lshlrev_b32 v112, 16, v96\n"
"v_mfma_f32_16x16x32_bf16 v[44:47], v[72:75], a[96:99], 0\n"
"v_and_b32 v113, 0xffff0000, v96\n"
"v_lshlrev_b32 v114, 16, v97\n"
"v_mfma_f32_16x16x32_bf16 v[44:47], v[76:79], a[100:103], v[44:47]\n"
"ds_write_addtid_b32 v40 offset:2048\n"
"v_and_b32 v115, 0xffff0000, v97\n"
"v_mfma_f32_16x16x32_bf16 v[44:47], v[80:83], a[104:107], v[44:47]\n"
"ds_write_addtid_b32 v41 offset:6688\n"
"v_lshlrev_b32 v108, 16, v98\n"
"v_mfma_f32_16x16x32_bf16 v[44:47], v[20:23], a[108:111], v[44:47]\n"
"ds_write_addtid_b32 v42 offset:11392\n"
"v_and_b32 v109, 0xffff0000, v98\n"
"v_mfma_f32_16x16x32_bf16 v[44:47], v[24:27], a[112:115], v[44:47]\n"
"ds_write_addtid_b32 v43 offset:16032\n"
"v_lshlrev_b32 v110, 16, v99\n"
"v_mfma_f32_16x16x32_bf16 v[44:47], v[28:31], a[116:119], v[44:47]\n"
"v_and_b32 v111, 0xffff0000, v99\n"
"v_mfma_f32_16x16x32_bf16 v[44:47], v[32:35], a[120:123], v[44:47]\n"
"v_mfma_f32_16x16x32_bf16 v[44:47], v[36:39], a[124:127], v[44:47]\n"
"buffer_store_dwordx4 v[48:51], v11, s[40:43], s94 offen sc1\n"
"v_mfma_f32_16x16x32_bf16 v[40:43], v[48:51], v[48:51], 0\n"
"v_mfma_f32_16x16x32_bf16 v[96:99], v[14:17], v[48:51], 0\n"
"v_mfma_f32_16x16x32_bf16 v[100:103], v[72:75], a[128:131], 0\n"
"v_cndmask_b32_e64 v40, v40, v41, s[62:63]\n"
"v_cndmask_b32_e64 v42, v42, v43, s[62:63]\n"
"v_mfma_f32_16x16x32_bf16 v[100:103], v[76:79], a[132:135], v[100:103]\n"
"ds_write_addtid_b32 v44 offset:2304\n"
"v_cndmask_b32_e64 v97, v40, v42, s[64:65]\n"
"v_mfma_f32_16x16x32_bf16 v[100:103], v[80:83], a[136:139], v[100:103]\n"
"buffer_store_dwordx2 v[96:97], v13, s[40:43], s94 offen offset:0 sc1\n"
"ds_write_addtid_b32 v45 offset:6944\n"
"v_mfma_f32_16x16x32_bf16 v[100:103], v[20:23], a[140:143], v[100:103]\n"
"ds_write_addtid_b32 v46 offset:11648\n"
"v_mfma_f32_16x16x32_bf16 v[100:103], v[24:27], a[144:147], v[100:103]\n"
"ds_write_addtid_b32 v47 offset:16288\n"
"v_mfma_f32_16x16x32_bf16 v[100:103], v[28:31], a[148:151], v[100:103]\n"
"v_mfma_f32_16x16x32_bf16 v[100:103], v[32:35], a[152:155], v[100:103]\n"
"v_mfma_f32_16x16x32_bf16 v[100:103], v[36:39], a[156:159], v[100:103]\n"
"v_mfma_f32_16x16x32_bf16 v[40:43], v[72:75], a[160:163], 0\n"
"v_mfma_f32_16x16x32_bf16 v[40:43], v[76:79], a[164:167], v[40:43]\n"
"ds_write_addtid_b32 v100 offset:2560\n"
"v_mfma_f32_16x16x32_bf16 v[40:43], v[80:83], a[168:171], v[40:43]\n"
"ds_write_addtid_b32 v101 offset:7200\n"
"v_mfma_f32_16x16x32_bf16 v[40:43], v[20:23], a[172:175], v[40:43]\n"
"ds_write_addtid_b32 v102 offset:11904\n"
"v_mfma_f32_16x16x32_bf16 v[40:43], v[24:27], a[176:179], v[40:43]\n"
"ds_write_addtid_b32 v103 offset:16544\n"
"v_mfma_f32_16x16x32_bf16 v[40:43], v[28:31], a[180:183], v[40:43]\n"
"v_mfma_f32_16x16x32_bf16 v[40:43], v[32:35], a[184:187], v[40:43]\n"
"v_mfma_f32_16x16x32_bf16 v[40:43], v[36:39], a[188:191], v[40:43]\n"
"v_mfma_f32_16x16x32_bf16 v[112:115], a[192:195], v[72:75], v[112:115]\n"
"v_mfma_f32_16x16x32_bf16 v[112:115], a[196:199], v[76:79], v[112:115]\n"
"ds_write_addtid_b32 v40 offset:2816\n"
"v_mfma_f32_16x16x32_bf16 v[112:115], a[200:203], v[80:83], v[112:115]\n"
"ds_write_addtid_b32 v41 offset:7456\n"
"v_mfma_f32_16x16x32_bf16 v[112:115], a[204:207], v[20:23], v[112:115]\n"
"ds_write_addtid_b32 v42 offset:12160\n"
"v_mfma_f32_16x16x32_bf16 v[112:115], a[208:211], v[24:27], v[112:115]\n"
"ds_write_addtid_b32 v43 offset:16800\n"
"v_mfma_f32_16x16x32_bf16 v[112:115], a[212:215], v[28:31], v[112:115]\n"
"v_mfma_f32_16x16x32_bf16 v[112:115], a[216:219], v[32:35], v[112:115]\n"
"v_mfma_f32_16x16x32_bf16 v[112:115], a[220:223], v[36:39], v[112:115]\n"
"v_mfma_f32_16x16x32_bf16 v[108:111], a[224:227], v[72:75], v[108:111]\n"
"s_waitcnt lgkmcnt(0)\n"
"v_mfma_f32_16x16x32_bf16 v[108:111], a[228:231], v[76:79], v[108:111]\n"
"s_barrier\n"
"v_mfma_f32_16x16x32_bf16 v[108:111], a[232:235], v[80:83], v[108:111]\n"
"v_mfma_f32_16x16x32_bf16 v[108:111], a[236:239], v[20:23], v[108:111]\n"
"ds_read_b128 v[72:75], v2 offset:0\n"
"v_mfma_f32_16x16x32_bf16 v[108:111], a[240:243], v[24:27], v[108:111]\n"
"ds_read_b128 v[44:47], v3 offset:2048\n"
"ds_read_b128 v[48:51], v4 offset:1024\n"
"v_mfma_f32_16x16x32_bf16 v[108:111], a[244:247], v[28:31], v[108:111]\n"
"ds_read_b128 v[76:79], v2 offset:256\n"
"ds_read_b128 v[80:83], v3 offset:2304\n"
"v_mfma_f32_16x16x32_bf16 v[108:111], a[248:251], v[32:35], v[108:111]\n"
"ds_read_b128 v[120:123], v4 offset:1280\n"
"v_mfma_f32_16x16x32_bf16 v[108:111], a[252:255], v[36:39], v[108:111]\n"
"s_waitcnt lgkmcnt(3)\n"
"v_add_f32 v72, v72, v44\n"
"v_add_f32 v73, v73, v45\n"
"v_add_f32 v74, v74, v46\n"
"v_add_f32 v75, v75, v47\n"
"v_add_f32 v72, v72, v48\n"
"v_add_f32 v73, v73, v49\n"
"v_add_f32 v74, v74, v50\n"
"v_add_f32 v75, v75, v51\n"
"v_add_f32 v72, v72, v112\n"
"v_add_f32 v73, v73, v113\n"
"v_add_f32 v74, v74, v114\n"
"v_add_f32 v75, v75, v115\n"
"s_add_u32 s95, s50, 0x0\n"
"s_and_b32 s94, s95, 0xfbffffff\n"
"v_cvt_pk_bf16_f32 v48, v72, v73\n"
"v_cvt_pk_bf16_f32 v49, v74, v75\n"
"s_waitcnt lgkmcnt(0)\n"
"v_add_f32 v76, v76, v80\n"
"v_add_f32 v77, v77, v81\n"
"v_add_f32 v78, v78, v82\n"
"v_add_f32 v79, v79, v83\n"
"v_add_f32 v76, v76, v120\n"
"v_add_f32 v77, v77, v121\n"
"v_add_f32 v78, v78, v122\n"
"v_add_f32 v79, v79, v123\n"
"v_add_f32 v76, v76, v108\n"
"s_waitcnt vmcnt(0)\n"
"buffer_store_dwordx2 v[0:1], v12, s[40:43], s93 offen offset:0 sc1\n"
"v_add_f32 v77, v77, v109\n"
"v_add_f32 v78, v78, v110\n"
"v_add_f32 v79, v79, v111\n"
"v_cvt_pk_bf16_f32 v50, v76, v77\n"
"v_cvt_pk_bf16_f32 v51, v78, v79\n"
"buffer_store_dwordx4 v[48:51], v11, s[40:43], s94 offen sc1\n"
"s_nop 0\n"
"v_mfma_f32_16x16x32_bf16 v[80:83], v[48:51], v[48:51], 0\n"
"v_mfma_f32_16x16x32_bf16 v[108:111], v[14:17], v[48:51], 0\n"
"s_nop 6\n"
"v_cndmask_b32_e64 v80, v80, v81, s[62:63]\n"
"v_cndmask_b32_e64 v82, v82, v83, s[62:63]\n"
"v_cndmask_b32_e64 v109, v80, v82, s[64:65]\n"
"buffer_store_dwordx2 v[108:109], v13, s[40:43], s94 offen offset:0 sc1\n"
"s_waitcnt vmcnt(0)\n"
"buffer_store_dwordx2 v[0:1], v12, s[40:43], s95 offen offset:0 sc1\n"
"s_branch .LO_end\n"
".LO_t1:\n"
"v_mfma_f32_16x16x32_bf16 v[112:115], a[196:199], v[76:79], v[112:115]\n"
"ds_write_addtid_b32 v40 offset:2816\n"
"v_mfma_f32_16x16x32_bf16 v[112:115], a[200:203], v[80:83], v[112:115]\n"
"ds_write_addtid_b32 v41 offset:7456\n"
"v_mfma_f32_16x16x32_bf16 v[112:115], a[204:207], v[20:23], v[112:115]\n"
"ds_write_addtid_b32 v42 offset:12160\n"
"v_mfma_f32_16x16x32_bf16 v[112:115], a[208:211], v[24:27], v[112:115]\n"
"ds_write_addtid_b32 v43 offset:16800\n"
"v_mfma_f32_16x16x32_bf16 v[112:115], a[212:215], v[28:31], v[112:115]\n"
"v_mfma_f32_16x16x32_bf16 v[112:115], a[216:219], v[32:35], v[112:115]\n"
"v_mfma_f32_16x16x32_bf16 v[112:115], a[220:223], v[36:39], v[112:115]\n"
"v_mfma_f32_16x16x32_bf16 v[108:111], a[224:227], v[72:75], v[108:111]\n"
"s_waitcnt lgkmcnt(0)\n"
"v_mfma_f32_16x16x32_bf16 v[108:111], a[228:231], v[76:79], v[108:111]\n"
"s_barrier\n"
"v_mfma_f32_16x16x32_bf16 v[108:111], a[232:235], v[80:83], v[108:111]\n"
"v_mfma_f32_16x16x32_bf16 v[108:111], a[236:239], v[20:23], v[108:111]\n"
"ds_read_b128 v[72:75], v2 offset:0\n"
"v_mfma_f32_16x16x32_bf16 v[108:111], a[240:243], v[24:27], v[108:111]\n"
"ds_read_b128 v[44:47], v3 offset:2048\n"
"ds_read_b128 v[48:51], v4 offset:1024\n"
"v_mfma_f32_16x16x32_bf16 v[108:111], a[244:247], v[28:31], v[108:111]\n"
"ds_read_b128 v[76:79], v2 offset:256\n"
"ds_read_b128 v[80:83], v3 offset:2304\n"
"v_mfma_f32_16x16x32_bf16 v[108:111], a[248:251], v[32:35], v[108:111]\n"
"ds_read_b128 v[120:123], v4 offset:1280\n"
"v_mfma_f32_16x16x32_bf16 v[108:111], a[252:255], v[36:39], v[108:111]\n"
"s_waitcnt vmcnt(11)\n"
"v_mfma_f32_16x16x32_bf16 v[40:43], v[84:87], a[0:3], 0\n"
"v_mfma_f32_16x16x32_bf16 v[40:43], v[88:91], a[4:7], v[40:43]\n"
"s_waitcnt vmcnt(8)\n"
"v_mfma_f32_16x16x32_bf16 v[40:43], v[92:95], a[8:11], v[40:43]\n"
"s_waitcnt lgkmcnt(3)\n"
"v_add_f32 v72, v72, v44\n"
"v_add_f32 v73, v73, v45\n"
"v_mfma_f32_16x16x32_bf16 v[40:43], v[52:55], a[12:15], v[40:43]\n"
"s_waitcnt vmcnt(6)\n"
"v_add_f32 v74, v74, v46\n"
"v_add_f32 v75, v75, v47\n"
"v_mfma_f32_16x16x32_bf16 v[40:43], v[56:59], a[16:19], v[40:43]\n"
"v_add_f32 v72, v72, v48\n"
"v_add_f32 v73, v73, v49\n"
"v_mfma_f32_16x16x32_bf16 v[40:43], v[60:63], a[20:23], v[40:43]\n"
"s_waitcnt vmcnt(3)\n"
"v_add_f32 v74, v74, v50\n"
"v_mfma_f32_16x16x32_bf16 v[40:43], v[64:67], a[24:27], v[40:43]\n"
"v_add_f32 v75, v75, v51\n"
"v_add_f32 v72, v72, v112\n"
"v_mfma_f32_16x16x32_bf16 v[40:43], v[68:71], a[28:31], v[40:43]\n"
"v_add_f32 v73, v73, v113\n"
"v_add_f32 v74, v74, v114\n"
"v_mfma_f32_16x16x32_bf16 v[44:47], v[84:87], a[32:35], 0\n"
"v_add_f32 v75, v75, v115\n"
"v_mfma_f32_16x16x32_bf16 v[44:47], v[88:91], a[36:39], v[44:47]\n"
"s_add_u32 s95, s50, 0xffe80000\n"
"s_and_b32 s94, s95, 0xfbffffff\n"
"ds_write_addtid_b32 v40 offset:0\n"
"v_mfma_f32_16x16x32_bf16 v[44:47], v[92:95], a[40:43], v[44:47]\n"
"ds_write_addtid_b32 v41 offset:4640\n"
"v_cvt_pk_bf16_f32 v48, v72, v73\n"
"v_mfma_f32_16x16x32_bf16 v[44:47], v[52:55], a[44:47], v[44:47]\n"
"ds_write_addtid_b32 v42 offset:9344\n"
"v_cvt_pk_bf16_f32 v49, v74, v75\n"
"v_mfma_f32_16x16x32_bf16 v[44:47], v[56:59], a[48:51], v[44:47]\n"
"s_waitcnt lgkmcnt(3)\n"
"ds_write_addtid_b32 v43 offset:13984\n"
"v_add_f32 v76, v76, v80\n"
"v_mfma_f32_16x16x32_bf16 v[44:47], v[60:63], a[52:55], v[44:47]\n"
"v_add_f32 v77, v77, v81\n"
"v_mfma_f32_16x16x32_bf16 v[44:47], v[64:67], a[56:59], v[44:47]\n"
"v_add_f32 v78, v78, v82\n"
"v_add_f32 v79, v79, v83\n"
"v_mfma_f32_16x16x32_bf16 v[44:47], v[68:71], a[60:63], v[44:47]\n"
"v_add_f32 v76, v76, v120\n"
"v_add_f32 v77, v77, v121\n"
"v_mfma_f32_16x16x32_bf16 v[40:43], v[84:87], a[64:67], 0\n"
"v_add_f32 v78, v78, v122\n"
"v_add_f32 v79, v79, v123\n"
"v_mfma_f32_16x16x32_bf16 v[40:43], v[88:91], a[68:71], v[40:43]\n"
"ds_write_addtid_b32 v44 offset:256\n"
"v_add_f32 v76, v76, v108\n"
"v_mfma_f32_16x16x32_bf16 v[40:43], v[92:95], a[72:75], v[40:43]\n"
"s_waitcnt vmcnt(1)\n"
"buffer_store_dwordx2 v[0:1], v12, s[40:43], s93 offen offset:0 sc1\n"
"ds_write_addtid_b32 v45 offset:4896\n"
"v_mfma_f32_16x16x32_bf16 v[40:43], v[52:55], a[76:79], v[40:43]\n"
"ds_write_addtid_b32 v46 offset:9600\n"
"v_add_f32 v77, v77, v109\n"
"v_mfma_f32_16x16x32_bf16 v[40:43], v[56:59], a[80:83], v[40:43]\n"
"s_waitcnt vmcnt(1)\n"
"ds_write_addtid_b32 v47 offset:14240\n"
"v_mfma_f32_16x16x32_bf16 v[40:43], v[60:63], a[84:87], v[40:43]\n"
"v_add_f32 v78, v78, v110\n"
"v_add_f32 v79, v79, v111\n"
"v_mfma_f32_16x16x32_bf16 v[40:43], v[64:67], a[88:91], v[40:43]\n"
"v_cvt_pk_bf16_f32 v50, v76, v77\n"
"v_cvt_pk_bf16_f32 v51, v78, v79\n"
"v_mfma_f32_16x16x32_bf16 v[40:43], v[68:71], a[92:95], v[40:43]\n"
"v_lshlrev_b32 v72, 16, v116\n"
"v_and_b32 v73, 0xffff0000, v116\n"
"v_mfma_f32_16x16x32_bf16 v[44:47], v[84:87], a[96:99], 0\n"
"v_lshlrev_b32 v74, 16, v117\n"
"v_and_b32 v75, 0xffff0000, v117\n"
"v_mfma_f32_16x16x32_bf16 v[44:47], v[88:91], a[100:103], v[44:47]\n"
"ds_write_addtid_b32 v40 offset:512\n"
"v_lshlrev_b32 v76, 16, v118\n"
"v_mfma_f32_16x16x32_bf16 v[44:47], v[92:95], a[104:107], v[44:47]\n"
"ds_write_addtid_b32 v41 offset:5152\n"
"v_and_b32 v77, 0xffff0000, v118\n"
"v_mfma_f32_16x16x32_bf16 v[44:47], v[52:55], a[108:111], v[44:47]\n"
"ds_write_addtid_b32 v42 offset:9856\n"
"v_lshlrev_b32 v78, 16, v119\n"
"v_mfma_f32_16x16x32_bf16 v[44:47], v[56:59], a[112:115], v[44:47]\n"
"ds_write_addtid_b32 v43 offset:14496\n"
"v_and_b32 v79, 0xffff0000, v119\n"
"v_mfma_f32_16x16x32_bf16 v[44:47], v[60:63], a[116:119], v[44:47]\n"
"v_mfma_f32_16x16x32_bf16 v[44:47], v[64:67], a[120:123], v[44:47]\n"
"v_mfma_f32_16x16x32_bf16 v[44:47], v[68:71], a[124:127], v[44:47]\n"
"v_mfma_f32_16x16x32_bf16 v[40:43], v[84:87], a[128:131], 0\n"
"v_mfma_f32_16x16x32_bf16 v[40:43], v[88:91], a[132:135], v[40:43]\n"
"ds_write_addtid_b32 v44 offset:768\n"
"v_mfma_f32_16x16x32_bf16 v[40:43], v[92:95], a[136:139], v[40:43]\n"
"buffer_store_dwordx4 v[48:51], v11, s[40:43], s94 offen sc1\n"
"ds_write_addtid_b32 v45 offset:5408\n"
"v_mfma_f32_16x16x32_bf16 v[80:83], v[48:51], v[48:51], 0\n"
"v_mfma_f32_16x16x32_bf16 v[108:111], v[14:17], v[48:51], 0\n"
"v_mfma_f32_16x16x32_bf16 v[40:43], v[52:55], a[140:143], v[40:43]\n"
"ds_write_addtid_b32 v46 offset:10112\n"
"v_cndmask_b32_e64 v80, v80, v81, s[62:63]\n"
"v_mfma_f32_16x16x32_bf16 v[40:43], v[56:59], a[144:147], v[40:43]\n"
"ds_write_addtid_b32 v47 offset:14752\n"
"v_cndmask_b32_e64 v82, v82, v83, s[62:63]\n"
"v_mfma_f32_16x16x32_bf16 v[40:43], v[60:63], a[148:151], v[40:43]\n"
"v_cndmask_b32_e64 v109, v80, v82, s[64:65]\n"
"v_mfma_f32_16x16x32_bf16 v[40:43], v[64:67], a[152:155], v[40:43]\n"
"buffer_store_dwordx2 v[108:109], v13, s[40:43], s94 offen offset:0 sc1\n"
"v_mfma_f32_16x16x32_bf16 v[40:43], v[68:71], a[156:159], v[40:43]\n"
"v_mfma_f32_16x16x32_bf16 v[48:51], v[84:87], a[160:163], 0\n"
"v_mfma_f32_16x16x32_bf16 v[48:51], v[88:91], a[164:167], v[48:51]\n"
"ds_write_addtid_b32 v40 offset:1024\n"
"v_mfma_f32_16x16x32_bf16 v[48:51], v[92:95], a[168:171], v[48:51]\n"
"ds_write_addtid_b32 v41 offset:5664\n"
"v_mfma_f32_16x16x32_bf16 v[48:51], v[52:55], a[172:175], v[48:51]\n"
"ds_write_addtid_b32 v42 offset:10368\n"
"v_mfma_f32_16x16x32_bf16 v[48:51], v[56:59], a[176:179], v[48:51]\n"
"ds_write_addtid_b32 v43 offset:15008\n"
"v_mfma_f32_16x16x32_bf16 v[48:51], v[60:63], a[180:183], v[48:51]\n"
"v_mfma_f32_16x16x32_bf16 v[48:51], v[64:67], a[184:187], v[48:51]\n"
"v_mfma_f32_16x16x32_bf16 v[48:51], v[68:71], a[188:191], v[48:51]\n"
"v_mfma_f32_16x16x32_bf16 v[72:75], a[192:195], v[84:87], v[72:75]\n"
"v_mfma_f32_16x16x32_bf16 v[72:75], a[196:199], v[88:91], v[72:75]\n"
"ds_write_addtid_b32 v48 offset:1280\n"
"v_mfma_f32_16x16x32_bf16 v[72:75], a[200:203], v[92:95], v[72:75]\n"
"ds_write_addtid_b32 v49 offset:5920\n"
"v_mfma_f32_16x16x32_bf16 v[72:75], a[204:207], v[52:55], v[72:75]\n"
"ds_write_addtid_b32 v50 offset:10624\n"
"v_mfma_f32_16x16x32_bf16 v[72:75], a[208:211], v[56:59], v[72:75]\n"
"ds_write_addtid_b32 v51 offset:15264\n"
"v_mfma_f32_16x16x32_bf16 v[72:75], a[212:215], v[60:63], v[72:75]\n"
"v_mfma_f32_16x16x32_bf16 v[72:75], a[216:219], v[64:67], v[72:75]\n"
"v_mfma_f32_16x16x32_bf16 v[72:75], a[220:223], v[68:71], v[72:75]\n"
"v_mfma_f32_16x16x32_bf16 v[76:79], a[224:227], v[84:87], v[76:79]\n"
"s_waitcnt lgkmcnt(0)\n"
"v_mfma_f32_16x16x32_bf16 v[76:79], a[228:231], v[88:91], v[76:79]\n"
"s_barrier\n"
"v_mfma_f32_16x16x32_bf16 v[76:79], a[232:235], v[92:95], v[76:79]\n"
"ds_read_b128 v[80:83], v2 offset:1536\n"
"ds_read_b128 v[84:87], v3 offset:512\n"
"v_mfma_f32_16x16x32_bf16 v[76:79], a[236:239], v[52:55], v[76:79]\n"
"v_mfma_f32_16x16x32_bf16 v[76:79], a[240:243], v[56:59], v[76:79]\n"
"ds_read_b128 v[88:91], v4 offset:2560\n"
"v_mfma_f32_16x16x32_bf16 v[76:79], a[244:247], v[60:63], v[76:79]\n"
"ds_read_b128 v[92:95], v2 offset:1792\n"
"ds_read_b128 v[108:111], v3 offset:768\n"
"v_mfma_f32_16x16x32_bf16 v[76:79], a[248:251], v[64:67], v[76:79]\n"
"ds_read_b128 v[116:119], v4 offset:2816\n"
"v_mfma_f32_16x16x32_bf16 v[76:79], a[252:255], v[68:71], v[76:79]\n"
"s_waitcnt lgkmcnt(3)\n"
"v_add_f32 v80, v80, v84\n"
"v_add_f32 v81, v81, v85\n"
"v_add_f32 v82, v82, v86\n"
"v_add_f32 v83, v83, v87\n"
"v_add_f32 v80, v80, v88\n"
"v_add_f32 v81, v81, v89\n"
"v_add_f32 v82, v82, v90\n"
"v_add_f32 v83, v83, v91\n"
"v_add_f32 v80, v80, v72\n"
"v_add_f32 v81, v81, v73\n"
"v_add_f32 v82, v82, v74\n"
"v_add_f32 v83, v83, v75\n"
"s_add_u32 s94, s50, 0x0\n"
"s_and_b32 s93, s94, 0xfbffffff\n"
"v_cvt_pk_bf16_f32 v72, v80, v81\n"
"v_cvt_pk_bf16_f32 v73, v82, v83\n"
"s_waitcnt lgkmcnt(0)\n"
"v_add_f32 v92, v92, v108\n"
"v_add_f32 v93, v93, v109\n"
"v_add_f32 v94, v94, v110\n"
"v_add_f32 v95, v95, v111\n"
"v_add_f32 v92, v92, v116\n"
"v_add_f32 v93, v93, v117\n"
"v_add_f32 v94, v94, v118\n"
"v_add_f32 v95, v95, v119\n"
"v_add_f32 v92, v92, v76\n"
"v_add_f32 v93, v93, v77\n"
"v_add_f32 v94, v94, v78\n"
"v_add_f32 v95, v95, v79\n"
"s_waitcnt vmcnt(0)\n"
"v_cvt_pk_bf16_f32 v74, v92, v93\n"
"v_cvt_pk_bf16_f32 v75, v94, v95\n"
"buffer_store_dwordx4 v[72:75], v11, s[40:43], s93 offen sc1\n"
"s_nop 0\n"
"v_mfma_f32_16x16x32_bf16 v[76:79], v[72:75], v[72:75], 0\n"
"buffer_store_dwordx2 v[0:1], v12, s[40:43], s95 offen offset:0 sc1\n"
"v_mfma_f32_16x16x32_bf16 v[88:91], v[14:17], v[72:75], 0\n"
"s_nop 5\n"
"v_cndmask_b32_e64 v76, v76, v77, s[62:63]\n"
"v_cndmask_b32_e64 v78, v78, v79, s[62:63]\n"
"v_cndmask_b32_e64 v89, v76, v78, s[64:65]\n"
"buffer_store_dwordx2 v[88:89], v13, s[40:43], s93 offen offset:0 sc1\n"
"s_waitcnt vmcnt(0)\n"
"buffer_store_dwordx2 v[0:1], v12, s[40:43], s94 offen offset:0 sc1\n"
"s_branch .LO_end\n"
".LO_t2:\n"
"v_mfma_f32_16x16x32_bf16 v[48:51], v[68:71], a[188:191], v[48:51]\n"
"v_mfma_f32_16x16x32_bf16 v[72:75], a[192:195], v[84:87], v[72:75]\n"
"v_mfma_f32_16x16x32_bf16 v[72:75], a[196:199], v[88:91], v[72:75]\n"
"ds_write_addtid_b32 v48 offset:1280\n"
"v_mfma_f32_16x16x32_bf16 v[72:75], a[200:203], v[92:95], v[72:75]\n"
"ds_write_addtid_b32 v49 offset:5920\n"
"v_mfma_f32_16x16x32_bf16 v[72:75], a[204:207], v[52:55], v[72:75]\n"
"ds_write_addtid_b32 v50 offset:10624\n"
"v_mfma_f32_16x16x32_bf16 v[72:75], a[208:211], v[56:59], v[72:75]\n"
"ds_write_addtid_b32 v51 offset:15264\n"
"v_mfma_f32_16x16x32_bf16 v[72:75], a[212:215], v[60:63], v[72:75]\n"
"v_mfma_f32_16x16x32_bf16 v[72:75], a[216:219], v[64:67], v[72:75]\n"
"v_mfma_f32_16x16x32_bf16 v[72:75], a[220:223], v[68:71], v[72:75]\n"
"v_mfma_f32_16x16x32_bf16 v[76:79], a[224:227], v[84:87], v[76:79]\n"
"s_waitcnt lgkmcnt(0)\n"
"v_mfma_f32_16x16x32_bf16 v[76:79], a[228:231], v[88:91], v[76:79]\n"
"s_barrier\n"
"v_mfma_f32_16x16x32_bf16 v[76:79], a[232:235], v[92:95], v[76:79]\n"
"ds_read_b128 v[80:83], v2 offset:1536\n"
"ds_read_b128 v[84:87], v3 offset:512\n"
"v_mfma_f32_16x16x32_bf16 v[76:79], a[236:239], v[52:55], v[76:79]\n"
"v_mfma_f32_16x16x32_bf16 v[76:79], a[240:243], v[56:59], v[76:79]\n"
"ds_read_b128 v[88:91], v4 offset:2560\n"
"v_mfma_f32_16x16x32_bf16 v[76:79], a[244:247], v[60:63], v[76:79]\n"
"ds_read_b128 v[92:95], v2 offset:1792\n"
"ds_read_b128 v[108:111], v3 offset:768\n"
"v_mfma_f32_16x16x32_bf16 v[76:79], a[248:251], v[64:67], v[76:79]\n"
"ds_read_b128 v[116:119], v4 offset:2816\n"
"v_mfma_f32_16x16x32_bf16 v[76:79], a[252:255], v[68:71], v[76:79]\n"
"s_waitcnt vmcnt(11)\n"
"v_mfma_f32_16x16x32_bf16 v[64:67], v[96:99], a[0:3], 0\n"
"v_mfma_f32_16x16x32_bf16 v[64:67], v[100:103], a[4:7], v[64:67]\n"
"s_waitcnt vmcnt(8)\n"
"v_mfma_f32_16x16x32_bf16 v[64:67], v[104:107], a[8:11], v[64:67]\n"
"s_waitcnt lgkmcnt(3)\n"
"v_add_f32 v80, v80, v84\n"
"v_add_f32 v81, v81, v85\n"
"v_mfma_f32_16x16x32_bf16 v[64:67], v[20:23], a[12:15], v[64:67]\n"
"s_waitcnt vmcnt(6)\n"
"v_add_f32 v82, v82, v86\n"
"v_mfma_f32_16x16x32_bf16 v[64:67], v[24:27], a[16:19], v[64:67]\n"
"v_add_f32 v83, v83, v87\n"
"v_add_f32 v80, v80, v88\n"
"v_mfma_f32_16x16x32_bf16 v[64:67], v[28:31], a[20:23], v[64:67]\n"
"s_waitcnt vmcnt(3)\n"
"v_add_f32 v81, v81, v89\n"
"v_add_f32 v82, v82, v90\n"
"v_mfma_f32_16x16x32_bf16 v[64:67], v[32:35], a[24:27], v[64:67]\n"
"v_add_f32 v83, v83, v91\n"
"v_add_f32 v80, v80, v72\n"
"v_mfma_f32_16x16x32_bf16 v[64:67], v[36:39], a[28:31], v[64:67]\n"
"v_add_f32 v81, v81, v73\n"
"v_add_f32 v82, v82, v74\n"
"v_mfma_f32_16x16x32_bf16 v[84:87], v[96:99], a[32:35], 0\n"
"v_add_f32 v83, v83, v75\n"
"v_mfma_f32_16x16x32_bf16 v[84:87], v[100:103], a[36:39], v[84:87]\n"
"s_add_u32 s94, s50, 0xffe80000\n"
"s_and_b32 s93, s94, 0xfbffffff\n"
"ds_write_addtid_b32 v64 offset:1536\n"
"v_mfma_f32_16x16x32_bf16 v[84:87], v[104:107], a[40:43], v[84:87]\n"
"ds_write_addtid_b32 v65 offset:6176\n"
"v_cvt_pk_bf16_f32 v72, v80, v81\n"
"v_mfma_f32_16x16x32_bf16 v[84:87], v[20:23], a[44:47], v[84:87]\n"
"ds_write_addtid_b32 v66 offset:10880\n"
"v_mfma_f32_16x16x32_bf16 v[84:87], v[24:27], a[48:51], v[84:87]\n"
"ds_write_addtid_b32 v67 offset:15520\n"
"v_cvt_pk_bf16_f32 v73, v82, v83\n"
"v_mfma_f32_16x16x32_bf16 v[84:87], v[28:31], a[52:55], v[84:87]\n"
"s_waitcnt lgkmcnt(4)\n"
"v_add_f32 v92, v92, v108\n"
"v_add_f32 v93, v93, v109\n"
"v_mfma_f32_16x16x32_bf16 v[84:87], v[32:35], a[56:59], v[84:87]\n"
"v_add_f32 v94, v94, v110\n"
"v_add_f32 v95, v95, v111\n"
"v_mfma_f32_16x16x32_bf16 v[84:87], v[36:39], a[60:63], v[84:87]\n"
"v_add_f32 v92, v92, v116\n"
"v_add_f32 v93, v93, v117\n"
"v_mfma_f32_16x16x32_bf16 v[80:83], v[96:99], a[64:67], 0\n"
"v_add_f32 v94, v94, v118\n"
"v_mfma_f32_16x16x32_bf16 v[80:83], v[100:103], a[68:71], v[80:83]\n"
"ds_write_addtid_b32 v84 offset:1792\n"
"v_mfma_f32_16x16x32_bf16 v[80:83], v[104:107], a[72:75], v[80:83]\n"
"ds_write_addtid_b32 v85 offset:6432\n"
"v_add_f32 v95, v95, v119\n"
"v_mfma_f32_16x16x32_bf16 v[80:83], v[20:23], a[76:79], v[80:83]\n"
"ds_write_addtid_b32 v86 offset:11136\n"
"v_add_f32 v92, v92, v76\n"
"v_mfma_f32_16x16x32_bf16 v[80:83], v[24:27], a[80:83], v[80:83]\n"
"ds_write_addtid_b32 v87 offset:15776\n"
"v_add_f32 v93, v93, v77\n"
"v_mfma_f32_16x16x32_bf16 v[80:83], v[28:31], a[84:87], v[80:83]\n"
"s_waitcnt vmcnt(1)\n"
"v_add_f32 v94, v94, v78\n"
"v_add_f32 v95, v95, v79\n"
"v_mfma_f32_16x16x32_bf16 v[80:83], v[32:35], a[88:91], v[80:83]\n"
"s_waitcnt vmcnt(0)\n"
"v_cvt_pk_bf16_f32 v74, v92, v93\n"
"v_cvt_pk_bf16_f32 v75, v94, v95\n"
"v_mfma_f32_16x16x32_bf16 v[80:83], v[36:39], a[92:95], v[80:83]\n"
"buffer_store_dwordx4 v[72:75], v11, s[40:43], s93 offen sc1\n"
"v_lshlrev_b32 v84, 16, v112\n"
"v_and_b32 v85, 0xffff0000, v112\n"
"v_lshlrev_b32 v86, 16, v113\n"
"v_mfma_f32_16x16x32_bf16 v[76:79], v[72:75], v[72:75], 0\n"
"buffer_store_dwordx2 v[0:1], v12, s[40:43], s95 offen offset:0 sc1\n"
"v_and_b32 v87, 0xffff0000, v113\n"
"v_mfma_f32_16x16x32_bf16 v[88:91], v[14:17], v[72:75], 0\n"
"v_lshlrev_b32 v92, 16, v114\n"
"v_and_b32 v93, 0xffff0000, v114\n"
"v_mfma_f32_16x16x32_bf16 v[108:111], v[96:99], a[96:99], 0\n"
"v_cndmask_b32_e64 v76, v76, v77, s[62:63]\n"
"v_cndmask_b32_e64 v78, v78, v79, s[62:63]\n"
"v_mfma_f32_16x16x32_bf16 v[108:111], v[100:103], a[100:103], v[108:111]\n"
"ds_write_addtid_b32 v80 offset:2048\n"
"v_cndmask_b32_e64 v89, v76, v78, s[64:65]\n"
"v_mfma_f32_16x16x32_bf16 v[108:111], v[104:107], a[104:107], v[108:111]\n"
"buffer_store_dwordx2 v[88:89], v13, s[40:43], s93 offen offset:0 sc1\n"
"ds_write_addtid_b32 v81 offset:6688\n"
"v_mfma_f32_16x16x32_bf16 v[108:111], v[20:23], a[108:111], v[108:111]\n"
"ds_write_addtid_b32 v82 offset:11392\n"
"v_lshlrev_b32 v94, 16, v115\n"
"v_mfma_f32_16x16x32_bf16 v[108:111], v[24:27], a[112:115], v[108:111]\n"
"ds_write_addtid_b32 v83 offset:16032\n"
"v_and_b32 v95, 0xffff0000, v115\n"
"v_mfma_f32_16x16x32_bf16 v[108:111], v[28:31], a[116:119], v[108:111]\n"
"v_mfma_f32_16x16x32_bf16 v[108:111], v[32:35], a[120:123], v[108:111]\n"
"v_mfma_f32_16x16x32_bf16 v[108:111], v[36:39], a[124:127], v[108:111]\n"
"v_mfma_f32_16x16x32_bf16 v[72:75], v[96:99], a[128:131], 0\n"
"v_mfma_f32_16x16x32_bf16 v[72:75], v[100:103], a[132:135], v[72:75]\n"
"ds_write_addtid_b32 v108 offset:2304\n"
"v_mfma_f32_16x16x32_bf16 v[72:75], v[104:107], a[136:139], v[72:75]\n"
"ds_write_addtid_b32 v109 offset:6944\n"
"v_mfma_f32_16x16x32_bf16 v[72:75], v[20:23], a[140:143], v[72:75]\n"
"ds_write_addtid_b32 v110 offset:11648\n"
"v_mfma_f32_16x16x32_bf16 v[72:75], v[24:27], a[144:147], v[72:75]\n"
"ds_write_addtid_b32 v111 offset:16288\n"
"v_mfma_f32_16x16x32_bf16 v[72:75], v[28:31], a[148:151], v[72:75]\n"
"v_mfma_f32_16x16x32_bf16 v[72:75], v[32:35], a[152:155], v[72:75]\n"
"v_mfma_f32_16x16x32_bf16 v[72:75], v[36:39], a[156:159], v[72:75]\n"
"v_mfma_f32_16x16x32_bf16 v[76:79], v[96:99], a[160:163], 0\n"
"v_mfma_f32_16x16x32_bf16 v[76:79], v[100:103], a[164:167], v[76:79]\n"
"ds_write_addtid_b32 v72 offset:2560\n"
"v_mfma_f32_16x16x32_bf16 v[76:79], v[104:107], a[168:171], v[76:79]\n"
"ds_write_addtid_b32 v73 offset:7200\n"
"v_mfma_f32_16x16x32_bf16 v[76:79], v[20:23], a[172:175], v[76:79]\n"
"ds_write_addtid_b32 v74 offset:11904\n"
"v_mfma_f32_16x16x32_bf16 v[76:79], v[24:27], a[176:179], v[76:79]\n"
"ds_write_addtid_b32 v75 offset:16544\n"
"v_mfma_f32_16x16x32_bf16 v[76:79], v[28:31], a[180:183], v[76:79]\n"
"v_mfma_f32_16x16x32_bf16 v[76:79], v[32:35], a[184:187], v[76:79]\n"
"v_mfma_f32_16x16x32_bf16 v[76:79], v[36:39], a[188:191], v[76:79]\n"
"v_mfma_f32_16x16x32_bf16 v[84:87], a[192:195], v[96:99], v[84:87]\n"
"v_mfma_f32_16x16x32_bf16 v[84:87], a[196:199], v[100:103], v[84:87]\n"
"ds_write_addtid_b32 v76 offset:2816\n"
"v_mfma_f32_16x16x32_bf16 v[84:87], a[200:203], v[104:107], v[84:87]\n"
"ds_write_addtid_b32 v77 offset:7456\n"
"v_mfma_f32_16x16x32_bf16 v[84:87], a[204:207], v[20:23], v[84:87]\n"
"ds_write_addtid_b32 v78 offset:12160\n"
"v_mfma_f32_16x16x32_bf16 v[84:87], a[208:211], v[24:27], v[84:87]\n"
"ds_write_addtid_b32 v79 offset:16800\n"
"v_mfma_f32_16x16x32_bf16 v[84:87], a[212:215], v[28:31], v[84:87]\n"
"v_mfma_f32_16x16x32_bf16 v[84:87], a[216:219], v[32:35], v[84:87]\n"
"v_mfma_f32_16x16x32_bf16 v[84:87], a[220:223], v[36:39], v[84:87]\n"
"v_mfma_f32_16x16x32_bf16 v[92:95], a[224:227], v[96:99], v[92:95]\n"
"s_waitcnt lgkmcnt(0)\n"
"v_mfma_f32_16x16x32_bf16 v[92:95], a[228:231], v[100:103], v[92:95]\n"
"s_barrier\n"
"v_mfma_f32_16x16x32_bf16 v[92:95], a[232:235], v[104:107], v[92:95]\n"
"ds_read_b128 v[96:99], v2 offset:0\n"
"ds_read_b128 v[100:103], v3 offset:2048\n"
"v_mfma_f32_16x16x32_bf16 v[92:95], a[236:239], v[20:23], v[92:95]\n"
"ds_read_b128 v[104:107], v4 offset:1024\n"
"ds_read_b128 v[108:111], v2 offset:256\n"
"v_mfma_f32_16x16x32_bf16 v[92:95], a[240:243], v[24:27], v[92:95]\n"
"ds_read_b128 v[112:115], v3 offset:2304\n"
"ds_read_b128 v[116:119], v4 offset:1280\n"
"v_mfma_f32_16x16x32_bf16 v[92:95], a[244:247], v[28:31], v[92:95]\n"
"v_mfma_f32_16x16x32_bf16 v[92:95], a[248:251], v[32:35], v[92:95]\n"
"v_mfma_f32_16x16x32_bf16 v[92:95], a[252:255], v[36:39], v[92:95]\n"
"s_waitcnt lgkmcnt(3)\n"
"v_add_f32 v96, v96, v100\n"
"v_add_f32 v97, v97, v101\n"
"v_add_f32 v98, v98, v102\n"
"v_add_f32 v99, v99, v103\n"
"v_add_f32 v96, v96, v104\n"
"v_add_f32 v97, v97, v105\n"
"v_add_f32 v98, v98, v106\n"
"v_add_f32 v99, v99, v107\n"
"v_add_f32 v96, v96, v84\n"
"v_add_f32 v97, v97, v85\n"
"v_add_f32 v98, v98, v86\n"
"v_add_f32 v99, v99, v87\n"
"s_add_u32 s95, s50, 0x0\n"
"s_and_b32 s93, s95, 0xfbffffff\n"
"s_waitcnt vmcnt(0)\n"
"buffer_store_dwordx2 v[0:1], v12, s[40:43], s94 offen offset:0 sc1\n"
"v_cvt_pk_bf16_f32 v84, v96, v97\n"
"v_cvt_pk_bf16_f32 v85, v98, v99\n"
"s_waitcnt lgkmcnt(0)\n"
"v_add_f32 v108, v108, v112\n"
"v_add_f32 v109, v109, v113\n"
"v_add_f32 v110, v110, v114\n"
"v_add_f32 v111, v111, v115\n"
"v_add_f32 v108, v108, v116\n"
"v_add_f32 v109, v109, v117\n"
"v_add_f32 v110, v110, v118\n"
"v_add_f32 v111, v111, v119\n"
"v_add_f32 v108, v108, v92\n"
"v_add_f32 v109, v109, v93\n"
"v_add_f32 v110, v110, v94\n"
"v_add_f32 v111, v111, v95\n"
"v_cvt_pk_bf16_f32 v86, v108, v109\n"
"v_cvt_pk_bf16_f32 v87, v110, v111\n"
"buffer_store_dwordx4 v[84:87], v11, s[40:43], s93 offen sc1\n"
"s_nop 0\n"
"v_mfma_f32_16x16x32_bf16 v[108:111], v[84:87], v[84:87], 0\n"
"v_mfma_f32_16x16x32_bf16 v[112:115], v[14:17], v[84:87], 0\n"
"s_nop 6\n"
"v_cndmask_b32_e64 v108, v108, v109, s[62:63]\n"
"v_cndmask_b32_e64 v110, v110, v111, s[62:63]\n"
"v_cndmask_b32_e64 v113, v108, v110, s[64:65]\n"
"buffer_store_dwordx2 v[112:113], v13, s[40:43], s93 offen offset:0 sc1\n"
"s_waitcnt vmcnt(0)\n"
"buffer_store_dwordx2 v[0:1], v12, s[40:43], s95 offen offset:0 sc1\n"
"s_branch .LO_end\n"
".LO_t3:\n"
"v_mfma_f32_16x16x32_bf16 v[84:87], a[196:199], v[100:103], v[84:87]\n"
"ds_write_addtid_b32 v76 offset:2816\n"
"v_mfma_f32_16x16x32_bf16 v[84:87], a[200:203], v[104:107], v[84:87]\n"
"ds_write_addtid_b32 v77 offset:7456\n"
"v_mfma_f32_16x16x32_bf16 v[84:87], a[204:207], v[20:23], v[84:87]\n"
"ds_write_addtid_b32 v78 offset:12160\n"
"v_mfma_f32_16x16x32_bf16 v[84:87], a[208:211], v[24:27], v[84:87]\n"
"ds_write_addtid_b32 v79 offset:16800\n"
"v_mfma_f32_16x16x32_bf16 v[84:87], a[212:215], v[28:31], v[84:87]\n"
"v_mfma_f32_16x16x32_bf16 v[84:87], a[216:219], v[32:35], v[84:87]\n"
"v_mfma_f32_16x16x32_bf16 v[84:87], a[220:223], v[36:39], v[84:87]\n"
"v_mfma_f32_16x16x32_bf16 v[92:95], a[224:227], v[96:99], v[92:95]\n"
"s_waitcnt lgkmcnt(0)\n"
"v_mfma_f32_16x16x32_bf16 v[92:95], a[228:231], v[100:103], v[92:95]\n"
"s_barrier\n"
"v_mfma_f32_16x16x32_bf16 v[92:95], a[232:235], v[104:107], v[92:95]\n"
"ds_read_b128 v[96:99], v2 offset:0\n"
"ds_read_b128 v[100:103], v3 offset:2048\n"
"v_mfma_f32_16x16x32_bf16 v[92:95], a[236:239], v[20:23], v[92:95]\n"
"ds_read_b128 v[104:107], v4 offset:1024\n"
"ds_read_b128 v[108:111], v2 offset:256\n"
"v_mfma_f32_16x16x32_bf16 v[92:95], a[240:243], v[24:27], v[92:95]\n"
"ds_read_b128 v[112:115], v3 offset:2304\n"
"ds_read_b128 v[116:119], v4 offset:1280\n"
"v_mfma_f32_16x16x32_bf16 v[92:95], a[244:247], v[28:31], v[92:95]\n"
"v_mfma_f32_16x16x32_bf16 v[92:95], a[248:251], v[32:35], v[92:95]\n"
"v_mfma_f32_16x16x32_bf16 v[92:95], a[252:255], v[36:39], v[92:95]\n"
"s_waitcnt vmcnt(11)\n"
"v_mfma_f32_16x16x32_bf16 v[120:123], v[40:43], a[0:3], 0\n"
"v_mfma_f32_16x16x32_bf16 v[120:123], v[44:47], a[4:7], v[120:123]\n"
"s_waitcnt vmcnt(8)\n"
"v_mfma_f32_16x16x32_bf16 v[120:123], v[48:51], a[8:11], v[120:123]\n"
"s_waitcnt lgkmcnt(3)\n"
"v_add_f32 v96, v96, v100\n"
"v_add_f32 v97, v97, v101\n"
"v_mfma_f32_16x16x32_bf16 v[120:123], v[52:55], a[12:15], v[120:123]\n"
"s_waitcnt vmcnt(6)\n"
"v_add_f32 v98, v98, v102\n"
"v_add_f32 v99, v99, v103\n"
"v_mfma_f32_16x16x32_bf16 v[120:123], v[56:59], a[16:19], v[120:123]\n"
"v_add_f32 v96, v96, v104\n"
"v_add_f32 v97, v97, v105\n"
"v_mfma_f32_16x16x32_bf16 v[120:123], v[60:63], a[20:23], v[120:123]\n"
"s_waitcnt vmcnt(4)\n"
"v_add_f32 v98, v98, v106\n"
"v_add_f32 v99, v99, v107\n"
"v_mfma_f32_16x16x32_bf16 v[120:123], v[64:67], a[24:27], v[120:123]\n"
"v_add_f32 v96, v96, v84\n"
"v_mfma_f32_16x16x32_bf16 v[120:123], v[68:71], a[28:31], v[120:123]\n"
"v_add_f32 v97, v97, v85\n"
"v_mfma_f32_16x16x32_bf16 v[100:103], v[40:43], a[32:35], 0\n"
"v_add_f32 v98, v98, v86\n"
"v_add_f32 v99, v99, v87\n"
"v_mfma_f32_16x16x32_bf16 v[100:103], v[44:47], a[36:39], v[100:103]\n"
"s_add_u32 s95, s50, 0xffe80000\n"
"s_and_b32 s93, s95, 0xfbffffff\n"
"ds_write_addtid_b32 v120 offset:0\n"
"v_mfma_f32_16x16x32_bf16 v[100:103], v[48:51], a[40:43], v[100:103]\n"
"s_waitcnt vmcnt(1)\n"
"buffer_store_dwordx2 v[0:1], v12, s[40:43], s94 offen offset:0 sc1\n"
"ds_write_addtid_b32 v121 offset:4640\n"
"v_mfma_f32_16x16x32_bf16 v[100:103], v[52:55], a[44:47], v[100:103]\n"
"ds_write_addtid_b32 v122 offset:9344\n"
"v_cvt_pk_bf16_f32 v84, v96, v97\n"
"v_mfma_f32_16x16x32_bf16 v[100:103], v[56:59], a[48:51], v[100:103]\n"
"ds_write_addtid_b32 v123 offset:13984\n"
"v_mfma_f32_16x16x32_bf16 v[100:103], v[60:63], a[52:55], v[100:103]\n"
"v_cvt_pk_bf16_f32 v85, v98, v99\n"
"v_mfma_f32_16x16x32_bf16 v[100:103], v[64:67], a[56:59], v[100:103]\n"
"s_waitcnt lgkmcnt(4)\n"
"v_add_f32 v108, v108, v112\n"
"v_add_f32 v109, v109, v113\n"
"v_mfma_f32_16x16x32_bf16 v[100:103], v[68:71], a[60:63], v[100:103]\n"
"v_add_f32 v110, v110, v114\n"
"v_add_f32 v111, v111, v115\n"
"v_mfma_f32_16x16x32_bf16 v[96:99], v[40:43], a[64:67], 0\n"
"v_add_f32 v108, v108, v116\n"
"v_add_f32 v109, v109, v117\n"
"v_mfma_f32_16x16x32_bf16 v[96:99], v[44:47], a[68:71], v[96:99]\n"
"ds_write_addtid_b32 v100 offset:256\n"
"v_add_f32 v110, v110, v118\n"
"v_mfma_f32_16x16x32_bf16 v[96:99], v[48:51], a[72:75], v[96:99]\n"
"ds_write_addtid_b32 v101 offset:4896\n"
"v_add_f32 v111, v111, v119\n"
"v_mfma_f32_16x16x32_bf16 v[96:99], v[52:55], a[76:79], v[96:99]\n"
"ds_write_addtid_b32 v102 offset:9600\n"
"v_add_f32 v108, v108, v92\n"
"v_mfma_f32_16x16x32_bf16 v[96:99], v[56:59], a[80:83], v[96:99]\n"
"ds_write_addtid_b32 v103 offset:14240\n"
"v_add_f32 v109, v109, v93\n"
"v_mfma_f32_16x16x32_bf16 v[96:99], v[60:63], a[84:87], v[96:99]\n"
"v_add_f32 v110, v110, v94\n"
"v_mfma_f32_16x16x32_bf16 v[96:99], v[64:67], a[88:91], v[96:99]\n"
"s_waitcnt vmcnt(1)\n"
"v_add_f32 v111, v111, v95\n"
"v_cvt_pk_bf16_f32 v86, v108, v109\n"
"v_mfma_f32_16x16x32_bf16 v[96:99], v[68:71], a[92:95], v[96:99]\n"
"v_cvt_pk_bf16_f32 v87, v110, v111\n"
"v_lshlrev_b32 v100, 16, v88\n"
"v_mfma_f32_16x16x32_bf16 v[92:95], v[40:43], a[96:99], 0\n"
"v_and_b32 v101, 0xffff0000, v88\n"
"v_lshlrev_b32 v102, 16, v89\n"
"v_mfma_f32_16x16x32_bf16 v[92:95], v[44:47], a[100:103], v[92:95]\n"
"ds_write_addtid_b32 v96 offset:512\n"
"v_and_b32 v103, 0xffff0000, v89\n"
"v_mfma_f32_16x16x32_bf16 v[92:95], v[48:51], a[104:107], v[92:95]\n"
"ds_write_addtid_b32 v97 offset:5152\n"
"v_lshlrev_b32 v104, 16, v90\n"
"v_mfma_f32_16x16x32_bf16 v[92:95], v[52:55], a[108:111], v[92:95]\n"
"buffer_store_dwordx4 v[84:87], v11, s[40:43], s93 offen sc1\n"
"ds_write_addtid_b32 v98 offset:9856\n"
"v_and_b32 v105, 0xffff0000, v90\n"
"v_lshlrev_b32 v106, 16, v91\n"
"v_mfma_f32_16x16x32_bf16 v[108:111], v[84:87], v[84:87], 0\n"
"v_and_b32 v107, 0xffff0000, v91\n"
"v_mfma_f32_16x16x32_bf16 v[112:115], v[14:17], v[84:87], 0\n"
"v_mfma_f32_16x16x32_bf16 v[92:95], v[56:59], a[112:115], v[92:95]\n"
"ds_write_addtid_b32 v99 offset:14496\n"
"v_cndmask_b32_e64 v108, v108, v109, s[62:63]\n"
"v_mfma_f32_16x16x32_bf16 v[92:95], v[60:63], a[116:119], v[92:95]\n"
"v_cndmask_b32_e64 v110, v110, v111, s[62:63]\n"
"v_cndmask_b32_e64 v113, v108, v110, s[64:65]\n"
"v_mfma_f32_16x16x32_bf16 v[92:95], v[64:67], a[120:123], v[92:95]\n"
"buffer_store_dwordx2 v[112:113], v13, s[40:43], s93 offen offset:0 sc1\n"
"v_mfma_f32_16x16x32_bf16 v[92:95], v[68:71], a[124:127], v[92:95]\n"
"v_mfma_f32_16x16x32_bf16 v[84:87], v[40:43], a[128:131], 0\n"
"v_mfma_f32_16x16x32_bf16 v[84:87], v[44:47], a[132:135], v[84:87]\n"
"ds_write_addtid_b32 v92 offset:768\n"
"v_mfma_f32_16x16x32_bf16 v[84:87], v[48:51], a[136:139], v[84:87]\n"
"ds_write_addtid_b32 v93 offset:5408\n"
"v_mfma_f32_16x16x32_bf16 v[84:87], v[52:55], a[140:143], v[84:87]\n"
"ds_write_addtid_b32 v94 offset:10112\n"
"v_mfma_f32_16x16x32_bf16 v[84:87], v[56:59], a[144:147], v[84:87]\n"
"ds_write_addtid_b32 v95 offset:14752\n"
"v_mfma_f32_16x16x32_bf16 v[84:87], v[60:63], a[148:151], v[84:87]\n"
"v_mfma_f32_16x16x32_bf16 v[84:87], v[64:67], a[152:155], v[84:87]\n"
"v_mfma_f32_16x16x32_bf16 v[84:87], v[68:71], a[156:159], v[84:87]\n"
"v_mfma_f32_16x16x32_bf16 v[92:95], v[40:43], a[160:163], 0\n"
"v_mfma_f32_16x16x32_bf16 v[92:95], v[44:47], a[164:167], v[92:95]\n"
"ds_write_addtid_b32 v84 offset:1024\n"
"v_mfma_f32_16x16x32_bf16 v[92:95], v[48:51], a[168:171], v[92:95]\n"
"ds_write_addtid_b32 v85 offset:5664\n"
"v_mfma_f32_16x16x32_bf16 v[92:95], v[52:55], a[172:175], v[92:95]\n"
"ds_write_addtid_b32 v86 offset:10368\n"
"v_mfma_f32_16x16x32_bf16 v[92:95], v[56:59], a[176:179], v[92:95]\n"
"ds_write_addtid_b32 v87 offset:15008\n"
"v_mfma_f32_16x16x32_bf16 v[92:95], v[60:63], a[180:183], v[92:95]\n"
"v_mfma_f32_16x16x32_bf16 v[92:95], v[64:67], a[184:187], v[92:95]\n"
"v_mfma_f32_16x16x32_bf16 v[92:95], v[68:71], a[188:191], v[92:95]\n"
"v_mfma_f32_16x16x32_bf16 v[100:103], a[192:195], v[40:43], v[100:103]\n"
"v_mfma_f32_16x16x32_bf16 v[100:103], a[196:199], v[44:47], v[100:103]\n"
"ds_write_addtid_b32 v92 offset:1280\n"
"v_mfma_f32_16x16x32_bf16 v[100:103], a[200:203], v[48:51], v[100:103]\n"
"ds_write_addtid_b32 v93 offset:5920\n"
"v_mfma_f32_16x16x32_bf16 v[100:103], a[204:207], v[52:55], v[100:103]\n"
"ds_write_addtid_b32 v94 offset:10624\n"
"v_mfma_f32_16x16x32_bf16 v[100:103], a[208:211], v[56:59], v[100:103]\n"
"ds_write_addtid_b32 v95 offset:15264\n"
"v_mfma_f32_16x16x32_bf16 v[100:103], a[212:215], v[60:63], v[100:103]\n"
"v_mfma_f32_16x16x32_bf16 v[100:103], a[216:219], v[64:67], v[100:103]\n"
"v_mfma_f32_16x16x32_bf16 v[100:103], a[220:223], v[68:71], v[100:103]\n"
"v_mfma_f32_16x16x32_bf16 v[104:107], a[224:227], v[40:43], v[104:107]\n"
"s_waitcnt lgkmcnt(0)\n"
"v_mfma_f32_16x16x32_bf16 v[104:107], a[228:231], v[44:47], v[104:107]\n"
"s_barrier\n"
"v_mfma_f32_16x16x32_bf16 v[104:107], a[232:235], v[48:51], v[104:107]\n"
"ds_read_b128 v[40:43], v2 offset:1536\n"
"v_mfma_f32_16x16x32_bf16 v[104:107], a[236:239], v[52:55], v[104:107]\n"
"ds_read_b128 v[44:47], v3 offset:512\n"
"ds_read_b128 v[48:51], v4 offset:2560\n"
"v_mfma_f32_16x16x32_bf16 v[104:107], a[240:243], v[56:59], v[104:107]\n"
"ds_read_b128 v[108:111], v2 offset:1792\n"
"ds_read_b128 v[112:115], v3 offset:768\n"
"v_mfma_f32_16x16x32_bf16 v[104:107], a[244:247], v[60:63], v[104:107]\n"
"ds_read_b128 v[116:119], v4 offset:2816\n"
"v_mfma_f32_16x16x32_bf16 v[104:107], a[248:251], v[64:67], v[104:107]\n"
"v_mfma_f32_16x16x32_bf16 v[104:107], a[252:255], v[68:71], v[104:107]\n"
"s_waitcnt lgkmcnt(3)\n"
"v_add_f32 v40, v40, v44\n"
"v_add_f32 v41, v41, v45\n"
"v_add_f32 v42, v42, v46\n"
"v_add_f32 v43, v43, v47\n"
"v_add_f32 v40, v40, v48\n"
"v_add_f32 v41, v41, v49\n"
"v_add_f32 v42, v42, v50\n"
"v_add_f32 v43, v43, v51\n"
"v_add_f32 v40, v40, v100\n"
"v_add_f32 v41, v41, v101\n"
"v_add_f32 v42, v42, v102\n"
"v_add_f32 v43, v43, v103\n"
"s_add_u32 s93, s50, 0x0\n"
"s_and_b32 s94, s93, 0xfbffffff\n"
"v_cvt_pk_bf16_f32 v48, v40, v41\n"
"v_cvt_pk_bf16_f32 v49, v42, v43\n"
"s_waitcnt lgkmcnt(0)\n"
"v_add_f32 v108, v108, v112\n"
"v_add_f32 v109, v109, v113\n"
"s_waitcnt vmcnt(0)\n"
"v_add_f32 v110, v110, v114\n"
"buffer_store_dwordx2 v[0:1], v12, s[40:43], s95 offen offset:0 sc1\n"
"v_add_f32 v111, v111, v115\n"
"v_add_f32 v108, v108, v116\n"
"v_add_f32 v109, v109, v117\n"
"v_add_f32 v110, v110, v118\n"
"v_add_f32 v111, v111, v119\n"
"v_add_f32 v108, v108, v104\n"
"v_add_f32 v109, v109, v105\n"
"v_add_f32 v110, v110, v106\n"
"v_add_f32 v111, v111, v107\n"
"v_cvt_pk_bf16_f32 v50, v108, v109\n"
"v_cvt_pk_bf16_f32 v51, v110, v111\n"
"buffer_store_dwordx4 v[48:51], v11, s[40:43], s94 offen sc1\n"
"s_nop 0\n"
"v_mfma_f32_16x16x32_bf16 v[40:43], v[48:51], v[48:51], 0\n"
"v_mfma_f32_16x16x32_bf16 v[96:99], v[14:17], v[48:51], 0\n"
"s_nop 6\n"
"v_cndmask_b32_e64 v40, v40, v41, s[62:63]\n"
"v_cndmask_b32_e64 v42, v42, v43, s[62:63]\n"
"v_cndmask_b32_e64 v97, v40, v42, s[64:65]\n"
"buffer_store_dwordx2 v[96:97], v13, s[40:43], s94 offen offset:0 sc1\n"
"s_waitcnt vmcnt(0)\n"
"buffer_store_dwordx2 v[0:1], v12, s[40:43], s93 offen offset:0 sc1\n"
"s_branch .LO_end\n"
".LO_end:\n"
"s_waitcnt vmcnt(0) lgkmcnt(0)\n"
"s_endpgm\n"
      :: "{v2}"((unsigned)(lds0 + ((wid + 3) & 3) * 18592 + trp)), "{v3}"((unsigned)(lds0 + ((wid + 2) & 3) * 18592 + trp)), "{v4}"((unsigned)(lds0 + ((wid + 1) & 3) * 18592 + trp)), "{v5}"((unsigned)(lane * 16 + wid * 8192 + ((0 + rot) % 4) * 2048 + 134250496)), "{v6}"((unsigned)(lane * 16 + wid * 8192 + ((1 + rot) % 4) * 2048 + 134250496)), "{v7}"((unsigned)(lane * 16 + wid * 8192 + ((2 + rot) % 4) * 2048 + 134250496)), "{v8}"((unsigned)(lane * 16 + wid * 8192 + ((3 + rot) % 4) * 2048 + 134250496)), "{v9}"((unsigned)((wid * 8 + (lane & 7)) * 8 + 352512 + O_GL * 1572864)), "{v10}"((unsigned)(lane * 16 + pair * 1024 + 134217728)), "{v11}"((unsigned)(lane * 16 + pair * 1024 + 65536)), "{v12}"((unsigned)(lane ? DROP : pair * 8 + 352768)), "{v13}"((unsigned)(lg == (li >> 2) ? pair * 128 + li * 8 + 344064 : DROP)), "{v14}"((unsigned)(0x3f803f80)), "{v15}"((unsigned)(0x3f803f80)), "{v16}"((unsigned)(0x3f803f80)), "{v17}"((unsigned)(0x3f803f80)), "{v20}"((unsigned)(vT + tsw)), "{v21}"((unsigned)(vT + (tsw ^ 8))), "{v22}"((unsigned)(vD + ((0 + rot) % 4) * 131072)), "{v23}"((unsigned)(vD + ((1 + rot) % 4) * 131072)), "{v24}"((unsigned)(vD + ((2 + rot) % 4) * 131072)), "{v25}"((unsigned)(vD + ((3 + rot) % 4) * 131072)), "{s[40:43]}"(rS), "{s[44:47]}"(rW), "{s[48:49]}"(wsp), "{s50}"((unsigned)(sA)), "{s51}"((unsigned)(sLo)), "{s52}"((unsigned)(sHi)), "{s53}"((unsigned)(sEx)), "{s54}"((unsigned)(sEnt)), "{s55}"((unsigned)(sLd)), "{s[56:57]}"(wsb + (sLo & 0x3ffffff)), "{s59}"((unsigned)(lds0 + wid * 18592)), "{s[62:63]}"(0xAAAAAAAAAAAAAAAAull), "{s[64:65]}"(0xCCCCCCCCCCCCCCCCull), "{s66}"((unsigned)(sW0 + 64 * ((wid + 1) & 3))), "{s67}"((unsigned)(sW0 + 64 * ((wid + 2) & 3))), "{s68}"((unsigned)(sW0 + 64 * ((wid + 3) & 3))), "{s69}"((unsigned)(sW0 + 64 * ((wid + 4) & 3)))
      : "v255", "a255", "s95", "vcc", "scc", "memory");

  } else if (role == 2) {
    const unsigned sEx = sHi + F_EX * 3 * RS, sEnt = -wid & 3, tsw = 0, pair = idx * 4 + wid, rot = SLP & 4 ? FR0 * (xo >= 0) : KR * (set ? set & 2 : 1);
    const unsigned sW0 = wid * 256 * 7168 + idx * 256, vD = (lane >> 2) * 7168 + (lane & 3) * 16;
    const u32x4 rW = desc(a.w1, 1024 * 7168);
    asm volatile(
"s_nop 4\n"
"v_mov_b32 v186, 0\n"
"global_load_dwordx2 v[0:1], v186, s[48:49] sc1\n"
"global_load_dwordx4 v[30:33], v38, s[64:65]\n"
"v_mov_b32 v188, 0x3f803f80\n"
"v_mov_b32 v189, 0x3f803f80\n"
"v_mov_b32 v190, 0x3f803f80\n"
"v_mov_b32 v191, 0x3f803f80\n"
"s_add_u32 m0, s55, 0\n"
"s_add_u32 s60, s66, 0\n"
"buffer_load_dwordx4 v39, s[44:47], s60 offen lds\n"
"s_add_u32 m0, s55, 1024\n"
"s_add_u32 s60, s66, 114688\n"
"buffer_load_dwordx4 v39, s[44:47], s60 offen lds\n"
"s_add_u32 m0, s55, 2048\n"
"s_add_u32 s60, s67, 0\n"
"buffer_load_dwordx4 v39, s[44:47], s60 offen lds\n"
"s_add_u32 m0, s55, 3072\n"
"s_add_u32 s60, s67, 114688\n"
"buffer_load_dwordx4 v39, s[44:47], s60 offen lds\n"
"s_add_u32 m0, s55, 4096\n"
"s_add_u32 s60, s68, 0\n"
"buffer_load_dwordx4 v39, s[44:47], s60 offen lds\n"
"s_add_u32 m0, s55, 5120\n"
"s_add_u32 s60, s68, 114688\n"
"buffer_load_dwordx4 v39, s[44:47], s60 offen lds\n"
"s_add_u32 m0, s55, 6144\n"
"s_add_u32 s60, s69, 0\n"
"buffer_load_dwordx4 v39, s[44:47], s60 offen lds\n"
"s_add_u32 m0, s55, 7168\n"
"s_add_u32 s60, s69, 114688\n"
"buffer_load_dwordx4 v39, s[44:47], s60 offen lds\n"
"s_add_u32 m0, s55, 8192\n"
"s_add_u32 s60, s66, 229376\n"
"buffer_load_dwordx4 v39, s[44:47], s60 offen lds\n"
"s_add_u32 m0, s55, 9216\n"
"s_add_u32 s60, s66, 344064\n"
"buffer_load_dwordx4 v39, s[44:47], s60 offen lds\n"
"s_add_u32 m0, s55, 10240\n"
"s_add_u32 s60, s67, 229376\n"
"buffer_load_dwordx4 v39, s[44:47], s60 offen lds\n"
"s_add_u32 m0, s55, 11264\n"
"s_add_u32 s60, s67, 344064\n"
"buffer_load_dwordx4 v39, s[44:47], s60 offen lds\n"
"s_add_u32 m0, s55, 12288\n"
"s_add_u32 s60, s68, 229376\n"
"buffer_load_dwordx4 v39, s[44:47], s60 offen lds\n"
"s_add_u32 m0, s55, 13312\n"
"s_add_u32 s60, s68, 344064\n"
"buffer_load_dwordx4 v39, s[44:47], s60 offen lds\n"
"s_add_u32 m0, s55, 14336\n"
"s_add_u32 s60, s69, 229376\n"
"buffer_load_dwordx4 v39, s[44:47], s60 offen lds\n"
"s_add_u32 m0, s55, 15360\n"
"s_add_u32 s60, s69, 344064\n"
"buffer_load_dwordx4 v39, s[44:47], s60 offen lds\n"
"s_add_u32 m0, s55, 16384\n"
"s_add_u32 s60, s66, 0\n"
"buffer_load_dwordx4 v40, s[44:47], s60 offen lds\n"
"s_add_u32 m0, s55, 17408\n"
"s_add_u32 s60, s66, 114688\n"
"buffer_load_dwordx4 v40, s[44:47], s60 offen lds\n"
"s_add_u32 m0, s55, 18432\n"
"s_add_u32 s60, s67, 0\n"
"buffer_load_dwordx4 v40, s[44:47], s60 offen lds\n"
"s_add_u32 m0, s55, 19456\n"
"s_add_u32 s60, s67, 114688\n"
"buffer_load_dwordx4 v40, s[44:47], s60 offen lds\n"
"s_add_u32 m0, s55, 20480\n"
"s_add_u32 s60, s68, 0\n"
"buffer_load_dwordx4 v40, s[44:47], s60 offen lds\n"
"s_add_u32 m0, s55, 21504\n"
"s_add_u32 s60, s68, 114688\n"
"buffer_load_dwordx4 v40, s[44:47], s60 offen lds\n"
"s_add_u32 m0, s55, 22528\n"
"s_add_u32 s60, s69, 0\n"
"buffer_load_dwordx4 v40, s[44:47], s60 offen lds\n"
"s_add_u32 m0, s55, 23552\n"
"s_add_u32 s60, s69, 114688\n"
"buffer_load_dwordx4 v40, s[44:47], s60 offen lds\n"
"s_add_u32 m0, s55, 24576\n"
"s_add_u32 s60, s66, 229376\n"
"buffer_load_dwordx4 v40, s[44:47], s60 offen lds\n"
"s_add_u32 m0, s55, 25600\n"
"s_add_u32 s60, s66, 344064\n"
"buffer_load_dwordx4 v40, s[44:47], s60 offen lds\n"
"s_add_u32 m0, s55, 26624\n"
"s_add_u32 s60, s67, 229376\n"
"buffer_load_dwordx4 v40, s[44:47], s60 offen lds\n"
"s_add_u32 m0, s55, 27648\n"
"s_add_u32 s60, s67, 344064\n"
"buffer_load_dwordx4 v40, s[44:47], s60 offen lds\n"
"s_add_u32 m0, s55, 28672\n"
"s_add_u32 s60, s68, 229376\n"
"buffer_load_dwordx4 v40, s[44:47], s60 offen lds\n"
"s_add_u32 m0, s55, 29696\n"
"s_add_u32 s60, s68, 344064\n"
"buffer_load_dwordx4 v40, s[44:47], s60 offen lds\n"
"s_add_u32 m0, s55, 30720\n"
"s_add_u32 s60, s69, 229376\n"
"buffer_load_dwordx4 v40, s[44:47], s60 offen lds\n"
"s_add_u32 m0, s55, 31744\n"
"s_add_u32 s60, s69, 344064\n"
"buffer_load_dwordx4 v40, s[44:47], s60 offen lds\n"
"s_add_u32 m0, s55, 32768\n"
"s_add_u32 s60, s66, 0\n"
"buffer_load_dwordx4 v41, s[44:47], s60 offen lds\n"
"s_add_u32 m0, s55, 33792\n"
"s_add_u32 s60, s66, 114688\n"
"buffer_load_dwordx4 v41, s[44:47], s60 offen lds\n"
"s_add_u32 m0, s55, 34816\n"
"s_add_u32 s60, s67, 0\n"
"buffer_load_dwordx4 v41, s[44:47], s60 offen lds\n"
"s_add_u32 m0, s55, 35840\n"
"s_add_u32 s60, s67, 114688\n"
"buffer_load_dwordx4 v41, s[44:47], s60 offen lds\n"
"s_add_u32 m0, s55, 36864\n"
"s_add_u32 s60, s68, 0\n"
"buffer_load_dwordx4 v41, s[44:47], s60 offen lds\n"
"s_add_u32 m0, s55, 37888\n"
"s_add_u32 s60, s68, 114688\n"
"buffer_load_dwordx4 v41, s[44:47], s60 offen lds\n"
"s_add_u32 m0, s55, 38912\n"
"s_add_u32 s60, s69, 0\n"
"buffer_load_dwordx4 v41, s[44:47], s60 offen lds\n"
"s_add_u32 m0, s55, 39936\n"
"s_add_u32 s60, s69, 114688\n"
"buffer_load_dwordx4 v41, s[44:47], s60 offen lds\n"
"s_waitcnt vmcnt(38)\n"
"ds_read_b64_tr_b16 a[0:1], v36 offset:0\n"
"ds_read_b64_tr_b16 a[2:3], v36 offset:256\n"
"ds_read_b64_tr_b16 a[32:33], v37 offset:0\n"
"ds_read_b64_tr_b16 a[34:35], v37 offset:256\n"
"v_add_u32 v0, 2, v0\n"
"v_add_u32 v1, 0x9e3779b9, v1\n"
"v_or_b32 v0, 1, v0\n"
"v_readfirstlane_b32 s49, v1\n"
"v_readfirstlane_b32 s48, v0\n"
"s_waitcnt vmcnt(36)\n"
"ds_read_b64_tr_b16 a[64:65], v36 offset:2048\n"
"ds_read_b64_tr_b16 a[66:67], v36 offset:2304\n"
"ds_read_b64_tr_b16 a[96:97], v37 offset:2048\n"
"ds_read_b64_tr_b16 a[98:99], v37 offset:2304\n"
"s_waitcnt lgkmcnt(4)\n"
"s_add_u32 m0, s55, 0\n"
"s_add_u32 s60, s66, 229376\n"
"buffer_load_dwordx4 v41, s[44:47], s60 offen lds\n"
"s_add_u32 m0, s55, 1024\n"
"s_add_u32 s60, s66, 344064\n"
"buffer_load_dwordx4 v41, s[44:47], s60 offen lds\n"
"v_mfma_f32_16x16x32_bf16 v[192:195], v[188:191], a[0:3], 0\n"
"v_mfma_f32_16x16x32_bf16 v[196:199], v[188:191], a[32:35], 0\n"
"s_waitcnt vmcnt(36)\n"
"ds_read_b64_tr_b16 a[128:129], v36 offset:4096\n"
"ds_read_b64_tr_b16 a[130:131], v36 offset:4352\n"
"ds_read_b64_tr_b16 a[160:161], v37 offset:4096\n"
"ds_read_b64_tr_b16 a[162:163], v37 offset:4352\n"
"s_waitcnt lgkmcnt(4)\n"
"s_add_u32 m0, s55, 2048\n"
"s_add_u32 s60, s67, 229376\n"
"buffer_load_dwordx4 v41, s[44:47], s60 offen lds\n"
"s_add_u32 m0, s55, 3072\n"
"s_add_u32 s60, s67, 344064\n"
"buffer_load_dwordx4 v41, s[44:47], s60 offen lds\n"
"v_mfma_f32_16x16x32_bf16 v[200:203], v[188:191], a[64:67], 0\n"
"v_mfma_f32_16x16x32_bf16 v[204:207], v[188:191], a[96:99], 0\n"
"s_waitcnt vmcnt(36)\n"
"ds_read_b64_tr_b16 a[192:193], v36 offset:6144\n"
"ds_read_b64_tr_b16 a[194:195], v36 offset:6400\n"
"ds_read_b64_tr_b16 a[224:225], v37 offset:6144\n"
"ds_read_b64_tr_b16 a[226:227], v37 offset:6400\n"
"s_waitcnt lgkmcnt(4)\n"
"s_add_u32 m0, s55, 4096\n"
"s_add_u32 s60, s68, 229376\n"
"buffer_load_dwordx4 v41, s[44:47], s60 offen lds\n"
"s_add_u32 m0, s55, 5120\n"
"s_add_u32 s60, s68, 344064\n"
"buffer_load_dwordx4 v41, s[44:47], s60 offen lds\n"
"v_mfma_f32_16x16x32_bf16 v[208:211], v[188:191], a[128:131], 0\n"
"v_mfma_f32_16x16x32_bf16 v[212:215], v[188:191], a[160:163], 0\n"
"s_waitcnt vmcnt(36)\n"
"ds_read_b64_tr_b16 a[4:5], v36 offset:8192\n"
"ds_read_b64_tr_b16 a[6:7], v36 offset:8448\n"
"ds_read_b64_tr_b16 a[36:37], v37 offset:8192\n"
"ds_read_b64_tr_b16 a[38:39], v37 offset:8448\n"
"s_waitcnt lgkmcnt(4)\n"
"s_add_u32 m0, s55, 6144\n"
"s_add_u32 s60, s69, 229376\n"
"buffer_load_dwordx4 v41, s[44:47], s60 offen lds\n"
"s_add_u32 m0, s55, 7168\n"
"s_add_u32 s60, s69, 344064\n"
"buffer_load_dwordx4 v41, s[44:47], s60 offen lds\n"
"v_mfma_f32_16x16x32_bf16 v[216:219], v[188:191], a[192:195], 0\n"
"v_mfma_f32_16x16x32_bf16 v[220:223], v[188:191], a[224:227], 0\n"
"s_waitcnt vmcnt(36)\n"
"ds_read_b64_tr_b16 a[68:69], v36 offset:10240\n"
"ds_read_b64_tr_b16 a[70:71], v36 offset:10496\n"
"ds_read_b64_tr_b16 a[100:101], v37 offset:10240\n"
"ds_read_b64_tr_b16 a[102:103], v37 offset:10496\n"
"s_waitcnt lgkmcnt(4)\n"
"s_add_u32 m0, s55, 8192\n"
"s_add_u32 s60, s66, 0\n"
"buffer_load_dwordx4 v42, s[44:47], s60 offen lds\n"
"s_add_u32 m0, s55, 9216\n"
"s_add_u32 s60, s66, 114688\n"
"buffer_load_dwordx4 v42, s[44:47], s60 offen lds\n"
"v_mfma_f32_16x16x32_bf16 v[192:195], v[188:191], a[4:7], v[192:195]\n"
"v_mfma_f32_16x16x32_bf16 v[196:199], v[188:191], a[36:39], v[196:199]\n"
"s_waitcnt vmcnt(36)\n"
"ds_read_b64_tr_b16 a[132:133], v36 offset:12288\n"
"ds_read_b64_tr_b16 a[134:135], v36 offset:12544\n"
"ds_read_b64_tr_b16 a[164:165], v37 offset:12288\n"
"ds_read_b64_tr_b16 a[166:167], v37 offset:12544\n"
"s_waitcnt lgkmcnt(4)\n"
"s_add_u32 m0, s55, 10240\n"
"s_add_u32 s60, s67, 0\n"
"buffer_load_dwordx4 v42, s[44:47], s60 offen lds\n"
"s_add_u32 m0, s55, 11264\n"
"s_add_u32 s60, s67, 114688\n"
"buffer_load_dwordx4 v42, s[44:47], s60 offen lds\n"
"v_mfma_f32_16x16x32_bf16 v[200:203], v[188:191], a[68:71], v[200:203]\n"
"v_mfma_f32_16x16x32_bf16 v[204:207], v[188:191], a[100:103], v[204:207]\n"
"s_waitcnt vmcnt(36)\n"
"ds_read_b64_tr_b16 a[196:197], v36 offset:14336\n"
"ds_read_b64_tr_b16 a[198:199], v36 offset:14592\n"
"ds_read_b64_tr_b16 a[228:229], v37 offset:14336\n"
"ds_read_b64_tr_b16 a[230:231], v37 offset:14592\n"
"s_waitcnt lgkmcnt(4)\n"
"s_add_u32 m0, s55, 12288\n"
"s_add_u32 s60, s68, 0\n"
"buffer_load_dwordx4 v42, s[44:47], s60 offen lds\n"
"s_add_u32 m0, s55, 13312\n"
"s_add_u32 s60, s68, 114688\n"
"buffer_load_dwordx4 v42, s[44:47], s60 offen lds\n"
"v_mfma_f32_16x16x32_bf16 v[208:211], v[188:191], a[132:135], v[208:211]\n"
"v_mfma_f32_16x16x32_bf16 v[212:215], v[188:191], a[164:167], v[212:215]\n"
"s_waitcnt vmcnt(36)\n"
"ds_read_b64_tr_b16 a[8:9], v36 offset:16384\n"
"ds_read_b64_tr_b16 a[10:11], v36 offset:16640\n"
"ds_read_b64_tr_b16 a[40:41], v37 offset:16384\n"
"ds_read_b64_tr_b16 a[42:43], v37 offset:16640\n"
"s_waitcnt lgkmcnt(4)\n"
"s_add_u32 m0, s55, 14336\n"
"s_add_u32 s60, s69, 0\n"
"buffer_load_dwordx4 v42, s[44:47], s60 offen lds\n"
"s_add_u32 m0, s55, 15360\n"
"s_add_u32 s60, s69, 114688\n"
"buffer_load_dwordx4 v42, s[44:47], s60 offen lds\n"
"v_mfma_f32_16x16x32_bf16 v[216:219], v[188:191], a[196:199], v[216:219]\n"
"v_mfma_f32_16x16x32_bf16 v[220:223], v[188:191], a[228:231], v[220:223]\n"
"s_waitcnt vmcnt(36)\n"
"ds_read_b64_tr_b16 a[72:73], v36 offset:18432\n"
"ds_read_b64_tr_b16 a[74:75], v36 offset:18688\n"
"ds_read_b64_tr_b16 a[104:105], v37 offset:18432\n"
"ds_read_b64_tr_b16 a[106:107], v37 offset:18688\n"
"s_waitcnt lgkmcnt(4)\n"
"s_add_u32 m0, s55, 16384\n"
"s_add_u32 s60, s66, 229376\n"
"buffer_load_dwordx4 v42, s[44:47], s60 offen lds\n"
"s_add_u32 m0, s55, 17408\n"
"s_add_u32 s60, s66, 344064\n"
"buffer_load_dwordx4 v42, s[44:47], s60 offen lds\n"
"v_mfma_f32_16x16x32_bf16 v[192:195], v[188:191], a[8:11], v[192:195]\n"
"v_mfma_f32_16x16x32_bf16 v[196:199], v[188:191], a[40:43], v[196:199]\n"
"s_waitcnt vmcnt(36)\n"
"ds_read_b64_tr_b16 a[136:137], v36 offset:20480\n"
"ds_read_b64_tr_b16 a[138:139], v36 offset:20736\n"
"ds_read_b64_tr_b16 a[168:169], v37 offset:20480\n"
"ds_read_b64_tr_b16 a[170:171], v37 offset:20736\n"
"s_waitcnt lgkmcnt(4)\n"
"s_add_u32 m0, s55, 18432\n"
"s_add_u32 s60, s67, 229376\n"
"buffer_load_dwordx4 v42, s[44:47], s60 offen lds\n"
"s_add_u32 m0, s55, 19456\n"
"s_add_u32 s60, s67, 344064\n"
"buffer_load_dwordx4 v42, s[44:47], s60 offen lds\n"
"v_mfma_f32_16x16x32_bf16 v[200:203], v[188:191], a[72:75], v[200:203]\n"
"v_mfma_f32_16x16x32_bf16 v[204:207], v[188:191], a[104:107], v[204:207]\n"
"s_waitcnt vmcnt(36)\n"
"ds_read_b64_tr_b16 a[200:201], v36 offset:22528\n"
"ds_read_b64_tr_b16 a[202:203], v36 offset:22784\n"
"ds_read_b64_tr_b16 a[232:233], v37 offset:22528\n"
"ds_read_b64_tr_b16 a[234:235], v37 offset:22784\n"
"s_waitcnt lgkmcnt(4)\n"
"s_add_u32 m0, s55, 20480\n"
"s_add_u32 s60, s68, 229376\n"
"buffer_load_dwordx4 v42, s[44:47], s60 offen lds\n"
"s_add_u32 m0, s55, 21504\n"
"s_add_u32 s60, s68, 344064\n"
"buffer_load_dwordx4 v42, s[44:47], s60 offen lds\n"
"v_mfma_f32_16x16x32_bf16 v[208:211], v[188:191], a[136:139], v[208:211]\n"
"v_mfma_f32_16x16x32_bf16 v[212:215], v[188:191], a[168:171], v[212:215]\n"
"s_waitcnt vmcnt(36)\n"
"ds_read_b64_tr_b16 a[12:13], v36 offset:24576\n"
"ds_read_b64_tr_b16 a[14:15], v36 offset:24832\n"
"ds_read_b64_tr_b16 a[44:45], v37 offset:24576\n"
"ds_read_b64_tr_b16 a[46:47], v37 offset:24832\n"
"s_waitcnt lgkmcnt(4)\n"
"s_add_u32 m0, s55, 22528\n"
"s_add_u32 s60, s69, 229376\n"
"buffer_load_dwordx4 v42, s[44:47], s60 offen lds\n"
"s_add_u32 m0, s55, 23552\n"
"s_add_u32 s60, s69, 344064\n"
"buffer_load_dwordx4 v42, s[44:47], s60 offen lds\n"
"v_mfma_f32_16x16x32_bf16 v[216:219], v[188:191], a[200:203], v[216:219]\n"
"v_mfma_f32_16x16x32_bf16 v[220:223], v[188:191], a[232:235], v[220:223]\n"
"s_waitcnt vmcnt(36)\n"
"ds_read_b64_tr_b16 a[76:77], v36 offset:26624\n"
"ds_read_b64_tr_b16 a[78:79], v36 offset:26880\n"
"ds_read_b64_tr_b16 a[108:109], v37 offset:26624\n"
"ds_read_b64_tr_b16 a[110:111], v37 offset:26880\n"
"s_waitcnt lgkmcnt(4)\n"
"v_mfma_f32_16x16x32_bf16 v[192:195], v[188:191], a[12:15], v[192:195]\n"
"v_mfma_f32_16x16x32_bf16 v[196:199], v[188:191], a[44:47], v[196:199]\n"
"s_waitcnt vmcnt(34)\n"
"ds_read_b64_tr_b16 a[140:141], v36 offset:28672\n"
"ds_read_b64_tr_b16 a[142:143], v36 offset:28928\n"
"ds_read_b64_tr_b16 a[172:173], v37 offset:28672\n"
"ds_read_b64_tr_b16 a[174:175], v37 offset:28928\n"
"s_waitcnt lgkmcnt(4)\n"
"v_mfma_f32_16x16x32_bf16 v[200:203], v[188:191], a[76:79], v[200:203]\n"
"v_mfma_f32_16x16x32_bf16 v[204:207], v[188:191], a[108:111], v[204:207]\n"
"s_waitcnt vmcnt(32)\n"
"ds_read_b64_tr_b16 a[204:205], v36 offset:30720\n"
"ds_read_b64_tr_b16 a[206:207], v36 offset:30976\n"
"ds_read_b64_tr_b16 a[236:237], v37 offset:30720\n"
"ds_read_b64_tr_b16 a[238:239], v37 offset:30976\n"
"s_waitcnt lgkmcnt(4)\n"
"v_mfma_f32_16x16x32_bf16 v[208:211], v[188:191], a[140:143], v[208:211]\n"
"v_mfma_f32_16x16x32_bf16 v[212:215], v[188:191], a[172:175], v[212:215]\n"
"s_waitcnt vmcnt(30)\n"
"ds_read_b64_tr_b16 a[16:17], v36 offset:32768\n"
"ds_read_b64_tr_b16 a[18:19], v36 offset:33024\n"
"ds_read_b64_tr_b16 a[48:49], v37 offset:32768\n"
"ds_read_b64_tr_b16 a[50:51], v37 offset:33024\n"
"s_waitcnt lgkmcnt(4)\n"
"v_mfma_f32_16x16x32_bf16 v[216:219], v[188:191], a[204:207], v[216:219]\n"
"v_mfma_f32_16x16x32_bf16 v[220:223], v[188:191], a[236:239], v[220:223]\n"
"s_waitcnt vmcnt(28)\n"
"ds_read_b64_tr_b16 a[80:81], v36 offset:34816\n"
"ds_read_b64_tr_b16 a[82:83], v36 offset:35072\n"
"ds_read_b64_tr_b16 a[112:113], v37 offset:34816\n"
"ds_read_b64_tr_b16 a[114:115], v37 offset:35072\n"
"s_waitcnt lgkmcnt(4)\n"
"v_mfma_f32_16x16x32_bf16 v[192:195], v[188:191], a[16:19], v[192:195]\n"
"v_mfma_f32_16x16x32_bf16 v[196:199], v[188:191], a[48:51], v[196:199]\n"
"s_waitcnt vmcnt(26)\n"
"ds_read_b64_tr_b16 a[144:145], v36 offset:36864\n"
"ds_read_b64_tr_b16 a[146:147], v36 offset:37120\n"
"ds_read_b64_tr_b16 a[176:177], v37 offset:36864\n"
"ds_read_b64_tr_b16 a[178:179], v37 offset:37120\n"
"s_waitcnt lgkmcnt(4)\n"
"v_mfma_f32_16x16x32_bf16 v[200:203], v[188:191], a[80:83], v[200:203]\n"
"v_mfma_f32_16x16x32_bf16 v[204:207], v[188:191], a[112:115], v[204:207]\n"
"s_waitcnt vmcnt(24)\n"
"ds_read_b64_tr_b16 a[208:209], v36 offset:38912\n"
"ds_read_b64_tr_b16 a[210:211], v36 offset:39168\n"
"ds_read_b64_tr_b16 a[240:241], v37 offset:38912\n"
"ds_read_b64_tr_b16 a[242:243], v37 offset:39168\n"
"s_waitcnt lgkmcnt(4)\n"
"v_mfma_f32_16x16x32_bf16 v[208:211], v[188:191], a[144:147], v[208:211]\n"
"v_mfma_f32_16x16x32_bf16 v[212:215], v[188:191], a[176:179], v[212:215]\n"
"s_waitcnt vmcnt(22)\n"
"ds_read_b64_tr_b16 a[20:21], v36 offset:0\n"
"ds_read_b64_tr_b16 a[22:23], v36 offset:256\n"
"ds_read_b64_tr_b16 a[52:53], v37 offset:0\n"
"ds_read_b64_tr_b16 a[54:55], v37 offset:256\n"
"s_waitcnt lgkmcnt(4)\n"
"v_mfma_f32_16x16x32_bf16 v[216:219], v[188:191], a[208:211], v[216:219]\n"
"v_mfma_f32_16x16x32_bf16 v[220:223], v[188:191], a[240:243], v[220:223]\n"
"s_waitcnt vmcnt(20)\n"
"ds_read_b64_tr_b16 a[84:85], v36 offset:2048\n"
"ds_read_b64_tr_b16 a[86:87], v36 offset:2304\n"
"ds_read_b64_tr_b16 a[116:117], v37 offset:2048\n"
"ds_read_b64_tr_b16 a[118:119], v37 offset:2304\n"
"s_waitcnt lgkmcnt(4)\n"
"v_mfma_f32_16x16x32_bf16 v[192:195], v[188:191], a[20:23], v[192:195]\n"
"v_mfma_f32_16x16x32_bf16 v[196:199], v[188:191], a[52:55], v[196:199]\n"
"s_waitcnt vmcnt(18)\n"
"ds_read_b64_tr_b16 a[148:149], v36 offset:4096\n"
"ds_read_b64_tr_b16 a[150:151], v36 offset:4352\n"
"ds_read_b64_tr_b16 a[180:181], v37 offset:4096\n"
"ds_read_b64_tr_b16 a[182:183], v37 offset:4352\n"
"s_waitcnt lgkmcnt(4)\n"
"v_mfma_f32_16x16x32_bf16 v[200:203], v[188:191], a[84:87], v[200:203]\n"
"v_mfma_f32_16x16x32_bf16 v[204:207], v[188:191], a[116:119], v[204:207]\n"
"s_waitcnt vmcnt(16)\n"
"ds_read_b64_tr_b16 a[212:213], v36 offset:6144\n"
"ds_read_b64_tr_b16 a[214:215], v36 offset:6400\n"
"ds_read_b64_tr_b16 a[244:245], v37 offset:6144\n"
"ds_read_b64_tr_b16 a[246:247], v37 offset:6400\n"
"s_waitcnt lgkmcnt(4)\n"
"v_mfma_f32_16x16x32_bf16 v[208:211], v[188:191], a[148:151], v[208:211]\n"
"v_mfma_f32_16x16x32_bf16 v[212:215], v[188:191], a[180:183], v[212:215]\n"
"s_waitcnt vmcnt(14)\n"
"ds_read_b64_tr_b16 a[24:25], v36 offset:8192\n"
"ds_read_b64_tr_b16 a[26:27], v36 offset:8448\n"
"ds_read_b64_tr_b16 a[56:57], v37 offset:8192\n"
"ds_read_b64_tr_b16 a[58:59], v37 offset:8448\n"
"s_waitcnt lgkmcnt(4)\n"
"v_mfma_f32_16x16x32_bf16 v[216:219], v[188:191], a[212:215], v[216:219]\n"
"v_mfma_f32_16x16x32_bf16 v[220:223], v[188:191], a[244:247], v[220:223]\n"
"s_waitcnt vmcnt(12)\n"
"ds_read_b64_tr_b16 a[88:89], v36 offset:10240\n"
"ds_read_b64_tr_b16 a[90:91], v36 offset:10496\n"
"ds_read_b64_tr_b16 a[120:121], v37 offset:10240\n"
"ds_read_b64_tr_b16 a[122:123], v37 offset:10496\n"
"s_waitcnt lgkmcnt(4)\n"
"v_mfma_f32_16x16x32_bf16 v[192:195], v[188:191], a[24:27], v[192:195]\n"
"v_mfma_f32_16x16x32_bf16 v[196:199], v[188:191], a[56:59], v[196:199]\n"
"s_waitcnt vmcnt(10)\n"
"ds_read_b64_tr_b16 a[152:153], v36 offset:12288\n"
"ds_read_b64_tr_b16 a[154:155], v36 offset:12544\n"
"ds_read_b64_tr_b16 a[184:185], v37 offset:12288\n"
"ds_read_b64_tr_b16 a[186:187], v37 offset:12544\n"
"s_waitcnt lgkmcnt(4)\n"
"v_mfma_f32_16x16x32_bf16 v[200:203], v[188:191], a[88:91], v[200:203]\n"
"v_mfma_f32_16x16x32_bf16 v[204:207], v[188:191], a[120:123], v[204:207]\n"
"s_waitcnt vmcnt(8)\n"
"ds_read_b64_tr_b16 a[216:217], v36 offset:14336\n"
"ds_read_b64_tr_b16 a[218:219], v36 offset:14592\n"
"ds_read_b64_tr_b16 a[248:249], v37 offset:14336\n"
"ds_read_b64_tr_b16 a[250:251], v37 offset:14592\n"
"s_waitcnt lgkmcnt(4)\n"
"v_mfma_f32_16x16x32_bf16 v[208:211], v[188:191], a[152:155], v[208:211]\n"
"v_mfma_f32_16x16x32_bf16 v[212:215], v[188:191], a[184:187], v[212:215]\n"
"s_waitcnt vmcnt(6)\n"
"ds_read_b64_tr_b16 a[28:29], v36 offset:16384\n"
"ds_read_b64_tr_b16 a[30:31], v36 offset:16640\n"
"ds_read_b64_tr_b16 a[60:61], v37 offset:16384\n"
"ds_read_b64_tr_b16 a[62:63], v37 offset:16640\n"
"s_waitcnt lgkmcnt(4)\n"
"v_mfma_f32_16x16x32_bf16 v[216:219], v[188:191], a[216:219], v[216:219]\n"
"v_mfma_f32_16x16x32_bf16 v[220:223], v[188:191], a[248:251], v[220:223]\n"
"s_waitcnt vmcnt(4)\n"
"ds_read_b64_tr_b16 a[92:93], v36 offset:18432\n"
"ds_read_b64_tr_b16 a[94:95], v36 offset:18688\n"
"ds_read_b64_tr_b16 a[124:125], v37 offset:18432\n"
"ds_read_b64_tr_b16 a[126:127], v37 offset:18688\n"
"s_waitcnt lgkmcnt(4)\n"
"v_mfma_f32_16x16x32_bf16 v[192:195], v[188:191], a[28:31], v[192:195]\n"
"v_mfma_f32_16x16x32_bf16 v[196:199], v[188:191], a[60:63], v[196:199]\n"
"s_waitcnt vmcnt(2)\n"
"ds_read_b64_tr_b16 a[156:157], v36 offset:20480\n"
"ds_read_b64_tr_b16 a[158:159], v36 offset:20736\n"
"ds_read_b64_tr_b16 a[188:189], v37 offset:20480\n"
"ds_read_b64_tr_b16 a[190:191], v37 offset:20736\n"
"s_waitcnt lgkmcnt(4)\n"
"v_mfma_f32_16x16x32_bf16 v[200:203], v[188:191], a[92:95], v[200:203]\n"
"v_mfma_f32_16x16x32_bf16 v[204:207], v[188:191], a[124:127], v[204:207]\n"
"s_waitcnt vmcnt(0)\n"
"ds_read_b64_tr_b16 a[220:221], v36 offset:22528\n"
"ds_read_b64_tr_b16 a[222:223], v36 offset:22784\n"
"ds_read_b64_tr_b16 a[252:253], v37 offset:22528\n"
"ds_read_b64_tr_b16 a[254:255], v37 offset:22784\n"
"s_waitcnt lgkmcnt(4)\n"
"v_mfma_f32_16x16x32_bf16 v[208:211], v[188:191], a[156:159], v[208:211]\n"
"v_mfma_f32_16x16x32_bf16 v[212:215], v[188:191], a[188:191], v[212:215]\n"
"s_waitcnt lgkmcnt(0)\n"
"v_mfma_f32_16x16x32_bf16 v[216:219], v[188:191], a[220:223], v[216:219]\n"
"v_mfma_f32_16x16x32_bf16 v[220:223], v[188:191], a[252:255], v[220:223]\n"
"s_mov_b32 m0, s59\n"
"s_barrier\n"
"ds_write_addtid_b32 v192 offset:0\n"
"ds_write_addtid_b32 v193 offset:4640\n"
"ds_write_addtid_b32 v194 offset:9344\n"
"ds_write_addtid_b32 v195 offset:13984\n"
"ds_write_addtid_b32 v196 offset:256\n"
"ds_write_addtid_b32 v197 offset:4896\n"
"ds_write_addtid_b32 v198 offset:9600\n"
"ds_write_addtid_b32 v199 offset:14240\n"
"ds_write_addtid_b32 v200 offset:512\n"
"ds_write_addtid_b32 v201 offset:5152\n"
"ds_write_addtid_b32 v202 offset:9856\n"
"ds_write_addtid_b32 v203 offset:14496\n"
"ds_write_addtid_b32 v204 offset:768\n"
"ds_write_addtid_b32 v205 offset:5408\n"
"ds_write_addtid_b32 v206 offset:10112\n"
"ds_write_addtid_b32 v207 offset:14752\n"
"ds_write_addtid_b32 v208 offset:1024\n"
"ds_write_addtid_b32 v209 offset:5664\n"
"ds_write_addtid_b32 v210 offset:10368\n"
"ds_write_addtid_b32 v211 offset:15008\n"
"ds_write_addtid_b32 v212 offset:1280\n"
"ds_write_addtid_b32 v213 offset:5920\n"
"ds_write_addtid_b32 v214 offset:10624\n"
"ds_write_addtid_b32 v215 offset:15264\n"
"ds_write_addtid_b32 v216 offset:1536\n"
"ds_write_addtid_b32 v217 offset:6176\n"
"ds_write_addtid_b32 v218 offset:10880\n"
"ds_write_addtid_b32 v219 offset:15520\n"
"ds_write_addtid_b32 v220 offset:1792\n"
"ds_write_addtid_b32 v221 offset:6432\n"
"ds_write_addtid_b32 v222 offset:11136\n"
"ds_write_addtid_b32 v223 offset:15776\n"
"s_waitcnt lgkmcnt(0)\n"
"s_barrier\n"
"ds_read_b128 v[96:99], v2 offset:0\n"
"ds_read_b128 v[100:103], v3 offset:512\n"
"ds_read_b128 v[104:107], v4 offset:1024\n"
"ds_read_b128 v[108:111], v43 offset:1536\n"
"ds_read_b128 v[112:115], v2 offset:256\n"
"ds_read_b128 v[116:119], v3 offset:768\n"
"ds_read_b128 v[120:123], v4 offset:1280\n"
"ds_read_b128 v[124:127], v43 offset:1792\n"
"s_waitcnt lgkmcnt(0)\n"
"s_barrier\n"
"v_add_f32 v96, v96, v100\n"
"v_add_f32 v104, v104, v108\n"
"v_add_f32 v18, v96, v104\n"
"v_add_f32 v97, v97, v101\n"
"v_add_f32 v105, v105, v109\n"
"v_add_f32 v19, v97, v105\n"
"v_add_f32 v98, v98, v102\n"
"v_add_f32 v106, v106, v110\n"
"v_add_f32 v20, v98, v106\n"
"v_add_f32 v99, v99, v103\n"
"v_add_f32 v107, v107, v111\n"
"v_add_f32 v21, v99, v107\n"
"v_add_f32 v112, v112, v116\n"
"v_add_f32 v120, v120, v124\n"
"v_add_f32 v22, v112, v120\n"
"v_add_f32 v113, v113, v117\n"
"v_add_f32 v121, v121, v125\n"
"v_add_f32 v23, v113, v121\n"
"v_add_f32 v114, v114, v118\n"
"v_add_f32 v122, v122, v126\n"
"v_add_f32 v24, v114, v122\n"
"v_add_f32 v115, v115, v119\n"
"v_add_f32 v123, v123, v127\n"
"v_add_f32 v25, v115, v123\n"
"v_lshlrev_b32 v26, 16, v30\n"
"v_and_b32 v27, 0xffff0000, v30\n"
"v_lshlrev_b32 v28, 16, v31\n"
"v_and_b32 v29, 0xffff0000, v31\n"
"v_lshlrev_b32 v30, 16, v32\n"
"v_and_b32 v31, 0xffff0000, v32\n"
"v_lshlrev_b32 v32, 16, v33\n"
"v_and_b32 v33, 0xffff0000, v33\n"
"s_mov_b32 s94, 0x80000000\n"
"s_mov_b32 s93, 0x80000000\n"
"s_mov_b32 s95, 0x80000000\n"
"s_mov_b32 s93, 0x80000000\n"
"s_mov_b32 s93, 0x80000000\n"
"s_mov_b32 s94, 0x80000000\n"
"s_mov_b32 s95, 0x80000000\n"
"s_mov_b32 s94, 0x80000000\n"
"v_mov_b32 v134, 0\n"
"v_mov_b32 v135, 0\n"
"v_mov_b32 v154, 0\n"
"v_mov_b32 v155, 0\n"
"v_mov_b32 v144, 0\n"
"v_mov_b32 v145, 0\n"
"v_mov_b32 v138, 0\n"
"v_mov_b32 v139, 0\n"
"s_cmp_eq_u32 s54, 0\n"
"s_cbranch_scc1 .LF_1s\n"
"s_cmp_eq_u32 s54, 1\n"
"s_cbranch_scc1 .LF_2s\n"
"s_cmp_eq_u32 s54, 2\n"
"s_cbranch_scc1 .LF_3s\n"
"s_cmp_eq_u32 s54, 3\n"
"s_cbranch_scc1 .LF_4s\n"
".p2align 6\n"
".LF_top:\n"
"v_mfma_f32_16x16x32_bf16 v[48:51], v[100:103], a[0:3], 0\n"
"global_load_dwordx4 v[36:39], v6, s[56:57] offset:1024 sc1\n"
"v_mul_f32 v125, v125, v124\n"
"v_mfma_f32_16x16x32_bf16 v[48:51], v[104:107], a[4:7], v[48:51]\n"
"s_waitcnt vmcnt(10)\n"
"ds_write_addtid_b32 v124 offset:18592\n"
"ds_write_addtid_b32 v125 offset:18848\n"
"v_mfma_f32_16x16x32_bf16 v[48:51], v[108:111], a[8:11], v[48:51]\n"
"v_cvt_pk_bf16_f32 v139, v142, v143\n"
"v_pk_max_i16 v138, v138, 0\n"
"v_mfma_f32_16x16x32_bf16 v[48:51], v[64:67], a[12:15], v[48:51]\n"
"s_waitcnt vmcnt(8)\n"
"ds_read2st64_b32 v[140:141], v14 offset0:0 offset1:1\n"
"v_pk_max_i16 v139, v139, 0\n"
"v_mfma_f32_16x16x32_bf16 v[48:51], v[68:71], a[16:19], v[48:51]\n"
"ds_read_b128 v[56:59], v2 offset:0\n"
"ds_read_b128 v[52:55], v3 offset:2048\n"
"v_mfma_f32_16x16x32_bf16 v[48:51], v[72:75], a[20:23], v[48:51]\n"
"s_waitcnt vmcnt(6)\n"
"global_load_dwordx4 v[40:43], v7, s[56:57] offset:0 sc1\n"
"ds_read_b128 v[60:63], v4 offset:1024\n"
"v_mfma_f32_16x16x32_bf16 v[48:51], v[76:79], a[24:27], v[48:51]\n"
"ds_read_b128 v[84:87], v2 offset:256\n"
"ds_read_b128 v[88:91], v3 offset:2304\n"
"v_mfma_f32_16x16x32_bf16 v[48:51], v[80:83], a[28:31], v[48:51]\n"
"ds_read_b128 v[92:95], v4 offset:1280\n"
"v_mfma_f32_16x16x32_bf16 v[96:99], v[100:103], a[32:35], 0\n"
"v_mfma_f32_16x16x32_bf16 v[96:99], v[104:107], a[36:39], v[96:99]\n"
"ds_write_addtid_b32 v48 offset:0\n"
"v_mfma_f32_16x16x32_bf16 v[96:99], v[108:111], a[40:43], v[96:99]\n"
"ds_write_addtid_b32 v49 offset:4640\n"
"global_load_dwordx4 v[44:47], v7, s[56:57] offset:1024 sc1\n"
"v_mfma_f32_16x16x32_bf16 v[96:99], v[64:67], a[44:47], v[96:99]\n"
"s_waitcnt lgkmcnt(5)\n"
"buffer_store_dwordx4 v[136:139], v15, s[40:43], s94 offen sc1\n"
"ds_write_addtid_b32 v50 offset:9344\n"
"v_add_f32 v56, v56, v52\n"
"v_add_f32 v57, v57, v53\n"
"v_mfma_f32_16x16x32_bf16 v[96:99], v[68:71], a[48:51], v[96:99]\n"
"ds_write_addtid_b32 v51 offset:13984\n"
"v_add_f32 v58, v58, v54\n"
"v_mfma_f32_16x16x32_bf16 v[96:99], v[72:75], a[52:55], v[96:99]\n"
"v_add_f32 v59, v59, v55\n"
"v_add_f32 v56, v56, v60\n"
"v_mfma_f32_16x16x32_bf16 v[96:99], v[76:79], a[56:59], v[96:99]\n"
"v_add_f32 v57, v57, v61\n"
"v_add_f32 v58, v58, v62\n"
"v_mfma_f32_16x16x32_bf16 v[96:99], v[80:83], a[60:63], v[96:99]\n"
"v_add_f32 v59, v59, v63\n"
"v_add_f32 v56, v56, v128\n"
"v_mfma_f32_16x16x32_bf16 v[124:127], v[100:103], a[64:67], 0\n"
"global_load_dwordx4 v[48:51], v8, s[56:57] offset:0 sc1\n"
"v_add_f32 v57, v57, v129\n"
"v_mfma_f32_16x16x32_bf16 v[124:127], v[104:107], a[68:71], v[124:127]\n"
"ds_write_addtid_b32 v96 offset:256\n"
"v_add_f32 v58, v58, v130\n"
"v_mfma_f32_16x16x32_bf16 v[124:127], v[108:111], a[72:75], v[124:127]\n"
"ds_write_addtid_b32 v97 offset:4896\n"
"v_add_f32 v59, v59, v131\n"
"v_mfma_f32_16x16x32_bf16 v[124:127], v[64:67], a[76:79], v[124:127]\n"
"s_add_u32 s95, s50, 0xffd00000\n"
"s_and_b32 s94, s95, 0xfbffffff\n"
"ds_write_addtid_b32 v98 offset:9600\n"
"v_mfma_f32_16x16x32_bf16 v[124:127], v[68:71], a[80:83], v[124:127]\n"
"ds_write_addtid_b32 v99 offset:14240\n"
"global_load_dwordx4 v[52:55], v8, s[56:57] offset:1024 sc1\n"
"v_mfma_f32_16x16x32_bf16 v[124:127], v[72:75], a[84:87], v[124:127]\n"
"v_fma_f32 v60, v18, v141, v26\n"
"v_fma_f32 v61, v19, v141, v27\n"
"v_mfma_f32_16x16x32_bf16 v[124:127], v[76:79], a[88:91], v[124:127]\n"
"v_fma_f32 v62, v20, v141, v28\n"
"v_fma_f32 v63, v21, v141, v29\n"
"v_mfma_f32_16x16x32_bf16 v[124:127], v[80:83], a[92:95], v[124:127]\n"
"v_fma_f32 v56, v56, v140, v60\n"
"v_fma_f32 v57, v57, v140, v61\n"
"v_mfma_f32_16x16x32_bf16 v[96:99], v[100:103], a[96:99], 0\n"
"v_fma_f32 v58, v58, v140, v62\n"
"v_fma_f32 v59, v59, v140, v63\n"
"v_mfma_f32_16x16x32_bf16 v[96:99], v[104:107], a[100:103], v[96:99]\n"
"ds_write_addtid_b32 v124 offset:512\n"
"v_mul_f32_e64 v56, v56, |v56|\n"
"v_mfma_f32_16x16x32_bf16 v[96:99], v[108:111], a[104:107], v[96:99]\n"
"ds_write_addtid_b32 v125 offset:5152\n"
"v_mul_f32_e64 v57, v57, |v57|\n"
"v_mfma_f32_16x16x32_bf16 v[96:99], v[64:67], a[108:111], v[96:99]\n"
"ds_write_addtid_b32 v126 offset:9856\n"
"v_mul_f32_e64 v58, v58, |v58|\n"
"v_mfma_f32_16x16x32_bf16 v[96:99], v[68:71], a[112:115], v[96:99]\n"
"ds_write_addtid_b32 v127 offset:14496\n"
"v_mul_f32_e64 v59, v59, |v59|\n"
"v_mfma_f32_16x16x32_bf16 v[96:99], v[72:75], a[116:119], v[96:99]\n"
"v_cvt_pk_bf16_f32 v128, v56, v57\n"
"v_cvt_pk_bf16_f32 v129, v58, v59\n"
"v_mfma_f32_16x16x32_bf16 v[96:99], v[76:79], a[120:123], v[96:99]\n"
"v_pk_max_i16 v128, v128, 0\n"
"v_pk_max_i16 v129, v129, 0\n"
"v_mfma_f32_16x16x32_bf16 v[96:99], v[80:83], a[124:127], v[96:99]\n"
"s_waitcnt lgkmcnt(12)\n"
"v_add_f32 v84, v84, v88\n"
"v_add_f32 v85, v85, v89\n"
"v_mfma_f32_16x16x32_bf16 v[56:59], v[100:103], a[128:131], 0\n"
"v_add_f32 v86, v86, v90\n"
"v_add_f32 v87, v87, v91\n"
"v_mfma_f32_16x16x32_bf16 v[56:59], v[104:107], a[132:135], v[56:59]\n"
"ds_write_addtid_b32 v96 offset:768\n"
"v_add_f32 v84, v84, v92\n"
"v_mfma_f32_16x16x32_bf16 v[56:59], v[108:111], a[136:139], v[56:59]\n"
"ds_write_addtid_b32 v97 offset:5408\n"
"v_add_f32 v85, v85, v93\n"
"v_mfma_f32_16x16x32_bf16 v[56:59], v[64:67], a[140:143], v[56:59]\n"
"ds_write_addtid_b32 v98 offset:10112\n"
"v_add_f32 v86, v86, v94\n"
"v_mfma_f32_16x16x32_bf16 v[56:59], v[68:71], a[144:147], v[56:59]\n"
"ds_write_addtid_b32 v99 offset:14752\n"
"v_add_f32 v87, v87, v95\n"
"v_mfma_f32_16x16x32_bf16 v[56:59], v[72:75], a[148:151], v[56:59]\n"
"v_add_f32 v84, v84, v132\n"
"v_add_f32 v85, v85, v133\n"
"v_mfma_f32_16x16x32_bf16 v[56:59], v[76:79], a[152:155], v[56:59]\n"
"v_add_f32 v86, v86, v134\n"
"v_add_f32 v87, v87, v135\n"
"v_mfma_f32_16x16x32_bf16 v[56:59], v[80:83], a[156:159], v[56:59]\n"
"v_fma_f32 v60, v22, v141, v30\n"
"v_fma_f32 v61, v23, v141, v31\n"
"v_mfma_f32_16x16x32_bf16 v[88:91], v[100:103], a[160:163], 0\n"
"v_fma_f32 v62, v24, v141, v32\n"
"v_fma_f32 v63, v25, v141, v33\n"
"v_mfma_f32_16x16x32_bf16 v[88:91], v[104:107], a[164:167], v[88:91]\n"
"ds_write_addtid_b32 v56 offset:1024\n"
"v_fma_f32 v84, v84, v140, v60\n"
"v_mfma_f32_16x16x32_bf16 v[88:91], v[108:111], a[168:171], v[88:91]\n"
"s_add_u32 s58, s50, 0x180000\n"
"ds_write_addtid_b32 v57 offset:5664\n"
"v_fma_f32 v85, v85, v140, v61\n"
"v_mfma_f32_16x16x32_bf16 v[88:91], v[64:67], a[172:175], v[88:91]\n"
"s_and_b32 s58, s58, 0x3ffffff\n"
"ds_write_addtid_b32 v58 offset:10368\n"
"v_fma_f32 v86, v86, v140, v62\n"
"v_mfma_f32_16x16x32_bf16 v[88:91], v[68:71], a[176:179], v[88:91]\n"
"s_add_u32 s56, s40, s58\n"
"ds_write_addtid_b32 v59 offset:15008\n"
"v_fma_f32 v87, v87, v140, v63\n"
"v_mfma_f32_16x16x32_bf16 v[88:91], v[72:75], a[180:183], v[88:91]\n"
"s_addc_u32 s57, s41, 0\n"
"v_mul_f32_e64 v84, v84, |v84|\n"
"v_mul_f32_e64 v85, v85, |v85|\n"
"v_mfma_f32_16x16x32_bf16 v[88:91], v[76:79], a[184:187], v[88:91]\n"
"s_waitcnt vmcnt(6)\n"
"v_cmp_ne_u64 vcc, s[48:49], v[144:145]\n"
"v_mul_f32_e64 v86, v86, |v86|\n"
"v_mfma_f32_16x16x32_bf16 v[88:91], v[80:83], a[188:191], v[88:91]\n"
"v_mul_f32_e64 v87, v87, |v87|\n"
"v_cvt_pk_bf16_f32 v130, v84, v85\n"
"v_mfma_f32_16x16x32_bf16 v[92:95], a[192:195], v[100:103], 0\n"
"s_cbranch_vccnz .LF_3s\n"
".LF_3r:\n"
"v_cvt_pk_bf16_f32 v131, v86, v87\n"
"v_mfma_f32_16x16x32_bf16 v[92:95], a[196:199], v[104:107], v[92:95]\n"
"s_add_u32 s50, s50, 0x180000\n"
"ds_write_addtid_b32 v88 offset:1280\n"
"global_load_dwordx4 v[56:59], v5, s[56:57] offset:0 sc1\n"
"v_mfma_f32_16x16x32_bf16 v[92:95], a[200:203], v[108:111], v[92:95]\n"
"ds_write_addtid_b32 v89 offset:5920\n"
"v_pk_max_i16 v130, v130, 0\n"
"v_mfma_f32_16x16x32_bf16 v[92:95], a[204:207], v[64:67], v[92:95]\n"
"ds_write_addtid_b32 v90 offset:10624\n"
"v_pk_max_i16 v131, v131, 0\n"
"v_mfma_f32_16x16x32_bf16 v[92:95], a[208:211], v[68:71], v[92:95]\n"
"ds_write_addtid_b32 v91 offset:15264\n"
"v_mfma_f32_16x16x32_bf16 v[92:95], a[212:215], v[72:75], v[92:95]\n"
"v_mfma_f32_16x16x32_bf16 v[92:95], a[216:219], v[76:79], v[92:95]\n"
"v_mfma_f32_16x16x32_bf16 v[92:95], a[220:223], v[80:83], v[92:95]\n"
"v_mfma_f32_16x16x32_bf16 v[88:91], a[224:227], v[100:103], 0\n"
"s_waitcnt lgkmcnt(0)\n"
"global_load_dwordx4 v[60:63], v5, s[56:57] offset:1024 sc1\n"
"v_mfma_f32_16x16x32_bf16 v[88:91], a[228:231], v[104:107], v[88:91]\n"
"s_barrier\n"
"v_mfma_f32_16x16x32_bf16 v[88:91], a[232:235], v[108:111], v[88:91]\n"
"ds_read2st64_b32 v[136:137], v11 offset0:0 offset1:1\n"
"ds_read_b128 v[96:99], v2 offset:1536\n"
"v_mfma_f32_16x16x32_bf16 v[88:91], a[236:239], v[64:67], v[88:91]\n"
"s_waitcnt vmcnt(4)\n"
"buffer_load_dwordx2 v[138:139], v9, s[40:43], s50 offen sc1\n"
"v_mfma_f32_16x16x32_bf16 v[88:91], a[240:243], v[68:71], v[88:91]\n"
"global_load_dwordx4 v[64:67], v6, s[56:57] offset:0 sc1\n"
"ds_read_b128 v[84:87], v3 offset:512\n"
"v_mfma_f32_16x16x32_bf16 v[88:91], a[244:247], v[72:75], v[88:91]\n"
"ds_read_b128 v[100:103], v4 offset:2560\n"
"ds_read_b128 v[104:107], v2 offset:1792\n"
"v_mfma_f32_16x16x32_bf16 v[88:91], a[248:251], v[76:79], v[88:91]\n"
"ds_read_b128 v[108:111], v3 offset:768\n"
"ds_read_b128 v[124:127], v4 offset:2816\n"
"v_mfma_f32_16x16x32_bf16 v[88:91], a[252:255], v[80:83], v[88:91]\n"
"buffer_store_dwordx2 v[0:1], v16, s[40:43], s93 offen offset:0 sc1\n"
"v_mfma_f32_16x16x32_bf16 v[132:135], v[112:115], a[0:3], 0\n"
"buffer_store_dwordx4 v[128:131], v15, s[40:43], s94 offen sc1\n"
"v_mfma_f32_16x16x32_bf16 v[132:135], v[116:119], a[4:7], v[132:135]\n"
"v_mfma_f32_16x16x32_bf16 v[132:135], v[120:123], a[8:11], v[132:135]\n"
"global_load_dwordx4 v[68:71], v6, s[56:57] offset:1024 sc1\n"
"v_mfma_f32_16x16x32_bf16 v[132:135], v[36:39], a[12:15], v[132:135]\n"
"v_mfma_f32_16x16x32_bf16 v[132:135], v[40:43], a[16:19], v[132:135]\n"
"s_waitcnt lgkmcnt(3)\n"
"v_add_f32 v96, v96, v84\n"
"v_add_f32 v97, v97, v85\n"
"v_mfma_f32_16x16x32_bf16 v[132:135], v[44:47], a[20:23], v[132:135]\n"
"s_waitcnt vmcnt(7)\n"
"global_load_dwordx4 v[72:75], v7, s[56:57] offset:0 sc1\n"
"v_add_f32 v98, v98, v86\n"
"v_mfma_f32_16x16x32_bf16 v[132:135], v[48:51], a[24:27], v[132:135]\n"
"v_add_f32 v99, v99, v87\n"
"v_add_f32 v96, v96, v100\n"
"v_mfma_f32_16x16x32_bf16 v[132:135], v[52:55], a[28:31], v[132:135]\n"
"global_load_dwordx4 v[76:79], v7, s[56:57] offset:1024 sc1\n"
"v_add_f32 v97, v97, v101\n"
"v_mfma_f32_16x16x32_bf16 v[128:131], v[112:115], a[32:35], 0\n"
"v_add_f32 v98, v98, v102\n"
"v_add_f32 v99, v99, v103\n"
"v_mfma_f32_16x16x32_bf16 v[128:131], v[116:119], a[36:39], v[128:131]\n"
"ds_write_addtid_b32 v132 offset:1536\n"
"v_add_f32 v96, v96, v92\n"
"v_mfma_f32_16x16x32_bf16 v[128:131], v[120:123], a[40:43], v[128:131]\n"
"ds_write_addtid_b32 v133 offset:6176\n"
"v_add_f32 v97, v97, v93\n"
"v_mfma_f32_16x16x32_bf16 v[128:131], v[36:39], a[44:47], v[128:131]\n"
"ds_write_addtid_b32 v134 offset:10880\n"
"v_add_f32 v98, v98, v94\n"
"v_mfma_f32_16x16x32_bf16 v[128:131], v[40:43], a[48:51], v[128:131]\n"
"ds_write_addtid_b32 v135 offset:15520\n"
"v_add_f32 v99, v99, v95\n"
"v_mfma_f32_16x16x32_bf16 v[128:131], v[44:47], a[52:55], v[128:131]\n"
"s_add_u32 s94, s50, 0xffd00000\n"
"s_and_b32 s93, s94, 0xfbffffff\n"
"v_fma_f32 v84, v18, v137, v26\n"
"v_mfma_f32_16x16x32_bf16 v[128:131], v[48:51], a[56:59], v[128:131]\n"
"v_fma_f32 v85, v19, v137, v27\n"
"v_fma_f32 v86, v20, v137, v28\n"
"v_mfma_f32_16x16x32_bf16 v[128:131], v[52:55], a[60:63], v[128:131]\n"
"v_fma_f32 v87, v21, v137, v29\n"
"v_fma_f32 v96, v96, v136, v84\n"
"v_mfma_f32_16x16x32_bf16 v[92:95], v[112:115], a[64:67], 0\n"
"v_fma_f32 v97, v97, v136, v85\n"
"v_fma_f32 v98, v98, v136, v86\n"
"v_mfma_f32_16x16x32_bf16 v[92:95], v[116:119], a[68:71], v[92:95]\n"
"ds_write_addtid_b32 v128 offset:1792\n"
"global_load_dwordx4 v[80:83], v8, s[56:57] offset:0 sc1\n"
"v_mfma_f32_16x16x32_bf16 v[92:95], v[120:123], a[72:75], v[92:95]\n"
"ds_write_addtid_b32 v129 offset:6432\n"
"v_fma_f32 v99, v99, v136, v87\n"
"v_mfma_f32_16x16x32_bf16 v[92:95], v[36:39], a[76:79], v[92:95]\n"
"ds_write_addtid_b32 v130 offset:11136\n"
"v_mul_f32_e64 v96, v96, |v96|\n"
"v_mfma_f32_16x16x32_bf16 v[92:95], v[40:43], a[80:83], v[92:95]\n"
"ds_write_addtid_b32 v131 offset:15776\n"
"v_mul_f32_e64 v97, v97, |v97|\n"
"v_mfma_f32_16x16x32_bf16 v[92:95], v[44:47], a[84:87], v[92:95]\n"
"global_load_dwordx4 v[84:87], v8, s[56:57] offset:1024 sc1\n"
"v_mul_f32_e64 v98, v98, |v98|\n"
"v_mfma_f32_16x16x32_bf16 v[92:95], v[48:51], a[88:91], v[92:95]\n"
"v_mul_f32_e64 v99, v99, |v99|\n"
"v_cvt_pk_bf16_f32 v100, v96, v97\n"
"v_mfma_f32_16x16x32_bf16 v[92:95], v[52:55], a[92:95], v[92:95]\n"
"v_cvt_pk_bf16_f32 v101, v98, v99\n"
"v_pk_max_i16 v100, v100, 0\n"
"v_mfma_f32_16x16x32_bf16 v[128:131], v[112:115], a[96:99], 0\n"
"v_pk_max_i16 v101, v101, 0\n"
"v_mfma_f32_16x16x32_bf16 v[128:131], v[116:119], a[100:103], v[128:131]\n"
"s_waitcnt lgkmcnt(8)\n"
"ds_write_addtid_b32 v92 offset:2048\n"
"v_add_f32 v104, v104, v108\n"
"v_mfma_f32_16x16x32_bf16 v[128:131], v[120:123], a[104:107], v[128:131]\n"
"ds_write_addtid_b32 v93 offset:6688\n"
"v_add_f32 v105, v105, v109\n"
"v_mfma_f32_16x16x32_bf16 v[128:131], v[36:39], a[108:111], v[128:131]\n"
"ds_write_addtid_b32 v94 offset:11392\n"
"v_add_f32 v106, v106, v110\n"
"v_mfma_f32_16x16x32_bf16 v[128:131], v[40:43], a[112:115], v[128:131]\n"
"ds_write_addtid_b32 v95 offset:16032\n"
"v_add_f32 v107, v107, v111\n"
"v_mfma_f32_16x16x32_bf16 v[128:131], v[44:47], a[116:119], v[128:131]\n"
"v_add_f32 v104, v104, v124\n"
"v_add_f32 v105, v105, v125\n"
"v_mfma_f32_16x16x32_bf16 v[128:131], v[48:51], a[120:123], v[128:131]\n"
"v_add_f32 v106, v106, v126\n"
"v_add_f32 v107, v107, v127\n"
"v_mfma_f32_16x16x32_bf16 v[128:131], v[52:55], a[124:127], v[128:131]\n"
"v_add_f32 v104, v104, v88\n"
"v_add_f32 v105, v105, v89\n"
"v_mfma_f32_16x16x32_bf16 v[92:95], v[112:115], a[128:131], 0\n"
"v_add_f32 v106, v106, v90\n"
"v_add_f32 v107, v107, v91\n"
"v_mfma_f32_16x16x32_bf16 v[92:95], v[116:119], a[132:135], v[92:95]\n"
"ds_write_addtid_b32 v128 offset:2304\n"
"v_fma_f32 v96, v22, v137, v30\n"
"v_mfma_f32_16x16x32_bf16 v[92:95], v[120:123], a[136:139], v[92:95]\n"
"ds_write_addtid_b32 v129 offset:6944\n"
"v_fma_f32 v97, v23, v137, v31\n"
"v_mfma_f32_16x16x32_bf16 v[92:95], v[36:39], a[140:143], v[92:95]\n"
"ds_write_addtid_b32 v130 offset:11648\n"
"v_fma_f32 v98, v24, v137, v32\n"
"v_mfma_f32_16x16x32_bf16 v[92:95], v[40:43], a[144:147], v[92:95]\n"
"ds_write_addtid_b32 v131 offset:16288\n"
"v_fma_f32 v99, v25, v137, v33\n"
"v_mfma_f32_16x16x32_bf16 v[92:95], v[44:47], a[148:151], v[92:95]\n"
"v_fma_f32 v104, v104, v136, v96\n"
"v_fma_f32 v105, v105, v136, v97\n"
"v_mfma_f32_16x16x32_bf16 v[92:95], v[48:51], a[152:155], v[92:95]\n"
"v_fma_f32 v106, v106, v136, v98\n"
"v_fma_f32 v107, v107, v136, v99\n"
"v_mfma_f32_16x16x32_bf16 v[92:95], v[52:55], a[156:159], v[92:95]\n"
"v_mul_f32_e64 v104, v104, |v104|\n"
"v_mul_f32_e64 v105, v105, |v105|\n"
"v_mfma_f32_16x16x32_bf16 v[96:99], v[112:115], a[160:163], 0\n"
"v_mul_f32_e64 v106, v106, |v106|\n"
"v_mul_f32_e64 v107, v107, |v107|\n"
"v_mfma_f32_16x16x32_bf16 v[96:99], v[116:119], a[164:167], v[96:99]\n"
"ds_write_addtid_b32 v92 offset:2560\n"
"v_cvt_pk_bf16_f32 v102, v104, v105\n"
"v_mfma_f32_16x16x32_bf16 v[96:99], v[120:123], a[168:171], v[96:99]\n"
"s_add_u32 s58, s50, 0x180000\n"
"ds_write_addtid_b32 v93 offset:7200\n"
"v_cvt_pk_bf16_f32 v103, v106, v107\n"
"v_mfma_f32_16x16x32_bf16 v[96:99], v[36:39], a[172:175], v[96:99]\n"
"s_and_b32 s58, s58, 0x3ffffff\n"
"ds_write_addtid_b32 v94 offset:11904\n"
"v_pk_max_i16 v102, v102, 0\n"
"v_mfma_f32_16x16x32_bf16 v[96:99], v[40:43], a[176:179], v[96:99]\n"
"s_add_u32 s56, s40, s58\n"
"ds_write_addtid_b32 v95 offset:16544\n"
"v_pk_max_i16 v103, v103, 0\n"
"v_mfma_f32_16x16x32_bf16 v[96:99], v[44:47], a[180:183], v[96:99]\n"
"s_addc_u32 s57, s41, 0\n"
"buffer_store_dwordx4 v[100:103], v15, s[40:43], s93 offen sc1\n"
"v_mfma_f32_16x16x32_bf16 v[96:99], v[48:51], a[184:187], v[96:99]\n"
"s_waitcnt vmcnt(9)\n"
"v_cmp_ne_u64 vcc, s[48:49], v[138:139]\n"
"v_mfma_f32_16x16x32_bf16 v[96:99], v[52:55], a[188:191], v[96:99]\n"
"v_mfma_f32_16x16x32_bf16 v[100:103], a[192:195], v[112:115], 0\n"
"s_cbranch_vccnz .LF_4s\n"
".LF_4r:\n"
"v_mfma_f32_16x16x32_bf16 v[100:103], a[196:199], v[116:119], v[100:103]\n"
"s_add_u32 s50, s50, 0x180000\n"
"ds_write_addtid_b32 v96 offset:2816\n"
"v_mfma_f32_16x16x32_bf16 v[100:103], a[200:203], v[120:123], v[100:103]\n"
"s_waitcnt vmcnt(6)\n"
"buffer_store_dwordx2 v[0:1], v16, s[40:43], s95 offen offset:0 sc1\n"
"ds_write_addtid_b32 v97 offset:7456\n"
"v_mfma_f32_16x16x32_bf16 v[100:103], a[204:207], v[36:39], v[100:103]\n"
"ds_write_addtid_b32 v98 offset:12160\n"
"global_load_dwordx4 v[88:91], v5, s[56:57] offset:0 sc1\n"
"v_mfma_f32_16x16x32_bf16 v[100:103], a[208:211], v[40:43], v[100:103]\n"
"ds_write_addtid_b32 v99 offset:16800\n"
"v_mfma_f32_16x16x32_bf16 v[100:103], a[212:215], v[44:47], v[100:103]\n"
"v_mfma_f32_16x16x32_bf16 v[100:103], a[216:219], v[48:51], v[100:103]\n"
"v_mfma_f32_16x16x32_bf16 v[100:103], a[220:223], v[52:55], v[100:103]\n"
"v_mfma_f32_16x16x32_bf16 v[104:107], a[224:227], v[112:115], 0\n"
"s_waitcnt lgkmcnt(0)\n"
"global_load_dwordx4 v[92:95], v5, s[56:57] offset:1024 sc1\n"
"v_mfma_f32_16x16x32_bf16 v[104:107], a[228:231], v[116:119], v[104:107]\n"
"s_barrier\n"
"v_mfma_f32_16x16x32_bf16 v[104:107], a[232:235], v[120:123], v[104:107]\n"
"ds_read2st64_b32 v[132:133], v12 offset0:0 offset1:1\n"
"ds_read_b128 v[108:111], v2 offset:0\n"
"v_mfma_f32_16x16x32_bf16 v[104:107], a[236:239], v[36:39], v[104:107]\n"
"ds_read_b128 v[96:99], v3 offset:2048\n"
"ds_read_b128 v[112:115], v4 offset:1024\n"
"v_mfma_f32_16x16x32_bf16 v[104:107], a[240:243], v[40:43], v[104:107]\n"
"buffer_load_dwordx2 v[134:135], v9, s[40:43], s50 offen sc1\n"
"v_mfma_f32_16x16x32_bf16 v[104:107], a[244:247], v[44:47], v[104:107]\n"
"global_load_dwordx4 v[36:39], v6, s[56:57] offset:0 sc1\n"
"ds_read_b128 v[116:119], v2 offset:256\n"
"v_mfma_f32_16x16x32_bf16 v[104:107], a[248:251], v[48:51], v[104:107]\n"
"ds_read_b128 v[120:123], v3 offset:2304\n"
"ds_read_b128 v[124:127], v4 offset:1280\n"
"v_mfma_f32_16x16x32_bf16 v[104:107], a[252:255], v[52:55], v[104:107]\n"
"global_load_dwordx4 v[40:43], v6, s[56:57] offset:1024 sc1\n"
"v_mfma_f32_16x16x32_bf16 v[128:131], v[56:59], a[0:3], 0\n"
"v_mfma_f32_16x16x32_bf16 v[128:131], v[60:63], a[4:7], v[128:131]\n"
"s_waitcnt vmcnt(11)\n"
"v_mfma_f32_16x16x32_bf16 v[128:131], v[64:67], a[8:11], v[128:131]\n"
"s_waitcnt lgkmcnt(3)\n"
"v_add_f32 v108, v108, v96\n"
"v_add_f32 v109, v109, v97\n"
"v_mfma_f32_16x16x32_bf16 v[128:131], v[68:71], a[12:15], v[128:131]\n"
"s_waitcnt vmcnt(9)\n"
"v_add_f32 v110, v110, v98\n"
"v_add_f32 v111, v111, v99\n"
"v_mfma_f32_16x16x32_bf16 v[128:131], v[72:75], a[16:19], v[128:131]\n"
"v_add_f32 v108, v108, v112\n"
"v_add_f32 v109, v109, v113\n"
"v_mfma_f32_16x16x32_bf16 v[128:131], v[76:79], a[20:23], v[128:131]\n"
"s_waitcnt vmcnt(7)\n"
"v_add_f32 v110, v110, v114\n"
"v_add_f32 v111, v111, v115\n"
"v_mfma_f32_16x16x32_bf16 v[128:131], v[80:83], a[24:27], v[128:131]\n"
"v_add_f32 v108, v108, v100\n"
"v_add_f32 v109, v109, v101\n"
"v_mfma_f32_16x16x32_bf16 v[128:131], v[84:87], a[28:31], v[128:131]\n"
"global_load_dwordx4 v[44:47], v7, s[56:57] offset:0 sc1\n"
"v_add_f32 v110, v110, v102\n"
"v_mfma_f32_16x16x32_bf16 v[112:115], v[56:59], a[32:35], 0\n"
"v_add_f32 v111, v111, v103\n"
"v_mfma_f32_16x16x32_bf16 v[112:115], v[60:63], a[36:39], v[112:115]\n"
"ds_write_addtid_b32 v128 offset:0\n"
"global_load_dwordx4 v[48:51], v7, s[56:57] offset:1024 sc1\n"
"v_mfma_f32_16x16x32_bf16 v[112:115], v[64:67], a[40:43], v[112:115]\n"
"s_add_u32 s95, s50, 0xffd00000\n"
"s_and_b32 s93, s95, 0xfbffffff\n"
"ds_write_addtid_b32 v129 offset:4640\n"
"v_mfma_f32_16x16x32_bf16 v[112:115], v[68:71], a[44:47], v[112:115]\n"
"ds_write_addtid_b32 v130 offset:9344\n"
"v_fma_f32 v96, v18, v133, v26\n"
"v_mfma_f32_16x16x32_bf16 v[112:115], v[72:75], a[48:51], v[112:115]\n"
"ds_write_addtid_b32 v131 offset:13984\n"
"v_fma_f32 v97, v19, v133, v27\n"
"v_mfma_f32_16x16x32_bf16 v[112:115], v[76:79], a[52:55], v[112:115]\n"
"v_fma_f32 v98, v20, v133, v28\n"
"v_fma_f32 v99, v21, v133, v29\n"
"v_mfma_f32_16x16x32_bf16 v[112:115], v[80:83], a[56:59], v[112:115]\n"
"v_fma_f32 v108, v108, v132, v96\n"
"v_fma_f32 v109, v109, v132, v97\n"
"v_mfma_f32_16x16x32_bf16 v[112:115], v[84:87], a[60:63], v[112:115]\n"
"v_fma_f32 v110, v110, v132, v98\n"
"v_fma_f32 v111, v111, v132, v99\n"
"v_mfma_f32_16x16x32_bf16 v[100:103], v[56:59], a[64:67], 0\n"
"global_load_dwordx4 v[52:55], v8, s[56:57] offset:0 sc1\n"
"v_mul_f32_e64 v108, v108, |v108|\n"
"v_mfma_f32_16x16x32_bf16 v[100:103], v[60:63], a[68:71], v[100:103]\n"
"ds_write_addtid_b32 v112 offset:256\n"
"v_mul_f32_e64 v109, v109, |v109|\n"
"v_mfma_f32_16x16x32_bf16 v[100:103], v[64:67], a[72:75], v[100:103]\n"
"ds_write_addtid_b32 v113 offset:4896\n"
"v_mul_f32_e64 v110, v110, |v110|\n"
"v_mfma_f32_16x16x32_bf16 v[100:103], v[68:71], a[76:79], v[100:103]\n"
"ds_write_addtid_b32 v114 offset:9600\n"
"v_mul_f32_e64 v111, v111, |v111|\n"
"v_mfma_f32_16x16x32_bf16 v[100:103], v[72:75], a[80:83], v[100:103]\n"
"ds_write_addtid_b32 v115 offset:14240\n"
"global_load_dwordx4 v[96:99], v8, s[56:57] offset:1024 sc1\n"
"v_mfma_f32_16x16x32_bf16 v[100:103], v[76:79], a[84:87], v[100:103]\n"
"v_cvt_pk_bf16_f32 v128, v108, v109\n"
"v_cvt_pk_bf16_f32 v129, v110, v111\n"
"v_mfma_f32_16x16x32_bf16 v[100:103], v[80:83], a[88:91], v[100:103]\n"
"v_pk_max_i16 v128, v128, 0\n"
"v_pk_max_i16 v129, v129, 0\n"
"v_mfma_f32_16x16x32_bf16 v[100:103], v[84:87], a[92:95], v[100:103]\n"
"s_waitcnt lgkmcnt(8)\n"
"v_add_f32 v116, v116, v120\n"
"v_add_f32 v117, v117, v121\n"
"v_mfma_f32_16x16x32_bf16 v[108:111], v[56:59], a[96:99], 0\n"
"v_add_f32 v118, v118, v122\n"
"v_add_f32 v119, v119, v123\n"
"v_mfma_f32_16x16x32_bf16 v[108:111], v[60:63], a[100:103], v[108:111]\n"
"ds_write_addtid_b32 v100 offset:512\n"
"v_add_f32 v116, v116, v124\n"
"v_mfma_f32_16x16x32_bf16 v[108:111], v[64:67], a[104:107], v[108:111]\n"
"ds_write_addtid_b32 v101 offset:5152\n"
"v_add_f32 v117, v117, v125\n"
"v_mfma_f32_16x16x32_bf16 v[108:111], v[68:71], a[108:111], v[108:111]\n"
"ds_write_addtid_b32 v102 offset:9856\n"
"v_add_f32 v118, v118, v126\n"
"v_mfma_f32_16x16x32_bf16 v[108:111], v[72:75], a[112:115], v[108:111]\n"
"ds_write_addtid_b32 v103 offset:14496\n"
"v_add_f32 v119, v119, v127\n"
"v_mfma_f32_16x16x32_bf16 v[108:111], v[76:79], a[116:119], v[108:111]\n"
"s_waitcnt vmcnt(10)\n"
"buffer_store_dwordx2 v[0:1], v16, s[40:43], s94 offen offset:0 sc1\n"
"v_add_f32 v116, v116, v104\n"
"v_mfma_f32_16x16x32_bf16 v[108:111], v[80:83], a[120:123], v[108:111]\n"
"v_add_f32 v117, v117, v105\n"
"v_add_f32 v118, v118, v106\n"
"v_mfma_f32_16x16x32_bf16 v[108:111], v[84:87], a[124:127], v[108:111]\n"
"v_add_f32 v119, v119, v107\n"
"v_fma_f32 v100, v22, v133, v30\n"
"v_mfma_f32_16x16x32_bf16 v[112:115], v[56:59], a[128:131], 0\n"
"v_fma_f32 v101, v23, v133, v31\n"
"v_fma_f32 v102, v24, v133, v32\n"
"v_mfma_f32_16x16x32_bf16 v[112:115], v[60:63], a[132:135], v[112:115]\n"
"ds_write_addtid_b32 v108 offset:768\n"
"v_fma_f32 v103, v25, v133, v33\n"
"v_mfma_f32_16x16x32_bf16 v[112:115], v[64:67], a[136:139], v[112:115]\n"
"ds_write_addtid_b32 v109 offset:5408\n"
"v_fma_f32 v116, v116, v132, v100\n"
"v_mfma_f32_16x16x32_bf16 v[112:115], v[68:71], a[140:143], v[112:115]\n"
"ds_write_addtid_b32 v110 offset:10112\n"
"v_fma_f32 v117, v117, v132, v101\n"
"v_mfma_f32_16x16x32_bf16 v[112:115], v[72:75], a[144:147], v[112:115]\n"
"ds_write_addtid_b32 v111 offset:14752\n"
"v_fma_f32 v118, v118, v132, v102\n"
"v_mfma_f32_16x16x32_bf16 v[112:115], v[76:79], a[148:151], v[112:115]\n"
"v_fma_f32 v119, v119, v132, v103\n"
"v_mul_f32_e64 v116, v116, |v116|\n"
"v_mfma_f32_16x16x32_bf16 v[112:115], v[80:83], a[152:155], v[112:115]\n"
"v_mul_f32_e64 v117, v117, |v117|\n"
"v_mul_f32_e64 v118, v118, |v118|\n"
"v_mfma_f32_16x16x32_bf16 v[112:115], v[84:87], a[156:159], v[112:115]\n"
"v_mul_f32_e64 v119, v119, |v119|\n"
"v_cvt_pk_bf16_f32 v130, v116, v117\n"
"v_mfma_f32_16x16x32_bf16 v[108:111], v[56:59], a[160:163], 0\n"
"v_cvt_pk_bf16_f32 v131, v118, v119\n"
"v_pk_max_i16 v130, v130, 0\n"
"v_mfma_f32_16x16x32_bf16 v[108:111], v[60:63], a[164:167], v[108:111]\n"
"s_add_u32 s58, s50, 0x180000\n"
"ds_write_addtid_b32 v112 offset:1024\n"
"v_pk_max_i16 v131, v131, 0\n"
"v_mfma_f32_16x16x32_bf16 v[108:111], v[64:67], a[168:171], v[108:111]\n"
"s_and_b32 s58, s58, 0x3ffffff\n"
"ds_write_addtid_b32 v113 offset:5664\n"
"v_mfma_f32_16x16x32_bf16 v[108:111], v[68:71], a[172:175], v[108:111]\n"
"s_add_u32 s56, s40, s58\n"
"ds_write_addtid_b32 v114 offset:10368\n"
"v_mfma_f32_16x16x32_bf16 v[108:111], v[72:75], a[176:179], v[108:111]\n"
"s_addc_u32 s57, s41, 0\n"
"ds_write_addtid_b32 v115 offset:15008\n"
"v_mfma_f32_16x16x32_bf16 v[108:111], v[76:79], a[180:183], v[108:111]\n"
"s_waitcnt vmcnt(7)\n"
"buffer_store_dwordx4 v[128:131], v15, s[40:43], s93 offen sc1\n"
"v_cmp_ne_u64 vcc, s[48:49], v[134:135]\n"
"v_mfma_f32_16x16x32_bf16 v[108:111], v[80:83], a[184:187], v[108:111]\n"
"v_mfma_f32_16x16x32_bf16 v[108:111], v[84:87], a[188:191], v[108:111]\n"
"s_cbranch_vccnz .LF_1s\n"
".LF_1r:\n"
"v_mfma_f32_16x16x32_bf16 v[112:115], a[192:195], v[56:59], 0\n"
"s_add_u32 s50, s50, 0x180000\n"
"global_load_dwordx4 v[100:103], v5, s[56:57] offset:0 sc1\n"
"v_mfma_f32_16x16x32_bf16 v[112:115], a[196:199], v[60:63], v[112:115]\n"
"ds_write_addtid_b32 v108 offset:1280\n"
"global_load_dwordx4 v[124:127], v10, s[56:57] offset:0 sc1\n"
"v_mfma_f32_16x16x32_bf16 v[112:115], a[200:203], v[64:67], v[112:115]\n"
"ds_write_addtid_b32 v109 offset:5920\n"
"global_load_dwordx4 v[116:119], v10, s[56:57] offset:1024 sc1\n"
"v_mfma_f32_16x16x32_bf16 v[112:115], a[204:207], v[68:71], v[112:115]\n"
"ds_write_addtid_b32 v110 offset:10624\n"
"global_load_dwordx4 v[120:123], v10, s[56:57] offset:2048 sc1\n"
"v_mfma_f32_16x16x32_bf16 v[112:115], a[208:211], v[72:75], v[112:115]\n"
"ds_write_addtid_b32 v111 offset:15264\n"
"global_load_dwordx4 v[128:131], v10, s[56:57] offset:3072 sc1\n"
"v_mfma_f32_16x16x32_bf16 v[112:115], a[212:215], v[76:79], v[112:115]\n"
"global_load_dwordx4 v[104:107], v5, s[56:57] offset:1024 sc1\n"
"v_mfma_f32_16x16x32_bf16 v[112:115], a[216:219], v[80:83], v[112:115]\n"
"v_mfma_f32_16x16x32_bf16 v[112:115], a[220:223], v[84:87], v[112:115]\n"
"v_mfma_f32_16x16x32_bf16 v[132:135], a[224:227], v[56:59], 0\n"
"s_waitcnt lgkmcnt(0)\n"
"v_mfma_f32_16x16x32_bf16 v[132:135], a[228:231], v[60:63], v[132:135]\n"
"s_barrier\n"
"v_mfma_f32_16x16x32_bf16 v[132:135], a[232:235], v[64:67], v[132:135]\n"
"global_load_dwordx4 v[108:111], v6, s[56:57] offset:0 sc1\n"
"ds_read2st64_b32 v[152:153], v13 offset0:0 offset1:1\n"
"v_mfma_f32_16x16x32_bf16 v[132:135], a[236:239], v[68:71], v[132:135]\n"
"ds_read_b128 v[56:59], v2 offset:1536\n"
"ds_read_b128 v[60:63], v3 offset:512\n"
"v_mfma_f32_16x16x32_bf16 v[132:135], a[240:243], v[72:75], v[132:135]\n"
"ds_read_b128 v[136:139], v4 offset:2560\n"
"ds_read_b128 v[140:143], v2 offset:1792\n"
"v_mfma_f32_16x16x32_bf16 v[132:135], a[244:247], v[76:79], v[132:135]\n"
"buffer_load_dwordx2 v[154:155], v9, s[40:43], s50 offen sc1\n"
"v_mfma_f32_16x16x32_bf16 v[132:135], a[248:251], v[80:83], v[132:135]\n"
"global_load_dwordx4 v[64:67], v6, s[56:57] offset:1024 sc1\n"
"ds_read_b128 v[144:147], v3 offset:768\n"
"v_mfma_f32_16x16x32_bf16 v[132:135], a[252:255], v[84:87], v[132:135]\n"
"ds_read_b128 v[148:151], v4 offset:2816\n"
"v_mfma_f32_16x16x32_bf16 v[76:79], v[88:91], a[0:3], 0\n"
"v_mfma_f32_16x16x32_bf16 v[76:79], v[92:95], a[4:7], v[76:79]\n"
"s_waitcnt vmcnt(15)\n"
"v_mfma_f32_16x16x32_bf16 v[76:79], v[36:39], a[8:11], v[76:79]\n"
"s_waitcnt lgkmcnt(3)\n"
"v_add_f32 v56, v56, v60\n"
"v_add_f32 v57, v57, v61\n"
"v_mfma_f32_16x16x32_bf16 v[76:79], v[40:43], a[12:15], v[76:79]\n"
"s_waitcnt vmcnt(13)\n"
"v_add_f32 v58, v58, v62\n"
"v_add_f32 v59, v59, v63\n"
"v_mfma_f32_16x16x32_bf16 v[76:79], v[44:47], a[16:19], v[76:79]\n"
"global_load_dwordx4 v[68:71], v7, s[56:57] offset:0 sc1\n"
"v_add_f32 v56, v56, v136\n"
"v_mfma_f32_16x16x32_bf16 v[76:79], v[48:51], a[20:23], v[76:79]\n"
"s_waitcnt vmcnt(12)\n"
"v_add_f32 v57, v57, v137\n"
"v_add_f32 v58, v58, v138\n"
"v_mfma_f32_16x16x32_bf16 v[76:79], v[52:55], a[24:27], v[76:79]\n"
"v_add_f32 v59, v59, v139\n"
"v_add_f32 v56, v56, v112\n"
"v_mfma_f32_16x16x32_bf16 v[76:79], v[96:99], a[28:31], v[76:79]\n"
"v_add_f32 v57, v57, v113\n"
"v_add_f32 v58, v58, v114\n"
"v_mfma_f32_16x16x32_bf16 v[60:63], v[88:91], a[32:35], 0\n"
"v_add_f32 v59, v59, v115\n"
"v_mfma_f32_16x16x32_bf16 v[60:63], v[92:95], a[36:39], v[60:63]\n"
"ds_write_addtid_b32 v76 offset:1536\n"
"global_load_dwordx4 v[72:75], v7, s[56:57] offset:1024 sc1\n"
"v_mfma_f32_16x16x32_bf16 v[60:63], v[36:39], a[40:43], v[60:63]\n"
"s_add_u32 s93, s50, 0xffd00000\n"
"s_and_b32 s94, s93, 0xfbffffff\n"
"ds_write_addtid_b32 v77 offset:6176\n"
"v_mfma_f32_16x16x32_bf16 v[60:63], v[40:43], a[44:47], v[60:63]\n"
"ds_write_addtid_b32 v78 offset:10880\n"
"v_fma_f32 v80, v18, v153, v26\n"
"v_mfma_f32_16x16x32_bf16 v[60:63], v[44:47], a[48:51], v[60:63]\n"
"ds_write_addtid_b32 v79 offset:15520\n"
"v_fma_f32 v81, v19, v153, v27\n"
"v_mfma_f32_16x16x32_bf16 v[60:63], v[48:51], a[52:55], v[60:63]\n"
"v_fma_f32 v82, v20, v153, v28\n"
"v_fma_f32 v83, v21, v153, v29\n"
"v_mfma_f32_16x16x32_bf16 v[60:63], v[52:55], a[56:59], v[60:63]\n"
"v_fma_f32 v56, v56, v152, v80\n"
"v_fma_f32 v57, v57, v152, v81\n"
"v_mfma_f32_16x16x32_bf16 v[60:63], v[96:99], a[60:63], v[60:63]\n"
"global_load_dwordx4 v[76:79], v8, s[56:57] offset:0 sc1\n"
"v_fma_f32 v58, v58, v152, v82\n"
"v_mfma_f32_16x16x32_bf16 v[84:87], v[88:91], a[64:67], 0\n"
"v_fma_f32 v59, v59, v152, v83\n"
"v_mul_f32_e64 v56, v56, |v56|\n"
"v_mfma_f32_16x16x32_bf16 v[84:87], v[92:95], a[68:71], v[84:87]\n"
"ds_write_addtid_b32 v60 offset:1792\n"
"v_mul_f32_e64 v57, v57, |v57|\n"
"v_mfma_f32_16x16x32_bf16 v[84:87], v[36:39], a[72:75], v[84:87]\n"
"ds_write_addtid_b32 v61 offset:6432\n"
"v_mul_f32_e64 v58, v58, |v58|\n"
"v_mfma_f32_16x16x32_bf16 v[84:87], v[40:43], a[76:79], v[84:87]\n"
"ds_write_addtid_b32 v62 offset:11136\n"
"global_load_dwordx4 v[80:83], v8, s[56:57] offset:1024 sc1\n"
"v_mfma_f32_16x16x32_bf16 v[84:87], v[44:47], a[80:83], v[84:87]\n"
"ds_write_addtid_b32 v63 offset:15776\n"
"v_mul_f32_e64 v59, v59, |v59|\n"
"v_mfma_f32_16x16x32_bf16 v[84:87], v[48:51], a[84:87], v[84:87]\n"
"v_cvt_pk_bf16_f32 v136, v56, v57\n"
"v_cvt_pk_bf16_f32 v137, v58, v59\n"
"v_mfma_f32_16x16x32_bf16 v[84:87], v[52:55], a[88:91], v[84:87]\n"
"v_pk_max_i16 v136, v136, 0\n"
"v_pk_max_i16 v137, v137, 0\n"
"v_mfma_f32_16x16x32_bf16 v[84:87], v[96:99], a[92:95], v[84:87]\n"
"s_waitcnt lgkmcnt(8)\n"
"v_add_f32 v140, v140, v144\n"
"v_add_f32 v141, v141, v145\n"
"v_mfma_f32_16x16x32_bf16 v[56:59], v[88:91], a[96:99], 0\n"
"v_add_f32 v142, v142, v146\n"
"v_add_f32 v143, v143, v147\n"
"v_mfma_f32_16x16x32_bf16 v[56:59], v[92:95], a[100:103], v[56:59]\n"
"ds_write_addtid_b32 v84 offset:2048\n"
"v_add_f32 v140, v140, v148\n"
"v_mfma_f32_16x16x32_bf16 v[56:59], v[36:39], a[104:107], v[56:59]\n"
"ds_write_addtid_b32 v85 offset:6688\n"
"v_add_f32 v141, v141, v149\n"
"v_mfma_f32_16x16x32_bf16 v[56:59], v[40:43], a[108:111], v[56:59]\n"
"ds_write_addtid_b32 v86 offset:11392\n"
"v_add_f32 v142, v142, v150\n"
"v_mfma_f32_16x16x32_bf16 v[56:59], v[44:47], a[112:115], v[56:59]\n"
"ds_write_addtid_b32 v87 offset:16032\n"
"v_add_f32 v143, v143, v151\n"
"v_mfma_f32_16x16x32_bf16 v[56:59], v[48:51], a[116:119], v[56:59]\n"
"s_waitcnt vmcnt(13)\n"
"buffer_store_dwordx2 v[0:1], v16, s[40:43], s95 offen offset:0 sc1\n"
"v_add_f32 v140, v140, v132\n"
"v_mfma_f32_16x16x32_bf16 v[56:59], v[52:55], a[120:123], v[56:59]\n"
"v_add_f32 v141, v141, v133\n"
"v_add_f32 v142, v142, v134\n"
"v_mfma_f32_16x16x32_bf16 v[56:59], v[96:99], a[124:127], v[56:59]\n"
"v_add_f32 v143, v143, v135\n"
"v_fma_f32 v60, v22, v153, v30\n"
"v_mfma_f32_16x16x32_bf16 v[84:87], v[88:91], a[128:131], 0\n"
"v_fma_f32 v61, v23, v153, v31\n"
"v_fma_f32 v62, v24, v153, v32\n"
"v_mfma_f32_16x16x32_bf16 v[84:87], v[92:95], a[132:135], v[84:87]\n"
"ds_write_addtid_b32 v56 offset:2304\n"
"v_fma_f32 v63, v25, v153, v33\n"
"v_mfma_f32_16x16x32_bf16 v[84:87], v[36:39], a[136:139], v[84:87]\n"
"ds_write_addtid_b32 v57 offset:6944\n"
"v_fma_f32 v140, v140, v152, v60\n"
"v_mfma_f32_16x16x32_bf16 v[84:87], v[40:43], a[140:143], v[84:87]\n"
"ds_write_addtid_b32 v58 offset:11648\n"
"v_fma_f32 v141, v141, v152, v61\n"
"v_mfma_f32_16x16x32_bf16 v[84:87], v[44:47], a[144:147], v[84:87]\n"
"s_waitcnt vmcnt(9)\n"
"ds_write_addtid_b32 v59 offset:16288\n"
"v_add_f32 v124, v124, v116\n"
"v_mfma_f32_16x16x32_bf16 v[84:87], v[48:51], a[148:151], v[84:87]\n"
"v_add_f32 v125, v125, v117\n"
"v_add_f32 v126, v126, v118\n"
"v_mfma_f32_16x16x32_bf16 v[84:87], v[52:55], a[152:155], v[84:87]\n"
"v_add_f32 v127, v127, v119\n"
"v_add_f32 v120, v120, v128\n"
"v_mfma_f32_16x16x32_bf16 v[84:87], v[96:99], a[156:159], v[84:87]\n"
"v_add_f32 v121, v121, v129\n"
"v_add_f32 v122, v122, v130\n"
"v_mfma_f32_16x16x32_bf16 v[56:59], v[88:91], a[160:163], 0\n"
"v_add_f32 v123, v123, v131\n"
"v_add_f32 v124, v124, v120\n"
"v_mfma_f32_16x16x32_bf16 v[56:59], v[92:95], a[164:167], v[56:59]\n"
"ds_write_addtid_b32 v84 offset:2560\n"
"v_add_f32 v125, v125, v121\n"
"v_mfma_f32_16x16x32_bf16 v[56:59], v[36:39], a[168:171], v[56:59]\n"
"s_add_u32 s58, s50, 0x180000\n"
"ds_write_addtid_b32 v85 offset:7200\n"
"v_add_f32 v126, v126, v122\n"
"v_mfma_f32_16x16x32_bf16 v[56:59], v[40:43], a[172:175], v[56:59]\n"
"s_and_b32 s58, s58, 0x3ffffff\n"
"ds_write_addtid_b32 v86 offset:11904\n"
"v_add_f32 v127, v127, v123\n"
"v_mfma_f32_16x16x32_bf16 v[56:59], v[44:47], a[176:179], v[56:59]\n"
"s_add_u32 s56, s40, s58\n"
"ds_write_addtid_b32 v87 offset:16544\n"
"v_fma_f32 v142, v142, v152, v62\n"
"v_mfma_f32_16x16x32_bf16 v[56:59], v[48:51], a[180:183], v[56:59]\n"
"s_addc_u32 s57, s41, 0\n"
"v_permlane32_swap_b32 v124, v126\n"
"v_mfma_f32_16x16x32_bf16 v[56:59], v[52:55], a[184:187], v[56:59]\n"
"s_waitcnt vmcnt(6)\n"
"v_cmp_ne_u64 vcc, s[48:49], v[154:155]\n"
"v_fma_f32 v143, v143, v152, v63\n"
"v_mfma_f32_16x16x32_bf16 v[56:59], v[96:99], a[188:191], v[56:59]\n"
"v_permlane32_swap_b32 v125, v127\n"
"v_mfma_f32_16x16x32_bf16 v[128:131], a[192:195], v[88:91], 0\n"
"s_cbranch_vccnz .LF_2s\n"
".LF_2r:\n"
"v_add_f32 v124, v124, v126\n"
"v_mfma_f32_16x16x32_bf16 v[128:131], a[196:199], v[92:95], v[128:131]\n"
"s_add_u32 s50, s50, 0x180000\n"
"ds_write_addtid_b32 v56 offset:2816\n"
"v_add_f32 v125, v125, v127\n"
"v_mfma_f32_16x16x32_bf16 v[128:131], a[200:203], v[36:39], v[128:131]\n"
"ds_write_addtid_b32 v57 offset:7456\n"
"global_load_dwordx4 v[112:115], v5, s[56:57] offset:0 sc1\n"
"v_mfma_f32_16x16x32_bf16 v[128:131], a[204:207], v[40:43], v[128:131]\n"
"ds_write_addtid_b32 v58 offset:12160\n"
"v_mul_f32_e64 v140, v140, |v140|\n"
"v_mfma_f32_16x16x32_bf16 v[128:131], a[208:211], v[44:47], v[128:131]\n"
"ds_write_addtid_b32 v59 offset:16800\n"
"v_mul_f32_e64 v141, v141, |v141|\n"
"v_mfma_f32_16x16x32_bf16 v[128:131], a[212:215], v[48:51], v[128:131]\n"
"v_permlane16_swap_b32 v124, v125\n"
"v_mfma_f32_16x16x32_bf16 v[128:131], a[216:219], v[52:55], v[128:131]\n"
"v_add_f32 v124, v124, v125\n"
"v_mul_f32_e64 v142, v142, |v142|\n"
"v_mfma_f32_16x16x32_bf16 v[128:131], a[220:223], v[96:99], v[128:131]\n"
"global_load_dwordx4 v[116:119], v5, s[56:57] offset:1024 sc1\n"
"v_add_f32_dpp v125, v124, v124 row_ror:8 row_mask:0xf bank_mask:0xf\n"
"v_mfma_f32_16x16x32_bf16 v[132:135], a[224:227], v[88:91], 0\n"
"s_waitcnt lgkmcnt(0)\n"
"v_mov_b32 v124, v125\n"
"v_mul_f32_e64 v143, v143, |v143|\n"
"v_mfma_f32_16x16x32_bf16 v[132:135], a[228:231], v[92:95], v[132:135]\n"
"s_barrier\n"
"v_mfma_f32_16x16x32_bf16 v[132:135], a[232:235], v[36:39], v[132:135]\n"
"v_permlane16_swap_b32 v125, v124\n"
"v_mfma_f32_16x16x32_bf16 v[132:135], a[236:239], v[40:43], v[132:135]\n"
"global_load_dwordx4 v[120:123], v6, s[56:57] offset:0 sc1\n"
"v_mul_f32 v125, 0xba800000, v125\n"
"v_mfma_f32_16x16x32_bf16 v[132:135], a[240:243], v[44:47], v[132:135]\n"
"buffer_load_dwordx2 v[144:145], v9, s[40:43], s50 offen sc1\n"
"v_mfma_f32_16x16x32_bf16 v[132:135], a[244:247], v[48:51], v[132:135]\n"
"v_fma_f32 v124, v124, s62, v17\n"
"v_fma_f32 v124, -v125, v125, v124\n"
"v_mfma_f32_16x16x32_bf16 v[132:135], a[248:251], v[52:55], v[132:135]\n"
"v_max_f32 v124, v17, v124\n"
"v_cvt_pk_bf16_f32 v138, v140, v141\n"
"v_mfma_f32_16x16x32_bf16 v[132:135], a[252:255], v[96:99], v[132:135]\n"
"v_rsq_f32 v124, v124\n"
"s_branch .LF_top\n"
".LF_1s:\n"
"s_cmp_ge_u32 s50, s53\n"
"s_cbranch_scc1 .LF_t0\n"
"s_cmp_ge_u32 s50, s52\n"
"s_cbranch_scc1 .LF_1r\n"
"s_sub_u32 s60, s50, 0x0\n"
"s_mov_b32 s61, 200000\n"
"s_nop 7\n"
".LF_1p:\n"
"buffer_load_dwordx2 v[134:135], v9, s[40:43], s60 offen sc1\n"
"s_waitcnt vmcnt(0)\n"
"v_cmp_ne_u64 vcc, s[48:49], v[134:135]\n"
"s_cbranch_vccz .LF_1px\n"
"s_sleep 1\n"
"s_sub_u32 s61, s61, 1\n"
"s_cbranch_scc0 .LF_1p\n"
"s_branch .LF_end\n"
".LF_1px:\n"
"s_branch .LF_1r\n"
".LF_2s:\n"
"s_cmp_ge_u32 s50, s53\n"
"s_cbranch_scc1 .LF_t1\n"
"s_cmp_ge_u32 s50, s52\n"
"s_cbranch_scc1 .LF_2r\n"
"s_sub_u32 s60, s50, 0x0\n"
"s_mov_b32 s61, 200000\n"
"s_nop 7\n"
".LF_2p:\n"
"buffer_load_dwordx2 v[154:155], v9, s[40:43], s60 offen sc1\n"
"s_waitcnt vmcnt(0)\n"
"v_cmp_ne_u64 vcc, s[48:49], v[154:155]\n"
"s_cbranch_vccz .LF_2px\n"
"s_sleep 1\n"
"s_sub_u32 s61, s61, 1\n"
"s_cbranch_scc0 .LF_2p\n"
"s_branch .LF_end\n"
".LF_2px:\n"
"s_branch .LF_2r\n"
".LF_3s:\n"
"s_cmp_ge_u32 s50, s53\n"
"s_cbranch_scc1 .LF_t2\n"
"s_cmp_ge_u32 s50, s52\n"
"s_cbranch_scc1 .LF_3r\n"
"s_sub_u32 s60, s50, 0x0\n"
"s_mov_b32 s61, 200000\n"
"s_nop 7\n"
".LF_3p:\n"
"buffer_load_dwordx2 v[144:145], v9, s[40:43], s60 offen sc1\n"
"s_waitcnt vmcnt(0)\n"
"v_cmp_ne_u64 vcc, s[48:49], v[144:145]\n"
"s_cbranch_vccz .LF_3px\n"
"s_sleep 1\n"
"s_sub_u32 s61, s61, 1\n"
"s_cbranch_scc0 .LF_3p\n"
"s_branch .LF_end\n"
".LF_3px:\n"
"s_branch .LF_3r\n"
".LF_4s:\n"
"s_cmp_ge_u32 s50, s53\n"
"s_cbranch_scc1 .LF_t3\n"
"s_cmp_ge_u32 s50, s52\n"
"s_cbranch_scc1 .LF_4r\n"
"s_sub_u32 s60, s50, 0x0\n"
"s_mov_b32 s61, 200000\n"
"s_nop 7\n"
".LF_4p:\n"
"buffer_load_dwordx2 v[138:139], v9, s[40:43], s60 offen sc1\n"
"s_waitcnt vmcnt(0)\n"
"v_cmp_ne_u64 vcc, s[48:49], v[138:139]\n"
"s_cbranch_vccz .LF_4px\n"
"s_sleep 1\n"
"s_sub_u32 s61, s61, 1\n"
"s_cbranch_scc0 .LF_4p\n"
"s_branch .LF_end\n"
".LF_4px:\n"
"s_branch .LF_4r\n"
".LF_t0:\n"
"v_mfma_f32_16x16x32_bf16 v[112:115], a[192:195], v[56:59], 0\n"
"v_mfma_f32_16x16x32_bf16 v[112:115], a[196:199], v[60:63], v[112:115]\n"
"ds_write_addtid_b32 v108 offset:1280\n"
"v_mfma_f32_16x16x32_bf16 v[112:115], a[200:203], v[64:67], v[112:115]\n"
"ds_write_addtid_b32 v109 offset:5920\n"
"v_mfma_f32_16x16x32_bf16 v[112:115], a[204:207], v[68:71], v[112:115]\n"
"ds_write_addtid_b32 v110 offset:10624\n"
"v_mfma_f32_16x16x32_bf16 v[112:115], a[208:211], v[72:75], v[112:115]\n"
"ds_write_addtid_b32 v111 offset:15264\n"
"v_mfma_f32_16x16x32_bf16 v[112:115], a[212:215], v[76:79], v[112:115]\n"
"v_mfma_f32_16x16x32_bf16 v[112:115], a[216:219], v[80:83], v[112:115]\n"
"v_mfma_f32_16x16x32_bf16 v[112:115], a[220:223], v[84:87], v[112:115]\n"
"v_mfma_f32_16x16x32_bf16 v[132:135], a[224:227], v[56:59], 0\n"
"s_waitcnt lgkmcnt(0)\n"
"v_mfma_f32_16x16x32_bf16 v[132:135], a[228:231], v[60:63], v[132:135]\n"
"s_barrier\n"
"v_mfma_f32_16x16x32_bf16 v[132:135], a[232:235], v[64:67], v[132:135]\n"
"ds_read2st64_b32 v[152:153], v13 offset0:0 offset1:1\n"
"v_mfma_f32_16x16x32_bf16 v[132:135], a[236:239], v[68:71], v[132:135]\n"
"ds_read_b128 v[56:59], v2 offset:1536\n"
"ds_read_b128 v[60:63], v3 offset:512\n"
"v_mfma_f32_16x16x32_bf16 v[132:135], a[240:243], v[72:75], v[132:135]\n"
"ds_read_b128 v[136:139], v4 offset:2560\n"
"ds_read_b128 v[140:143], v2 offset:1792\n"
"v_mfma_f32_16x16x32_bf16 v[132:135], a[244:247], v[76:79], v[132:135]\n"
"v_mfma_f32_16x16x32_bf16 v[132:135], a[248:251], v[80:83], v[132:135]\n"
"ds_read_b128 v[144:147], v3 offset:768\n"
"v_mfma_f32_16x16x32_bf16 v[132:135], a[252:255], v[84:87], v[132:135]\n"
"s_waitcnt vmcnt(9)\n"
"ds_read_b128 v[148:151], v4 offset:2816\n"
"v_mfma_f32_16x16x32_bf16 v[76:79], v[88:91], a[0:3], 0\n"
"v_mfma_f32_16x16x32_bf16 v[76:79], v[92:95], a[4:7], v[76:79]\n"
"s_waitcnt vmcnt(6)\n"
"v_mfma_f32_16x16x32_bf16 v[76:79], v[36:39], a[8:11], v[76:79]\n"
"s_waitcnt lgkmcnt(3)\n"
"v_add_f32 v56, v56, v60\n"
"v_add_f32 v57, v57, v61\n"
"v_mfma_f32_16x16x32_bf16 v[76:79], v[40:43], a[12:15], v[76:79]\n"
"s_waitcnt vmcnt(4)\n"
"v_add_f32 v58, v58, v62\n"
"v_add_f32 v59, v59, v63\n"
"v_mfma_f32_16x16x32_bf16 v[76:79], v[44:47], a[16:19], v[76:79]\n"
"v_add_f32 v56, v56, v136\n"
"v_mfma_f32_16x16x32_bf16 v[76:79], v[48:51], a[20:23], v[76:79]\n"
"s_waitcnt vmcnt(2)\n"
"v_add_f32 v57, v57, v137\n"
"v_add_f32 v58, v58, v138\n"
"v_mfma_f32_16x16x32_bf16 v[76:79], v[52:55], a[24:27], v[76:79]\n"
"v_add_f32 v59, v59, v139\n"
"v_add_f32 v56, v56, v112\n"
"v_mfma_f32_16x16x32_bf16 v[76:79], v[96:99], a[28:31], v[76:79]\n"
"v_add_f32 v57, v57, v113\n"
"v_add_f32 v58, v58, v114\n"
"v_mfma_f32_16x16x32_bf16 v[60:63], v[88:91], a[32:35], 0\n"
"v_add_f32 v59, v59, v115\n"
"v_mfma_f32_16x16x32_bf16 v[60:63], v[92:95], a[36:39], v[60:63]\n"
"ds_write_addtid_b32 v76 offset:1536\n"
"v_mfma_f32_16x16x32_bf16 v[60:63], v[36:39], a[40:43], v[60:63]\n"
"s_add_u32 s93, s50, 0xffe80000\n"
"s_and_b32 s94, s93, 0xfbffffff\n"
"ds_write_addtid_b32 v77 offset:6176\n"
"v_mfma_f32_16x16x32_bf16 v[60:63], v[40:43], a[44:47], v[60:63]\n"
"ds_write_addtid_b32 v78 offset:10880\n"
"v_fma_f32 v80, v18, v153, v26\n"
"v_mfma_f32_16x16x32_bf16 v[60:63], v[44:47], a[48:51], v[60:63]\n"
"ds_write_addtid_b32 v79 offset:15520\n"
"v_fma_f32 v81, v19, v153, v27\n"
"v_mfma_f32_16x16x32_bf16 v[60:63], v[48:51], a[52:55], v[60:63]\n"
"v_fma_f32 v82, v20, v153, v28\n"
"v_fma_f32 v83, v21, v153, v29\n"
"v_mfma_f32_16x16x32_bf16 v[60:63], v[52:55], a[56:59], v[60:63]\n"
"v_fma_f32 v56, v56, v152, v80\n"
"v_fma_f32 v57, v57, v152, v81\n"
"v_mfma_f32_16x16x32_bf16 v[60:63], v[96:99], a[60:63], v[60:63]\n"
"v_fma_f32 v58, v58, v152, v82\n"
"v_mfma_f32_16x16x32_bf16 v[84:87], v[88:91], a[64:67], 0\n"
"v_fma_f32 v59, v59, v152, v83\n"
"v_mul_f32_e64 v56, v56, |v56|\n"
"v_mfma_f32_16x16x32_bf16 v[84:87], v[92:95], a[68:71], v[84:87]\n"
"ds_write_addtid_b32 v60 offset:1792\n"
"v_mul_f32_e64 v57, v57, |v57|\n"
"v_mfma_f32_16x16x32_bf16 v[84:87], v[36:39], a[72:75], v[84:87]\n"
"ds_write_addtid_b32 v61 offset:6432\n"
"v_mul_f32_e64 v58, v58, |v58|\n"
"v_mfma_f32_16x16x32_bf16 v[84:87], v[40:43], a[76:79], v[84:87]\n"
"ds_write_addtid_b32 v62 offset:11136\n"
"v_mfma_f32_16x16x32_bf16 v[84:87], v[44:47], a[80:83], v[84:87]\n"
"ds_write_addtid_b32 v63 offset:15776\n"
"v_mul_f32_e64 v59, v59, |v59|\n"
"v_mfma_f32_16x16x32_bf16 v[84:87], v[48:51], a[84:87], v[84:87]\n"
"v_cvt_pk_bf16_f32 v136, v56, v57\n"
"v_cvt_pk_bf16_f32 v137, v58, v59\n"
"v_mfma_f32_16x16x32_bf16 v[84:87], v[52:55], a[88:91], v[84:87]\n"
"v_pk_max_i16 v136, v136, 0\n"
"v_pk_max_i16 v137, v137, 0\n"
"v_mfma_f32_16x16x32_bf16 v[84:87], v[96:99], a[92:95], v[84:87]\n"
"s_waitcnt lgkmcnt(8)\n"
"v_add_f32 v140, v140, v144\n"
"v_add_f32 v141, v141, v145\n"
"v_mfma_f32_16x16x32_bf16 v[56:59], v[88:91], a[96:99], 0\n"
"v_add_f32 v142, v142, v146\n"
"v_add_f32 v143, v143, v147\n"
"v_mfma_f32_16x16x32_bf16 v[56:59], v[92:95], a[100:103], v[56:59]\n"
"ds_write_addtid_b32 v84 offset:2048\n"
"v_add_f32 v140, v140, v148\n"
"v_mfma_f32_16x16x32_bf16 v[56:59], v[36:39], a[104:107], v[56:59]\n"
"ds_write_addtid_b32 v85 offset:6688\n"
"v_add_f32 v141, v141, v149\n"
"v_mfma_f32_16x16x32_bf16 v[56:59], v[40:43], a[108:111], v[56:59]\n"
"ds_write_addtid_b32 v86 offset:11392\n"
"v_add_f32 v142, v142, v150\n"
"v_mfma_f32_16x16x32_bf16 v[56:59], v[44:47], a[112:115], v[56:59]\n"
"ds_write_addtid_b32 v87 offset:16032\n"
"v_add_f32 v143, v143, v151\n"
"v_mfma_f32_16x16x32_bf16 v[56:59], v[48:51], a[116:119], v[56:59]\n"
"s_waitcnt vmcnt(0)\n"
"buffer_store_dwordx2 v[0:1], v16, s[40:43], s95 offen offset:0 sc1\n"
"v_add_f32 v140, v140, v132\n"
"v_mfma_f32_16x16x32_bf16 v[56:59], v[52:55], a[120:123], v[56:59]\n"
"v_add_f32 v141, v141, v133\n"
"v_add_f32 v142, v142, v134\n"
"v_mfma_f32_16x16x32_bf16 v[56:59], v[96:99], a[124:127], v[56:59]\n"
"v_add_f32 v143, v143, v135\n"
"v_fma_f32 v60, v22, v153, v30\n"
"v_mfma_f32_16x16x32_bf16 v[84:87], v[88:91], a[128:131], 0\n"
"v_fma_f32 v61, v23, v153, v31\n"
"v_fma_f32 v62, v24, v153, v32\n"
"v_mfma_f32_16x16x32_bf16 v[84:87], v[92:95], a[132:135], v[84:87]\n"
"ds_write_addtid_b32 v56 offset:2304\n"
"v_fma_f32 v63, v25, v153, v33\n"
"v_mfma_f32_16x16x32_bf16 v[84:87], v[36:39], a[136:139], v[84:87]\n"
"ds_write_addtid_b32 v57 offset:6944\n"
"v_fma_f32 v140, v140, v152, v60\n"
"v_mfma_f32_16x16x32_bf16 v[84:87], v[40:43], a[140:143], v[84:87]\n"
"ds_write_addtid_b32 v58 offset:11648\n"
"v_fma_f32 v141, v141, v152, v61\n"
"v_mfma_f32_16x16x32_bf16 v[84:87], v[44:47], a[144:147], v[84:87]\n"
"ds_write_addtid_b32 v59 offset:16288\n"
"v_mfma_f32_16x16x32_bf16 v[84:87], v[48:51], a[148:151], v[84:87]\n"
"v_mfma_f32_16x16x32_bf16 v[84:87], v[52:55], a[152:155], v[84:87]\n"
"v_mfma_f32_16x16x32_bf16 v[84:87], v[96:99], a[156:159], v[84:87]\n"
"v_mfma_f32_16x16x32_bf16 v[56:59], v[88:91], a[160:163], 0\n"
"v_mfma_f32_16x16x32_bf16 v[56:59], v[92:95], a[164:167], v[56:59]\n"
"ds_write_addtid_b32 v84 offset:2560\n"
"v_mfma_f32_16x16x32_bf16 v[56:59], v[36:39], a[168:171], v[56:59]\n"
"ds_write_addtid_b32 v85 offset:7200\n"
"v_mfma_f32_16x16x32_bf16 v[56:59], v[40:43], a[172:175], v[56:59]\n"
"ds_write_addtid_b32 v86 offset:11904\n"
"v_mfma_f32_16x16x32_bf16 v[56:59], v[44:47], a[176:179], v[56:59]\n"
"ds_write_addtid_b32 v87 offset:16544\n"
"v_fma_f32 v142, v142, v152, v62\n"
"v_mfma_f32_16x16x32_bf16 v[56:59], v[48:51], a[180:183], v[56:59]\n"
"v_mfma_f32_16x16x32_bf16 v[56:59], v[52:55], a[184:187], v[56:59]\n"
"v_fma_f32 v143, v143, v152, v63\n"
"v_mfma_f32_16x16x32_bf16 v[56:59], v[96:99], a[188:191], v[56:59]\n"
"v_mfma_f32_16x16x32_bf16 v[128:131], a[192:195], v[88:91], 0\n"
"v_mfma_f32_16x16x32_bf16 v[128:131], a[196:199], v[92:95], v[128:131]\n"
"ds_write_addtid_b32 v56 offset:2816\n"
"v_mfma_f32_16x16x32_bf16 v[128:131], a[200:203], v[36:39], v[128:131]\n"
"ds_write_addtid_b32 v57 offset:7456\n"
"v_mfma_f32_16x16x32_bf16 v[128:131], a[204:207], v[40:43], v[128:131]\n"
"ds_write_addtid_b32 v58 offset:12160\n"
"v_mul_f32_e64 v140, v140, |v140|\n"
"v_mfma_f32_16x16x32_bf16 v[128:131], a[208:211], v[44:47], v[128:131]\n"
"ds_write_addtid_b32 v59 offset:16800\n"
"v_mul_f32_e64 v141, v141, |v141|\n"
"v_mfma_f32_16x16x32_bf16 v[128:131], a[212:215], v[48:51], v[128:131]\n"
"v_mfma_f32_16x16x32_bf16 v[128:131], a[216:219], v[52:55], v[128:131]\n"
"v_mul_f32_e64 v142, v142, |v142|\n"
"v_mfma_f32_16x16x32_bf16 v[128:131], a[220:223], v[96:99], v[128:131]\n"
"v_mfma_f32_16x16x32_bf16 v[132:135], a[224:227], v[88:91], 0\n"
"s_waitcnt lgkmcnt(0)\n"
"v_mul_f32_e64 v143, v143, |v143|\n"
"v_mfma_f32_16x16x32_bf16 v[132:135], a[228:231], v[92:95], v[132:135]\n"
"s_barrier\n"
"v_mfma_f32_16x16x32_bf16 v[132:135], a[232:235], v[36:39], v[132:135]\n"
"v_mfma_f32_16x16x32_bf16 v[132:135], a[236:239], v[40:43], v[132:135]\n"
"v_mfma_f32_16x16x32_bf16 v[132:135], a[240:243], v[44:47], v[132:135]\n"
"v_mfma_f32_16x16x32_bf16 v[132:135], a[244:247], v[48:51], v[132:135]\n"
"v_mfma_f32_16x16x32_bf16 v[132:135], a[248:251], v[52:55], v[132:135]\n"
"v_cvt_pk_bf16_f32 v138, v140, v141\n"
"v_mfma_f32_16x16x32_bf16 v[132:135], a[252:255], v[96:99], v[132:135]\n"
"v_cvt_pk_bf16_f32 v139, v142, v143\n"
"v_pk_max_i16 v138, v138, 0\n"
"ds_read2st64_b32 v[140:141], v14 offset0:0 offset1:1\n"
"v_pk_max_i16 v139, v139, 0\n"
"ds_read_b128 v[56:59], v2 offset:0\n"
"ds_read_b128 v[52:55], v3 offset:2048\n"
"ds_read_b128 v[60:63], v4 offset:1024\n"
"ds_read_b128 v[84:87], v2 offset:256\n"
"ds_read_b128 v[88:91], v3 offset:2304\n"
"ds_read_b128 v[92:95], v4 offset:1280\n"
"s_waitcnt lgkmcnt(3)\n"
"buffer_store_dwordx4 v[136:139], v15, s[40:43], s94 offen sc1\n"
"v_add_f32 v56, v56, v52\n"
"v_add_f32 v57, v57, v53\n"
"v_add_f32 v58, v58, v54\n"
"v_add_f32 v59, v59, v55\n"
"v_add_f32 v56, v56, v60\n"
"v_add_f32 v57, v57, v61\n"
"v_add_f32 v58, v58, v62\n"
"v_add_f32 v59, v59, v63\n"
"v_add_f32 v56, v56, v128\n"
"v_add_f32 v57, v57, v129\n"
"v_add_f32 v58, v58, v130\n"
"v_add_f32 v59, v59, v131\n"
"s_add_u32 s95, s50, 0x0\n"
"s_and_b32 s94, s95, 0xfbffffff\n"
"v_fma_f32 v60, v18, v141, v26\n"
"v_fma_f32 v61, v19, v141, v27\n"
"v_fma_f32 v62, v20, v141, v28\n"
"v_fma_f32 v63, v21, v141, v29\n"
"v_fma_f32 v56, v56, v140, v60\n"
"v_fma_f32 v57, v57, v140, v61\n"
"v_fma_f32 v58, v58, v140, v62\n"
"v_fma_f32 v59, v59, v140, v63\n"
"v_mul_f32_e64 v56, v56, |v56|\n"
"v_mul_f32_e64 v57, v57, |v57|\n"
"v_mul_f32_e64 v58, v58, |v58|\n"
"v_mul_f32_e64 v59, v59, |v59|\n"
"v_cvt_pk_bf16_f32 v128, v56, v57\n"
"v_cvt_pk_bf16_f32 v129, v58, v59\n"
"v_pk_max_i16 v128, v128, 0\n"
"v_pk_max_i16 v129, v129, 0\n"
"s_waitcnt lgkmcnt(0)\n"
"v_add_f32 v84, v84, v88\n"
"v_add_f32 v85, v85, v89\n"
"v_add_f32 v86, v86, v90\n"
"v_add_f32 v87, v87, v91\n"
"v_add_f32 v84, v84, v92\n"
"v_add_f32 v85, v85, v93\n"
"v_add_f32 v86, v86, v94\n"
"v_add_f32 v87, v87, v95\n"
"v_add_f32 v84, v84, v132\n"
"v_add_f32 v85, v85, v133\n"
"v_add_f32 v86, v86, v134\n"
"v_add_f32 v87, v87, v135\n"
"v_fma_f32 v60, v22, v141, v30\n"
"v_fma_f32 v61, v23, v141, v31\n"
"v_fma_f32 v62, v24, v141, v32\n"
"v_fma_f32 v63, v25, v141, v33\n"
"v_fma_f32 v84, v84, v140, v60\n"
"v_fma_f32 v85, v85, v140, v61\n"
"v_fma_f32 v86, v86, v140, v62\n"
"v_fma_f32 v87, v87, v140, v63\n"
"v_mul_f32_e64 v84, v84, |v84|\n"
"v_mul_f32_e64 v85, v85, |v85|\n"
"v_mul_f32_e64 v86, v86, |v86|\n"
"v_mul_f32_e64 v87, v87, |v87|\n"
"v_cvt_pk_bf16_f32 v130, v84, v85\n"
"v_cvt_pk_bf16_f32 v131, v86, v87\n"
"v_pk_max_i16 v130, v130, 0\n"
"v_pk_max_i16 v131, v131, 0\n"
"s_waitcnt vmcnt(0)\n"
"buffer_store_dwordx2 v[0:1], v16, s[40:43], s93 offen offset:0 sc1\n"
"buffer_store_dwordx4 v[128:131], v15, s[40:43], s94 offen sc1\n"
"s_waitcnt vmcnt(0)\n"
"buffer_store_dwordx2 v[0:1], v16, s[40:43], s95 offen offset:0 sc1\n"
"s_branch .LF_end\n"
".LF_t1:\n"
"v_add_f32 v124, v124, v126\n"
"v_mfma_f32_16x16x32_bf16 v[128:131], a[196:199], v[92:95], v[128:131]\n"
"ds_write_addtid_b32 v56 offset:2816\n"
"v_add_f32 v125, v125, v127\n"
"v_mfma_f32_16x16x32_bf16 v[128:131], a[200:203], v[36:39], v[128:131]\n"
"ds_write_addtid_b32 v57 offset:7456\n"
"v_mfma_f32_16x16x32_bf16 v[128:131], a[204:207], v[40:43], v[128:131]\n"
"ds_write_addtid_b32 v58 offset:12160\n"
"v_mul_f32_e64 v140, v140, |v140|\n"
"v_mfma_f32_16x16x32_bf16 v[128:131], a[208:211], v[44:47], v[128:131]\n"
"ds_write_addtid_b32 v59 offset:16800\n"
"v_mul_f32_e64 v141, v141, |v141|\n"
"v_mfma_f32_16x16x32_bf16 v[128:131], a[212:215], v[48:51], v[128:131]\n"
"v_permlane16_swap_b32 v124, v125\n"
"v_mfma_f32_16x16x32_bf16 v[128:131], a[216:219], v[52:55], v[128:131]\n"
"v_add_f32 v124, v124, v125\n"
"v_mul_f32_e64 v142, v142, |v142|\n"
"v_mfma_f32_16x16x32_bf16 v[128:131], a[220:223], v[96:99], v[128:131]\n"
"v_add_f32_dpp v125, v124, v124 row_ror:8 row_mask:0xf bank_mask:0xf\n"
"v_mfma_f32_16x16x32_bf16 v[132:135], a[224:227], v[88:91], 0\n"
"s_waitcnt lgkmcnt(0)\n"
"v_mov_b32 v124, v125\n"
"v_mul_f32_e64 v143, v143, |v143|\n"
"v_mfma_f32_16x16x32_bf16 v[132:135], a[228:231], v[92:95], v[132:135]\n"
"s_barrier\n"
"v_mfma_f32_16x16x32_bf16 v[132:135], a[232:235], v[36:39], v[132:135]\n"
"v_permlane16_swap_b32 v125, v124\n"
"v_mfma_f32_16x16x32_bf16 v[132:135], a[236:239], v[40:43], v[132:135]\n"
"v_mul_f32 v125, 0xba800000, v125\n"
"v_mfma_f32_16x16x32_bf16 v[132:135], a[240:243], v[44:47], v[132:135]\n"
"v_mfma_f32_16x16x32_bf16 v[132:135], a[244:247], v[48:51], v[132:135]\n"
"v_fma_f32 v124, v124, s62, v17\n"
"v_fma_f32 v124, -v125, v125, v124\n"
"v_mfma_f32_16x16x32_bf16 v[132:135], a[248:251], v[52:55], v[132:135]\n"
"v_max_f32 v124, v17, v124\n"
"v_cvt_pk_bf16_f32 v138, v140, v141\n"
"v_mfma_f32_16x16x32_bf16 v[132:135], a[252:255], v[96:99], v[132:135]\n"
"s_waitcnt vmcnt(8)\n"
"v_rsq_f32 v124, v124\n"
"v_mfma_f32_16x16x32_bf16 v[48:51], v[100:103], a[0:3], 0\n"
"v_mul_f32 v125, v125, v124\n"
"v_mfma_f32_16x16x32_bf16 v[48:51], v[104:107], a[4:7], v[48:51]\n"
"s_waitcnt vmcnt(5)\n"
"ds_write_addtid_b32 v124 offset:18592\n"
"ds_write_addtid_b32 v125 offset:18848\n"
"v_mfma_f32_16x16x32_bf16 v[48:51], v[108:111], a[8:11], v[48:51]\n"
"v_cvt_pk_bf16_f32 v139, v142, v143\n"
"v_pk_max_i16 v138, v138, 0\n"
"v_mfma_f32_16x16x32_bf16 v[48:51], v[64:67], a[12:15], v[48:51]\n"
"s_waitcnt vmcnt(3)\n"
"ds_read2st64_b32 v[140:141], v14 offset0:0 offset1:1\n"
"v_pk_max_i16 v139, v139, 0\n"
"v_mfma_f32_16x16x32_bf16 v[48:51], v[68:71], a[16:19], v[48:51]\n"
"ds_read_b128 v[56:59], v2 offset:0\n"
"ds_read_b128 v[52:55], v3 offset:2048\n"
"v_mfma_f32_16x16x32_bf16 v[48:51], v[72:75], a[20:23], v[48:51]\n"
"s_waitcnt vmcnt(1)\n"
"ds_read_b128 v[60:63], v4 offset:1024\n"
"v_mfma_f32_16x16x32_bf16 v[48:51], v[76:79], a[24:27], v[48:51]\n"
"ds_read_b128 v[84:87], v2 offset:256\n"
"ds_read_b128 v[88:91], v3 offset:2304\n"
"v_mfma_f32_16x16x32_bf16 v[48:51], v[80:83], a[28:31], v[48:51]\n"
"ds_read_b128 v[92:95], v4 offset:1280\n"
"v_mfma_f32_16x16x32_bf16 v[96:99], v[100:103], a[32:35], 0\n"
"v_mfma_f32_16x16x32_bf16 v[96:99], v[104:107], a[36:39], v[96:99]\n"
"ds_write_addtid_b32 v48 offset:0\n"
"v_mfma_f32_16x16x32_bf16 v[96:99], v[108:111], a[40:43], v[96:99]\n"
"ds_write_addtid_b32 v49 offset:4640\n"
"v_mfma_f32_16x16x32_bf16 v[96:99], v[64:67], a[44:47], v[96:99]\n"
"s_waitcnt lgkmcnt(5)\n"
"buffer_store_dwordx4 v[136:139], v15, s[40:43], s94 offen sc1\n"
"ds_write_addtid_b32 v50 offset:9344\n"
"v_add_f32 v56, v56, v52\n"
"v_add_f32 v57, v57, v53\n"
"v_mfma_f32_16x16x32_bf16 v[96:99], v[68:71], a[48:51], v[96:99]\n"
"ds_write_addtid_b32 v51 offset:13984\n"
"v_add_f32 v58, v58, v54\n"
"v_mfma_f32_16x16x32_bf16 v[96:99], v[72:75], a[52:55], v[96:99]\n"
"v_add_f32 v59, v59, v55\n"
"v_add_f32 v56, v56, v60\n"
"v_mfma_f32_16x16x32_bf16 v[96:99], v[76:79], a[56:59], v[96:99]\n"
"v_add_f32 v57, v57, v61\n"
"v_add_f32 v58, v58, v62\n"
"v_mfma_f32_16x16x32_bf16 v[96:99], v[80:83], a[60:63], v[96:99]\n"
"v_add_f32 v59, v59, v63\n"
"v_add_f32 v56, v56, v128\n"
"v_mfma_f32_16x16x32_bf16 v[124:127], v[100:103], a[64:67], 0\n"
"v_add_f32 v57, v57, v129\n"
"v_mfma_f32_16x16x32_bf16 v[124:127], v[104:107], a[68:71], v[124:127]\n"
"ds_write_addtid_b32 v96 offset:256\n"
"v_add_f32 v58, v58, v130\n"
"v_mfma_f32_16x16x32_bf16 v[124:127], v[108:111], a[72:75], v[124:127]\n"
"ds_write_addtid_b32 v97 offset:4896\n"
"v_add_f32 v59, v59, v131\n"
"v_mfma_f32_16x16x32_bf16 v[124:127], v[64:67], a[76:79], v[124:127]\n"
"s_add_u32 s95, s50, 0xffe80000\n"
"s_and_b32 s94, s95, 0xfbffffff\n"
"ds_write_addtid_b32 v98 offset:9600\n"
"v_mfma_f32_16x16x32_bf16 v[124:127], v[68:71], a[80:83], v[124:127]\n"
"ds_write_addtid_b32 v99 offset:14240\n"
"v_mfma_f32_16x16x32_bf16 v[124:127], v[72:75], a[84:87], v[124:127]\n"
"v_fma_f32 v60, v18, v141, v26\n"
"v_fma_f32 v61, v19, v141, v27\n"
"v_mfma_f32_16x16x32_bf16 v[124:127], v[76:79], a[88:91], v[124:127]\n"
"v_fma_f32 v62, v20, v141, v28\n"
"v_fma_f32 v63, v21, v141, v29\n"
"v_mfma_f32_16x16x32_bf16 v[124:127], v[80:83], a[92:95], v[124:127]\n"
"v_fma_f32 v56, v56, v140, v60\n"
"v_fma_f32 v57, v57, v140, v61\n"
"v_mfma_f32_16x16x32_bf16 v[96:99], v[100:103], a[96:99], 0\n"
"v_fma_f32 v58, v58, v140, v62\n"
"v_fma_f32 v59, v59, v140, v63\n"
"v_mfma_f32_16x16x32_bf16 v[96:99], v[104:107], a[100:103], v[96:99]\n"
"ds_write_addtid_b32 v124 offset:512\n"
"v_mul_f32_e64 v56, v56, |v56|\n"
"v_mfma_f32_16x16x32_bf16 v[96:99], v[108:111], a[104:107], v[96:99]\n"
"ds_write_addtid_b32 v125 offset:5152\n"
"v_mul_f32_e64 v57, v57, |v57|\n"
"v_mfma_f32_16x16x32_bf16 v[96:99], v[64:67], a[108:111], v[96:99]\n"
"ds_write_addtid_b32 v126 offset:9856\n"
"v_mul_f32_e64 v58, v58, |v58|\n"
"v_mfma_f32_16x16x32_bf16 v[96:99], v[68:71], a[112:115], v[96:99]\n"
"ds_write_addtid_b32 v127 offset:14496\n"
"v_mul_f32_e64 v59, v59, |v59|\n"
"v_mfma_f32_16x16x32_bf16 v[96:99], v[72:75], a[116:119], v[96:99]\n"
"v_cvt_pk_bf16_f32 v128, v56, v57\n"
"v_cvt_pk_bf16_f32 v129, v58, v59\n"
"v_mfma_f32_16x16x32_bf16 v[96:99], v[76:79], a[120:123], v[96:99]\n"
"v_pk_max_i16 v128, v128, 0\n"
"v_pk_max_i16 v129, v129, 0\n"
"v_mfma_f32_16x16x32_bf16 v[96:99], v[80:83], a[124:127], v[96:99]\n"
"s_waitcnt lgkmcnt(12)\n"
"v_add_f32 v84, v84, v88\n"
"v_add_f32 v85, v85, v89\n"
"v_mfma_f32_16x16x32_bf16 v[56:59], v[100:103], a[128:131], 0\n"
"v_add_f32 v86, v86, v90\n"
"v_add_f32 v87, v87, v91\n"
"v_mfma_f32_16x16x32_bf16 v[56:59], v[104:107], a[132:135], v[56:59]\n"
"ds_write_addtid_b32 v96 offset:768\n"
"v_add_f32 v84, v84, v92\n"
"v_mfma_f32_16x16x32_bf16 v[56:59], v[108:111], a[136:139], v[56:59]\n"
"ds_write_addtid_b32 v97 offset:5408\n"
"v_add_f32 v85, v85, v93\n"
"v_mfma_f32_16x16x32_bf16 v[56:59], v[64:67], a[140:143], v[56:59]\n"
"ds_write_addtid_b32 v98 offset:10112\n"
"v_add_f32 v86, v86, v94\n"
"v_mfma_f32_16x16x32_bf16 v[56:59], v[68:71], a[144:147], v[56:59]\n"
"ds_write_addtid_b32 v99 offset:14752\n"
"v_add_f32 v87, v87, v95\n"
"v_mfma_f32_16x16x32_bf16 v[56:59], v[72:75], a[148:151], v[56:59]\n"
"v_add_f32 v84, v84, v132\n"
"v_add_f32 v85, v85, v133\n"
"v_mfma_f32_16x16x32_bf16 v[56:59], v[76:79], a[152:155], v[56:59]\n"
"v_add_f32 v86, v86, v134\n"
"v_add_f32 v87, v87, v135\n"
"v_mfma_f32_16x16x32_bf16 v[56:59], v[80:83], a[156:159], v[56:59]\n"
"v_fma_f32 v60, v22, v141, v30\n"
"v_fma_f32 v61, v23, v141, v31\n"
"v_mfma_f32_16x16x32_bf16 v[88:91], v[100:103], a[160:163], 0\n"
"v_fma_f32 v62, v24, v141, v32\n"
"v_fma_f32 v63, v25, v141, v33\n"
"v_mfma_f32_16x16x32_bf16 v[88:91], v[104:107], a[164:167], v[88:91]\n"
"ds_write_addtid_b32 v56 offset:1024\n"
"v_fma_f32 v84, v84, v140, v60\n"
"v_mfma_f32_16x16x32_bf16 v[88:91], v[108:111], a[168:171], v[88:91]\n"
"ds_write_addtid_b32 v57 offset:5664\n"
"v_fma_f32 v85, v85, v140, v61\n"
"v_mfma_f32_16x16x32_bf16 v[88:91], v[64:67], a[172:175], v[88:91]\n"
"ds_write_addtid_b32 v58 offset:10368\n"
"v_fma_f32 v86, v86, v140, v62\n"
"v_mfma_f32_16x16x32_bf16 v[88:91], v[68:71], a[176:179], v[88:91]\n"
"ds_write_addtid_b32 v59 offset:15008\n"
"v_fma_f32 v87, v87, v140, v63\n"
"v_mfma_f32_16x16x32_bf16 v[88:91], v[72:75], a[180:183], v[88:91]\n"
"v_mul_f32_e64 v84, v84, |v84|\n"
"v_mul_f32_e64 v85, v85, |v85|\n"
"v_mfma_f32_16x16x32_bf16 v[88:91], v[76:79], a[184:187], v[88:91]\n"
"v_mul_f32_e64 v86, v86, |v86|\n"
"v_mfma_f32_16x16x32_bf16 v[88:91], v[80:83], a[188:191], v[88:91]\n"
"v_mul_f32_e64 v87, v87, |v87|\n"
"v_cvt_pk_bf16_f32 v130, v84, v85\n"
"v_mfma_f32_16x16x32_bf16 v[92:95], a[192:195], v[100:103], 0\n"
"v_cvt_pk_bf16_f32 v131, v86, v87\n"
"v_mfma_f32_16x16x32_bf16 v[92:95], a[196:199], v[104:107], v[92:95]\n"
"ds_write_addtid_b32 v88 offset:1280\n"
"v_mfma_f32_16x16x32_bf16 v[92:95], a[200:203], v[108:111], v[92:95]\n"
"ds_write_addtid_b32 v89 offset:5920\n"
"v_pk_max_i16 v130, v130, 0\n"
"v_mfma_f32_16x16x32_bf16 v[92:95], a[204:207], v[64:67], v[92:95]\n"
"ds_write_addtid_b32 v90 offset:10624\n"
"v_pk_max_i16 v131, v131, 0\n"
"v_mfma_f32_16x16x32_bf16 v[92:95], a[208:211], v[68:71], v[92:95]\n"
"ds_write_addtid_b32 v91 offset:15264\n"
"v_mfma_f32_16x16x32_bf16 v[92:95], a[212:215], v[72:75], v[92:95]\n"
"v_mfma_f32_16x16x32_bf16 v[92:95], a[216:219], v[76:79], v[92:95]\n"
"v_mfma_f32_16x16x32_bf16 v[92:95], a[220:223], v[80:83], v[92:95]\n"
"v_mfma_f32_16x16x32_bf16 v[88:91], a[224:227], v[100:103], 0\n"
"s_waitcnt lgkmcnt(0)\n"
"v_mfma_f32_16x16x32_bf16 v[88:91], a[228:231], v[104:107], v[88:91]\n"
"s_barrier\n"
"v_mfma_f32_16x16x32_bf16 v[88:91], a[232:235], v[108:111], v[88:91]\n"
"ds_read2st64_b32 v[136:137], v11 offset0:0 offset1:1\n"
"ds_read_b128 v[96:99], v2 offset:1536\n"
"v_mfma_f32_16x16x32_bf16 v[88:91], a[236:239], v[64:67], v[88:91]\n"
"s_waitcnt vmcnt(0)\n"
"v_mfma_f32_16x16x32_bf16 v[88:91], a[240:243], v[68:71], v[88:91]\n"
"ds_read_b128 v[84:87], v3 offset:512\n"
"v_mfma_f32_16x16x32_bf16 v[88:91], a[244:247], v[72:75], v[88:91]\n"
"ds_read_b128 v[100:103], v4 offset:2560\n"
"ds_read_b128 v[104:107], v2 offset:1792\n"
"v_mfma_f32_16x16x32_bf16 v[88:91], a[248:251], v[76:79], v[88:91]\n"
"ds_read_b128 v[108:111], v3 offset:768\n"
"ds_read_b128 v[124:127], v4 offset:2816\n"
"v_mfma_f32_16x16x32_bf16 v[88:91], a[252:255], v[80:83], v[88:91]\n"
"buffer_store_dwordx2 v[0:1], v16, s[40:43], s93 offen offset:0 sc1\n"
"buffer_store_dwordx4 v[128:131], v15, s[40:43], s94 offen sc1\n"
"s_waitcnt lgkmcnt(3)\n"
"v_add_f32 v96, v96, v84\n"
"v_add_f32 v97, v97, v85\n"
"v_add_f32 v98, v98, v86\n"
"v_add_f32 v99, v99, v87\n"
"v_add_f32 v96, v96, v100\n"
"v_add_f32 v97, v97, v101\n"
"v_add_f32 v98, v98, v102\n"
"v_add_f32 v99, v99, v103\n"
"v_add_f32 v96, v96, v92\n"
"v_add_f32 v97, v97, v93\n"
"v_add_f32 v98, v98, v94\n"
"v_add_f32 v99, v99, v95\n"
"s_add_u32 s94, s50, 0x0\n"
"s_and_b32 s93, s94, 0xfbffffff\n"
"v_fma_f32 v84, v18, v137, v26\n"
"v_fma_f32 v85, v19, v137, v27\n"
"v_fma_f32 v86, v20, v137, v28\n"
"v_fma_f32 v87, v21, v137, v29\n"
"v_fma_f32 v96, v96, v136, v84\n"
"v_fma_f32 v97, v97, v136, v85\n"
"v_fma_f32 v98, v98, v136, v86\n"
"v_fma_f32 v99, v99, v136, v87\n"
"v_mul_f32_e64 v96, v96, |v96|\n"
"v_mul_f32_e64 v97, v97, |v97|\n"
"v_mul_f32_e64 v98, v98, |v98|\n"
"v_mul_f32_e64 v99, v99, |v99|\n"
"v_cvt_pk_bf16_f32 v100, v96, v97\n"
"v_cvt_pk_bf16_f32 v101, v98, v99\n"
"v_pk_max_i16 v100, v100, 0\n"
"v_pk_max_i16 v101, v101, 0\n"
"s_waitcnt lgkmcnt(0)\n"
"v_add_f32 v104, v104, v108\n"
"v_add_f32 v105, v105, v109\n"
"v_add_f32 v106, v106, v110\n"
"v_add_f32 v107, v107, v111\n"
"v_add_f32 v104, v104, v124\n"
"v_add_f32 v105, v105, v125\n"
"v_add_f32 v106, v106, v126\n"
"v_add_f32 v107, v107, v127\n"
"v_add_f32 v104, v104, v88\n"
"v_add_f32 v105, v105, v89\n"
"v_add_f32 v106, v106, v90\n"
"v_add_f32 v107, v107, v91\n"
"v_fma_f32 v96, v22, v137, v30\n"
"v_fma_f32 v97, v23, v137, v31\n"
"v_fma_f32 v98, v24, v137, v32\n"
"v_fma_f32 v99, v25, v137, v33\n"
"v_fma_f32 v104, v104, v136, v96\n"
"v_fma_f32 v105, v105, v136, v97\n"
"v_fma_f32 v106, v106, v136, v98\n"
"v_fma_f32 v107, v107, v136, v99\n"
"v_mul_f32_e64 v104, v104, |v104|\n"
"v_mul_f32_e64 v105, v105, |v105|\n"
"v_mul_f32_e64 v106, v106, |v106|\n"
"v_mul_f32_e64 v107, v107, |v107|\n"
"v_cvt_pk_bf16_f32 v102, v104, v105\n"
"v_cvt_pk_bf16_f32 v103, v106, v107\n"
"v_pk_max_i16 v102, v102, 0\n"
"v_pk_max_i16 v103, v103, 0\n"
"buffer_store_dwordx4 v[100:103], v15, s[40:43], s93 offen sc1\n"
"s_waitcnt vmcnt(1)\n"
"buffer_store_dwordx2 v[0:1], v16, s[40:43], s95 offen offset:0 sc1\n"
"s_waitcnt vmcnt(1)\n"
"buffer_store_dwordx2 v[0:1], v16, s[40:43], s94 offen offset:0 sc1\n"
"s_branch .LF_end\n"
".LF_t2:\n"
"v_cvt_pk_bf16_f32 v131, v86, v87\n"
"v_mfma_f32_16x16x32_bf16 v[92:95], a[196:199], v[104:107], v[92:95]\n"
"ds_write_addtid_b32 v88 offset:1280\n"
"v_mfma_f32_16x16x32_bf16 v[92:95], a[200:203], v[108:111], v[92:95]\n"
"ds_write_addtid_b32 v89 offset:5920\n"
"v_pk_max_i16 v130, v130, 0\n"
"v_mfma_f32_16x16x32_bf16 v[92:95], a[204:207], v[64:67], v[92:95]\n"
"ds_write_addtid_b32 v90 offset:10624\n"
"v_pk_max_i16 v131, v131, 0\n"
"v_mfma_f32_16x16x32_bf16 v[92:95], a[208:211], v[68:71], v[92:95]\n"
"ds_write_addtid_b32 v91 offset:15264\n"
"v_mfma_f32_16x16x32_bf16 v[92:95], a[212:215], v[72:75], v[92:95]\n"
"v_mfma_f32_16x16x32_bf16 v[92:95], a[216:219], v[76:79], v[92:95]\n"
"v_mfma_f32_16x16x32_bf16 v[92:95], a[220:223], v[80:83], v[92:95]\n"
"v_mfma_f32_16x16x32_bf16 v[88:91], a[224:227], v[100:103], 0\n"
"s_waitcnt lgkmcnt(0)\n"
"v_mfma_f32_16x16x32_bf16 v[88:91], a[228:231], v[104:107], v[88:91]\n"
"s_barrier\n"
"v_mfma_f32_16x16x32_bf16 v[88:91], a[232:235], v[108:111], v[88:91]\n"
"ds_read2st64_b32 v[136:137], v11 offset0:0 offset1:1\n"
"ds_read_b128 v[96:99], v2 offset:1536\n"
"v_mfma_f32_16x16x32_bf16 v[88:91], a[236:239], v[64:67], v[88:91]\n"
"s_waitcnt vmcnt(2)\n"
"v_mfma_f32_16x16x32_bf16 v[88:91], a[240:243], v[68:71], v[88:91]\n"
"ds_read_b128 v[84:87], v3 offset:512\n"
"v_mfma_f32_16x16x32_bf16 v[88:91], a[244:247], v[72:75], v[88:91]\n"
"ds_read_b128 v[100:103], v4 offset:2560\n"
"ds_read_b128 v[104:107], v2 offset:1792\n"
"v_mfma_f32_16x16x32_bf16 v[88:91], a[248:251], v[76:79], v[88:91]\n"
"ds_read_b128 v[108:111], v3 offset:768\n"
"ds_read_b128 v[124:127], v4 offset:2816\n"
"v_mfma_f32_16x16x32_bf16 v[88:91], a[252:255], v[80:83], v[88:91]\n"
"s_waitcnt vmcnt(8)\n"
"buffer_store_dwordx2 v[0:1], v16, s[40:43], s93 offen offset:0 sc1\n"
"v_mfma_f32_16x16x32_bf16 v[132:135], v[112:115], a[0:3], 0\n"
"buffer_store_dwordx4 v[128:131], v15, s[40:43], s94 offen sc1\n"
"v_mfma_f32_16x16x32_bf16 v[132:135], v[116:119], a[4:7], v[132:135]\n"
"s_waitcnt vmcnt(7)\n"
"v_mfma_f32_16x16x32_bf16 v[132:135], v[120:123], a[8:11], v[132:135]\n"
"v_mfma_f32_16x16x32_bf16 v[132:135], v[36:39], a[12:15], v[132:135]\n"
"s_waitcnt vmcnt(5)\n"
"v_mfma_f32_16x16x32_bf16 v[132:135], v[40:43], a[16:19], v[132:135]\n"
"s_waitcnt lgkmcnt(3)\n"
"v_add_f32 v96, v96, v84\n"
"v_add_f32 v97, v97, v85\n"
"v_mfma_f32_16x16x32_bf16 v[132:135], v[44:47], a[20:23], v[132:135]\n"
"s_waitcnt vmcnt(2)\n"
"v_add_f32 v98, v98, v86\n"
"v_mfma_f32_16x16x32_bf16 v[132:135], v[48:51], a[24:27], v[132:135]\n"
"v_add_f32 v99, v99, v87\n"
"v_add_f32 v96, v96, v100\n"
"v_mfma_f32_16x16x32_bf16 v[132:135], v[52:55], a[28:31], v[132:135]\n"
"v_add_f32 v97, v97, v101\n"
"v_mfma_f32_16x16x32_bf16 v[128:131], v[112:115], a[32:35], 0\n"
"v_add_f32 v98, v98, v102\n"
"v_add_f32 v99, v99, v103\n"
"v_mfma_f32_16x16x32_bf16 v[128:131], v[116:119], a[36:39], v[128:131]\n"
"ds_write_addtid_b32 v132 offset:1536\n"
"v_add_f32 v96, v96, v92\n"
"v_mfma_f32_16x16x32_bf16 v[128:131], v[120:123], a[40:43], v[128:131]\n"
"ds_write_addtid_b32 v133 offset:6176\n"
"v_add_f32 v97, v97, v93\n"
"v_mfma_f32_16x16x32_bf16 v[128:131], v[36:39], a[44:47], v[128:131]\n"
"ds_write_addtid_b32 v134 offset:10880\n"
"v_add_f32 v98, v98, v94\n"
"v_mfma_f32_16x16x32_bf16 v[128:131], v[40:43], a[48:51], v[128:131]\n"
"ds_write_addtid_b32 v135 offset:15520\n"
"v_add_f32 v99, v99, v95\n"
"v_mfma_f32_16x16x32_bf16 v[128:131], v[44:47], a[52:55], v[128:131]\n"
"s_add_u32 s94, s50, 0xffe80000\n"
"s_and_b32 s93, s94, 0xfbffffff\n"
"v_fma_f32 v84, v18, v137, v26\n"
"v_mfma_f32_16x16x32_bf16 v[128:131], v[48:51], a[56:59], v[128:131]\n"
"v_fma_f32 v85, v19, v137, v27\n"
"v_fma_f32 v86, v20, v137, v28\n"
"v_mfma_f32_16x16x32_bf16 v[128:131], v[52:55], a[60:63], v[128:131]\n"
"v_fma_f32 v87, v21, v137, v29\n"
"v_fma_f32 v96, v96, v136, v84\n"
"v_mfma_f32_16x16x32_bf16 v[92:95], v[112:115], a[64:67], 0\n"
"v_fma_f32 v97, v97, v136, v85\n"
"v_fma_f32 v98, v98, v136, v86\n"
"v_mfma_f32_16x16x32_bf16 v[92:95], v[116:119], a[68:71], v[92:95]\n"
"ds_write_addtid_b32 v128 offset:1792\n"
"v_mfma_f32_16x16x32_bf16 v[92:95], v[120:123], a[72:75], v[92:95]\n"
"ds_write_addtid_b32 v129 offset:6432\n"
"v_fma_f32 v99, v99, v136, v87\n"
"v_mfma_f32_16x16x32_bf16 v[92:95], v[36:39], a[76:79], v[92:95]\n"
"ds_write_addtid_b32 v130 offset:11136\n"
"v_mul_f32_e64 v96, v96, |v96|\n"
"v_mfma_f32_16x16x32_bf16 v[92:95], v[40:43], a[80:83], v[92:95]\n"
"ds_write_addtid_b32 v131 offset:15776\n"
"v_mul_f32_e64 v97, v97, |v97|\n"
"v_mfma_f32_16x16x32_bf16 v[92:95], v[44:47], a[84:87], v[92:95]\n"
"v_mul_f32_e64 v98, v98, |v98|\n"
"v_mfma_f32_16x16x32_bf16 v[92:95], v[48:51], a[88:91], v[92:95]\n"
"v_mul_f32_e64 v99, v99, |v99|\n"
"v_cvt_pk_bf16_f32 v100, v96, v97\n"
"v_mfma_f32_16x16x32_bf16 v[92:95], v[52:55], a[92:95], v[92:95]\n"
"v_cvt_pk_bf16_f32 v101, v98, v99\n"
"v_pk_max_i16 v100, v100, 0\n"
"v_mfma_f32_16x16x32_bf16 v[128:131], v[112:115], a[96:99], 0\n"
"v_pk_max_i16 v101, v101, 0\n"
"v_mfma_f32_16x16x32_bf16 v[128:131], v[116:119], a[100:103], v[128:131]\n"
"s_waitcnt lgkmcnt(8)\n"
"ds_write_addtid_b32 v92 offset:2048\n"
"v_add_f32 v104, v104, v108\n"
"v_mfma_f32_16x16x32_bf16 v[128:131], v[120:123], a[104:107], v[128:131]\n"
"ds_write_addtid_b32 v93 offset:6688\n"
"v_add_f32 v105, v105, v109\n"
"v_mfma_f32_16x16x32_bf16 v[128:131], v[36:39], a[108:111], v[128:131]\n"
"ds_write_addtid_b32 v94 offset:11392\n"
"v_add_f32 v106, v106, v110\n"
"v_mfma_f32_16x16x32_bf16 v[128:131], v[40:43], a[112:115], v[128:131]\n"
"ds_write_addtid_b32 v95 offset:16032\n"
"v_add_f32 v107, v107, v111\n"
"v_mfma_f32_16x16x32_bf16 v[128:131], v[44:47], a[116:119], v[128:131]\n"
"v_add_f32 v104, v104, v124\n"
"v_add_f32 v105, v105, v125\n"
"v_mfma_f32_16x16x32_bf16 v[128:131], v[48:51], a[120:123], v[128:131]\n"
"v_add_f32 v106, v106, v126\n"
"v_add_f32 v107, v107, v127\n"
"v_mfma_f32_16x16x32_bf16 v[128:131], v[52:55], a[124:127], v[128:131]\n"
"v_add_f32 v104, v104, v88\n"
"v_add_f32 v105, v105, v89\n"
"v_mfma_f32_16x16x32_bf16 v[92:95], v[112:115], a[128:131], 0\n"
"v_add_f32 v106, v106, v90\n"
"v_add_f32 v107, v107, v91\n"
"v_mfma_f32_16x16x32_bf16 v[92:95], v[116:119], a[132:135], v[92:95]\n"
"ds_write_addtid_b32 v128 offset:2304\n"
"v_fma_f32 v96, v22, v137, v30\n"
"v_mfma_f32_16x16x32_bf16 v[92:95], v[120:123], a[136:139], v[92:95]\n"
"ds_write_addtid_b32 v129 offset:6944\n"
"v_fma_f32 v97, v23, v137, v31\n"
"v_mfma_f32_16x16x32_bf16 v[92:95], v[36:39], a[140:143], v[92:95]\n"
"ds_write_addtid_b32 v130 offset:11648\n"
"v_fma_f32 v98, v24, v137, v32\n"
"v_mfma_f32_16x16x32_bf16 v[92:95], v[40:43], a[144:147], v[92:95]\n"
"ds_write_addtid_b32 v131 offset:16288\n"
"v_fma_f32 v99, v25, v137, v33\n"
"v_mfma_f32_16x16x32_bf16 v[92:95], v[44:47], a[148:151], v[92:95]\n"
"v_fma_f32 v104, v104, v136, v96\n"
"v_fma_f32 v105, v105, v136, v97\n"
"v_mfma_f32_16x16x32_bf16 v[92:95], v[48:51], a[152:155], v[92:95]\n"
"v_fma_f32 v106, v106, v136, v98\n"
"v_fma_f32 v107, v107, v136, v99\n"
"v_mfma_f32_16x16x32_bf16 v[92:95], v[52:55], a[156:159], v[92:95]\n"
"v_mul_f32_e64 v104, v104, |v104|\n"
"v_mul_f32_e64 v105, v105, |v105|\n"
"v_mfma_f32_16x16x32_bf16 v[96:99], v[112:115], a[160:163], 0\n"
"v_mul_f32_e64 v106, v106, |v106|\n"
"v_mul_f32_e64 v107, v107, |v107|\n"
"v_mfma_f32_16x16x32_bf16 v[96:99], v[116:119], a[164:167], v[96:99]\n"
"ds_write_addtid_b32 v92 offset:2560\n"
"v_cvt_pk_bf16_f32 v102, v104, v105\n"
"v_mfma_f32_16x16x32_bf16 v[96:99], v[120:123], a[168:171], v[96:99]\n"
"ds_write_addtid_b32 v93 offset:7200\n"
"v_cvt_pk_bf16_f32 v103, v106, v107\n"
"v_mfma_f32_16x16x32_bf16 v[96:99], v[36:39], a[172:175], v[96:99]\n"
"ds_write_addtid_b32 v94 offset:11904\n"
"v_pk_max_i16 v102, v102, 0\n"
"v_mfma_f32_16x16x32_bf16 v[96:99], v[40:43], a[176:179], v[96:99]\n"
"ds_write_addtid_b32 v95 offset:16544\n"
"v_pk_max_i16 v103, v103, 0\n"
"v_mfma_f32_16x16x32_bf16 v[96:99], v[44:47], a[180:183], v[96:99]\n"
"buffer_store_dwordx4 v[100:103], v15, s[40:43], s93 offen sc1\n"
"v_mfma_f32_16x16x32_bf16 v[96:99], v[48:51], a[184:187], v[96:99]\n"
"v_mfma_f32_16x16x32_bf16 v[96:99], v[52:55], a[188:191], v[96:99]\n"
"v_mfma_f32_16x16x32_bf16 v[100:103], a[192:195], v[112:115], 0\n"
"v_mfma_f32_16x16x32_bf16 v[100:103], a[196:199], v[116:119], v[100:103]\n"
"ds_write_addtid_b32 v96 offset:2816\n"
"v_mfma_f32_16x16x32_bf16 v[100:103], a[200:203], v[120:123], v[100:103]\n"
"s_waitcnt vmcnt(1)\n"
"buffer_store_dwordx2 v[0:1], v16, s[40:43], s95 offen offset:0 sc1\n"
"ds_write_addtid_b32 v97 offset:7456\n"
"v_mfma_f32_16x16x32_bf16 v[100:103], a[204:207], v[36:39], v[100:103]\n"
"ds_write_addtid_b32 v98 offset:12160\n"
"v_mfma_f32_16x16x32_bf16 v[100:103], a[208:211], v[40:43], v[100:103]\n"
"ds_write_addtid_b32 v99 offset:16800\n"
"v_mfma_f32_16x16x32_bf16 v[100:103], a[212:215], v[44:47], v[100:103]\n"
"v_mfma_f32_16x16x32_bf16 v[100:103], a[216:219], v[48:51], v[100:103]\n"
"v_mfma_f32_16x16x32_bf16 v[100:103], a[220:223], v[52:55], v[100:103]\n"
"v_mfma_f32_16x16x32_bf16 v[104:107], a[224:227], v[112:115], 0\n"
"s_waitcnt lgkmcnt(0)\n"
"v_mfma_f32_16x16x32_bf16 v[104:107], a[228:231], v[116:119], v[104:107]\n"
"s_barrier\n"
"v_mfma_f32_16x16x32_bf16 v[104:107], a[232:235], v[120:123], v[104:107]\n"
"ds_read2st64_b32 v[132:133], v12 offset0:0 offset1:1\n"
"ds_read_b128 v[108:111], v2 offset:0\n"
"v_mfma_f32_16x16x32_bf16 v[104:107], a[236:239], v[36:39], v[104:107]\n"
"ds_read_b128 v[96:99], v3 offset:2048\n"
"ds_read_b128 v[112:115], v4 offset:1024\n"
"v_mfma_f32_16x16x32_bf16 v[104:107], a[240:243], v[40:43], v[104:107]\n"
"v_mfma_f32_16x16x32_bf16 v[104:107], a[244:247], v[44:47], v[104:107]\n"
"ds_read_b128 v[116:119], v2 offset:256\n"
"v_mfma_f32_16x16x32_bf16 v[104:107], a[248:251], v[48:51], v[104:107]\n"
"ds_read_b128 v[120:123], v3 offset:2304\n"
"ds_read_b128 v[124:127], v4 offset:1280\n"
"v_mfma_f32_16x16x32_bf16 v[104:107], a[252:255], v[52:55], v[104:107]\n"
"s_waitcnt lgkmcnt(3)\n"
"v_add_f32 v108, v108, v96\n"
"v_add_f32 v109, v109, v97\n"
"v_add_f32 v110, v110, v98\n"
"v_add_f32 v111, v111, v99\n"
"v_add_f32 v108, v108, v112\n"
"v_add_f32 v109, v109, v113\n"
"v_add_f32 v110, v110, v114\n"
"v_add_f32 v111, v111, v115\n"
"v_add_f32 v108, v108, v100\n"
"v_add_f32 v109, v109, v101\n"
"v_add_f32 v110, v110, v102\n"
"v_add_f32 v111, v111, v103\n"
"s_add_u32 s95, s50, 0x0\n"
"s_and_b32 s93, s95, 0xfbffffff\n"
"v_fma_f32 v96, v18, v133, v26\n"
"v_fma_f32 v97, v19, v133, v27\n"
"v_fma_f32 v98, v20, v133, v28\n"
"v_fma_f32 v99, v21, v133, v29\n"
"v_fma_f32 v108, v108, v132, v96\n"
"v_fma_f32 v109, v109, v132, v97\n"
"v_fma_f32 v110, v110, v132, v98\n"
"v_fma_f32 v111, v111, v132, v99\n"
"v_mul_f32_e64 v108, v108, |v108|\n"
"v_mul_f32_e64 v109, v109, |v109|\n"
"v_mul_f32_e64 v110, v110, |v110|\n"
"v_mul_f32_e64 v111, v111, |v111|\n"
"v_cvt_pk_bf16_f32 v128, v108, v109\n"
"v_cvt_pk_bf16_f32 v129, v110, v111\n"
"v_pk_max_i16 v128, v128, 0\n"
"v_pk_max_i16 v129, v129, 0\n"
"s_waitcnt lgkmcnt(0)\n"
"v_add_f32 v116, v116, v120\n"
"v_add_f32 v117, v117, v121\n"
"v_add_f32 v118, v118, v122\n"
"v_add_f32 v119, v119, v123\n"
"v_add_f32 v116, v116, v124\n"
"v_add_f32 v117, v117, v125\n"
"v_add_f32 v118, v118, v126\n"
"v_add_f32 v119, v119, v127\n"
"s_waitcnt vmcnt(1)\n"
"buffer_store_dwordx2 v[0:1], v16, s[40:43], s94 offen offset:0 sc1\n"
"v_add_f32 v116, v116, v104\n"
"v_add_f32 v117, v117, v105\n"
"v_add_f32 v118, v118, v106\n"
"v_add_f32 v119, v119, v107\n"
"v_fma_f32 v100, v22, v133, v30\n"
"v_fma_f32 v101, v23, v133, v31\n"
"v_fma_f32 v102, v24, v133, v32\n"
"v_fma_f32 v103, v25, v133, v33\n"
"v_fma_f32 v116, v116, v132, v100\n"
"v_fma_f32 v117, v117, v132, v101\n"
"v_fma_f32 v118, v118, v132, v102\n"
"v_fma_f32 v119, v119, v132, v103\n"
"v_mul_f32_e64 v116, v116, |v116|\n"
"v_mul_f32_e64 v117, v117, |v117|\n"
"v_mul_f32_e64 v118, v118, |v118|\n"
"v_mul_f32_e64 v119, v119, |v119|\n"
"v_cvt_pk_bf16_f32 v130, v116, v117\n"
"v_cvt_pk_bf16_f32 v131, v118, v119\n"
"v_pk_max_i16 v130, v130, 0\n"
"v_pk_max_i16 v131, v131, 0\n"
"buffer_store_dwordx4 v[128:131], v15, s[40:43], s93 offen sc1\n"
"s_waitcnt vmcnt(0)\n"
"buffer_store_dwordx2 v[0:1], v16, s[40:43], s95 offen offset:0 sc1\n"
"s_branch .LF_end\n"
".LF_t3:\n"
"v_mfma_f32_16x16x32_bf16 v[100:103], a[196:199], v[116:119], v[100:103]\n"
"ds_write_addtid_b32 v96 offset:2816\n"
"v_mfma_f32_16x16x32_bf16 v[100:103], a[200:203], v[120:123], v[100:103]\n"
"s_waitcnt vmcnt(6)\n"
"buffer_store_dwordx2 v[0:1], v16, s[40:43], s95 offen offset:0 sc1\n"
"ds_write_addtid_b32 v97 offset:7456\n"
"v_mfma_f32_16x16x32_bf16 v[100:103], a[204:207], v[36:39], v[100:103]\n"
"ds_write_addtid_b32 v98 offset:12160\n"
"v_mfma_f32_16x16x32_bf16 v[100:103], a[208:211], v[40:43], v[100:103]\n"
"ds_write_addtid_b32 v99 offset:16800\n"
"v_mfma_f32_16x16x32_bf16 v[100:103], a[212:215], v[44:47], v[100:103]\n"
"v_mfma_f32_16x16x32_bf16 v[100:103], a[216:219], v[48:51], v[100:103]\n"
"v_mfma_f32_16x16x32_bf16 v[100:103], a[220:223], v[52:55], v[100:103]\n"
"v_mfma_f32_16x16x32_bf16 v[104:107], a[224:227], v[112:115], 0\n"
"s_waitcnt lgkmcnt(0)\n"
"v_mfma_f32_16x16x32_bf16 v[104:107], a[228:231], v[116:119], v[104:107]\n"
"s_barrier\n"
"v_mfma_f32_16x16x32_bf16 v[104:107], a[232:235], v[120:123], v[104:107]\n"
"ds_read2st64_b32 v[132:133], v12 offset0:0 offset1:1\n"
"ds_read_b128 v[108:111], v2 offset:0\n"
"v_mfma_f32_16x16x32_bf16 v[104:107], a[236:239], v[36:39], v[104:107]\n"
"ds_read_b128 v[96:99], v3 offset:2048\n"
"ds_read_b128 v[112:115], v4 offset:1024\n"
"v_mfma_f32_16x16x32_bf16 v[104:107], a[240:243], v[40:43], v[104:107]\n"
"v_mfma_f32_16x16x32_bf16 v[104:107], a[244:247], v[44:47], v[104:107]\n"
"ds_read_b128 v[116:119], v2 offset:256\n"
"v_mfma_f32_16x16x32_bf16 v[104:107], a[248:251], v[48:51], v[104:107]\n"
"ds_read_b128 v[120:123], v3 offset:2304\n"
"ds_read_b128 v[124:127], v4 offset:1280\n"
"v_mfma_f32_16x16x32_bf16 v[104:107], a[252:255], v[52:55], v[104:107]\n"
"s_waitcnt vmcnt(11)\n"
"v_mfma_f32_16x16x32_bf16 v[128:131], v[56:59], a[0:3], 0\n"
"v_mfma_f32_16x16x32_bf16 v[128:131], v[60:63], a[4:7], v[128:131]\n"
"s_waitcnt vmcnt(6)\n"
"v_mfma_f32_16x16x32_bf16 v[128:131], v[64:67], a[8:11], v[128:131]\n"
"s_waitcnt lgkmcnt(3)\n"
"v_add_f32 v108, v108, v96\n"
"v_add_f32 v109, v109, v97\n"
"v_mfma_f32_16x16x32_bf16 v[128:131], v[68:71], a[12:15], v[128:131]\n"
"s_waitcnt vmcnt(4)\n"
"v_add_f32 v110, v110, v98\n"
"v_add_f32 v111, v111, v99\n"
"v_mfma_f32_16x16x32_bf16 v[128:131], v[72:75], a[16:19], v[128:131]\n"
"v_add_f32 v108, v108, v112\n"
"v_add_f32 v109, v109, v113\n"
"v_mfma_f32_16x16x32_bf16 v[128:131], v[76:79], a[20:23], v[128:131]\n"
"s_waitcnt vmcnt(2)\n"
"v_add_f32 v110, v110, v114\n"
"v_add_f32 v111, v111, v115\n"
"v_mfma_f32_16x16x32_bf16 v[128:131], v[80:83], a[24:27], v[128:131]\n"
"v_add_f32 v108, v108, v100\n"
"v_add_f32 v109, v109, v101\n"
"v_mfma_f32_16x16x32_bf16 v[128:131], v[84:87], a[28:31], v[128:131]\n"
"v_add_f32 v110, v110, v102\n"
"v_mfma_f32_16x16x32_bf16 v[112:115], v[56:59], a[32:35], 0\n"
"v_add_f32 v111, v111, v103\n"
"v_mfma_f32_16x16x32_bf16 v[112:115], v[60:63], a[36:39], v[112:115]\n"
"ds_write_addtid_b32 v128 offset:0\n"
"v_mfma_f32_16x16x32_bf16 v[112:115], v[64:67], a[40:43], v[112:115]\n"
"s_add_u32 s95, s50, 0xffe80000\n"
"s_and_b32 s93, s95, 0xfbffffff\n"
"ds_write_addtid_b32 v129 offset:4640\n"
"v_mfma_f32_16x16x32_bf16 v[112:115], v[68:71], a[44:47], v[112:115]\n"
"ds_write_addtid_b32 v130 offset:9344\n"
"v_fma_f32 v96, v18, v133, v26\n"
"v_mfma_f32_16x16x32_bf16 v[112:115], v[72:75], a[48:51], v[112:115]\n"
"ds_write_addtid_b32 v131 offset:13984\n"
"v_fma_f32 v97, v19, v133, v27\n"
"v_mfma_f32_16x16x32_bf16 v[112:115], v[76:79], a[52:55], v[112:115]\n"
"v_fma_f32 v98, v20, v133, v28\n"
"v_fma_f32 v99, v21, v133, v29\n"
"v_mfma_f32_16x16x32_bf16 v[112:115], v[80:83], a[56:59], v[112:115]\n"
"v_fma_f32 v108, v108, v132, v96\n"
"v_fma_f32 v109, v109, v132, v97\n"
"v_mfma_f32_16x16x32_bf16 v[112:115], v[84:87], a[60:63], v[112:115]\n"
"v_fma_f32 v110, v110, v132, v98\n"
"v_fma_f32 v111, v111, v132, v99\n"
"v_mfma_f32_16x16x32_bf16 v[100:103], v[56:59], a[64:67], 0\n"
"v_mul_f32_e64 v108, v108, |v108|\n"
"v_mfma_f32_16x16x32_bf16 v[100:103], v[60:63], a[68:71], v[100:103]\n"
"ds_write_addtid_b32 v112 offset:256\n"
"v_mul_f32_e64 v109, v109, |v109|\n"
"v_mfma_f32_16x16x32_bf16 v[100:103], v[64:67], a[72:75], v[100:103]\n"
"ds_write_addtid_b32 v113 offset:4896\n"
"v_mul_f32_e64 v110, v110, |v110|\n"
"v_mfma_f32_16x16x32_bf16 v[100:103], v[68:71], a[76:79], v[100:103]\n"
"ds_write_addtid_b32 v114 offset:9600\n"
"v_mul_f32_e64 v111, v111, |v111|\n"
"v_mfma_f32_16x16x32_bf16 v[100:103], v[72:75], a[80:83], v[100:103]\n"
"ds_write_addtid_b32 v115 offset:14240\n"
"v_mfma_f32_16x16x32_bf16 v[100:103], v[76:79], a[84:87], v[100:103]\n"
"v_cvt_pk_bf16_f32 v128, v108, v109\n"
"v_cvt_pk_bf16_f32 v129, v110, v111\n"
"v_mfma_f32_16x16x32_bf16 v[100:103], v[80:83], a[88:91], v[100:103]\n"
"v_pk_max_i16 v128, v128, 0\n"
"v_pk_max_i16 v129, v129, 0\n"
"v_mfma_f32_16x16x32_bf16 v[100:103], v[84:87], a[92:95], v[100:103]\n"
"s_waitcnt lgkmcnt(8)\n"
"v_add_f32 v116, v116, v120\n"
"v_add_f32 v117, v117, v121\n"
"v_mfma_f32_16x16x32_bf16 v[108:111], v[56:59], a[96:99], 0\n"
"v_add_f32 v118, v118, v122\n"
"v_add_f32 v119, v119, v123\n"
"v_mfma_f32_16x16x32_bf16 v[108:111], v[60:63], a[100:103], v[108:111]\n"
"ds_write_addtid_b32 v100 offset:512\n"
"v_add_f32 v116, v116, v124\n"
"v_mfma_f32_16x16x32_bf16 v[108:111], v[64:67], a[104:107], v[108:111]\n"
"ds_write_addtid_b32 v101 offset:5152\n"
"v_add_f32 v117, v117, v125\n"
"v_mfma_f32_16x16x32_bf16 v[108:111], v[68:71], a[108:111], v[108:111]\n"
"ds_write_addtid_b32 v102 offset:9856\n"
"v_add_f32 v118, v118, v126\n"
"v_mfma_f32_16x16x32_bf16 v[108:111], v[72:75], a[112:115], v[108:111]\n"
"ds_write_addtid_b32 v103 offset:14496\n"
"v_add_f32 v119, v119, v127\n"
"v_mfma_f32_16x16x32_bf16 v[108:111], v[76:79], a[116:119], v[108:111]\n"
"s_waitcnt vmcnt(1)\n"
"buffer_store_dwordx2 v[0:1], v16, s[40:43], s94 offen offset:0 sc1\n"
"v_add_f32 v116, v116, v104\n"
"v_mfma_f32_16x16x32_bf16 v[108:111], v[80:83], a[120:123], v[108:111]\n"
"v_add_f32 v117, v117, v105\n"
"v_add_f32 v118, v118, v106\n"
"v_mfma_f32_16x16x32_bf16 v[108:111], v[84:87], a[124:127], v[108:111]\n"
"v_add_f32 v119, v119, v107\n"
"v_fma_f32 v100, v22, v133, v30\n"
"v_mfma_f32_16x16x32_bf16 v[112:115], v[56:59], a[128:131], 0\n"
"v_fma_f32 v101, v23, v133, v31\n"
"v_fma_f32 v102, v24, v133, v32\n"
"v_mfma_f32_16x16x32_bf16 v[112:115], v[60:63], a[132:135], v[112:115]\n"
"ds_write_addtid_b32 v108 offset:768\n"
"v_fma_f32 v103, v25, v133, v33\n"
"v_mfma_f32_16x16x32_bf16 v[112:115], v[64:67], a[136:139], v[112:115]\n"
"ds_write_addtid_b32 v109 offset:5408\n"
"v_fma_f32 v116, v116, v132, v100\n"
"v_mfma_f32_16x16x32_bf16 v[112:115], v[68:71], a[140:143], v[112:115]\n"
"ds_write_addtid_b32 v110 offset:10112\n"
"v_fma_f32 v117, v117, v132, v101\n"
"v_mfma_f32_16x16x32_bf16 v[112:115], v[72:75], a[144:147], v[112:115]\n"
"ds_write_addtid_b32 v111 offset:14752\n"
"v_fma_f32 v118, v118, v132, v102\n"
"v_mfma_f32_16x16x32_bf16 v[112:115], v[76:79], a[148:151], v[112:115]\n"
"v_fma_f32 v119, v119, v132, v103\n"
"v_mul_f32_e64 v116, v116, |v116|\n"
"v_mfma_f32_16x16x32_bf16 v[112:115], v[80:83], a[152:155], v[112:115]\n"
"v_mul_f32_e64 v117, v117, |v117|\n"
"v_mul_f32_e64 v118, v118, |v118|\n"
"v_mfma_f32_16x16x32_bf16 v[112:115], v[84:87], a[156:159], v[112:115]\n"
"v_mul_f32_e64 v119, v119, |v119|\n"
"v_cvt_pk_bf16_f32 v130, v116, v117\n"
"v_mfma_f32_16x16x32_bf16 v[108:111], v[56:59], a[160:163], 0\n"
"v_cvt_pk_bf16_f32 v131, v118, v119\n"
"v_pk_max_i16 v130, v130, 0\n"
"v_mfma_f32_16x16x32_bf16 v[108:111], v[60:63], a[164:167], v[108:111]\n"
"ds_write_addtid_b32 v112 offset:1024\n"
"v_pk_max_i16 v131, v131, 0\n"
"v_mfma_f32_16x16x32_bf16 v[108:111], v[64:67], a[168:171], v[108:111]\n"
"ds_write_addtid_b32 v113 offset:5664\n"
"v_mfma_f32_16x16x32_bf16 v[108:111], v[68:71], a[172:175], v[108:111]\n"
"ds_write_addtid_b32 v114 offset:10368\n"
"v_mfma_f32_16x16x32_bf16 v[108:111], v[72:75], a[176:179], v[108:111]\n"
"ds_write_addtid_b32 v115 offset:15008\n"
"v_mfma_f32_16x16x32_bf16 v[108:111], v[76:79], a[180:183], v[108:111]\n"
"buffer_store_dwordx4 v[128:131], v15, s[40:43], s93 offen sc1\n"
"v_mfma_f32_16x16x32_bf16 v[108:111], v[80:83], a[184:187], v[108:111]\n"
"v_mfma_f32_16x16x32_bf16 v[108:111], v[84:87], a[188:191], v[108:111]\n"
"v_mfma_f32_16x16x32_bf16 v[112:115], a[192:195], v[56:59], 0\n"
"v_mfma_f32_16x16x32_bf16 v[112:115], a[196:199], v[60:63], v[112:115]\n"
"ds_write_addtid_b32 v108 offset:1280\n"
"v_mfma_f32_16x16x32_bf16 v[112:115], a[200:203], v[64:67], v[112:115]\n"
"ds_write_addtid_b32 v109 offset:5920\n"
"v_mfma_f32_16x16x32_bf16 v[112:115], a[204:207], v[68:71], v[112:115]\n"
"ds_write_addtid_b32 v110 offset:10624\n"
"v_mfma_f32_16x16x32_bf16 v[112:115], a[208:211], v[72:75], v[112:115]\n"
"ds_write_addtid_b32 v111 offset:15264\n"
"v_mfma_f32_16x16x32_bf16 v[112:115], a[212:215], v[76:79], v[112:115]\n"
"v_mfma_f32_16x16x32_bf16 v[112:115], a[216:219], v[80:83], v[112:115]\n"
"v_mfma_f32_16x16x32_bf16 v[112:115], a[220:223], v[84:87], v[112:115]\n"
"v_mfma_f32_16x16x32_bf16 v[132:135], a[224:227], v[56:59], 0\n"
"s_waitcnt lgkmcnt(0)\n"
"v_mfma_f32_16x16x32_bf16 v[132:135], a[228:231], v[60:63], v[132:135]\n"
"s_barrier\n"
"v_mfma_f32_16x16x32_bf16 v[132:135], a[232:235], v[64:67], v[132:135]\n"
"ds_read2st64_b32 v[152:153], v13 offset0:0 offset1:1\n"
"v_mfma_f32_16x16x32_bf16 v[132:135], a[236:239], v[68:71], v[132:135]\n"
"ds_read_b128 v[56:59], v2 offset:1536\n"
"ds_read_b128 v[60:63], v3 offset:512\n"
"v_mfma_f32_16x16x32_bf16 v[132:135], a[240:243], v[72:75], v[132:135]\n"
"ds_read_b128 v[136:139], v4 offset:2560\n"
"ds_read_b128 v[140:143], v2 offset:1792\n"
"v_mfma_f32_16x16x32_bf16 v[132:135], a[244:247], v[76:79], v[132:135]\n"
"v_mfma_f32_16x16x32_bf16 v[132:135], a[248:251], v[80:83], v[132:135]\n"
"ds_read_b128 v[144:147], v3 offset:768\n"
"v_mfma_f32_16x16x32_bf16 v[132:135], a[252:255], v[84:87], v[132:135]\n"
"ds_read_b128 v[148:151], v4 offset:2816\n"
"s_waitcnt lgkmcnt(3)\n"
"v_add_f32 v56, v56, v60\n"
"v_add_f32 v57, v57, v61\n"
"v_add_f32 v58, v58, v62\n"
"v_add_f32 v59, v59, v63\n"
"v_add_f32 v56, v56, v136\n"
"v_add_f32 v57, v57, v137\n"
"v_add_f32 v58, v58, v138\n"
"v_add_f32 v59, v59, v139\n"
"v_add_f32 v56, v56, v112\n"
"v_add_f32 v57, v57, v113\n"
"v_add_f32 v58, v58, v114\n"
"v_add_f32 v59, v59, v115\n"
"s_add_u32 s93, s50, 0x0\n"
"s_and_b32 s94, s93, 0xfbffffff\n"
"v_fma_f32 v80, v18, v153, v26\n"
"v_fma_f32 v81, v19, v153, v27\n"
"v_fma_f32 v82, v20, v153, v28\n"
"v_fma_f32 v83, v21, v153, v29\n"
"v_fma_f32 v56, v56, v152, v80\n"
"v_fma_f32 v57, v57, v152, v81\n"
"v_fma_f32 v58, v58, v152, v82\n"
"v_fma_f32 v59, v59, v152, v83\n"
"v_mul_f32_e64 v56, v56, |v56|\n"
"v_mul_f32_e64 v57, v57, |v57|\n"
"v_mul_f32_e64 v58, v58, |v58|\n"
"v_mul_f32_e64 v59, v59, |v59|\n"
"v_cvt_pk_bf16_f32 v136, v56, v57\n"
"v_cvt_pk_bf16_f32 v137, v58, v59\n"
"v_pk_max_i16 v136, v136, 0\n"
"v_pk_max_i16 v137, v137, 0\n"
"s_waitcnt lgkmcnt(0)\n"
"v_add_f32 v140, v140, v144\n"
"v_add_f32 v141, v141, v145\n"
"v_add_f32 v142, v142, v146\n"
"v_add_f32 v143, v143, v147\n"
"v_add_f32 v140, v140, v148\n"
"v_add_f32 v141, v141, v149\n"
"v_add_f32 v142, v142, v150\n"
"v_add_f32 v143, v143, v151\n"
"s_waitcnt vmcnt(0)\n"
"buffer_store_dwordx2 v[0:1], v16, s[40:43], s95 offen offset:0 sc1\n"
"v_add_f32 v140, v140, v132\n"
"v_add_f32 v141, v141, v133\n"
"v_add_f32 v142, v142, v134\n"
"v_add_f32 v143, v143, v135\n"
"v_fma_f32 v60, v22, v153, v30\n"
"v_fma_f32 v61, v23, v153, v31\n"
"v_fma_f32 v62, v24, v153, v32\n"
"v_fma_f32 v63, v25, v153, v33\n"
"v_fma_f32 v140, v140, v152, v60\n"
"v_fma_f32 v141, v141, v152, v61\n"
"v_fma_f32 v142, v142, v152, v62\n"
"v_fma_f32 v143, v143, v152, v63\n"
"v_mul_f32_e64 v140, v140, |v140|\n"
"v_mul_f32_e64 v141, v141, |v141|\n"
"v_mul_f32_e64 v142, v142, |v142|\n"
"v_mul_f32_e64 v143, v143, |v143|\n"
"v_cvt_pk_bf16_f32 v138, v140, v141\n"
"v_cvt_pk_bf16_f32 v139, v142, v143\n"
"v_pk_max_i16 v138, v138, 0\n"
"v_pk_max_i16 v139, v139, 0\n"
"buffer_store_dwordx4 v[136:139], v15, s[40:43], s94 offen sc1\n"
"s_waitcnt vmcnt(0)\n"
"buffer_store_dwordx2 v[0:1], v16, s[40:43], s93 offen offset:0 sc1\n"
"s_branch .LF_end\n"
".LF_end:\n"
"s_waitcnt vmcnt(0) lgkmcnt(0)\n"
"s_endpgm\n"
      :: "{v2}"((unsigned)(lds0 + ((wid + 3) & 3) * 19104 + trp)), "{v3}"((unsigned)(lds0 + ((wid + 2) & 3) * 19104 + trp)), "{v4}"((unsigned)(lds0 + ((wid + 1) & 3) * 19104 + trp)), "{v5}"((unsigned)(lane * 16 + wid * 8192 + ((0 + rot) % 4) * 2048 + 134283264)), "{v6}"((unsigned)(lane * 16 + wid * 8192 + ((1 + rot) % 4) * 2048 + 134283264)), "{v7}"((unsigned)(lane * 16 + wid * 8192 + ((2 + rot) % 4) * 2048 + 134283264)), "{v8}"((unsigned)(lane * 16 + wid * 8192 + ((3 + rot) % 4) * 2048 + 134283264)), "{v9}"((unsigned)((lane & 31) * 8 + 352768 + F_GL * 1572864)), "{v10}"((unsigned)(lane * 16 + 134561792)), "{v11}"((unsigned)(lds0 + ((wid + 0) & 3) * 19104 + 18592 + ((li >> 1) + 32 * (li & 1)) * 4)), "{v12}"((unsigned)(lds0 + ((wid + 1) & 3) * 19104 + 18592 + ((li >> 1) + 32 * (li & 1)) * 4)), "{v13}"((unsigned)(lds0 + ((wid + 2) & 3) * 19104 + 18592 + ((li >> 1) + 32 * (li & 1)) * 4)), "{v14}"((unsigned)(lds0 + ((wid + 3) & 3) * 19104 + 18592 + ((li >> 1) + 32 * (li & 1)) * 4)), "{v15}"((unsigned)(lane * 16 + pair * 1024 + 98304)), "{v16}"((unsigned)(lane ? DROP : pair * 8 + idx / 7 * 32 + 353024)), "{v17}"((unsigned)(0x3727c5ac)), "{v36}"((unsigned)(vT + tsw)), "{v37}"((unsigned)(vT + (tsw ^ 8))), "{v38}"((unsigned)(pair * 64 + lg * 16)), "{v39}"((unsigned)(vD + ((0 + rot) % 4) * 458752)), "{v40}"((unsigned)(vD + ((1 + rot) % 4) * 458752)), "{v41}"((unsigned)(vD + ((2 + rot) % 4) * 458752)), "{v42}"((unsigned)(vD + ((3 + rot) % 4) * 458752)), "{v43}"((unsigned)(lds0 + wid * 19104 + trp)), "{s[40:43]}"(rS), "{s[44:47]}"(rW), "{s[48:49]}"(wsp), "{s50}"((unsigned)(sA)), "{s51}"((unsigned)(sLo)), "{s52}"((unsigned)(sHi)), "{s53}"((unsigned)(sEx)), "{s54}"((unsigned)(sEnt)), "{s55}"((unsigned)(sLd)), "{s[56:57]}"(wsb + (sLo & 0x3ffffff)), "{s59}"((unsigned)(lds0 + wid * 19104)), "{s62}"((unsigned)(0x3a800000)), "{s[64:65]}"((u64)a.b1), "{s66}"((unsigned)(sW0 + 64 * ((wid + 1) & 3))), "{s67}"((unsigned)(sW0 + 64 * ((wid + 2) & 3))), "{s68}"((unsigned)(sW0 + 64 * ((wid + 3) & 3))), "{s69}"((unsigned)(sW0 + 64 * ((wid + 4) & 3)))
      : "v255", "a255", "s95", "vcc", "scc", "memory");

  } else {
    const int kq = idx / 7, grp = idx % 7, f0 = kq * 28 + wid * 7, pg0 = grp < 4 ? grp * 5 : 4 + grp * 4, pair = pg0 + wid;
    const unsigned rot = SLP & 8 ? 0 : (set * 5) >> 1, tsw = (kq & 1) * 8, sW0 = f0 * 32 * 2048 + pg0 * 64, vD = (lane >> 2) * 2048 + (lane & 3) * 16;
    const u32x4 rW = desc(a.w2, 3584 * 2048);
    if (idx == 0) {
      const unsigned sEx = sHi + H_EX * 3 * RS, sEnt = -wid & 3;
    asm volatile(
"s_nop 4\n"
"v_mov_b32 v186, 0\n"
"global_load_dwordx2 v[0:1], v186, s[48:49] sc1\n"
"v_mov_b32 v188, 0x3f803f80\n"
"v_mov_b32 v189, 0x3f803f80\n"
"v_mov_b32 v190, 0x3f803f80\n"
"v_mov_b32 v191, 0x3f803f80\n"
"s_add_u32 m0, s55, 0\n"
"s_add_u32 s60, s66, 0\n"
"buffer_load_dwordx4 v50, s[44:47], s60 offen lds\n"
"s_add_u32 m0, s55, 1024\n"
"s_add_u32 s60, s66, 32768\n"
"buffer_load_dwordx4 v50, s[44:47], s60 offen lds\n"
"s_add_u32 m0, s55, 2048\n"
"s_add_u32 s60, s67, 0\n"
"buffer_load_dwordx4 v50, s[44:47], s60 offen lds\n"
"s_add_u32 m0, s55, 3072\n"
"s_add_u32 s60, s67, 32768\n"
"buffer_load_dwordx4 v50, s[44:47], s60 offen lds\n"
"s_add_u32 m0, s55, 4096\n"
"s_add_u32 s60, s68, 0\n"
"buffer_load_dwordx4 v50, s[44:47], s60 offen lds\n"
"s_add_u32 m0, s55, 5120\n"
"s_add_u32 s60, s68, 32768\n"
"buffer_load_dwordx4 v50, s[44:47], s60 offen lds\n"
"s_add_u32 m0, s55, 6144\n"
"s_add_u32 s60, s69, 0\n"
"buffer_load_dwordx4 v50, s[44:47], s60 offen lds\n"
"s_add_u32 m0, s55, 7168\n"
"s_add_u32 s60, s69, 32768\n"
"buffer_load_dwordx4 v50, s[44:47], s60 offen lds\n"
"s_add_u32 m0, s55, 8192\n"
"s_add_u32 s60, s70, 0\n"
"buffer_load_dwordx4 v50, s[44:47], s60 offen lds\n"
"s_add_u32 m0, s55, 9216\n"
"s_add_u32 s60, s70, 32768\n"
"buffer_load_dwordx4 v50, s[44:47], s60 offen lds\n"
"s_add_u32 m0, s55, 10240\n"
"s_add_u32 s60, s66, 0\n"
"buffer_load_dwordx4 v51, s[44:47], s60 offen lds\n"
"s_add_u32 m0, s55, 11264\n"
"s_add_u32 s60, s66, 32768\n"
"buffer_load_dwordx4 v51, s[44:47], s60 offen lds\n"
"s_add_u32 m0, s55, 12288\n"
"s_add_u32 s60, s67, 0\n"
"buffer_load_dwordx4 v51, s[44:47], s60 offen lds\n"
"s_add_u32 m0, s55, 13312\n"
"s_add_u32 s60, s67, 32768\n"
"buffer_load_dwordx4 v51, s[44:47], s60 offen lds\n"
"s_add_u32 m0, s55, 14336\n"
"s_add_u32 s60, s68, 0\n"
"buffer_load_dwordx4 v51, s[44:47], s60 offen lds\n"
"s_add_u32 m0, s55, 15360\n"
"s_add_u32 s60, s68, 32768\n"
"buffer_load_dwordx4 v51, s[44:47], s60 offen lds\n"
"s_add_u32 m0, s55, 16384\n"
"s_add_u32 s60, s69, 0\n"
"buffer_load_dwordx4 v51, s[44:47], s60 offen lds\n"
"s_add_u32 m0, s55, 17408\n"
"s_add_u32 s60, s69, 32768\n"
"buffer_load_dwordx4 v51, s[44:47], s60 offen lds\n"
"s_add_u32 m0, s55, 18432\n"
"s_add_u32 s60, s70, 0\n"
"buffer_load_dwordx4 v51, s[44:47], s60 offen lds\n"
"s_add_u32 m0, s55, 19456\n"
"s_add_u32 s60, s70, 32768\n"
"buffer_load_dwordx4 v51, s[44:47], s60 offen lds\n"
"s_add_u32 m0, s55, 20480\n"
"s_add_u32 s60, s66, 0\n"
"buffer_load_dwordx4 v52, s[44:47], s60 offen lds\n"
"s_add_u32 m0, s55, 21504\n"
"s_add_u32 s60, s66, 32768\n"
"buffer_load_dwordx4 v52, s[44:47], s60 offen lds\n"
"s_add_u32 m0, s55, 22528\n"
"s_add_u32 s60, s67, 0\n"
"buffer_load_dwordx4 v52, s[44:47], s60 offen lds\n"
"s_add_u32 m0, s55, 23552\n"
"s_add_u32 s60, s67, 32768\n"
"buffer_load_dwordx4 v52, s[44:47], s60 offen lds\n"
"s_add_u32 m0, s55, 24576\n"
"s_add_u32 s60, s68, 0\n"
"buffer_load_dwordx4 v52, s[44:47], s60 offen lds\n"
"s_add_u32 m0, s55, 25600\n"
"s_add_u32 s60, s68, 32768\n"
"buffer_load_dwordx4 v52, s[44:47], s60 offen lds\n"
"s_add_u32 m0, s55, 26624\n"
"s_add_u32 s60, s69, 0\n"
"buffer_load_dwordx4 v52, s[44:47], s60 offen lds\n"
"s_add_u32 m0, s55, 27648\n"
"s_add_u32 s60, s69, 32768\n"
"buffer_load_dwordx4 v52, s[44:47], s60 offen lds\n"
"s_add_u32 m0, s55, 28672\n"
"s_add_u32 s60, s70, 0\n"
"buffer_load_dwordx4 v52, s[44:47], s60 offen lds\n"
"s_add_u32 m0, s55, 29696\n"
"s_add_u32 s60, s70, 32768\n"
"buffer_load_dwordx4 v52, s[44:47], s60 offen lds\n"
"s_add_u32 m0, s55, 30720\n"
"s_add_u32 s60, s66, 0\n"
"buffer_load_dwordx4 v53, s[44:47], s60 offen lds\n"
"s_add_u32 m0, s55, 31744\n"
"s_add_u32 s60, s66, 32768\n"
"buffer_load_dwordx4 v53, s[44:47], s60 offen lds\n"
"s_add_u32 m0, s55, 32768\n"
"s_add_u32 s60, s67, 0\n"
"buffer_load_dwordx4 v53, s[44:47], s60 offen lds\n"
"s_add_u32 m0, s55, 33792\n"
"s_add_u32 s60, s67, 32768\n"
"buffer_load_dwordx4 v53, s[44:47], s60 offen lds\n"
"s_add_u32 m0, s55, 34816\n"
"s_add_u32 s60, s68, 0\n"
"buffer_load_dwordx4 v53, s[44:47], s60 offen lds\n"
"s_add_u32 m0, s55, 35840\n"
"s_add_u32 s60, s68, 32768\n"
"buffer_load_dwordx4 v53, s[44:47], s60 offen lds\n"
"s_add_u32 m0, s55, 36864\n"
"s_add_u32 s60, s69, 0\n"
"buffer_load_dwordx4 v53, s[44:47], s60 offen lds\n"
"s_add_u32 m0, s55, 37888\n"
"s_add_u32 s60, s69, 32768\n"
"buffer_load_dwordx4 v53, s[44:47], s60 offen lds\n"
"s_add_u32 m0, s55, 38912\n"
"s_add_u32 s60, s70, 0\n"
"buffer_load_dwordx4 v53, s[44:47], s60 offen lds\n"
"s_add_u32 m0, s55, 39936\n"
"s_add_u32 s60, s70, 32768\n"
"buffer_load_dwordx4 v53, s[44:47], s60 offen lds\n"
"s_waitcnt vmcnt(38)\n"
"ds_read_b64_tr_b16 a[0:1], v48 offset:0\n"
"ds_read_b64_tr_b16 a[2:3], v48 offset:256\n"
"ds_read_b64_tr_b16 a[28:29], v49 offset:0\n"
"ds_read_b64_tr_b16 a[30:31], v49 offset:256\n"
"v_add_u32 v0, 2, v0\n"
"v_add_u32 v1, 0x9e3779b9, v1\n"
"v_or_b32 v0, 1, v0\n"
"v_readfirstlane_b32 s49, v1\n"
"v_readfirstlane_b32 s48, v0\n"
"s_waitcnt vmcnt(36)\n"
"ds_read_b64_tr_b16 a[56:57], v48 offset:2048\n"
"ds_read_b64_tr_b16 a[58:59], v48 offset:2304\n"
"ds_read_b64_tr_b16 a[84:85], v49 offset:2048\n"
"ds_read_b64_tr_b16 a[86:87], v49 offset:2304\n"
"s_waitcnt lgkmcnt(4)\n"
"s_add_u32 m0, s55, 0\n"
"s_add_u32 s60, s66, 0\n"
"buffer_load_dwordx4 v54, s[44:47], s60 offen lds\n"
"s_add_u32 m0, s55, 1024\n"
"s_add_u32 s60, s66, 32768\n"
"buffer_load_dwordx4 v54, s[44:47], s60 offen lds\n"
"s_waitcnt vmcnt(36)\n"
"ds_read_b64_tr_b16 a[112:113], v48 offset:4096\n"
"ds_read_b64_tr_b16 a[114:115], v48 offset:4352\n"
"ds_read_b64_tr_b16 a[140:141], v49 offset:4096\n"
"ds_read_b64_tr_b16 a[142:143], v49 offset:4352\n"
"s_waitcnt lgkmcnt(4)\n"
"s_add_u32 m0, s55, 2048\n"
"s_add_u32 s60, s67, 0\n"
"buffer_load_dwordx4 v54, s[44:47], s60 offen lds\n"
"s_add_u32 m0, s55, 3072\n"
"s_add_u32 s60, s67, 32768\n"
"buffer_load_dwordx4 v54, s[44:47], s60 offen lds\n"
"s_waitcnt vmcnt(36)\n"
"ds_read_b64_tr_b16 a[168:169], v48 offset:6144\n"
"ds_read_b64_tr_b16 a[170:171], v48 offset:6400\n"
"ds_read_b64_tr_b16 a[196:197], v49 offset:6144\n"
"ds_read_b64_tr_b16 a[198:199], v49 offset:6400\n"
"s_waitcnt lgkmcnt(4)\n"
"s_add_u32 m0, s55, 4096\n"
"s_add_u32 s60, s68, 0\n"
"buffer_load_dwordx4 v54, s[44:47], s60 offen lds\n"
"s_add_u32 m0, s55, 5120\n"
"s_add_u32 s60, s68, 32768\n"
"buffer_load_dwordx4 v54, s[44:47], s60 offen lds\n"
"s_waitcnt vmcnt(36)\n"
"ds_read_b64_tr_b16 a[224:225], v48 offset:8192\n"
"ds_read_b64_tr_b16 a[226:227], v48 offset:8448\n"
"ds_read_b64_tr_b16 a[252:253], v49 offset:8192\n"
"ds_read_b64_tr_b16 a[254:255], v49 offset:8448\n"
"s_waitcnt lgkmcnt(4)\n"
"s_add_u32 m0, s55, 6144\n"
"s_add_u32 s60, s69, 0\n"
"buffer_load_dwordx4 v54, s[44:47], s60 offen lds\n"
"s_add_u32 m0, s55, 7168\n"
"s_add_u32 s60, s69, 32768\n"
"buffer_load_dwordx4 v54, s[44:47], s60 offen lds\n"
"s_waitcnt vmcnt(36)\n"
"ds_read_b64_tr_b16 a[4:5], v48 offset:10240\n"
"ds_read_b64_tr_b16 a[6:7], v48 offset:10496\n"
"ds_read_b64_tr_b16 a[32:33], v49 offset:10240\n"
"ds_read_b64_tr_b16 a[34:35], v49 offset:10496\n"
"s_waitcnt lgkmcnt(4)\n"
"s_add_u32 m0, s55, 8192\n"
"s_add_u32 s60, s70, 0\n"
"buffer_load_dwordx4 v54, s[44:47], s60 offen lds\n"
"s_add_u32 m0, s55, 9216\n"
"s_add_u32 s60, s70, 32768\n"
"buffer_load_dwordx4 v54, s[44:47], s60 offen lds\n"
"s_waitcnt vmcnt(36)\n"
"ds_read_b64_tr_b16 a[60:61], v48 offset:12288\n"
"ds_read_b64_tr_b16 a[62:63], v48 offset:12544\n"
"ds_read_b64_tr_b16 a[88:89], v49 offset:12288\n"
"ds_read_b64_tr_b16 a[90:91], v49 offset:12544\n"
"s_waitcnt lgkmcnt(4)\n"
"s_add_u32 m0, s55, 10240\n"
"s_add_u32 s60, s66, 0\n"
"buffer_load_dwordx4 v55, s[44:47], s60 offen lds\n"
"s_add_u32 m0, s55, 11264\n"
"s_add_u32 s60, s66, 32768\n"
"buffer_load_dwordx4 v55, s[44:47], s60 offen lds\n"
"s_waitcnt vmcnt(36)\n"
"ds_read_b64_tr_b16 a[116:117], v48 offset:14336\n"
"ds_read_b64_tr_b16 a[118:119], v48 offset:14592\n"
"ds_read_b64_tr_b16 a[144:145], v49 offset:14336\n"
"ds_read_b64_tr_b16 a[146:147], v49 offset:14592\n"
"s_waitcnt lgkmcnt(4)\n"
"s_add_u32 m0, s55, 12288\n"
"s_add_u32 s60, s67, 0\n"
"buffer_load_dwordx4 v55, s[44:47], s60 offen lds\n"
"s_add_u32 m0, s55, 13312\n"
"s_add_u32 s60, s67, 32768\n"
"buffer_load_dwordx4 v55, s[44:47], s60 offen lds\n"
"s_waitcnt vmcnt(36)\n"
"ds_read_b64_tr_b16 a[172:173], v48 offset:16384\n"
"ds_read_b64_tr_b16 a[174:175], v48 offset:16640\n"
"ds_read_b64_tr_b16 a[200:201], v49 offset:16384\n"
"ds_read_b64_tr_b16 a[202:203], v49 offset:16640\n"
"s_waitcnt lgkmcnt(4)\n"
"s_add_u32 m0, s55, 14336\n"
"s_add_u32 s60, s68, 0\n"
"buffer_load_dwordx4 v55, s[44:47], s60 offen lds\n"
"s_add_u32 m0, s55, 15360\n"
"s_add_u32 s60, s68, 32768\n"
"buffer_load_dwordx4 v55, s[44:47], s60 offen lds\n"
"s_waitcnt vmcnt(36)\n"
"ds_read_b64_tr_b16 a[228:229], v48 offset:18432\n"
"ds_read_b64_tr_b16 a[230:231], v48 offset:18688\n"
"ds_read_b64_tr_b16 v[24:25], v49 offset:18432\n"
"ds_read_b64_tr_b16 v[26:27], v49 offset:18688\n"
"s_waitcnt lgkmcnt(4)\n"
"s_add_u32 m0, s55, 16384\n"
"s_add_u32 s60, s69, 0\n"
"buffer_load_dwordx4 v55, s[44:47], s60 offen lds\n"
"s_add_u32 m0, s55, 17408\n"
"s_add_u32 s60, s69, 32768\n"
"buffer_load_dwordx4 v55, s[44:47], s60 offen lds\n"
"s_waitcnt vmcnt(36)\n"
"ds_read_b64_tr_b16 a[8:9], v48 offset:20480\n"
"ds_read_b64_tr_b16 a[10:11], v48 offset:20736\n"
"ds_read_b64_tr_b16 a[36:37], v49 offset:20480\n"
"ds_read_b64_tr_b16 a[38:39], v49 offset:20736\n"
"s_waitcnt lgkmcnt(4)\n"
"s_add_u32 m0, s55, 18432\n"
"s_add_u32 s60, s70, 0\n"
"buffer_load_dwordx4 v55, s[44:47], s60 offen lds\n"
"s_add_u32 m0, s55, 19456\n"
"s_add_u32 s60, s70, 32768\n"
"buffer_load_dwordx4 v55, s[44:47], s60 offen lds\n"
"s_waitcnt vmcnt(36)\n"
"ds_read_b64_tr_b16 a[64:65], v48 offset:22528\n"
"ds_read_b64_tr_b16 a[66:67], v48 offset:22784\n"
"ds_read_b64_tr_b16 a[92:93], v49 offset:22528\n"
"ds_read_b64_tr_b16 a[94:95], v49 offset:22784\n"
"s_waitcnt lgkmcnt(4)\n"
"s_add_u32 m0, s55, 20480\n"
"s_add_u32 s60, s66, 0\n"
"buffer_load_dwordx4 v56, s[44:47], s60 offen lds\n"
"s_add_u32 m0, s55, 21504\n"
"s_add_u32 s60, s66, 32768\n"
"buffer_load_dwordx4 v56, s[44:47], s60 offen lds\n"
"s_waitcnt vmcnt(36)\n"
"ds_read_b64_tr_b16 a[120:121], v48 offset:24576\n"
"ds_read_b64_tr_b16 a[122:123], v48 offset:24832\n"
"ds_read_b64_tr_b16 a[148:149], v49 offset:24576\n"
"ds_read_b64_tr_b16 a[150:151], v49 offset:24832\n"
"s_waitcnt lgkmcnt(4)\n"
"s_add_u32 m0, s55, 22528\n"
"s_add_u32 s60, s67, 0\n"
"buffer_load_dwordx4 v56, s[44:47], s60 offen lds\n"
"s_add_u32 m0, s55, 23552\n"
"s_add_u32 s60, s67, 32768\n"
"buffer_load_dwordx4 v56, s[44:47], s60 offen lds\n"
"s_waitcnt vmcnt(36)\n"
"ds_read_b64_tr_b16 a[176:177], v48 offset:26624\n"
"ds_read_b64_tr_b16 a[178:179], v48 offset:26880\n"
"ds_read_b64_tr_b16 a[204:205], v49 offset:26624\n"
"ds_read_b64_tr_b16 a[206:207], v49 offset:26880\n"
"s_waitcnt lgkmcnt(4)\n"
"s_add_u32 m0, s55, 24576\n"
"s_add_u32 s60, s68, 0\n"
"buffer_load_dwordx4 v56, s[44:47], s60 offen lds\n"
"s_add_u32 m0, s55, 25600\n"
"s_add_u32 s60, s68, 32768\n"
"buffer_load_dwordx4 v56, s[44:47], s60 offen lds\n"
"s_waitcnt vmcnt(36)\n"
"ds_read_b64_tr_b16 a[232:233], v48 offset:28672\n"
"ds_read_b64_tr_b16 a[234:235], v48 offset:28928\n"
"ds_read_b64_tr_b16 v[28:29], v49 offset:28672\n"
"ds_read_b64_tr_b16 v[30:31], v49 offset:28928\n"
"s_waitcnt lgkmcnt(4)\n"
"s_add_u32 m0, s55, 26624\n"
"s_add_u32 s60, s69, 0\n"
"buffer_load_dwordx4 v56, s[44:47], s60 offen lds\n"
"s_add_u32 m0, s55, 27648\n"
"s_add_u32 s60, s69, 32768\n"
"buffer_load_dwordx4 v56, s[44:47], s60 offen lds\n"
"s_waitcnt vmcnt(36)\n"
"ds_read_b64_tr_b16 a[12:13], v48 offset:30720\n"
"ds_read_b64_tr_b16 a[14:15], v48 offset:30976\n"
"ds_read_b64_tr_b16 a[40:41], v49 offset:30720\n"
"ds_read_b64_tr_b16 a[42:43], v49 offset:30976\n"
"s_waitcnt lgkmcnt(4)\n"
"s_add_u32 m0, s55, 28672\n"
"s_add_u32 s60, s70, 0\n"
"buffer_load_dwordx4 v56, s[44:47], s60 offen lds\n"
"s_add_u32 m0, s55, 29696\n"
"s_add_u32 s60, s70, 32768\n"
"buffer_load_dwordx4 v56, s[44:47], s60 offen lds\n"
"s_waitcnt vmcnt(36)\n"
"ds_read_b64_tr_b16 a[68:69], v48 offset:32768\n"
"ds_read_b64_tr_b16 a[70:71], v48 offset:33024\n"
"ds_read_b64_tr_b16 a[96:97], v49 offset:32768\n"
"ds_read_b64_tr_b16 a[98:99], v49 offset:33024\n"
"s_waitcnt lgkmcnt(4)\n"
"s_waitcnt vmcnt(34)\n"
"ds_read_b64_tr_b16 a[124:125], v48 offset:34816\n"
"ds_read_b64_tr_b16 a[126:127], v48 offset:35072\n"
"ds_read_b64_tr_b16 a[152:153], v49 offset:34816\n"
"ds_read_b64_tr_b16 a[154:155], v49 offset:35072\n"
"s_waitcnt lgkmcnt(4)\n"
"s_waitcnt vmcnt(32)\n"
"ds_read_b64_tr_b16 a[180:181], v48 offset:36864\n"
"ds_read_b64_tr_b16 a[182:183], v48 offset:37120\n"
"ds_read_b64_tr_b16 a[208:209], v49 offset:36864\n"
"ds_read_b64_tr_b16 a[210:211], v49 offset:37120\n"
"s_waitcnt lgkmcnt(4)\n"
"s_waitcnt vmcnt(30)\n"
"ds_read_b64_tr_b16 a[236:237], v48 offset:38912\n"
"ds_read_b64_tr_b16 a[238:239], v48 offset:39168\n"
"ds_read_b64_tr_b16 v[32:33], v49 offset:38912\n"
"ds_read_b64_tr_b16 v[34:35], v49 offset:39168\n"
"s_waitcnt lgkmcnt(4)\n"
"s_waitcnt vmcnt(28)\n"
"ds_read_b64_tr_b16 a[16:17], v48 offset:0\n"
"ds_read_b64_tr_b16 a[18:19], v48 offset:256\n"
"ds_read_b64_tr_b16 a[44:45], v49 offset:0\n"
"ds_read_b64_tr_b16 a[46:47], v49 offset:256\n"
"s_waitcnt lgkmcnt(4)\n"
"s_waitcnt vmcnt(26)\n"
"ds_read_b64_tr_b16 a[72:73], v48 offset:2048\n"
"ds_read_b64_tr_b16 a[74:75], v48 offset:2304\n"
"ds_read_b64_tr_b16 a[100:101], v49 offset:2048\n"
"ds_read_b64_tr_b16 a[102:103], v49 offset:2304\n"
"s_waitcnt lgkmcnt(4)\n"
"s_waitcnt vmcnt(24)\n"
"ds_read_b64_tr_b16 a[128:129], v48 offset:4096\n"
"ds_read_b64_tr_b16 a[130:131], v48 offset:4352\n"
"ds_read_b64_tr_b16 a[156:157], v49 offset:4096\n"
"ds_read_b64_tr_b16 a[158:159], v49 offset:4352\n"
"s_waitcnt lgkmcnt(4)\n"
"s_waitcnt vmcnt(22)\n"
"ds_read_b64_tr_b16 a[184:185], v48 offset:6144\n"
"ds_read_b64_tr_b16 a[186:187], v48 offset:6400\n"
"ds_read_b64_tr_b16 a[212:213], v49 offset:6144\n"
"ds_read_b64_tr_b16 a[214:215], v49 offset:6400\n"
"s_waitcnt lgkmcnt(4)\n"
"s_waitcnt vmcnt(20)\n"
"ds_read_b64_tr_b16 a[240:241], v48 offset:8192\n"
"ds_read_b64_tr_b16 a[242:243], v48 offset:8448\n"
"ds_read_b64_tr_b16 v[36:37], v49 offset:8192\n"
"ds_read_b64_tr_b16 v[38:39], v49 offset:8448\n"
"s_waitcnt lgkmcnt(4)\n"
"s_waitcnt vmcnt(18)\n"
"ds_read_b64_tr_b16 a[20:21], v48 offset:10240\n"
"ds_read_b64_tr_b16 a[22:23], v48 offset:10496\n"
"ds_read_b64_tr_b16 a[48:49], v49 offset:10240\n"
"ds_read_b64_tr_b16 a[50:51], v49 offset:10496\n"
"s_waitcnt lgkmcnt(4)\n"
"s_waitcnt vmcnt(16)\n"
"ds_read_b64_tr_b16 a[76:77], v48 offset:12288\n"
"ds_read_b64_tr_b16 a[78:79], v48 offset:12544\n"
"ds_read_b64_tr_b16 a[104:105], v49 offset:12288\n"
"ds_read_b64_tr_b16 a[106:107], v49 offset:12544\n"
"s_waitcnt lgkmcnt(4)\n"
"s_waitcnt vmcnt(14)\n"
"ds_read_b64_tr_b16 a[132:133], v48 offset:14336\n"
"ds_read_b64_tr_b16 a[134:135], v48 offset:14592\n"
"ds_read_b64_tr_b16 a[160:161], v49 offset:14336\n"
"ds_read_b64_tr_b16 a[162:163], v49 offset:14592\n"
"s_waitcnt lgkmcnt(4)\n"
"s_waitcnt vmcnt(12)\n"
"ds_read_b64_tr_b16 a[188:189], v48 offset:16384\n"
"ds_read_b64_tr_b16 a[190:191], v48 offset:16640\n"
"ds_read_b64_tr_b16 a[216:217], v49 offset:16384\n"
"ds_read_b64_tr_b16 a[218:219], v49 offset:16640\n"
"s_waitcnt lgkmcnt(4)\n"
"s_waitcnt vmcnt(10)\n"
"ds_read_b64_tr_b16 a[244:245], v48 offset:18432\n"
"ds_read_b64_tr_b16 a[246:247], v48 offset:18688\n"
"ds_read_b64_tr_b16 v[40:41], v49 offset:18432\n"
"ds_read_b64_tr_b16 v[42:43], v49 offset:18688\n"
"s_waitcnt lgkmcnt(4)\n"
"s_waitcnt vmcnt(8)\n"
"ds_read_b64_tr_b16 a[24:25], v48 offset:20480\n"
"ds_read_b64_tr_b16 a[26:27], v48 offset:20736\n"
"ds_read_b64_tr_b16 a[52:53], v49 offset:20480\n"
"ds_read_b64_tr_b16 a[54:55], v49 offset:20736\n"
"s_waitcnt lgkmcnt(4)\n"
"s_waitcnt vmcnt(6)\n"
"ds_read_b64_tr_b16 a[80:81], v48 offset:22528\n"
"ds_read_b64_tr_b16 a[82:83], v48 offset:22784\n"
"ds_read_b64_tr_b16 a[108:109], v49 offset:22528\n"
"ds_read_b64_tr_b16 a[110:111], v49 offset:22784\n"
"s_waitcnt lgkmcnt(4)\n"
"s_waitcnt vmcnt(4)\n"
"ds_read_b64_tr_b16 a[136:137], v48 offset:24576\n"
"ds_read_b64_tr_b16 a[138:139], v48 offset:24832\n"
"ds_read_b64_tr_b16 a[164:165], v49 offset:24576\n"
"ds_read_b64_tr_b16 a[166:167], v49 offset:24832\n"
"s_waitcnt lgkmcnt(4)\n"
"s_waitcnt vmcnt(2)\n"
"ds_read_b64_tr_b16 a[192:193], v48 offset:26624\n"
"ds_read_b64_tr_b16 a[194:195], v48 offset:26880\n"
"ds_read_b64_tr_b16 a[220:221], v49 offset:26624\n"
"ds_read_b64_tr_b16 a[222:223], v49 offset:26880\n"
"s_waitcnt lgkmcnt(4)\n"
"s_waitcnt vmcnt(0)\n"
"ds_read_b64_tr_b16 a[248:249], v48 offset:28672\n"
"ds_read_b64_tr_b16 a[250:251], v48 offset:28928\n"
"ds_read_b64_tr_b16 v[44:45], v49 offset:28672\n"
"ds_read_b64_tr_b16 v[46:47], v49 offset:28928\n"
"s_waitcnt lgkmcnt(4)\n"
"s_waitcnt vmcnt(0)\n"
"s_waitcnt lgkmcnt(0)\n"
"s_mov_b32 m0, s59\n"
"s_barrier\n"
"s_mov_b64 s[64:65], s[48:49]\n"
"s_mov_b32 s93, 0x80000000\n"
"s_mov_b32 s92, 0x80000000\n"
"s_mov_b32 s95, 0x80000000\n"
"s_mov_b32 s92, 0x80000000\n"
"s_mov_b32 s93, 0x80000000\n"
"s_mov_b32 s95, 0x80000000\n"
"s_mov_b32 s95, 0x80000000\n"
"s_mov_b32 s94, 0x80000000\n"
"s_mov_b32 s94, 0x80000000\n"
"v_mov_b32 v138, 0\n"
"v_mov_b32 v139, 0\n"
"v_mov_b32 v144, 0\n"
"v_mov_b32 v145, 0\n"
"v_mov_b32 v136, 0\n"
"v_mov_b32 v137, 0\n"
"v_mov_b32 v172, 0\n"
"v_mov_b32 v173, 0\n"
"v_mov_b32 v136, 0\n"
"v_mov_b32 v137, 0\n"
"s_cmp_eq_u32 s54, 0\n"
"s_cbranch_scc1 .LH_1s\n"
"s_cmp_eq_u32 s54, 1\n"
"s_cbranch_scc1 .LH_2s\n"
"s_cmp_eq_u32 s54, 2\n"
"s_cbranch_scc1 .LH_3s\n"
"s_cmp_eq_u32 s54, 3\n"
"s_cbranch_scc1 .LH_4s\n"
".p2align 6\n"
".LH_top:\n"
"v_mfma_f32_16x16x32_bf16 v[60:63], v[104:107], a[0:3], 0\n"
"v_mfma_f32_16x16x32_bf16 v[60:63], v[72:75], a[4:7], v[60:63]\n"
"global_load_dwordx4 v[48:51], v7, s[56:57] offset:0 sc1\n"
"v_mfma_f32_16x16x32_bf16 v[60:63], v[76:79], a[8:11], v[60:63]\n"
"v_mfma_f32_16x16x32_bf16 v[60:63], v[80:83], a[12:15], v[60:63]\n"
"s_waitcnt vmcnt(6)\n"
"v_mfma_f32_16x16x32_bf16 v[60:63], v[84:87], a[16:19], v[60:63]\n"
"s_waitcnt lgkmcnt(3)\n"
"v_add_f32 v100, v100, v68\n"
"v_add_f32 v101, v101, v69\n"
"v_mfma_f32_16x16x32_bf16 v[60:63], v[88:91], a[20:23], v[60:63]\n"
"s_waitcnt vmcnt(5)\n"
"v_add_f32 v102, v102, v70\n"
"v_add_f32 v103, v103, v71\n"
"v_mfma_f32_16x16x32_bf16 v[60:63], v[92:95], a[24:27], v[60:63]\n"
"global_load_dwordx4 v[52:55], v8, s[56:57] offset:0 sc1\n"
"v_add_f32 v100, v100, v96\n"
"v_mfma_f32_16x16x32_bf16 v[64:67], v[104:107], a[28:31], 0\n"
"buffer_load_dwordx2 v[136:137], v12, s[40:43], s50 offen sc1\n"
"v_mfma_f32_16x16x32_bf16 v[64:67], v[72:75], a[32:35], v[64:67]\n"
"ds_write_addtid_b32 v60 offset:0\n"
"v_add_f32 v101, v101, v97\n"
"v_mfma_f32_16x16x32_bf16 v[64:67], v[76:79], a[36:39], v[64:67]\n"
"ds_write_addtid_b32 v61 offset:4640\n"
"global_load_dwordx4 v[56:59], v9, s[56:57] offset:0 sc1\n"
"v_mfma_f32_16x16x32_bf16 v[64:67], v[80:83], a[40:43], v[64:67]\n"
"ds_write_addtid_b32 v62 offset:9344\n"
"v_add_f32 v102, v102, v98\n"
"v_mfma_f32_16x16x32_bf16 v[64:67], v[84:87], a[44:47], v[64:67]\n"
"ds_write_addtid_b32 v63 offset:13984\n"
"v_add_f32 v103, v103, v99\n"
"v_mfma_f32_16x16x32_bf16 v[64:67], v[88:91], a[48:51], v[64:67]\n"
"v_add_f32 v100, v100, v116\n"
"v_add_f32 v101, v101, v117\n"
"v_mfma_f32_16x16x32_bf16 v[64:67], v[92:95], a[52:55], v[64:67]\n"
"v_add_f32 v102, v102, v118\n"
"v_add_f32 v103, v103, v119\n"
"v_mfma_f32_16x16x32_bf16 v[68:71], v[104:107], a[56:59], 0\n"
"s_waitcnt vmcnt(7)\n"
"buffer_store_dwordx2 v[0:1], v14, s[40:43], s93 offen offset:0 sc1\n"
"v_mfma_f32_16x16x32_bf16 v[68:71], v[72:75], a[60:63], v[68:71]\n"
"s_add_u32 s95, s50, 0xffd00000\n"
"s_and_b32 s94, s95, 0xfbffffff\n"
"ds_write_addtid_b32 v64 offset:256\n"
"v_mfma_f32_16x16x32_bf16 v[68:71], v[76:79], a[64:67], v[68:71]\n"
"ds_write_addtid_b32 v65 offset:4896\n"
"global_load_dwordx4 v[60:63], v10, s[56:57] offset:0 sc1\n"
"v_mfma_f32_16x16x32_bf16 v[68:71], v[80:83], a[68:71], v[68:71]\n"
"ds_write_addtid_b32 v66 offset:9600\n"
"v_cvt_pk_bf16_f32 v96, v100, v101\n"
"v_mfma_f32_16x16x32_bf16 v[68:71], v[84:87], a[72:75], v[68:71]\n"
"ds_write_addtid_b32 v67 offset:14240\n"
"v_cvt_pk_bf16_f32 v97, v102, v103\n"
"v_mfma_f32_16x16x32_bf16 v[68:71], v[88:91], a[76:79], v[68:71]\n"
"s_waitcnt lgkmcnt(8)\n"
"v_add_f32 v124, v124, v128\n"
"v_add_f32 v125, v125, v129\n"
"v_mfma_f32_16x16x32_bf16 v[68:71], v[92:95], a[80:83], v[68:71]\n"
"v_add_f32 v126, v126, v130\n"
"v_add_f32 v127, v127, v131\n"
"v_mfma_f32_16x16x32_bf16 v[100:103], v[104:107], a[84:87], 0\n"
"global_load_dwordx4 v[64:67], v11, s[56:57] offset:0 sc1\n"
"v_add_f32 v124, v124, v132\n"
"v_mfma_f32_16x16x32_bf16 v[100:103], v[72:75], a[88:91], v[100:103]\n"
"ds_write_addtid_b32 v68 offset:512\n"
"v_add_f32 v125, v125, v133\n"
"v_mfma_f32_16x16x32_bf16 v[100:103], v[76:79], a[92:95], v[100:103]\n"
"ds_write_addtid_b32 v69 offset:5152\n"
"v_add_f32 v126, v126, v134\n"
"v_mfma_f32_16x16x32_bf16 v[100:103], v[80:83], a[96:99], v[100:103]\n"
"ds_write_addtid_b32 v70 offset:9856\n"
"v_add_f32 v127, v127, v135\n"
"v_mfma_f32_16x16x32_bf16 v[100:103], v[84:87], a[100:103], v[100:103]\n"
"ds_write_addtid_b32 v71 offset:14496\n"
"v_add_f32 v124, v124, v120\n"
"v_mfma_f32_16x16x32_bf16 v[100:103], v[88:91], a[104:107], v[100:103]\n"
"v_add_f32 v125, v125, v121\n"
"v_add_f32 v126, v126, v122\n"
"v_mfma_f32_16x16x32_bf16 v[100:103], v[92:95], a[108:111], v[100:103]\n"
"v_add_f32 v127, v127, v123\n"
"v_cvt_pk_bf16_f32 v98, v124, v125\n"
"v_mfma_f32_16x16x32_bf16 v[68:71], v[104:107], a[112:115], 0\n"
"v_cvt_pk_bf16_f32 v99, v126, v127\n"
"v_mfma_f32_16x16x32_bf16 v[68:71], v[72:75], a[116:119], v[68:71]\n"
"ds_write_addtid_b32 v100 offset:768\n"
"v_mfma_f32_16x16x32_bf16 v[68:71], v[76:79], a[120:123], v[68:71]\n"
"ds_write_addtid_b32 v101 offset:5408\n"
"v_mfma_f32_16x16x32_bf16 v[68:71], v[80:83], a[124:127], v[68:71]\n"
"ds_write_addtid_b32 v102 offset:10112\n"
"v_mfma_f32_16x16x32_bf16 v[68:71], v[84:87], a[128:131], v[68:71]\n"
"ds_write_addtid_b32 v103 offset:14752\n"
"v_mfma_f32_16x16x32_bf16 v[68:71], v[88:91], a[132:135], v[68:71]\n"
"v_mfma_f32_16x16x32_bf16 v[68:71], v[92:95], a[136:139], v[68:71]\n"
"v_mfma_f32_16x16x32_bf16 v[116:119], v[104:107], a[140:143], 0\n"
"v_mfma_f32_16x16x32_bf16 v[116:119], v[72:75], a[144:147], v[116:119]\n"
"ds_write_addtid_b32 v68 offset:1024\n"
"v_mfma_f32_16x16x32_bf16 v[116:119], v[76:79], a[148:151], v[116:119]\n"
"ds_write_addtid_b32 v69 offset:5664\n"
"v_mfma_f32_16x16x32_bf16 v[116:119], v[80:83], a[152:155], v[116:119]\n"
"ds_write_addtid_b32 v70 offset:10368\n"
"v_mfma_f32_16x16x32_bf16 v[116:119], v[84:87], a[156:159], v[116:119]\n"
"ds_write_addtid_b32 v71 offset:15008\n"
"v_mfma_f32_16x16x32_bf16 v[116:119], v[88:91], a[160:163], v[116:119]\n"
"buffer_store_dwordx4 v[96:99], v13, s[40:43], s94 offen sc1\n"
"v_mfma_f32_16x16x32_bf16 v[116:119], v[92:95], a[164:167], v[116:119]\n"
"v_mfma_f32_16x16x32_bf16 v[100:103], a[168:171], v[104:107], 0\n"
"v_mfma_f32_16x16x32_bf16 v[100:103], a[172:175], v[72:75], v[100:103]\n"
"ds_write_addtid_b32 v116 offset:1280\n"
"v_mfma_f32_16x16x32_bf16 v[100:103], a[176:179], v[76:79], v[100:103]\n"
"ds_write_addtid_b32 v117 offset:5920\n"
"v_mfma_f32_16x16x32_bf16 v[100:103], a[180:183], v[80:83], v[100:103]\n"
"ds_write_addtid_b32 v118 offset:10624\n"
"v_mfma_f32_16x16x32_bf16 v[100:103], a[184:187], v[84:87], v[100:103]\n"
"ds_write_addtid_b32 v119 offset:15264\n"
"v_mfma_f32_16x16x32_bf16 v[100:103], a[188:191], v[88:91], v[100:103]\n"
"v_mfma_f32_16x16x32_bf16 v[100:103], a[192:195], v[92:95], v[100:103]\n"
"v_mfma_f32_16x16x32_bf16 v[96:99], a[196:199], v[104:107], 0\n"
"v_mfma_f32_16x16x32_bf16 v[96:99], a[200:203], v[72:75], v[96:99]\n"
"v_mfma_f32_16x16x32_bf16 v[96:99], a[204:207], v[76:79], v[96:99]\n"
"v_mfma_f32_16x16x32_bf16 v[96:99], a[208:211], v[80:83], v[96:99]\n"
"s_add_u32 s58, s50, 0x180000\n"
"v_mfma_f32_16x16x32_bf16 v[96:99], a[212:215], v[84:87], v[96:99]\n"
"s_and_b32 s58, s58, 0x3ffffff\n"
"v_mfma_f32_16x16x32_bf16 v[96:99], a[216:219], v[88:91], v[96:99]\n"
"s_add_u32 s56, s40, s58\n"
"v_mfma_f32_16x16x32_bf16 v[96:99], a[220:223], v[92:95], v[96:99]\n"
"s_addc_u32 s57, s41, 0\n"
"v_mfma_f32_16x16x32_bf16 v[116:119], a[224:227], v[104:107], 0\n"
"s_waitcnt vmcnt(5)\n"
"v_cmp_ne_u64 vcc, s[48:49], v[136:137]\n"
"v_mfma_f32_16x16x32_bf16 v[116:119], a[228:231], v[72:75], v[116:119]\n"
"v_mfma_f32_16x16x32_bf16 v[116:119], a[232:235], v[76:79], v[116:119]\n"
"s_cbranch_vccnz .LH_3s\n"
".LH_3r:\n"
"v_mfma_f32_16x16x32_bf16 v[116:119], a[236:239], v[80:83], v[116:119]\n"
"s_add_u32 s50, s50, 0x180000\n"
"global_load_dwordx4 v[68:71], v5, s[56:57] offset:0 sc1\n"
"v_mfma_f32_16x16x32_bf16 v[116:119], a[240:243], v[84:87], v[116:119]\n"
"v_mfma_f32_16x16x32_bf16 v[116:119], a[244:247], v[88:91], v[116:119]\n"
"v_mfma_f32_16x16x32_bf16 v[116:119], a[248:251], v[92:95], v[116:119]\n"
"v_mfma_f32_16x16x32_bf16 v[120:123], a[252:255], v[104:107], 0\n"
"v_mfma_f32_16x16x32_bf16 v[120:123], v[24:27], v[72:75], v[120:123]\n"
"s_waitcnt lgkmcnt(0)\n"
"v_mfma_f32_16x16x32_bf16 v[120:123], v[28:31], v[76:79], v[120:123]\n"
"s_barrier\n"
"v_mfma_f32_16x16x32_bf16 v[120:123], v[32:35], v[80:83], v[120:123]\n"
"ds_read_b128 v[104:107], v2 offset:2048\n"
"ds_read_b128 v[124:127], v3 offset:512\n"
"v_mfma_f32_16x16x32_bf16 v[120:123], v[36:39], v[84:87], v[120:123]\n"
"global_load_dwordx4 v[72:75], v6, s[56:57] offset:0 sc1\n"
"ds_read_b128 v[128:131], v4 offset:3072\n"
"v_mfma_f32_16x16x32_bf16 v[120:123], v[40:43], v[88:91], v[120:123]\n"
"ds_read_b128 v[132:135], v2 offset:2304\n"
"ds_read_b128 v[136:139], v3 offset:768\n"
"v_mfma_f32_16x16x32_bf16 v[120:123], v[44:47], v[92:95], v[120:123]\n"
"global_load_dwordx4 v[76:79], v7, s[56:57] offset:0 sc1\n"
"ds_read_b128 v[140:143], v4 offset:3328\n"
"v_mfma_f32_16x16x32_bf16 v[88:91], v[108:111], a[0:3], 0\n"
"ds_read_b128 v[144:147], v2 offset:3584\n"
"ds_read_b128 v[148:151], v3 offset:1536\n"
"v_mfma_f32_16x16x32_bf16 v[88:91], v[112:115], a[4:7], v[88:91]\n"
"ds_read_b128 v[152:155], v4 offset:3584\n"
"ds_read_b128 v[156:159], v2 offset:3840\n"
"v_mfma_f32_16x16x32_bf16 v[88:91], v[48:51], a[8:11], v[88:91]\n"
"ds_read_b128 v[160:163], v3 offset:1792\n"
"ds_read_b128 v[164:167], v4 offset:3840\n"
"v_mfma_f32_16x16x32_bf16 v[88:91], v[52:55], a[12:15], v[88:91]\n"
"s_waitcnt vmcnt(5)\n"
"v_mfma_f32_16x16x32_bf16 v[88:91], v[56:59], a[16:19], v[88:91]\n"
"s_waitcnt lgkmcnt(9)\n"
"global_load_dwordx4 v[80:83], v8, s[56:57] offset:0 sc1\n"
"v_add_f32 v104, v104, v124\n"
"v_mfma_f32_16x16x32_bf16 v[88:91], v[60:63], a[20:23], v[88:91]\n"
"s_waitcnt vmcnt(5)\n"
"v_add_f32 v105, v105, v125\n"
"v_add_f32 v106, v106, v126\n"
"v_mfma_f32_16x16x32_bf16 v[88:91], v[64:67], a[24:27], v[88:91]\n"
"s_add_u32 s94, s50, 0xff000000\n"
"v_add_f32 v107, v107, v127\n"
"v_add_f32 v104, v104, v128\n"
"v_mfma_f32_16x16x32_bf16 v[168:171], v[108:111], a[28:31], 0\n"
"buffer_load_dwordx2 v[172:173], v12, s[40:43], s50 offen sc1\n"
"v_mfma_f32_16x16x32_bf16 v[168:171], v[112:115], a[32:35], v[168:171]\n"
"ds_write_addtid_b32 v88 offset:2048\n"
"v_add_f32 v105, v105, v129\n"
"v_mfma_f32_16x16x32_bf16 v[168:171], v[48:51], a[36:39], v[168:171]\n"
"ds_write_addtid_b32 v89 offset:6688\n"
"v_add_f32 v106, v106, v130\n"
"v_mfma_f32_16x16x32_bf16 v[168:171], v[52:55], a[40:43], v[168:171]\n"
"ds_write_addtid_b32 v90 offset:11392\n"
"v_add_f32 v107, v107, v131\n"
"v_mfma_f32_16x16x32_bf16 v[168:171], v[56:59], a[44:47], v[168:171]\n"
"ds_write_addtid_b32 v91 offset:16032\n"
"v_add_f32 v104, v104, v100\n"
"v_mfma_f32_16x16x32_bf16 v[168:171], v[60:63], a[48:51], v[168:171]\n"
"global_load_dwordx4 v[84:87], v9, s[56:57] offset:0 sc1\n"
"v_add_f32 v105, v105, v101\n"
"v_mfma_f32_16x16x32_bf16 v[168:171], v[64:67], a[52:55], v[168:171]\n"
"v_add_f32 v106, v106, v102\n"
"v_add_f32 v107, v107, v103\n"
"v_mfma_f32_16x16x32_bf16 v[124:127], v[108:111], a[56:59], 0\n"
"s_add_u32 s93, s50, 0xffd00000\n"
"s_and_b32 s92, s93, 0xfbffffff\n"
"v_cvt_pk_bf16_f32 v128, v104, v105\n"
"v_mfma_f32_16x16x32_bf16 v[124:127], v[112:115], a[60:63], v[124:127]\n"
"ds_write_addtid_b32 v168 offset:2304\n"
"v_cvt_pk_bf16_f32 v129, v106, v107\n"
"v_mfma_f32_16x16x32_bf16 v[124:127], v[48:51], a[64:67], v[124:127]\n"
"s_waitcnt lgkmcnt(11)\n"
"ds_write_addtid_b32 v169 offset:6944\n"
"v_add_f32 v132, v132, v136\n"
"v_mfma_f32_16x16x32_bf16 v[124:127], v[52:55], a[68:71], v[124:127]\n"
"ds_write_addtid_b32 v170 offset:11648\n"
"v_add_f32 v133, v133, v137\n"
"v_mfma_f32_16x16x32_bf16 v[124:127], v[56:59], a[72:75], v[124:127]\n"
"s_waitcnt vmcnt(6)\n"
"ds_write_addtid_b32 v171 offset:16288\n"
"global_load_dwordx4 v[88:91], v10, s[56:57] offset:0 sc1\n"
"v_mfma_f32_16x16x32_bf16 v[124:127], v[60:63], a[76:79], v[124:127]\n"
"global_load_dwordx4 v[92:95], v11, s[56:57] offset:0 sc1\n"
"v_add_f32 v134, v134, v138\n"
"v_mfma_f32_16x16x32_bf16 v[124:127], v[64:67], a[80:83], v[124:127]\n"
"v_add_f32 v135, v135, v139\n"
"v_add_f32 v132, v132, v140\n"
"v_mfma_f32_16x16x32_bf16 v[100:103], v[108:111], a[84:87], 0\n"
"v_add_f32 v133, v133, v141\n"
"v_add_f32 v134, v134, v142\n"
"v_mfma_f32_16x16x32_bf16 v[100:103], v[112:115], a[88:91], v[100:103]\n"
"ds_write_addtid_b32 v124 offset:2560\n"
"v_add_f32 v135, v135, v143\n"
"v_mfma_f32_16x16x32_bf16 v[100:103], v[48:51], a[92:95], v[100:103]\n"
"ds_write_addtid_b32 v125 offset:7200\n"
"v_add_f32 v132, v132, v96\n"
"v_mfma_f32_16x16x32_bf16 v[100:103], v[52:55], a[96:99], v[100:103]\n"
"ds_write_addtid_b32 v126 offset:11904\n"
"v_add_f32 v133, v133, v97\n"
"v_mfma_f32_16x16x32_bf16 v[100:103], v[56:59], a[100:103], v[100:103]\n"
"ds_write_addtid_b32 v127 offset:16544\n"
"v_add_f32 v134, v134, v98\n"
"v_mfma_f32_16x16x32_bf16 v[100:103], v[60:63], a[104:107], v[100:103]\n"
"v_add_f32 v135, v135, v99\n"
"v_cvt_pk_bf16_f32 v130, v132, v133\n"
"v_mfma_f32_16x16x32_bf16 v[100:103], v[64:67], a[108:111], v[100:103]\n"
"buffer_store_dwordx2 v[0:1], v14, s[40:43], s95 offen offset:0 sc1\n"
"v_cvt_pk_bf16_f32 v131, v134, v135\n"
"v_mfma_f32_16x16x32_bf16 v[96:99], v[108:111], a[112:115], 0\n"
"buffer_store_dwordx4 v[128:131], v13, s[40:43], s92 offen sc1\n"
"v_mfma_f32_16x16x32_bf16 v[96:99], v[112:115], a[116:119], v[96:99]\n"
"s_waitcnt lgkmcnt(15)\n"
"ds_write_addtid_b32 v100 offset:2816\n"
"v_add_f32 v144, v144, v148\n"
"v_mfma_f32_16x16x32_bf16 v[96:99], v[48:51], a[120:123], v[96:99]\n"
"ds_write_addtid_b32 v101 offset:7456\n"
"v_add_f32 v145, v145, v149\n"
"v_mfma_f32_16x16x32_bf16 v[96:99], v[52:55], a[124:127], v[96:99]\n"
"ds_write_addtid_b32 v102 offset:12160\n"
"v_add_f32 v146, v146, v150\n"
"v_mfma_f32_16x16x32_bf16 v[96:99], v[56:59], a[128:131], v[96:99]\n"
"ds_write_addtid_b32 v103 offset:16800\n"
"v_add_f32 v147, v147, v151\n"
"v_mfma_f32_16x16x32_bf16 v[96:99], v[60:63], a[132:135], v[96:99]\n"
"v_add_f32 v144, v144, v152\n"
"v_add_f32 v145, v145, v153\n"
"v_mfma_f32_16x16x32_bf16 v[96:99], v[64:67], a[136:139], v[96:99]\n"
"v_add_f32 v146, v146, v154\n"
"v_add_f32 v147, v147, v155\n"
"v_mfma_f32_16x16x32_bf16 v[100:103], v[108:111], a[140:143], 0\n"
"v_add_f32 v144, v144, v116\n"
"v_add_f32 v145, v145, v117\n"
"v_mfma_f32_16x16x32_bf16 v[100:103], v[112:115], a[144:147], v[100:103]\n"
"ds_write_addtid_b32 v96 offset:3072\n"
"v_add_f32 v146, v146, v118\n"
"v_mfma_f32_16x16x32_bf16 v[100:103], v[48:51], a[148:151], v[100:103]\n"
"ds_write_addtid_b32 v97 offset:7712\n"
"v_add_f32 v147, v147, v119\n"
"v_mfma_f32_16x16x32_bf16 v[100:103], v[52:55], a[152:155], v[100:103]\n"
"ds_write_addtid_b32 v98 offset:12416\n"
"v_cvt_pk_bf16_f32 v104, v144, v145\n"
"v_mfma_f32_16x16x32_bf16 v[100:103], v[56:59], a[156:159], v[100:103]\n"
"ds_write_addtid_b32 v99 offset:17056\n"
"v_cvt_pk_bf16_f32 v105, v146, v147\n"
"v_mfma_f32_16x16x32_bf16 v[100:103], v[60:63], a[160:163], v[100:103]\n"
"s_waitcnt lgkmcnt(15)\n"
"v_add_f32 v156, v156, v160\n"
"v_add_f32 v157, v157, v161\n"
"v_mfma_f32_16x16x32_bf16 v[100:103], v[64:67], a[164:167], v[100:103]\n"
"v_add_f32 v158, v158, v162\n"
"v_add_f32 v159, v159, v163\n"
"v_mfma_f32_16x16x32_bf16 v[96:99], v[108:111], a[224:227], 0\n"
"v_add_f32 v156, v156, v164\n"
"v_add_f32 v157, v157, v165\n"
"v_mfma_f32_16x16x32_bf16 v[96:99], v[112:115], a[228:231], v[96:99]\n"
"ds_write_addtid_b32 v100 offset:3328\n"
"v_add_f32 v158, v158, v166\n"
"v_mfma_f32_16x16x32_bf16 v[96:99], v[48:51], a[232:235], v[96:99]\n"
"ds_write_addtid_b32 v101 offset:7968\n"
"v_add_f32 v159, v159, v167\n"
"v_mfma_f32_16x16x32_bf16 v[96:99], v[52:55], a[236:239], v[96:99]\n"
"ds_write_addtid_b32 v102 offset:12672\n"
"v_add_f32 v156, v156, v120\n"
"v_mfma_f32_16x16x32_bf16 v[96:99], v[56:59], a[240:243], v[96:99]\n"
"ds_write_addtid_b32 v103 offset:17312\n"
"v_add_f32 v157, v157, v121\n"
"v_mfma_f32_16x16x32_bf16 v[96:99], v[60:63], a[244:247], v[96:99]\n"
"v_add_f32 v158, v158, v122\n"
"v_add_f32 v159, v159, v123\n"
"v_mfma_f32_16x16x32_bf16 v[96:99], v[64:67], a[248:251], v[96:99]\n"
"v_cvt_pk_bf16_f32 v106, v156, v157\n"
"v_cvt_pk_bf16_f32 v107, v158, v159\n"
"v_mfma_f32_16x16x32_bf16 v[100:103], v[108:111], a[252:255], 0\n"
"buffer_store_dwordx4 v[104:107], v15, s[40:43], s92 offen sc1\n"
"v_mfma_f32_16x16x32_bf16 v[100:103], v[112:115], v[24:27], v[100:103]\n"
"s_cmp_lt_u32 s94, 0x8000000\n"
"s_cselect_b32 s94, 0x80000000, s94\n"
"ds_write_addtid_b32 v96 offset:3584\n"
"v_mfma_f32_16x16x32_bf16 v[100:103], v[48:51], v[28:31], v[100:103]\n"
"ds_write_addtid_b32 v97 offset:8224\n"
"v_mfma_f32_16x16x32_bf16 v[100:103], v[52:55], v[32:35], v[100:103]\n"
"ds_write_addtid_b32 v98 offset:12928\n"
"v_mfma_f32_16x16x32_bf16 v[100:103], v[56:59], v[36:39], v[100:103]\n"
"ds_write_addtid_b32 v99 offset:17568\n"
"v_mfma_f32_16x16x32_bf16 v[100:103], v[60:63], v[40:43], v[100:103]\n"
"buffer_load_dwordx2 v[136:137], v17, s[40:43], s94 offen offset:0 sc1\n"
"v_mfma_f32_16x16x32_bf16 v[100:103], v[64:67], v[44:47], v[100:103]\n"
"s_add_u32 s58, s50, 0x180000\n"
"v_mfma_f32_16x16x32_bf16 v[104:107], a[168:171], v[108:111], 0\n"
"s_and_b32 s58, s58, 0x3ffffff\n"
"v_mfma_f32_16x16x32_bf16 v[104:107], a[172:175], v[112:115], v[104:107]\n"
"s_add_u32 s56, s40, s58\n"
"ds_write_addtid_b32 v100 offset:3840\n"
"v_mfma_f32_16x16x32_bf16 v[104:107], a[176:179], v[48:51], v[104:107]\n"
"s_addc_u32 s57, s41, 0\n"
"ds_write_addtid_b32 v101 offset:8480\n"
"v_mfma_f32_16x16x32_bf16 v[104:107], a[180:183], v[52:55], v[104:107]\n"
"s_waitcnt vmcnt(7)\n"
"ds_write_addtid_b32 v102 offset:13184\n"
"v_cmp_ne_u64 vcc, s[48:49], v[172:173]\n"
"v_mfma_f32_16x16x32_bf16 v[104:107], a[184:187], v[56:59], v[104:107]\n"
"ds_write_addtid_b32 v103 offset:17824\n"
"v_mfma_f32_16x16x32_bf16 v[104:107], a[188:191], v[60:63], v[104:107]\n"
"s_cbranch_vccnz .LH_4s\n"
".LH_4r:\n"
"v_mfma_f32_16x16x32_bf16 v[104:107], a[192:195], v[64:67], v[104:107]\n"
"s_add_u32 s50, s50, 0x180000\n"
"global_load_dwordx4 v[96:99], v5, s[56:57] offset:0 sc1\n"
"v_mfma_f32_16x16x32_bf16 v[116:119], a[196:199], v[108:111], 0\n"
"global_load_dwordx4 v[100:103], v6, s[56:57] offset:0 sc1\n"
"v_mfma_f32_16x16x32_bf16 v[116:119], a[200:203], v[112:115], v[116:119]\n"
"s_waitcnt lgkmcnt(0)\n"
"v_mfma_f32_16x16x32_bf16 v[116:119], a[204:207], v[48:51], v[116:119]\n"
"s_barrier\n"
"v_mfma_f32_16x16x32_bf16 v[116:119], a[208:211], v[52:55], v[116:119]\n"
"ds_read_b128 v[108:111], v2 offset:0\n"
"ds_read_b128 v[112:115], v3 offset:2560\n"
"v_mfma_f32_16x16x32_bf16 v[116:119], a[212:215], v[56:59], v[116:119]\n"
"ds_read_b128 v[120:123], v4 offset:1024\n"
"ds_read_b128 v[124:127], v2 offset:256\n"
"v_mfma_f32_16x16x32_bf16 v[116:119], a[216:219], v[60:63], v[116:119]\n"
"ds_read_b128 v[128:131], v3 offset:2816\n"
"ds_read_b128 v[132:135], v4 offset:1280\n"
"v_mfma_f32_16x16x32_bf16 v[116:119], a[220:223], v[64:67], v[116:119]\n"
"v_mfma_f32_16x16x32_bf16 v[56:59], v[68:71], a[0:3], 0\n"
"v_mfma_f32_16x16x32_bf16 v[56:59], v[72:75], a[4:7], v[56:59]\n"
"v_mfma_f32_16x16x32_bf16 v[56:59], v[76:79], a[8:11], v[56:59]\n"
"global_load_dwordx4 v[48:51], v7, s[56:57] offset:0 sc1\n"
"v_mfma_f32_16x16x32_bf16 v[56:59], v[80:83], a[12:15], v[56:59]\n"
"s_waitcnt vmcnt(8)\n"
"buffer_load_dwordx2 v[138:139], v12, s[40:43], s50 offen sc1\n"
"v_mfma_f32_16x16x32_bf16 v[56:59], v[84:87], a[16:19], v[56:59]\n"
"s_waitcnt lgkmcnt(3)\n"
"v_add_f32 v108, v108, v112\n"
"v_add_f32 v109, v109, v113\n"
"v_mfma_f32_16x16x32_bf16 v[56:59], v[88:91], a[20:23], v[56:59]\n"
"s_waitcnt vmcnt(8)\n"
"v_add_f32 v110, v110, v114\n"
"v_add_f32 v111, v111, v115\n"
"v_mfma_f32_16x16x32_bf16 v[56:59], v[92:95], a[24:27], v[56:59]\n"
"v_add_f32 v108, v108, v120\n"
"v_add_f32 v109, v109, v121\n"
"v_mfma_f32_16x16x32_bf16 v[64:67], v[68:71], a[28:31], 0\n"
"global_load_dwordx4 v[52:55], v8, s[56:57] offset:0 sc1\n"
"v_add_f32 v110, v110, v122\n"
"v_mfma_f32_16x16x32_bf16 v[64:67], v[72:75], a[32:35], v[64:67]\n"
"ds_write_addtid_b32 v56 offset:0\n"
"v_add_f32 v111, v111, v123\n"
"v_mfma_f32_16x16x32_bf16 v[64:67], v[76:79], a[36:39], v[64:67]\n"
"ds_write_addtid_b32 v57 offset:4640\n"
"v_add_f32 v108, v108, v104\n"
"v_mfma_f32_16x16x32_bf16 v[64:67], v[80:83], a[40:43], v[64:67]\n"
"ds_write_addtid_b32 v58 offset:9344\n"
"v_add_f32 v109, v109, v105\n"
"v_mfma_f32_16x16x32_bf16 v[64:67], v[84:87], a[44:47], v[64:67]\n"
"ds_write_addtid_b32 v59 offset:13984\n"
"v_add_f32 v110, v110, v106\n"
"v_mfma_f32_16x16x32_bf16 v[64:67], v[88:91], a[48:51], v[64:67]\n"
"v_add_f32 v111, v111, v107\n"
"v_mfma_f32_16x16x32_bf16 v[64:67], v[92:95], a[52:55], v[64:67]\n"
"s_add_u32 s95, s50, 0xffd00000\n"
"s_and_b32 s92, s95, 0xfbffffff\n"
"global_load_dwordx4 v[56:59], v9, s[56:57] offset:0 sc1\n"
"v_mfma_f32_16x16x32_bf16 v[104:107], v[68:71], a[56:59], 0\n"
"v_cvt_pk_bf16_f32 v112, v108, v109\n"
"v_cvt_pk_bf16_f32 v113, v110, v111\n"
"v_mfma_f32_16x16x32_bf16 v[104:107], v[72:75], a[60:63], v[104:107]\n"
"s_waitcnt lgkmcnt(4)\n"
"ds_write_addtid_b32 v64 offset:256\n"
"v_add_f32 v124, v124, v128\n"
"v_mfma_f32_16x16x32_bf16 v[104:107], v[76:79], a[64:67], v[104:107]\n"
"ds_write_addtid_b32 v65 offset:4896\n"
"v_add_f32 v125, v125, v129\n"
"v_mfma_f32_16x16x32_bf16 v[104:107], v[80:83], a[68:71], v[104:107]\n"
"ds_write_addtid_b32 v66 offset:9600\n"
"global_load_dwordx4 v[60:63], v10, s[56:57] offset:0 sc1\n"
"v_mfma_f32_16x16x32_bf16 v[104:107], v[84:87], a[72:75], v[104:107]\n"
"ds_write_addtid_b32 v67 offset:14240\n"
"v_add_f32 v126, v126, v130\n"
"v_mfma_f32_16x16x32_bf16 v[104:107], v[88:91], a[76:79], v[104:107]\n"
"v_add_f32 v127, v127, v131\n"
"v_add_f32 v124, v124, v132\n"
"v_mfma_f32_16x16x32_bf16 v[104:107], v[92:95], a[80:83], v[104:107]\n"
"v_add_f32 v125, v125, v133\n"
"v_add_f32 v126, v126, v134\n"
"v_mfma_f32_16x16x32_bf16 v[108:111], v[68:71], a[84:87], 0\n"
"v_add_f32 v127, v127, v135\n"
"v_add_f32 v124, v124, v116\n"
"v_mfma_f32_16x16x32_bf16 v[108:111], v[72:75], a[88:91], v[108:111]\n"
"ds_write_addtid_b32 v104 offset:512\n"
"v_add_f32 v125, v125, v117\n"
"v_mfma_f32_16x16x32_bf16 v[108:111], v[76:79], a[92:95], v[108:111]\n"
"ds_write_addtid_b32 v105 offset:5152\n"
"global_load_dwordx4 v[64:67], v11, s[56:57] offset:0 sc1\n"
"v_mfma_f32_16x16x32_bf16 v[108:111], v[80:83], a[96:99], v[108:111]\n"
"ds_write_addtid_b32 v106 offset:9856\n"
"v_add_f32 v126, v126, v118\n"
"v_mfma_f32_16x16x32_bf16 v[108:111], v[84:87], a[100:103], v[108:111]\n"
"ds_write_addtid_b32 v107 offset:14496\n"
"v_add_f32 v127, v127, v119\n"
"v_mfma_f32_16x16x32_bf16 v[108:111], v[88:91], a[104:107], v[108:111]\n"
"v_cvt_pk_bf16_f32 v114, v124, v125\n"
"v_cvt_pk_bf16_f32 v115, v126, v127\n"
"v_mfma_f32_16x16x32_bf16 v[108:111], v[92:95], a[108:111], v[108:111]\n"
"s_waitcnt vmcnt(9)\n"
"buffer_store_dwordx2 v[0:1], v16, s[40:43], s93 offen offset:0 sc1\n"
"v_mfma_f32_16x16x32_bf16 v[104:107], v[68:71], a[112:115], 0\n"
"v_mfma_f32_16x16x32_bf16 v[104:107], v[72:75], a[116:119], v[104:107]\n"
"ds_write_addtid_b32 v108 offset:768\n"
"v_mfma_f32_16x16x32_bf16 v[104:107], v[76:79], a[120:123], v[104:107]\n"
"buffer_store_dwordx4 v[112:115], v13, s[40:43], s92 offen sc1\n"
"ds_write_addtid_b32 v109 offset:5408\n"
"v_mfma_f32_16x16x32_bf16 v[104:107], v[80:83], a[124:127], v[104:107]\n"
"ds_write_addtid_b32 v110 offset:10112\n"
"v_mfma_f32_16x16x32_bf16 v[104:107], v[84:87], a[128:131], v[104:107]\n"
"s_waitcnt vmcnt(10)\n"
"ds_write_addtid_b32 v111 offset:14752\n"
"v_cmp_ne_u64 vcc, s[64:65], v[136:137]\n"
"v_mfma_f32_16x16x32_bf16 v[104:107], v[88:91], a[132:135], v[104:107]\n"
"v_mfma_f32_16x16x32_bf16 v[104:107], v[92:95], a[136:139], v[104:107]\n"
"s_cbranch_vccnz .LH_5s\n"
".LH_5r:\n"
"v_mfma_f32_16x16x32_bf16 v[108:111], v[68:71], a[140:143], 0\n"
"buffer_load_dwordx4 v[112:115], v18, s[40:43], s94 offen offset:0 sc1\n"
"v_mfma_f32_16x16x32_bf16 v[108:111], v[72:75], a[144:147], v[108:111]\n"
"ds_write_addtid_b32 v104 offset:1024\n"
"v_mfma_f32_16x16x32_bf16 v[108:111], v[76:79], a[148:151], v[108:111]\n"
"ds_write_addtid_b32 v105 offset:5664\n"
"v_mfma_f32_16x16x32_bf16 v[108:111], v[80:83], a[152:155], v[108:111]\n"
"ds_write_addtid_b32 v106 offset:10368\n"
"v_mfma_f32_16x16x32_bf16 v[108:111], v[84:87], a[156:159], v[108:111]\n"
"ds_write_addtid_b32 v107 offset:15008\n"
"v_mfma_f32_16x16x32_bf16 v[108:111], v[88:91], a[160:163], v[108:111]\n"
"buffer_load_dwordx4 v[116:119], v18, s[40:43], s94 offen offset:1024 sc1\n"
"v_mfma_f32_16x16x32_bf16 v[108:111], v[92:95], a[164:167], v[108:111]\n"
"v_mfma_f32_16x16x32_bf16 v[104:107], v[68:71], a[224:227], 0\n"
"v_mfma_f32_16x16x32_bf16 v[104:107], v[72:75], a[228:231], v[104:107]\n"
"ds_write_addtid_b32 v108 offset:1280\n"
"v_mfma_f32_16x16x32_bf16 v[104:107], v[76:79], a[232:235], v[104:107]\n"
"ds_write_addtid_b32 v109 offset:5920\n"
"v_mfma_f32_16x16x32_bf16 v[104:107], v[80:83], a[236:239], v[104:107]\n"
"ds_write_addtid_b32 v110 offset:10624\n"
"v_mfma_f32_16x16x32_bf16 v[104:107], v[84:87], a[240:243], v[104:107]\n"
"ds_write_addtid_b32 v111 offset:15264\n"
"v_mfma_f32_16x16x32_bf16 v[104:107], v[88:91], a[244:247], v[104:107]\n"
"v_mfma_f32_16x16x32_bf16 v[104:107], v[92:95], a[248:251], v[104:107]\n"
"v_mfma_f32_16x16x32_bf16 v[120:123], v[68:71], a[252:255], 0\n"
"v_mfma_f32_16x16x32_bf16 v[120:123], v[72:75], v[24:27], v[120:123]\n"
"s_add_u32 s58, s50, 0x180000\n"
"ds_write_addtid_b32 v104 offset:1536\n"
"v_mfma_f32_16x16x32_bf16 v[120:123], v[76:79], v[28:31], v[120:123]\n"
"s_and_b32 s58, s58, 0x3ffffff\n"
"ds_write_addtid_b32 v105 offset:6176\n"
"v_mfma_f32_16x16x32_bf16 v[120:123], v[80:83], v[32:35], v[120:123]\n"
"s_add_u32 s56, s40, s58\n"
"ds_write_addtid_b32 v106 offset:10880\n"
"v_mfma_f32_16x16x32_bf16 v[120:123], v[84:87], v[36:39], v[120:123]\n"
"s_addc_u32 s57, s41, 0\n"
"ds_write_addtid_b32 v107 offset:15520\n"
"v_mfma_f32_16x16x32_bf16 v[120:123], v[88:91], v[40:43], v[120:123]\n"
"s_waitcnt vmcnt(8)\n"
"v_cmp_ne_u64 vcc, s[48:49], v[138:139]\n"
"v_mfma_f32_16x16x32_bf16 v[120:123], v[92:95], v[44:47], v[120:123]\n"
"v_mfma_f32_16x16x32_bf16 v[108:111], a[168:171], v[68:71], 0\n"
"s_cbranch_vccnz .LH_1s\n"
".LH_1r:\n"
"v_mfma_f32_16x16x32_bf16 v[108:111], a[172:175], v[72:75], v[108:111]\n"
"s_add_u32 s50, s50, 0x180000\n"
"ds_write_addtid_b32 v120 offset:1792\n"
"v_mfma_f32_16x16x32_bf16 v[108:111], a[176:179], v[76:79], v[108:111]\n"
"ds_write_addtid_b32 v121 offset:6432\n"
"v_mfma_f32_16x16x32_bf16 v[108:111], a[180:183], v[80:83], v[108:111]\n"
"ds_write_addtid_b32 v122 offset:11136\n"
"v_mfma_f32_16x16x32_bf16 v[108:111], a[184:187], v[84:87], v[108:111]\n"
"ds_write_addtid_b32 v123 offset:15776\n"
"global_load_dwordx4 v[104:107], v5, s[56:57] offset:0 sc1\n"
"v_mfma_f32_16x16x32_bf16 v[108:111], a[188:191], v[88:91], v[108:111]\n"
"v_mfma_f32_16x16x32_bf16 v[108:111], a[192:195], v[92:95], v[108:111]\n"
"v_mfma_f32_16x16x32_bf16 v[120:123], a[196:199], v[68:71], 0\n"
"v_mfma_f32_16x16x32_bf16 v[120:123], a[200:203], v[72:75], v[120:123]\n"
"s_waitcnt lgkmcnt(0)\n"
"v_mfma_f32_16x16x32_bf16 v[120:123], a[204:207], v[76:79], v[120:123]\n"
"s_barrier\n"
"v_mfma_f32_16x16x32_bf16 v[120:123], a[208:211], v[80:83], v[120:123]\n"
"global_load_dwordx4 v[72:75], v6, s[56:57] offset:0 sc1\n"
"ds_read_b128 v[68:71], v2 offset:2048\n"
"v_mfma_f32_16x16x32_bf16 v[120:123], a[212:215], v[84:87], v[120:123]\n"
"ds_read_b128 v[124:127], v3 offset:512\n"
"ds_read_b128 v[128:131], v4 offset:3072\n"
"v_mfma_f32_16x16x32_bf16 v[120:123], a[216:219], v[88:91], v[120:123]\n"
"ds_read_b128 v[132:135], v2 offset:2304\n"
"ds_read_b128 v[136:139], v3 offset:768\n"
"v_mfma_f32_16x16x32_bf16 v[120:123], a[220:223], v[92:95], v[120:123]\n"
"ds_read_b128 v[140:143], v4 offset:3328\n"
"v_mfma_f32_16x16x32_bf16 v[88:91], v[96:99], a[0:3], 0\n"
"global_load_dwordx4 v[76:79], v7, s[56:57] offset:0 sc1\n"
"v_mfma_f32_16x16x32_bf16 v[88:91], v[100:103], a[4:7], v[88:91]\n"
"s_waitcnt vmcnt(10)\n"
"v_mfma_f32_16x16x32_bf16 v[88:91], v[48:51], a[8:11], v[88:91]\n"
"v_mfma_f32_16x16x32_bf16 v[88:91], v[52:55], a[12:15], v[88:91]\n"
"s_waitcnt vmcnt(8)\n"
"v_mfma_f32_16x16x32_bf16 v[88:91], v[56:59], a[16:19], v[88:91]\n"
"s_waitcnt lgkmcnt(3)\n"
"v_add_f32 v68, v68, v124\n"
"v_add_f32 v69, v69, v125\n"
"v_mfma_f32_16x16x32_bf16 v[88:91], v[60:63], a[20:23], v[88:91]\n"
"s_waitcnt vmcnt(7)\n"
"global_load_dwordx4 v[80:83], v8, s[56:57] offset:0 sc1\n"
"v_add_f32 v70, v70, v126\n"
"v_mfma_f32_16x16x32_bf16 v[88:91], v[64:67], a[24:27], v[88:91]\n"
"buffer_load_dwordx2 v[144:145], v12, s[40:43], s50 offen sc1\n"
"v_mfma_f32_16x16x32_bf16 v[92:95], v[96:99], a[28:31], 0\n"
"v_add_f32 v71, v71, v127\n"
"v_add_f32 v68, v68, v128\n"
"v_mfma_f32_16x16x32_bf16 v[92:95], v[100:103], a[32:35], v[92:95]\n"
"s_waitcnt vmcnt(7)\n"
"buffer_store_dwordx2 v[0:1], v14, s[40:43], s95 offen offset:0 sc1\n"
"ds_write_addtid_b32 v88 offset:2048\n"
"v_mfma_f32_16x16x32_bf16 v[92:95], v[48:51], a[36:39], v[92:95]\n"
"ds_write_addtid_b32 v89 offset:6688\n"
"v_add_f32 v69, v69, v129\n"
"v_mfma_f32_16x16x32_bf16 v[92:95], v[52:55], a[40:43], v[92:95]\n"
"ds_write_addtid_b32 v90 offset:11392\n"
"global_load_dwordx4 v[84:87], v9, s[56:57] offset:0 sc1\n"
"v_mfma_f32_16x16x32_bf16 v[92:95], v[56:59], a[44:47], v[92:95]\n"
"ds_write_addtid_b32 v91 offset:16032\n"
"v_add_f32 v70, v70, v130\n"
"v_mfma_f32_16x16x32_bf16 v[92:95], v[60:63], a[48:51], v[92:95]\n"
"v_add_f32 v71, v71, v131\n"
"v_add_f32 v68, v68, v108\n"
"v_mfma_f32_16x16x32_bf16 v[92:95], v[64:67], a[52:55], v[92:95]\n"
"v_add_f32 v69, v69, v109\n"
"v_add_f32 v70, v70, v110\n"
"v_mfma_f32_16x16x32_bf16 v[124:127], v[96:99], a[56:59], 0\n"
"v_add_f32 v71, v71, v111\n"
"v_mfma_f32_16x16x32_bf16 v[124:127], v[100:103], a[60:63], v[124:127]\n"
"ds_write_addtid_b32 v92 offset:2304\n"
"global_load_dwordx4 v[88:91], v10, s[56:57] offset:0 sc1\n"
"v_mfma_f32_16x16x32_bf16 v[124:127], v[48:51], a[64:67], v[124:127]\n"
"s_add_u32 s93, s50, 0xffd00000\n"
"s_and_b32 s95, s93, 0xfbffffff\n"
"ds_write_addtid_b32 v93 offset:6944\n"
"v_mfma_f32_16x16x32_bf16 v[124:127], v[52:55], a[68:71], v[124:127]\n"
"ds_write_addtid_b32 v94 offset:11648\n"
"v_cvt_pk_bf16_f32 v108, v68, v69\n"
"v_mfma_f32_16x16x32_bf16 v[124:127], v[56:59], a[72:75], v[124:127]\n"
"s_waitcnt vmcnt(8)\n"
"ds_write_addtid_b32 v95 offset:16288\n"
"v_cvt_pk_bf16_f32 v109, v70, v71\n"
"v_mfma_f32_16x16x32_bf16 v[124:127], v[60:63], a[76:79], v[124:127]\n"
"s_waitcnt lgkmcnt(8)\n"
"v_add_f32 v132, v132, v136\n"
"v_add_f32 v133, v133, v137\n"
"v_mfma_f32_16x16x32_bf16 v[124:127], v[64:67], a[80:83], v[124:127]\n"
"global_load_dwordx4 v[92:95], v11, s[56:57] offset:0 sc1\n"
"v_add_f32 v134, v134, v138\n"
"v_mfma_f32_16x16x32_bf16 v[68:71], v[96:99], a[84:87], 0\n"
"v_add_f32 v135, v135, v139\n"
"v_add_f32 v132, v132, v140\n"
"v_mfma_f32_16x16x32_bf16 v[68:71], v[100:103], a[88:91], v[68:71]\n"
"ds_write_addtid_b32 v124 offset:2560\n"
"v_add_f32 v133, v133, v141\n"
"v_mfma_f32_16x16x32_bf16 v[68:71], v[48:51], a[92:95], v[68:71]\n"
"ds_write_addtid_b32 v125 offset:7200\n"
"v_add_f32 v134, v134, v142\n"
"v_mfma_f32_16x16x32_bf16 v[68:71], v[52:55], a[96:99], v[68:71]\n"
"ds_write_addtid_b32 v126 offset:11904\n"
"v_add_f32 v135, v135, v143\n"
"v_mfma_f32_16x16x32_bf16 v[68:71], v[56:59], a[100:103], v[68:71]\n"
"ds_write_addtid_b32 v127 offset:16544\n"
"v_add_f32 v132, v132, v120\n"
"v_mfma_f32_16x16x32_bf16 v[68:71], v[60:63], a[104:107], v[68:71]\n"
"v_add_f32 v133, v133, v121\n"
"v_add_f32 v134, v134, v122\n"
"v_mfma_f32_16x16x32_bf16 v[68:71], v[64:67], a[108:111], v[68:71]\n"
"v_add_f32 v135, v135, v123\n"
"v_cvt_pk_bf16_f32 v110, v132, v133\n"
"v_mfma_f32_16x16x32_bf16 v[124:127], v[96:99], a[112:115], 0\n"
"v_cvt_pk_bf16_f32 v111, v134, v135\n"
"v_add_f32 v112, v112, v116\n"
"v_mfma_f32_16x16x32_bf16 v[124:127], v[100:103], a[116:119], v[124:127]\n"
"ds_write_addtid_b32 v68 offset:2816\n"
"v_add_f32 v113, v113, v117\n"
"v_mfma_f32_16x16x32_bf16 v[124:127], v[48:51], a[120:123], v[124:127]\n"
"ds_write_addtid_b32 v69 offset:7456\n"
"v_add_f32 v114, v114, v118\n"
"v_mfma_f32_16x16x32_bf16 v[124:127], v[52:55], a[124:127], v[124:127]\n"
"ds_write_addtid_b32 v70 offset:12160\n"
"v_add_f32 v115, v115, v119\n"
"v_mfma_f32_16x16x32_bf16 v[124:127], v[56:59], a[128:131], v[124:127]\n"
"ds_write_addtid_b32 v71 offset:16800\n"
"v_mfma_f32_16x16x32_bf16 v[124:127], v[60:63], a[132:135], v[124:127]\n"
"v_permlane32_swap_b32 v112, v114\n"
"v_mfma_f32_16x16x32_bf16 v[124:127], v[64:67], a[136:139], v[124:127]\n"
"v_permlane32_swap_b32 v113, v115\n"
"v_mfma_f32_16x16x32_bf16 v[68:71], v[96:99], a[140:143], 0\n"
"v_add_f32 v112, v112, v114\n"
"v_add_f32 v113, v113, v115\n"
"v_mfma_f32_16x16x32_bf16 v[68:71], v[100:103], a[144:147], v[68:71]\n"
"buffer_store_dwordx4 v[108:111], v13, s[40:43], s95 offen sc1\n"
"ds_write_addtid_b32 v124 offset:3072\n"
"v_permlane16_swap_b32 v112, v113\n"
"v_mfma_f32_16x16x32_bf16 v[68:71], v[48:51], a[148:151], v[68:71]\n"
"ds_write_addtid_b32 v125 offset:7712\n"
"v_add_f32 v112, v112, v113\n"
"v_mfma_f32_16x16x32_bf16 v[68:71], v[52:55], a[152:155], v[68:71]\n"
"ds_write_addtid_b32 v126 offset:12416\n"
"v_add_f32_dpp v113, v112, v112 row_ror:8 row_mask:0xf bank_mask:0xf\n"
"v_mfma_f32_16x16x32_bf16 v[68:71], v[56:59], a[156:159], v[68:71]\n"
"ds_write_addtid_b32 v127 offset:17056\n"
"v_mov_b32 v112, v113\n"
"v_mfma_f32_16x16x32_bf16 v[68:71], v[60:63], a[160:163], v[68:71]\n"
"v_permlane16_swap_b32 v113, v112\n"
"v_mfma_f32_16x16x32_bf16 v[68:71], v[64:67], a[164:167], v[68:71]\n"
"v_mul_f32 v113, 0xba800000, v113\n"
"v_fma_f32 v112, v112, s62, v20\n"
"v_mfma_f32_16x16x32_bf16 v[108:111], v[96:99], a[224:227], 0\n"
"v_fma_f32 v112, -v113, v113, v112\n"
"v_max_f32 v112, v20, v112\n"
"v_mfma_f32_16x16x32_bf16 v[108:111], v[100:103], a[228:231], v[108:111]\n"
"ds_write_addtid_b32 v68 offset:3328\n"
"v_mfma_f32_16x16x32_bf16 v[108:111], v[48:51], a[232:235], v[108:111]\n"
"ds_write_addtid_b32 v69 offset:7968\n"
"v_mfma_f32_16x16x32_bf16 v[108:111], v[52:55], a[236:239], v[108:111]\n"
"ds_write_addtid_b32 v70 offset:12672\n"
"v_mfma_f32_16x16x32_bf16 v[108:111], v[56:59], a[240:243], v[108:111]\n"
"ds_write_addtid_b32 v71 offset:17312\n"
"v_mfma_f32_16x16x32_bf16 v[108:111], v[60:63], a[244:247], v[108:111]\n"
"v_rsq_f32 v112, v112\n"
"v_mfma_f32_16x16x32_bf16 v[108:111], v[64:67], a[248:251], v[108:111]\n"
"v_mov_b32 v114, v0\n"
"v_mov_b32 v115, v1\n"
"v_mfma_f32_16x16x32_bf16 v[68:71], v[96:99], a[252:255], 0\n"
"buffer_store_dwordx4 v[112:115], v19, s[40:43], s94 offen sc1\n"
"v_mfma_f32_16x16x32_bf16 v[68:71], v[100:103], v[24:27], v[68:71]\n"
"s_add_u32 s58, s50, 0x180000\n"
"ds_write_addtid_b32 v108 offset:3584\n"
"v_mfma_f32_16x16x32_bf16 v[68:71], v[48:51], v[28:31], v[68:71]\n"
"s_and_b32 s58, s58, 0x3ffffff\n"
"ds_write_addtid_b32 v109 offset:8224\n"
"v_mfma_f32_16x16x32_bf16 v[68:71], v[52:55], v[32:35], v[68:71]\n"
"s_add_u32 s56, s40, s58\n"
"ds_write_addtid_b32 v110 offset:12928\n"
"v_mfma_f32_16x16x32_bf16 v[68:71], v[56:59], v[36:39], v[68:71]\n"
"s_addc_u32 s57, s41, 0\n"
"ds_write_addtid_b32 v111 offset:17568\n"
"v_mfma_f32_16x16x32_bf16 v[68:71], v[60:63], v[40:43], v[68:71]\n"
"s_waitcnt vmcnt(6)\n"
"v_cmp_ne_u64 vcc, s[48:49], v[144:145]\n"
"v_mfma_f32_16x16x32_bf16 v[68:71], v[64:67], v[44:47], v[68:71]\n"
"v_mfma_f32_16x16x32_bf16 v[116:119], a[168:171], v[96:99], 0\n"
"s_cbranch_vccnz .LH_2s\n"
".LH_2r:\n"
"v_mfma_f32_16x16x32_bf16 v[116:119], a[172:175], v[100:103], v[116:119]\n"
"s_add_u32 s50, s50, 0x180000\n"
"ds_write_addtid_b32 v68 offset:3840\n"
"v_mfma_f32_16x16x32_bf16 v[116:119], a[176:179], v[48:51], v[116:119]\n"
"ds_write_addtid_b32 v69 offset:8480\n"
"v_mfma_f32_16x16x32_bf16 v[116:119], a[180:183], v[52:55], v[116:119]\n"
"ds_write_addtid_b32 v70 offset:13184\n"
"v_mfma_f32_16x16x32_bf16 v[116:119], a[184:187], v[56:59], v[116:119]\n"
"ds_write_addtid_b32 v71 offset:17824\n"
"v_mfma_f32_16x16x32_bf16 v[116:119], a[188:191], v[60:63], v[116:119]\n"
"global_load_dwordx4 v[108:111], v5, s[56:57] offset:0 sc1\n"
"v_mfma_f32_16x16x32_bf16 v[116:119], a[192:195], v[64:67], v[116:119]\n"
"v_mfma_f32_16x16x32_bf16 v[120:123], a[196:199], v[96:99], 0\n"
"v_mfma_f32_16x16x32_bf16 v[120:123], a[200:203], v[100:103], v[120:123]\n"
"s_waitcnt lgkmcnt(0)\n"
"global_load_dwordx4 v[112:115], v6, s[56:57] offset:0 sc1\n"
"v_mfma_f32_16x16x32_bf16 v[120:123], a[204:207], v[48:51], v[120:123]\n"
"s_barrier\n"
"v_mfma_f32_16x16x32_bf16 v[120:123], a[208:211], v[52:55], v[120:123]\n"
"ds_read_b128 v[100:103], v2 offset:0\n"
"ds_read_b128 v[68:71], v3 offset:2560\n"
"v_mfma_f32_16x16x32_bf16 v[120:123], a[212:215], v[56:59], v[120:123]\n"
"ds_read_b128 v[96:99], v4 offset:1024\n"
"ds_read_b128 v[124:127], v2 offset:256\n"
"v_mfma_f32_16x16x32_bf16 v[120:123], a[216:219], v[60:63], v[120:123]\n"
"ds_read_b128 v[128:131], v3 offset:2816\n"
"ds_read_b128 v[132:135], v4 offset:1280\n"
"v_mfma_f32_16x16x32_bf16 v[120:123], a[220:223], v[64:67], v[120:123]\n"
"s_branch .LH_top\n"
".LH_1s:\n"
"s_cmp_ge_u32 s50, s53\n"
"s_cbranch_scc1 .LH_t0\n"
"s_cmp_ge_u32 s50, s52\n"
"s_cbranch_scc1 .LH_1r\n"
"s_sub_u32 s60, s50, 0x0\n"
"s_mov_b32 s61, 200000\n"
"s_nop 7\n"
".LH_1p:\n"
"buffer_load_dwordx2 v[138:139], v12, s[40:43], s60 offen sc1\n"
"s_waitcnt vmcnt(0)\n"
"v_cmp_ne_u64 vcc, s[48:49], v[138:139]\n"
"s_cbranch_vccz .LH_1px\n"
"s_sleep 1\n"
"s_sub_u32 s61, s61, 1\n"
"s_cbranch_scc0 .LH_1p\n"
"s_branch .LH_end\n"
".LH_1px:\n"
"s_branch .LH_1r\n"
".LH_2s:\n"
"s_cmp_ge_u32 s50, s53\n"
"s_cbranch_scc1 .LH_t1\n"
"s_cmp_ge_u32 s50, s52\n"
"s_cbranch_scc1 .LH_2r\n"
"s_sub_u32 s60, s50, 0x0\n"
"s_mov_b32 s61, 200000\n"
"s_nop 7\n"
".LH_2p:\n"
"buffer_load_dwordx2 v[144:145], v12, s[40:43], s60 offen sc1\n"
"s_waitcnt vmcnt(0)\n"
"v_cmp_ne_u64 vcc, s[48:49], v[144:145]\n"
"s_cbranch_vccz .LH_2px\n"
"s_sleep 1\n"
"s_sub_u32 s61, s61, 1\n"
"s_cbranch_scc0 .LH_2p\n"
"s_branch .LH_end\n"
".LH_2px:\n"
"s_branch .LH_2r\n"
".LH_3s:\n"
"s_cmp_ge_u32 s50, s53\n"
"s_cbranch_scc1 .LH_t2\n"
"s_cmp_ge_u32 s50, s52\n"
"s_cbranch_scc1 .LH_3r\n"
"s_sub_u32 s60, s50, 0x0\n"
"s_mov_b32 s61, 200000\n"
"s_nop 7\n"
".LH_3p:\n"
"buffer_load_dwordx2 v[136:137], v12, s[40:43], s60 offen sc1\n"
"s_waitcnt vmcnt(0)\n"
"v_cmp_ne_u64 vcc, s[48:49], v[136:137]\n"
"s_cbranch_vccz .LH_3px\n"
"s_sleep 1\n"
"s_sub_u32 s61, s61, 1\n"
"s_cbranch_scc0 .LH_3p\n"
"s_branch .LH_end\n"
".LH_3px:\n"
"s_branch .LH_3r\n"
".LH_4s:\n"
"s_cmp_ge_u32 s50, s53\n"
"s_cbranch_scc1 .LH_t3\n"
"s_cmp_ge_u32 s50, s52\n"
"s_cbranch_scc1 .LH_4r\n"
"s_sub_u32 s60, s50, 0x0\n"
"s_mov_b32 s61, 200000\n"
"s_nop 7\n"
".LH_4p:\n"
"buffer_load_dwordx2 v[172:173], v12, s[40:43], s60 offen sc1\n"
"s_waitcnt vmcnt(0)\n"
"v_cmp_ne_u64 vcc, s[48:49], v[172:173]\n"
"s_cbranch_vccz .LH_4px\n"
"s_sleep 1\n"
"s_sub_u32 s61, s61, 1\n"
"s_cbranch_scc0 .LH_4p\n"
"s_branch .LH_end\n"
".LH_4px:\n"
"s_branch .LH_4r\n"
".LH_5s:\n"
"s_cmp_ge_i32 s94, 0xc000000\n"
"s_cselect_b64 s[64:65], 0, s[64:65]\n"
"s_cmp_ge_u32 s94, 0xc000000\n"
"s_cbranch_scc1 .LH_5r\n"
"s_mov_b32 s61, 200000\n"
"s_nop 7\n"
".LH_5p:\n"
"buffer_load_dwordx2 v[136:137], v17, s[40:43], s94 offen offset:0 sc1\n"
"s_waitcnt vmcnt(0)\n"
"v_cmp_ne_u64 vcc, s[64:65], v[136:137]\n"
"s_cbranch_vccz .LH_5px\n"
"s_sleep 1\n"
"s_sub_u32 s61, s61, 1\n"
"s_cbranch_scc0 .LH_5p\n"
"s_branch .LH_end\n"
".LH_5px:\n"
"s_branch .LH_5r\n"
".LH_t0:\n"
"v_mfma_f32_16x16x32_bf16 v[108:111], a[172:175], v[72:75], v[108:111]\n"
"ds_write_addtid_b32 v120 offset:1792\n"
"v_mfma_f32_16x16x32_bf16 v[108:111], a[176:179], v[76:79], v[108:111]\n"
"ds_write_addtid_b32 v121 offset:6432\n"
"v_mfma_f32_16x16x32_bf16 v[108:111], a[180:183], v[80:83], v[108:111]\n"
"ds_write_addtid_b32 v122 offset:11136\n"
"v_mfma_f32_16x16x32_bf16 v[108:111], a[184:187], v[84:87], v[108:111]\n"
"ds_write_addtid_b32 v123 offset:15776\n"
"v_mfma_f32_16x16x32_bf16 v[108:111], a[188:191], v[88:91], v[108:111]\n"
"v_mfma_f32_16x16x32_bf16 v[108:111], a[192:195], v[92:95], v[108:111]\n"
"v_mfma_f32_16x16x32_bf16 v[120:123], a[196:199], v[68:71], 0\n"
"v_mfma_f32_16x16x32_bf16 v[120:123], a[200:203], v[72:75], v[120:123]\n"
"s_waitcnt lgkmcnt(0)\n"
"v_mfma_f32_16x16x32_bf16 v[120:123], a[204:207], v[76:79], v[120:123]\n"
"s_barrier\n"
"v_mfma_f32_16x16x32_bf16 v[120:123], a[208:211], v[80:83], v[120:123]\n"
"ds_read_b128 v[68:71], v2 offset:2048\n"
"v_mfma_f32_16x16x32_bf16 v[120:123], a[212:215], v[84:87], v[120:123]\n"
"ds_read_b128 v[124:127], v3 offset:512\n"
"ds_read_b128 v[128:131], v4 offset:3072\n"
"v_mfma_f32_16x16x32_bf16 v[120:123], a[216:219], v[88:91], v[120:123]\n"
"ds_read_b128 v[132:135], v2 offset:2304\n"
"ds_read_b128 v[136:139], v3 offset:768\n"
"v_mfma_f32_16x16x32_bf16 v[120:123], a[220:223], v[92:95], v[120:123]\n"
"s_waitcnt vmcnt(10)\n"
"ds_read_b128 v[140:143], v4 offset:3328\n"
"v_mfma_f32_16x16x32_bf16 v[88:91], v[96:99], a[0:3], 0\n"
"v_mfma_f32_16x16x32_bf16 v[88:91], v[100:103], a[4:7], v[88:91]\n"
"s_waitcnt vmcnt(7)\n"
"v_mfma_f32_16x16x32_bf16 v[88:91], v[48:51], a[8:11], v[88:91]\n"
"v_mfma_f32_16x16x32_bf16 v[88:91], v[52:55], a[12:15], v[88:91]\n"
"s_waitcnt vmcnt(5)\n"
"v_mfma_f32_16x16x32_bf16 v[88:91], v[56:59], a[16:19], v[88:91]\n"
"s_waitcnt lgkmcnt(3)\n"
"v_add_f32 v68, v68, v124\n"
"v_add_f32 v69, v69, v125\n"
"v_mfma_f32_16x16x32_bf16 v[88:91], v[60:63], a[20:23], v[88:91]\n"
"s_waitcnt vmcnt(4)\n"
"v_add_f32 v70, v70, v126\n"
"v_mfma_f32_16x16x32_bf16 v[88:91], v[64:67], a[24:27], v[88:91]\n"
"v_mfma_f32_16x16x32_bf16 v[92:95], v[96:99], a[28:31], 0\n"
"v_add_f32 v71, v71, v127\n"
"v_add_f32 v68, v68, v128\n"
"v_mfma_f32_16x16x32_bf16 v[92:95], v[100:103], a[32:35], v[92:95]\n"
"s_waitcnt vmcnt(2)\n"
"buffer_store_dwordx2 v[0:1], v14, s[40:43], s95 offen offset:0 sc1\n"
"ds_write_addtid_b32 v88 offset:2048\n"
"v_mfma_f32_16x16x32_bf16 v[92:95], v[48:51], a[36:39], v[92:95]\n"
"ds_write_addtid_b32 v89 offset:6688\n"
"v_add_f32 v69, v69, v129\n"
"v_mfma_f32_16x16x32_bf16 v[92:95], v[52:55], a[40:43], v[92:95]\n"
"ds_write_addtid_b32 v90 offset:11392\n"
"v_mfma_f32_16x16x32_bf16 v[92:95], v[56:59], a[44:47], v[92:95]\n"
"ds_write_addtid_b32 v91 offset:16032\n"
"v_add_f32 v70, v70, v130\n"
"v_mfma_f32_16x16x32_bf16 v[92:95], v[60:63], a[48:51], v[92:95]\n"
"v_add_f32 v71, v71, v131\n"
"v_add_f32 v68, v68, v108\n"
"v_mfma_f32_16x16x32_bf16 v[92:95], v[64:67], a[52:55], v[92:95]\n"
"v_add_f32 v69, v69, v109\n"
"v_add_f32 v70, v70, v110\n"
"v_mfma_f32_16x16x32_bf16 v[124:127], v[96:99], a[56:59], 0\n"
"v_add_f32 v71, v71, v111\n"
"v_mfma_f32_16x16x32_bf16 v[124:127], v[100:103], a[60:63], v[124:127]\n"
"ds_write_addtid_b32 v92 offset:2304\n"
"v_mfma_f32_16x16x32_bf16 v[124:127], v[48:51], a[64:67], v[124:127]\n"
"s_add_u32 s93, s50, 0xffe80000\n"
"s_and_b32 s95, s93, 0xfbffffff\n"
"ds_write_addtid_b32 v93 offset:6944\n"
"v_mfma_f32_16x16x32_bf16 v[124:127], v[52:55], a[68:71], v[124:127]\n"
"ds_write_addtid_b32 v94 offset:11648\n"
"v_cvt_pk_bf16_f32 v108, v68, v69\n"
"v_mfma_f32_16x16x32_bf16 v[124:127], v[56:59], a[72:75], v[124:127]\n"
"s_waitcnt vmcnt(1)\n"
"ds_write_addtid_b32 v95 offset:16288\n"
"v_cvt_pk_bf16_f32 v109, v70, v71\n"
"v_mfma_f32_16x16x32_bf16 v[124:127], v[60:63], a[76:79], v[124:127]\n"
"s_waitcnt lgkmcnt(8)\n"
"v_add_f32 v132, v132, v136\n"
"v_add_f32 v133, v133, v137\n"
"v_mfma_f32_16x16x32_bf16 v[124:127], v[64:67], a[80:83], v[124:127]\n"
"v_add_f32 v134, v134, v138\n"
"v_mfma_f32_16x16x32_bf16 v[68:71], v[96:99], a[84:87], 0\n"
"v_add_f32 v135, v135, v139\n"
"v_add_f32 v132, v132, v140\n"
"v_mfma_f32_16x16x32_bf16 v[68:71], v[100:103], a[88:91], v[68:71]\n"
"ds_write_addtid_b32 v124 offset:2560\n"
"v_add_f32 v133, v133, v141\n"
"v_mfma_f32_16x16x32_bf16 v[68:71], v[48:51], a[92:95], v[68:71]\n"
"ds_write_addtid_b32 v125 offset:7200\n"
"v_add_f32 v134, v134, v142\n"
"v_mfma_f32_16x16x32_bf16 v[68:71], v[52:55], a[96:99], v[68:71]\n"
"ds_write_addtid_b32 v126 offset:11904\n"
"v_add_f32 v135, v135, v143\n"
"v_mfma_f32_16x16x32_bf16 v[68:71], v[56:59], a[100:103], v[68:71]\n"
"ds_write_addtid_b32 v127 offset:16544\n"
"v_add_f32 v132, v132, v120\n"
"v_mfma_f32_16x16x32_bf16 v[68:71], v[60:63], a[104:107], v[68:71]\n"
"v_add_f32 v133, v133, v121\n"
"v_add_f32 v134, v134, v122\n"
"v_mfma_f32_16x16x32_bf16 v[68:71], v[64:67], a[108:111], v[68:71]\n"
"v_add_f32 v135, v135, v123\n"
"v_cvt_pk_bf16_f32 v110, v132, v133\n"
"v_mfma_f32_16x16x32_bf16 v[124:127], v[96:99], a[112:115], 0\n"
"v_cvt_pk_bf16_f32 v111, v134, v135\n"
"v_mfma_f32_16x16x32_bf16 v[124:127], v[100:103], a[116:119], v[124:127]\n"
"ds_write_addtid_b32 v68 offset:2816\n"
"v_mfma_f32_16x16x32_bf16 v[124:127], v[48:51], a[120:123], v[124:127]\n"
"ds_write_addtid_b32 v69 offset:7456\n"
"v_mfma_f32_16x16x32_bf16 v[124:127], v[52:55], a[124:127], v[124:127]\n"
"ds_write_addtid_b32 v70 offset:12160\n"
"v_mfma_f32_16x16x32_bf16 v[124:127], v[56:59], a[128:131], v[124:127]\n"
"ds_write_addtid_b32 v71 offset:16800\n"
"v_mfma_f32_16x16x32_bf16 v[124:127], v[60:63], a[132:135], v[124:127]\n"
"v_mfma_f32_16x16x32_bf16 v[124:127], v[64:67], a[136:139], v[124:127]\n"
"v_mfma_f32_16x16x32_bf16 v[68:71], v[96:99], a[140:143], 0\n"
"v_mfma_f32_16x16x32_bf16 v[68:71], v[100:103], a[144:147], v[68:71]\n"
"buffer_store_dwordx4 v[108:111], v13, s[40:43], s95 offen sc1\n"
"ds_write_addtid_b32 v124 offset:3072\n"
"v_mfma_f32_16x16x32_bf16 v[68:71], v[48:51], a[148:151], v[68:71]\n"
"ds_write_addtid_b32 v125 offset:7712\n"
"v_mfma_f32_16x16x32_bf16 v[68:71], v[52:55], a[152:155], v[68:71]\n"
"ds_write_addtid_b32 v126 offset:12416\n"
"v_mfma_f32_16x16x32_bf16 v[68:71], v[56:59], a[156:159], v[68:71]\n"
"ds_write_addtid_b32 v127 offset:17056\n"
"v_mfma_f32_16x16x32_bf16 v[68:71], v[60:63], a[160:163], v[68:71]\n"
"v_mfma_f32_16x16x32_bf16 v[68:71], v[64:67], a[164:167], v[68:71]\n"
"v_mfma_f32_16x16x32_bf16 v[108:111], v[96:99], a[224:227], 0\n"
"v_mfma_f32_16x16x32_bf16 v[108:111], v[100:103], a[228:231], v[108:111]\n"
"ds_write_addtid_b32 v68 offset:3328\n"
"v_mfma_f32_16x16x32_bf16 v[108:111], v[48:51], a[232:235], v[108:111]\n"
"ds_write_addtid_b32 v69 offset:7968\n"
"v_mfma_f32_16x16x32_bf16 v[108:111], v[52:55], a[236:239], v[108:111]\n"
"ds_write_addtid_b32 v70 offset:12672\n"
"v_mfma_f32_16x16x32_bf16 v[108:111], v[56:59], a[240:243], v[108:111]\n"
"ds_write_addtid_b32 v71 offset:17312\n"
"v_mfma_f32_16x16x32_bf16 v[108:111], v[60:63], a[244:247], v[108:111]\n"
"v_mfma_f32_16x16x32_bf16 v[108:111], v[64:67], a[248:251], v[108:111]\n"
"v_mfma_f32_16x16x32_bf16 v[68:71], v[96:99], a[252:255], 0\n"
"v_mfma_f32_16x16x32_bf16 v[68:71], v[100:103], v[24:27], v[68:71]\n"
"ds_write_addtid_b32 v108 offset:3584\n"
"v_mfma_f32_16x16x32_bf16 v[68:71], v[48:51], v[28:31], v[68:71]\n"
"ds_write_addtid_b32 v109 offset:8224\n"
"v_mfma_f32_16x16x32_bf16 v[68:71], v[52:55], v[32:35], v[68:71]\n"
"ds_write_addtid_b32 v110 offset:12928\n"
"v_mfma_f32_16x16x32_bf16 v[68:71], v[56:59], v[36:39], v[68:71]\n"
"ds_write_addtid_b32 v111 offset:17568\n"
"v_mfma_f32_16x16x32_bf16 v[68:71], v[60:63], v[40:43], v[68:71]\n"
"v_mfma_f32_16x16x32_bf16 v[68:71], v[64:67], v[44:47], v[68:71]\n"
"v_mfma_f32_16x16x32_bf16 v[116:119], a[168:171], v[96:99], 0\n"
"v_mfma_f32_16x16x32_bf16 v[116:119], a[172:175], v[100:103], v[116:119]\n"
"ds_write_addtid_b32 v68 offset:3840\n"
"v_mfma_f32_16x16x32_bf16 v[116:119], a[176:179], v[48:51], v[116:119]\n"
"ds_write_addtid_b32 v69 offset:8480\n"
"v_mfma_f32_16x16x32_bf16 v[116:119], a[180:183], v[52:55], v[116:119]\n"
"ds_write_addtid_b32 v70 offset:13184\n"
"v_mfma_f32_16x16x32_bf16 v[116:119], a[184:187], v[56:59], v[116:119]\n"
"ds_write_addtid_b32 v71 offset:17824\n"
"v_mfma_f32_16x16x32_bf16 v[116:119], a[188:191], v[60:63], v[116:119]\n"
"v_mfma_f32_16x16x32_bf16 v[116:119], a[192:195], v[64:67], v[116:119]\n"
"v_mfma_f32_16x16x32_bf16 v[120:123], a[196:199], v[96:99], 0\n"
"v_mfma_f32_16x16x32_bf16 v[120:123], a[200:203], v[100:103], v[120:123]\n"
"s_waitcnt lgkmcnt(0)\n"
"v_mfma_f32_16x16x32_bf16 v[120:123], a[204:207], v[48:51], v[120:123]\n"
"s_barrier\n"
"v_mfma_f32_16x16x32_bf16 v[120:123], a[208:211], v[52:55], v[120:123]\n"
"ds_read_b128 v[100:103], v2 offset:0\n"
"ds_read_b128 v[68:71], v3 offset:2560\n"
"v_mfma_f32_16x16x32_bf16 v[120:123], a[212:215], v[56:59], v[120:123]\n"
"ds_read_b128 v[96:99], v4 offset:1024\n"
"ds_read_b128 v[124:127], v2 offset:256\n"
"v_mfma_f32_16x16x32_bf16 v[120:123], a[216:219], v[60:63], v[120:123]\n"
"ds_read_b128 v[128:131], v3 offset:2816\n"
"ds_read_b128 v[132:135], v4 offset:1280\n"
"v_mfma_f32_16x16x32_bf16 v[120:123], a[220:223], v[64:67], v[120:123]\n"
"s_waitcnt lgkmcnt(3)\n"
"v_add_f32 v100, v100, v68\n"
"v_add_f32 v101, v101, v69\n"
"v_add_f32 v102, v102, v70\n"
"v_add_f32 v103, v103, v71\n"
"v_add_f32 v100, v100, v96\n"
"v_add_f32 v101, v101, v97\n"
"v_add_f32 v102, v102, v98\n"
"v_add_f32 v103, v103, v99\n"
"v_add_f32 v100, v100, v116\n"
"v_add_f32 v101, v101, v117\n"
"v_add_f32 v102, v102, v118\n"
"v_add_f32 v103, v103, v119\n"
"s_waitcnt vmcnt(0)\n"
"buffer_store_dwordx2 v[0:1], v14, s[40:43], s93 offen offset:0 sc1\n"
"s_add_u32 s95, s50, 0x0\n"
"s_and_b32 s94, s95, 0xfbffffff\n"
"v_cvt_pk_bf16_f32 v96, v100, v101\n"
"v_cvt_pk_bf16_f32 v97, v102, v103\n"
"s_waitcnt lgkmcnt(0)\n"
"v_add_f32 v124, v124, v128\n"
"v_add_f32 v125, v125, v129\n"
"v_add_f32 v126, v126, v130\n"
"v_add_f32 v127, v127, v131\n"
"v_add_f32 v124, v124, v132\n"
"v_add_f32 v125, v125, v133\n"
"v_add_f32 v126, v126, v134\n"
"v_add_f32 v127, v127, v135\n"
"v_add_f32 v124, v124, v120\n"
"v_add_f32 v125, v125, v121\n"
"v_add_f32 v126, v126, v122\n"
"v_add_f32 v127, v127, v123\n"
"v_cvt_pk_bf16_f32 v98, v124, v125\n"
"v_cvt_pk_bf16_f32 v99, v126, v127\n"
"buffer_store_dwordx4 v[96:99], v13, s[40:43], s94 offen sc1\n"
"s_waitcnt vmcnt(0)\n"
"buffer_store_dwordx2 v[0:1], v14, s[40:43], s95 offen offset:0 sc1\n"
"s_branch .LH_end\n"
".LH_t1:\n"
"v_mfma_f32_16x16x32_bf16 v[116:119], a[172:175], v[100:103], v[116:119]\n"
"ds_write_addtid_b32 v68 offset:3840\n"
"v_mfma_f32_16x16x32_bf16 v[116:119], a[176:179], v[48:51], v[116:119]\n"
"ds_write_addtid_b32 v69 offset:8480\n"
"v_mfma_f32_16x16x32_bf16 v[116:119], a[180:183], v[52:55], v[116:119]\n"
"ds_write_addtid_b32 v70 offset:13184\n"
"v_mfma_f32_16x16x32_bf16 v[116:119], a[184:187], v[56:59], v[116:119]\n"
"ds_write_addtid_b32 v71 offset:17824\n"
"v_mfma_f32_16x16x32_bf16 v[116:119], a[188:191], v[60:63], v[116:119]\n"
"v_mfma_f32_16x16x32_bf16 v[116:119], a[192:195], v[64:67], v[116:119]\n"
"v_mfma_f32_16x16x32_bf16 v[120:123], a[196:199], v[96:99], 0\n"
"v_mfma_f32_16x16x32_bf16 v[120:123], a[200:203], v[100:103], v[120:123]\n"
"s_waitcnt lgkmcnt(0)\n"
"v_mfma_f32_16x16x32_bf16 v[120:123], a[204:207], v[48:51], v[120:123]\n"
"s_barrier\n"
"v_mfma_f32_16x16x32_bf16 v[120:123], a[208:211], v[52:55], v[120:123]\n"
"ds_read_b128 v[100:103], v2 offset:0\n"
"ds_read_b128 v[68:71], v3 offset:2560\n"
"v_mfma_f32_16x16x32_bf16 v[120:123], a[212:215], v[56:59], v[120:123]\n"
"ds_read_b128 v[96:99], v4 offset:1024\n"
"ds_read_b128 v[124:127], v2 offset:256\n"
"v_mfma_f32_16x16x32_bf16 v[120:123], a[216:219], v[60:63], v[120:123]\n"
"ds_read_b128 v[128:131], v3 offset:2816\n"
"ds_read_b128 v[132:135], v4 offset:1280\n"
"v_mfma_f32_16x16x32_bf16 v[120:123], a[220:223], v[64:67], v[120:123]\n"
"s_waitcnt vmcnt(9)\n"
"v_mfma_f32_16x16x32_bf16 v[60:63], v[104:107], a[0:3], 0\n"
"v_mfma_f32_16x16x32_bf16 v[60:63], v[72:75], a[4:7], v[60:63]\n"
"s_waitcnt vmcnt(7)\n"
"v_mfma_f32_16x16x32_bf16 v[60:63], v[76:79], a[8:11], v[60:63]\n"
"v_mfma_f32_16x16x32_bf16 v[60:63], v[80:83], a[12:15], v[60:63]\n"
"s_waitcnt vmcnt(3)\n"
"v_mfma_f32_16x16x32_bf16 v[60:63], v[84:87], a[16:19], v[60:63]\n"
"s_waitcnt lgkmcnt(3)\n"
"v_add_f32 v100, v100, v68\n"
"v_add_f32 v101, v101, v69\n"
"v_mfma_f32_16x16x32_bf16 v[60:63], v[88:91], a[20:23], v[60:63]\n"
"s_waitcnt vmcnt(2)\n"
"v_add_f32 v102, v102, v70\n"
"v_add_f32 v103, v103, v71\n"
"v_mfma_f32_16x16x32_bf16 v[60:63], v[92:95], a[24:27], v[60:63]\n"
"v_add_f32 v100, v100, v96\n"
"v_mfma_f32_16x16x32_bf16 v[64:67], v[104:107], a[28:31], 0\n"
"v_mfma_f32_16x16x32_bf16 v[64:67], v[72:75], a[32:35], v[64:67]\n"
"ds_write_addtid_b32 v60 offset:0\n"
"v_add_f32 v101, v101, v97\n"
"v_mfma_f32_16x16x32_bf16 v[64:67], v[76:79], a[36:39], v[64:67]\n"
"ds_write_addtid_b32 v61 offset:4640\n"
"v_mfma_f32_16x16x32_bf16 v[64:67], v[80:83], a[40:43], v[64:67]\n"
"ds_write_addtid_b32 v62 offset:9344\n"
"v_add_f32 v102, v102, v98\n"
"v_mfma_f32_16x16x32_bf16 v[64:67], v[84:87], a[44:47], v[64:67]\n"
"ds_write_addtid_b32 v63 offset:13984\n"
"v_add_f32 v103, v103, v99\n"
"v_mfma_f32_16x16x32_bf16 v[64:67], v[88:91], a[48:51], v[64:67]\n"
"v_add_f32 v100, v100, v116\n"
"v_add_f32 v101, v101, v117\n"
"v_mfma_f32_16x16x32_bf16 v[64:67], v[92:95], a[52:55], v[64:67]\n"
"v_add_f32 v102, v102, v118\n"
"v_add_f32 v103, v103, v119\n"
"v_mfma_f32_16x16x32_bf16 v[68:71], v[104:107], a[56:59], 0\n"
"s_waitcnt vmcnt(1)\n"
"buffer_store_dwordx2 v[0:1], v14, s[40:43], s93 offen offset:0 sc1\n"
"v_mfma_f32_16x16x32_bf16 v[68:71], v[72:75], a[60:63], v[68:71]\n"
"s_add_u32 s95, s50, 0xffe80000\n"
"s_and_b32 s94, s95, 0xfbffffff\n"
"ds_write_addtid_b32 v64 offset:256\n"
"v_mfma_f32_16x16x32_bf16 v[68:71], v[76:79], a[64:67], v[68:71]\n"
"ds_write_addtid_b32 v65 offset:4896\n"
"v_mfma_f32_16x16x32_bf16 v[68:71], v[80:83], a[68:71], v[68:71]\n"
"ds_write_addtid_b32 v66 offset:9600\n"
"v_cvt_pk_bf16_f32 v96, v100, v101\n"
"v_mfma_f32_16x16x32_bf16 v[68:71], v[84:87], a[72:75], v[68:71]\n"
"ds_write_addtid_b32 v67 offset:14240\n"
"v_cvt_pk_bf16_f32 v97, v102, v103\n"
"v_mfma_f32_16x16x32_bf16 v[68:71], v[88:91], a[76:79], v[68:71]\n"
"s_waitcnt lgkmcnt(8)\n"
"v_add_f32 v124, v124, v128\n"
"v_add_f32 v125, v125, v129\n"
"v_mfma_f32_16x16x32_bf16 v[68:71], v[92:95], a[80:83], v[68:71]\n"
"v_add_f32 v126, v126, v130\n"
"v_add_f32 v127, v127, v131\n"
"v_mfma_f32_16x16x32_bf16 v[100:103], v[104:107], a[84:87], 0\n"
"v_add_f32 v124, v124, v132\n"
"v_mfma_f32_16x16x32_bf16 v[100:103], v[72:75], a[88:91], v[100:103]\n"
"ds_write_addtid_b32 v68 offset:512\n"
"v_add_f32 v125, v125, v133\n"
"v_mfma_f32_16x16x32_bf16 v[100:103], v[76:79], a[92:95], v[100:103]\n"
"ds_write_addtid_b32 v69 offset:5152\n"
"v_add_f32 v126, v126, v134\n"
"v_mfma_f32_16x16x32_bf16 v[100:103], v[80:83], a[96:99], v[100:103]\n"
"ds_write_addtid_b32 v70 offset:9856\n"
"v_add_f32 v127, v127, v135\n"
"v_mfma_f32_16x16x32_bf16 v[100:103], v[84:87], a[100:103], v[100:103]\n"
"ds_write_addtid_b32 v71 offset:14496\n"
"v_add_f32 v124, v124, v120\n"
"v_mfma_f32_16x16x32_bf16 v[100:103], v[88:91], a[104:107], v[100:103]\n"
"v_add_f32 v125, v125, v121\n"
"v_add_f32 v126, v126, v122\n"
"v_mfma_f32_16x16x32_bf16 v[100:103], v[92:95], a[108:111], v[100:103]\n"
"v_add_f32 v127, v127, v123\n"
"v_cvt_pk_bf16_f32 v98, v124, v125\n"
"v_mfma_f32_16x16x32_bf16 v[68:71], v[104:107], a[112:115], 0\n"
"v_cvt_pk_bf16_f32 v99, v126, v127\n"
"v_mfma_f32_16x16x32_bf16 v[68:71], v[72:75], a[116:119], v[68:71]\n"
"ds_write_addtid_b32 v100 offset:768\n"
"v_mfma_f32_16x16x32_bf16 v[68:71], v[76:79], a[120:123], v[68:71]\n"
"ds_write_addtid_b32 v101 offset:5408\n"
"v_mfma_f32_16x16x32_bf16 v[68:71], v[80:83], a[124:127], v[68:71]\n"
"ds_write_addtid_b32 v102 offset:10112\n"
"v_mfma_f32_16x16x32_bf16 v[68:71], v[84:87], a[128:131], v[68:71]\n"
"ds_write_addtid_b32 v103 offset:14752\n"
"v_mfma_f32_16x16x32_bf16 v[68:71], v[88:91], a[132:135], v[68:71]\n"
"v_mfma_f32_16x16x32_bf16 v[68:71], v[92:95], a[136:139], v[68:71]\n"
"v_mfma_f32_16x16x32_bf16 v[116:119], v[104:107], a[140:143], 0\n"
"v_mfma_f32_16x16x32_bf16 v[116:119], v[72:75], a[144:147], v[116:119]\n"
"ds_write_addtid_b32 v68 offset:1024\n"
"v_mfma_f32_16x16x32_bf16 v[116:119], v[76:79], a[148:151], v[116:119]\n"
"ds_write_addtid_b32 v69 offset:5664\n"
"v_mfma_f32_16x16x32_bf16 v[116:119], v[80:83], a[152:155], v[116:119]\n"
"ds_write_addtid_b32 v70 offset:10368\n"
"v_mfma_f32_16x16x32_bf16 v[116:119], v[84:87], a[156:159], v[116:119]\n"
"ds_write_addtid_b32 v71 offset:15008\n"
"v_mfma_f32_16x16x32_bf16 v[116:119], v[88:91], a[160:163], v[116:119]\n"
"buffer_store_dwordx4 v[96:99], v13, s[40:43], s94 offen sc1\n"
"v_mfma_f32_16x16x32_bf16 v[116:119], v[92:95], a[164:167], v[116:119]\n"
"v_mfma_f32_16x16x32_bf16 v[100:103], a[168:171], v[104:107], 0\n"
"v_mfma_f32_16x16x32_bf16 v[100:103], a[172:175], v[72:75], v[100:103]\n"
"ds_write_addtid_b32 v116 offset:1280\n"
"v_mfma_f32_16x16x32_bf16 v[100:103], a[176:179], v[76:79], v[100:103]\n"
"ds_write_addtid_b32 v117 offset:5920\n"
"v_mfma_f32_16x16x32_bf16 v[100:103], a[180:183], v[80:83], v[100:103]\n"
"ds_write_addtid_b32 v118 offset:10624\n"
"v_mfma_f32_16x16x32_bf16 v[100:103], a[184:187], v[84:87], v[100:103]\n"
"ds_write_addtid_b32 v119 offset:15264\n"
"v_mfma_f32_16x16x32_bf16 v[100:103], a[188:191], v[88:91], v[100:103]\n"
"v_mfma_f32_16x16x32_bf16 v[100:103], a[192:195], v[92:95], v[100:103]\n"
"v_mfma_f32_16x16x32_bf16 v[96:99], a[196:199], v[104:107], 0\n"
"v_mfma_f32_16x16x32_bf16 v[96:99], a[200:203], v[72:75], v[96:99]\n"
"v_mfma_f32_16x16x32_bf16 v[96:99], a[204:207], v[76:79], v[96:99]\n"
"v_mfma_f32_16x16x32_bf16 v[96:99], a[208:211], v[80:83], v[96:99]\n"
"v_mfma_f32_16x16x32_bf16 v[96:99], a[212:215], v[84:87], v[96:99]\n"
"v_mfma_f32_16x16x32_bf16 v[96:99], a[216:219], v[88:91], v[96:99]\n"
"v_mfma_f32_16x16x32_bf16 v[96:99], a[220:223], v[92:95], v[96:99]\n"
"v_mfma_f32_16x16x32_bf16 v[116:119], a[224:227], v[104:107], 0\n"
"v_mfma_f32_16x16x32_bf16 v[116:119], a[228:231], v[72:75], v[116:119]\n"
"v_mfma_f32_16x16x32_bf16 v[116:119], a[232:235], v[76:79], v[116:119]\n"
"v_mfma_f32_16x16x32_bf16 v[116:119], a[236:239], v[80:83], v[116:119]\n"
"v_mfma_f32_16x16x32_bf16 v[116:119], a[240:243], v[84:87], v[116:119]\n"
"v_mfma_f32_16x16x32_bf16 v[116:119], a[244:247], v[88:91], v[116:119]\n"
"v_mfma_f32_16x16x32_bf16 v[116:119], a[248:251], v[92:95], v[116:119]\n"
"v_mfma_f32_16x16x32_bf16 v[120:123], a[252:255], v[104:107], 0\n"
"v_mfma_f32_16x16x32_bf16 v[120:123], v[24:27], v[72:75], v[120:123]\n"
"s_waitcnt lgkmcnt(0)\n"
"v_mfma_f32_16x16x32_bf16 v[120:123], v[28:31], v[76:79], v[120:123]\n"
"s_barrier\n"
"v_mfma_f32_16x16x32_bf16 v[120:123], v[32:35], v[80:83], v[120:123]\n"
"ds_read_b128 v[104:107], v2 offset:2048\n"
"ds_read_b128 v[124:127], v3 offset:512\n"
"v_mfma_f32_16x16x32_bf16 v[120:123], v[36:39], v[84:87], v[120:123]\n"
"ds_read_b128 v[128:131], v4 offset:3072\n"
"v_mfma_f32_16x16x32_bf16 v[120:123], v[40:43], v[88:91], v[120:123]\n"
"ds_read_b128 v[132:135], v2 offset:2304\n"
"ds_read_b128 v[136:139], v3 offset:768\n"
"v_mfma_f32_16x16x32_bf16 v[120:123], v[44:47], v[92:95], v[120:123]\n"
"ds_read_b128 v[140:143], v4 offset:3328\n"
"ds_read_b128 v[144:147], v2 offset:3584\n"
"ds_read_b128 v[148:151], v3 offset:1536\n"
"ds_read_b128 v[152:155], v4 offset:3584\n"
"ds_read_b128 v[156:159], v2 offset:3840\n"
"ds_read_b128 v[160:163], v3 offset:1792\n"
"ds_read_b128 v[164:167], v4 offset:3840\n"
"s_waitcnt lgkmcnt(9)\n"
"v_add_f32 v104, v104, v124\n"
"v_add_f32 v105, v105, v125\n"
"v_add_f32 v106, v106, v126\n"
"v_add_f32 v107, v107, v127\n"
"v_add_f32 v104, v104, v128\n"
"v_add_f32 v105, v105, v129\n"
"v_add_f32 v106, v106, v130\n"
"v_add_f32 v107, v107, v131\n"
"v_add_f32 v104, v104, v100\n"
"v_add_f32 v105, v105, v101\n"
"v_add_f32 v106, v106, v102\n"
"v_add_f32 v107, v107, v103\n"
"s_add_u32 s93, s50, 0x0\n"
"s_and_b32 s92, s93, 0xfbffffff\n"
"v_cvt_pk_bf16_f32 v128, v104, v105\n"
"v_cvt_pk_bf16_f32 v129, v106, v107\n"
"s_waitcnt lgkmcnt(6)\n"
"v_add_f32 v132, v132, v136\n"
"v_add_f32 v133, v133, v137\n"
"s_waitcnt vmcnt(0)\n"
"v_add_f32 v134, v134, v138\n"
"v_add_f32 v135, v135, v139\n"
"v_add_f32 v132, v132, v140\n"
"v_add_f32 v133, v133, v141\n"
"v_add_f32 v134, v134, v142\n"
"v_add_f32 v135, v135, v143\n"
"v_add_f32 v132, v132, v96\n"
"v_add_f32 v133, v133, v97\n"
"v_add_f32 v134, v134, v98\n"
"v_add_f32 v135, v135, v99\n"
"v_cvt_pk_bf16_f32 v130, v132, v133\n"
"buffer_store_dwordx2 v[0:1], v14, s[40:43], s95 offen offset:0 sc1\n"
"v_cvt_pk_bf16_f32 v131, v134, v135\n"
"buffer_store_dwordx4 v[128:131], v13, s[40:43], s92 offen sc1\n"
"s_waitcnt lgkmcnt(3)\n"
"v_add_f32 v144, v144, v148\n"
"v_add_f32 v145, v145, v149\n"
"v_add_f32 v146, v146, v150\n"
"v_add_f32 v147, v147, v151\n"
"v_add_f32 v144, v144, v152\n"
"v_add_f32 v145, v145, v153\n"
"v_add_f32 v146, v146, v154\n"
"v_add_f32 v147, v147, v155\n"
"v_add_f32 v144, v144, v116\n"
"v_add_f32 v145, v145, v117\n"
"v_add_f32 v146, v146, v118\n"
"v_add_f32 v147, v147, v119\n"
"v_cvt_pk_bf16_f32 v104, v144, v145\n"
"v_cvt_pk_bf16_f32 v105, v146, v147\n"
"s_waitcnt lgkmcnt(0)\n"
"v_add_f32 v156, v156, v160\n"
"v_add_f32 v157, v157, v161\n"
"v_add_f32 v158, v158, v162\n"
"v_add_f32 v159, v159, v163\n"
"v_add_f32 v156, v156, v164\n"
"v_add_f32 v157, v157, v165\n"
"v_add_f32 v158, v158, v166\n"
"v_add_f32 v159, v159, v167\n"
"v_add_f32 v156, v156, v120\n"
"v_add_f32 v157, v157, v121\n"
"v_add_f32 v158, v158, v122\n"
"v_add_f32 v159, v159, v123\n"
"v_cvt_pk_bf16_f32 v106, v156, v157\n"
"v_cvt_pk_bf16_f32 v107, v158, v159\n"
"buffer_store_dwordx4 v[104:107], v15, s[40:43], s92 offen sc1\n"
"s_waitcnt vmcnt(0)\n"
"buffer_store_dwordx2 v[0:1], v16, s[40:43], s93 offen offset:0 sc1\n"
"s_branch .LH_end\n"
".LH_t2:\n"
"v_mfma_f32_16x16x32_bf16 v[116:119], a[236:239], v[80:83], v[116:119]\n"
"v_mfma_f32_16x16x32_bf16 v[116:119], a[240:243], v[84:87], v[116:119]\n"
"v_mfma_f32_16x16x32_bf16 v[116:119], a[244:247], v[88:91], v[116:119]\n"
"v_mfma_f32_16x16x32_bf16 v[116:119], a[248:251], v[92:95], v[116:119]\n"
"v_mfma_f32_16x16x32_bf16 v[120:123], a[252:255], v[104:107], 0\n"
"v_mfma_f32_16x16x32_bf16 v[120:123], v[24:27], v[72:75], v[120:123]\n"
"s_waitcnt lgkmcnt(0)\n"
"v_mfma_f32_16x16x32_bf16 v[120:123], v[28:31], v[76:79], v[120:123]\n"
"s_barrier\n"
"v_mfma_f32_16x16x32_bf16 v[120:123], v[32:35], v[80:83], v[120:123]\n"
"ds_read_b128 v[104:107], v2 offset:2048\n"
"ds_read_b128 v[124:127], v3 offset:512\n"
"v_mfma_f32_16x16x32_bf16 v[120:123], v[36:39], v[84:87], v[120:123]\n"
"ds_read_b128 v[128:131], v4 offset:3072\n"
"v_mfma_f32_16x16x32_bf16 v[120:123], v[40:43], v[88:91], v[120:123]\n"
"ds_read_b128 v[132:135], v2 offset:2304\n"
"ds_read_b128 v[136:139], v3 offset:768\n"
"v_mfma_f32_16x16x32_bf16 v[120:123], v[44:47], v[92:95], v[120:123]\n"
"s_waitcnt vmcnt(8)\n"
"ds_read_b128 v[140:143], v4 offset:3328\n"
"v_mfma_f32_16x16x32_bf16 v[88:91], v[108:111], a[0:3], 0\n"
"ds_read_b128 v[144:147], v2 offset:3584\n"
"ds_read_b128 v[148:151], v3 offset:1536\n"
"v_mfma_f32_16x16x32_bf16 v[88:91], v[112:115], a[4:7], v[88:91]\n"
"s_waitcnt vmcnt(6)\n"
"ds_read_b128 v[152:155], v4 offset:3584\n"
"ds_read_b128 v[156:159], v2 offset:3840\n"
"v_mfma_f32_16x16x32_bf16 v[88:91], v[48:51], a[8:11], v[88:91]\n"
"ds_read_b128 v[160:163], v3 offset:1792\n"
"ds_read_b128 v[164:167], v4 offset:3840\n"
"v_mfma_f32_16x16x32_bf16 v[88:91], v[52:55], a[12:15], v[88:91]\n"
"s_waitcnt vmcnt(2)\n"
"v_mfma_f32_16x16x32_bf16 v[88:91], v[56:59], a[16:19], v[88:91]\n"
"s_waitcnt lgkmcnt(9)\n"
"v_add_f32 v104, v104, v124\n"
"v_mfma_f32_16x16x32_bf16 v[88:91], v[60:63], a[20:23], v[88:91]\n"
"s_waitcnt vmcnt(1)\n"
"v_add_f32 v105, v105, v125\n"
"v_add_f32 v106, v106, v126\n"
"v_mfma_f32_16x16x32_bf16 v[88:91], v[64:67], a[24:27], v[88:91]\n"
"v_add_f32 v107, v107, v127\n"
"v_add_f32 v104, v104, v128\n"
"v_mfma_f32_16x16x32_bf16 v[168:171], v[108:111], a[28:31], 0\n"
"v_mfma_f32_16x16x32_bf16 v[168:171], v[112:115], a[32:35], v[168:171]\n"
"ds_write_addtid_b32 v88 offset:2048\n"
"v_add_f32 v105, v105, v129\n"
"v_mfma_f32_16x16x32_bf16 v[168:171], v[48:51], a[36:39], v[168:171]\n"
"ds_write_addtid_b32 v89 offset:6688\n"
"v_add_f32 v106, v106, v130\n"
"v_mfma_f32_16x16x32_bf16 v[168:171], v[52:55], a[40:43], v[168:171]\n"
"ds_write_addtid_b32 v90 offset:11392\n"
"v_add_f32 v107, v107, v131\n"
"v_mfma_f32_16x16x32_bf16 v[168:171], v[56:59], a[44:47], v[168:171]\n"
"ds_write_addtid_b32 v91 offset:16032\n"
"v_add_f32 v104, v104, v100\n"
"v_mfma_f32_16x16x32_bf16 v[168:171], v[60:63], a[48:51], v[168:171]\n"
"v_add_f32 v105, v105, v101\n"
"v_mfma_f32_16x16x32_bf16 v[168:171], v[64:67], a[52:55], v[168:171]\n"
"v_add_f32 v106, v106, v102\n"
"v_add_f32 v107, v107, v103\n"
"v_mfma_f32_16x16x32_bf16 v[124:127], v[108:111], a[56:59], 0\n"
"s_add_u32 s93, s50, 0xffe80000\n"
"s_and_b32 s92, s93, 0xfbffffff\n"
"v_cvt_pk_bf16_f32 v128, v104, v105\n"
"v_mfma_f32_16x16x32_bf16 v[124:127], v[112:115], a[60:63], v[124:127]\n"
"ds_write_addtid_b32 v168 offset:2304\n"
"v_cvt_pk_bf16_f32 v129, v106, v107\n"
"v_mfma_f32_16x16x32_bf16 v[124:127], v[48:51], a[64:67], v[124:127]\n"
"s_waitcnt lgkmcnt(11)\n"
"ds_write_addtid_b32 v169 offset:6944\n"
"v_add_f32 v132, v132, v136\n"
"v_mfma_f32_16x16x32_bf16 v[124:127], v[52:55], a[68:71], v[124:127]\n"
"ds_write_addtid_b32 v170 offset:11648\n"
"v_add_f32 v133, v133, v137\n"
"v_mfma_f32_16x16x32_bf16 v[124:127], v[56:59], a[72:75], v[124:127]\n"
"s_waitcnt vmcnt(0)\n"
"ds_write_addtid_b32 v171 offset:16288\n"
"v_mfma_f32_16x16x32_bf16 v[124:127], v[60:63], a[76:79], v[124:127]\n"
"v_add_f32 v134, v134, v138\n"
"v_mfma_f32_16x16x32_bf16 v[124:127], v[64:67], a[80:83], v[124:127]\n"
"v_add_f32 v135, v135, v139\n"
"v_add_f32 v132, v132, v140\n"
"v_mfma_f32_16x16x32_bf16 v[100:103], v[108:111], a[84:87], 0\n"
"v_add_f32 v133, v133, v141\n"
"v_add_f32 v134, v134, v142\n"
"v_mfma_f32_16x16x32_bf16 v[100:103], v[112:115], a[88:91], v[100:103]\n"
"ds_write_addtid_b32 v124 offset:2560\n"
"v_add_f32 v135, v135, v143\n"
"v_mfma_f32_16x16x32_bf16 v[100:103], v[48:51], a[92:95], v[100:103]\n"
"ds_write_addtid_b32 v125 offset:7200\n"
"v_add_f32 v132, v132, v96\n"
"v_mfma_f32_16x16x32_bf16 v[100:103], v[52:55], a[96:99], v[100:103]\n"
"ds_write_addtid_b32 v126 offset:11904\n"
"v_add_f32 v133, v133, v97\n"
"v_mfma_f32_16x16x32_bf16 v[100:103], v[56:59], a[100:103], v[100:103]\n"
"ds_write_addtid_b32 v127 offset:16544\n"
"v_add_f32 v134, v134, v98\n"
"v_mfma_f32_16x16x32_bf16 v[100:103], v[60:63], a[104:107], v[100:103]\n"
"v_add_f32 v135, v135, v99\n"
"v_cvt_pk_bf16_f32 v130, v132, v133\n"
"v_mfma_f32_16x16x32_bf16 v[100:103], v[64:67], a[108:111], v[100:103]\n"
"buffer_store_dwordx2 v[0:1], v14, s[40:43], s95 offen offset:0 sc1\n"
"v_cvt_pk_bf16_f32 v131, v134, v135\n"
"v_mfma_f32_16x16x32_bf16 v[96:99], v[108:111], a[112:115], 0\n"
"buffer_store_dwordx4 v[128:131], v13, s[40:43], s92 offen sc1\n"
"v_mfma_f32_16x16x32_bf16 v[96:99], v[112:115], a[116:119], v[96:99]\n"
"s_waitcnt lgkmcnt(15)\n"
"ds_write_addtid_b32 v100 offset:2816\n"
"v_add_f32 v144, v144, v148\n"
"v_mfma_f32_16x16x32_bf16 v[96:99], v[48:51], a[120:123], v[96:99]\n"
"ds_write_addtid_b32 v101 offset:7456\n"
"v_add_f32 v145, v145, v149\n"
"v_mfma_f32_16x16x32_bf16 v[96:99], v[52:55], a[124:127], v[96:99]\n"
"ds_write_addtid_b32 v102 offset:12160\n"
"v_add_f32 v146, v146, v150\n"
"v_mfma_f32_16x16x32_bf16 v[96:99], v[56:59], a[128:131], v[96:99]\n"
"ds_write_addtid_b32 v103 offset:16800\n"
"v_add_f32 v147, v147, v151\n"
"v_mfma_f32_16x16x32_bf16 v[96:99], v[60:63], a[132:135], v[96:99]\n"
"v_add_f32 v144, v144, v152\n"
"v_add_f32 v145, v145, v153\n"
"v_mfma_f32_16x16x32_bf16 v[96:99], v[64:67], a[136:139], v[96:99]\n"
"v_add_f32 v146, v146, v154\n"
"v_add_f32 v147, v147, v155\n"
"v_mfma_f32_16x16x32_bf16 v[100:103], v[108:111], a[140:143], 0\n"
"v_add_f32 v144, v144, v116\n"
"v_add_f32 v145, v145, v117\n"
"v_mfma_f32_16x16x32_bf16 v[100:103], v[112:115], a[144:147], v[100:103]\n"
"ds_write_addtid_b32 v96 offset:3072\n"
"v_add_f32 v146, v146, v118\n"
"v_mfma_f32_16x16x32_bf16 v[100:103], v[48:51], a[148:151], v[100:103]\n"
"ds_write_addtid_b32 v97 offset:7712\n"
"v_add_f32 v147, v147, v119\n"
"v_mfma_f32_16x16x32_bf16 v[100:103], v[52:55], a[152:155], v[100:103]\n"
"ds_write_addtid_b32 v98 offset:12416\n"
"v_cvt_pk_bf16_f32 v104, v144, v145\n"
"v_mfma_f32_16x16x32_bf16 v[100:103], v[56:59], a[156:159], v[100:103]\n"
"ds_write_addtid_b32 v99 offset:17056\n"
"v_cvt_pk_bf16_f32 v105, v146, v147\n"
"v_mfma_f32_16x16x32_bf16 v[100:103], v[60:63], a[160:163], v[100:103]\n"
"s_waitcnt lgkmcnt(15)\n"
"v_add_f32 v156, v156, v160\n"
"v_add_f32 v157, v157, v161\n"
"v_mfma_f32_16x16x32_bf16 v[100:103], v[64:67], a[164:167], v[100:103]\n"
"v_add_f32 v158, v158, v162\n"
"v_add_f32 v159, v159, v163\n"
"v_mfma_f32_16x16x32_bf16 v[96:99], v[108:111], a[224:227], 0\n"
"v_add_f32 v156, v156, v164\n"
"v_add_f32 v157, v157, v165\n"
"v_mfma_f32_16x16x32_bf16 v[96:99], v[112:115], a[228:231], v[96:99]\n"
"ds_write_addtid_b32 v100 offset:3328\n"
"v_add_f32 v158, v158, v166\n"
"v_mfma_f32_16x16x32_bf16 v[96:99], v[48:51], a[232:235], v[96:99]\n"
"ds_write_addtid_b32 v101 offset:7968\n"
"v_add_f32 v159, v159, v167\n"
"v_mfma_f32_16x16x32_bf16 v[96:99], v[52:55], a[236:239], v[96:99]\n"
"ds_write_addtid_b32 v102 offset:12672\n"
"v_add_f32 v156, v156, v120\n"
"v_mfma_f32_16x16x32_bf16 v[96:99], v[56:59], a[240:243], v[96:99]\n"
"ds_write_addtid_b32 v103 offset:17312\n"
"v_add_f32 v157, v157, v121\n"
"v_mfma_f32_16x16x32_bf16 v[96:99], v[60:63], a[244:247], v[96:99]\n"
"v_add_f32 v158, v158, v122\n"
"v_add_f32 v159, v159, v123\n"
"v_mfma_f32_16x16x32_bf16 v[96:99], v[64:67], a[248:251], v[96:99]\n"
"v_cvt_pk_bf16_f32 v106, v156, v157\n"
"v_cvt_pk_bf16_f32 v107, v158, v159\n"
"v_mfma_f32_16x16x32_bf16 v[100:103], v[108:111], a[252:255], 0\n"
"buffer_store_dwordx4 v[104:107], v15, s[40:43], s92 offen sc1\n"
"v_mfma_f32_16x16x32_bf16 v[100:103], v[112:115], v[24:27], v[100:103]\n"
"ds_write_addtid_b32 v96 offset:3584\n"
"v_mfma_f32_16x16x32_bf16 v[100:103], v[48:51], v[28:31], v[100:103]\n"
"ds_write_addtid_b32 v97 offset:8224\n"
"v_mfma_f32_16x16x32_bf16 v[100:103], v[52:55], v[32:35], v[100:103]\n"
"ds_write_addtid_b32 v98 offset:12928\n"
"v_mfma_f32_16x16x32_bf16 v[100:103], v[56:59], v[36:39], v[100:103]\n"
"ds_write_addtid_b32 v99 offset:17568\n"
"v_mfma_f32_16x16x32_bf16 v[100:103], v[60:63], v[40:43], v[100:103]\n"
"v_mfma_f32_16x16x32_bf16 v[100:103], v[64:67], v[44:47], v[100:103]\n"
"v_mfma_f32_16x16x32_bf16 v[104:107], a[168:171], v[108:111], 0\n"
"v_mfma_f32_16x16x32_bf16 v[104:107], a[172:175], v[112:115], v[104:107]\n"
"ds_write_addtid_b32 v100 offset:3840\n"
"v_mfma_f32_16x16x32_bf16 v[104:107], a[176:179], v[48:51], v[104:107]\n"
"ds_write_addtid_b32 v101 offset:8480\n"
"v_mfma_f32_16x16x32_bf16 v[104:107], a[180:183], v[52:55], v[104:107]\n"
"ds_write_addtid_b32 v102 offset:13184\n"
"v_mfma_f32_16x16x32_bf16 v[104:107], a[184:187], v[56:59], v[104:107]\n"
"ds_write_addtid_b32 v103 offset:17824\n"
"v_mfma_f32_16x16x32_bf16 v[104:107], a[188:191], v[60:63], v[104:107]\n"
"v_mfma_f32_16x16x32_bf16 v[104:107], a[192:195], v[64:67], v[104:107]\n"
"v_mfma_f32_16x16x32_bf16 v[116:119], a[196:199], v[108:111], 0\n"
"v_mfma_f32_16x16x32_bf16 v[116:119], a[200:203], v[112:115], v[116:119]\n"
"s_waitcnt lgkmcnt(0)\n"
"v_mfma_f32_16x16x32_bf16 v[116:119], a[204:207], v[48:51], v[116:119]\n"
"s_barrier\n"
"v_mfma_f32_16x16x32_bf16 v[116:119], a[208:211], v[52:55], v[116:119]\n"
"ds_read_b128 v[108:111], v2 offset:0\n"
"ds_read_b128 v[112:115], v3 offset:2560\n"
"v_mfma_f32_16x16x32_bf16 v[116:119], a[212:215], v[56:59], v[116:119]\n"
"ds_read_b128 v[120:123], v4 offset:1024\n"
"ds_read_b128 v[124:127], v2 offset:256\n"
"v_mfma_f32_16x16x32_bf16 v[116:119], a[216:219], v[60:63], v[116:119]\n"
"ds_read_b128 v[128:131], v3 offset:2816\n"
"ds_read_b128 v[132:135], v4 offset:1280\n"
"v_mfma_f32_16x16x32_bf16 v[116:119], a[220:223], v[64:67], v[116:119]\n"
"s_waitcnt lgkmcnt(3)\n"
"v_add_f32 v108, v108, v112\n"
"v_add_f32 v109, v109, v113\n"
"v_add_f32 v110, v110, v114\n"
"v_add_f32 v111, v111, v115\n"
"v_add_f32 v108, v108, v120\n"
"v_add_f32 v109, v109, v121\n"
"v_add_f32 v110, v110, v122\n"
"v_add_f32 v111, v111, v123\n"
"v_add_f32 v108, v108, v104\n"
"v_add_f32 v109, v109, v105\n"
"v_add_f32 v110, v110, v106\n"
"v_add_f32 v111, v111, v107\n"
"s_add_u32 s95, s50, 0x0\n"
"s_and_b32 s92, s95, 0xfbffffff\n"
"v_cvt_pk_bf16_f32 v112, v108, v109\n"
"v_cvt_pk_bf16_f32 v113, v110, v111\n"
"s_waitcnt lgkmcnt(0)\n"
"v_add_f32 v124, v124, v128\n"
"v_add_f32 v125, v125, v129\n"
"v_add_f32 v126, v126, v130\n"
"v_add_f32 v127, v127, v131\n"
"v_add_f32 v124, v124, v132\n"
"v_add_f32 v125, v125, v133\n"
"v_add_f32 v126, v126, v134\n"
"v_add_f32 v127, v127, v135\n"
"v_add_f32 v124, v124, v116\n"
"v_add_f32 v125, v125, v117\n"
"v_add_f32 v126, v126, v118\n"
"v_add_f32 v127, v127, v119\n"
"v_cvt_pk_bf16_f32 v114, v124, v125\n"
"v_cvt_pk_bf16_f32 v115, v126, v127\n"
"s_waitcnt vmcnt(0)\n"
"buffer_store_dwordx2 v[0:1], v16, s[40:43], s93 offen offset:0 sc1\n"
"buffer_store_dwordx4 v[112:115], v13, s[40:43], s92 offen sc1\n"
"s_waitcnt vmcnt(0)\n"
"buffer_store_dwordx2 v[0:1], v14, s[40:43], s95 offen offset:0 sc1\n"
"s_branch .LH_end\n"
".LH_t3:\n"
"v_mfma_f32_16x16x32_bf16 v[104:107], a[192:195], v[64:67], v[104:107]\n"
"v_mfma_f32_16x16x32_bf16 v[116:119], a[196:199], v[108:111], 0\n"
"v_mfma_f32_16x16x32_bf16 v[116:119], a[200:203], v[112:115], v[116:119]\n"
"s_waitcnt lgkmcnt(0)\n"
"v_mfma_f32_16x16x32_bf16 v[116:119], a[204:207], v[48:51], v[116:119]\n"
"s_barrier\n"
"v_mfma_f32_16x16x32_bf16 v[116:119], a[208:211], v[52:55], v[116:119]\n"
"ds_read_b128 v[108:111], v2 offset:0\n"
"ds_read_b128 v[112:115], v3 offset:2560\n"
"v_mfma_f32_16x16x32_bf16 v[116:119], a[212:215], v[56:59], v[116:119]\n"
"ds_read_b128 v[120:123], v4 offset:1024\n"
"ds_read_b128 v[124:127], v2 offset:256\n"
"v_mfma_f32_16x16x32_bf16 v[116:119], a[216:219], v[60:63], v[116:119]\n"
"ds_read_b128 v[128:131], v3 offset:2816\n"
"ds_read_b128 v[132:135], v4 offset:1280\n"
"v_mfma_f32_16x16x32_bf16 v[116:119], a[220:223], v[64:67], v[116:119]\n"
"s_waitcnt vmcnt(10)\n"
"v_mfma_f32_16x16x32_bf16 v[56:59], v[68:71], a[0:3], 0\n"
"v_mfma_f32_16x16x32_bf16 v[56:59], v[72:75], a[4:7], v[56:59]\n"
"s_waitcnt vmcnt(8)\n"
"v_mfma_f32_16x16x32_bf16 v[56:59], v[76:79], a[8:11], v[56:59]\n"
"v_mfma_f32_16x16x32_bf16 v[56:59], v[80:83], a[12:15], v[56:59]\n"
"s_waitcnt vmcnt(5)\n"
"v_mfma_f32_16x16x32_bf16 v[56:59], v[84:87], a[16:19], v[56:59]\n"
"s_waitcnt lgkmcnt(3)\n"
"v_add_f32 v108, v108, v112\n"
"v_add_f32 v109, v109, v113\n"
"v_mfma_f32_16x16x32_bf16 v[56:59], v[88:91], a[20:23], v[56:59]\n"
"s_waitcnt vmcnt(4)\n"
"v_add_f32 v110, v110, v114\n"
"v_add_f32 v111, v111, v115\n"
"v_mfma_f32_16x16x32_bf16 v[56:59], v[92:95], a[24:27], v[56:59]\n"
"v_add_f32 v108, v108, v120\n"
"v_add_f32 v109, v109, v121\n"
"v_mfma_f32_16x16x32_bf16 v[64:67], v[68:71], a[28:31], 0\n"
"v_add_f32 v110, v110, v122\n"
"v_mfma_f32_16x16x32_bf16 v[64:67], v[72:75], a[32:35], v[64:67]\n"
"ds_write_addtid_b32 v56 offset:0\n"
"v_add_f32 v111, v111, v123\n"
"v_mfma_f32_16x16x32_bf16 v[64:67], v[76:79], a[36:39], v[64:67]\n"
"ds_write_addtid_b32 v57 offset:4640\n"
"v_add_f32 v108, v108, v104\n"
"v_mfma_f32_16x16x32_bf16 v[64:67], v[80:83], a[40:43], v[64:67]\n"
"ds_write_addtid_b32 v58 offset:9344\n"
"v_add_f32 v109, v109, v105\n"
"v_mfma_f32_16x16x32_bf16 v[64:67], v[84:87], a[44:47], v[64:67]\n"
"ds_write_addtid_b32 v59 offset:13984\n"
"v_add_f32 v110, v110, v106\n"
"v_mfma_f32_16x16x32_bf16 v[64:67], v[88:91], a[48:51], v[64:67]\n"
"v_add_f32 v111, v111, v107\n"
"v_mfma_f32_16x16x32_bf16 v[64:67], v[92:95], a[52:55], v[64:67]\n"
"s_add_u32 s95, s50, 0xffe80000\n"
"s_and_b32 s92, s95, 0xfbffffff\n"
"v_mfma_f32_16x16x32_bf16 v[104:107], v[68:71], a[56:59], 0\n"
"v_cvt_pk_bf16_f32 v112, v108, v109\n"
"v_cvt_pk_bf16_f32 v113, v110, v111\n"
"v_mfma_f32_16x16x32_bf16 v[104:107], v[72:75], a[60:63], v[104:107]\n"
"s_waitcnt lgkmcnt(4)\n"
"ds_write_addtid_b32 v64 offset:256\n"
"v_add_f32 v124, v124, v128\n"
"v_mfma_f32_16x16x32_bf16 v[104:107], v[76:79], a[64:67], v[104:107]\n"
"ds_write_addtid_b32 v65 offset:4896\n"
"v_add_f32 v125, v125, v129\n"
"v_mfma_f32_16x16x32_bf16 v[104:107], v[80:83], a[68:71], v[104:107]\n"
"ds_write_addtid_b32 v66 offset:9600\n"
"v_mfma_f32_16x16x32_bf16 v[104:107], v[84:87], a[72:75], v[104:107]\n"
"ds_write_addtid_b32 v67 offset:14240\n"
"v_add_f32 v126, v126, v130\n"
"v_mfma_f32_16x16x32_bf16 v[104:107], v[88:91], a[76:79], v[104:107]\n"
"v_add_f32 v127, v127, v131\n"
"v_add_f32 v124, v124, v132\n"
"v_mfma_f32_16x16x32_bf16 v[104:107], v[92:95], a[80:83], v[104:107]\n"
"v_add_f32 v125, v125, v133\n"
"v_add_f32 v126, v126, v134\n"
"v_mfma_f32_16x16x32_bf16 v[108:111], v[68:71], a[84:87], 0\n"
"v_add_f32 v127, v127, v135\n"
"v_add_f32 v124, v124, v116\n"
"v_mfma_f32_16x16x32_bf16 v[108:111], v[72:75], a[88:91], v[108:111]\n"
"ds_write_addtid_b32 v104 offset:512\n"
"v_add_f32 v125, v125, v117\n"
"v_mfma_f32_16x16x32_bf16 v[108:111], v[76:79], a[92:95], v[108:111]\n"
"ds_write_addtid_b32 v105 offset:5152\n"
"v_mfma_f32_16x16x32_bf16 v[108:111], v[80:83], a[96:99], v[108:111]\n"
"ds_write_addtid_b32 v106 offset:9856\n"
"v_add_f32 v126, v126, v118\n"
"v_mfma_f32_16x16x32_bf16 v[108:111], v[84:87], a[100:103], v[108:111]\n"
"ds_write_addtid_b32 v107 offset:14496\n"
"v_add_f32 v127, v127, v119\n"
"v_mfma_f32_16x16x32_bf16 v[108:111], v[88:91], a[104:107], v[108:111]\n"
"v_cvt_pk_bf16_f32 v114, v124, v125\n"
"v_cvt_pk_bf16_f32 v115, v126, v127\n"
"v_mfma_f32_16x16x32_bf16 v[108:111], v[92:95], a[108:111], v[108:111]\n"
"s_waitcnt vmcnt(1)\n"
"buffer_store_dwordx2 v[0:1], v16, s[40:43], s93 offen offset:0 sc1\n"
"v_mfma_f32_16x16x32_bf16 v[104:107], v[68:71], a[112:115], 0\n"
"v_mfma_f32_16x16x32_bf16 v[104:107], v[72:75], a[116:119], v[104:107]\n"
"ds_write_addtid_b32 v108 offset:768\n"
"v_mfma_f32_16x16x32_bf16 v[104:107], v[76:79], a[120:123], v[104:107]\n"
"buffer_store_dwordx4 v[112:115], v13, s[40:43], s92 offen sc1\n"
"ds_write_addtid_b32 v109 offset:5408\n"
"v_mfma_f32_16x16x32_bf16 v[104:107], v[80:83], a[124:127], v[104:107]\n"
"ds_write_addtid_b32 v110 offset:10112\n"
"v_mfma_f32_16x16x32_bf16 v[104:107], v[84:87], a[128:131], v[104:107]\n"
"s_waitcnt vmcnt(2)\n"
"ds_write_addtid_b32 v111 offset:14752\n"
"v_mfma_f32_16x16x32_bf16 v[104:107], v[88:91], a[132:135], v[104:107]\n"
"v_mfma_f32_16x16x32_bf16 v[104:107], v[92:95], a[136:139], v[104:107]\n"
"v_mfma_f32_16x16x32_bf16 v[108:111], v[68:71], a[140:143], 0\n"
"v_mfma_f32_16x16x32_bf16 v[108:111], v[72:75], a[144:147], v[108:111]\n"
"ds_write_addtid_b32 v104 offset:1024\n"
"v_mfma_f32_16x16x32_bf16 v[108:111], v[76:79], a[148:151], v[108:111]\n"
"ds_write_addtid_b32 v105 offset:5664\n"
"v_mfma_f32_16x16x32_bf16 v[108:111], v[80:83], a[152:155], v[108:111]\n"
"ds_write_addtid_b32 v106 offset:10368\n"
"v_mfma_f32_16x16x32_bf16 v[108:111], v[84:87], a[156:159], v[108:111]\n"
"ds_write_addtid_b32 v107 offset:15008\n"
"v_mfma_f32_16x16x32_bf16 v[108:111], v[88:91], a[160:163], v[108:111]\n"
"v_mfma_f32_16x16x32_bf16 v[108:111], v[92:95], a[164:167], v[108:111]\n"
"v_mfma_f32_16x16x32_bf16 v[104:107], v[68:71], a[224:227], 0\n"
"v_mfma_f32_16x16x32_bf16 v[104:107], v[72:75], a[228:231], v[104:107]\n"
"ds_write_addtid_b32 v108 offset:1280\n"
"v_mfma_f32_16x16x32_bf16 v[104:107], v[76:79], a[232:235], v[104:107]\n"
"ds_write_addtid_b32 v109 offset:5920\n"
"v_mfma_f32_16x16x32_bf16 v[104:107], v[80:83], a[236:239], v[104:107]\n"
"ds_write_addtid_b32 v110 offset:10624\n"
"v_mfma_f32_16x16x32_bf16 v[104:107], v[84:87], a[240:243], v[104:107]\n"
"ds_write_addtid_b32 v111 offset:15264\n"
"v_mfma_f32_16x16x32_bf16 v[104:107], v[88:91], a[244:247], v[104:107]\n"
"v_mfma_f32_16x16x32_bf16 v[104:107], v[92:95], a[248:251], v[104:107]\n"
"v_mfma_f32_16x16x32_bf16 v[120:123], v[68:71], a[252:255], 0\n"
"v_mfma_f32_16x16x32_bf16 v[120:123], v[72:75], v[24:27], v[120:123]\n"
"ds_write_addtid_b32 v104 offset:1536\n"
"v_mfma_f32_16x16x32_bf16 v[120:123], v[76:79], v[28:31], v[120:123]\n"
"ds_write_addtid_b32 v105 offset:6176\n"
"v_mfma_f32_16x16x32_bf16 v[120:123], v[80:83], v[32:35], v[120:123]\n"
"ds_write_addtid_b32 v106 offset:10880\n"
"v_mfma_f32_16x16x32_bf16 v[120:123], v[84:87], v[36:39], v[120:123]\n"
"ds_write_addtid_b32 v107 offset:15520\n"
"v_mfma_f32_16x16x32_bf16 v[120:123], v[88:91], v[40:43], v[120:123]\n"
"v_mfma_f32_16x16x32_bf16 v[120:123], v[92:95], v[44:47], v[120:123]\n"
"v_mfma_f32_16x16x32_bf16 v[108:111], a[168:171], v[68:71], 0\n"
"v_mfma_f32_16x16x32_bf16 v[108:111], a[172:175], v[72:75], v[108:111]\n"
"ds_write_addtid_b32 v120 offset:1792\n"
"v_mfma_f32_16x16x32_bf16 v[108:111], a[176:179], v[76:79], v[108:111]\n"
"ds_write_addtid_b32 v121 offset:6432\n"
"v_mfma_f32_16x16x32_bf16 v[108:111], a[180:183], v[80:83], v[108:111]\n"
"ds_write_addtid_b32 v122 offset:11136\n"
"v_mfma_f32_16x16x32_bf16 v[108:111], a[184:187], v[84:87], v[108:111]\n"
"ds_write_addtid_b32 v123 offset:15776\n"
"v_mfma_f32_16x16x32_bf16 v[108:111], a[188:191], v[88:91], v[108:111]\n"
"v_mfma_f32_16x16x32_bf16 v[108:111], a[192:195], v[92:95], v[108:111]\n"
"v_mfma_f32_16x16x32_bf16 v[120:123], a[196:199], v[68:71], 0\n"
"v_mfma_f32_16x16x32_bf16 v[120:123], a[200:203], v[72:75], v[120:123]\n"
"s_waitcnt lgkmcnt(0)\n"
"v_mfma_f32_16x16x32_bf16 v[120:123], a[204:207], v[76:79], v[120:123]\n"
"s_barrier\n"
"v_mfma_f32_16x16x32_bf16 v[120:123], a[208:211], v[80:83], v[120:123]\n"
"ds_read_b128 v[68:71], v2 offset:2048\n"
"v_mfma_f32_16x16x32_bf16 v[120:123], a[212:215], v[84:87], v[120:123]\n"
"ds_read_b128 v[124:127], v3 offset:512\n"
"ds_read_b128 v[128:131], v4 offset:3072\n"
"v_mfma_f32_16x16x32_bf16 v[120:123], a[216:219], v[88:91], v[120:123]\n"
"ds_read_b128 v[132:135], v2 offset:2304\n"
"ds_read_b128 v[136:139], v3 offset:768\n"
"v_mfma_f32_16x16x32_bf16 v[120:123], a[220:223], v[92:95], v[120:123]\n"
"ds_read_b128 v[140:143], v4 offset:3328\n"
"s_waitcnt lgkmcnt(3)\n"
"v_add_f32 v68, v68, v124\n"
"v_add_f32 v69, v69, v125\n"
"v_add_f32 v70, v70, v126\n"
"v_add_f32 v71, v71, v127\n"
"v_add_f32 v68, v68, v128\n"
"s_waitcnt vmcnt(0)\n"
"buffer_store_dwordx2 v[0:1], v14, s[40:43], s95 offen offset:0 sc1\n"
"v_add_f32 v69, v69, v129\n"
"v_add_f32 v70, v70, v130\n"
"v_add_f32 v71, v71, v131\n"
"v_add_f32 v68, v68, v108\n"
"v_add_f32 v69, v69, v109\n"
"v_add_f32 v70, v70, v110\n"
"v_add_f32 v71, v71, v111\n"
"s_add_u32 s93, s50, 0x0\n"
"s_and_b32 s95, s93, 0xfbffffff\n"
"v_cvt_pk_bf16_f32 v108, v68, v69\n"
"v_cvt_pk_bf16_f32 v109, v70, v71\n"
"s_waitcnt lgkmcnt(0)\n"
"v_add_f32 v132, v132, v136\n"
"v_add_f32 v133, v133, v137\n"
"v_add_f32 v134, v134, v138\n"
"v_add_f32 v135, v135, v139\n"
"v_add_f32 v132, v132, v140\n"
"v_add_f32 v133, v133, v141\n"
"v_add_f32 v134, v134, v142\n"
"v_add_f32 v135, v135, v143\n"
"v_add_f32 v132, v132, v120\n"
"v_add_f32 v133, v133, v121\n"
"v_add_f32 v134, v134, v122\n"
"v_add_f32 v135, v135, v123\n"
"v_cvt_pk_bf16_f32 v110, v132, v133\n"
"v_cvt_pk_bf16_f32 v111, v134, v135\n"
"buffer_store_dwordx4 v[108:111], v13, s[40:43], s95 offen sc1\n"
"s_waitcnt vmcnt(0)\n"
"buffer_store_dwordx2 v[0:1], v14, s[40:43], s93 offen offset:0 sc1\n"
"s_branch .LH_end\n"
".LH_end:\n"
"s_waitcnt vmcnt(0) lgkmcnt(0)\n"
"s_endpgm\n"
      :: "{v2}"((unsigned)(lds0 + ((wid + 3) & 3) * 18592 + trp)), "{v3}"((unsigned)(lds0 + ((wid + 2) & 3) * 18592 + trp)), "{v4}"((unsigned)(lds0 + ((wid + 1) & 3) * 18592 + trp)), "{v5}"((unsigned)(lane * 16 + f0 * 1024 + ((0 + rot) % 7) * 1024 + 134316032)), "{v6}"((unsigned)(lane * 16 + f0 * 1024 + ((1 + rot) % 7) * 1024 + 134316032)), "{v7}"((unsigned)(lane * 16 + f0 * 1024 + ((2 + rot) % 7) * 1024 + 134316032)), "{v8}"((unsigned)(lane * 16 + f0 * 1024 + ((3 + rot) % 7) * 1024 + 134316032)), "{v9}"((unsigned)(lane * 16 + f0 * 1024 + ((4 + rot) % 7) * 1024 + 134316032)), "{v10}"((unsigned)(lane * 16 + f0 * 1024 + ((5 + rot) % 7) * 1024 + 134316032)), "{v11}"((unsigned)(lane * 16 + f0 * 1024 + ((6 + rot) % 7) * 1024 + 134316032)), "{v12}"((unsigned)((f0 + lane % 7) * 8 + kq * 32 + 353024 + H_GL * 1572864)), "{v13}"((unsigned)(lane * 16 + pair * 4096 + kq * 1024 + 212992)), "{v14}"((unsigned)(lane ? DROP : pair * 64 + kq * 8 + 354304)), "{v15}"((unsigned)(lane * 16 + (pg0 + 4) * 4096 + kq * 1024 + 212992)), "{v16}"((unsigned)(lane > 1 ? DROP : (lane ? pg0 + 4 : pair) * 64 + kq * 8 + 354304)), "{v17}"((unsigned)((lane & 15) * 8 + 67462912)), "{v18}"((unsigned)(lane * 16 + 67457024)), "{v19}"((unsigned)((lane & 24) ? DROP : (2 * (lane & 7) + (lane >> 5)) * 16 + 67461120)), "{v20}"((unsigned)(0x3727c5ac)), "{v48}"((unsigned)(vT + tsw)), "{v49}"((unsigned)(vT + (tsw ^ 8))), "{v50}"((unsigned)(vD + ((0 + rot) % 7) * 65536)), "{v51}"((unsigned)(vD + ((1 + rot) % 7) * 65536)), "{v52}"((unsigned)(vD + ((2 + rot) % 7) * 65536)), "{v53}"((unsigned)(vD + ((3 + rot) % 7) * 65536)), "{v54}"((unsigned)(vD + ((4 + rot) % 7) * 65536)), "{v55}"((unsigned)(vD + ((5 + rot) % 7) * 65536)), "{v56}"((unsigned)(vD + ((6 + rot) % 7) * 65536)), "{s[40:43]}"(rS), "{s[44:47]}"(rW), "{s[48:49]}"(wsp), "{s50}"((unsigned)(sA)), "{s51}"((unsigned)(sLo)), "{s52}"((unsigned)(sHi)), "{s53}"((unsigned)(sEx)), "{s54}"((unsigned)(sEnt)), "{s55}"((unsigned)(sLd)), "{s[56:57]}"(wsb + (sLo & 0x3ffffff)), "{s59}"((unsigned)(lds0 + wid * 18592)), "{s62}"((unsigned)(0x3a800000)), "{s66}"((unsigned)(sW0 + 64 * ((wid + 1) & 3))), "{s67}"((unsigned)(sW0 + 64 * ((wid + 2) & 3))), "{s68}"((unsigned)(sW0 + 64 * ((wid + 3) & 3))), "{s69}"((unsigned)(sW0 + 64 * ((wid + 4) & 3))), "{s70}"((unsigned)(sW0 + 256))
      : "v255", "a255", "s95", "vcc", "scc", "memory");

    } else if (grp < 4) {
      const unsigned sEx = sHi + G_EX * 3 * RS, sEnt = -wid & 3;
    asm volatile(
"s_nop 4\n"
"v_mov_b32 v186, 0\n"
"global_load_dwordx2 v[0:1], v186, s[48:49] sc1\n"
"v_mov_b32 v188, 0x3f803f80\n"
"v_mov_b32 v189, 0x3f803f80\n"
"v_mov_b32 v190, 0x3f803f80\n"
"v_mov_b32 v191, 0x3f803f80\n"
"s_add_u32 m0, s55, 0\n"
"s_add_u32 s60, s62, 0\n"
"buffer_load_dwordx4 v46, s[44:47], s60 offen lds\n"
"s_add_u32 m0, s55, 1024\n"
"s_add_u32 s60, s62, 32768\n"
"buffer_load_dwordx4 v46, s[44:47], s60 offen lds\n"
"s_add_u32 m0, s55, 2048\n"
"s_add_u32 s60, s63, 0\n"
"buffer_load_dwordx4 v46, s[44:47], s60 offen lds\n"
"s_add_u32 m0, s55, 3072\n"
"s_add_u32 s60, s63, 32768\n"
"buffer_load_dwordx4 v46, s[44:47], s60 offen lds\n"
"s_add_u32 m0, s55, 4096\n"
"s_add_u32 s60, s64, 0\n"
"buffer_load_dwordx4 v46, s[44:47], s60 offen lds\n"
"s_add_u32 m0, s55, 5120\n"
"s_add_u32 s60, s64, 32768\n"
"buffer_load_dwordx4 v46, s[44:47], s60 offen lds\n"
"s_add_u32 m0, s55, 6144\n"
"s_add_u32 s60, s65, 0\n"
"buffer_load_dwordx4 v46, s[44:47], s60 offen lds\n"
"s_add_u32 m0, s55, 7168\n"
"s_add_u32 s60, s65, 32768\n"
"buffer_load_dwordx4 v46, s[44:47], s60 offen lds\n"
"s_add_u32 m0, s55, 8192\n"
"s_add_u32 s60, s66, 0\n"
"buffer_load_dwordx4 v46, s[44:47], s60 offen lds\n"
"s_add_u32 m0, s55, 9216\n"
"s_add_u32 s60, s66, 32768\n"
"buffer_load_dwordx4 v46, s[44:47], s60 offen lds\n"
"s_add_u32 m0, s55, 10240\n"
"s_add_u32 s60, s62, 0\n"
"buffer_load_dwordx4 v47, s[44:47], s60 offen lds\n"
"s_add_u32 m0, s55, 11264\n"
"s_add_u32 s60, s62, 32768\n"
"buffer_load_dwordx4 v47, s[44:47], s60 offen lds\n"
"s_add_u32 m0, s55, 12288\n"
"s_add_u32 s60, s63, 0\n"
"buffer_load_dwordx4 v47, s[44:47], s60 offen lds\n"
"s_add_u32 m0, s55, 13312\n"
"s_add_u32 s60, s63, 32768\n"
"buffer_load_dwordx4 v47, s[44:47], s60 offen lds\n"
"s_add_u32 m0, s55, 14336\n"
"s_add_u32 s60, s64, 0\n"
"buffer_load_dwordx4 v47, s[44:47], s60 offen lds\n"
"s_add_u32 m0, s55, 15360\n"
"s_add_u32 s60, s64, 32768\n"
"buffer_load_dwordx4 v47, s[44:47], s60 offen lds\n"
"s_add_u32 m0, s55, 16384\n"
"s_add_u32 s60, s65, 0\n"
"buffer_load_dwordx4 v47, s[44:47], s60 offen lds\n"
"s_add_u32 m0, s55, 17408\n"
"s_add_u32 s60, s65, 32768\n"
"buffer_load_dwordx4 v47, s[44:47], s60 offen lds\n"
"s_add_u32 m0, s55, 18432\n"
"s_add_u32 s60, s66, 0\n"
"buffer_load_dwordx4 v47, s[44:47], s60 offen lds\n"
"s_add_u32 m0, s55, 19456\n"
"s_add_u32 s60, s66, 32768\n"
"buffer_load_dwordx4 v47, s[44:47], s60 offen lds\n"
"s_add_u32 m0, s55, 20480\n"
"s_add_u32 s60, s62, 0\n"
"buffer_load_dwordx4 v48, s[44:47], s60 offen lds\n"
"s_add_u32 m0, s55, 21504\n"
"s_add_u32 s60, s62, 32768\n"
"buffer_load_dwordx4 v48, s[44:47], s60 offen lds\n"
"s_add_u32 m0, s55, 22528\n"
"s_add_u32 s60, s63, 0\n"
"buffer_load_dwordx4 v48, s[44:47], s60 offen lds\n"
"s_add_u32 m0, s55, 23552\n"
"s_add_u32 s60, s63, 32768\n"
"buffer_load_dwordx4 v48, s[44:47], s60 offen lds\n"
"s_add_u32 m0, s55, 24576\n"
"s_add_u32 s60, s64, 0\n"
"buffer_load_dwordx4 v48, s[44:47], s60 offen lds\n"
"s_add_u32 m0, s55, 25600\n"
"s_add_u32 s60, s64, 32768\n"
"buffer_load_dwordx4 v48, s[44:47], s60 offen lds\n"
"s_add_u32 m0, s55, 26624\n"
"s_add_u32 s60, s65, 0\n"
"buffer_load_dwordx4 v48, s[44:47], s60 offen lds\n"
"s_add_u32 m0, s55, 27648\n"
"s_add_u32 s60, s65, 32768\n"
"buffer_load_dwordx4 v48, s[44:47], s60 offen lds\n"
"s_add_u32 m0, s55, 28672\n"
"s_add_u32 s60, s66, 0\n"
"buffer_load_dwordx4 v48, s[44:47], s60 offen lds\n"
"s_add_u32 m0, s55, 29696\n"
"s_add_u32 s60, s66, 32768\n"
"buffer_load_dwordx4 v48, s[44:47], s60 offen lds\n"
"s_add_u32 m0, s55, 30720\n"
"s_add_u32 s60, s62, 0\n"
"buffer_load_dwordx4 v49, s[44:47], s60 offen lds\n"
"s_add_u32 m0, s55, 31744\n"
"s_add_u32 s60, s62, 32768\n"
"buffer_load_dwordx4 v49, s[44:47], s60 offen lds\n"
"s_add_u32 m0, s55, 32768\n"
"s_add_u32 s60, s63, 0\n"
"buffer_load_dwordx4 v49, s[44:47], s60 offen lds\n"
"s_add_u32 m0, s55, 33792\n"
"s_add_u32 s60, s63, 32768\n"
"buffer_load_dwordx4 v49, s[44:47], s60 offen lds\n"
"s_add_u32 m0, s55, 34816\n"
"s_add_u32 s60, s64, 0\n"
"buffer_load_dwordx4 v49, s[44:47], s60 offen lds\n"
"s_add_u32 m0, s55, 35840\n"
"s_add_u32 s60, s64, 32768\n"
"buffer_load_dwordx4 v49, s[44:47], s60 offen lds\n"
"s_add_u32 m0, s55, 36864\n"
"s_add_u32 s60, s65, 0\n"
"buffer_load_dwordx4 v49, s[44:47], s60 offen lds\n"
"s_add_u32 m0, s55, 37888\n"
"s_add_u32 s60, s65, 32768\n"
"buffer_load_dwordx4 v49, s[44:47], s60 offen lds\n"
"s_add_u32 m0, s55, 38912\n"
"s_add_u32 s60, s66, 0\n"
"buffer_load_dwordx4 v49, s[44:47], s60 offen lds\n"
"s_add_u32 m0, s55, 39936\n"
"s_add_u32 s60, s66, 32768\n"
"buffer_load_dwordx4 v49, s[44:47], s60 offen lds\n"
"s_waitcnt vmcnt(38)\n"
"ds_read_b64_tr_b16 a[0:1], v44 offset:0\n"
"ds_read_b64_tr_b16 a[2:3], v44 offset:256\n"
"ds_read_b64_tr_b16 a[28:29], v45 offset:0\n"
"ds_read_b64_tr_b16 a[30:31], v45 offset:256\n"
"v_add_u32 v0, 2, v0\n"
"v_add_u32 v1, 0x9e3779b9, v1\n"
"v_or_b32 v0, 1, v0\n"
"v_readfirstlane_b32 s49, v1\n"
"v_readfirstlane_b32 s48, v0\n"
"s_waitcnt vmcnt(36)\n"
"ds_read_b64_tr_b16 a[56:57], v44 offset:2048\n"
"ds_read_b64_tr_b16 a[58:59], v44 offset:2304\n"
"ds_read_b64_tr_b16 a[84:85], v45 offset:2048\n"
"ds_read_b64_tr_b16 a[86:87], v45 offset:2304\n"
"s_waitcnt lgkmcnt(4)\n"
"s_add_u32 m0, s55, 0\n"
"s_add_u32 s60, s62, 0\n"
"buffer_load_dwordx4 v50, s[44:47], s60 offen lds\n"
"s_add_u32 m0, s55, 1024\n"
"s_add_u32 s60, s62, 32768\n"
"buffer_load_dwordx4 v50, s[44:47], s60 offen lds\n"
"s_waitcnt vmcnt(36)\n"
"ds_read_b64_tr_b16 a[112:113], v44 offset:4096\n"
"ds_read_b64_tr_b16 a[114:115], v44 offset:4352\n"
"ds_read_b64_tr_b16 a[140:141], v45 offset:4096\n"
"ds_read_b64_tr_b16 a[142:143], v45 offset:4352\n"
"s_waitcnt lgkmcnt(4)\n"
"s_add_u32 m0, s55, 2048\n"
"s_add_u32 s60, s63, 0\n"
"buffer_load_dwordx4 v50, s[44:47], s60 offen lds\n"
"s_add_u32 m0, s55, 3072\n"
"s_add_u32 s60, s63, 32768\n"
"buffer_load_dwordx4 v50, s[44:47], s60 offen lds\n"
"s_waitcnt vmcnt(36)\n"
"ds_read_b64_tr_b16 a[168:169], v44 offset:6144\n"
"ds_read_b64_tr_b16 a[170:171], v44 offset:6400\n"
"ds_read_b64_tr_b16 a[196:197], v45 offset:6144\n"
"ds_read_b64_tr_b16 a[198:199], v45 offset:6400\n"
"s_waitcnt lgkmcnt(4)\n"
"s_add_u32 m0, s55, 4096\n"
"s_add_u32 s60, s64, 0\n"
"buffer_load_dwordx4 v50, s[44:47], s60 offen lds\n"
"s_add_u32 m0, s55, 5120\n"
"s_add_u32 s60, s64, 32768\n"
"buffer_load_dwordx4 v50, s[44:47], s60 offen lds\n"
"s_waitcnt vmcnt(36)\n"
"ds_read_b64_tr_b16 a[224:225], v44 offset:8192\n"
"ds_read_b64_tr_b16 a[226:227], v44 offset:8448\n"
"ds_read_b64_tr_b16 a[252:253], v45 offset:8192\n"
"ds_read_b64_tr_b16 a[254:255], v45 offset:8448\n"
"s_waitcnt lgkmcnt(4)\n"
"s_add_u32 m0, s55, 6144\n"
"s_add_u32 s60, s65, 0\n"
"buffer_load_dwordx4 v50, s[44:47], s60 offen lds\n"
"s_add_u32 m0, s55, 7168\n"
"s_add_u32 s60, s65, 32768\n"
"buffer_load_dwordx4 v50, s[44:47], s60 offen lds\n"
"s_waitcnt vmcnt(36)\n"
"ds_read_b64_tr_b16 a[4:5], v44 offset:10240\n"
"ds_read_b64_tr_b16 a[6:7], v44 offset:10496\n"
"ds_read_b64_tr_b16 a[32:33], v45 offset:10240\n"
"ds_read_b64_tr_b16 a[34:35], v45 offset:10496\n"
"s_waitcnt lgkmcnt(4)\n"
"s_add_u32 m0, s55, 8192\n"
"s_add_u32 s60, s66, 0\n"
"buffer_load_dwordx4 v50, s[44:47], s60 offen lds\n"
"s_add_u32 m0, s55, 9216\n"
"s_add_u32 s60, s66, 32768\n"
"buffer_load_dwordx4 v50, s[44:47], s60 offen lds\n"
"s_waitcnt vmcnt(36)\n"
"ds_read_b64_tr_b16 a[60:61], v44 offset:12288\n"
"ds_read_b64_tr_b16 a[62:63], v44 offset:12544\n"
"ds_read_b64_tr_b16 a[88:89], v45 offset:12288\n"
"ds_read_b64_tr_b16 a[90:91], v45 offset:12544\n"
"s_waitcnt lgkmcnt(4)\n"
"s_add_u32 m0, s55, 10240\n"
"s_add_u32 s60, s62, 0\n"
"buffer_load_dwordx4 v51, s[44:47], s60 offen lds\n"
"s_add_u32 m0, s55, 11264\n"
"s_add_u32 s60, s62, 32768\n"
"buffer_load_dwordx4 v51, s[44:47], s60 offen lds\n"
"s_waitcnt vmcnt(36)\n"
"ds_read_b64_tr_b16 a[116:117], v44 offset:14336\n"
"ds_read_b64_tr_b16 a[118:119], v44 offset:14592\n"
"ds_read_b64_tr_b16 a[144:145], v45 offset:14336\n"
"ds_read_b64_tr_b16 a[146:147], v45 offset:14592\n"
"s_waitcnt lgkmcnt(4)\n"
"s_add_u32 m0, s55, 12288\n"
"s_add_u32 s60, s63, 0\n"
"buffer_load_dwordx4 v51, s[44:47], s60 offen lds\n"
"s_add_u32 m0, s55, 13312\n"
"s_add_u32 s60, s63, 32768\n"
"buffer_load_dwordx4 v51, s[44:47], s60 offen lds\n"
"s_waitcnt vmcnt(36)\n"
"ds_read_b64_tr_b16 a[172:173], v44 offset:16384\n"
"ds_read_b64_tr_b16 a[174:175], v44 offset:16640\n"
"ds_read_b64_tr_b16 a[200:201], v45 offset:16384\n"
"ds_read_b64_tr_b16 a[202:203], v45 offset:16640\n"
"s_waitcnt lgkmcnt(4)\n"
"s_add_u32 m0, s55, 14336\n"
"s_add_u32 s60, s64, 0\n"
"buffer_load_dwordx4 v51, s[44:47], s60 offen lds\n"
"s_add_u32 m0, s55, 15360\n"
"s_add_u32 s60, s64, 32768\n"
"buffer_load_dwordx4 v51, s[44:47], s60 offen lds\n"
"s_waitcnt vmcnt(36)\n"
"ds_read_b64_tr_b16 a[228:229], v44 offset:18432\n"
"ds_read_b64_tr_b16 a[230:231], v44 offset:18688\n"
"ds_read_b64_tr_b16 v[20:21], v45 offset:18432\n"
"ds_read_b64_tr_b16 v[22:23], v45 offset:18688\n"
"s_waitcnt lgkmcnt(4)\n"
"s_add_u32 m0, s55, 16384\n"
"s_add_u32 s60, s65, 0\n"
"buffer_load_dwordx4 v51, s[44:47], s60 offen lds\n"
"s_add_u32 m0, s55, 17408\n"
"s_add_u32 s60, s65, 32768\n"
"buffer_load_dwordx4 v51, s[44:47], s60 offen lds\n"
"s_waitcnt vmcnt(36)\n"
"ds_read_b64_tr_b16 a[8:9], v44 offset:20480\n"
"ds_read_b64_tr_b16 a[10:11], v44 offset:20736\n"
"ds_read_b64_tr_b16 a[36:37], v45 offset:20480\n"
"ds_read_b64_tr_b16 a[38:39], v45 offset:20736\n"
"s_waitcnt lgkmcnt(4)\n"
"s_add_u32 m0, s55, 18432\n"
"s_add_u32 s60, s66, 0\n"
"buffer_load_dwordx4 v51, s[44:47], s60 offen lds\n"
"s_add_u32 m0, s55, 19456\n"
"s_add_u32 s60, s66, 32768\n"
"buffer_load_dwordx4 v51, s[44:47], s60 offen lds\n"
"s_waitcnt vmcnt(36)\n"
"ds_read_b64_tr_b16 a[64:65], v44 offset:22528\n"
"ds_read_b64_tr_b16 a[66:67], v44 offset:22784\n"
"ds_read_b64_tr_b16 a[92:93], v45 offset:22528\n"
"ds_read_b64_tr_b16 a[94:95], v45 offset:22784\n"
"s_waitcnt lgkmcnt(4)\n"
"s_add_u32 m0, s55, 20480\n"
"s_add_u32 s60, s62, 0\n"
"buffer_load_dwordx4 v52, s[44:47], s60 offen lds\n"
"s_add_u32 m0, s55, 21504\n"
"s_add_u32 s60, s62, 32768\n"
"buffer_load_dwordx4 v52, s[44:47], s60 offen lds\n"
"s_waitcnt vmcnt(36)\n"
"ds_read_b64_tr_b16 a[120:121], v44 offset:24576\n"
"ds_read_b64_tr_b16 a[122:123], v44 offset:24832\n"
"ds_read_b64_tr_b16 a[148:149], v45 offset:24576\n"
"ds_read_b64_tr_b16 a[150:151], v45 offset:24832\n"
"s_waitcnt lgkmcnt(4)\n"
"s_add_u32 m0, s55, 22528\n"
"s_add_u32 s60, s63, 0\n"
"buffer_load_dwordx4 v52, s[44:47], s60 offen lds\n"
"s_add_u32 m0, s55, 23552\n"
"s_add_u32 s60, s63, 32768\n"
"buffer_load_dwordx4 v52, s[44:47], s60 offen lds\n"
"s_waitcnt vmcnt(36)\n"
"ds_read_b64_tr_b16 a[176:177], v44 offset:26624\n"
"ds_read_b64_tr_b16 a[178:179], v44 offset:26880\n"
"ds_read_b64_tr_b16 a[204:205], v45 offset:26624\n"
"ds_read_b64_tr_b16 a[206:207], v45 offset:26880\n"
"s_waitcnt lgkmcnt(4)\n"
"s_add_u32 m0, s55, 24576\n"
"s_add_u32 s60, s64, 0\n"
"buffer_load_dwordx4 v52, s[44:47], s60 offen lds\n"
"s_add_u32 m0, s55, 25600\n"
"s_add_u32 s60, s64, 32768\n"
"buffer_load_dwordx4 v52, s[44:47], s60 offen lds\n"
"s_waitcnt vmcnt(36)\n"
"ds_read_b64_tr_b16 a[232:233], v44 offset:28672\n"
"ds_read_b64_tr_b16 a[234:235], v44 offset:28928\n"
"ds_read_b64_tr_b16 v[24:25], v45 offset:28672\n"
"ds_read_b64_tr_b16 v[26:27], v45 offset:28928\n"
"s_waitcnt lgkmcnt(4)\n"
"s_add_u32 m0, s55, 26624\n"
"s_add_u32 s60, s65, 0\n"
"buffer_load_dwordx4 v52, s[44:47], s60 offen lds\n"
"s_add_u32 m0, s55, 27648\n"
"s_add_u32 s60, s65, 32768\n"
"buffer_load_dwordx4 v52, s[44:47], s60 offen lds\n"
"s_waitcnt vmcnt(36)\n"
"ds_read_b64_tr_b16 a[12:13], v44 offset:30720\n"
"ds_read_b64_tr_b16 a[14:15], v44 offset:30976\n"
"ds_read_b64_tr_b16 a[40:41], v45 offset:30720\n"
"ds_read_b64_tr_b16 a[42:43], v45 offset:30976\n"
"s_waitcnt lgkmcnt(4)\n"
"s_add_u32 m0, s55, 28672\n"
"s_add_u32 s60, s66, 0\n"
"buffer_load_dwordx4 v52, s[44:47], s60 offen lds\n"
"s_add_u32 m0, s55, 29696\n"
"s_add_u32 s60, s66, 32768\n"
"buffer_load_dwordx4 v52, s[44:47], s60 offen lds\n"
"s_waitcnt vmcnt(36)\n"
"ds_read_b64_tr_b16 a[68:69], v44 offset:32768\n"
"ds_read_b64_tr_b16 a[70:71], v44 offset:33024\n"
"ds_read_b64_tr_b16 a[96:97], v45 offset:32768\n"
"ds_read_b64_tr_b16 a[98:99], v45 offset:33024\n"
"s_waitcnt lgkmcnt(4)\n"
"s_waitcnt vmcnt(34)\n"
"ds_read_b64_tr_b16 a[124:125], v44 offset:34816\n"
"ds_read_b64_tr_b16 a[126:127], v44 offset:35072\n"
"ds_read_b64_tr_b16 a[152:153], v45 offset:34816\n"
"ds_read_b64_tr_b16 a[154:155], v45 offset:35072\n"
"s_waitcnt lgkmcnt(4)\n"
"s_waitcnt vmcnt(32)\n"
"ds_read_b64_tr_b16 a[180:181], v44 offset:36864\n"
"ds_read_b64_tr_b16 a[182:183], v44 offset:37120\n"
"ds_read_b64_tr_b16 a[208:209], v45 offset:36864\n"
"ds_read_b64_tr_b16 a[210:211], v45 offset:37120\n"
"s_waitcnt lgkmcnt(4)\n"
"s_waitcnt vmcnt(30)\n"
"ds_read_b64_tr_b16 a[236:237], v44 offset:38912\n"
"ds_read_b64_tr_b16 a[238:239], v44 offset:39168\n"
"ds_read_b64_tr_b16 v[28:29], v45 offset:38912\n"
"ds_read_b64_tr_b16 v[30:31], v45 offset:39168\n"
"s_waitcnt lgkmcnt(4)\n"
"s_waitcnt vmcnt(28)\n"
"ds_read_b64_tr_b16 a[16:17], v44 offset:0\n"
"ds_read_b64_tr_b16 a[18:19], v44 offset:256\n"
"ds_read_b64_tr_b16 a[44:45], v45 offset:0\n"
"ds_read_b64_tr_b16 a[46:47], v45 offset:256\n"
"s_waitcnt lgkmcnt(4)\n"
"s_waitcnt vmcnt(26)\n"
"ds_read_b64_tr_b16 a[72:73], v44 offset:2048\n"
"ds_read_b64_tr_b16 a[74:75], v44 offset:2304\n"
"ds_read_b64_tr_b16 a[100:101], v45 offset:2048\n"
"ds_read_b64_tr_b16 a[102:103], v45 offset:2304\n"
"s_waitcnt lgkmcnt(4)\n"
"s_waitcnt vmcnt(24)\n"
"ds_read_b64_tr_b16 a[128:129], v44 offset:4096\n"
"ds_read_b64_tr_b16 a[130:131], v44 offset:4352\n"
"ds_read_b64_tr_b16 a[156:157], v45 offset:4096\n"
"ds_read_b64_tr_b16 a[158:159], v45 offset:4352\n"
"s_waitcnt lgkmcnt(4)\n"
"s_waitcnt vmcnt(22)\n"
"ds_read_b64_tr_b16 a[184:185], v44 offset:6144\n"
"ds_read_b64_tr_b16 a[186:187], v44 offset:6400\n"
"ds_read_b64_tr_b16 a[212:213], v45 offset:6144\n"
"ds_read_b64_tr_b16 a[214:215], v45 offset:6400\n"
"s_waitcnt lgkmcnt(4)\n"
"s_waitcnt vmcnt(20)\n"
"ds_read_b64_tr_b16 a[240:241], v44 offset:8192\n"
"ds_read_b64_tr_b16 a[242:243], v44 offset:8448\n"
"ds_read_b64_tr_b16 v[32:33], v45 offset:8192\n"
"ds_read_b64_tr_b16 v[34:35], v45 offset:8448\n"
"s_waitcnt lgkmcnt(4)\n"
"s_waitcnt vmcnt(18)\n"
"ds_read_b64_tr_b16 a[20:21], v44 offset:10240\n"
"ds_read_b64_tr_b16 a[22:23], v44 offset:10496\n"
"ds_read_b64_tr_b16 a[48:49], v45 offset:10240\n"
"ds_read_b64_tr_b16 a[50:51], v45 offset:10496\n"
"s_waitcnt lgkmcnt(4)\n"
"s_waitcnt vmcnt(16)\n"
"ds_read_b64_tr_b16 a[76:77], v44 offset:12288\n"
"ds_read_b64_tr_b16 a[78:79], v44 offset:12544\n"
"ds_read_b64_tr_b16 a[104:105], v45 offset:12288\n"
"ds_read_b64_tr_b16 a[106:107], v45 offset:12544\n"
"s_waitcnt lgkmcnt(4)\n"
"s_waitcnt vmcnt(14)\n"
"ds_read_b64_tr_b16 a[132:133], v44 offset:14336\n"
"ds_read_b64_tr_b16 a[134:135], v44 offset:14592\n"
"ds_read_b64_tr_b16 a[160:161], v45 offset:14336\n"
"ds_read_b64_tr_b16 a[162:163], v45 offset:14592\n"
"s_waitcnt lgkmcnt(4)\n"
"s_waitcnt vmcnt(12)\n"
"ds_read_b64_tr_b16 a[188:189], v44 offset:16384\n"
"ds_read_b64_tr_b16 a[190:191], v44 offset:16640\n"
"ds_read_b64_tr_b16 a[216:217], v45 offset:16384\n"
"ds_read_b64_tr_b16 a[218:219], v45 offset:16640\n"
"s_waitcnt lgkmcnt(4)\n"
"s_waitcnt vmcnt(10)\n"
"ds_read_b64_tr_b16 a[244:245], v44 offset:18432\n"
"ds_read_b64_tr_b16 a[246:247], v44 offset:18688\n"
"ds_read_b64_tr_b16 v[36:37], v45 offset:18432\n"
"ds_read_b64_tr_b16 v[38:39], v45 offset:18688\n"
"s_waitcnt lgkmcnt(4)\n"
"s_waitcnt vmcnt(8)\n"
"ds_read_b64_tr_b16 a[24:25], v44 offset:20480\n"
"ds_read_b64_tr_b16 a[26:27], v44 offset:20736\n"
"ds_read_b64_tr_b16 a[52:53], v45 offset:20480\n"
"ds_read_b64_tr_b16 a[54:55], v45 offset:20736\n"
"s_waitcnt lgkmcnt(4)\n"
"s_waitcnt vmcnt(6)\n"
"ds_read_b64_tr_b16 a[80:81], v44 offset:22528\n"
"ds_read_b64_tr_b16 a[82:83], v44 offset:22784\n"
"ds_read_b64_tr_b16 a[108:109], v45 offset:22528\n"
"ds_read_b64_tr_b16 a[110:111], v45 offset:22784\n"
"s_waitcnt lgkmcnt(4)\n"
"s_waitcnt vmcnt(4)\n"
"ds_read_b64_tr_b16 a[136:137], v44 offset:24576\n"
"ds_read_b64_tr_b16 a[138:139], v44 offset:24832\n"
"ds_read_b64_tr_b16 a[164:165], v45 offset:24576\n"
"ds_read_b64_tr_b16 a[166:167], v45 offset:24832\n"
"s_waitcnt lgkmcnt(4)\n"
"s_waitcnt vmcnt(2)\n"
"ds_read_b64_tr_b16 a[192:193], v44 offset:26624\n"
"ds_read_b64_tr_b16 a[194:195], v44 offset:26880\n"
"ds_read_b64_tr_b16 a[220:221], v45 offset:26624\n"
"ds_read_b64_tr_b16 a[222:223], v45 offset:26880\n"
"s_waitcnt lgkmcnt(4)\n"
"s_waitcnt vmcnt(0)\n"
"ds_read_b64_tr_b16 a[248:249], v44 offset:28672\n"
"ds_read_b64_tr_b16 a[250:251], v44 offset:28928\n"
"ds_read_b64_tr_b16 v[40:41], v45 offset:28672\n"
"ds_read_b64_tr_b16 v[42:43], v45 offset:28928\n"
"s_waitcnt lgkmcnt(4)\n"
"s_waitcnt vmcnt(0)\n"
"s_waitcnt lgkmcnt(0)\n"
"s_mov_b32 m0, s59\n"
"s_barrier\n"
"s_mov_b32 s94, 0x80000000\n"
"s_mov_b32 s93, 0x80000000\n"
"s_mov_b32 s95, 0x80000000\n"
"s_mov_b32 s93, 0x80000000\n"
"s_mov_b32 s93, 0x80000000\n"
"s_mov_b32 s95, 0x80000000\n"
"s_mov_b32 s95, 0x80000000\n"
"s_mov_b32 s94, 0x80000000\n"
"v_mov_b32 v128, 0\n"
"v_mov_b32 v129, 0\n"
"v_mov_b32 v132, 0\n"
"v_mov_b32 v133, 0\n"
"v_mov_b32 v136, 0\n"
"v_mov_b32 v137, 0\n"
"v_mov_b32 v168, 0\n"
"v_mov_b32 v169, 0\n"
"s_cmp_eq_u32 s54, 0\n"
"s_cbranch_scc1 .LG_1s\n"
"s_cmp_eq_u32 s54, 1\n"
"s_cbranch_scc1 .LG_2s\n"
"s_cmp_eq_u32 s54, 2\n"
"s_cbranch_scc1 .LG_3s\n"
"s_cmp_eq_u32 s54, 3\n"
"s_cbranch_scc1 .LG_4s\n"
".p2align 6\n"
".LG_top:\n"
"v_mfma_f32_16x16x32_bf16 v[56:59], v[100:103], a[0:3], 0\n"
"v_mfma_f32_16x16x32_bf16 v[56:59], v[68:71], a[4:7], v[56:59]\n"
"global_load_dwordx4 v[44:47], v7, s[56:57] offset:0 sc1\n"
"v_mfma_f32_16x16x32_bf16 v[56:59], v[72:75], a[8:11], v[56:59]\n"
"v_mfma_f32_16x16x32_bf16 v[56:59], v[76:79], a[12:15], v[56:59]\n"
"s_waitcnt vmcnt(5)\n"
"v_mfma_f32_16x16x32_bf16 v[56:59], v[80:83], a[16:19], v[56:59]\n"
"s_waitcnt lgkmcnt(3)\n"
"v_add_f32 v120, v120, v64\n"
"v_add_f32 v121, v121, v65\n"
"v_mfma_f32_16x16x32_bf16 v[56:59], v[84:87], a[20:23], v[56:59]\n"
"s_waitcnt vmcnt(4)\n"
"v_add_f32 v122, v122, v66\n"
"v_add_f32 v123, v123, v67\n"
"v_mfma_f32_16x16x32_bf16 v[56:59], v[88:91], a[24:27], v[56:59]\n"
"global_load_dwordx4 v[48:51], v8, s[56:57] offset:0 sc1\n"
"v_add_f32 v120, v120, v92\n"
"v_mfma_f32_16x16x32_bf16 v[60:63], v[100:103], a[28:31], 0\n"
"buffer_load_dwordx2 v[136:137], v12, s[40:43], s50 offen sc1\n"
"v_mfma_f32_16x16x32_bf16 v[60:63], v[68:71], a[32:35], v[60:63]\n"
"ds_write_addtid_b32 v56 offset:0\n"
"v_add_f32 v121, v121, v93\n"
"v_mfma_f32_16x16x32_bf16 v[60:63], v[72:75], a[36:39], v[60:63]\n"
"ds_write_addtid_b32 v57 offset:4640\n"
"global_load_dwordx4 v[52:55], v9, s[56:57] offset:0 sc1\n"
"v_mfma_f32_16x16x32_bf16 v[60:63], v[76:79], a[40:43], v[60:63]\n"
"ds_write_addtid_b32 v58 offset:9344\n"
"v_add_f32 v122, v122, v94\n"
"v_mfma_f32_16x16x32_bf16 v[60:63], v[80:83], a[44:47], v[60:63]\n"
"ds_write_addtid_b32 v59 offset:13984\n"
"v_add_f32 v123, v123, v95\n"
"v_mfma_f32_16x16x32_bf16 v[60:63], v[84:87], a[48:51], v[60:63]\n"
"v_add_f32 v120, v120, v116\n"
"v_add_f32 v121, v121, v117\n"
"v_mfma_f32_16x16x32_bf16 v[60:63], v[88:91], a[52:55], v[60:63]\n"
"v_add_f32 v122, v122, v118\n"
"v_add_f32 v123, v123, v119\n"
"v_mfma_f32_16x16x32_bf16 v[64:67], v[100:103], a[56:59], 0\n"
"s_waitcnt vmcnt(6)\n"
"buffer_store_dwordx2 v[0:1], v14, s[40:43], s93 offen offset:0 sc1\n"
"v_mfma_f32_16x16x32_bf16 v[64:67], v[68:71], a[60:63], v[64:67]\n"
"s_add_u32 s95, s50, 0xffd00000\n"
"s_and_b32 s94, s95, 0xfbffffff\n"
"ds_write_addtid_b32 v60 offset:256\n"
"v_mfma_f32_16x16x32_bf16 v[64:67], v[72:75], a[64:67], v[64:67]\n"
"ds_write_addtid_b32 v61 offset:4896\n"
"global_load_dwordx4 v[56:59], v10, s[56:57] offset:0 sc1\n"
"v_mfma_f32_16x16x32_bf16 v[64:67], v[76:79], a[68:71], v[64:67]\n"
"ds_write_addtid_b32 v62 offset:9600\n"
"v_cvt_pk_bf16_f32 v92, v120, v121\n"
"v_mfma_f32_16x16x32_bf16 v[64:67], v[80:83], a[72:75], v[64:67]\n"
"ds_write_addtid_b32 v63 offset:14240\n"
"v_cvt_pk_bf16_f32 v93, v122, v123\n"
"v_mfma_f32_16x16x32_bf16 v[64:67], v[84:87], a[76:79], v[64:67]\n"
"s_waitcnt lgkmcnt(8)\n"
"v_add_f32 v124, v124, v128\n"
"v_add_f32 v125, v125, v129\n"
"v_mfma_f32_16x16x32_bf16 v[64:67], v[88:91], a[80:83], v[64:67]\n"
"v_add_f32 v126, v126, v130\n"
"v_add_f32 v127, v127, v131\n"
"v_mfma_f32_16x16x32_bf16 v[96:99], v[100:103], a[84:87], 0\n"
"global_load_dwordx4 v[60:63], v11, s[56:57] offset:0 sc1\n"
"v_add_f32 v124, v124, v132\n"
"v_mfma_f32_16x16x32_bf16 v[96:99], v[68:71], a[88:91], v[96:99]\n"
"ds_write_addtid_b32 v64 offset:512\n"
"v_add_f32 v125, v125, v133\n"
"v_mfma_f32_16x16x32_bf16 v[96:99], v[72:75], a[92:95], v[96:99]\n"
"ds_write_addtid_b32 v65 offset:5152\n"
"v_add_f32 v126, v126, v134\n"
"v_mfma_f32_16x16x32_bf16 v[96:99], v[76:79], a[96:99], v[96:99]\n"
"ds_write_addtid_b32 v66 offset:9856\n"
"v_add_f32 v127, v127, v135\n"
"v_mfma_f32_16x16x32_bf16 v[96:99], v[80:83], a[100:103], v[96:99]\n"
"ds_write_addtid_b32 v67 offset:14496\n"
"v_add_f32 v124, v124, v112\n"
"v_mfma_f32_16x16x32_bf16 v[96:99], v[84:87], a[104:107], v[96:99]\n"
"v_add_f32 v125, v125, v113\n"
"v_add_f32 v126, v126, v114\n"
"v_mfma_f32_16x16x32_bf16 v[96:99], v[88:91], a[108:111], v[96:99]\n"
"v_add_f32 v127, v127, v115\n"
"v_cvt_pk_bf16_f32 v94, v124, v125\n"
"v_mfma_f32_16x16x32_bf16 v[64:67], v[100:103], a[112:115], 0\n"
"v_cvt_pk_bf16_f32 v95, v126, v127\n"
"v_mfma_f32_16x16x32_bf16 v[64:67], v[68:71], a[116:119], v[64:67]\n"
"ds_write_addtid_b32 v96 offset:768\n"
"v_mfma_f32_16x16x32_bf16 v[64:67], v[72:75], a[120:123], v[64:67]\n"
"ds_write_addtid_b32 v97 offset:5408\n"
"v_mfma_f32_16x16x32_bf16 v[64:67], v[76:79], a[124:127], v[64:67]\n"
"ds_write_addtid_b32 v98 offset:10112\n"
"v_mfma_f32_16x16x32_bf16 v[64:67], v[80:83], a[128:131], v[64:67]\n"
"ds_write_addtid_b32 v99 offset:14752\n"
"v_mfma_f32_16x16x32_bf16 v[64:67], v[84:87], a[132:135], v[64:67]\n"
"v_mfma_f32_16x16x32_bf16 v[64:67], v[88:91], a[136:139], v[64:67]\n"
"v_mfma_f32_16x16x32_bf16 v[112:115], v[100:103], a[140:143], 0\n"
"v_mfma_f32_16x16x32_bf16 v[112:115], v[68:71], a[144:147], v[112:115]\n"
"ds_write_addtid_b32 v64 offset:1024\n"
"v_mfma_f32_16x16x32_bf16 v[112:115], v[72:75], a[148:151], v[112:115]\n"
"ds_write_addtid_b32 v65 offset:5664\n"
"v_mfma_f32_16x16x32_bf16 v[112:115], v[76:79], a[152:155], v[112:115]\n"
"ds_write_addtid_b32 v66 offset:10368\n"
"v_mfma_f32_16x16x32_bf16 v[112:115], v[80:83], a[156:159], v[112:115]\n"
"ds_write_addtid_b32 v67 offset:15008\n"
"v_mfma_f32_16x16x32_bf16 v[112:115], v[84:87], a[160:163], v[112:115]\n"
"buffer_store_dwordx4 v[92:95], v13, s[40:43], s94 offen sc1\n"
"v_mfma_f32_16x16x32_bf16 v[112:115], v[88:91], a[164:167], v[112:115]\n"
"v_mfma_f32_16x16x32_bf16 v[96:99], a[168:171], v[100:103], 0\n"
"v_mfma_f32_16x16x32_bf16 v[96:99], a[172:175], v[68:71], v[96:99]\n"
"ds_write_addtid_b32 v112 offset:1280\n"
"v_mfma_f32_16x16x32_bf16 v[96:99], a[176:179], v[72:75], v[96:99]\n"
"ds_write_addtid_b32 v113 offset:5920\n"
"v_mfma_f32_16x16x32_bf16 v[96:99], a[180:183], v[76:79], v[96:99]\n"
"ds_write_addtid_b32 v114 offset:10624\n"
"v_mfma_f32_16x16x32_bf16 v[96:99], a[184:187], v[80:83], v[96:99]\n"
"ds_write_addtid_b32 v115 offset:15264\n"
"v_mfma_f32_16x16x32_bf16 v[96:99], a[188:191], v[84:87], v[96:99]\n"
"v_mfma_f32_16x16x32_bf16 v[96:99], a[192:195], v[88:91], v[96:99]\n"
"v_mfma_f32_16x16x32_bf16 v[92:95], a[196:199], v[100:103], 0\n"
"v_mfma_f32_16x16x32_bf16 v[92:95], a[200:203], v[68:71], v[92:95]\n"
"v_mfma_f32_16x16x32_bf16 v[92:95], a[204:207], v[72:75], v[92:95]\n"
"v_mfma_f32_16x16x32_bf16 v[92:95], a[208:211], v[76:79], v[92:95]\n"
"s_add_u32 s58, s50, 0x180000\n"
"v_mfma_f32_16x16x32_bf16 v[92:95], a[212:215], v[80:83], v[92:95]\n"
"s_and_b32 s58, s58, 0x3ffffff\n"
"v_mfma_f32_16x16x32_bf16 v[92:95], a[216:219], v[84:87], v[92:95]\n"
"s_add_u32 s56, s40, s58\n"
"v_mfma_f32_16x16x32_bf16 v[92:95], a[220:223], v[88:91], v[92:95]\n"
"s_addc_u32 s57, s41, 0\n"
"v_mfma_f32_16x16x32_bf16 v[112:115], a[224:227], v[100:103], 0\n"
"s_waitcnt vmcnt(5)\n"
"v_cmp_ne_u64 vcc, s[48:49], v[136:137]\n"
"v_mfma_f32_16x16x32_bf16 v[112:115], a[228:231], v[68:71], v[112:115]\n"
"v_mfma_f32_16x16x32_bf16 v[112:115], a[232:235], v[72:75], v[112:115]\n"
"s_cbranch_vccnz .LG_3s\n"
".LG_3r:\n"
"v_mfma_f32_16x16x32_bf16 v[112:115], a[236:239], v[76:79], v[112:115]\n"
"s_add_u32 s50, s50, 0x180000\n"
"global_load_dwordx4 v[64:67], v5, s[56:57] offset:0 sc1\n"
"v_mfma_f32_16x16x32_bf16 v[112:115], a[240:243], v[80:83], v[112:115]\n"
"v_mfma_f32_16x16x32_bf16 v[112:115], a[244:247], v[84:87], v[112:115]\n"
"v_mfma_f32_16x16x32_bf16 v[112:115], a[248:251], v[88:91], v[112:115]\n"
"v_mfma_f32_16x16x32_bf16 v[116:119], a[252:255], v[100:103], 0\n"
"v_mfma_f32_16x16x32_bf16 v[116:119], v[20:23], v[68:71], v[116:119]\n"
"s_waitcnt lgkmcnt(0)\n"
"v_mfma_f32_16x16x32_bf16 v[116:119], v[24:27], v[72:75], v[116:119]\n"
"s_barrier\n"
"v_mfma_f32_16x16x32_bf16 v[116:119], v[28:31], v[76:79], v[116:119]\n"
"ds_read_b128 v[100:103], v2 offset:2048\n"
"ds_read_b128 v[120:123], v3 offset:512\n"
"v_mfma_f32_16x16x32_bf16 v[116:119], v[32:35], v[80:83], v[116:119]\n"
"global_load_dwordx4 v[68:71], v6, s[56:57] offset:0 sc1\n"
"ds_read_b128 v[124:127], v4 offset:3072\n"
"v_mfma_f32_16x16x32_bf16 v[116:119], v[36:39], v[84:87], v[116:119]\n"
"ds_read_b128 v[128:131], v2 offset:2304\n"
"ds_read_b128 v[132:135], v3 offset:768\n"
"v_mfma_f32_16x16x32_bf16 v[116:119], v[40:43], v[88:91], v[116:119]\n"
"global_load_dwordx4 v[72:75], v7, s[56:57] offset:0 sc1\n"
"ds_read_b128 v[136:139], v4 offset:3328\n"
"v_mfma_f32_16x16x32_bf16 v[84:87], v[104:107], a[0:3], 0\n"
"ds_read_b128 v[140:143], v2 offset:3584\n"
"ds_read_b128 v[144:147], v3 offset:1536\n"
"v_mfma_f32_16x16x32_bf16 v[84:87], v[108:111], a[4:7], v[84:87]\n"
"ds_read_b128 v[148:151], v4 offset:3584\n"
"ds_read_b128 v[152:155], v2 offset:3840\n"
"v_mfma_f32_16x16x32_bf16 v[84:87], v[44:47], a[8:11], v[84:87]\n"
"ds_read_b128 v[156:159], v3 offset:1792\n"
"ds_read_b128 v[160:163], v4 offset:3840\n"
"v_mfma_f32_16x16x32_bf16 v[84:87], v[48:51], a[12:15], v[84:87]\n"
"s_waitcnt vmcnt(5)\n"
"v_mfma_f32_16x16x32_bf16 v[84:87], v[52:55], a[16:19], v[84:87]\n"
"s_waitcnt lgkmcnt(9)\n"
"global_load_dwordx4 v[76:79], v8, s[56:57] offset:0 sc1\n"
"v_add_f32 v100, v100, v120\n"
"v_mfma_f32_16x16x32_bf16 v[84:87], v[56:59], a[20:23], v[84:87]\n"
"s_waitcnt vmcnt(5)\n"
"v_add_f32 v101, v101, v121\n"
"v_add_f32 v102, v102, v122\n"
"v_mfma_f32_16x16x32_bf16 v[84:87], v[60:63], a[24:27], v[84:87]\n"
"v_add_f32 v103, v103, v123\n"
"v_add_f32 v100, v100, v124\n"
"v_mfma_f32_16x16x32_bf16 v[164:167], v[104:107], a[28:31], 0\n"
"buffer_load_dwordx2 v[168:169], v12, s[40:43], s50 offen sc1\n"
"v_mfma_f32_16x16x32_bf16 v[164:167], v[108:111], a[32:35], v[164:167]\n"
"ds_write_addtid_b32 v84 offset:2048\n"
"v_add_f32 v101, v101, v125\n"
"v_mfma_f32_16x16x32_bf16 v[164:167], v[44:47], a[36:39], v[164:167]\n"
"ds_write_addtid_b32 v85 offset:6688\n"
"v_add_f32 v102, v102, v126\n"
"v_mfma_f32_16x16x32_bf16 v[164:167], v[48:51], a[40:43], v[164:167]\n"
"ds_write_addtid_b32 v86 offset:11392\n"
"v_add_f32 v103, v103, v127\n"
"v_mfma_f32_16x16x32_bf16 v[164:167], v[52:55], a[44:47], v[164:167]\n"
"ds_write_addtid_b32 v87 offset:16032\n"
"v_add_f32 v100, v100, v96\n"
"v_mfma_f32_16x16x32_bf16 v[164:167], v[56:59], a[48:51], v[164:167]\n"
"global_load_dwordx4 v[80:83], v9, s[56:57] offset:0 sc1\n"
"v_add_f32 v101, v101, v97\n"
"v_mfma_f32_16x16x32_bf16 v[164:167], v[60:63], a[52:55], v[164:167]\n"
"v_add_f32 v102, v102, v98\n"
"v_add_f32 v103, v103, v99\n"
"v_mfma_f32_16x16x32_bf16 v[120:123], v[104:107], a[56:59], 0\n"
"s_add_u32 s94, s50, 0xffd00000\n"
"s_and_b32 s93, s94, 0xfbffffff\n"
"v_cvt_pk_bf16_f32 v124, v100, v101\n"
"v_mfma_f32_16x16x32_bf16 v[120:123], v[108:111], a[60:63], v[120:123]\n"
"ds_write_addtid_b32 v164 offset:2304\n"
"v_cvt_pk_bf16_f32 v125, v102, v103\n"
"v_mfma_f32_16x16x32_bf16 v[120:123], v[44:47], a[64:67], v[120:123]\n"
"s_waitcnt lgkmcnt(11)\n"
"ds_write_addtid_b32 v165 offset:6944\n"
"v_add_f32 v128, v128, v132\n"
"v_mfma_f32_16x16x32_bf16 v[120:123], v[48:51], a[68:71], v[120:123]\n"
"ds_write_addtid_b32 v166 offset:11648\n"
"v_add_f32 v129, v129, v133\n"
"v_mfma_f32_16x16x32_bf16 v[120:123], v[52:55], a[72:75], v[120:123]\n"
"s_waitcnt vmcnt(6)\n"
"ds_write_addtid_b32 v167 offset:16288\n"
"global_load_dwordx4 v[84:87], v10, s[56:57] offset:0 sc1\n"
"v_mfma_f32_16x16x32_bf16 v[120:123], v[56:59], a[76:79], v[120:123]\n"
"global_load_dwordx4 v[88:91], v11, s[56:57] offset:0 sc1\n"
"v_add_f32 v130, v130, v134\n"
"v_mfma_f32_16x16x32_bf16 v[120:123], v[60:63], a[80:83], v[120:123]\n"
"v_add_f32 v131, v131, v135\n"
"v_add_f32 v128, v128, v136\n"
"v_mfma_f32_16x16x32_bf16 v[96:99], v[104:107], a[84:87], 0\n"
"v_add_f32 v129, v129, v137\n"
"v_add_f32 v130, v130, v138\n"
"v_mfma_f32_16x16x32_bf16 v[96:99], v[108:111], a[88:91], v[96:99]\n"
"ds_write_addtid_b32 v120 offset:2560\n"
"v_add_f32 v131, v131, v139\n"
"v_mfma_f32_16x16x32_bf16 v[96:99], v[44:47], a[92:95], v[96:99]\n"
"ds_write_addtid_b32 v121 offset:7200\n"
"v_add_f32 v128, v128, v92\n"
"v_mfma_f32_16x16x32_bf16 v[96:99], v[48:51], a[96:99], v[96:99]\n"
"ds_write_addtid_b32 v122 offset:11904\n"
"v_add_f32 v129, v129, v93\n"
"v_mfma_f32_16x16x32_bf16 v[96:99], v[52:55], a[100:103], v[96:99]\n"
"ds_write_addtid_b32 v123 offset:16544\n"
"v_add_f32 v130, v130, v94\n"
"v_mfma_f32_16x16x32_bf16 v[96:99], v[56:59], a[104:107], v[96:99]\n"
"v_add_f32 v131, v131, v95\n"
"v_cvt_pk_bf16_f32 v126, v128, v129\n"
"v_mfma_f32_16x16x32_bf16 v[96:99], v[60:63], a[108:111], v[96:99]\n"
"buffer_store_dwordx2 v[0:1], v14, s[40:43], s95 offen offset:0 sc1\n"
"v_cvt_pk_bf16_f32 v127, v130, v131\n"
"v_mfma_f32_16x16x32_bf16 v[92:95], v[104:107], a[112:115], 0\n"
"buffer_store_dwordx4 v[124:127], v13, s[40:43], s93 offen sc1\n"
"v_mfma_f32_16x16x32_bf16 v[92:95], v[108:111], a[116:119], v[92:95]\n"
"s_waitcnt lgkmcnt(15)\n"
"ds_write_addtid_b32 v96 offset:2816\n"
"v_add_f32 v140, v140, v144\n"
"v_mfma_f32_16x16x32_bf16 v[92:95], v[44:47], a[120:123], v[92:95]\n"
"ds_write_addtid_b32 v97 offset:7456\n"
"v_add_f32 v141, v141, v145\n"
"v_mfma_f32_16x16x32_bf16 v[92:95], v[48:51], a[124:127], v[92:95]\n"
"ds_write_addtid_b32 v98 offset:12160\n"
"v_add_f32 v142, v142, v146\n"
"v_mfma_f32_16x16x32_bf16 v[92:95], v[52:55], a[128:131], v[92:95]\n"
"ds_write_addtid_b32 v99 offset:16800\n"
"v_add_f32 v143, v143, v147\n"
"v_mfma_f32_16x16x32_bf16 v[92:95], v[56:59], a[132:135], v[92:95]\n"
"v_add_f32 v140, v140, v148\n"
"v_add_f32 v141, v141, v149\n"
"v_mfma_f32_16x16x32_bf16 v[92:95], v[60:63], a[136:139], v[92:95]\n"
"v_add_f32 v142, v142, v150\n"
"v_add_f32 v143, v143, v151\n"
"v_mfma_f32_16x16x32_bf16 v[96:99], v[104:107], a[140:143], 0\n"
"v_add_f32 v140, v140, v112\n"
"v_add_f32 v141, v141, v113\n"
"v_mfma_f32_16x16x32_bf16 v[96:99], v[108:111], a[144:147], v[96:99]\n"
"ds_write_addtid_b32 v92 offset:3072\n"
"v_add_f32 v142, v142, v114\n"
"v_mfma_f32_16x16x32_bf16 v[96:99], v[44:47], a[148:151], v[96:99]\n"
"ds_write_addtid_b32 v93 offset:7712\n"
"v_add_f32 v143, v143, v115\n"
"v_mfma_f32_16x16x32_bf16 v[96:99], v[48:51], a[152:155], v[96:99]\n"
"ds_write_addtid_b32 v94 offset:12416\n"
"v_cvt_pk_bf16_f32 v100, v140, v141\n"
"v_mfma_f32_16x16x32_bf16 v[96:99], v[52:55], a[156:159], v[96:99]\n"
"ds_write_addtid_b32 v95 offset:17056\n"
"v_cvt_pk_bf16_f32 v101, v142, v143\n"
"v_mfma_f32_16x16x32_bf16 v[96:99], v[56:59], a[160:163], v[96:99]\n"
"s_waitcnt lgkmcnt(15)\n"
"v_add_f32 v152, v152, v156\n"
"v_add_f32 v153, v153, v157\n"
"v_mfma_f32_16x16x32_bf16 v[96:99], v[60:63], a[164:167], v[96:99]\n"
"v_add_f32 v154, v154, v158\n"
"v_add_f32 v155, v155, v159\n"
"v_mfma_f32_16x16x32_bf16 v[92:95], v[104:107], a[224:227], 0\n"
"v_add_f32 v152, v152, v160\n"
"v_add_f32 v153, v153, v161\n"
"v_mfma_f32_16x16x32_bf16 v[92:95], v[108:111], a[228:231], v[92:95]\n"
"ds_write_addtid_b32 v96 offset:3328\n"
"v_add_f32 v154, v154, v162\n"
"v_mfma_f32_16x16x32_bf16 v[92:95], v[44:47], a[232:235], v[92:95]\n"
"ds_write_addtid_b32 v97 offset:7968\n"
"v_add_f32 v155, v155, v163\n"
"v_mfma_f32_16x16x32_bf16 v[92:95], v[48:51], a[236:239], v[92:95]\n"
"ds_write_addtid_b32 v98 offset:12672\n"
"v_add_f32 v152, v152, v116\n"
"v_mfma_f32_16x16x32_bf16 v[92:95], v[52:55], a[240:243], v[92:95]\n"
"ds_write_addtid_b32 v99 offset:17312\n"
"v_add_f32 v153, v153, v117\n"
"v_mfma_f32_16x16x32_bf16 v[92:95], v[56:59], a[244:247], v[92:95]\n"
"v_add_f32 v154, v154, v118\n"
"v_add_f32 v155, v155, v119\n"
"v_mfma_f32_16x16x32_bf16 v[92:95], v[60:63], a[248:251], v[92:95]\n"
"v_cvt_pk_bf16_f32 v102, v152, v153\n"
"v_cvt_pk_bf16_f32 v103, v154, v155\n"
"v_mfma_f32_16x16x32_bf16 v[96:99], v[104:107], a[252:255], 0\n"
"buffer_store_dwordx4 v[100:103], v15, s[40:43], s93 offen sc1\n"
"v_mfma_f32_16x16x32_bf16 v[96:99], v[108:111], v[20:23], v[96:99]\n"
"ds_write_addtid_b32 v92 offset:3584\n"
"v_mfma_f32_16x16x32_bf16 v[96:99], v[44:47], v[24:27], v[96:99]\n"
"ds_write_addtid_b32 v93 offset:8224\n"
"v_mfma_f32_16x16x32_bf16 v[96:99], v[48:51], v[28:31], v[96:99]\n"
"ds_write_addtid_b32 v94 offset:12928\n"
"v_mfma_f32_16x16x32_bf16 v[96:99], v[52:55], v[32:35], v[96:99]\n"
"ds_write_addtid_b32 v95 offset:17568\n"
"v_mfma_f32_16x16x32_bf16 v[96:99], v[56:59], v[36:39], v[96:99]\n"
"v_mfma_f32_16x16x32_bf16 v[96:99], v[60:63], v[40:43], v[96:99]\n"
"s_add_u32 s58, s50, 0x180000\n"
"v_mfma_f32_16x16x32_bf16 v[100:103], a[168:171], v[104:107], 0\n"
"s_and_b32 s58, s58, 0x3ffffff\n"
"v_mfma_f32_16x16x32_bf16 v[100:103], a[172:175], v[108:111], v[100:103]\n"
"s_add_u32 s56, s40, s58\n"
"ds_write_addtid_b32 v96 offset:3840\n"
"v_mfma_f32_16x16x32_bf16 v[100:103], a[176:179], v[44:47], v[100:103]\n"
"s_addc_u32 s57, s41, 0\n"
"ds_write_addtid_b32 v97 offset:8480\n"
"v_mfma_f32_16x16x32_bf16 v[100:103], a[180:183], v[48:51], v[100:103]\n"
"s_waitcnt vmcnt(6)\n"
"ds_write_addtid_b32 v98 offset:13184\n"
"v_cmp_ne_u64 vcc, s[48:49], v[168:169]\n"
"v_mfma_f32_16x16x32_bf16 v[100:103], a[184:187], v[52:55], v[100:103]\n"
"ds_write_addtid_b32 v99 offset:17824\n"
"v_mfma_f32_16x16x32_bf16 v[100:103], a[188:191], v[56:59], v[100:103]\n"
"s_cbranch_vccnz .LG_4s\n"
".LG_4r:\n"
"v_mfma_f32_16x16x32_bf16 v[100:103], a[192:195], v[60:63], v[100:103]\n"
"s_add_u32 s50, s50, 0x180000\n"
"global_load_dwordx4 v[92:95], v5, s[56:57] offset:0 sc1\n"
"v_mfma_f32_16x16x32_bf16 v[112:115], a[196:199], v[104:107], 0\n"
"v_mfma_f32_16x16x32_bf16 v[112:115], a[200:203], v[108:111], v[112:115]\n"
"s_waitcnt lgkmcnt(0)\n"
"v_mfma_f32_16x16x32_bf16 v[112:115], a[204:207], v[44:47], v[112:115]\n"
"s_barrier\n"
"v_mfma_f32_16x16x32_bf16 v[112:115], a[208:211], v[48:51], v[112:115]\n"
"ds_read_b128 v[96:99], v2 offset:0\n"
"ds_read_b128 v[104:107], v3 offset:2560\n"
"v_mfma_f32_16x16x32_bf16 v[112:115], a[212:215], v[52:55], v[112:115]\n"
"ds_read_b128 v[108:111], v4 offset:1024\n"
"ds_read_b128 v[116:119], v2 offset:256\n"
"v_mfma_f32_16x16x32_bf16 v[112:115], a[216:219], v[56:59], v[112:115]\n"
"global_load_dwordx4 v[44:47], v6, s[56:57] offset:0 sc1\n"
"ds_read_b128 v[120:123], v3 offset:2816\n"
"v_mfma_f32_16x16x32_bf16 v[112:115], a[220:223], v[60:63], v[112:115]\n"
"ds_read_b128 v[124:127], v4 offset:1280\n"
"v_mfma_f32_16x16x32_bf16 v[56:59], v[64:67], a[0:3], 0\n"
"v_mfma_f32_16x16x32_bf16 v[56:59], v[68:71], a[4:7], v[56:59]\n"
"v_mfma_f32_16x16x32_bf16 v[56:59], v[72:75], a[8:11], v[56:59]\n"
"global_load_dwordx4 v[48:51], v7, s[56:57] offset:0 sc1\n"
"v_mfma_f32_16x16x32_bf16 v[56:59], v[76:79], a[12:15], v[56:59]\n"
"s_waitcnt vmcnt(7)\n"
"buffer_load_dwordx2 v[128:129], v12, s[40:43], s50 offen sc1\n"
"v_mfma_f32_16x16x32_bf16 v[56:59], v[80:83], a[16:19], v[56:59]\n"
"s_waitcnt lgkmcnt(3)\n"
"v_add_f32 v96, v96, v104\n"
"v_add_f32 v97, v97, v105\n"
"v_mfma_f32_16x16x32_bf16 v[56:59], v[84:87], a[20:23], v[56:59]\n"
"s_waitcnt vmcnt(7)\n"
"v_add_f32 v98, v98, v106\n"
"v_add_f32 v99, v99, v107\n"
"v_mfma_f32_16x16x32_bf16 v[56:59], v[88:91], a[24:27], v[56:59]\n"
"v_add_f32 v96, v96, v108\n"
"v_add_f32 v97, v97, v109\n"
"v_mfma_f32_16x16x32_bf16 v[104:107], v[64:67], a[28:31], 0\n"
"global_load_dwordx4 v[52:55], v8, s[56:57] offset:0 sc1\n"
"v_add_f32 v98, v98, v110\n"
"v_mfma_f32_16x16x32_bf16 v[104:107], v[68:71], a[32:35], v[104:107]\n"
"ds_write_addtid_b32 v56 offset:0\n"
"v_add_f32 v99, v99, v111\n"
"v_mfma_f32_16x16x32_bf16 v[104:107], v[72:75], a[36:39], v[104:107]\n"
"ds_write_addtid_b32 v57 offset:4640\n"
"v_add_f32 v96, v96, v100\n"
"v_mfma_f32_16x16x32_bf16 v[104:107], v[76:79], a[40:43], v[104:107]\n"
"ds_write_addtid_b32 v58 offset:9344\n"
"v_add_f32 v97, v97, v101\n"
"v_mfma_f32_16x16x32_bf16 v[104:107], v[80:83], a[44:47], v[104:107]\n"
"ds_write_addtid_b32 v59 offset:13984\n"
"v_add_f32 v98, v98, v102\n"
"v_mfma_f32_16x16x32_bf16 v[104:107], v[84:87], a[48:51], v[104:107]\n"
"v_add_f32 v99, v99, v103\n"
"v_mfma_f32_16x16x32_bf16 v[104:107], v[88:91], a[52:55], v[104:107]\n"
"s_add_u32 s95, s50, 0xffd00000\n"
"s_and_b32 s93, s95, 0xfbffffff\n"
"global_load_dwordx4 v[56:59], v9, s[56:57] offset:0 sc1\n"
"v_mfma_f32_16x16x32_bf16 v[100:103], v[64:67], a[56:59], 0\n"
"v_cvt_pk_bf16_f32 v108, v96, v97\n"
"v_cvt_pk_bf16_f32 v109, v98, v99\n"
"v_mfma_f32_16x16x32_bf16 v[100:103], v[68:71], a[60:63], v[100:103]\n"
"s_waitcnt lgkmcnt(4)\n"
"ds_write_addtid_b32 v104 offset:256\n"
"v_add_f32 v116, v116, v120\n"
"v_mfma_f32_16x16x32_bf16 v[100:103], v[72:75], a[64:67], v[100:103]\n"
"ds_write_addtid_b32 v105 offset:4896\n"
"v_add_f32 v117, v117, v121\n"
"v_mfma_f32_16x16x32_bf16 v[100:103], v[76:79], a[68:71], v[100:103]\n"
"ds_write_addtid_b32 v106 offset:9600\n"
"global_load_dwordx4 v[60:63], v10, s[56:57] offset:0 sc1\n"
"v_mfma_f32_16x16x32_bf16 v[100:103], v[80:83], a[72:75], v[100:103]\n"
"ds_write_addtid_b32 v107 offset:14240\n"
"v_add_f32 v118, v118, v122\n"
"v_mfma_f32_16x16x32_bf16 v[100:103], v[84:87], a[76:79], v[100:103]\n"
"v_add_f32 v119, v119, v123\n"
"v_add_f32 v116, v116, v124\n"
"v_mfma_f32_16x16x32_bf16 v[100:103], v[88:91], a[80:83], v[100:103]\n"
"v_add_f32 v117, v117, v125\n"
"v_add_f32 v118, v118, v126\n"
"v_mfma_f32_16x16x32_bf16 v[104:107], v[64:67], a[84:87], 0\n"
"v_add_f32 v119, v119, v127\n"
"v_add_f32 v116, v116, v112\n"
"v_mfma_f32_16x16x32_bf16 v[104:107], v[68:71], a[88:91], v[104:107]\n"
"ds_write_addtid_b32 v100 offset:512\n"
"v_add_f32 v117, v117, v113\n"
"v_mfma_f32_16x16x32_bf16 v[104:107], v[72:75], a[92:95], v[104:107]\n"
"ds_write_addtid_b32 v101 offset:5152\n"
"global_load_dwordx4 v[96:99], v11, s[56:57] offset:0 sc1\n"
"v_mfma_f32_16x16x32_bf16 v[104:107], v[76:79], a[96:99], v[104:107]\n"
"ds_write_addtid_b32 v102 offset:9856\n"
"v_add_f32 v118, v118, v114\n"
"v_mfma_f32_16x16x32_bf16 v[104:107], v[80:83], a[100:103], v[104:107]\n"
"ds_write_addtid_b32 v103 offset:14496\n"
"v_add_f32 v119, v119, v115\n"
"v_mfma_f32_16x16x32_bf16 v[104:107], v[84:87], a[104:107], v[104:107]\n"
"v_cvt_pk_bf16_f32 v110, v116, v117\n"
"v_cvt_pk_bf16_f32 v111, v118, v119\n"
"v_mfma_f32_16x16x32_bf16 v[104:107], v[88:91], a[108:111], v[104:107]\n"
"s_waitcnt vmcnt(8)\n"
"buffer_store_dwordx2 v[0:1], v16, s[40:43], s94 offen offset:0 sc1\n"
"v_mfma_f32_16x16x32_bf16 v[100:103], v[64:67], a[112:115], 0\n"
"v_mfma_f32_16x16x32_bf16 v[100:103], v[68:71], a[116:119], v[100:103]\n"
"ds_write_addtid_b32 v104 offset:768\n"
"v_mfma_f32_16x16x32_bf16 v[100:103], v[72:75], a[120:123], v[100:103]\n"
"buffer_store_dwordx4 v[108:111], v13, s[40:43], s93 offen sc1\n"
"ds_write_addtid_b32 v105 offset:5408\n"
"v_mfma_f32_16x16x32_bf16 v[100:103], v[76:79], a[124:127], v[100:103]\n"
"ds_write_addtid_b32 v106 offset:10112\n"
"v_mfma_f32_16x16x32_bf16 v[100:103], v[80:83], a[128:131], v[100:103]\n"
"ds_write_addtid_b32 v107 offset:14752\n"
"v_mfma_f32_16x16x32_bf16 v[100:103], v[84:87], a[132:135], v[100:103]\n"
"v_mfma_f32_16x16x32_bf16 v[100:103], v[88:91], a[136:139], v[100:103]\n"
"v_mfma_f32_16x16x32_bf16 v[108:111], v[64:67], a[140:143], 0\n"
"v_mfma_f32_16x16x32_bf16 v[108:111], v[68:71], a[144:147], v[108:111]\n"
"ds_write_addtid_b32 v100 offset:1024\n"
"v_mfma_f32_16x16x32_bf16 v[108:111], v[72:75], a[148:151], v[108:111]\n"
"ds_write_addtid_b32 v101 offset:5664\n"
"v_mfma_f32_16x16x32_bf16 v[108:111], v[76:79], a[152:155], v[108:111]\n"
"ds_write_addtid_b32 v102 offset:10368\n"
"v_mfma_f32_16x16x32_bf16 v[108:111], v[80:83], a[156:159], v[108:111]\n"
"ds_write_addtid_b32 v103 offset:15008\n"
"v_mfma_f32_16x16x32_bf16 v[108:111], v[84:87], a[160:163], v[108:111]\n"
"v_mfma_f32_16x16x32_bf16 v[108:111], v[88:91], a[164:167], v[108:111]\n"
"v_mfma_f32_16x16x32_bf16 v[104:107], v[64:67], a[224:227], 0\n"
"v_mfma_f32_16x16x32_bf16 v[104:107], v[68:71], a[228:231], v[104:107]\n"
"ds_write_addtid_b32 v108 offset:1280\n"
"v_mfma_f32_16x16x32_bf16 v[104:107], v[72:75], a[232:235], v[104:107]\n"
"ds_write_addtid_b32 v109 offset:5920\n"
"v_mfma_f32_16x16x32_bf16 v[104:107], v[76:79], a[236:239], v[104:107]\n"
"ds_write_addtid_b32 v110 offset:10624\n"
"v_mfma_f32_16x16x32_bf16 v[104:107], v[80:83], a[240:243], v[104:107]\n"
"ds_write_addtid_b32 v111 offset:15264\n"
"v_mfma_f32_16x16x32_bf16 v[104:107], v[84:87], a[244:247], v[104:107]\n"
"v_mfma_f32_16x16x32_bf16 v[104:107], v[88:91], a[248:251], v[104:107]\n"
"v_mfma_f32_16x16x32_bf16 v[112:115], v[64:67], a[252:255], 0\n"
"v_mfma_f32_16x16x32_bf16 v[112:115], v[68:71], v[20:23], v[112:115]\n"
"s_add_u32 s58, s50, 0x180000\n"
"ds_write_addtid_b32 v104 offset:1536\n"
"v_mfma_f32_16x16x32_bf16 v[112:115], v[72:75], v[24:27], v[112:115]\n"
"s_and_b32 s58, s58, 0x3ffffff\n"
"ds_write_addtid_b32 v105 offset:6176\n"
"v_mfma_f32_16x16x32_bf16 v[112:115], v[76:79], v[28:31], v[112:115]\n"
"s_add_u32 s56, s40, s58\n"
"ds_write_addtid_b32 v106 offset:10880\n"
"v_mfma_f32_16x16x32_bf16 v[112:115], v[80:83], v[32:35], v[112:115]\n"
"s_addc_u32 s57, s41, 0\n"
"ds_write_addtid_b32 v107 offset:15520\n"
"v_mfma_f32_16x16x32_bf16 v[112:115], v[84:87], v[36:39], v[112:115]\n"
"s_waitcnt vmcnt(6)\n"
"v_cmp_ne_u64 vcc, s[48:49], v[128:129]\n"
"v_mfma_f32_16x16x32_bf16 v[112:115], v[88:91], v[40:43], v[112:115]\n"
"v_mfma_f32_16x16x32_bf16 v[104:107], a[168:171], v[64:67], 0\n"
"s_cbranch_vccnz .LG_1s\n"
".LG_1r:\n"
"v_mfma_f32_16x16x32_bf16 v[104:107], a[172:175], v[68:71], v[104:107]\n"
"s_add_u32 s50, s50, 0x180000\n"
"ds_write_addtid_b32 v112 offset:1792\n"
"v_mfma_f32_16x16x32_bf16 v[104:107], a[176:179], v[72:75], v[104:107]\n"
"ds_write_addtid_b32 v113 offset:6432\n"
"v_mfma_f32_16x16x32_bf16 v[104:107], a[180:183], v[76:79], v[104:107]\n"
"ds_write_addtid_b32 v114 offset:11136\n"
"v_mfma_f32_16x16x32_bf16 v[104:107], a[184:187], v[80:83], v[104:107]\n"
"ds_write_addtid_b32 v115 offset:15776\n"
"global_load_dwordx4 v[100:103], v5, s[56:57] offset:0 sc1\n"
"v_mfma_f32_16x16x32_bf16 v[104:107], a[188:191], v[84:87], v[104:107]\n"
"v_mfma_f32_16x16x32_bf16 v[104:107], a[192:195], v[88:91], v[104:107]\n"
"v_mfma_f32_16x16x32_bf16 v[108:111], a[196:199], v[64:67], 0\n"
"v_mfma_f32_16x16x32_bf16 v[108:111], a[200:203], v[68:71], v[108:111]\n"
"s_waitcnt lgkmcnt(0)\n"
"v_mfma_f32_16x16x32_bf16 v[108:111], a[204:207], v[72:75], v[108:111]\n"
"s_barrier\n"
"v_mfma_f32_16x16x32_bf16 v[108:111], a[208:211], v[76:79], v[108:111]\n"
"global_load_dwordx4 v[68:71], v6, s[56:57] offset:0 sc1\n"
"ds_read_b128 v[64:67], v2 offset:2048\n"
"v_mfma_f32_16x16x32_bf16 v[108:111], a[212:215], v[80:83], v[108:111]\n"
"ds_read_b128 v[112:115], v3 offset:512\n"
"ds_read_b128 v[116:119], v4 offset:3072\n"
"v_mfma_f32_16x16x32_bf16 v[108:111], a[216:219], v[84:87], v[108:111]\n"
"ds_read_b128 v[120:123], v2 offset:2304\n"
"ds_read_b128 v[124:127], v3 offset:768\n"
"v_mfma_f32_16x16x32_bf16 v[108:111], a[220:223], v[88:91], v[108:111]\n"
"ds_read_b128 v[128:131], v4 offset:3328\n"
"v_mfma_f32_16x16x32_bf16 v[84:87], v[92:95], a[0:3], 0\n"
"global_load_dwordx4 v[72:75], v7, s[56:57] offset:0 sc1\n"
"v_mfma_f32_16x16x32_bf16 v[84:87], v[44:47], a[4:7], v[84:87]\n"
"s_waitcnt vmcnt(8)\n"
"v_mfma_f32_16x16x32_bf16 v[84:87], v[48:51], a[8:11], v[84:87]\n"
"v_mfma_f32_16x16x32_bf16 v[84:87], v[52:55], a[12:15], v[84:87]\n"
"s_waitcnt vmcnt(6)\n"
"v_mfma_f32_16x16x32_bf16 v[84:87], v[56:59], a[16:19], v[84:87]\n"
"s_waitcnt lgkmcnt(3)\n"
"v_add_f32 v64, v64, v112\n"
"v_add_f32 v65, v65, v113\n"
"v_mfma_f32_16x16x32_bf16 v[84:87], v[60:63], a[20:23], v[84:87]\n"
"s_waitcnt vmcnt(5)\n"
"global_load_dwordx4 v[76:79], v8, s[56:57] offset:0 sc1\n"
"v_add_f32 v66, v66, v114\n"
"v_mfma_f32_16x16x32_bf16 v[84:87], v[96:99], a[24:27], v[84:87]\n"
"buffer_load_dwordx2 v[132:133], v12, s[40:43], s50 offen sc1\n"
"v_mfma_f32_16x16x32_bf16 v[88:91], v[92:95], a[28:31], 0\n"
"v_add_f32 v67, v67, v115\n"
"v_add_f32 v64, v64, v116\n"
"v_mfma_f32_16x16x32_bf16 v[88:91], v[44:47], a[32:35], v[88:91]\n"
"s_waitcnt vmcnt(5)\n"
"buffer_store_dwordx2 v[0:1], v14, s[40:43], s95 offen offset:0 sc1\n"
"ds_write_addtid_b32 v84 offset:2048\n"
"v_mfma_f32_16x16x32_bf16 v[88:91], v[48:51], a[36:39], v[88:91]\n"
"ds_write_addtid_b32 v85 offset:6688\n"
"v_add_f32 v65, v65, v117\n"
"v_mfma_f32_16x16x32_bf16 v[88:91], v[52:55], a[40:43], v[88:91]\n"
"ds_write_addtid_b32 v86 offset:11392\n"
"global_load_dwordx4 v[80:83], v9, s[56:57] offset:0 sc1\n"
"v_mfma_f32_16x16x32_bf16 v[88:91], v[56:59], a[44:47], v[88:91]\n"
"ds_write_addtid_b32 v87 offset:16032\n"
"v_add_f32 v66, v66, v118\n"
"v_mfma_f32_16x16x32_bf16 v[88:91], v[60:63], a[48:51], v[88:91]\n"
"v_add_f32 v67, v67, v119\n"
"v_add_f32 v64, v64, v104\n"
"v_mfma_f32_16x16x32_bf16 v[88:91], v[96:99], a[52:55], v[88:91]\n"
"v_add_f32 v65, v65, v105\n"
"v_add_f32 v66, v66, v106\n"
"v_mfma_f32_16x16x32_bf16 v[112:115], v[92:95], a[56:59], 0\n"
"v_add_f32 v67, v67, v107\n"
"v_mfma_f32_16x16x32_bf16 v[112:115], v[44:47], a[60:63], v[112:115]\n"
"ds_write_addtid_b32 v88 offset:2304\n"
"global_load_dwordx4 v[84:87], v10, s[56:57] offset:0 sc1\n"
"v_mfma_f32_16x16x32_bf16 v[112:115], v[48:51], a[64:67], v[112:115]\n"
"s_add_u32 s93, s50, 0xffd00000\n"
"s_and_b32 s95, s93, 0xfbffffff\n"
"ds_write_addtid_b32 v89 offset:6944\n"
"v_mfma_f32_16x16x32_bf16 v[112:115], v[52:55], a[68:71], v[112:115]\n"
"ds_write_addtid_b32 v90 offset:11648\n"
"v_cvt_pk_bf16_f32 v104, v64, v65\n"
"v_mfma_f32_16x16x32_bf16 v[112:115], v[56:59], a[72:75], v[112:115]\n"
"ds_write_addtid_b32 v91 offset:16288\n"
"v_cvt_pk_bf16_f32 v105, v66, v67\n"
"v_mfma_f32_16x16x32_bf16 v[112:115], v[60:63], a[76:79], v[112:115]\n"
"s_waitcnt lgkmcnt(8)\n"
"v_add_f32 v120, v120, v124\n"
"v_add_f32 v121, v121, v125\n"
"v_mfma_f32_16x16x32_bf16 v[112:115], v[96:99], a[80:83], v[112:115]\n"
"global_load_dwordx4 v[88:91], v11, s[56:57] offset:0 sc1\n"
"v_add_f32 v122, v122, v126\n"
"v_mfma_f32_16x16x32_bf16 v[64:67], v[92:95], a[84:87], 0\n"
"v_add_f32 v123, v123, v127\n"
"v_add_f32 v120, v120, v128\n"
"v_mfma_f32_16x16x32_bf16 v[64:67], v[44:47], a[88:91], v[64:67]\n"
"ds_write_addtid_b32 v112 offset:2560\n"
"v_add_f32 v121, v121, v129\n"
"v_mfma_f32_16x16x32_bf16 v[64:67], v[48:51], a[92:95], v[64:67]\n"
"ds_write_addtid_b32 v113 offset:7200\n"
"v_add_f32 v122, v122, v130\n"
"v_mfma_f32_16x16x32_bf16 v[64:67], v[52:55], a[96:99], v[64:67]\n"
"ds_write_addtid_b32 v114 offset:11904\n"
"v_add_f32 v123, v123, v131\n"
"v_mfma_f32_16x16x32_bf16 v[64:67], v[56:59], a[100:103], v[64:67]\n"
"ds_write_addtid_b32 v115 offset:16544\n"
"v_add_f32 v120, v120, v108\n"
"v_mfma_f32_16x16x32_bf16 v[64:67], v[60:63], a[104:107], v[64:67]\n"
"v_add_f32 v121, v121, v109\n"
"v_add_f32 v122, v122, v110\n"
"v_mfma_f32_16x16x32_bf16 v[64:67], v[96:99], a[108:111], v[64:67]\n"
"v_add_f32 v123, v123, v111\n"
"v_cvt_pk_bf16_f32 v106, v120, v121\n"
"v_mfma_f32_16x16x32_bf16 v[112:115], v[92:95], a[112:115], 0\n"
"v_cvt_pk_bf16_f32 v107, v122, v123\n"
"v_mfma_f32_16x16x32_bf16 v[112:115], v[44:47], a[116:119], v[112:115]\n"
"ds_write_addtid_b32 v64 offset:2816\n"
"v_mfma_f32_16x16x32_bf16 v[112:115], v[48:51], a[120:123], v[112:115]\n"
"ds_write_addtid_b32 v65 offset:7456\n"
"v_mfma_f32_16x16x32_bf16 v[112:115], v[52:55], a[124:127], v[112:115]\n"
"ds_write_addtid_b32 v66 offset:12160\n"
"v_mfma_f32_16x16x32_bf16 v[112:115], v[56:59], a[128:131], v[112:115]\n"
"ds_write_addtid_b32 v67 offset:16800\n"
"v_mfma_f32_16x16x32_bf16 v[112:115], v[60:63], a[132:135], v[112:115]\n"
"v_mfma_f32_16x16x32_bf16 v[112:115], v[96:99], a[136:139], v[112:115]\n"
"v_mfma_f32_16x16x32_bf16 v[108:111], v[92:95], a[140:143], 0\n"
"v_mfma_f32_16x16x32_bf16 v[108:111], v[44:47], a[144:147], v[108:111]\n"
"buffer_store_dwordx4 v[104:107], v13, s[40:43], s95 offen sc1\n"
"ds_write_addtid_b32 v112 offset:3072\n"
"v_mfma_f32_16x16x32_bf16 v[108:111], v[48:51], a[148:151], v[108:111]\n"
"ds_write_addtid_b32 v113 offset:7712\n"
"v_mfma_f32_16x16x32_bf16 v[108:111], v[52:55], a[152:155], v[108:111]\n"
"ds_write_addtid_b32 v114 offset:12416\n"
"v_mfma_f32_16x16x32_bf16 v[108:111], v[56:59], a[156:159], v[108:111]\n"
"ds_write_addtid_b32 v115 offset:17056\n"
"v_mfma_f32_16x16x32_bf16 v[108:111], v[60:63], a[160:163], v[108:111]\n"
"v_mfma_f32_16x16x32_bf16 v[108:111], v[96:99], a[164:167], v[108:111]\n"
"v_mfma_f32_16x16x32_bf16 v[64:67], v[92:95], a[224:227], 0\n"
"v_mfma_f32_16x16x32_bf16 v[64:67], v[44:47], a[228:231], v[64:67]\n"
"ds_write_addtid_b32 v108 offset:3328\n"
"v_mfma_f32_16x16x32_bf16 v[64:67], v[48:51], a[232:235], v[64:67]\n"
"ds_write_addtid_b32 v109 offset:7968\n"
"v_mfma_f32_16x16x32_bf16 v[64:67], v[52:55], a[236:239], v[64:67]\n"
"ds_write_addtid_b32 v110 offset:12672\n"
"v_mfma_f32_16x16x32_bf16 v[64:67], v[56:59], a[240:243], v[64:67]\n"
"ds_write_addtid_b32 v111 offset:17312\n"
"v_mfma_f32_16x16x32_bf16 v[64:67], v[60:63], a[244:247], v[64:67]\n"
"v_mfma_f32_16x16x32_bf16 v[64:67], v[96:99], a[248:251], v[64:67]\n"
"v_mfma_f32_16x16x32_bf16 v[112:115], v[92:95], a[252:255], 0\n"
"v_mfma_f32_16x16x32_bf16 v[112:115], v[44:47], v[20:23], v[112:115]\n"
"s_add_u32 s58, s50, 0x180000\n"
"ds_write_addtid_b32 v64 offset:3584\n"
"v_mfma_f32_16x16x32_bf16 v[112:115], v[48:51], v[24:27], v[112:115]\n"
"s_and_b32 s58, s58, 0x3ffffff\n"
"ds_write_addtid_b32 v65 offset:8224\n"
"v_mfma_f32_16x16x32_bf16 v[112:115], v[52:55], v[28:31], v[112:115]\n"
"s_add_u32 s56, s40, s58\n"
"ds_write_addtid_b32 v66 offset:12928\n"
"v_mfma_f32_16x16x32_bf16 v[112:115], v[56:59], v[32:35], v[112:115]\n"
"s_addc_u32 s57, s41, 0\n"
"ds_write_addtid_b32 v67 offset:17568\n"
"v_mfma_f32_16x16x32_bf16 v[112:115], v[60:63], v[36:39], v[112:115]\n"
"s_waitcnt vmcnt(5)\n"
"v_cmp_ne_u64 vcc, s[48:49], v[132:133]\n"
"v_mfma_f32_16x16x32_bf16 v[112:115], v[96:99], v[40:43], v[112:115]\n"
"v_mfma_f32_16x16x32_bf16 v[116:119], a[168:171], v[92:95], 0\n"
"s_cbranch_vccnz .LG_2s\n"
".LG_2r:\n"
"v_mfma_f32_16x16x32_bf16 v[116:119], a[172:175], v[44:47], v[116:119]\n"
"s_add_u32 s50, s50, 0x180000\n"
"ds_write_addtid_b32 v112 offset:3840\n"
"v_mfma_f32_16x16x32_bf16 v[116:119], a[176:179], v[48:51], v[116:119]\n"
"ds_write_addtid_b32 v113 offset:8480\n"
"v_mfma_f32_16x16x32_bf16 v[116:119], a[180:183], v[52:55], v[116:119]\n"
"ds_write_addtid_b32 v114 offset:13184\n"
"v_mfma_f32_16x16x32_bf16 v[116:119], a[184:187], v[56:59], v[116:119]\n"
"ds_write_addtid_b32 v115 offset:17824\n"
"v_mfma_f32_16x16x32_bf16 v[116:119], a[188:191], v[60:63], v[116:119]\n"
"global_load_dwordx4 v[104:107], v5, s[56:57] offset:0 sc1\n"
"v_mfma_f32_16x16x32_bf16 v[116:119], a[192:195], v[96:99], v[116:119]\n"
"v_mfma_f32_16x16x32_bf16 v[112:115], a[196:199], v[92:95], 0\n"
"v_mfma_f32_16x16x32_bf16 v[112:115], a[200:203], v[44:47], v[112:115]\n"
"s_waitcnt lgkmcnt(0)\n"
"global_load_dwordx4 v[108:111], v6, s[56:57] offset:0 sc1\n"
"v_mfma_f32_16x16x32_bf16 v[112:115], a[204:207], v[48:51], v[112:115]\n"
"s_barrier\n"
"v_mfma_f32_16x16x32_bf16 v[112:115], a[208:211], v[52:55], v[112:115]\n"
"ds_read_b128 v[120:123], v2 offset:0\n"
"ds_read_b128 v[64:67], v3 offset:2560\n"
"v_mfma_f32_16x16x32_bf16 v[112:115], a[212:215], v[56:59], v[112:115]\n"
"ds_read_b128 v[92:95], v4 offset:1024\n"
"ds_read_b128 v[124:127], v2 offset:256\n"
"v_mfma_f32_16x16x32_bf16 v[112:115], a[216:219], v[60:63], v[112:115]\n"
"ds_read_b128 v[128:131], v3 offset:2816\n"
"ds_read_b128 v[132:135], v4 offset:1280\n"
"v_mfma_f32_16x16x32_bf16 v[112:115], a[220:223], v[96:99], v[112:115]\n"
"s_branch .LG_top\n"
".LG_1s:\n"
"s_cmp_ge_u32 s50, s53\n"
"s_cbranch_scc1 .LG_t0\n"
"s_cmp_ge_u32 s50, s52\n"
"s_cbranch_scc1 .LG_1r\n"
"s_sub_u32 s60, s50, 0x0\n"
"s_mov_b32 s61, 200000\n"
"s_nop 7\n"
".LG_1p:\n"
"buffer_load_dwordx2 v[128:129], v12, s[40:43], s60 offen sc1\n"
"s_waitcnt vmcnt(0)\n"
"v_cmp_ne_u64 vcc, s[48:49], v[128:129]\n"
"s_cbranch_vccz .LG_1px\n"
"s_sleep 1\n"
"s_sub_u32 s61, s61, 1\n"
"s_cbranch_scc0 .LG_1p\n"
"s_branch .LG_end\n"
".LG_1px:\n"
"s_branch .LG_1r\n"
".LG_2s:\n"
"s_cmp_ge_u32 s50, s53\n"
"s_cbranch_scc1 .LG_t1\n"
"s_cmp_ge_u32 s50, s52\n"
"s_cbranch_scc1 .LG_2r\n"
"s_sub_u32 s60, s50, 0x0\n"
"s_mov_b32 s61, 200000\n"
"s_nop 7\n"
".LG_2p:\n"
"buffer_load_dwordx2 v[132:133], v12, s[40:43], s60 offen sc1\n"
"s_waitcnt vmcnt(0)\n"
"v_cmp_ne_u64 vcc, s[48:49], v[132:133]\n"
"s_cbranch_vccz .LG_2px\n"
"s_sleep 1\n"
"s_sub_u32 s61, s61, 1\n"
"s_cbranch_scc0 .LG_2p\n"
"s_branch .LG_end\n"
".LG_2px:\n"
"s_branch .LG_2r\n"
".LG_3s:\n"
"s_cmp_ge_u32 s50, s53\n"
"s_cbranch_scc1 .LG_t2\n"
"s_cmp_ge_u32 s50, s52\n"
"s_cbranch_scc1 .LG_3r\n"
"s_sub_u32 s60, s50, 0x0\n"
"s_mov_b32 s61, 200000\n"
"s_nop 7\n"
".LG_3p:\n"
"buffer_load_dwordx2 v[136:137], v12, s[40:43], s60 offen sc1\n"
"s_waitcnt vmcnt(0)\n"
"v_cmp_ne_u64 vcc, s[48:49], v[136:137]\n"
"s_cbranch_vccz .LG_3px\n"
"s_sleep 1\n"
"s_sub_u32 s61, s61, 1\n"
"s_cbranch_scc0 .LG_3p\n"
"s_branch .LG_end\n"
".LG_3px:\n"
"s_branch .LG_3r\n"
".LG_4s:\n"
"s_cmp_ge_u32 s50, s53\n"
"s_cbranch_scc1 .LG_t3\n"
"s_cmp_ge_u32 s50, s52\n"
"s_cbranch_scc1 .LG_4r\n"
"s_sub_u32 s60, s50, 0x0\n"
"s_mov_b32 s61, 200000\n"
"s_nop 7\n"
".LG_4p:\n"
"buffer_load_dwordx2 v[168:169], v12, s[40:43], s60 offen sc1\n"
"s_waitcnt vmcnt(0)\n"
"v_cmp_ne_u64 vcc, s[48:49], v[168:169]\n"
"s_cbranch_vccz .LG_4px\n"
"s_sleep 1\n"
"s_sub_u32 s61, s61, 1\n"
"s_cbranch_scc0 .LG_4p\n"
"s_branch .LG_end\n"
".LG_4px:\n"
"s_branch .LG_4r\n"
".LG_t0:\n"
"v_mfma_f32_16x16x32_bf16 v[104:107], a[172:175], v[68:71], v[104:107]\n"
"ds_write_addtid_b32 v112 offset:1792\n"
"v_mfma_f32_16x16x32_bf16 v[104:107], a[176:179], v[72:75], v[104:107]\n"
"ds_write_addtid_b32 v113 offset:6432\n"
"v_mfma_f32_16x16x32_bf16 v[104:107], a[180:183], v[76:79], v[104:107]\n"
"ds_write_addtid_b32 v114 offset:11136\n"
"v_mfma_f32_16x16x32_bf16 v[104:107], a[184:187], v[80:83], v[104:107]\n"
"ds_write_addtid_b32 v115 offset:15776\n"
"v_mfma_f32_16x16x32_bf16 v[104:107], a[188:191], v[84:87], v[104:107]\n"
"v_mfma_f32_16x16x32_bf16 v[104:107], a[192:195], v[88:91], v[104:107]\n"
"v_mfma_f32_16x16x32_bf16 v[108:111], a[196:199], v[64:67], 0\n"
"v_mfma_f32_16x16x32_bf16 v[108:111], a[200:203], v[68:71], v[108:111]\n"
"s_waitcnt lgkmcnt(0)\n"
"v_mfma_f32_16x16x32_bf16 v[108:111], a[204:207], v[72:75], v[108:111]\n"
"s_barrier\n"
"v_mfma_f32_16x16x32_bf16 v[108:111], a[208:211], v[76:79], v[108:111]\n"
"ds_read_b128 v[64:67], v2 offset:2048\n"
"v_mfma_f32_16x16x32_bf16 v[108:111], a[212:215], v[80:83], v[108:111]\n"
"ds_read_b128 v[112:115], v3 offset:512\n"
"ds_read_b128 v[116:119], v4 offset:3072\n"
"v_mfma_f32_16x16x32_bf16 v[108:111], a[216:219], v[84:87], v[108:111]\n"
"ds_read_b128 v[120:123], v2 offset:2304\n"
"ds_read_b128 v[124:127], v3 offset:768\n"
"v_mfma_f32_16x16x32_bf16 v[108:111], a[220:223], v[88:91], v[108:111]\n"
"s_waitcnt vmcnt(8)\n"
"ds_read_b128 v[128:131], v4 offset:3328\n"
"v_mfma_f32_16x16x32_bf16 v[84:87], v[92:95], a[0:3], 0\n"
"v_mfma_f32_16x16x32_bf16 v[84:87], v[44:47], a[4:7], v[84:87]\n"
"s_waitcnt vmcnt(5)\n"
"v_mfma_f32_16x16x32_bf16 v[84:87], v[48:51], a[8:11], v[84:87]\n"
"v_mfma_f32_16x16x32_bf16 v[84:87], v[52:55], a[12:15], v[84:87]\n"
"s_waitcnt vmcnt(3)\n"
"v_mfma_f32_16x16x32_bf16 v[84:87], v[56:59], a[16:19], v[84:87]\n"
"s_waitcnt lgkmcnt(3)\n"
"v_add_f32 v64, v64, v112\n"
"v_add_f32 v65, v65, v113\n"
"v_mfma_f32_16x16x32_bf16 v[84:87], v[60:63], a[20:23], v[84:87]\n"
"s_waitcnt vmcnt(2)\n"
"v_add_f32 v66, v66, v114\n"
"v_mfma_f32_16x16x32_bf16 v[84:87], v[96:99], a[24:27], v[84:87]\n"
"v_mfma_f32_16x16x32_bf16 v[88:91], v[92:95], a[28:31], 0\n"
"v_add_f32 v67, v67, v115\n"
"v_add_f32 v64, v64, v116\n"
"v_mfma_f32_16x16x32_bf16 v[88:91], v[44:47], a[32:35], v[88:91]\n"
"s_waitcnt vmcnt(0)\n"
"buffer_store_dwordx2 v[0:1], v14, s[40:43], s95 offen offset:0 sc1\n"
"ds_write_addtid_b32 v84 offset:2048\n"
"v_mfma_f32_16x16x32_bf16 v[88:91], v[48:51], a[36:39], v[88:91]\n"
"ds_write_addtid_b32 v85 offset:6688\n"
"v_add_f32 v65, v65, v117\n"
"v_mfma_f32_16x16x32_bf16 v[88:91], v[52:55], a[40:43], v[88:91]\n"
"ds_write_addtid_b32 v86 offset:11392\n"
"v_mfma_f32_16x16x32_bf16 v[88:91], v[56:59], a[44:47], v[88:91]\n"
"ds_write_addtid_b32 v87 offset:16032\n"
"v_add_f32 v66, v66, v118\n"
"v_mfma_f32_16x16x32_bf16 v[88:91], v[60:63], a[48:51], v[88:91]\n"
"v_add_f32 v67, v67, v119\n"
"v_add_f32 v64, v64, v104\n"
"v_mfma_f32_16x16x32_bf16 v[88:91], v[96:99], a[52:55], v[88:91]\n"
"v_add_f32 v65, v65, v105\n"
"v_add_f32 v66, v66, v106\n"
"v_mfma_f32_16x16x32_bf16 v[112:115], v[92:95], a[56:59], 0\n"
"v_add_f32 v67, v67, v107\n"
"v_mfma_f32_16x16x32_bf16 v[112:115], v[44:47], a[60:63], v[112:115]\n"
"ds_write_addtid_b32 v88 offset:2304\n"
"v_mfma_f32_16x16x32_bf16 v[112:115], v[48:51], a[64:67], v[112:115]\n"
"s_add_u32 s93, s50, 0xffe80000\n"
"s_and_b32 s95, s93, 0xfbffffff\n"
"ds_write_addtid_b32 v89 offset:6944\n"
"v_mfma_f32_16x16x32_bf16 v[112:115], v[52:55], a[68:71], v[112:115]\n"
"ds_write_addtid_b32 v90 offset:11648\n"
"v_cvt_pk_bf16_f32 v104, v64, v65\n"
"v_mfma_f32_16x16x32_bf16 v[112:115], v[56:59], a[72:75], v[112:115]\n"
"ds_write_addtid_b32 v91 offset:16288\n"
"v_cvt_pk_bf16_f32 v105, v66, v67\n"
"v_mfma_f32_16x16x32_bf16 v[112:115], v[60:63], a[76:79], v[112:115]\n"
"s_waitcnt lgkmcnt(8)\n"
"v_add_f32 v120, v120, v124\n"
"v_add_f32 v121, v121, v125\n"
"v_mfma_f32_16x16x32_bf16 v[112:115], v[96:99], a[80:83], v[112:115]\n"
"v_add_f32 v122, v122, v126\n"
"v_mfma_f32_16x16x32_bf16 v[64:67], v[92:95], a[84:87], 0\n"
"v_add_f32 v123, v123, v127\n"
"v_add_f32 v120, v120, v128\n"
"v_mfma_f32_16x16x32_bf16 v[64:67], v[44:47], a[88:91], v[64:67]\n"
"ds_write_addtid_b32 v112 offset:2560\n"
"v_add_f32 v121, v121, v129\n"
"v_mfma_f32_16x16x32_bf16 v[64:67], v[48:51], a[92:95], v[64:67]\n"
"ds_write_addtid_b32 v113 offset:7200\n"
"v_add_f32 v122, v122, v130\n"
"v_mfma_f32_16x16x32_bf16 v[64:67], v[52:55], a[96:99], v[64:67]\n"
"ds_write_addtid_b32 v114 offset:11904\n"
"v_add_f32 v123, v123, v131\n"
"v_mfma_f32_16x16x32_bf16 v[64:67], v[56:59], a[100:103], v[64:67]\n"
"ds_write_addtid_b32 v115 offset:16544\n"
"v_add_f32 v120, v120, v108\n"
"v_mfma_f32_16x16x32_bf16 v[64:67], v[60:63], a[104:107], v[64:67]\n"
"v_add_f32 v121, v121, v109\n"
"v_add_f32 v122, v122, v110\n"
"v_mfma_f32_16x16x32_bf16 v[64:67], v[96:99], a[108:111], v[64:67]\n"
"v_add_f32 v123, v123, v111\n"
"v_cvt_pk_bf16_f32 v106, v120, v121\n"
"v_mfma_f32_16x16x32_bf16 v[112:115], v[92:95], a[112:115], 0\n"
"v_cvt_pk_bf16_f32 v107, v122, v123\n"
"v_mfma_f32_16x16x32_bf16 v[112:115], v[44:47], a[116:119], v[112:115]\n"
"ds_write_addtid_b32 v64 offset:2816\n"
"v_mfma_f32_16x16x32_bf16 v[112:115], v[48:51], a[120:123], v[112:115]\n"
"ds_write_addtid_b32 v65 offset:7456\n"
"v_mfma_f32_16x16x32_bf16 v[112:115], v[52:55], a[124:127], v[112:115]\n"
"ds_write_addtid_b32 v66 offset:12160\n"
"v_mfma_f32_16x16x32_bf16 v[112:115], v[56:59], a[128:131], v[112:115]\n"
"ds_write_addtid_b32 v67 offset:16800\n"
"v_mfma_f32_16x16x32_bf16 v[112:115], v[60:63], a[132:135], v[112:115]\n"
"v_mfma_f32_16x16x32_bf16 v[112:115], v[96:99], a[136:139], v[112:115]\n"
"v_mfma_f32_16x16x32_bf16 v[108:111], v[92:95], a[140:143], 0\n"
"v_mfma_f32_16x16x32_bf16 v[108:111], v[44:47], a[144:147], v[108:111]\n"
"buffer_store_dwordx4 v[104:107], v13, s[40:43], s95 offen sc1\n"
"ds_write_addtid_b32 v112 offset:3072\n"
"v_mfma_f32_16x16x32_bf16 v[108:111], v[48:51], a[148:151], v[108:111]\n"
"ds_write_addtid_b32 v113 offset:7712\n"
"v_mfma_f32_16x16x32_bf16 v[108:111], v[52:55], a[152:155], v[108:111]\n"
"ds_write_addtid_b32 v114 offset:12416\n"
"v_mfma_f32_16x16x32_bf16 v[108:111], v[56:59], a[156:159], v[108:111]\n"
"ds_write_addtid_b32 v115 offset:17056\n"
"v_mfma_f32_16x16x32_bf16 v[108:111], v[60:63], a[160:163], v[108:111]\n"
"v_mfma_f32_16x16x32_bf16 v[108:111], v[96:99], a[164:167], v[108:111]\n"
"v_mfma_f32_16x16x32_bf16 v[64:67], v[92:95], a[224:227], 0\n"
"v_mfma_f32_16x16x32_bf16 v[64:67], v[44:47], a[228:231], v[64:67]\n"
"ds_write_addtid_b32 v108 offset:3328\n"
"v_mfma_f32_16x16x32_bf16 v[64:67], v[48:51], a[232:235], v[64:67]\n"
"ds_write_addtid_b32 v109 offset:7968\n"
"v_mfma_f32_16x16x32_bf16 v[64:67], v[52:55], a[236:239], v[64:67]\n"
"ds_write_addtid_b32 v110 offset:12672\n"
"v_mfma_f32_16x16x32_bf16 v[64:67], v[56:59], a[240:243], v[64:67]\n"
"ds_write_addtid_b32 v111 offset:17312\n"
"v_mfma_f32_16x16x32_bf16 v[64:67], v[60:63], a[244:247], v[64:67]\n"
"v_mfma_f32_16x16x32_bf16 v[64:67], v[96:99], a[248:251], v[64:67]\n"
"v_mfma_f32_16x16x32_bf16 v[112:115], v[92:95], a[252:255], 0\n"
"v_mfma_f32_16x16x32_bf16 v[112:115], v[44:47], v[20:23], v[112:115]\n"
"ds_write_addtid_b32 v64 offset:3584\n"
"v_mfma_f32_16x16x32_bf16 v[112:115], v[48:51], v[24:27], v[112:115]\n"
"ds_write_addtid_b32 v65 offset:8224\n"
"v_mfma_f32_16x16x32_bf16 v[112:115], v[52:55], v[28:31], v[112:115]\n"
"ds_write_addtid_b32 v66 offset:12928\n"
"v_mfma_f32_16x16x32_bf16 v[112:115], v[56:59], v[32:35], v[112:115]\n"
"ds_write_addtid_b32 v67 offset:17568\n"
"v_mfma_f32_16x16x32_bf16 v[112:115], v[60:63], v[36:39], v[112:115]\n"
"v_mfma_f32_16x16x32_bf16 v[112:115], v[96:99], v[40:43], v[112:115]\n"
"v_mfma_f32_16x16x32_bf16 v[116:119], a[168:171], v[92:95], 0\n"
"v_mfma_f32_16x16x32_bf16 v[116:119], a[172:175], v[44:47], v[116:119]\n"
"ds_write_addtid_b32 v112 offset:3840\n"
"v_mfma_f32_16x16x32_bf16 v[116:119], a[176:179], v[48:51], v[116:119]\n"
"ds_write_addtid_b32 v113 offset:8480\n"
"v_mfma_f32_16x16x32_bf16 v[116:119], a[180:183], v[52:55], v[116:119]\n"
"ds_write_addtid_b32 v114 offset:13184\n"
"v_mfma_f32_16x16x32_bf16 v[116:119], a[184:187], v[56:59], v[116:119]\n"
"ds_write_addtid_b32 v115 offset:17824\n"
"v_mfma_f32_16x16x32_bf16 v[116:119], a[188:191], v[60:63], v[116:119]\n"
"v_mfma_f32_16x16x32_bf16 v[116:119], a[192:195], v[96:99], v[116:119]\n"
"v_mfma_f32_16x16x32_bf16 v[112:115], a[196:199], v[92:95], 0\n"
"v_mfma_f32_16x16x32_bf16 v[112:115], a[200:203], v[44:47], v[112:115]\n"
"s_waitcnt lgkmcnt(0)\n"
"v_mfma_f32_16x16x32_bf16 v[112:115], a[204:207], v[48:51], v[112:115]\n"
"s_barrier\n"
"v_mfma_f32_16x16x32_bf16 v[112:115], a[208:211], v[52:55], v[112:115]\n"
"ds_read_b128 v[120:123], v2 offset:0\n"
"ds_read_b128 v[64:67], v3 offset:2560\n"
"v_mfma_f32_16x16x32_bf16 v[112:115], a[212:215], v[56:59], v[112:115]\n"
"ds_read_b128 v[92:95], v4 offset:1024\n"
"ds_read_b128 v[124:127], v2 offset:256\n"
"v_mfma_f32_16x16x32_bf16 v[112:115], a[216:219], v[60:63], v[112:115]\n"
"ds_read_b128 v[128:131], v3 offset:2816\n"
"ds_read_b128 v[132:135], v4 offset:1280\n"
"v_mfma_f32_16x16x32_bf16 v[112:115], a[220:223], v[96:99], v[112:115]\n"
"s_waitcnt lgkmcnt(3)\n"
"v_add_f32 v120, v120, v64\n"
"v_add_f32 v121, v121, v65\n"
"v_add_f32 v122, v122, v66\n"
"v_add_f32 v123, v123, v67\n"
"v_add_f32 v120, v120, v92\n"
"v_add_f32 v121, v121, v93\n"
"v_add_f32 v122, v122, v94\n"
"v_add_f32 v123, v123, v95\n"
"v_add_f32 v120, v120, v116\n"
"v_add_f32 v121, v121, v117\n"
"v_add_f32 v122, v122, v118\n"
"v_add_f32 v123, v123, v119\n"
"s_waitcnt vmcnt(0)\n"
"buffer_store_dwordx2 v[0:1], v14, s[40:43], s93 offen offset:0 sc1\n"
"s_add_u32 s95, s50, 0x0\n"
"s_and_b32 s94, s95, 0xfbffffff\n"
"v_cvt_pk_bf16_f32 v92, v120, v121\n"
"v_cvt_pk_bf16_f32 v93, v122, v123\n"
"s_waitcnt lgkmcnt(0)\n"
"v_add_f32 v124, v124, v128\n"
"v_add_f32 v125, v125, v129\n"
"v_add_f32 v126, v126, v130\n"
"v_add_f32 v127, v127, v131\n"
"v_add_f32 v124, v124, v132\n"
"v_add_f32 v125, v125, v133\n"
"v_add_f32 v126, v126, v134\n"
"v_add_f32 v127, v127, v135\n"
"v_add_f32 v124, v124, v112\n"
"v_add_f32 v125, v125, v113\n"
"v_add_f32 v126, v126, v114\n"
"v_add_f32 v127, v127, v115\n"
"v_cvt_pk_bf16_f32 v94, v124, v125\n"
"v_cvt_pk_bf16_f32 v95, v126, v127\n"
"buffer_store_dwordx4 v[92:95], v13, s[40:43], s94 offen sc1\n"
"s_waitcnt vmcnt(0)\n"
"buffer_store_dwordx2 v[0:1], v14, s[40:43], s95 offen offset:0 sc1\n"
"s_branch .LG_end\n"
".LG_t1:\n"
"v_mfma_f32_16x16x32_bf16 v[116:119], a[172:175], v[44:47], v[116:119]\n"
"ds_write_addtid_b32 v112 offset:3840\n"
"v_mfma_f32_16x16x32_bf16 v[116:119], a[176:179], v[48:51], v[116:119]\n"
"ds_write_addtid_b32 v113 offset:8480\n"
"v_mfma_f32_16x16x32_bf16 v[116:119], a[180:183], v[52:55], v[116:119]\n"
"ds_write_addtid_b32 v114 offset:13184\n"
"v_mfma_f32_16x16x32_bf16 v[116:119], a[184:187], v[56:59], v[116:119]\n"
"ds_write_addtid_b32 v115 offset:17824\n"
"v_mfma_f32_16x16x32_bf16 v[116:119], a[188:191], v[60:63], v[116:119]\n"
"v_mfma_f32_16x16x32_bf16 v[116:119], a[192:195], v[96:99], v[116:119]\n"
"v_mfma_f32_16x16x32_bf16 v[112:115], a[196:199], v[92:95], 0\n"
"v_mfma_f32_16x16x32_bf16 v[112:115], a[200:203], v[44:47], v[112:115]\n"
"s_waitcnt lgkmcnt(0)\n"
"v_mfma_f32_16x16x32_bf16 v[112:115], a[204:207], v[48:51], v[112:115]\n"
"s_barrier\n"
"v_mfma_f32_16x16x32_bf16 v[112:115], a[208:211], v[52:55], v[112:115]\n"
"ds_read_b128 v[120:123], v2 offset:0\n"
"ds_read_b128 v[64:67], v3 offset:2560\n"
"v_mfma_f32_16x16x32_bf16 v[112:115], a[212:215], v[56:59], v[112:115]\n"
"ds_read_b128 v[92:95], v4 offset:1024\n"
"ds_read_b128 v[124:127], v2 offset:256\n"
"v_mfma_f32_16x16x32_bf16 v[112:115], a[216:219], v[60:63], v[112:115]\n"
"ds_read_b128 v[128:131], v3 offset:2816\n"
"ds_read_b128 v[132:135], v4 offset:1280\n"
"v_mfma_f32_16x16x32_bf16 v[112:115], a[220:223], v[96:99], v[112:115]\n"
"s_waitcnt vmcnt(8)\n"
"v_mfma_f32_16x16x32_bf16 v[56:59], v[100:103], a[0:3], 0\n"
"v_mfma_f32_16x16x32_bf16 v[56:59], v[68:71], a[4:7], v[56:59]\n"
"s_waitcnt vmcnt(6)\n"
"v_mfma_f32_16x16x32_bf16 v[56:59], v[72:75], a[8:11], v[56:59]\n"
"v_mfma_f32_16x16x32_bf16 v[56:59], v[76:79], a[12:15], v[56:59]\n"
"s_waitcnt vmcnt(2)\n"
"v_mfma_f32_16x16x32_bf16 v[56:59], v[80:83], a[16:19], v[56:59]\n"
"s_waitcnt lgkmcnt(3)\n"
"v_add_f32 v120, v120, v64\n"
"v_add_f32 v121, v121, v65\n"
"v_mfma_f32_16x16x32_bf16 v[56:59], v[84:87], a[20:23], v[56:59]\n"
"s_waitcnt vmcnt(1)\n"
"v_add_f32 v122, v122, v66\n"
"v_add_f32 v123, v123, v67\n"
"v_mfma_f32_16x16x32_bf16 v[56:59], v[88:91], a[24:27], v[56:59]\n"
"v_add_f32 v120, v120, v92\n"
"v_mfma_f32_16x16x32_bf16 v[60:63], v[100:103], a[28:31], 0\n"
"v_mfma_f32_16x16x32_bf16 v[60:63], v[68:71], a[32:35], v[60:63]\n"
"ds_write_addtid_b32 v56 offset:0\n"
"v_add_f32 v121, v121, v93\n"
"v_mfma_f32_16x16x32_bf16 v[60:63], v[72:75], a[36:39], v[60:63]\n"
"ds_write_addtid_b32 v57 offset:4640\n"
"v_mfma_f32_16x16x32_bf16 v[60:63], v[76:79], a[40:43], v[60:63]\n"
"ds_write_addtid_b32 v58 offset:9344\n"
"v_add_f32 v122, v122, v94\n"
"v_mfma_f32_16x16x32_bf16 v[60:63], v[80:83], a[44:47], v[60:63]\n"
"ds_write_addtid_b32 v59 offset:13984\n"
"v_add_f32 v123, v123, v95\n"
"v_mfma_f32_16x16x32_bf16 v[60:63], v[84:87], a[48:51], v[60:63]\n"
"v_add_f32 v120, v120, v116\n"
"v_add_f32 v121, v121, v117\n"
"v_mfma_f32_16x16x32_bf16 v[60:63], v[88:91], a[52:55], v[60:63]\n"
"v_add_f32 v122, v122, v118\n"
"v_add_f32 v123, v123, v119\n"
"v_mfma_f32_16x16x32_bf16 v[64:67], v[100:103], a[56:59], 0\n"
"s_waitcnt vmcnt(0)\n"
"buffer_store_dwordx2 v[0:1], v14, s[40:43], s93 offen offset:0 sc1\n"
"v_mfma_f32_16x16x32_bf16 v[64:67], v[68:71], a[60:63], v[64:67]\n"
"s_add_u32 s95, s50, 0xffe80000\n"
"s_and_b32 s94, s95, 0xfbffffff\n"
"ds_write_addtid_b32 v60 offset:256\n"
"v_mfma_f32_16x16x32_bf16 v[64:67], v[72:75], a[64:67], v[64:67]\n"
"ds_write_addtid_b32 v61 offset:4896\n"
"v_mfma_f32_16x16x32_bf16 v[64:67], v[76:79], a[68:71], v[64:67]\n"
"ds_write_addtid_b32 v62 offset:9600\n"
"v_cvt_pk_bf16_f32 v92, v120, v121\n"
"v_mfma_f32_16x16x32_bf16 v[64:67], v[80:83], a[72:75], v[64:67]\n"
"ds_write_addtid_b32 v63 offset:14240\n"
"v_cvt_pk_bf16_f32 v93, v122, v123\n"
"v_mfma_f32_16x16x32_bf16 v[64:67], v[84:87], a[76:79], v[64:67]\n"
"s_waitcnt lgkmcnt(8)\n"
"v_add_f32 v124, v124, v128\n"
"v_add_f32 v125, v125, v129\n"
"v_mfma_f32_16x16x32_bf16 v[64:67], v[88:91], a[80:83], v[64:67]\n"
"v_add_f32 v126, v126, v130\n"
"v_add_f32 v127, v127, v131\n"
"v_mfma_f32_16x16x32_bf16 v[96:99], v[100:103], a[84:87], 0\n"
"v_add_f32 v124, v124, v132\n"
"v_mfma_f32_16x16x32_bf16 v[96:99], v[68:71], a[88:91], v[96:99]\n"
"ds_write_addtid_b32 v64 offset:512\n"
"v_add_f32 v125, v125, v133\n"
"v_mfma_f32_16x16x32_bf16 v[96:99], v[72:75], a[92:95], v[96:99]\n"
"ds_write_addtid_b32 v65 offset:5152\n"
"v_add_f32 v126, v126, v134\n"
"v_mfma_f32_16x16x32_bf16 v[96:99], v[76:79], a[96:99], v[96:99]\n"
"ds_write_addtid_b32 v66 offset:9856\n"
"v_add_f32 v127, v127, v135\n"
"v_mfma_f32_16x16x32_bf16 v[96:99], v[80:83], a[100:103], v[96:99]\n"
"ds_write_addtid_b32 v67 offset:14496\n"
"v_add_f32 v124, v124, v112\n"
"v_mfma_f32_16x16x32_bf16 v[96:99], v[84:87], a[104:107], v[96:99]\n"
"v_add_f32 v125, v125, v113\n"
"v_add_f32 v126, v126, v114\n"
"v_mfma_f32_16x16x32_bf16 v[96:99], v[88:91], a[108:111], v[96:99]\n"
"v_add_f32 v127, v127, v115\n"
"v_cvt_pk_bf16_f32 v94, v124, v125\n"
"v_mfma_f32_16x16x32_bf16 v[64:67], v[100:103], a[112:115], 0\n"
"v_cvt_pk_bf16_f32 v95, v126, v127\n"
"v_mfma_f32_16x16x32_bf16 v[64:67], v[68:71], a[116:119], v[64:67]\n"
"ds_write_addtid_b32 v96 offset:768\n"
"v_mfma_f32_16x16x32_bf16 v[64:67], v[72:75], a[120:123], v[64:67]\n"
"ds_write_addtid_b32 v97 offset:5408\n"
"v_mfma_f32_16x16x32_bf16 v[64:67], v[76:79], a[124:127], v[64:67]\n"
"ds_write_addtid_b32 v98 offset:10112\n"
"v_mfma_f32_16x16x32_bf16 v[64:67], v[80:83], a[128:131], v[64:67]\n"
"ds_write_addtid_b32 v99 offset:14752\n"
"v_mfma_f32_16x16x32_bf16 v[64:67], v[84:87], a[132:135], v[64:67]\n"
"v_mfma_f32_16x16x32_bf16 v[64:67], v[88:91], a[136:139], v[64:67]\n"
"v_mfma_f32_16x16x32_bf16 v[112:115], v[100:103], a[140:143], 0\n"
"v_mfma_f32_16x16x32_bf16 v[112:115], v[68:71], a[144:147], v[112:115]\n"
"ds_write_addtid_b32 v64 offset:1024\n"
"v_mfma_f32_16x16x32_bf16 v[112:115], v[72:75], a[148:151], v[112:115]\n"
"ds_write_addtid_b32 v65 offset:5664\n"
"v_mfma_f32_16x16x32_bf16 v[112:115], v[76:79], a[152:155], v[112:115]\n"
"ds_write_addtid_b32 v66 offset:10368\n"
"v_mfma_f32_16x16x32_bf16 v[112:115], v[80:83], a[156:159], v[112:115]\n"
"ds_write_addtid_b32 v67 offset:15008\n"
"v_mfma_f32_16x16x32_bf16 v[112:115], v[84:87], a[160:163], v[112:115]\n"
"buffer_store_dwordx4 v[92:95], v13, s[40:43], s94 offen sc1\n"
"v_mfma_f32_16x16x32_bf16 v[112:115], v[88:91], a[164:167], v[112:115]\n"
"v_mfma_f32_16x16x32_bf16 v[96:99], a[168:171], v[100:103], 0\n"
"v_mfma_f32_16x16x32_bf16 v[96:99], a[172:175], v[68:71], v[96:99]\n"
"ds_write_addtid_b32 v112 offset:1280\n"
"v_mfma_f32_16x16x32_bf16 v[96:99], a[176:179], v[72:75], v[96:99]\n"
"ds_write_addtid_b32 v113 offset:5920\n"
"v_mfma_f32_16x16x32_bf16 v[96:99], a[180:183], v[76:79], v[96:99]\n"
"ds_write_addtid_b32 v114 offset:10624\n"
"v_mfma_f32_16x16x32_bf16 v[96:99], a[184:187], v[80:83], v[96:99]\n"
"ds_write_addtid_b32 v115 offset:15264\n"
"v_mfma_f32_16x16x32_bf16 v[96:99], a[188:191], v[84:87], v[96:99]\n"
"v_mfma_f32_16x16x32_bf16 v[96:99], a[192:195], v[88:91], v[96:99]\n"
"v_mfma_f32_16x16x32_bf16 v[92:95], a[196:199], v[100:103], 0\n"
"v_mfma_f32_16x16x32_bf16 v[92:95], a[200:203], v[68:71], v[92:95]\n"
"v_mfma_f32_16x16x32_bf16 v[92:95], a[204:207], v[72:75], v[92:95]\n"
"v_mfma_f32_16x16x32_bf16 v[92:95], a[208:211], v[76:79], v[92:95]\n"
"v_mfma_f32_16x16x32_bf16 v[92:95], a[212:215], v[80:83], v[92:95]\n"
"v_mfma_f32_16x16x32_bf16 v[92:95], a[216:219], v[84:87], v[92:95]\n"
"v_mfma_f32_16x16x32_bf16 v[92:95], a[220:223], v[88:91], v[92:95]\n"
"v_mfma_f32_16x16x32_bf16 v[112:115], a[224:227], v[100:103], 0\n"
"v_mfma_f32_16x16x32_bf16 v[112:115], a[228:231], v[68:71], v[112:115]\n"
"v_mfma_f32_16x16x32_bf16 v[112:115], a[232:235], v[72:75], v[112:115]\n"
"v_mfma_f32_16x16x32_bf16 v[112:115], a[236:239], v[76:79], v[112:115]\n"
"v_mfma_f32_16x16x32_bf16 v[112:115], a[240:243], v[80:83], v[112:115]\n"
"v_mfma_f32_16x16x32_bf16 v[112:115], a[244:247], v[84:87], v[112:115]\n"
"v_mfma_f32_16x16x32_bf16 v[112:115], a[248:251], v[88:91], v[112:115]\n"
"v_mfma_f32_16x16x32_bf16 v[116:119], a[252:255], v[100:103], 0\n"
"v_mfma_f32_16x16x32_bf16 v[116:119], v[20:23], v[68:71], v[116:119]\n"
"s_waitcnt lgkmcnt(0)\n"
"v_mfma_f32_16x16x32_bf16 v[116:119], v[24:27], v[72:75], v[116:119]\n"
"s_barrier\n"
"v_mfma_f32_16x16x32_bf16 v[116:119], v[28:31], v[76:79], v[116:119]\n"
"ds_read_b128 v[100:103], v2 offset:2048\n"
"ds_read_b128 v[120:123], v3 offset:512\n"
"v_mfma_f32_16x16x32_bf16 v[116:119], v[32:35], v[80:83], v[116:119]\n"
"ds_read_b128 v[124:127], v4 offset:3072\n"
"v_mfma_f32_16x16x32_bf16 v[116:119], v[36:39], v[84:87], v[116:119]\n"
"ds_read_b128 v[128:131], v2 offset:2304\n"
"ds_read_b128 v[132:135], v3 offset:768\n"
"v_mfma_f32_16x16x32_bf16 v[116:119], v[40:43], v[88:91], v[116:119]\n"
"ds_read_b128 v[136:139], v4 offset:3328\n"
"ds_read_b128 v[140:143], v2 offset:3584\n"
"ds_read_b128 v[144:147], v3 offset:1536\n"
"ds_read_b128 v[148:151], v4 offset:3584\n"
"ds_read_b128 v[152:155], v2 offset:3840\n"
"ds_read_b128 v[156:159], v3 offset:1792\n"
"ds_read_b128 v[160:163], v4 offset:3840\n"
"s_waitcnt lgkmcnt(9)\n"
"v_add_f32 v100, v100, v120\n"
"v_add_f32 v101, v101, v121\n"
"v_add_f32 v102, v102, v122\n"
"v_add_f32 v103, v103, v123\n"
"v_add_f32 v100, v100, v124\n"
"v_add_f32 v101, v101, v125\n"
"v_add_f32 v102, v102, v126\n"
"v_add_f32 v103, v103, v127\n"
"v_add_f32 v100, v100, v96\n"
"v_add_f32 v101, v101, v97\n"
"v_add_f32 v102, v102, v98\n"
"v_add_f32 v103, v103, v99\n"
"s_add_u32 s94, s50, 0x0\n"
"s_and_b32 s93, s94, 0xfbffffff\n"
"v_cvt_pk_bf16_f32 v124, v100, v101\n"
"v_cvt_pk_bf16_f32 v125, v102, v103\n"
"s_waitcnt lgkmcnt(6)\n"
"v_add_f32 v128, v128, v132\n"
"v_add_f32 v129, v129, v133\n"
"s_waitcnt vmcnt(0)\n"
"v_add_f32 v130, v130, v134\n"
"v_add_f32 v131, v131, v135\n"
"v_add_f32 v128, v128, v136\n"
"v_add_f32 v129, v129, v137\n"
"v_add_f32 v130, v130, v138\n"
"v_add_f32 v131, v131, v139\n"
"v_add_f32 v128, v128, v92\n"
"v_add_f32 v129, v129, v93\n"
"v_add_f32 v130, v130, v94\n"
"v_add_f32 v131, v131, v95\n"
"v_cvt_pk_bf16_f32 v126, v128, v129\n"
"buffer_store_dwordx2 v[0:1], v14, s[40:43], s95 offen offset:0 sc1\n"
"v_cvt_pk_bf16_f32 v127, v130, v131\n"
"buffer_store_dwordx4 v[124:127], v13, s[40:43], s93 offen sc1\n"
"s_waitcnt lgkmcnt(3)\n"
"v_add_f32 v140, v140, v144\n"
"v_add_f32 v141, v141, v145\n"
"v_add_f32 v142, v142, v146\n"
"v_add_f32 v143, v143, v147\n"
"v_add_f32 v140, v140, v148\n"
"v_add_f32 v141, v141, v149\n"
"v_add_f32 v142, v142, v150\n"
"v_add_f32 v143, v143, v151\n"
"v_add_f32 v140, v140, v112\n"
"v_add_f32 v141, v141, v113\n"
"v_add_f32 v142, v142, v114\n"
"v_add_f32 v143, v143, v115\n"
"v_cvt_pk_bf16_f32 v100, v140, v141\n"
"v_cvt_pk_bf16_f32 v101, v142, v143\n"
"s_waitcnt lgkmcnt(0)\n"
"v_add_f32 v152, v152, v156\n"
"v_add_f32 v153, v153, v157\n"
"v_add_f32 v154, v154, v158\n"
"v_add_f32 v155, v155, v159\n"
"v_add_f32 v152, v152, v160\n"
"v_add_f32 v153, v153, v161\n"
"v_add_f32 v154, v154, v162\n"
"v_add_f32 v155, v155, v163\n"
"v_add_f32 v152, v152, v116\n"
"v_add_f32 v153, v153, v117\n"
"v_add_f32 v154, v154, v118\n"
"v_add_f32 v155, v155, v119\n"
"v_cvt_pk_bf16_f32 v102, v152, v153\n"
"v_cvt_pk_bf16_f32 v103, v154, v155\n"
"buffer_store_dwordx4 v[100:103], v15, s[40:43], s93 offen sc1\n"
"s_waitcnt vmcnt(0)\n"
"buffer_store_dwordx2 v[0:1], v16, s[40:43], s94 offen offset:0 sc1\n"
"s_branch .LG_end\n"
".LG_t2:\n"
"v_mfma_f32_16x16x32_bf16 v[112:115], a[236:239], v[76:79], v[112:115]\n"
"v_mfma_f32_16x16x32_bf16 v[112:115], a[240:243], v[80:83], v[112:115]\n"
"v_mfma_f32_16x16x32_bf16 v[112:115], a[244:247], v[84:87], v[112:115]\n"
"v_mfma_f32_16x16x32_bf16 v[112:115], a[248:251], v[88:91], v[112:115]\n"
"v_mfma_f32_16x16x32_bf16 v[116:119], a[252:255], v[100:103], 0\n"
"v_mfma_f32_16x16x32_bf16 v[116:119], v[20:23], v[68:71], v[116:119]\n"
"s_waitcnt lgkmcnt(0)\n"
"v_mfma_f32_16x16x32_bf16 v[116:119], v[24:27], v[72:75], v[116:119]\n"
"s_barrier\n"
"v_mfma_f32_16x16x32_bf16 v[116:119], v[28:31], v[76:79], v[116:119]\n"
"ds_read_b128 v[100:103], v2 offset:2048\n"
"ds_read_b128 v[120:123], v3 offset:512\n"
"v_mfma_f32_16x16x32_bf16 v[116:119], v[32:35], v[80:83], v[116:119]\n"
"ds_read_b128 v[124:127], v4 offset:3072\n"
"v_mfma_f32_16x16x32_bf16 v[116:119], v[36:39], v[84:87], v[116:119]\n"
"ds_read_b128 v[128:131], v2 offset:2304\n"
"ds_read_b128 v[132:135], v3 offset:768\n"
"v_mfma_f32_16x16x32_bf16 v[116:119], v[40:43], v[88:91], v[116:119]\n"
"s_waitcnt vmcnt(8)\n"
"ds_read_b128 v[136:139], v4 offset:3328\n"
"v_mfma_f32_16x16x32_bf16 v[84:87], v[104:107], a[0:3], 0\n"
"ds_read_b128 v[140:143], v2 offset:3584\n"
"ds_read_b128 v[144:147], v3 offset:1536\n"
"v_mfma_f32_16x16x32_bf16 v[84:87], v[108:111], a[4:7], v[84:87]\n"
"s_waitcnt vmcnt(6)\n"
"ds_read_b128 v[148:151], v4 offset:3584\n"
"ds_read_b128 v[152:155], v2 offset:3840\n"
"v_mfma_f32_16x16x32_bf16 v[84:87], v[44:47], a[8:11], v[84:87]\n"
"ds_read_b128 v[156:159], v3 offset:1792\n"
"ds_read_b128 v[160:163], v4 offset:3840\n"
"v_mfma_f32_16x16x32_bf16 v[84:87], v[48:51], a[12:15], v[84:87]\n"
"s_waitcnt vmcnt(2)\n"
"v_mfma_f32_16x16x32_bf16 v[84:87], v[52:55], a[16:19], v[84:87]\n"
"s_waitcnt lgkmcnt(9)\n"
"v_add_f32 v100, v100, v120\n"
"v_mfma_f32_16x16x32_bf16 v[84:87], v[56:59], a[20:23], v[84:87]\n"
"s_waitcnt vmcnt(1)\n"
"v_add_f32 v101, v101, v121\n"
"v_add_f32 v102, v102, v122\n"
"v_mfma_f32_16x16x32_bf16 v[84:87], v[60:63], a[24:27], v[84:87]\n"
"v_add_f32 v103, v103, v123\n"
"v_add_f32 v100, v100, v124\n"
"v_mfma_f32_16x16x32_bf16 v[164:167], v[104:107], a[28:31], 0\n"
"v_mfma_f32_16x16x32_bf16 v[164:167], v[108:111], a[32:35], v[164:167]\n"
"ds_write_addtid_b32 v84 offset:2048\n"
"v_add_f32 v101, v101, v125\n"
"v_mfma_f32_16x16x32_bf16 v[164:167], v[44:47], a[36:39], v[164:167]\n"
"ds_write_addtid_b32 v85 offset:6688\n"
"v_add_f32 v102, v102, v126\n"
"v_mfma_f32_16x16x32_bf16 v[164:167], v[48:51], a[40:43], v[164:167]\n"
"ds_write_addtid_b32 v86 offset:11392\n"
"v_add_f32 v103, v103, v127\n"
"v_mfma_f32_16x16x32_bf16 v[164:167], v[52:55], a[44:47], v[164:167]\n"
"ds_write_addtid_b32 v87 offset:16032\n"
"v_add_f32 v100, v100, v96\n"
"v_mfma_f32_16x16x32_bf16 v[164:167], v[56:59], a[48:51], v[164:167]\n"
"v_add_f32 v101, v101, v97\n"
"v_mfma_f32_16x16x32_bf16 v[164:167], v[60:63], a[52:55], v[164:167]\n"
"v_add_f32 v102, v102, v98\n"
"v_add_f32 v103, v103, v99\n"
"v_mfma_f32_16x16x32_bf16 v[120:123], v[104:107], a[56:59], 0\n"
"s_add_u32 s94, s50, 0xffe80000\n"
"s_and_b32 s93, s94, 0xfbffffff\n"
"v_cvt_pk_bf16_f32 v124, v100, v101\n"
"v_mfma_f32_16x16x32_bf16 v[120:123], v[108:111], a[60:63], v[120:123]\n"
"ds_write_addtid_b32 v164 offset:2304\n"
"v_cvt_pk_bf16_f32 v125, v102, v103\n"
"v_mfma_f32_16x16x32_bf16 v[120:123], v[44:47], a[64:67], v[120:123]\n"
"s_waitcnt lgkmcnt(11)\n"
"ds_write_addtid_b32 v165 offset:6944\n"
"v_add_f32 v128, v128, v132\n"
"v_mfma_f32_16x16x32_bf16 v[120:123], v[48:51], a[68:71], v[120:123]\n"
"ds_write_addtid_b32 v166 offset:11648\n"
"v_add_f32 v129, v129, v133\n"
"v_mfma_f32_16x16x32_bf16 v[120:123], v[52:55], a[72:75], v[120:123]\n"
"s_waitcnt vmcnt(0)\n"
"ds_write_addtid_b32 v167 offset:16288\n"
"v_mfma_f32_16x16x32_bf16 v[120:123], v[56:59], a[76:79], v[120:123]\n"
"v_add_f32 v130, v130, v134\n"
"v_mfma_f32_16x16x32_bf16 v[120:123], v[60:63], a[80:83], v[120:123]\n"
"v_add_f32 v131, v131, v135\n"
"v_add_f32 v128, v128, v136\n"
"v_mfma_f32_16x16x32_bf16 v[96:99], v[104:107], a[84:87], 0\n"
"v_add_f32 v129, v129, v137\n"
"v_add_f32 v130, v130, v138\n"
"v_mfma_f32_16x16x32_bf16 v[96:99], v[108:111], a[88:91], v[96:99]\n"
"ds_write_addtid_b32 v120 offset:2560\n"
"v_add_f32 v131, v131, v139\n"
"v_mfma_f32_16x16x32_bf16 v[96:99], v[44:47], a[92:95], v[96:99]\n"
"ds_write_addtid_b32 v121 offset:7200\n"
"v_add_f32 v128, v128, v92\n"
"v_mfma_f32_16x16x32_bf16 v[96:99], v[48:51], a[96:99], v[96:99]\n"
"ds_write_addtid_b32 v122 offset:11904\n"
"v_add_f32 v129, v129, v93\n"
"v_mfma_f32_16x16x32_bf16 v[96:99], v[52:55], a[100:103], v[96:99]\n"
"ds_write_addtid_b32 v123 offset:16544\n"
"v_add_f32 v130, v130, v94\n"
"v_mfma_f32_16x16x32_bf16 v[96:99], v[56:59], a[104:107], v[96:99]\n"
"v_add_f32 v131, v131, v95\n"
"v_cvt_pk_bf16_f32 v126, v128, v129\n"
"v_mfma_f32_16x16x32_bf16 v[96:99], v[60:63], a[108:111], v[96:99]\n"
"buffer_store_dwordx2 v[0:1], v14, s[40:43], s95 offen offset:0 sc1\n"
"v_cvt_pk_bf16_f32 v127, v130, v131\n"
"v_mfma_f32_16x16x32_bf16 v[92:95], v[104:107], a[112:115], 0\n"
"buffer_store_dwordx4 v[124:127], v13, s[40:43], s93 offen sc1\n"
"v_mfma_f32_16x16x32_bf16 v[92:95], v[108:111], a[116:119], v[92:95]\n"
"s_waitcnt lgkmcnt(15)\n"
"ds_write_addtid_b32 v96 offset:2816\n"
"v_add_f32 v140, v140, v144\n"
"v_mfma_f32_16x16x32_bf16 v[92:95], v[44:47], a[120:123], v[92:95]\n"
"ds_write_addtid_b32 v97 offset:7456\n"
"v_add_f32 v141, v141, v145\n"
"v_mfma_f32_16x16x32_bf16 v[92:95], v[48:51], a[124:127], v[92:95]\n"
"ds_write_addtid_b32 v98 offset:12160\n"
"v_add_f32 v142, v142, v146\n"
"v_mfma_f32_16x16x32_bf16 v[92:95], v[52:55], a[128:131], v[92:95]\n"
"ds_write_addtid_b32 v99 offset:16800\n"
"v_add_f32 v143, v143, v147\n"
"v_mfma_f32_16x16x32_bf16 v[92:95], v[56:59], a[132:135], v[92:95]\n"
"v_add_f32 v140, v140, v148\n"
"v_add_f32 v141, v141, v149\n"
"v_mfma_f32_16x16x32_bf16 v[92:95], v[60:63], a[136:139], v[92:95]\n"
"v_add_f32 v142, v142, v150\n"
"v_add_f32 v143, v143, v151\n"
"v_mfma_f32_16x16x32_bf16 v[96:99], v[104:107], a[140:143], 0\n"
"v_add_f32 v140, v140, v112\n"
"v_add_f32 v141, v141, v113\n"
"v_mfma_f32_16x16x32_bf16 v[96:99], v[108:111], a[144:147], v[96:99]\n"
"ds_write_addtid_b32 v92 offset:3072\n"
"v_add_f32 v142, v142, v114\n"
"v_mfma_f32_16x16x32_bf16 v[96:99], v[44:47], a[148:151], v[96:99]\n"
"ds_write_addtid_b32 v93 offset:7712\n"
"v_add_f32 v143, v143, v115\n"
"v_mfma_f32_16x16x32_bf16 v[96:99], v[48:51], a[152:155], v[96:99]\n"
"ds_write_addtid_b32 v94 offset:12416\n"
"v_cvt_pk_bf16_f32 v100, v140, v141\n"
"v_mfma_f32_16x16x32_bf16 v[96:99], v[52:55], a[156:159], v[96:99]\n"
"ds_write_addtid_b32 v95 offset:17056\n"
"v_cvt_pk_bf16_f32 v101, v142, v143\n"
"v_mfma_f32_16x16x32_bf16 v[96:99], v[56:59], a[160:163], v[96:99]\n"
"s_waitcnt lgkmcnt(15)\n"
"v_add_f32 v152, v152, v156\n"
"v_add_f32 v153, v153, v157\n"
"v_mfma_f32_16x16x32_bf16 v[96:99], v[60:63], a[164:167], v[96:99]\n"
"v_add_f32 v154, v154, v158\n"
"v_add_f32 v155, v155, v159\n"
"v_mfma_f32_16x16x32_bf16 v[92:95], v[104:107], a[224:227], 0\n"
"v_add_f32 v152, v152, v160\n"
"v_add_f32 v153, v153, v161\n"
"v_mfma_f32_16x16x32_bf16 v[92:95], v[108:111], a[228:231], v[92:95]\n"
"ds_write_addtid_b32 v96 offset:3328\n"
"v_add_f32 v154, v154, v162\n"
"v_mfma_f32_16x16x32_bf16 v[92:95], v[44:47], a[232:235], v[92:95]\n"
"ds_write_addtid_b32 v97 offset:7968\n"
"v_add_f32 v155, v155, v163\n"
"v_mfma_f32_16x16x32_bf16 v[92:95], v[48:51], a[236:239], v[92:95]\n"
"ds_write_addtid_b32 v98 offset:12672\n"
"v_add_f32 v152, v152, v116\n"
"v_mfma_f32_16x16x32_bf16 v[92:95], v[52:55], a[240:243], v[92:95]\n"
"ds_write_addtid_b32 v99 offset:17312\n"
"v_add_f32 v153, v153, v117\n"
"v_mfma_f32_16x16x32_bf16 v[92:95], v[56:59], a[244:247], v[92:95]\n"
"v_add_f32 v154, v154, v118\n"
"v_add_f32 v155, v155, v119\n"
"v_mfma_f32_16x16x32_bf16 v[92:95], v[60:63], a[248:251], v[92:95]\n"
"v_cvt_pk_bf16_f32 v102, v152, v153\n"
"v_cvt_pk_bf16_f32 v103, v154, v155\n"
"v_mfma_f32_16x16x32_bf16 v[96:99], v[104:107], a[252:255], 0\n"
"buffer_store_dwordx4 v[100:103], v15, s[40:43], s93 offen sc1\n"
"v_mfma_f32_16x16x32_bf16 v[96:99], v[108:111], v[20:23], v[96:99]\n"
"ds_write_addtid_b32 v92 offset:3584\n"
"v_mfma_f32_16x16x32_bf16 v[96:99], v[44:47], v[24:27], v[96:99]\n"
"ds_write_addtid_b32 v93 offset:8224\n"
"v_mfma_f32_16x16x32_bf16 v[96:99], v[48:51], v[28:31], v[96:99]\n"
"ds_write_addtid_b32 v94 offset:12928\n"
"v_mfma_f32_16x16x32_bf16 v[96:99], v[52:55], v[32:35], v[96:99]\n"
"ds_write_addtid_b32 v95 offset:17568\n"
"v_mfma_f32_16x16x32_bf16 v[96:99], v[56:59], v[36:39], v[96:99]\n"
"v_mfma_f32_16x16x32_bf16 v[96:99], v[60:63], v[40:43], v[96:99]\n"
"v_mfma_f32_16x16x32_bf16 v[100:103], a[168:171], v[104:107], 0\n"
"v_mfma_f32_16x16x32_bf16 v[100:103], a[172:175], v[108:111], v[100:103]\n"
"ds_write_addtid_b32 v96 offset:3840\n"
"v_mfma_f32_16x16x32_bf16 v[100:103], a[176:179], v[44:47], v[100:103]\n"
"ds_write_addtid_b32 v97 offset:8480\n"
"v_mfma_f32_16x16x32_bf16 v[100:103], a[180:183], v[48:51], v[100:103]\n"
"ds_write_addtid_b32 v98 offset:13184\n"
"v_mfma_f32_16x16x32_bf16 v[100:103], a[184:187], v[52:55], v[100:103]\n"
"ds_write_addtid_b32 v99 offset:17824\n"
"v_mfma_f32_16x16x32_bf16 v[100:103], a[188:191], v[56:59], v[100:103]\n"
"v_mfma_f32_16x16x32_bf16 v[100:103], a[192:195], v[60:63], v[100:103]\n"
"v_mfma_f32_16x16x32_bf16 v[112:115], a[196:199], v[104:107], 0\n"
"v_mfma_f32_16x16x32_bf16 v[112:115], a[200:203], v[108:111], v[112:115]\n"
"s_waitcnt lgkmcnt(0)\n"
"v_mfma_f32_16x16x32_bf16 v[112:115], a[204:207], v[44:47], v[112:115]\n"
"s_barrier\n"
"v_mfma_f32_16x16x32_bf16 v[112:115], a[208:211], v[48:51], v[112:115]\n"
"ds_read_b128 v[96:99], v2 offset:0\n"
"ds_read_b128 v[104:107], v3 offset:2560\n"
"v_mfma_f32_16x16x32_bf16 v[112:115], a[212:215], v[52:55], v[112:115]\n"
"ds_read_b128 v[108:111], v4 offset:1024\n"
"ds_read_b128 v[116:119], v2 offset:256\n"
"v_mfma_f32_16x16x32_bf16 v[112:115], a[216:219], v[56:59], v[112:115]\n"
"ds_read_b128 v[120:123], v3 offset:2816\n"
"v_mfma_f32_16x16x32_bf16 v[112:115], a[220:223], v[60:63], v[112:115]\n"
"ds_read_b128 v[124:127], v4 offset:1280\n"
"s_waitcnt lgkmcnt(3)\n"
"v_add_f32 v96, v96, v104\n"
"v_add_f32 v97, v97, v105\n"
"v_add_f32 v98, v98, v106\n"
"v_add_f32 v99, v99, v107\n"
"v_add_f32 v96, v96, v108\n"
"v_add_f32 v97, v97, v109\n"
"v_add_f32 v98, v98, v110\n"
"v_add_f32 v99, v99, v111\n"
"v_add_f32 v96, v96, v100\n"
"v_add_f32 v97, v97, v101\n"
"v_add_f32 v98, v98, v102\n"
"v_add_f32 v99, v99, v103\n"
"s_add_u32 s95, s50, 0x0\n"
"s_and_b32 s93, s95, 0xfbffffff\n"
"v_cvt_pk_bf16_f32 v108, v96, v97\n"
"v_cvt_pk_bf16_f32 v109, v98, v99\n"
"s_waitcnt lgkmcnt(0)\n"
"v_add_f32 v116, v116, v120\n"
"v_add_f32 v117, v117, v121\n"
"v_add_f32 v118, v118, v122\n"
"v_add_f32 v119, v119, v123\n"
"v_add_f32 v116, v116, v124\n"
"v_add_f32 v117, v117, v125\n"
"v_add_f32 v118, v118, v126\n"
"v_add_f32 v119, v119, v127\n"
"v_add_f32 v116, v116, v112\n"
"v_add_f32 v117, v117, v113\n"
"v_add_f32 v118, v118, v114\n"
"v_add_f32 v119, v119, v115\n"
"v_cvt_pk_bf16_f32 v110, v116, v117\n"
"v_cvt_pk_bf16_f32 v111, v118, v119\n"
"s_waitcnt vmcnt(0)\n"
"buffer_store_dwordx2 v[0:1], v16, s[40:43], s94 offen offset:0 sc1\n"
"buffer_store_dwordx4 v[108:111], v13, s[40:43], s93 offen sc1\n"
"s_waitcnt vmcnt(0)\n"
"buffer_store_dwordx2 v[0:1], v14, s[40:43], s95 offen offset:0 sc1\n"
"s_branch .LG_end\n"
".LG_t3:\n"
"v_mfma_f32_16x16x32_bf16 v[100:103], a[192:195], v[60:63], v[100:103]\n"
"v_mfma_f32_16x16x32_bf16 v[112:115], a[196:199], v[104:107], 0\n"
"v_mfma_f32_16x16x32_bf16 v[112:115], a[200:203], v[108:111], v[112:115]\n"
"s_waitcnt lgkmcnt(0)\n"
"v_mfma_f32_16x16x32_bf16 v[112:115], a[204:207], v[44:47], v[112:115]\n"
"s_barrier\n"
"v_mfma_f32_16x16x32_bf16 v[112:115], a[208:211], v[48:51], v[112:115]\n"
"ds_read_b128 v[96:99], v2 offset:0\n"
"ds_read_b128 v[104:107], v3 offset:2560\n"
"v_mfma_f32_16x16x32_bf16 v[112:115], a[212:215], v[52:55], v[112:115]\n"
"ds_read_b128 v[108:111], v4 offset:1024\n"
"ds_read_b128 v[116:119], v2 offset:256\n"
"v_mfma_f32_16x16x32_bf16 v[112:115], a[216:219], v[56:59], v[112:115]\n"
"ds_read_b128 v[120:123], v3 offset:2816\n"
"v_mfma_f32_16x16x32_bf16 v[112:115], a[220:223], v[60:63], v[112:115]\n"
"s_waitcnt vmcnt(9)\n"
"ds_read_b128 v[124:127], v4 offset:1280\n"
"v_mfma_f32_16x16x32_bf16 v[56:59], v[64:67], a[0:3], 0\n"
"v_mfma_f32_16x16x32_bf16 v[56:59], v[68:71], a[4:7], v[56:59]\n"
"s_waitcnt vmcnt(7)\n"
"v_mfma_f32_16x16x32_bf16 v[56:59], v[72:75], a[8:11], v[56:59]\n"
"v_mfma_f32_16x16x32_bf16 v[56:59], v[76:79], a[12:15], v[56:59]\n"
"s_waitcnt vmcnt(4)\n"
"v_mfma_f32_16x16x32_bf16 v[56:59], v[80:83], a[16:19], v[56:59]\n"
"s_waitcnt lgkmcnt(3)\n"
"v_add_f32 v96, v96, v104\n"
"v_add_f32 v97, v97, v105\n"
"v_mfma_f32_16x16x32_bf16 v[56:59], v[84:87], a[20:23], v[56:59]\n"
"s_waitcnt vmcnt(3)\n"
"v_add_f32 v98, v98, v106\n"
"v_add_f32 v99, v99, v107\n"
"v_mfma_f32_16x16x32_bf16 v[56:59], v[88:91], a[24:27], v[56:59]\n"
"v_add_f32 v96, v96, v108\n"
"v_add_f32 v97, v97, v109\n"
"v_mfma_f32_16x16x32_bf16 v[104:107], v[64:67], a[28:31], 0\n"
"v_add_f32 v98, v98, v110\n"
"v_mfma_f32_16x16x32_bf16 v[104:107], v[68:71], a[32:35], v[104:107]\n"
"ds_write_addtid_b32 v56 offset:0\n"
"v_add_f32 v99, v99, v111\n"
"v_mfma_f32_16x16x32_bf16 v[104:107], v[72:75], a[36:39], v[104:107]\n"
"ds_write_addtid_b32 v57 offset:4640\n"
"v_add_f32 v96, v96, v100\n"
"v_mfma_f32_16x16x32_bf16 v[104:107], v[76:79], a[40:43], v[104:107]\n"
"ds_write_addtid_b32 v58 offset:9344\n"
"v_add_f32 v97, v97, v101\n"
"v_mfma_f32_16x16x32_bf16 v[104:107], v[80:83], a[44:47], v[104:107]\n"
"ds_write_addtid_b32 v59 offset:13984\n"
"v_add_f32 v98, v98, v102\n"
"v_mfma_f32_16x16x32_bf16 v[104:107], v[84:87], a[48:51], v[104:107]\n"
"v_add_f32 v99, v99, v103\n"
"v_mfma_f32_16x16x32_bf16 v[104:107], v[88:91], a[52:55], v[104:107]\n"
"s_add_u32 s95, s50, 0xffe80000\n"
"s_and_b32 s93, s95, 0xfbffffff\n"
"v_mfma_f32_16x16x32_bf16 v[100:103], v[64:67], a[56:59], 0\n"
"v_cvt_pk_bf16_f32 v108, v96, v97\n"
"v_cvt_pk_bf16_f32 v109, v98, v99\n"
"v_mfma_f32_16x16x32_bf16 v[100:103], v[68:71], a[60:63], v[100:103]\n"
"s_waitcnt lgkmcnt(4)\n"
"ds_write_addtid_b32 v104 offset:256\n"
"v_add_f32 v116, v116, v120\n"
"v_mfma_f32_16x16x32_bf16 v[100:103], v[72:75], a[64:67], v[100:103]\n"
"ds_write_addtid_b32 v105 offset:4896\n"
"v_add_f32 v117, v117, v121\n"
"v_mfma_f32_16x16x32_bf16 v[100:103], v[76:79], a[68:71], v[100:103]\n"
"ds_write_addtid_b32 v106 offset:9600\n"
"v_mfma_f32_16x16x32_bf16 v[100:103], v[80:83], a[72:75], v[100:103]\n"
"ds_write_addtid_b32 v107 offset:14240\n"
"v_add_f32 v118, v118, v122\n"
"v_mfma_f32_16x16x32_bf16 v[100:103], v[84:87], a[76:79], v[100:103]\n"
"v_add_f32 v119, v119, v123\n"
"v_add_f32 v116, v116, v124\n"
"v_mfma_f32_16x16x32_bf16 v[100:103], v[88:91], a[80:83], v[100:103]\n"
"v_add_f32 v117, v117, v125\n"
"v_add_f32 v118, v118, v126\n"
"v_mfma_f32_16x16x32_bf16 v[104:107], v[64:67], a[84:87], 0\n"
"v_add_f32 v119, v119, v127\n"
"v_add_f32 v116, v116, v112\n"
"v_mfma_f32_16x16x32_bf16 v[104:107], v[68:71], a[88:91], v[104:107]\n"
"ds_write_addtid_b32 v100 offset:512\n"
"v_add_f32 v117, v117, v113\n"
"v_mfma_f32_16x16x32_bf16 v[104:107], v[72:75], a[92:95], v[104:107]\n"
"ds_write_addtid_b32 v101 offset:5152\n"
"v_mfma_f32_16x16x32_bf16 v[104:107], v[76:79], a[96:99], v[104:107]\n"
"ds_write_addtid_b32 v102 offset:9856\n"
"v_add_f32 v118, v118, v114\n"
"v_mfma_f32_16x16x32_bf16 v[104:107], v[80:83], a[100:103], v[104:107]\n"
"ds_write_addtid_b32 v103 offset:14496\n"
"v_add_f32 v119, v119, v115\n"
"v_mfma_f32_16x16x32_bf16 v[104:107], v[84:87], a[104:107], v[104:107]\n"
"v_cvt_pk_bf16_f32 v110, v116, v117\n"
"v_cvt_pk_bf16_f32 v111, v118, v119\n"
"v_mfma_f32_16x16x32_bf16 v[104:107], v[88:91], a[108:111], v[104:107]\n"
"s_waitcnt vmcnt(0)\n"
"buffer_store_dwordx2 v[0:1], v16, s[40:43], s94 offen offset:0 sc1\n"
"v_mfma_f32_16x16x32_bf16 v[100:103], v[64:67], a[112:115], 0\n"
"v_mfma_f32_16x16x32_bf16 v[100:103], v[68:71], a[116:119], v[100:103]\n"
"ds_write_addtid_b32 v104 offset:768\n"
"v_mfma_f32_16x16x32_bf16 v[100:103], v[72:75], a[120:123], v[100:103]\n"
"buffer_store_dwordx4 v[108:111], v13, s[40:43], s93 offen sc1\n"
"ds_write_addtid_b32 v105 offset:5408\n"
"v_mfma_f32_16x16x32_bf16 v[100:103], v[76:79], a[124:127], v[100:103]\n"
"ds_write_addtid_b32 v106 offset:10112\n"
"v_mfma_f32_16x16x32_bf16 v[100:103], v[80:83], a[128:131], v[100:103]\n"
"ds_write_addtid_b32 v107 offset:14752\n"
"v_mfma_f32_16x16x32_bf16 v[100:103], v[84:87], a[132:135], v[100:103]\n"
"v_mfma_f32_16x16x32_bf16 v[100:103], v[88:91], a[136:139], v[100:103]\n"
"v_mfma_f32_16x16x32_bf16 v[108:111], v[64:67], a[140:143], 0\n"
"v_mfma_f32_16x16x32_bf16 v[108:111], v[68:71], a[144:147], v[108:111]\n"
"ds_write_addtid_b32 v100 offset:1024\n"
"v_mfma_f32_16x16x32_bf16 v[108:111], v[72:75], a[148:151], v[108:111]\n"
"ds_write_addtid_b32 v101 offset:5664\n"
"v_mfma_f32_16x16x32_bf16 v[108:111], v[76:79], a[152:155], v[108:111]\n"
"ds_write_addtid_b32 v102 offset:10368\n"
"v_mfma_f32_16x16x32_bf16 v[108:111], v[80:83], a[156:159], v[108:111]\n"
"ds_write_addtid_b32 v103 offset:15008\n"
"v_mfma_f32_16x16x32_bf16 v[108:111], v[84:87], a[160:163], v[108:111]\n"
"v_mfma_f32_16x16x32_bf16 v[108:111], v[88:91], a[164:167], v[108:111]\n"
"v_mfma_f32_16x16x32_bf16 v[104:107], v[64:67], a[224:227], 0\n"
"v_mfma_f32_16x16x32_bf16 v[104:107], v[68:71], a[228:231], v[104:107]\n"
"ds_write_addtid_b32 v108 offset:1280\n"
"v_mfma_f32_16x16x32_bf16 v[104:107], v[72:75], a[232:235], v[104:107]\n"
"ds_write_addtid_b32 v109 offset:5920\n"
"v_mfma_f32_16x16x32_bf16 v[104:107], v[76:79], a[236:239], v[104:107]\n"
"ds_write_addtid_b32 v110 offset:10624\n"
"v_mfma_f32_16x16x32_bf16 v[104:107], v[80:83], a[240:243], v[104:107]\n"
"ds_write_addtid_b32 v111 offset:15264\n"
"v_mfma_f32_16x16x32_bf16 v[104:107], v[84:87], a[244:247], v[104:107]\n"
"v_mfma_f32_16x16x32_bf16 v[104:107], v[88:91], a[248:251], v[104:107]\n"
"v_mfma_f32_16x16x32_bf16 v[112:115], v[64:67], a[252:255], 0\n"
"v_mfma_f32_16x16x32_bf16 v[112:115], v[68:71], v[20:23], v[112:115]\n"
"ds_write_addtid_b32 v104 offset:1536\n"
"v_mfma_f32_16x16x32_bf16 v[112:115], v[72:75], v[24:27], v[112:115]\n"
"ds_write_addtid_b32 v105 offset:6176\n"
"v_mfma_f32_16x16x32_bf16 v[112:115], v[76:79], v[28:31], v[112:115]\n"
"ds_write_addtid_b32 v106 offset:10880\n"
"v_mfma_f32_16x16x32_bf16 v[112:115], v[80:83], v[32:35], v[112:115]\n"
"ds_write_addtid_b32 v107 offset:15520\n"
"v_mfma_f32_16x16x32_bf16 v[112:115], v[84:87], v[36:39], v[112:115]\n"
"v_mfma_f32_16x16x32_bf16 v[112:115], v[88:91], v[40:43], v[112:115]\n"
"v_mfma_f32_16x16x32_bf16 v[104:107], a[168:171], v[64:67], 0\n"
"v_mfma_f32_16x16x32_bf16 v[104:107], a[172:175], v[68:71], v[104:107]\n"
"ds_write_addtid_b32 v112 offset:1792\n"
"v_mfma_f32_16x16x32_bf16 v[104:107], a[176:179], v[72:75], v[104:107]\n"
"ds_write_addtid_b32 v113 offset:6432\n"
"v_mfma_f32_16x16x32_bf16 v[104:107], a[180:183], v[76:79], v[104:107]\n"
"ds_write_addtid_b32 v114 offset:11136\n"
"v_mfma_f32_16x16x32_bf16 v[104:107], a[184:187], v[80:83], v[104:107]\n"
"ds_write_addtid_b32 v115 offset:15776\n"
"v_mfma_f32_16x16x32_bf16 v[104:107], a[188:191], v[84:87], v[104:107]\n"
"v_mfma_f32_16x16x32_bf16 v[104:107], a[192:195], v[88:91], v[104:107]\n"
"v_mfma_f32_16x16x32_bf16 v[108:111], a[196:199], v[64:67], 0\n"
"v_mfma_f32_16x16x32_bf16 v[108:111], a[200:203], v[68:71], v[108:111]\n"
"s_waitcnt lgkmcnt(0)\n"
"v_mfma_f32_16x16x32_bf16 v[108:111], a[204:207], v[72:75], v[108:111]\n"
"s_barrier\n"
"v_mfma_f32_16x16x32_bf16 v[108:111], a[208:211], v[76:79], v[108:111]\n"
"ds_read_b128 v[64:67], v2 offset:2048\n"
"v_mfma_f32_16x16x32_bf16 v[108:111], a[212:215], v[80:83], v[108:111]\n"
"ds_read_b128 v[112:115], v3 offset:512\n"
"ds_read_b128 v[116:119], v4 offset:3072\n"
"v_mfma_f32_16x16x32_bf16 v[108:111], a[216:219], v[84:87], v[108:111]\n"
"ds_read_b128 v[120:123], v2 offset:2304\n"
"ds_read_b128 v[124:127], v3 offset:768\n"
"v_mfma_f32_16x16x32_bf16 v[108:111], a[220:223], v[88:91], v[108:111]\n"
"ds_read_b128 v[128:131], v4 offset:3328\n"
"s_waitcnt lgkmcnt(3)\n"
"v_add_f32 v64, v64, v112\n"
"v_add_f32 v65, v65, v113\n"
"v_add_f32 v66, v66, v114\n"
"v_add_f32 v67, v67, v115\n"
"v_add_f32 v64, v64, v116\n"
"s_waitcnt vmcnt(0)\n"
"buffer_store_dwordx2 v[0:1], v14, s[40:43], s95 offen offset:0 sc1\n"
"v_add_f32 v65, v65, v117\n"
"v_add_f32 v66, v66, v118\n"
"v_add_f32 v67, v67, v119\n"
"v_add_f32 v64, v64, v104\n"
"v_add_f32 v65, v65, v105\n"
"v_add_f32 v66, v66, v106\n"
"v_add_f32 v67, v67, v107\n"
"s_add_u32 s93, s50, 0x0\n"
"s_and_b32 s95, s93, 0xfbffffff\n"
"v_cvt_pk_bf16_f32 v104, v64, v65\n"
"v_cvt_pk_bf16_f32 v105, v66, v67\n"
"s_waitcnt lgkmcnt(0)\n"
"v_add_f32 v120, v120, v124\n"
"v_add_f32 v121, v121, v125\n"
"v_add_f32 v122, v122, v126\n"
"v_add_f32 v123, v123, v127\n"
"v_add_f32 v120, v120, v128\n"
"v_add_f32 v121, v121, v129\n"
"v_add_f32 v122, v122, v130\n"
"v_add_f32 v123, v123, v131\n"
"v_add_f32 v120, v120, v108\n"
"v_add_f32 v121, v121, v109\n"
"v_add_f32 v122, v122, v110\n"
"v_add_f32 v123, v123, v111\n"
"v_cvt_pk_bf16_f32 v106, v120, v121\n"
"v_cvt_pk_bf16_f32 v107, v122, v123\n"
"buffer_store_dwordx4 v[104:107], v13, s[40:43], s95 offen sc1\n"
"s_waitcnt vmcnt(0)\n"
"buffer_store_dwordx2 v[0:1], v14, s[40:43], s93 offen offset:0 sc1\n"
"s_branch .LG_end\n"
".LG_end:\n"
"s_waitcnt vmcnt(0) lgkmcnt(0)\n"
"s_endpgm\n"
      :: "{v2}"((unsigned)(lds0 + ((wid + 3) & 3) * 18592 + trp)), "{v3}"((unsigned)(lds0 + ((wid + 2) & 3) * 18592 + trp)), "{v4}"((unsigned)(lds0 + ((wid + 1) & 3) * 18592 + trp)), "{v5}"((unsigned)(lane * 16 + f0 * 1024 + ((0 + rot) % 7) * 1024 + 134316032)), "{v6}"((unsigned)(lane * 16 + f0 * 1024 + ((1 + rot) % 7) * 1024 + 134316032)), "{v7}"((unsigned)(lane * 16 + f0 * 1024 + ((2 + rot) % 7) * 1024 + 134316032)), "{v8}"((unsigned)(lane * 16 + f0 * 1024 + ((3 + rot) % 7) * 1024 + 134316032)), "{v9}"((unsigned)(lane * 16 + f0 * 1024 + ((4 + rot) % 7) * 1024 + 134316032)), "{v10}"((unsigned)(lane * 16 + f0 * 1024 + ((5 + rot) % 7) * 1024 + 134316032)), "{v11}"((unsigned)(lane * 16 + f0 * 1024 + ((6 + rot) % 7) * 1024 + 134316032)), "{v12}"((unsigned)((f0 + lane % 7) * 8 + kq * 32 + 353024 + G_GL * 1572864)), "{v13}"((unsigned)(lane * 16 + pair * 4096 + kq * 1024 + 212992)), "{v14}"((unsigned)(lane ? DROP : pair * 64 + kq * 8 + 354304)), "{v15}"((unsigned)(lane * 16 + (pg0 + 4) * 4096 + kq * 1024 + 212992)), "{v16}"((unsigned)(lane > 1 ? DROP : (lane ? pg0 + 4 : pair) * 64 + kq * 8 + 354304)), "{v44}"((unsigned)(vT + tsw)), "{v45}"((unsigned)(vT + (tsw ^ 8))), "{v46}"((unsigned)(vD + ((0 + rot) % 7) * 65536)), "{v47}"((unsigned)(vD + ((1 + rot) % 7) * 65536)), "{v48}"((unsigned)(vD + ((2 + rot) % 7) * 65536)), "{v49}"((unsigned)(vD + ((3 + rot) % 7) * 65536)), "{v50}"((unsigned)(vD + ((4 + rot) % 7) * 65536)), "{v51}"((unsigned)(vD + ((5 + rot) % 7) * 65536)), "{v52}"((unsigned)(vD + ((6 + rot) % 7) * 65536)), "{s[40:43]}"(rS), "{s[44:47]}"(rW), "{s[48:49]}"(wsp), "{s50}"((unsigned)(sA)), "{s51}"((unsigned)(sLo)), "{s52}"((unsigned)(sHi)), "{s53}"((unsigned)(sEx)), "{s54}"((unsigned)(sEnt)), "{s55}"((unsigned)(sLd)), "{s[56:57]}"(wsb + (sLo & 0x3ffffff)), "{s59}"((unsigned)(lds0 + wid * 18592)), "{s62}"((unsigned)(sW0 + 64 * ((wid + 1) & 3))), "{s63}"((unsigned)(sW0 + 64 * ((wid + 2) & 3))), "{s64}"((unsigned)(sW0 + 64 * ((wid + 3) & 3))), "{s65}"((unsigned)(sW0 + 64 * ((wid + 4) & 3))), "{s66}"((unsigned)(sW0 + 256))
      : "v255", "a255", "s95", "vcc", "scc", "memory");

    } else {
      const int v16 = kq * 4 + wid;
      const unsigned sEx = sHi + (J_EX + (set == 1 && !J_TL)) * 3 * RS, sEnt = grp - 4;
      const u32x4 rO = desc(a.out, 2048 * 2048), bia0 = *(const u32x4*)(a.b2 + v16 * (32 << JJ) + lg * 8), bia1 = *(const u32x4*)(a.b2 + (JJ ? 32 : 512) + v16 * (32 << JJ) + lg * 8);
      const unsigned se = lg == (li >> 2) ? 0x3f80u << (16 * (li & 1)) : 0u, sel0 = (li & 2) ? 0u : se, sel1 = (li & 2) ? se : 0u;
      const u64 ep = (set == 2 && idx == 27 && wid == 0) ? (u64)a.ws : 0ull;
    asm volatile(
"s_nop 4\n"
"v_mov_b32 v186, 0\n"
"global_load_dwordx2 v[0:1], v186, s[48:49] sc1\n"
"v_mov_b32 v188, 0x3f803f80\n"
"v_mov_b32 v189, 0x3f803f80\n"
"v_mov_b32 v190, 0x3f803f80\n"
"v_mov_b32 v191, 0x3f803f80\n"
"s_add_u32 m0, s55, 0\n"
"s_add_u32 s60, s86, 0\n"
"buffer_load_dwordx4 v46, s[44:47], s60 offen lds\n"
"s_add_u32 m0, s55, 1024\n"
"s_add_u32 s60, s86, 32768\n"
"buffer_load_dwordx4 v46, s[44:47], s60 offen lds\n"
"s_add_u32 m0, s55, 2048\n"
"s_add_u32 s60, s87, 0\n"
"buffer_load_dwordx4 v46, s[44:47], s60 offen lds\n"
"s_add_u32 m0, s55, 3072\n"
"s_add_u32 s60, s87, 32768\n"
"buffer_load_dwordx4 v46, s[44:47], s60 offen lds\n"
"s_add_u32 m0, s55, 4096\n"
"s_add_u32 s60, s88, 0\n"
"buffer_load_dwordx4 v46, s[44:47], s60 offen lds\n"
"s_add_u32 m0, s55, 5120\n"
"s_add_u32 s60, s88, 32768\n"
"buffer_load_dwordx4 v46, s[44:47], s60 offen lds\n"
"s_add_u32 m0, s55, 6144\n"
"s_add_u32 s60, s89, 0\n"
"buffer_load_dwordx4 v46, s[44:47], s60 offen lds\n"
"s_add_u32 m0, s55, 7168\n"
"s_add_u32 s60, s89, 32768\n"
"buffer_load_dwordx4 v46, s[44:47], s60 offen lds\n"
"s_add_u32 m0, s55, 8192\n"
"s_add_u32 s60, s86, 0\n"
"buffer_load_dwordx4 v47, s[44:47], s60 offen lds\n"
"s_add_u32 m0, s55, 9216\n"
"s_add_u32 s60, s86, 32768\n"
"buffer_load_dwordx4 v47, s[44:47], s60 offen lds\n"
"s_add_u32 m0, s55, 10240\n"
"s_add_u32 s60, s87, 0\n"
"buffer_load_dwordx4 v47, s[44:47], s60 offen lds\n"
"s_add_u32 m0, s55, 11264\n"
"s_add_u32 s60, s87, 32768\n"
"buffer_load_dwordx4 v47, s[44:47], s60 offen lds\n"
"s_add_u32 m0, s55, 12288\n"
"s_add_u32 s60, s88, 0\n"
"buffer_load_dwordx4 v47, s[44:47], s60 offen lds\n"
"s_add_u32 m0, s55, 13312\n"
"s_add_u32 s60, s88, 32768\n"
"buffer_load_dwordx4 v47, s[44:47], s60 offen lds\n"
"s_add_u32 m0, s55, 14336\n"
"s_add_u32 s60, s89, 0\n"
"buffer_load_dwordx4 v47, s[44:47], s60 offen lds\n"
"s_add_u32 m0, s55, 15360\n"
"s_add_u32 s60, s89, 32768\n"
"buffer_load_dwordx4 v47, s[44:47], s60 offen lds\n"
"s_add_u32 m0, s55, 16384\n"
"s_add_u32 s60, s86, 0\n"
"buffer_load_dwordx4 v48, s[44:47], s60 offen lds\n"
"s_add_u32 m0, s55, 17408\n"
"s_add_u32 s60, s86, 32768\n"
"buffer_load_dwordx4 v48, s[44:47], s60 offen lds\n"
"s_add_u32 m0, s55, 18432\n"
"s_add_u32 s60, s87, 0\n"
"buffer_load_dwordx4 v48, s[44:47], s60 offen lds\n"
"s_add_u32 m0, s55, 19456\n"
"s_add_u32 s60, s87, 32768\n"
"buffer_load_dwordx4 v48, s[44:47], s60 offen lds\n"
"s_add_u32 m0, s55, 20480\n"
"s_add_u32 s60, s88, 0\n"
"buffer_load_dwordx4 v48, s[44:47], s60 offen lds\n"
"s_add_u32 m0, s55, 21504\n"
"s_add_u32 s60, s88, 32768\n"
"buffer_load_dwordx4 v48, s[44:47], s60 offen lds\n"
"s_add_u32 m0, s55, 22528\n"
"s_add_u32 s60, s89, 0\n"
"buffer_load_dwordx4 v48, s[44:47], s60 offen lds\n"
"s_add_u32 m0, s55, 23552\n"
"s_add_u32 s60, s89, 32768\n"
"buffer_load_dwordx4 v48, s[44:47], s60 offen lds\n"
"s_add_u32 m0, s55, 24576\n"
"s_add_u32 s60, s86, 0\n"
"buffer_load_dwordx4 v49, s[44:47], s60 offen lds\n"
"s_add_u32 m0, s55, 25600\n"
"s_add_u32 s60, s86, 32768\n"
"buffer_load_dwordx4 v49, s[44:47], s60 offen lds\n"
"s_add_u32 m0, s55, 26624\n"
"s_add_u32 s60, s87, 0\n"
"buffer_load_dwordx4 v49, s[44:47], s60 offen lds\n"
"s_add_u32 m0, s55, 27648\n"
"s_add_u32 s60, s87, 32768\n"
"buffer_load_dwordx4 v49, s[44:47], s60 offen lds\n"
"s_add_u32 m0, s55, 28672\n"
"s_add_u32 s60, s88, 0\n"
"buffer_load_dwordx4 v49, s[44:47], s60 offen lds\n"
"s_add_u32 m0, s55, 29696\n"
"s_add_u32 s60, s88, 32768\n"
"buffer_load_dwordx4 v49, s[44:47], s60 offen lds\n"
"s_add_u32 m0, s55, 30720\n"
"s_add_u32 s60, s89, 0\n"
"buffer_load_dwordx4 v49, s[44:47], s60 offen lds\n"
"s_add_u32 m0, s55, 31744\n"
"s_add_u32 s60, s89, 32768\n"
"buffer_load_dwordx4 v49, s[44:47], s60 offen lds\n"
"s_add_u32 m0, s55, 32768\n"
"s_add_u32 s60, s86, 0\n"
"buffer_load_dwordx4 v50, s[44:47], s60 offen lds\n"
"s_add_u32 m0, s55, 33792\n"
"s_add_u32 s60, s86, 32768\n"
"buffer_load_dwordx4 v50, s[44:47], s60 offen lds\n"
"s_add_u32 m0, s55, 34816\n"
"s_add_u32 s60, s87, 0\n"
"buffer_load_dwordx4 v50, s[44:47], s60 offen lds\n"
"s_add_u32 m0, s55, 35840\n"
"s_add_u32 s60, s87, 32768\n"
"buffer_load_dwordx4 v50, s[44:47], s60 offen lds\n"
"s_add_u32 m0, s55, 36864\n"
"s_add_u32 s60, s88, 0\n"
"buffer_load_dwordx4 v50, s[44:47], s60 offen lds\n"
"s_add_u32 m0, s55, 37888\n"
"s_add_u32 s60, s88, 32768\n"
"buffer_load_dwordx4 v50, s[44:47], s60 offen lds\n"
"s_add_u32 m0, s55, 38912\n"
"s_add_u32 s60, s89, 0\n"
"buffer_load_dwordx4 v50, s[44:47], s60 offen lds\n"
"s_add_u32 m0, s55, 39936\n"
"s_add_u32 s60, s89, 32768\n"
"buffer_load_dwordx4 v50, s[44:47], s60 offen lds\n"
"s_waitcnt vmcnt(38)\n"
"ds_read_b64_tr_b16 a[0:1], v44 offset:0\n"
"ds_read_b64_tr_b16 a[2:3], v44 offset:256\n"
"ds_read_b64_tr_b16 a[28:29], v45 offset:0\n"
"ds_read_b64_tr_b16 a[30:31], v45 offset:256\n"
"v_add_u32 v0, 2, v0\n"
"v_add_u32 v1, 0x9e3779b9, v1\n"
"v_or_b32 v0, 1, v0\n"
"v_readfirstlane_b32 s49, v1\n"
"v_readfirstlane_b32 s48, v0\n"
"s_waitcnt vmcnt(36)\n"
"ds_read_b64_tr_b16 a[56:57], v44 offset:2048\n"
"ds_read_b64_tr_b16 a[58:59], v44 offset:2304\n"
"ds_read_b64_tr_b16 a[84:85], v45 offset:2048\n"
"ds_read_b64_tr_b16 a[86:87], v45 offset:2304\n"
"s_waitcnt lgkmcnt(4)\n"
"s_add_u32 m0, s55, 0\n"
"s_add_u32 s60, s86, 0\n"
"buffer_load_dwordx4 v51, s[44:47], s60 offen lds\n"
"s_add_u32 m0, s55, 1024\n"
"s_add_u32 s60, s86, 32768\n"
"buffer_load_dwordx4 v51, s[44:47], s60 offen lds\n"
"s_waitcnt vmcnt(36)\n"
"ds_read_b64_tr_b16 a[112:113], v44 offset:4096\n"
"ds_read_b64_tr_b16 a[114:115], v44 offset:4352\n"
"ds_read_b64_tr_b16 a[140:141], v45 offset:4096\n"
"ds_read_b64_tr_b16 a[142:143], v45 offset:4352\n"
"s_waitcnt lgkmcnt(4)\n"
"s_add_u32 m0, s55, 2048\n"
"s_add_u32 s60, s87, 0\n"
"buffer_load_dwordx4 v51, s[44:47], s60 offen lds\n"
"s_add_u32 m0, s55, 3072\n"
"s_add_u32 s60, s87, 32768\n"
"buffer_load_dwordx4 v51, s[44:47], s60 offen lds\n"
"s_waitcnt vmcnt(36)\n"
"ds_read_b64_tr_b16 a[168:169], v44 offset:6144\n"
"ds_read_b64_tr_b16 a[170:171], v44 offset:6400\n"
"ds_read_b64_tr_b16 a[196:197], v45 offset:6144\n"
"ds_read_b64_tr_b16 a[198:199], v45 offset:6400\n"
"s_waitcnt lgkmcnt(4)\n"
"s_add_u32 m0, s55, 4096\n"
"s_add_u32 s60, s88, 0\n"
"buffer_load_dwordx4 v51, s[44:47], s60 offen lds\n"
"s_add_u32 m0, s55, 5120\n"
"s_add_u32 s60, s88, 32768\n"
"buffer_load_dwordx4 v51, s[44:47], s60 offen lds\n"
"s_waitcnt vmcnt(36)\n"
"ds_read_b64_tr_b16 a[4:5], v44 offset:8192\n"
"ds_read_b64_tr_b16 a[6:7], v44 offset:8448\n"
"ds_read_b64_tr_b16 a[32:33], v45 offset:8192\n"
"ds_read_b64_tr_b16 a[34:35], v45 offset:8448\n"
"s_waitcnt lgkmcnt(4)\n"
"s_add_u32 m0, s55, 6144\n"
"s_add_u32 s60, s89, 0\n"
"buffer_load_dwordx4 v51, s[44:47], s60 offen lds\n"
"s_add_u32 m0, s55, 7168\n"
"s_add_u32 s60, s89, 32768\n"
"buffer_load_dwordx4 v51, s[44:47], s60 offen lds\n"
"s_waitcnt vmcnt(36)\n"
"ds_read_b64_tr_b16 a[60:61], v44 offset:10240\n"
"ds_read_b64_tr_b16 a[62:63], v44 offset:10496\n"
"ds_read_b64_tr_b16 a[88:89], v45 offset:10240\n"
"ds_read_b64_tr_b16 a[90:91], v45 offset:10496\n"
"s_waitcnt lgkmcnt(4)\n"
"s_add_u32 m0, s55, 8192\n"
"s_add_u32 s60, s86, 0\n"
"buffer_load_dwordx4 v52, s[44:47], s60 offen lds\n"
"s_add_u32 m0, s55, 9216\n"
"s_add_u32 s60, s86, 32768\n"
"buffer_load_dwordx4 v52, s[44:47], s60 offen lds\n"
"s_waitcnt vmcnt(36)\n"
"ds_read_b64_tr_b16 a[116:117], v44 offset:12288\n"
"ds_read_b64_tr_b16 a[118:119], v44 offset:12544\n"
"ds_read_b64_tr_b16 a[144:145], v45 offset:12288\n"
"ds_read_b64_tr_b16 a[146:147], v45 offset:12544\n"
"s_waitcnt lgkmcnt(4)\n"
"s_add_u32 m0, s55, 10240\n"
"s_add_u32 s60, s87, 0\n"
"buffer_load_dwordx4 v52, s[44:47], s60 offen lds\n"
"s_add_u32 m0, s55, 11264\n"
"s_add_u32 s60, s87, 32768\n"
"buffer_load_dwordx4 v52, s[44:47], s60 offen lds\n"
"s_waitcnt vmcnt(36)\n"
"ds_read_b64_tr_b16 a[172:173], v44 offset:14336\n"
"ds_read_b64_tr_b16 a[174:175], v44 offset:14592\n"
"ds_read_b64_tr_b16 a[200:201], v45 offset:14336\n"
"ds_read_b64_tr_b16 a[202:203], v45 offset:14592\n"
"s_waitcnt lgkmcnt(4)\n"
"s_add_u32 m0, s55, 12288\n"
"s_add_u32 s60, s88, 0\n"
"buffer_load_dwordx4 v52, s[44:47], s60 offen lds\n"
"s_add_u32 m0, s55, 13312\n"
"s_add_u32 s60, s88, 32768\n"
"buffer_load_dwordx4 v52, s[44:47], s60 offen lds\n"
"s_waitcnt vmcnt(36)\n"
"ds_read_b64_tr_b16 a[8:9], v44 offset:16384\n"
"ds_read_b64_tr_b16 a[10:11], v44 offset:16640\n"
"ds_read_b64_tr_b16 a[36:37], v45 offset:16384\n"
"ds_read_b64_tr_b16 a[38:39], v45 offset:16640\n"
"s_waitcnt lgkmcnt(4)\n"
"s_add_u32 m0, s55, 14336\n"
"s_add_u32 s60, s89, 0\n"
"buffer_load_dwordx4 v52, s[44:47], s60 offen lds\n"
"s_add_u32 m0, s55, 15360\n"
"s_add_u32 s60, s89, 32768\n"
"buffer_load_dwordx4 v52, s[44:47], s60 offen lds\n"
"s_waitcnt vmcnt(36)\n"
"ds_read_b64_tr_b16 a[64:65], v44 offset:18432\n"
"ds_read_b64_tr_b16 a[66:67], v44 offset:18688\n"
"ds_read_b64_tr_b16 a[92:93], v45 offset:18432\n"
"ds_read_b64_tr_b16 a[94:95], v45 offset:18688\n"
"s_waitcnt lgkmcnt(4)\n"
"s_waitcnt vmcnt(34)\n"
"ds_read_b64_tr_b16 a[120:121], v44 offset:20480\n"
"ds_read_b64_tr_b16 a[122:123], v44 offset:20736\n"
"ds_read_b64_tr_b16 a[148:149], v45 offset:20480\n"
"ds_read_b64_tr_b16 a[150:151], v45 offset:20736\n"
"s_waitcnt lgkmcnt(4)\n"
"s_waitcnt vmcnt(32)\n"
"ds_read_b64_tr_b16 a[176:177], v44 offset:22528\n"
"ds_read_b64_tr_b16 a[178:179], v44 offset:22784\n"
"ds_read_b64_tr_b16 a[204:205], v45 offset:22528\n"
"ds_read_b64_tr_b16 a[206:207], v45 offset:22784\n"
"s_waitcnt lgkmcnt(4)\n"
"s_waitcnt vmcnt(30)\n"
"ds_read_b64_tr_b16 a[12:13], v44 offset:24576\n"
"ds_read_b64_tr_b16 a[14:15], v44 offset:24832\n"
"ds_read_b64_tr_b16 a[40:41], v45 offset:24576\n"
"ds_read_b64_tr_b16 a[42:43], v45 offset:24832\n"
"s_waitcnt lgkmcnt(4)\n"
"s_waitcnt vmcnt(28)\n"
"ds_read_b64_tr_b16 a[68:69], v44 offset:26624\n"
"ds_read_b64_tr_b16 a[70:71], v44 offset:26880\n"
"ds_read_b64_tr_b16 a[96:97], v45 offset:26624\n"
"ds_read_b64_tr_b16 a[98:99], v45 offset:26880\n"
"s_waitcnt lgkmcnt(4)\n"
"s_waitcnt vmcnt(26)\n"
"ds_read_b64_tr_b16 a[124:125], v44 offset:28672\n"
"ds_read_b64_tr_b16 a[126:127], v44 offset:28928\n"
"ds_read_b64_tr_b16 a[152:153], v45 offset:28672\n"
"ds_read_b64_tr_b16 a[154:155], v45 offset:28928\n"
"s_waitcnt lgkmcnt(4)\n"
"s_waitcnt vmcnt(24)\n"
"ds_read_b64_tr_b16 a[180:181], v44 offset:30720\n"
"ds_read_b64_tr_b16 a[182:183], v44 offset:30976\n"
"ds_read_b64_tr_b16 a[208:209], v45 offset:30720\n"
"ds_read_b64_tr_b16 a[210:211], v45 offset:30976\n"
"s_waitcnt lgkmcnt(4)\n"
"s_waitcnt vmcnt(22)\n"
"ds_read_b64_tr_b16 a[16:17], v44 offset:32768\n"
"ds_read_b64_tr_b16 a[18:19], v44 offset:33024\n"
"ds_read_b64_tr_b16 a[44:45], v45 offset:32768\n"
"ds_read_b64_tr_b16 a[46:47], v45 offset:33024\n"
"s_waitcnt lgkmcnt(4)\n"
"s_waitcnt vmcnt(20)\n"
"ds_read_b64_tr_b16 a[72:73], v44 offset:34816\n"
"ds_read_b64_tr_b16 a[74:75], v44 offset:35072\n"
"ds_read_b64_tr_b16 a[100:101], v45 offset:34816\n"
"ds_read_b64_tr_b16 a[102:103], v45 offset:35072\n"
"s_waitcnt lgkmcnt(4)\n"
"s_waitcnt vmcnt(18)\n"
"ds_read_b64_tr_b16 a[128:129], v44 offset:36864\n"
"ds_read_b64_tr_b16 a[130:131], v44 offset:37120\n"
"ds_read_b64_tr_b16 a[156:157], v45 offset:36864\n"
"ds_read_b64_tr_b16 a[158:159], v45 offset:37120\n"
"s_waitcnt lgkmcnt(4)\n"
"s_waitcnt vmcnt(16)\n"
"ds_read_b64_tr_b16 a[184:185], v44 offset:38912\n"
"ds_read_b64_tr_b16 a[186:187], v44 offset:39168\n"
"ds_read_b64_tr_b16 a[212:213], v45 offset:38912\n"
"ds_read_b64_tr_b16 a[214:215], v45 offset:39168\n"
"s_waitcnt lgkmcnt(4)\n"
"s_waitcnt vmcnt(14)\n"
"ds_read_b64_tr_b16 a[20:21], v44 offset:0\n"
"ds_read_b64_tr_b16 a[22:23], v44 offset:256\n"
"ds_read_b64_tr_b16 a[48:49], v45 offset:0\n"
"ds_read_b64_tr_b16 a[50:51], v45 offset:256\n"
"s_waitcnt lgkmcnt(4)\n"
"s_waitcnt vmcnt(12)\n"
"ds_read_b64_tr_b16 a[76:77], v44 offset:2048\n"
"ds_read_b64_tr_b16 a[78:79], v44 offset:2304\n"
"ds_read_b64_tr_b16 a[104:105], v45 offset:2048\n"
"ds_read_b64_tr_b16 a[106:107], v45 offset:2304\n"
"s_waitcnt lgkmcnt(4)\n"
"s_waitcnt vmcnt(10)\n"
"ds_read_b64_tr_b16 a[132:133], v44 offset:4096\n"
"ds_read_b64_tr_b16 a[134:135], v44 offset:4352\n"
"ds_read_b64_tr_b16 a[160:161], v45 offset:4096\n"
"ds_read_b64_tr_b16 a[162:163], v45 offset:4352\n"
"s_waitcnt lgkmcnt(4)\n"
"s_waitcnt vmcnt(8)\n"
"ds_read_b64_tr_b16 a[188:189], v44 offset:6144\n"
"ds_read_b64_tr_b16 a[190:191], v44 offset:6400\n"
"ds_read_b64_tr_b16 a[216:217], v45 offset:6144\n"
"ds_read_b64_tr_b16 a[218:219], v45 offset:6400\n"
"s_waitcnt lgkmcnt(4)\n"
"s_waitcnt vmcnt(6)\n"
"ds_read_b64_tr_b16 a[24:25], v44 offset:8192\n"
"ds_read_b64_tr_b16 a[26:27], v44 offset:8448\n"
"ds_read_b64_tr_b16 a[52:53], v45 offset:8192\n"
"ds_read_b64_tr_b16 a[54:55], v45 offset:8448\n"
"s_waitcnt lgkmcnt(4)\n"
"s_waitcnt vmcnt(4)\n"
"ds_read_b64_tr_b16 a[80:81], v44 offset:10240\n"
"ds_read_b64_tr_b16 a[82:83], v44 offset:10496\n"
"ds_read_b64_tr_b16 a[108:109], v45 offset:10240\n"
"ds_read_b64_tr_b16 a[110:111], v45 offset:10496\n"
"s_waitcnt lgkmcnt(4)\n"
"s_waitcnt vmcnt(2)\n"
"ds_read_b64_tr_b16 a[136:137], v44 offset:12288\n"
"ds_read_b64_tr_b16 a[138:139], v44 offset:12544\n"
"ds_read_b64_tr_b16 a[164:165], v45 offset:12288\n"
"ds_read_b64_tr_b16 a[166:167], v45 offset:12544\n"
"s_waitcnt lgkmcnt(4)\n"
"s_waitcnt vmcnt(0)\n"
"ds_read_b64_tr_b16 a[192:193], v44 offset:14336\n"
"ds_read_b64_tr_b16 a[194:195], v44 offset:14592\n"
"ds_read_b64_tr_b16 a[220:221], v45 offset:14336\n"
"ds_read_b64_tr_b16 a[222:223], v45 offset:14592\n"
"s_waitcnt lgkmcnt(4)\n"
"s_waitcnt vmcnt(0)\n"
"s_waitcnt lgkmcnt(0)\n"
"s_mov_b32 m0, s59\n"
"s_barrier\n"
"s_mov_b64 s[84:85], s[48:49]\n"
"s_mov_b32 s30, 0x80000000\n"
"s_mov_b32 s29, 0x80000000\n"
"s_mov_b32 s29, 0x80000000\n"
"s_mov_b32 s31, 0x80000000\n"
"s_mov_b32 s31, 0x80000000\n"
"s_mov_b32 s30, 0x80000000\n"
"s_mov_b32 s90, 0x80000000\n"
"s_mov_b32 s91, 0x80000000\n"
"s_mov_b32 s31, 0x80000000\n"
"s_mov_b32 s30, 0x80000000\n"
"s_mov_b32 s28, 0x80000000\n"
"v_mov_b32 v144, 0\n"
"v_mov_b32 v145, 0\n"
"v_mov_b32 v160, 0\n"
"v_mov_b32 v161, 0\n"
"v_mov_b32 v164, 0\n"
"v_mov_b32 v165, 0\n"
"v_mov_b32 v92, 0\n"
"v_mov_b32 v93, 0\n"
"s_cmp_eq_u32 s54, 0\n"
"s_cbranch_scc1 .LJ_1s\n"
"s_cmp_eq_u32 s54, 1\n"
"s_cbranch_scc1 .LJ_2s\n"
"s_cmp_eq_u32 s54, 2\n"
"s_cbranch_scc1 .LJ_3s\n"
".p2align 6\n"
".LJ_top:\n"
"v_mfma_f32_16x16x32_bf16 v[44:47], v[96:99], a[0:3], 0\n"
"s_cmp_lt_u32 s90, 0x8000000\n"
"s_cselect_b32 s90, 0x80000000, s90\n"
"v_mfma_f32_16x16x32_bf16 v[44:47], v[100:103], a[4:7], v[44:47]\n"
"s_waitcnt vmcnt(15)\n"
"global_load_dwordx4 a[224:227], v10, s[56:57] offset:0 sc1\n"
"v_mfma_f32_16x16x32_bf16 v[44:47], v[104:107], a[8:11], v[44:47]\n"
"s_waitcnt lgkmcnt(6)\n"
"buffer_load_dwordx2 v[92:93], v15, s[40:43], s90 offen offset:0 sc1\n"
"v_mfma_f32_16x16x32_bf16 v[44:47], v[108:111], a[12:15], v[44:47]\n"
"s_waitcnt vmcnt(13)\n"
"buffer_store_dwordx4 v[48:51], v18, s[76:79], s91 offen sc1\n"
"v_mfma_f32_16x16x32_bf16 v[44:47], v[112:115], a[16:19], v[44:47]\n"
"s_waitcnt lgkmcnt(3)\n"
"v_add_f32 v60, v60, v56\n"
"v_add_f32 v61, v61, v57\n"
"v_mfma_f32_16x16x32_bf16 v[44:47], v[116:119], a[20:23], v[44:47]\n"
"s_waitcnt vmcnt(13)\n"
"v_add_f32 v62, v62, v58\n"
"v_add_f32 v63, v63, v59\n"
"v_mfma_f32_16x16x32_bf16 v[44:47], v[120:123], a[24:27], v[44:47]\n"
"global_load_dwordx4 a[228:231], v11, s[56:57] offset:0 sc1\n"
"v_add_f32 v60, v60, v64\n"
"v_mfma_f32_16x16x32_bf16 v[48:51], v[96:99], a[28:31], 0\n"
"v_add_f32 v61, v61, v65\n"
"v_add_f32 v62, v62, v66\n"
"v_mfma_f32_16x16x32_bf16 v[48:51], v[100:103], a[32:35], v[48:51]\n"
"ds_write_addtid_b32 v44 offset:0\n"
"v_add_f32 v63, v63, v67\n"
"v_mfma_f32_16x16x32_bf16 v[48:51], v[104:107], a[36:39], v[48:51]\n"
"ds_write_addtid_b32 v45 offset:4640\n"
"v_add_f32 v60, v60, v52\n"
"v_mfma_f32_16x16x32_bf16 v[48:51], v[108:111], a[40:43], v[48:51]\n"
"ds_write_addtid_b32 v46 offset:9344\n"
"v_add_f32 v61, v61, v53\n"
"v_mfma_f32_16x16x32_bf16 v[48:51], v[112:115], a[44:47], v[48:51]\n"
"ds_write_addtid_b32 v47 offset:13984\n"
"v_add_f32 v62, v62, v54\n"
"v_mfma_f32_16x16x32_bf16 v[48:51], v[116:119], a[48:51], v[48:51]\n"
"v_add_f32 v63, v63, v55\n"
"v_mfma_f32_16x16x32_bf16 v[48:51], v[120:123], a[52:55], v[48:51]\n"
"s_add_u32 s31, s50, 0xffd00000\n"
"s_and_b32 s30, s31, 0xfbffffff\n"
"v_cvt_pk_bf16_f32 v44, v60, v61\n"
"v_mfma_f32_16x16x32_bf16 v[52:55], v[96:99], a[56:59], 0\n"
"s_waitcnt vmcnt(10)\n"
"v_cvt_pk_bf16_f32 v45, v62, v63\n"
"v_mfma_f32_16x16x32_bf16 v[56:59], v[38:41], v[68:71], 0\n"
"v_mfma_f32_16x16x32_bf16 v[56:59], v[38:41], v[76:79], v[56:59]\n"
"v_mfma_f32_16x16x32_bf16 v[56:59], v[38:41], v[84:87], v[56:59]\n"
"v_mfma_f32_16x16x32_bf16 v[60:63], v[38:41], v[72:75], 0\n"
"v_mfma_f32_16x16x32_bf16 v[60:63], v[38:41], v[80:83], v[60:63]\n"
"v_mfma_f32_16x16x32_bf16 v[60:63], v[38:41], v[88:91], v[60:63]\n"
"v_cvt_pk_bf16_f32 v64, v56, v57\n"
"v_cvt_pk_bf16_f32 v65, v58, v59\n"
"v_mfma_f32_16x16x32_bf16 v[52:55], v[100:103], a[60:63], v[52:55]\n"
"s_waitcnt lgkmcnt(4)\n"
"ds_write_addtid_b32 v48 offset:256\n"
"v_add_f32 v152, v152, v156\n"
"v_mfma_f32_16x16x32_bf16 v[52:55], v[104:107], a[64:67], v[52:55]\n"
"ds_write_addtid_b32 v49 offset:4896\n"
"v_add_f32 v153, v153, v157\n"
"v_mfma_f32_16x16x32_bf16 v[52:55], v[108:111], a[68:71], v[52:55]\n"
"ds_write_addtid_b32 v50 offset:9600\n"
"v_add_f32 v154, v154, v158\n"
"v_mfma_f32_16x16x32_bf16 v[52:55], v[112:115], a[72:75], v[52:55]\n"
"ds_write_addtid_b32 v51 offset:14240\n"
"v_add_f32 v155, v155, v159\n"
"v_mfma_f32_16x16x32_bf16 v[52:55], v[116:119], a[76:79], v[52:55]\n"
"buffer_store_dwordx2 v[0:1], v14, s[40:43], s29 offen offset:0 sc1\n"
"v_add_f32 v152, v152, v160\n"
"v_mfma_f32_16x16x32_bf16 v[52:55], v[120:123], a[80:83], v[52:55]\n"
"v_add_f32 v153, v153, v161\n"
"v_add_f32 v154, v154, v162\n"
"v_mfma_f32_16x16x32_bf16 v[48:51], v[96:99], a[84:87], 0\n"
"v_add_f32 v155, v155, v163\n"
"v_add_f32 v152, v152, v148\n"
"v_mfma_f32_16x16x32_bf16 v[48:51], v[100:103], a[88:91], v[48:51]\n"
"s_add_u32 s58, s50, 0x180000\n"
"ds_write_addtid_b32 v52 offset:512\n"
"v_add_f32 v153, v153, v149\n"
"v_mfma_f32_16x16x32_bf16 v[48:51], v[104:107], a[92:95], v[48:51]\n"
"s_and_b32 s58, s58, 0x3ffffff\n"
"ds_write_addtid_b32 v53 offset:5152\n"
"v_add_f32 v154, v154, v150\n"
"v_mfma_f32_16x16x32_bf16 v[48:51], v[108:111], a[96:99], v[48:51]\n"
"s_add_u32 s56, s40, s58\n"
"ds_write_addtid_b32 v54 offset:9856\n"
"v_add_f32 v155, v155, v151\n"
"v_mfma_f32_16x16x32_bf16 v[48:51], v[112:115], a[100:103], v[48:51]\n"
"s_addc_u32 s57, s41, 0\n"
"ds_write_addtid_b32 v55 offset:14496\n"
"v_cvt_pk_bf16_f32 v46, v152, v153\n"
"v_mfma_f32_16x16x32_bf16 v[48:51], v[116:119], a[104:107], v[48:51]\n"
"s_waitcnt vmcnt(9)\n"
"v_cmp_ne_u64 vcc, s[48:49], v[164:165]\n"
"v_cvt_pk_bf16_f32 v47, v154, v155\n"
"v_mfma_f32_16x16x32_bf16 v[48:51], v[120:123], a[108:111], v[48:51]\n"
"buffer_store_dwordx4 v[44:47], v13, s[40:43], s30 offen sc1\n"
"v_cvt_pk_bf16_f32 v66, v60, v61\n"
"v_cvt_pk_bf16_f32 v67, v62, v63\n"
"v_mfma_f32_16x16x32_bf16 v[52:55], v[144:147], v[144:147], 0\n"
"v_mfma_f32_16x16x32_bf16 v[44:47], v[34:37], v[144:147], 0\n"
"v_mfma_f32_16x16x32_bf16 v[52:55], v[64:67], v[64:67], v[52:55]\n"
"v_mfma_f32_16x16x32_bf16 v[44:47], v[34:37], v[64:67], v[44:47]\n"
"v_mfma_f32_16x16x32_bf16 v[56:59], v[96:99], a[112:115], 0\n"
"s_cbranch_vccnz .LJ_3s\n"
".LJ_3r:\n"
"v_cndmask_b32_e64 v52, v52, v53, s[64:65]\n"
"v_mfma_f32_16x16x32_bf16 v[56:59], v[100:103], a[116:119], v[56:59]\n"
"s_add_u32 s50, s50, 0x180000\n"
"ds_write_addtid_b32 v48 offset:768\n"
"v_cndmask_b32_e64 v54, v54, v55, s[64:65]\n"
"v_mfma_f32_16x16x32_bf16 v[56:59], v[104:107], a[120:123], v[56:59]\n"
"ds_write_addtid_b32 v49 offset:5408\n"
"v_cndmask_b32_e64 v45, v52, v54, s[66:67]\n"
"v_mfma_f32_16x16x32_bf16 v[56:59], v[108:111], a[124:127], v[56:59]\n"
"buffer_store_dwordx2 v[44:45], v23, s[40:43], s28 offen offset:0 sc1\n"
"ds_write_addtid_b32 v50 offset:10112\n"
"v_mfma_f32_16x16x32_bf16 v[56:59], v[112:115], a[128:131], v[56:59]\n"
"ds_write_addtid_b32 v51 offset:14752\n"
"global_load_dwordx4 a[232:235], v5, s[56:57] offset:0 sc1\n"
"v_mfma_f32_16x16x32_bf16 v[56:59], v[116:119], a[132:135], v[56:59]\n"
"ds_bpermute_b32 v44, v21, v64\n"
"v_mfma_f32_16x16x32_bf16 v[56:59], v[120:123], a[136:139], v[56:59]\n"
"buffer_load_dwordx2 v[144:145], v12, s[40:43], s50 offen sc1\n"
"v_mfma_f32_16x16x32_bf16 v[48:51], v[96:99], a[140:143], 0\n"
"ds_bpermute_b32 v45, v21, v65\n"
"v_mfma_f32_16x16x32_bf16 v[48:51], v[100:103], a[144:147], v[48:51]\n"
"ds_write_addtid_b32 v56 offset:1024\n"
"v_mfma_f32_16x16x32_bf16 v[48:51], v[104:107], a[148:151], v[48:51]\n"
"ds_write_addtid_b32 v57 offset:5664\n"
"global_load_dwordx4 a[236:239], v6, s[56:57] offset:0 sc1\n"
"v_mfma_f32_16x16x32_bf16 v[48:51], v[108:111], a[152:155], v[48:51]\n"
"ds_write_addtid_b32 v58 offset:10368\n"
"v_mfma_f32_16x16x32_bf16 v[48:51], v[112:115], a[156:159], v[48:51]\n"
"ds_write_addtid_b32 v59 offset:15008\n"
"v_mfma_f32_16x16x32_bf16 v[48:51], v[116:119], a[160:163], v[48:51]\n"
"ds_bpermute_b32 v46, v21, v66\n"
"v_mfma_f32_16x16x32_bf16 v[48:51], v[120:123], a[164:167], v[48:51]\n"
"ds_bpermute_b32 v47, v21, v67\n"
"v_mfma_f32_16x16x32_bf16 v[68:71], a[168:171], v[96:99], 0\n"
"global_load_dwordx4 a[240:243], v7, s[56:57] offset:0 sc1\n"
"v_mfma_f32_16x16x32_bf16 v[68:71], a[172:175], v[100:103], v[68:71]\n"
"ds_write_addtid_b32 v48 offset:1280\n"
"v_mfma_f32_16x16x32_bf16 v[68:71], a[176:179], v[104:107], v[68:71]\n"
"ds_write_addtid_b32 v49 offset:5920\n"
"v_mfma_f32_16x16x32_bf16 v[68:71], a[180:183], v[108:111], v[68:71]\n"
"ds_write_addtid_b32 v50 offset:10624\n"
"v_mfma_f32_16x16x32_bf16 v[68:71], a[184:187], v[112:115], v[68:71]\n"
"ds_write_addtid_b32 v51 offset:15264\n"
"v_mfma_f32_16x16x32_bf16 v[68:71], a[188:191], v[116:119], v[68:71]\n"
"s_waitcnt lgkmcnt(4)\n"
"buffer_store_dwordx4 v[44:47], v19, s[76:79], s91 offen sc1\n"
"global_load_dwordx4 a[244:247], v8, s[56:57] offset:0 sc1\n"
"v_mfma_f32_16x16x32_bf16 v[68:71], a[192:195], v[120:123], v[68:71]\n"
"v_mfma_f32_16x16x32_bf16 v[72:75], a[196:199], v[96:99], 0\n"
"s_and_b32 s30, s90, 0x3ffffff\n"
"v_mfma_f32_16x16x32_bf16 v[72:75], a[200:203], v[100:103], v[72:75]\n"
"s_waitcnt lgkmcnt(0)\n"
"v_mfma_f32_16x16x32_bf16 v[72:75], a[204:207], v[104:107], v[72:75]\n"
"s_barrier\n"
"v_mfma_f32_16x16x32_bf16 v[72:75], a[208:211], v[108:111], v[72:75]\n"
"s_waitcnt vmcnt(11)\n"
"global_load_dwordx4 a[248:251], v9, s[56:57] offset:0 sc1\n"
"ds_read_b128 v[76:79], v2 offset:0\n"
"v_mfma_f32_16x16x32_bf16 v[72:75], a[212:215], v[112:115], v[72:75]\n"
"s_add_u32 s62, s40, s30\n"
"ds_read_b128 v[80:83], v3 offset:512\n"
"ds_read_b128 v[84:87], v4 offset:1024\n"
"v_mfma_f32_16x16x32_bf16 v[72:75], a[216:219], v[116:119], v[72:75]\n"
"s_addc_u32 s63, s41, 0\n"
"ds_read_b128 v[88:91], v2 offset:256\n"
"ds_read_b128 v[96:99], v3 offset:768\n"
"v_mfma_f32_16x16x32_bf16 v[72:75], a[220:223], v[120:123], v[72:75]\n"
"ds_read_b128 v[100:103], v4 offset:1280\n"
"v_mfma_f32_16x16x32_bf16 v[104:107], v[124:127], a[0:3], 0\n"
"v_cmp_ne_u64 vcc, s[84:85], v[92:93]\n"
"v_mfma_f32_16x16x32_bf16 v[104:107], v[128:131], a[4:7], v[104:107]\n"
"global_load_dwordx4 a[252:255], v10, s[56:57] offset:0 sc1\n"
"v_mfma_f32_16x16x32_bf16 v[104:107], v[132:135], a[8:11], v[104:107]\n"
"s_cbranch_vccnz .LJ_4s\n"
".LJ_4r:\n"
"v_mfma_f32_16x16x32_bf16 v[104:107], v[136:139], a[12:15], v[104:107]\n"
"global_load_dwordx4 v[46:49], v16, s[62:63] offset:-4096 sc1\n"
"v_mfma_f32_16x16x32_bf16 v[104:107], v[140:143], a[16:19], v[104:107]\n"
"s_waitcnt lgkmcnt(3)\n"
"v_add_f32 v76, v76, v80\n"
"v_add_f32 v77, v77, v81\n"
"v_mfma_f32_16x16x32_bf16 v[104:107], a[224:227], a[20:23], v[104:107]\n"
"s_waitcnt vmcnt(12)\n"
"v_add_f32 v78, v78, v82\n"
"v_add_f32 v79, v79, v83\n"
"v_mfma_f32_16x16x32_bf16 v[104:107], a[228:231], a[24:27], v[104:107]\n"
"global_load_dwordx4 v[92:95], v11, s[56:57] offset:0 sc1\n"
"v_add_f32 v76, v76, v84\n"
"v_mfma_f32_16x16x32_bf16 v[80:83], v[124:127], a[28:31], 0\n"
"v_add_f32 v77, v77, v85\n"
"v_add_f32 v78, v78, v86\n"
"v_mfma_f32_16x16x32_bf16 v[80:83], v[128:131], a[32:35], v[80:83]\n"
"ds_write_addtid_b32 v104 offset:1536\n"
"v_add_f32 v79, v79, v87\n"
"v_mfma_f32_16x16x32_bf16 v[80:83], v[132:135], a[36:39], v[80:83]\n"
"ds_write_addtid_b32 v105 offset:6176\n"
"v_add_f32 v76, v76, v68\n"
"v_mfma_f32_16x16x32_bf16 v[80:83], v[136:139], a[40:43], v[80:83]\n"
"ds_write_addtid_b32 v106 offset:10880\n"
"v_add_f32 v77, v77, v69\n"
"v_mfma_f32_16x16x32_bf16 v[80:83], v[140:143], a[44:47], v[80:83]\n"
"ds_write_addtid_b32 v107 offset:15520\n"
"v_add_f32 v78, v78, v70\n"
"v_mfma_f32_16x16x32_bf16 v[80:83], a[224:227], a[48:51], v[80:83]\n"
"global_load_dwordx4 v[50:53], v16, s[62:63] offset:-3072 sc1\n"
"v_add_f32 v79, v79, v71\n"
"v_mfma_f32_16x16x32_bf16 v[80:83], a[228:231], a[52:55], v[80:83]\n"
"s_add_u32 s30, s50, 0xffd00000\n"
"s_and_b32 s29, s30, 0xfbffffff\n"
"v_cvt_pk_bf16_f32 v84, v76, v77\n"
"v_mfma_f32_16x16x32_bf16 v[68:71], v[124:127], a[56:59], 0\n"
"v_cvt_pk_bf16_f32 v85, v78, v79\n"
"v_mfma_f32_16x16x32_bf16 v[68:71], v[128:131], a[60:63], v[68:71]\n"
"s_waitcnt lgkmcnt(4)\n"
"ds_write_addtid_b32 v80 offset:1792\n"
"v_add_f32 v88, v88, v96\n"
"v_mfma_f32_16x16x32_bf16 v[68:71], v[132:135], a[64:67], v[68:71]\n"
"ds_write_addtid_b32 v81 offset:6432\n"
"v_add_f32 v89, v89, v97\n"
"v_mfma_f32_16x16x32_bf16 v[68:71], v[136:139], a[68:71], v[68:71]\n"
"ds_write_addtid_b32 v82 offset:11136\n"
"v_add_f32 v90, v90, v98\n"
"v_mfma_f32_16x16x32_bf16 v[68:71], v[140:143], a[72:75], v[68:71]\n"
"ds_write_addtid_b32 v83 offset:15776\n"
"v_add_f32 v91, v91, v99\n"
"v_mfma_f32_16x16x32_bf16 v[68:71], a[224:227], a[76:79], v[68:71]\n"
"s_waitcnt vmcnt(12)\n"
"v_add_f32 v88, v88, v100\n"
"v_add_f32 v89, v89, v101\n"
"v_mfma_f32_16x16x32_bf16 v[68:71], a[228:231], a[80:83], v[68:71]\n"
"v_add_f32 v90, v90, v102\n"
"v_add_f32 v91, v91, v103\n"
"v_mfma_f32_16x16x32_bf16 v[76:79], v[124:127], a[84:87], 0\n"
"v_add_f32 v88, v88, v72\n"
"v_add_f32 v89, v89, v73\n"
"v_mfma_f32_16x16x32_bf16 v[76:79], v[128:131], a[88:91], v[76:79]\n"
"s_add_u32 s58, s50, 0x180000\n"
"ds_write_addtid_b32 v68 offset:2048\n"
"v_add_f32 v90, v90, v74\n"
"v_mfma_f32_16x16x32_bf16 v[76:79], v[132:135], a[92:95], v[76:79]\n"
"s_and_b32 s58, s58, 0x3ffffff\n"
"ds_write_addtid_b32 v69 offset:6688\n"
"v_add_f32 v91, v91, v75\n"
"v_mfma_f32_16x16x32_bf16 v[76:79], v[136:139], a[96:99], v[76:79]\n"
"s_add_u32 s56, s40, s58\n"
"ds_write_addtid_b32 v70 offset:11392\n"
"v_cvt_pk_bf16_f32 v86, v88, v89\n"
"v_mfma_f32_16x16x32_bf16 v[76:79], v[140:143], a[100:103], v[76:79]\n"
"s_addc_u32 s57, s41, 0\n"
"ds_write_addtid_b32 v71 offset:16032\n"
"v_cvt_pk_bf16_f32 v87, v90, v91\n"
"v_mfma_f32_16x16x32_bf16 v[76:79], a[224:227], a[104:107], v[76:79]\n"
"s_waitcnt vmcnt(9)\n"
"buffer_store_dwordx4 v[84:87], v13, s[40:43], s29 offen sc1\n"
"v_cmp_ne_u64 vcc, s[48:49], v[144:145]\n"
"v_mfma_f32_16x16x32_bf16 v[76:79], a[228:231], a[108:111], v[76:79]\n"
"buffer_store_dwordx2 v[0:1], v14, s[40:43], s31 offen offset:0 sc1\n"
"v_mfma_f32_16x16x32_bf16 v[68:71], v[124:127], a[112:115], 0\n"
"s_cbranch_vccnz .LJ_1s\n"
".LJ_1r:\n"
"v_mfma_f32_16x16x32_bf16 v[68:71], v[128:131], a[116:119], v[68:71]\n"
"s_add_u32 s50, s50, 0x180000\n"
"ds_write_addtid_b32 v76 offset:2304\n"
"global_load_dwordx4 v[54:57], v16, s[62:63] offset:-2048 sc1\n"
"v_mfma_f32_16x16x32_bf16 v[68:71], v[132:135], a[120:123], v[68:71]\n"
"ds_write_addtid_b32 v77 offset:6944\n"
"v_mfma_f32_16x16x32_bf16 v[68:71], v[136:139], a[124:127], v[68:71]\n"
"ds_write_addtid_b32 v78 offset:11648\n"
"v_mfma_f32_16x16x32_bf16 v[68:71], v[140:143], a[128:131], v[68:71]\n"
"ds_write_addtid_b32 v79 offset:16288\n"
"global_load_dwordx4 v[96:99], v5, s[56:57] offset:0 sc1\n"
"v_mfma_f32_16x16x32_bf16 v[68:71], a[224:227], a[132:135], v[68:71]\n"
"v_mfma_f32_16x16x32_bf16 v[68:71], a[228:231], a[136:139], v[68:71]\n"
"buffer_load_dwordx2 v[160:161], v12, s[40:43], s50 offen sc1\n"
"v_mfma_f32_16x16x32_bf16 v[112:115], v[124:127], a[140:143], 0\n"
"global_load_dwordx4 v[58:61], v16, s[62:63] offset:-1024 sc1\n"
"v_mfma_f32_16x16x32_bf16 v[112:115], v[128:131], a[144:147], v[112:115]\n"
"ds_write_addtid_b32 v68 offset:2560\n"
"v_mfma_f32_16x16x32_bf16 v[112:115], v[132:135], a[148:151], v[112:115]\n"
"ds_write_addtid_b32 v69 offset:7200\n"
"global_load_dwordx4 v[100:103], v6, s[56:57] offset:0 sc1\n"
"v_mfma_f32_16x16x32_bf16 v[112:115], v[136:139], a[152:155], v[112:115]\n"
"ds_write_addtid_b32 v70 offset:11904\n"
"v_mfma_f32_16x16x32_bf16 v[112:115], v[140:143], a[156:159], v[112:115]\n"
"s_waitcnt vmcnt(13)\n"
"buffer_store_dwordx2 v[0:1], v24, s[40:43], s28 offen offset:0 sc1\n"
"ds_write_addtid_b32 v71 offset:16544\n"
"v_mfma_f32_16x16x32_bf16 v[112:115], a[224:227], a[160:163], v[112:115]\n"
"global_load_dwordx4 v[62:65], v17, s[62:63] offset:0 sc1\n"
"v_mfma_f32_16x16x32_bf16 v[112:115], a[228:231], a[164:167], v[112:115]\n"
"v_mfma_f32_16x16x32_bf16 v[144:147], a[168:171], v[124:127], 0\n"
"global_load_dwordx4 v[104:107], v7, s[56:57] offset:0 sc1\n"
"v_mfma_f32_16x16x32_bf16 v[144:147], a[172:175], v[128:131], v[144:147]\n"
"ds_write_addtid_b32 v112 offset:2816\n"
"v_mfma_f32_16x16x32_bf16 v[144:147], a[176:179], v[132:135], v[144:147]\n"
"ds_write_addtid_b32 v113 offset:7456\n"
"v_mfma_f32_16x16x32_bf16 v[144:147], a[180:183], v[136:139], v[144:147]\n"
"ds_write_addtid_b32 v114 offset:12160\n"
"global_load_dwordx4 v[70:73], v16, s[62:63] offset:0 sc1\n"
"v_mfma_f32_16x16x32_bf16 v[144:147], a[184:187], v[140:143], v[144:147]\n"
"ds_write_addtid_b32 v115 offset:16800\n"
"v_mfma_f32_16x16x32_bf16 v[144:147], a[188:191], a[224:227], v[144:147]\n"
"global_load_dwordx4 v[108:111], v8, s[56:57] offset:0 sc1\n"
"v_mfma_f32_16x16x32_bf16 v[144:147], a[192:195], a[228:231], v[144:147]\n"
"v_mfma_f32_16x16x32_bf16 v[148:151], a[196:199], v[124:127], 0\n"
"v_mfma_f32_16x16x32_bf16 v[148:151], a[200:203], v[128:131], v[148:151]\n"
"s_waitcnt lgkmcnt(0)\n"
"global_load_dwordx4 v[74:77], v16, s[62:63] offset:1024 sc1\n"
"v_mfma_f32_16x16x32_bf16 v[148:151], a[204:207], v[132:135], v[148:151]\n"
"s_barrier\n"
"v_mfma_f32_16x16x32_bf16 v[148:151], a[208:211], v[136:139], v[148:151]\n"
"global_load_dwordx4 v[112:115], v9, s[56:57] offset:0 sc1\n"
"ds_read_b128 v[124:127], v2 offset:1536\n"
"v_mfma_f32_16x16x32_bf16 v[148:151], a[212:215], v[140:143], v[148:151]\n"
"ds_read_b128 v[128:131], v3 offset:2048\n"
"ds_read_b128 v[132:135], v4 offset:2560\n"
"v_mfma_f32_16x16x32_bf16 v[148:151], a[216:219], a[224:227], v[148:151]\n"
"ds_read_b128 v[136:139], v2 offset:1792\n"
"ds_read_b128 v[140:143], v3 offset:2304\n"
"v_mfma_f32_16x16x32_bf16 v[148:151], a[220:223], a[228:231], v[148:151]\n"
"ds_read_b128 v[152:155], v4 offset:2816\n"
"global_load_dwordx4 v[78:81], v16, s[62:63] offset:2048 sc1\n"
"v_mfma_f32_16x16x32_bf16 v[156:159], a[232:235], a[0:3], 0\n"
"v_mfma_f32_16x16x32_bf16 v[156:159], a[236:239], a[4:7], v[156:159]\n"
"s_waitcnt vmcnt(20)\n"
"global_load_dwordx4 v[116:119], v10, s[56:57] offset:0 sc1\n"
"v_mfma_f32_16x16x32_bf16 v[156:159], a[240:243], a[8:11], v[156:159]\n"
"v_mfma_f32_16x16x32_bf16 v[156:159], a[244:247], a[12:15], v[156:159]\n"
"s_waitcnt vmcnt(19)\n"
"v_mfma_f32_16x16x32_bf16 v[156:159], a[248:251], a[16:19], v[156:159]\n"
"s_waitcnt lgkmcnt(3)\n"
"v_add_f32 v124, v124, v128\n"
"v_add_f32 v125, v125, v129\n"
"v_mfma_f32_16x16x32_bf16 v[156:159], a[252:255], a[20:23], v[156:159]\n"
"s_waitcnt vmcnt(17)\n"
"v_add_f32 v126, v126, v130\n"
"v_add_f32 v127, v127, v131\n"
"v_mfma_f32_16x16x32_bf16 v[156:159], v[92:95], a[24:27], v[156:159]\n"
"global_load_dwordx4 v[120:123], v11, s[56:57] offset:0 sc1\n"
"v_add_f32 v124, v124, v132\n"
"v_mfma_f32_16x16x32_bf16 v[128:131], a[232:235], a[28:31], 0\n"
"v_add_f32 v125, v125, v133\n"
"v_add_f32 v126, v126, v134\n"
"v_mfma_f32_16x16x32_bf16 v[128:131], a[236:239], a[32:35], v[128:131]\n"
"ds_write_addtid_b32 v156 offset:3072\n"
"v_add_f32 v127, v127, v135\n"
"v_mfma_f32_16x16x32_bf16 v[128:131], a[240:243], a[36:39], v[128:131]\n"
"ds_write_addtid_b32 v157 offset:7712\n"
"v_add_f32 v124, v124, v144\n"
"v_mfma_f32_16x16x32_bf16 v[128:131], a[244:247], a[40:43], v[128:131]\n"
"ds_write_addtid_b32 v158 offset:12416\n"
"v_add_f32 v125, v125, v145\n"
"v_mfma_f32_16x16x32_bf16 v[128:131], a[248:251], a[44:47], v[128:131]\n"
"ds_write_addtid_b32 v159 offset:17056\n"
"v_add_f32 v126, v126, v146\n"
"v_mfma_f32_16x16x32_bf16 v[128:131], a[252:255], a[48:51], v[128:131]\n"
"global_load_dwordx4 v[82:85], v16, s[62:63] offset:3072 sc1\n"
"v_add_f32 v127, v127, v147\n"
"v_mfma_f32_16x16x32_bf16 v[128:131], v[92:95], a[52:55], v[128:131]\n"
"s_add_u32 s29, s50, 0xffd00000\n"
"s_and_b32 s31, s29, 0xfbffffff\n"
"v_cvt_pk_bf16_f32 v132, v124, v125\n"
"v_mfma_f32_16x16x32_bf16 v[144:147], a[232:235], a[56:59], 0\n"
"v_cvt_pk_bf16_f32 v133, v126, v127\n"
"v_mfma_f32_16x16x32_bf16 v[144:147], a[236:239], a[60:63], v[144:147]\n"
"s_waitcnt lgkmcnt(4)\n"
"ds_write_addtid_b32 v128 offset:3328\n"
"v_add_f32 v136, v136, v140\n"
"v_mfma_f32_16x16x32_bf16 v[144:147], a[240:243], a[64:67], v[144:147]\n"
"s_mov_b32 s28, s90\n"
"ds_write_addtid_b32 v129 offset:7968\n"
"v_add_f32 v137, v137, v141\n"
"v_mfma_f32_16x16x32_bf16 v[144:147], a[244:247], a[68:71], v[144:147]\n"
"ds_write_addtid_b32 v130 offset:12672\n"
"v_add_f32 v138, v138, v142\n"
"v_mfma_f32_16x16x32_bf16 v[144:147], a[248:251], a[72:75], v[144:147]\n"
"s_waitcnt vmcnt(17)\n"
"buffer_store_dwordx2 v[0:1], v14, s[40:43], s30 offen offset:0 sc1\n"
"ds_write_addtid_b32 v131 offset:17312\n"
"v_mfma_f32_16x16x32_bf16 v[144:147], a[252:255], a[76:79], v[144:147]\n"
"v_add_f32 v139, v139, v143\n"
"v_add_f32 v136, v136, v152\n"
"v_mfma_f32_16x16x32_bf16 v[144:147], v[92:95], a[80:83], v[144:147]\n"
"v_add_f32 v137, v137, v153\n"
"v_add_f32 v138, v138, v154\n"
"v_mfma_f32_16x16x32_bf16 v[128:131], a[232:235], a[84:87], 0\n"
"v_add_f32 v139, v139, v155\n"
"v_add_f32 v136, v136, v148\n"
"v_mfma_f32_16x16x32_bf16 v[128:131], a[236:239], a[88:91], v[128:131]\n"
"s_add_u32 s58, s50, 0x180000\n"
"ds_write_addtid_b32 v144 offset:3584\n"
"v_add_f32 v137, v137, v149\n"
"v_mfma_f32_16x16x32_bf16 v[128:131], a[240:243], a[92:95], v[128:131]\n"
"s_and_b32 s58, s58, 0x3ffffff\n"
"ds_write_addtid_b32 v145 offset:8224\n"
"v_add_f32 v138, v138, v150\n"
"v_mfma_f32_16x16x32_bf16 v[128:131], a[244:247], a[96:99], v[128:131]\n"
"s_add_u32 s56, s40, s58\n"
"ds_write_addtid_b32 v146 offset:12928\n"
"v_add_f32 v139, v139, v151\n"
"v_mfma_f32_16x16x32_bf16 v[128:131], a[248:251], a[100:103], v[128:131]\n"
"s_addc_u32 s57, s41, 0\n"
"ds_write_addtid_b32 v147 offset:17568\n"
"v_cvt_pk_bf16_f32 v134, v136, v137\n"
"v_mfma_f32_16x16x32_bf16 v[128:131], a[252:255], a[104:107], v[128:131]\n"
"s_waitcnt vmcnt(14)\n"
"v_cmp_ne_u64 vcc, s[48:49], v[160:161]\n"
"v_cvt_pk_bf16_f32 v135, v138, v139\n"
"v_mfma_f32_16x16x32_bf16 v[128:131], v[92:95], a[108:111], v[128:131]\n"
"buffer_store_dwordx4 v[132:135], v13, s[40:43], s31 offen sc1\n"
"global_load_dwordx4 v[86:89], v17, s[62:63] offset:1024 sc1\n"
"v_mfma_f32_16x16x32_bf16 v[136:139], a[232:235], a[112:115], 0\n"
"s_cbranch_vccnz .LJ_2s\n"
".LJ_2r:\n"
"v_mov_b32 v44, v26\n"
"v_mfma_f32_16x16x32_bf16 v[136:139], a[236:239], a[116:119], v[136:139]\n"
"s_add_u32 s50, s50, 0x180000\n"
"ds_write_addtid_b32 v128 offset:3840\n"
"v_mov_b32 v45, v27\n"
"v_mfma_f32_16x16x32_bf16 v[136:139], a[240:243], a[120:123], v[136:139]\n"
"ds_write_addtid_b32 v129 offset:8480\n"
"v_mov_b32 v66, v28\n"
"v_mfma_f32_16x16x32_bf16 v[136:139], a[244:247], a[124:127], v[136:139]\n"
"ds_write_addtid_b32 v130 offset:13184\n"
"v_mov_b32 v67, v29\n"
"v_mfma_f32_16x16x32_bf16 v[136:139], a[248:251], a[128:131], v[136:139]\n"
"s_waitcnt vmcnt(12)\n"
"ds_write_addtid_b32 v131 offset:17824\n"
"global_load_dwordx4 v[124:127], v5, s[56:57] offset:0 sc1\n"
"v_mfma_f32_16x16x32_bf16 v[132:135], v[38:41], v[44:47], 0\n"
"s_cmp_ge_i32 s28, s80\n"
"s_cbranch_scc1 .LJ_ws\n"
".LJ_wr:\n"
"v_mfma_f32_16x16x32_bf16 v[132:135], v[38:41], v[52:55], v[132:135]\n"
"s_lshr_b32 s91, s28, s81\n"
"v_mfma_f32_16x16x32_bf16 v[132:135], v[38:41], v[60:63], v[132:135]\n"
"s_bitcmp1_b32 s91, 15\n"
"s_cselect_b32 s31, s83, s82\n"
"v_mfma_f32_16x16x32_bf16 v[128:131], v[38:41], v[48:51], 0\n"
"s_add_u32 s91, s91, s31\n"
"v_mfma_f32_16x16x32_bf16 v[128:131], v[38:41], v[56:59], v[128:131]\n"
"v_mfma_f32_16x16x32_bf16 v[128:131], v[38:41], v[64:67], v[128:131]\n"
"v_cvt_pk_bf16_f32 v144, v132, v133\n"
"v_cvt_pk_bf16_f32 v145, v134, v135\n"
"v_mfma_f32_16x16x32_bf16 v[136:139], a[252:255], a[132:135], v[136:139]\n"
"v_mfma_f32_16x16x32_bf16 v[136:139], v[92:95], a[136:139], v[136:139]\n"
"buffer_load_dwordx2 v[164:165], v12, s[40:43], s50 offen sc1\n"
"v_mfma_f32_16x16x32_bf16 v[44:47], a[232:235], a[140:143], 0\n"
"v_cvt_pk_bf16_f32 v146, v128, v129\n"
"v_cvt_pk_bf16_f32 v147, v130, v131\n"
"v_mfma_f32_16x16x32_bf16 v[44:47], a[236:239], a[144:147], v[44:47]\n"
"ds_write_addtid_b32 v136 offset:4096\n"
"v_mov_b32 v68, v30\n"
"v_mfma_f32_16x16x32_bf16 v[44:47], a[240:243], a[148:151], v[44:47]\n"
"ds_write_addtid_b32 v137 offset:8736\n"
"global_load_dwordx4 v[128:131], v6, s[56:57] offset:0 sc1\n"
"v_mfma_f32_16x16x32_bf16 v[44:47], a[244:247], a[152:155], v[44:47]\n"
"ds_write_addtid_b32 v138 offset:13440\n"
"v_mov_b32 v69, v31\n"
"v_mfma_f32_16x16x32_bf16 v[44:47], a[248:251], a[156:159], v[44:47]\n"
"ds_write_addtid_b32 v139 offset:18080\n"
"v_mov_b32 v90, v32\n"
"v_mfma_f32_16x16x32_bf16 v[44:47], a[252:255], a[160:163], v[44:47]\n"
"ds_bpermute_b32 v48, v21, v144\n"
"v_mfma_f32_16x16x32_bf16 v[44:47], v[92:95], a[164:167], v[44:47]\n"
"ds_bpermute_b32 v49, v21, v145\n"
"v_mfma_f32_16x16x32_bf16 v[52:55], a[168:171], a[232:235], 0\n"
"global_load_dwordx4 v[132:135], v7, s[56:57] offset:0 sc1\n"
"v_mov_b32 v91, v33\n"
"v_mfma_f32_16x16x32_bf16 v[52:55], a[172:175], a[236:239], v[52:55]\n"
"ds_write_addtid_b32 v44 offset:4352\n"
"v_mfma_f32_16x16x32_bf16 v[52:55], a[176:179], a[240:243], v[52:55]\n"
"ds_write_addtid_b32 v45 offset:8992\n"
"v_mfma_f32_16x16x32_bf16 v[52:55], a[180:183], a[244:247], v[52:55]\n"
"ds_write_addtid_b32 v46 offset:13696\n"
"v_mfma_f32_16x16x32_bf16 v[52:55], a[184:187], a[248:251], v[52:55]\n"
"ds_write_addtid_b32 v47 offset:18336\n"
"v_mfma_f32_16x16x32_bf16 v[52:55], a[188:191], a[252:255], v[52:55]\n"
"global_load_dwordx4 v[136:139], v8, s[56:57] offset:0 sc1\n"
"v_mfma_f32_16x16x32_bf16 v[52:55], a[192:195], v[92:95], v[52:55]\n"
"ds_bpermute_b32 v50, v21, v146\n"
"v_mfma_f32_16x16x32_bf16 v[148:151], a[196:199], a[232:235], 0\n"
"ds_bpermute_b32 v51, v21, v147\n"
"v_mfma_f32_16x16x32_bf16 v[148:151], a[200:203], a[236:239], v[148:151]\n"
"s_waitcnt lgkmcnt(2)\n"
"v_mfma_f32_16x16x32_bf16 v[148:151], a[204:207], a[240:243], v[148:151]\n"
"s_barrier\n"
"v_mfma_f32_16x16x32_bf16 v[148:151], a[208:211], a[244:247], v[148:151]\n"
"global_load_dwordx4 v[140:143], v9, s[56:57] offset:0 sc1\n"
"ds_read_b128 v[60:63], v2 offset:3072\n"
"v_mfma_f32_16x16x32_bf16 v[148:151], a[212:215], a[248:251], v[148:151]\n"
"ds_read_b128 v[56:59], v3 offset:3584\n"
"ds_read_b128 v[64:67], v4 offset:4096\n"
"v_mfma_f32_16x16x32_bf16 v[148:151], a[216:219], a[252:255], v[148:151]\n"
"s_add_u32 s90, s50, 0xff980000\n"
"ds_read_b128 v[152:155], v2 offset:3328\n"
"ds_read_b128 v[156:159], v3 offset:3840\n"
"v_mfma_f32_16x16x32_bf16 v[148:151], a[220:223], v[92:95], v[148:151]\n"
"ds_read_b128 v[160:163], v4 offset:4352\n"
"s_branch .LJ_top\n"
".LJ_1s:\n"
"s_cmp_ge_u32 s50, s53\n"
"s_cbranch_scc1 .LJ_t0\n"
"s_cmp_ge_u32 s50, s52\n"
"s_cbranch_scc1 .LJ_1r\n"
"s_sub_u32 s60, s50, 0x0\n"
"s_mov_b32 s61, 200000\n"
"s_nop 7\n"
".LJ_1p:\n"
"buffer_load_dwordx2 v[144:145], v12, s[40:43], s60 offen sc1\n"
"s_waitcnt vmcnt(0)\n"
"v_cmp_ne_u64 vcc, s[48:49], v[144:145]\n"
"s_cbranch_vccz .LJ_1px\n"
"s_sleep 1\n"
"s_sub_u32 s61, s61, 1\n"
"s_cbranch_scc0 .LJ_1p\n"
"s_branch .LJ_end\n"
".LJ_1px:\n"
"s_branch .LJ_1r\n"
".LJ_2s:\n"
"s_cmp_ge_u32 s50, s53\n"
"s_cbranch_scc1 .LJ_t1\n"
"s_cmp_ge_u32 s50, s52\n"
"s_cbranch_scc1 .LJ_2r\n"
"s_sub_u32 s60, s50, 0x0\n"
"s_mov_b32 s61, 200000\n"
"s_nop 7\n"
".LJ_2p:\n"
"buffer_load_dwordx2 v[160:161], v12, s[40:43], s60 offen sc1\n"
"s_waitcnt vmcnt(0)\n"
"v_cmp_ne_u64 vcc, s[48:49], v[160:161]\n"
"s_cbranch_vccz .LJ_2px\n"
"s_sleep 1\n"
"s_sub_u32 s61, s61, 1\n"
"s_cbranch_scc0 .LJ_2p\n"
"s_branch .LJ_end\n"
".LJ_2px:\n"
"s_branch .LJ_2r\n"
".LJ_3s:\n"
"s_cmp_ge_u32 s50, s53\n"
"s_cbranch_scc1 .LJ_t2\n"
"s_cmp_ge_u32 s50, s52\n"
"s_cbranch_scc1 .LJ_3r\n"
"s_sub_u32 s60, s50, 0x0\n"
"s_mov_b32 s61, 200000\n"
"s_nop 7\n"
".LJ_3p:\n"
"buffer_load_dwordx2 v[164:165], v12, s[40:43], s60 offen sc1\n"
"s_waitcnt vmcnt(0)\n"
"v_cmp_ne_u64 vcc, s[48:49], v[164:165]\n"
"s_cbranch_vccz .LJ_3px\n"
"s_sleep 1\n"
"s_sub_u32 s61, s61, 1\n"
"s_cbranch_scc0 .LJ_3p\n"
"s_branch .LJ_end\n"
".LJ_3px:\n"
"s_branch .LJ_3r\n"
".LJ_4s:\n"
"s_cmp_ge_i32 s90, 0x10000000\n"
"s_cselect_b64 s[84:85], 0, s[84:85]\n"
"s_cmp_ge_u32 s90, 0x10000000\n"
"s_cbranch_scc1 .LJ_4r\n"
"s_mov_b32 s61, 200000\n"
"s_nop 7\n"
".LJ_4p:\n"
"buffer_load_dwordx2 v[92:93], v15, s[40:43], s90 offen offset:0 sc1\n"
"s_waitcnt vmcnt(0)\n"
"v_cmp_ne_u64 vcc, s[84:85], v[92:93]\n"
"s_cbranch_vccz .LJ_4px\n"
"s_sleep 1\n"
"s_sub_u32 s61, s61, 1\n"
"s_cbranch_scc0 .LJ_4p\n"
"s_branch .LJ_end\n"
".LJ_4px:\n"
"s_branch .LJ_4r\n"
".LJ_ws:\n"
"s_mov_b64 s[76:77], s[68:69]\n"
"s_mov_b32 s78, s70\n"
"s_nop 7\n"
"v_mov_b32 v18, v20\n"
"v_add_u32 v19, 64, v20\n"
"v_mov_b32 v21, v22\n"
"s_mov_b32 s80, 0x7fffffff\n"
"s_mov_b32 s81, 4\n"
"s_mov_b32 s82, 0xff400000\n"
"s_mov_b32 s83, 0xff3f8800\n"
"s_branch .LJ_wr\n"
".LJ_wt0k6s:\n"
"s_mov_b64 s[76:77], s[68:69]\n"
"s_mov_b32 s78, s70\n"
"s_nop 7\n"
"v_mov_b32 v18, v20\n"
"v_add_u32 v19, 64, v20\n"
"v_mov_b32 v21, v22\n"
"s_mov_b32 s80, 0x7fffffff\n"
"s_mov_b32 s81, 4\n"
"s_mov_b32 s82, 0xff400000\n"
"s_mov_b32 s83, 0xff3f8800\n"
"s_branch .LJ_wt0k6r\n"
".LJ_4t0k7s:\n"
"s_cmp_ge_i32 s90, 0x10000000\n"
"s_cselect_b64 s[84:85], 0, s[84:85]\n"
"s_cmp_ge_u32 s90, 0x10000000\n"
"s_cbranch_scc1 .LJ_4t0k7r\n"
"s_mov_b32 s61, 200000\n"
"s_nop 7\n"
".LJ_4t0k7p:\n"
"buffer_load_dwordx2 v[92:93], v15, s[40:43], s90 offen offset:0 sc1\n"
"s_waitcnt vmcnt(0)\n"
"v_cmp_ne_u64 vcc, s[84:85], v[92:93]\n"
"s_cbranch_vccz .LJ_4t0k7px\n"
"s_sleep 1\n"
"s_sub_u32 s61, s61, 1\n"
"s_cbranch_scc0 .LJ_4t0k7p\n"
"s_branch .LJ_end\n"
".LJ_4t0k7px:\n"
"s_branch .LJ_4t0k7r\n"
".LJ_wt0k7s:\n"
"s_mov_b64 s[76:77], s[68:69]\n"
"s_mov_b32 s78, s70\n"
"s_nop 7\n"
"v_mov_b32 v18, v20\n"
"v_add_u32 v19, 64, v20\n"
"v_mov_b32 v21, v22\n"
"s_mov_b32 s80, 0x7fffffff\n"
"s_mov_b32 s81, 4\n"
"s_mov_b32 s82, 0xff400000\n"
"s_mov_b32 s83, 0xff3f8800\n"
"s_branch .LJ_wt0k7r\n"
".LJ_4t0k0s:\n"
"s_cmp_ge_i32 s90, 0x10000000\n"
"s_cbranch_scc1 .LJ_end\n"
"s_cselect_b64 s[84:85], 0, s[84:85]\n"
"s_cmp_ge_u32 s90, 0x10000000\n"
"s_cbranch_scc1 .LJ_4t0k0r\n"
"s_mov_b32 s61, 200000\n"
"s_nop 7\n"
".LJ_4t0k0p:\n"
"buffer_load_dwordx2 v[92:93], v15, s[40:43], s90 offen offset:0 sc1\n"
"s_waitcnt vmcnt(0)\n"
"v_cmp_ne_u64 vcc, s[84:85], v[92:93]\n"
"s_cbranch_vccz .LJ_4t0k0px\n"
"s_sleep 1\n"
"s_sub_u32 s61, s61, 1\n"
"s_cbranch_scc0 .LJ_4t0k0p\n"
"s_branch .LJ_end\n"
".LJ_4t0k0px:\n"
"s_branch .LJ_4t0k0r\n"
".LJ_wt0k0s:\n"
"s_mov_b64 s[76:77], s[68:69]\n"
"s_mov_b32 s78, s70\n"
"s_nop 7\n"
"v_mov_b32 v18, v20\n"
"v_add_u32 v19, 64, v20\n"
"v_mov_b32 v21, v22\n"
"s_mov_b32 s80, 0x7fffffff\n"
"s_mov_b32 s81, 4\n"
"s_mov_b32 s82, 0xff400000\n"
"s_mov_b32 s83, 0xff3f8800\n"
"s_branch .LJ_wt0k0r\n"
".LJ_wt1k6s:\n"
"s_mov_b64 s[76:77], s[68:69]\n"
"s_mov_b32 s78, s70\n"
"s_nop 7\n"
"v_mov_b32 v18, v20\n"
"v_add_u32 v19, 64, v20\n"
"v_mov_b32 v21, v22\n"
"s_mov_b32 s80, 0x7fffffff\n"
"s_mov_b32 s81, 4\n"
"s_mov_b32 s82, 0xff400000\n"
"s_mov_b32 s83, 0xff3f8800\n"
"s_branch .LJ_wt1k6r\n"
".LJ_4t1k7s:\n"
"s_cmp_ge_i32 s90, 0x10000000\n"
"s_cselect_b64 s[84:85], 0, s[84:85]\n"
"s_cmp_ge_u32 s90, 0x10000000\n"
"s_cbranch_scc1 .LJ_4t1k7r\n"
"s_mov_b32 s61, 200000\n"
"s_nop 7\n"
".LJ_4t1k7p:\n"
"buffer_load_dwordx2 v[92:93], v15, s[40:43], s90 offen offset:0 sc1\n"
"s_waitcnt vmcnt(0)\n"
"v_cmp_ne_u64 vcc, s[84:85], v[92:93]\n"
"s_cbranch_vccz .LJ_4t1k7px\n"
"s_sleep 1\n"
"s_sub_u32 s61, s61, 1\n"
"s_cbranch_scc0 .LJ_4t1k7p\n"
"s_branch .LJ_end\n"
".LJ_4t1k7px:\n"
"s_branch .LJ_4t1k7r\n"
".LJ_wt1k7s:\n"
"s_mov_b64 s[76:77], s[68:69]\n"
"s_mov_b32 s78, s70\n"
"s_nop 7\n"
"v_mov_b32 v18, v20\n"
"v_add_u32 v19, 64, v20\n"
"v_mov_b32 v21, v22\n"
"s_mov_b32 s80, 0x7fffffff\n"
"s_mov_b32 s81, 4\n"
"s_mov_b32 s82, 0xff400000\n"
"s_mov_b32 s83, 0xff3f8800\n"
"s_branch .LJ_wt1k7r\n"
".LJ_4t1k0s:\n"
"s_cmp_ge_i32 s90, 0x10000000\n"
"s_cbranch_scc1 .LJ_end\n"
"s_cselect_b64 s[84:85], 0, s[84:85]\n"
"s_cmp_ge_u32 s90, 0x10000000\n"
"s_cbranch_scc1 .LJ_4t1k0r\n"
"s_mov_b32 s61, 200000\n"
"s_nop 7\n"
".LJ_4t1k0p:\n"
"buffer_load_dwordx2 v[92:93], v15, s[40:43], s90 offen offset:0 sc1\n"
"s_waitcnt vmcnt(0)\n"
"v_cmp_ne_u64 vcc, s[84:85], v[92:93]\n"
"s_cbranch_vccz .LJ_4t1k0px\n"
"s_sleep 1\n"
"s_sub_u32 s61, s61, 1\n"
"s_cbranch_scc0 .LJ_4t1k0p\n"
"s_branch .LJ_end\n"
".LJ_4t1k0px:\n"
"s_branch .LJ_4t1k0r\n"
".LJ_wt1k0s:\n"
"s_mov_b64 s[76:77], s[68:69]\n"
"s_mov_b32 s78, s70\n"
"s_nop 7\n"
"v_mov_b32 v18, v20\n"
"v_add_u32 v19, 64, v20\n"
"v_mov_b32 v21, v22\n"
"s_mov_b32 s80, 0x7fffffff\n"
"s_mov_b32 s81, 4\n"
"s_mov_b32 s82, 0xff400000\n"
"s_mov_b32 s83, 0xff3f8800\n"
"s_branch .LJ_wt1k0r\n"
".LJ_4t2k7s:\n"
"s_cmp_ge_i32 s90, 0x10000000\n"
"s_cselect_b64 s[84:85], 0, s[84:85]\n"
"s_cmp_ge_u32 s90, 0x10000000\n"
"s_cbranch_scc1 .LJ_4t2k7r\n"
"s_mov_b32 s61, 200000\n"
"s_nop 7\n"
".LJ_4t2k7p:\n"
"buffer_load_dwordx2 v[92:93], v15, s[40:43], s90 offen offset:0 sc1\n"
"s_waitcnt vmcnt(0)\n"
"v_cmp_ne_u64 vcc, s[84:85], v[92:93]\n"
"s_cbranch_vccz .LJ_4t2k7px\n"
"s_sleep 1\n"
"s_sub_u32 s61, s61, 1\n"
"s_cbranch_scc0 .LJ_4t2k7p\n"
"s_branch .LJ_end\n"
".LJ_4t2k7px:\n"
"s_branch .LJ_4t2k7r\n"
".LJ_wt2k7s:\n"
"s_mov_b64 s[76:77], s[68:69]\n"
"s_mov_b32 s78, s70\n"
"s_nop 7\n"
"v_mov_b32 v18, v20\n"
"v_add_u32 v19, 64, v20\n"
"v_mov_b32 v21, v22\n"
"s_mov_b32 s80, 0x7fffffff\n"
"s_mov_b32 s81, 4\n"
"s_mov_b32 s82, 0xff400000\n"
"s_mov_b32 s83, 0xff3f8800\n"
"s_branch .LJ_wt2k7r\n"
".LJ_4t2k0s:\n"
"s_cmp_ge_i32 s90, 0x10000000\n"
"s_cbranch_scc1 .LJ_end\n"
"s_cselect_b64 s[84:85], 0, s[84:85]\n"
"s_cmp_ge_u32 s90, 0x10000000\n"
"s_cbranch_scc1 .LJ_4t2k0r\n"
"s_mov_b32 s61, 200000\n"
"s_nop 7\n"
".LJ_4t2k0p:\n"
"buffer_load_dwordx2 v[92:93], v15, s[40:43], s90 offen offset:0 sc1\n"
"s_waitcnt vmcnt(0)\n"
"v_cmp_ne_u64 vcc, s[84:85], v[92:93]\n"
"s_cbranch_vccz .LJ_4t2k0px\n"
"s_sleep 1\n"
"s_sub_u32 s61, s61, 1\n"
"s_cbranch_scc0 .LJ_4t2k0p\n"
"s_branch .LJ_end\n"
".LJ_4t2k0px:\n"
"s_branch .LJ_4t2k0r\n"
".LJ_wt2k0s:\n"
"s_mov_b64 s[76:77], s[68:69]\n"
"s_mov_b32 s78, s70\n"
"s_nop 7\n"
"v_mov_b32 v18, v20\n"
"v_add_u32 v19, 64, v20\n"
"v_mov_b32 v21, v22\n"
"s_mov_b32 s80, 0x7fffffff\n"
"s_mov_b32 s81, 4\n"
"s_mov_b32 s82, 0xff400000\n"
"s_mov_b32 s83, 0xff3f8800\n"
"s_branch .LJ_wt2k0r\n"
".LJ_t0:\n"
"v_mfma_f32_16x16x32_bf16 v[68:71], v[128:131], a[116:119], v[68:71]\n"
"ds_write_addtid_b32 v76 offset:2304\n"
"global_load_dwordx4 v[54:57], v16, s[62:63] offset:-2048 sc1\n"
"v_mfma_f32_16x16x32_bf16 v[68:71], v[132:135], a[120:123], v[68:71]\n"
"ds_write_addtid_b32 v77 offset:6944\n"
"v_mfma_f32_16x16x32_bf16 v[68:71], v[136:139], a[124:127], v[68:71]\n"
"ds_write_addtid_b32 v78 offset:11648\n"
"v_mfma_f32_16x16x32_bf16 v[68:71], v[140:143], a[128:131], v[68:71]\n"
"ds_write_addtid_b32 v79 offset:16288\n"
"v_mfma_f32_16x16x32_bf16 v[68:71], a[224:227], a[132:135], v[68:71]\n"
"v_mfma_f32_16x16x32_bf16 v[68:71], a[228:231], a[136:139], v[68:71]\n"
"v_mfma_f32_16x16x32_bf16 v[112:115], v[124:127], a[140:143], 0\n"
"global_load_dwordx4 v[58:61], v16, s[62:63] offset:-1024 sc1\n"
"v_mfma_f32_16x16x32_bf16 v[112:115], v[128:131], a[144:147], v[112:115]\n"
"ds_write_addtid_b32 v68 offset:2560\n"
"v_mfma_f32_16x16x32_bf16 v[112:115], v[132:135], a[148:151], v[112:115]\n"
"ds_write_addtid_b32 v69 offset:7200\n"
"v_mfma_f32_16x16x32_bf16 v[112:115], v[136:139], a[152:155], v[112:115]\n"
"ds_write_addtid_b32 v70 offset:11904\n"
"v_mfma_f32_16x16x32_bf16 v[112:115], v[140:143], a[156:159], v[112:115]\n"
"ds_write_addtid_b32 v71 offset:16544\n"
"v_mfma_f32_16x16x32_bf16 v[112:115], a[224:227], a[160:163], v[112:115]\n"
"global_load_dwordx4 v[62:65], v17, s[62:63] offset:0 sc1\n"
"v_mfma_f32_16x16x32_bf16 v[112:115], a[228:231], a[164:167], v[112:115]\n"
"v_mfma_f32_16x16x32_bf16 v[144:147], a[168:171], v[124:127], 0\n"
"v_mfma_f32_16x16x32_bf16 v[144:147], a[172:175], v[128:131], v[144:147]\n"
"ds_write_addtid_b32 v112 offset:2816\n"
"v_mfma_f32_16x16x32_bf16 v[144:147], a[176:179], v[132:135], v[144:147]\n"
"ds_write_addtid_b32 v113 offset:7456\n"
"v_mfma_f32_16x16x32_bf16 v[144:147], a[180:183], v[136:139], v[144:147]\n"
"ds_write_addtid_b32 v114 offset:12160\n"
"global_load_dwordx4 v[70:73], v16, s[62:63] offset:0 sc1\n"
"v_mfma_f32_16x16x32_bf16 v[144:147], a[184:187], v[140:143], v[144:147]\n"
"ds_write_addtid_b32 v115 offset:16800\n"
"v_mfma_f32_16x16x32_bf16 v[144:147], a[188:191], a[224:227], v[144:147]\n"
"v_mfma_f32_16x16x32_bf16 v[144:147], a[192:195], a[228:231], v[144:147]\n"
"v_mfma_f32_16x16x32_bf16 v[148:151], a[196:199], v[124:127], 0\n"
"v_mfma_f32_16x16x32_bf16 v[148:151], a[200:203], v[128:131], v[148:151]\n"
"s_waitcnt lgkmcnt(0)\n"
"global_load_dwordx4 v[74:77], v16, s[62:63] offset:1024 sc1\n"
"v_mfma_f32_16x16x32_bf16 v[148:151], a[204:207], v[132:135], v[148:151]\n"
"s_barrier\n"
"v_mfma_f32_16x16x32_bf16 v[148:151], a[208:211], v[136:139], v[148:151]\n"
"ds_read_b128 v[124:127], v2 offset:1536\n"
"v_mfma_f32_16x16x32_bf16 v[148:151], a[212:215], v[140:143], v[148:151]\n"
"ds_read_b128 v[128:131], v3 offset:2048\n"
"ds_read_b128 v[132:135], v4 offset:2560\n"
"v_mfma_f32_16x16x32_bf16 v[148:151], a[216:219], a[224:227], v[148:151]\n"
"ds_read_b128 v[136:139], v2 offset:1792\n"
"ds_read_b128 v[140:143], v3 offset:2304\n"
"v_mfma_f32_16x16x32_bf16 v[148:151], a[220:223], a[228:231], v[148:151]\n"
"s_waitcnt vmcnt(15)\n"
"ds_read_b128 v[152:155], v4 offset:2816\n"
"global_load_dwordx4 v[78:81], v16, s[62:63] offset:2048 sc1\n"
"v_mfma_f32_16x16x32_bf16 v[156:159], a[232:235], a[0:3], 0\n"
"v_mfma_f32_16x16x32_bf16 v[156:159], a[236:239], a[4:7], v[156:159]\n"
"s_waitcnt vmcnt(13)\n"
"v_mfma_f32_16x16x32_bf16 v[156:159], a[240:243], a[8:11], v[156:159]\n"
"v_mfma_f32_16x16x32_bf16 v[156:159], a[244:247], a[12:15], v[156:159]\n"
"s_waitcnt vmcnt(11)\n"
"v_mfma_f32_16x16x32_bf16 v[156:159], a[248:251], a[16:19], v[156:159]\n"
"s_waitcnt lgkmcnt(3)\n"
"v_add_f32 v124, v124, v128\n"
"v_add_f32 v125, v125, v129\n"
"v_mfma_f32_16x16x32_bf16 v[156:159], a[252:255], a[20:23], v[156:159]\n"
"s_waitcnt vmcnt(9)\n"
"v_add_f32 v126, v126, v130\n"
"v_add_f32 v127, v127, v131\n"
"v_mfma_f32_16x16x32_bf16 v[156:159], v[92:95], a[24:27], v[156:159]\n"
"v_add_f32 v124, v124, v132\n"
"v_mfma_f32_16x16x32_bf16 v[128:131], a[232:235], a[28:31], 0\n"
"v_add_f32 v125, v125, v133\n"
"v_add_f32 v126, v126, v134\n"
"v_mfma_f32_16x16x32_bf16 v[128:131], a[236:239], a[32:35], v[128:131]\n"
"ds_write_addtid_b32 v156 offset:3072\n"
"v_add_f32 v127, v127, v135\n"
"v_mfma_f32_16x16x32_bf16 v[128:131], a[240:243], a[36:39], v[128:131]\n"
"ds_write_addtid_b32 v157 offset:7712\n"
"v_add_f32 v124, v124, v144\n"
"v_mfma_f32_16x16x32_bf16 v[128:131], a[244:247], a[40:43], v[128:131]\n"
"ds_write_addtid_b32 v158 offset:12416\n"
"v_add_f32 v125, v125, v145\n"
"v_mfma_f32_16x16x32_bf16 v[128:131], a[248:251], a[44:47], v[128:131]\n"
"ds_write_addtid_b32 v159 offset:17056\n"
"v_add_f32 v126, v126, v146\n"
"v_mfma_f32_16x16x32_bf16 v[128:131], a[252:255], a[48:51], v[128:131]\n"
"global_load_dwordx4 v[82:85], v16, s[62:63] offset:3072 sc1\n"
"v_add_f32 v127, v127, v147\n"
"v_mfma_f32_16x16x32_bf16 v[128:131], v[92:95], a[52:55], v[128:131]\n"
"s_add_u32 s29, s50, 0xffe80000\n"
"s_and_b32 s31, s29, 0xfbffffff\n"
"v_cvt_pk_bf16_f32 v132, v124, v125\n"
"v_mfma_f32_16x16x32_bf16 v[144:147], a[232:235], a[56:59], 0\n"
"v_cvt_pk_bf16_f32 v133, v126, v127\n"
"v_mfma_f32_16x16x32_bf16 v[144:147], a[236:239], a[60:63], v[144:147]\n"
"s_waitcnt lgkmcnt(4)\n"
"ds_write_addtid_b32 v128 offset:3328\n"
"v_add_f32 v136, v136, v140\n"
"v_mfma_f32_16x16x32_bf16 v[144:147], a[240:243], a[64:67], v[144:147]\n"
"s_mov_b32 s28, s90\n"
"ds_write_addtid_b32 v129 offset:7968\n"
"v_add_f32 v137, v137, v141\n"
"v_mfma_f32_16x16x32_bf16 v[144:147], a[244:247], a[68:71], v[144:147]\n"
"ds_write_addtid_b32 v130 offset:12672\n"
"v_add_f32 v138, v138, v142\n"
"v_mfma_f32_16x16x32_bf16 v[144:147], a[248:251], a[72:75], v[144:147]\n"
"s_waitcnt vmcnt(8)\n"
"buffer_store_dwordx2 v[0:1], v14, s[40:43], s30 offen offset:0 sc1\n"
"ds_write_addtid_b32 v131 offset:17312\n"
"v_mfma_f32_16x16x32_bf16 v[144:147], a[252:255], a[76:79], v[144:147]\n"
"v_add_f32 v139, v139, v143\n"
"v_add_f32 v136, v136, v152\n"
"v_mfma_f32_16x16x32_bf16 v[144:147], v[92:95], a[80:83], v[144:147]\n"
"v_add_f32 v137, v137, v153\n"
"v_add_f32 v138, v138, v154\n"
"v_mfma_f32_16x16x32_bf16 v[128:131], a[232:235], a[84:87], 0\n"
"v_add_f32 v139, v139, v155\n"
"v_add_f32 v136, v136, v148\n"
"v_mfma_f32_16x16x32_bf16 v[128:131], a[236:239], a[88:91], v[128:131]\n"
"ds_write_addtid_b32 v144 offset:3584\n"
"v_add_f32 v137, v137, v149\n"
"v_mfma_f32_16x16x32_bf16 v[128:131], a[240:243], a[92:95], v[128:131]\n"
"ds_write_addtid_b32 v145 offset:8224\n"
"v_add_f32 v138, v138, v150\n"
"v_mfma_f32_16x16x32_bf16 v[128:131], a[244:247], a[96:99], v[128:131]\n"
"ds_write_addtid_b32 v146 offset:12928\n"
"v_add_f32 v139, v139, v151\n"
"v_mfma_f32_16x16x32_bf16 v[128:131], a[248:251], a[100:103], v[128:131]\n"
"ds_write_addtid_b32 v147 offset:17568\n"
"v_cvt_pk_bf16_f32 v134, v136, v137\n"
"v_mfma_f32_16x16x32_bf16 v[128:131], a[252:255], a[104:107], v[128:131]\n"
"v_cvt_pk_bf16_f32 v135, v138, v139\n"
"v_mfma_f32_16x16x32_bf16 v[128:131], v[92:95], a[108:111], v[128:131]\n"
"buffer_store_dwordx4 v[132:135], v13, s[40:43], s31 offen sc1\n"
"global_load_dwordx4 v[86:89], v17, s[62:63] offset:1024 sc1\n"
"v_mfma_f32_16x16x32_bf16 v[136:139], a[232:235], a[112:115], 0\n"
"v_mov_b32 v44, v26\n"
"v_mfma_f32_16x16x32_bf16 v[136:139], a[236:239], a[116:119], v[136:139]\n"
"ds_write_addtid_b32 v128 offset:3840\n"
"v_mov_b32 v45, v27\n"
"v_mfma_f32_16x16x32_bf16 v[136:139], a[240:243], a[120:123], v[136:139]\n"
"ds_write_addtid_b32 v129 offset:8480\n"
"v_mov_b32 v66, v28\n"
"v_mfma_f32_16x16x32_bf16 v[136:139], a[244:247], a[124:127], v[136:139]\n"
"ds_write_addtid_b32 v130 offset:13184\n"
"v_mov_b32 v67, v29\n"
"v_mfma_f32_16x16x32_bf16 v[136:139], a[248:251], a[128:131], v[136:139]\n"
"s_waitcnt vmcnt(7)\n"
"ds_write_addtid_b32 v131 offset:17824\n"
"v_mfma_f32_16x16x32_bf16 v[132:135], v[38:41], v[44:47], 0\n"
"s_cmp_ge_i32 s28, s80\n"
"s_cbranch_scc1 .LJ_wt0k6s\n"
".LJ_wt0k6r:\n"
"v_mfma_f32_16x16x32_bf16 v[132:135], v[38:41], v[52:55], v[132:135]\n"
"s_lshr_b32 s91, s28, s81\n"
"v_mfma_f32_16x16x32_bf16 v[132:135], v[38:41], v[60:63], v[132:135]\n"
"s_bitcmp1_b32 s91, 15\n"
"s_cselect_b32 s31, s83, s82\n"
"v_mfma_f32_16x16x32_bf16 v[128:131], v[38:41], v[48:51], 0\n"
"s_add_u32 s91, s91, s31\n"
"v_mfma_f32_16x16x32_bf16 v[128:131], v[38:41], v[56:59], v[128:131]\n"
"v_mfma_f32_16x16x32_bf16 v[128:131], v[38:41], v[64:67], v[128:131]\n"
"v_cvt_pk_bf16_f32 v144, v132, v133\n"
"v_cvt_pk_bf16_f32 v145, v134, v135\n"
"v_mfma_f32_16x16x32_bf16 v[136:139], a[252:255], a[132:135], v[136:139]\n"
"v_mfma_f32_16x16x32_bf16 v[136:139], v[92:95], a[136:139], v[136:139]\n"
"v_mfma_f32_16x16x32_bf16 v[44:47], a[232:235], a[140:143], 0\n"
"v_cvt_pk_bf16_f32 v146, v128, v129\n"
"v_cvt_pk_bf16_f32 v147, v130, v131\n"
"v_mfma_f32_16x16x32_bf16 v[44:47], a[236:239], a[144:147], v[44:47]\n"
"ds_write_addtid_b32 v136 offset:4096\n"
"v_mov_b32 v68, v30\n"
"v_mfma_f32_16x16x32_bf16 v[44:47], a[240:243], a[148:151], v[44:47]\n"
"ds_write_addtid_b32 v137 offset:8736\n"
"v_mfma_f32_16x16x32_bf16 v[44:47], a[244:247], a[152:155], v[44:47]\n"
"ds_write_addtid_b32 v138 offset:13440\n"
"v_mov_b32 v69, v31\n"
"v_mfma_f32_16x16x32_bf16 v[44:47], a[248:251], a[156:159], v[44:47]\n"
"ds_write_addtid_b32 v139 offset:18080\n"
"v_mov_b32 v90, v32\n"
"v_mfma_f32_16x16x32_bf16 v[44:47], a[252:255], a[160:163], v[44:47]\n"
"ds_bpermute_b32 v48, v21, v144\n"
"v_mfma_f32_16x16x32_bf16 v[44:47], v[92:95], a[164:167], v[44:47]\n"
"ds_bpermute_b32 v49, v21, v145\n"
"v_mfma_f32_16x16x32_bf16 v[52:55], a[168:171], a[232:235], 0\n"
"v_mov_b32 v91, v33\n"
"v_mfma_f32_16x16x32_bf16 v[52:55], a[172:175], a[236:239], v[52:55]\n"
"ds_write_addtid_b32 v44 offset:4352\n"
"v_mfma_f32_16x16x32_bf16 v[52:55], a[176:179], a[240:243], v[52:55]\n"
"ds_write_addtid_b32 v45 offset:8992\n"
"v_mfma_f32_16x16x32_bf16 v[52:55], a[180:183], a[244:247], v[52:55]\n"
"ds_write_addtid_b32 v46 offset:13696\n"
"v_mfma_f32_16x16x32_bf16 v[52:55], a[184:187], a[248:251], v[52:55]\n"
"ds_write_addtid_b32 v47 offset:18336\n"
"v_mfma_f32_16x16x32_bf16 v[52:55], a[188:191], a[252:255], v[52:55]\n"
"v_mfma_f32_16x16x32_bf16 v[52:55], a[192:195], v[92:95], v[52:55]\n"
"ds_bpermute_b32 v50, v21, v146\n"
"v_mfma_f32_16x16x32_bf16 v[148:151], a[196:199], a[232:235], 0\n"
"ds_bpermute_b32 v51, v21, v147\n"
"v_mfma_f32_16x16x32_bf16 v[148:151], a[200:203], a[236:239], v[148:151]\n"
"s_waitcnt lgkmcnt(2)\n"
"v_mfma_f32_16x16x32_bf16 v[148:151], a[204:207], a[240:243], v[148:151]\n"
"s_barrier\n"
"v_mfma_f32_16x16x32_bf16 v[148:151], a[208:211], a[244:247], v[148:151]\n"
"ds_read_b128 v[60:63], v2 offset:3072\n"
"v_mfma_f32_16x16x32_bf16 v[148:151], a[212:215], a[248:251], v[148:151]\n"
"ds_read_b128 v[56:59], v3 offset:3584\n"
"ds_read_b128 v[64:67], v4 offset:4096\n"
"v_mfma_f32_16x16x32_bf16 v[148:151], a[216:219], a[252:255], v[148:151]\n"
"s_add_u32 s90, s50, 0xffc80000\n"
"ds_read_b128 v[152:155], v2 offset:3328\n"
"ds_read_b128 v[156:159], v3 offset:3840\n"
"v_mfma_f32_16x16x32_bf16 v[148:151], a[220:223], v[92:95], v[148:151]\n"
"ds_read_b128 v[160:163], v4 offset:4352\n"
"s_cmp_lt_u32 s90, 0x8000000\n"
"s_cselect_b32 s90, 0x80000000, s90\n"
"s_waitcnt lgkmcnt(6)\n"
"buffer_load_dwordx2 v[92:93], v15, s[40:43], s90 offen offset:0 sc1\n"
"buffer_store_dwordx4 v[48:51], v18, s[76:79], s91 offen sc1\n"
"s_waitcnt lgkmcnt(3)\n"
"v_add_f32 v60, v60, v56\n"
"v_add_f32 v61, v61, v57\n"
"v_add_f32 v62, v62, v58\n"
"v_add_f32 v63, v63, v59\n"
"v_add_f32 v60, v60, v64\n"
"v_add_f32 v61, v61, v65\n"
"v_add_f32 v62, v62, v66\n"
"v_add_f32 v63, v63, v67\n"
"v_add_f32 v60, v60, v52\n"
"v_add_f32 v61, v61, v53\n"
"v_add_f32 v62, v62, v54\n"
"v_add_f32 v63, v63, v55\n"
"s_add_u32 s31, s50, 0x0\n"
"s_and_b32 s30, s31, 0xfbffffff\n"
"v_cvt_pk_bf16_f32 v44, v60, v61\n"
"s_waitcnt vmcnt(2)\n"
"v_cvt_pk_bf16_f32 v45, v62, v63\n"
"v_mfma_f32_16x16x32_bf16 v[56:59], v[38:41], v[68:71], 0\n"
"v_mfma_f32_16x16x32_bf16 v[56:59], v[38:41], v[76:79], v[56:59]\n"
"v_mfma_f32_16x16x32_bf16 v[56:59], v[38:41], v[84:87], v[56:59]\n"
"v_mfma_f32_16x16x32_bf16 v[60:63], v[38:41], v[72:75], 0\n"
"v_mfma_f32_16x16x32_bf16 v[60:63], v[38:41], v[80:83], v[60:63]\n"
"v_mfma_f32_16x16x32_bf16 v[60:63], v[38:41], v[88:91], v[60:63]\n"
"v_cvt_pk_bf16_f32 v64, v56, v57\n"
"v_cvt_pk_bf16_f32 v65, v58, v59\n"
"s_waitcnt lgkmcnt(0)\n"
"v_add_f32 v152, v152, v156\n"
"v_add_f32 v153, v153, v157\n"
"v_add_f32 v154, v154, v158\n"
"v_add_f32 v155, v155, v159\n"
"s_waitcnt vmcnt(3)\n"
"buffer_store_dwordx2 v[0:1], v14, s[40:43], s29 offen offset:0 sc1\n"
"v_add_f32 v152, v152, v160\n"
"v_add_f32 v153, v153, v161\n"
"v_add_f32 v154, v154, v162\n"
"v_add_f32 v155, v155, v163\n"
"v_add_f32 v152, v152, v148\n"
"v_add_f32 v153, v153, v149\n"
"v_add_f32 v154, v154, v150\n"
"v_add_f32 v155, v155, v151\n"
"v_cvt_pk_bf16_f32 v46, v152, v153\n"
"v_cvt_pk_bf16_f32 v47, v154, v155\n"
"buffer_store_dwordx4 v[44:47], v13, s[40:43], s30 offen sc1\n"
"v_cvt_pk_bf16_f32 v66, v60, v61\n"
"v_cvt_pk_bf16_f32 v67, v62, v63\n"
"ds_bpermute_b32 v44, v21, v64\n"
"ds_bpermute_b32 v45, v21, v65\n"
"ds_bpermute_b32 v46, v21, v66\n"
"ds_bpermute_b32 v47, v21, v67\n"
"s_waitcnt lgkmcnt(0)\n"
"buffer_store_dwordx4 v[44:47], v19, s[76:79], s91 offen sc1\n"
"s_and_b32 s30, s90, 0x3ffffff\n"
"s_waitcnt vmcnt(4)\n"
"s_add_u32 s62, s40, s30\n"
"s_addc_u32 s63, s41, 0\n"
"v_cmp_ne_u64 vcc, s[84:85], v[92:93]\n"
"s_cbranch_vccnz .LJ_4t0k7s\n"
".LJ_4t0k7r:\n"
"global_load_dwordx4 v[46:49], v16, s[62:63] offset:-4096 sc1\n"
"global_load_dwordx4 v[50:53], v16, s[62:63] offset:-3072 sc1\n"
"s_waitcnt vmcnt(3)\n"
"buffer_store_dwordx2 v[0:1], v14, s[40:43], s31 offen offset:0 sc1\n"
"global_load_dwordx4 v[54:57], v16, s[62:63] offset:-2048 sc1\n"
"global_load_dwordx4 v[58:61], v16, s[62:63] offset:-1024 sc1\n"
"global_load_dwordx4 v[62:65], v17, s[62:63] offset:0 sc1\n"
"global_load_dwordx4 v[70:73], v16, s[62:63] offset:0 sc1\n"
"global_load_dwordx4 v[74:77], v16, s[62:63] offset:1024 sc1\n"
"global_load_dwordx4 v[78:81], v16, s[62:63] offset:2048 sc1\n"
"global_load_dwordx4 v[82:85], v16, s[62:63] offset:3072 sc1\n"
"s_mov_b32 s28, s90\n"
"global_load_dwordx4 v[86:89], v17, s[62:63] offset:1024 sc1\n"
"v_mov_b32 v44, v26\n"
"v_mov_b32 v45, v27\n"
"v_mov_b32 v66, v28\n"
"v_mov_b32 v67, v29\n"
"s_waitcnt vmcnt(5)\n"
"v_mfma_f32_16x16x32_bf16 v[132:135], v[38:41], v[44:47], 0\n"
"s_cmp_ge_i32 s28, s80\n"
"s_cbranch_scc1 .LJ_wt0k7s\n"
".LJ_wt0k7r:\n"
"v_mfma_f32_16x16x32_bf16 v[132:135], v[38:41], v[52:55], v[132:135]\n"
"s_lshr_b32 s91, s28, s81\n"
"v_mfma_f32_16x16x32_bf16 v[132:135], v[38:41], v[60:63], v[132:135]\n"
"s_bitcmp1_b32 s91, 15\n"
"s_cselect_b32 s31, s83, s82\n"
"v_mfma_f32_16x16x32_bf16 v[128:131], v[38:41], v[48:51], 0\n"
"s_add_u32 s91, s91, s31\n"
"v_mfma_f32_16x16x32_bf16 v[128:131], v[38:41], v[56:59], v[128:131]\n"
"v_mfma_f32_16x16x32_bf16 v[128:131], v[38:41], v[64:67], v[128:131]\n"
"v_cvt_pk_bf16_f32 v144, v132, v133\n"
"v_cvt_pk_bf16_f32 v145, v134, v135\n"
"s_nop 5\n"
"v_cvt_pk_bf16_f32 v146, v128, v129\n"
"v_cvt_pk_bf16_f32 v147, v130, v131\n"
"v_mov_b32 v68, v30\n"
"v_mov_b32 v69, v31\n"
"v_mov_b32 v90, v32\n"
"ds_bpermute_b32 v48, v21, v144\n"
"ds_bpermute_b32 v49, v21, v145\n"
"v_mov_b32 v91, v33\n"
"ds_bpermute_b32 v50, v21, v146\n"
"ds_bpermute_b32 v51, v21, v147\n"
"s_add_u32 s90, s50, 0x100000\n"
"s_cmp_lt_u32 s90, 0x8000000\n"
"s_cselect_b32 s90, 0x80000000, s90\n"
"s_waitcnt lgkmcnt(0)\n"
"buffer_load_dwordx2 v[92:93], v15, s[40:43], s90 offen offset:0 sc1\n"
"buffer_store_dwordx4 v[48:51], v18, s[76:79], s91 offen sc1\n"
"s_waitcnt vmcnt(2)\n"
"v_mfma_f32_16x16x32_bf16 v[56:59], v[38:41], v[68:71], 0\n"
"v_mfma_f32_16x16x32_bf16 v[56:59], v[38:41], v[76:79], v[56:59]\n"
"v_mfma_f32_16x16x32_bf16 v[56:59], v[38:41], v[84:87], v[56:59]\n"
"v_mfma_f32_16x16x32_bf16 v[60:63], v[38:41], v[72:75], 0\n"
"v_mfma_f32_16x16x32_bf16 v[60:63], v[38:41], v[80:83], v[60:63]\n"
"v_mfma_f32_16x16x32_bf16 v[60:63], v[38:41], v[88:91], v[60:63]\n"
"v_cvt_pk_bf16_f32 v64, v56, v57\n"
"v_cvt_pk_bf16_f32 v65, v58, v59\n"
"s_nop 5\n"
"v_cvt_pk_bf16_f32 v66, v60, v61\n"
"v_cvt_pk_bf16_f32 v67, v62, v63\n"
"ds_bpermute_b32 v44, v21, v64\n"
"ds_bpermute_b32 v45, v21, v65\n"
"ds_bpermute_b32 v46, v21, v66\n"
"ds_bpermute_b32 v47, v21, v67\n"
"s_waitcnt lgkmcnt(0)\n"
"buffer_store_dwordx4 v[44:47], v19, s[76:79], s91 offen sc1\n"
"s_and_b32 s30, s90, 0x3ffffff\n"
"s_waitcnt vmcnt(2)\n"
"s_add_u32 s62, s40, s30\n"
"s_addc_u32 s63, s41, 0\n"
"v_cmp_ne_u64 vcc, s[84:85], v[92:93]\n"
"s_cbranch_vccnz .LJ_4t0k0s\n"
".LJ_4t0k0r:\n"
"global_load_dwordx4 v[46:49], v16, s[62:63] offset:-4096 sc1\n"
"global_load_dwordx4 v[50:53], v16, s[62:63] offset:-3072 sc1\n"
"global_load_dwordx4 v[54:57], v16, s[62:63] offset:-2048 sc1\n"
"global_load_dwordx4 v[58:61], v16, s[62:63] offset:-1024 sc1\n"
"global_load_dwordx4 v[62:65], v17, s[62:63] offset:0 sc1\n"
"global_load_dwordx4 v[70:73], v16, s[62:63] offset:0 sc1\n"
"global_load_dwordx4 v[74:77], v16, s[62:63] offset:1024 sc1\n"
"global_load_dwordx4 v[78:81], v16, s[62:63] offset:2048 sc1\n"
"global_load_dwordx4 v[82:85], v16, s[62:63] offset:3072 sc1\n"
"s_mov_b32 s28, s90\n"
"global_load_dwordx4 v[86:89], v17, s[62:63] offset:1024 sc1\n"
"v_mov_b32 v44, v26\n"
"v_mov_b32 v45, v27\n"
"v_mov_b32 v66, v28\n"
"v_mov_b32 v67, v29\n"
"s_waitcnt vmcnt(5)\n"
"v_mfma_f32_16x16x32_bf16 v[132:135], v[38:41], v[44:47], 0\n"
"s_cmp_ge_i32 s28, s80\n"
"s_cbranch_scc1 .LJ_wt0k0s\n"
".LJ_wt0k0r:\n"
"v_mfma_f32_16x16x32_bf16 v[132:135], v[38:41], v[52:55], v[132:135]\n"
"s_lshr_b32 s91, s28, s81\n"
"v_mfma_f32_16x16x32_bf16 v[132:135], v[38:41], v[60:63], v[132:135]\n"
"s_bitcmp1_b32 s91, 15\n"
"s_cselect_b32 s31, s83, s82\n"
"v_mfma_f32_16x16x32_bf16 v[128:131], v[38:41], v[48:51], 0\n"
"s_add_u32 s91, s91, s31\n"
"v_mfma_f32_16x16x32_bf16 v[128:131], v[38:41], v[56:59], v[128:131]\n"
"v_mfma_f32_16x16x32_bf16 v[128:131], v[38:41], v[64:67], v[128:131]\n"
"v_cvt_pk_bf16_f32 v144, v132, v133\n"
"v_cvt_pk_bf16_f32 v145, v134, v135\n"
"s_nop 5\n"
"v_cvt_pk_bf16_f32 v146, v128, v129\n"
"v_cvt_pk_bf16_f32 v147, v130, v131\n"
"v_mov_b32 v68, v30\n"
"v_mov_b32 v69, v31\n"
"v_mov_b32 v90, v32\n"
"ds_bpermute_b32 v48, v21, v144\n"
"ds_bpermute_b32 v49, v21, v145\n"
"v_mov_b32 v91, v33\n"
"ds_bpermute_b32 v50, v21, v146\n"
"ds_bpermute_b32 v51, v21, v147\n"
"s_waitcnt lgkmcnt(0)\n"
"buffer_store_dwordx4 v[48:51], v18, s[76:79], s91 offen sc1\n"
"s_waitcnt vmcnt(1)\n"
"v_mfma_f32_16x16x32_bf16 v[56:59], v[38:41], v[68:71], 0\n"
"v_mfma_f32_16x16x32_bf16 v[56:59], v[38:41], v[76:79], v[56:59]\n"
"v_mfma_f32_16x16x32_bf16 v[56:59], v[38:41], v[84:87], v[56:59]\n"
"v_mfma_f32_16x16x32_bf16 v[60:63], v[38:41], v[72:75], 0\n"
"v_mfma_f32_16x16x32_bf16 v[60:63], v[38:41], v[80:83], v[60:63]\n"
"v_mfma_f32_16x16x32_bf16 v[60:63], v[38:41], v[88:91], v[60:63]\n"
"v_cvt_pk_bf16_f32 v64, v56, v57\n"
"v_cvt_pk_bf16_f32 v65, v58, v59\n"
"s_nop 5\n"
"v_cvt_pk_bf16_f32 v66, v60, v61\n"
"v_cvt_pk_bf16_f32 v67, v62, v63\n"
"ds_bpermute_b32 v44, v21, v64\n"
"ds_bpermute_b32 v45, v21, v65\n"
"ds_bpermute_b32 v46, v21, v66\n"
"ds_bpermute_b32 v47, v21, v67\n"
"s_waitcnt lgkmcnt(0)\n"
"buffer_store_dwordx4 v[44:47], v19, s[76:79], s91 offen sc1\n"
"s_branch .LJ_end\n"
".LJ_t1:\n"
"v_mov_b32 v44, v26\n"
"v_mfma_f32_16x16x32_bf16 v[136:139], a[236:239], a[116:119], v[136:139]\n"
"ds_write_addtid_b32 v128 offset:3840\n"
"v_mov_b32 v45, v27\n"
"v_mfma_f32_16x16x32_bf16 v[136:139], a[240:243], a[120:123], v[136:139]\n"
"ds_write_addtid_b32 v129 offset:8480\n"
"v_mov_b32 v66, v28\n"
"v_mfma_f32_16x16x32_bf16 v[136:139], a[244:247], a[124:127], v[136:139]\n"
"ds_write_addtid_b32 v130 offset:13184\n"
"v_mov_b32 v67, v29\n"
"v_mfma_f32_16x16x32_bf16 v[136:139], a[248:251], a[128:131], v[136:139]\n"
"s_waitcnt vmcnt(12)\n"
"ds_write_addtid_b32 v131 offset:17824\n"
"v_mfma_f32_16x16x32_bf16 v[132:135], v[38:41], v[44:47], 0\n"
"s_cmp_ge_i32 s28, s80\n"
"s_cbranch_scc1 .LJ_wt1k6s\n"
".LJ_wt1k6r:\n"
"v_mfma_f32_16x16x32_bf16 v[132:135], v[38:41], v[52:55], v[132:135]\n"
"s_lshr_b32 s91, s28, s81\n"
"v_mfma_f32_16x16x32_bf16 v[132:135], v[38:41], v[60:63], v[132:135]\n"
"s_bitcmp1_b32 s91, 15\n"
"s_cselect_b32 s31, s83, s82\n"
"v_mfma_f32_16x16x32_bf16 v[128:131], v[38:41], v[48:51], 0\n"
"s_add_u32 s91, s91, s31\n"
"v_mfma_f32_16x16x32_bf16 v[128:131], v[38:41], v[56:59], v[128:131]\n"
"v_mfma_f32_16x16x32_bf16 v[128:131], v[38:41], v[64:67], v[128:131]\n"
"v_cvt_pk_bf16_f32 v144, v132, v133\n"
"v_cvt_pk_bf16_f32 v145, v134, v135\n"
"v_mfma_f32_16x16x32_bf16 v[136:139], a[252:255], a[132:135], v[136:139]\n"
"v_mfma_f32_16x16x32_bf16 v[136:139], v[92:95], a[136:139], v[136:139]\n"
"v_mfma_f32_16x16x32_bf16 v[44:47], a[232:235], a[140:143], 0\n"
"v_cvt_pk_bf16_f32 v146, v128, v129\n"
"v_cvt_pk_bf16_f32 v147, v130, v131\n"
"v_mfma_f32_16x16x32_bf16 v[44:47], a[236:239], a[144:147], v[44:47]\n"
"ds_write_addtid_b32 v136 offset:4096\n"
"v_mov_b32 v68, v30\n"
"v_mfma_f32_16x16x32_bf16 v[44:47], a[240:243], a[148:151], v[44:47]\n"
"ds_write_addtid_b32 v137 offset:8736\n"
"v_mfma_f32_16x16x32_bf16 v[44:47], a[244:247], a[152:155], v[44:47]\n"
"ds_write_addtid_b32 v138 offset:13440\n"
"v_mov_b32 v69, v31\n"
"v_mfma_f32_16x16x32_bf16 v[44:47], a[248:251], a[156:159], v[44:47]\n"
"ds_write_addtid_b32 v139 offset:18080\n"
"v_mov_b32 v90, v32\n"
"v_mfma_f32_16x16x32_bf16 v[44:47], a[252:255], a[160:163], v[44:47]\n"
"ds_bpermute_b32 v48, v21, v144\n"
"v_mfma_f32_16x16x32_bf16 v[44:47], v[92:95], a[164:167], v[44:47]\n"
"ds_bpermute_b32 v49, v21, v145\n"
"v_mfma_f32_16x16x32_bf16 v[52:55], a[168:171], a[232:235], 0\n"
"v_mov_b32 v91, v33\n"
"v_mfma_f32_16x16x32_bf16 v[52:55], a[172:175], a[236:239], v[52:55]\n"
"ds_write_addtid_b32 v44 offset:4352\n"
"v_mfma_f32_16x16x32_bf16 v[52:55], a[176:179], a[240:243], v[52:55]\n"
"ds_write_addtid_b32 v45 offset:8992\n"
"v_mfma_f32_16x16x32_bf16 v[52:55], a[180:183], a[244:247], v[52:55]\n"
"ds_write_addtid_b32 v46 offset:13696\n"
"v_mfma_f32_16x16x32_bf16 v[52:55], a[184:187], a[248:251], v[52:55]\n"
"ds_write_addtid_b32 v47 offset:18336\n"
"v_mfma_f32_16x16x32_bf16 v[52:55], a[188:191], a[252:255], v[52:55]\n"
"v_mfma_f32_16x16x32_bf16 v[52:55], a[192:195], v[92:95], v[52:55]\n"
"ds_bpermute_b32 v50, v21, v146\n"
"v_mfma_f32_16x16x32_bf16 v[148:151], a[196:199], a[232:235], 0\n"
"ds_bpermute_b32 v51, v21, v147\n"
"v_mfma_f32_16x16x32_bf16 v[148:151], a[200:203], a[236:239], v[148:151]\n"
"s_waitcnt lgkmcnt(2)\n"
"v_mfma_f32_16x16x32_bf16 v[148:151], a[204:207], a[240:243], v[148:151]\n"
"s_barrier\n"
"v_mfma_f32_16x16x32_bf16 v[148:151], a[208:211], a[244:247], v[148:151]\n"
"ds_read_b128 v[60:63], v2 offset:3072\n"
"v_mfma_f32_16x16x32_bf16 v[148:151], a[212:215], a[248:251], v[148:151]\n"
"ds_read_b128 v[56:59], v3 offset:3584\n"
"ds_read_b128 v[64:67], v4 offset:4096\n"
"v_mfma_f32_16x16x32_bf16 v[148:151], a[216:219], a[252:255], v[148:151]\n"
"s_add_u32 s90, s50, 0xffb00000\n"
"ds_read_b128 v[152:155], v2 offset:3328\n"
"ds_read_b128 v[156:159], v3 offset:3840\n"
"v_mfma_f32_16x16x32_bf16 v[148:151], a[220:223], v[92:95], v[148:151]\n"
"s_waitcnt vmcnt(14)\n"
"ds_read_b128 v[160:163], v4 offset:4352\n"
"v_mfma_f32_16x16x32_bf16 v[44:47], v[96:99], a[0:3], 0\n"
"s_cmp_lt_u32 s90, 0x8000000\n"
"s_cselect_b32 s90, 0x80000000, s90\n"
"v_mfma_f32_16x16x32_bf16 v[44:47], v[100:103], a[4:7], v[44:47]\n"
"s_waitcnt vmcnt(9)\n"
"v_mfma_f32_16x16x32_bf16 v[44:47], v[104:107], a[8:11], v[44:47]\n"
"s_waitcnt lgkmcnt(6)\n"
"buffer_load_dwordx2 v[92:93], v15, s[40:43], s90 offen offset:0 sc1\n"
"v_mfma_f32_16x16x32_bf16 v[44:47], v[108:111], a[12:15], v[44:47]\n"
"s_waitcnt vmcnt(6)\n"
"buffer_store_dwordx4 v[48:51], v18, s[76:79], s91 offen sc1\n"
"v_mfma_f32_16x16x32_bf16 v[44:47], v[112:115], a[16:19], v[44:47]\n"
"s_waitcnt lgkmcnt(3)\n"
"v_add_f32 v60, v60, v56\n"
"v_add_f32 v61, v61, v57\n"
"v_mfma_f32_16x16x32_bf16 v[44:47], v[116:119], a[20:23], v[44:47]\n"
"s_waitcnt vmcnt(6)\n"
"v_add_f32 v62, v62, v58\n"
"v_add_f32 v63, v63, v59\n"
"v_mfma_f32_16x16x32_bf16 v[44:47], v[120:123], a[24:27], v[44:47]\n"
"v_add_f32 v60, v60, v64\n"
"v_mfma_f32_16x16x32_bf16 v[48:51], v[96:99], a[28:31], 0\n"
"v_add_f32 v61, v61, v65\n"
"v_add_f32 v62, v62, v66\n"
"v_mfma_f32_16x16x32_bf16 v[48:51], v[100:103], a[32:35], v[48:51]\n"
"ds_write_addtid_b32 v44 offset:0\n"
"v_add_f32 v63, v63, v67\n"
"v_mfma_f32_16x16x32_bf16 v[48:51], v[104:107], a[36:39], v[48:51]\n"
"ds_write_addtid_b32 v45 offset:4640\n"
"v_add_f32 v60, v60, v52\n"
"v_mfma_f32_16x16x32_bf16 v[48:51], v[108:111], a[40:43], v[48:51]\n"
"ds_write_addtid_b32 v46 offset:9344\n"
"v_add_f32 v61, v61, v53\n"
"v_mfma_f32_16x16x32_bf16 v[48:51], v[112:115], a[44:47], v[48:51]\n"
"ds_write_addtid_b32 v47 offset:13984\n"
"v_add_f32 v62, v62, v54\n"
"v_mfma_f32_16x16x32_bf16 v[48:51], v[116:119], a[48:51], v[48:51]\n"
"v_add_f32 v63, v63, v55\n"
"v_mfma_f32_16x16x32_bf16 v[48:51], v[120:123], a[52:55], v[48:51]\n"
"s_add_u32 s31, s50, 0xffe80000\n"
"s_and_b32 s30, s31, 0xfbffffff\n"
"v_cvt_pk_bf16_f32 v44, v60, v61\n"
"v_mfma_f32_16x16x32_bf16 v[52:55], v[96:99], a[56:59], 0\n"
"s_waitcnt vmcnt(2)\n"
"v_cvt_pk_bf16_f32 v45, v62, v63\n"
"v_mfma_f32_16x16x32_bf16 v[56:59], v[38:41], v[68:71], 0\n"
"v_mfma_f32_16x16x32_bf16 v[56:59], v[38:41], v[76:79], v[56:59]\n"
"v_mfma_f32_16x16x32_bf16 v[56:59], v[38:41], v[84:87], v[56:59]\n"
"v_mfma_f32_16x16x32_bf16 v[60:63], v[38:41], v[72:75], 0\n"
"v_mfma_f32_16x16x32_bf16 v[60:63], v[38:41], v[80:83], v[60:63]\n"
"v_mfma_f32_16x16x32_bf16 v[60:63], v[38:41], v[88:91], v[60:63]\n"
"v_cvt_pk_bf16_f32 v64, v56, v57\n"
"v_cvt_pk_bf16_f32 v65, v58, v59\n"
"v_mfma_f32_16x16x32_bf16 v[52:55], v[100:103], a[60:63], v[52:55]\n"
"s_waitcnt lgkmcnt(4)\n"
"ds_write_addtid_b32 v48 offset:256\n"
"v_add_f32 v152, v152, v156\n"
"v_mfma_f32_16x16x32_bf16 v[52:55], v[104:107], a[64:67], v[52:55]\n"
"ds_write_addtid_b32 v49 offset:4896\n"
"v_add_f32 v153, v153, v157\n"
"v_mfma_f32_16x16x32_bf16 v[52:55], v[108:111], a[68:71], v[52:55]\n"
"ds_write_addtid_b32 v50 offset:9600\n"
"v_add_f32 v154, v154, v158\n"
"v_mfma_f32_16x16x32_bf16 v[52:55], v[112:115], a[72:75], v[52:55]\n"
"ds_write_addtid_b32 v51 offset:14240\n"
"v_add_f32 v155, v155, v159\n"
"v_mfma_f32_16x16x32_bf16 v[52:55], v[116:119], a[76:79], v[52:55]\n"
"s_waitcnt vmcnt(3)\n"
"buffer_store_dwordx2 v[0:1], v14, s[40:43], s29 offen offset:0 sc1\n"
"v_add_f32 v152, v152, v160\n"
"v_mfma_f32_16x16x32_bf16 v[52:55], v[120:123], a[80:83], v[52:55]\n"
"v_add_f32 v153, v153, v161\n"
"v_add_f32 v154, v154, v162\n"
"v_mfma_f32_16x16x32_bf16 v[48:51], v[96:99], a[84:87], 0\n"
"v_add_f32 v155, v155, v163\n"
"v_add_f32 v152, v152, v148\n"
"v_mfma_f32_16x16x32_bf16 v[48:51], v[100:103], a[88:91], v[48:51]\n"
"ds_write_addtid_b32 v52 offset:512\n"
"v_add_f32 v153, v153, v149\n"
"v_mfma_f32_16x16x32_bf16 v[48:51], v[104:107], a[92:95], v[48:51]\n"
"ds_write_addtid_b32 v53 offset:5152\n"
"v_add_f32 v154, v154, v150\n"
"v_mfma_f32_16x16x32_bf16 v[48:51], v[108:111], a[96:99], v[48:51]\n"
"ds_write_addtid_b32 v54 offset:9856\n"
"v_add_f32 v155, v155, v151\n"
"v_mfma_f32_16x16x32_bf16 v[48:51], v[112:115], a[100:103], v[48:51]\n"
"ds_write_addtid_b32 v55 offset:14496\n"
"v_cvt_pk_bf16_f32 v46, v152, v153\n"
"v_mfma_f32_16x16x32_bf16 v[48:51], v[116:119], a[104:107], v[48:51]\n"
"v_cvt_pk_bf16_f32 v47, v154, v155\n"
"v_mfma_f32_16x16x32_bf16 v[48:51], v[120:123], a[108:111], v[48:51]\n"
"buffer_store_dwordx4 v[44:47], v13, s[40:43], s30 offen sc1\n"
"v_cvt_pk_bf16_f32 v66, v60, v61\n"
"v_cvt_pk_bf16_f32 v67, v62, v63\n"
"v_mfma_f32_16x16x32_bf16 v[56:59], v[96:99], a[112:115], 0\n"
"v_mfma_f32_16x16x32_bf16 v[56:59], v[100:103], a[116:119], v[56:59]\n"
"ds_write_addtid_b32 v48 offset:768\n"
"v_mfma_f32_16x16x32_bf16 v[56:59], v[104:107], a[120:123], v[56:59]\n"
"ds_write_addtid_b32 v49 offset:5408\n"
"v_mfma_f32_16x16x32_bf16 v[56:59], v[108:111], a[124:127], v[56:59]\n"
"ds_write_addtid_b32 v50 offset:10112\n"
"v_mfma_f32_16x16x32_bf16 v[56:59], v[112:115], a[128:131], v[56:59]\n"
"ds_write_addtid_b32 v51 offset:14752\n"
"v_mfma_f32_16x16x32_bf16 v[56:59], v[116:119], a[132:135], v[56:59]\n"
"ds_bpermute_b32 v44, v21, v64\n"
"v_mfma_f32_16x16x32_bf16 v[56:59], v[120:123], a[136:139], v[56:59]\n"
"v_mfma_f32_16x16x32_bf16 v[48:51], v[96:99], a[140:143], 0\n"
"ds_bpermute_b32 v45, v21, v65\n"
"v_mfma_f32_16x16x32_bf16 v[48:51], v[100:103], a[144:147], v[48:51]\n"
"ds_write_addtid_b32 v56 offset:1024\n"
"v_mfma_f32_16x16x32_bf16 v[48:51], v[104:107], a[148:151], v[48:51]\n"
"ds_write_addtid_b32 v57 offset:5664\n"
"v_mfma_f32_16x16x32_bf16 v[48:51], v[108:111], a[152:155], v[48:51]\n"
"ds_write_addtid_b32 v58 offset:10368\n"
"v_mfma_f32_16x16x32_bf16 v[48:51], v[112:115], a[156:159], v[48:51]\n"
"ds_write_addtid_b32 v59 offset:15008\n"
"v_mfma_f32_16x16x32_bf16 v[48:51], v[116:119], a[160:163], v[48:51]\n"
"ds_bpermute_b32 v46, v21, v66\n"
"v_mfma_f32_16x16x32_bf16 v[48:51], v[120:123], a[164:167], v[48:51]\n"
"ds_bpermute_b32 v47, v21, v67\n"
"v_mfma_f32_16x16x32_bf16 v[68:71], a[168:171], v[96:99], 0\n"
"v_mfma_f32_16x16x32_bf16 v[68:71], a[172:175], v[100:103], v[68:71]\n"
"ds_write_addtid_b32 v48 offset:1280\n"
"v_mfma_f32_16x16x32_bf16 v[68:71], a[176:179], v[104:107], v[68:71]\n"
"ds_write_addtid_b32 v49 offset:5920\n"
"v_mfma_f32_16x16x32_bf16 v[68:71], a[180:183], v[108:111], v[68:71]\n"
"ds_write_addtid_b32 v50 offset:10624\n"
"v_mfma_f32_16x16x32_bf16 v[68:71], a[184:187], v[112:115], v[68:71]\n"
"ds_write_addtid_b32 v51 offset:15264\n"
"v_mfma_f32_16x16x32_bf16 v[68:71], a[188:191], v[116:119], v[68:71]\n"
"s_waitcnt lgkmcnt(4)\n"
"buffer_store_dwordx4 v[44:47], v19, s[76:79], s91 offen sc1\n"
"v_mfma_f32_16x16x32_bf16 v[68:71], a[192:195], v[120:123], v[68:71]\n"
"v_mfma_f32_16x16x32_bf16 v[72:75], a[196:199], v[96:99], 0\n"
"s_and_b32 s30, s90, 0x3ffffff\n"
"v_mfma_f32_16x16x32_bf16 v[72:75], a[200:203], v[100:103], v[72:75]\n"
"s_waitcnt lgkmcnt(0)\n"
"v_mfma_f32_16x16x32_bf16 v[72:75], a[204:207], v[104:107], v[72:75]\n"
"s_barrier\n"
"v_mfma_f32_16x16x32_bf16 v[72:75], a[208:211], v[108:111], v[72:75]\n"
"s_waitcnt vmcnt(4)\n"
"ds_read_b128 v[76:79], v2 offset:0\n"
"v_mfma_f32_16x16x32_bf16 v[72:75], a[212:215], v[112:115], v[72:75]\n"
"s_add_u32 s62, s40, s30\n"
"ds_read_b128 v[80:83], v3 offset:512\n"
"ds_read_b128 v[84:87], v4 offset:1024\n"
"v_mfma_f32_16x16x32_bf16 v[72:75], a[216:219], v[116:119], v[72:75]\n"
"s_addc_u32 s63, s41, 0\n"
"ds_read_b128 v[88:91], v2 offset:256\n"
"ds_read_b128 v[96:99], v3 offset:768\n"
"v_mfma_f32_16x16x32_bf16 v[72:75], a[220:223], v[120:123], v[72:75]\n"
"ds_read_b128 v[100:103], v4 offset:1280\n"
"v_cmp_ne_u64 vcc, s[84:85], v[92:93]\n"
"s_cbranch_vccnz .LJ_4t1k7s\n"
".LJ_4t1k7r:\n"
"global_load_dwordx4 v[46:49], v16, s[62:63] offset:-4096 sc1\n"
"s_waitcnt lgkmcnt(3)\n"
"v_add_f32 v76, v76, v80\n"
"v_add_f32 v77, v77, v81\n"
"v_add_f32 v78, v78, v82\n"
"v_add_f32 v79, v79, v83\n"
"v_add_f32 v76, v76, v84\n"
"v_add_f32 v77, v77, v85\n"
"v_add_f32 v78, v78, v86\n"
"v_add_f32 v79, v79, v87\n"
"v_add_f32 v76, v76, v68\n"
"v_add_f32 v77, v77, v69\n"
"v_add_f32 v78, v78, v70\n"
"global_load_dwordx4 v[50:53], v16, s[62:63] offset:-3072 sc1\n"
"v_add_f32 v79, v79, v71\n"
"s_add_u32 s30, s50, 0x0\n"
"s_and_b32 s29, s30, 0xfbffffff\n"
"v_cvt_pk_bf16_f32 v84, v76, v77\n"
"v_cvt_pk_bf16_f32 v85, v78, v79\n"
"s_waitcnt lgkmcnt(0)\n"
"v_add_f32 v88, v88, v96\n"
"v_add_f32 v89, v89, v97\n"
"v_add_f32 v90, v90, v98\n"
"v_add_f32 v91, v91, v99\n"
"s_waitcnt vmcnt(3)\n"
"v_add_f32 v88, v88, v100\n"
"v_add_f32 v89, v89, v101\n"
"v_add_f32 v90, v90, v102\n"
"v_add_f32 v91, v91, v103\n"
"v_add_f32 v88, v88, v72\n"
"v_add_f32 v89, v89, v73\n"
"v_add_f32 v90, v90, v74\n"
"v_add_f32 v91, v91, v75\n"
"v_cvt_pk_bf16_f32 v86, v88, v89\n"
"v_cvt_pk_bf16_f32 v87, v90, v91\n"
"buffer_store_dwordx4 v[84:87], v13, s[40:43], s29 offen sc1\n"
"buffer_store_dwordx2 v[0:1], v14, s[40:43], s31 offen offset:0 sc1\n"
"global_load_dwordx4 v[54:57], v16, s[62:63] offset:-2048 sc1\n"
"global_load_dwordx4 v[58:61], v16, s[62:63] offset:-1024 sc1\n"
"global_load_dwordx4 v[62:65], v17, s[62:63] offset:0 sc1\n"
"global_load_dwordx4 v[70:73], v16, s[62:63] offset:0 sc1\n"
"global_load_dwordx4 v[74:77], v16, s[62:63] offset:1024 sc1\n"
"global_load_dwordx4 v[78:81], v16, s[62:63] offset:2048 sc1\n"
"global_load_dwordx4 v[82:85], v16, s[62:63] offset:3072 sc1\n"
"s_mov_b32 s28, s90\n"
"s_waitcnt vmcnt(8)\n"
"buffer_store_dwordx2 v[0:1], v14, s[40:43], s30 offen offset:0 sc1\n"
"global_load_dwordx4 v[86:89], v17, s[62:63] offset:1024 sc1\n"
"v_mov_b32 v44, v26\n"
"v_mov_b32 v45, v27\n"
"v_mov_b32 v66, v28\n"
"v_mov_b32 v67, v29\n"
"s_waitcnt vmcnt(6)\n"
"v_mfma_f32_16x16x32_bf16 v[132:135], v[38:41], v[44:47], 0\n"
"s_cmp_ge_i32 s28, s80\n"
"s_cbranch_scc1 .LJ_wt1k7s\n"
".LJ_wt1k7r:\n"
"v_mfma_f32_16x16x32_bf16 v[132:135], v[38:41], v[52:55], v[132:135]\n"
"s_lshr_b32 s91, s28, s81\n"
"v_mfma_f32_16x16x32_bf16 v[132:135], v[38:41], v[60:63], v[132:135]\n"
"s_bitcmp1_b32 s91, 15\n"
"s_cselect_b32 s31, s83, s82\n"
"v_mfma_f32_16x16x32_bf16 v[128:131], v[38:41], v[48:51], 0\n"
"s_add_u32 s91, s91, s31\n"
"v_mfma_f32_16x16x32_bf16 v[128:131], v[38:41], v[56:59], v[128:131]\n"
"v_mfma_f32_16x16x32_bf16 v[128:131], v[38:41], v[64:67], v[128:131]\n"
"v_cvt_pk_bf16_f32 v144, v132, v133\n"
"v_cvt_pk_bf16_f32 v145, v134, v135\n"
"s_nop 5\n"
"v_cvt_pk_bf16_f32 v146, v128, v129\n"
"v_cvt_pk_bf16_f32 v147, v130, v131\n"
"v_mov_b32 v68, v30\n"
"v_mov_b32 v69, v31\n"
"v_mov_b32 v90, v32\n"
"ds_bpermute_b32 v48, v21, v144\n"
"ds_bpermute_b32 v49, v21, v145\n"
"v_mov_b32 v91, v33\n"
"ds_bpermute_b32 v50, v21, v146\n"
"ds_bpermute_b32 v51, v21, v147\n"
"s_add_u32 s90, s50, 0xfff80000\n"
"s_cmp_lt_u32 s90, 0x8000000\n"
"s_cselect_b32 s90, 0x80000000, s90\n"
"s_waitcnt lgkmcnt(0)\n"
"buffer_load_dwordx2 v[92:93], v15, s[40:43], s90 offen offset:0 sc1\n"
"buffer_store_dwordx4 v[48:51], v18, s[76:79], s91 offen sc1\n"
"s_waitcnt vmcnt(2)\n"
"v_mfma_f32_16x16x32_bf16 v[56:59], v[38:41], v[68:71], 0\n"
"v_mfma_f32_16x16x32_bf16 v[56:59], v[38:41], v[76:79], v[56:59]\n"
"v_mfma_f32_16x16x32_bf16 v[56:59], v[38:41], v[84:87], v[56:59]\n"
"v_mfma_f32_16x16x32_bf16 v[60:63], v[38:41], v[72:75], 0\n"
"v_mfma_f32_16x16x32_bf16 v[60:63], v[38:41], v[80:83], v[60:63]\n"
"v_mfma_f32_16x16x32_bf16 v[60:63], v[38:41], v[88:91], v[60:63]\n"
"v_cvt_pk_bf16_f32 v64, v56, v57\n"
"v_cvt_pk_bf16_f32 v65, v58, v59\n"
"s_nop 5\n"
"v_cvt_pk_bf16_f32 v66, v60, v61\n"
"v_cvt_pk_bf16_f32 v67, v62, v63\n"
"ds_bpermute_b32 v44, v21, v64\n"
"ds_bpermute_b32 v45, v21, v65\n"
"ds_bpermute_b32 v46, v21, v66\n"
"ds_bpermute_b32 v47, v21, v67\n"
"s_waitcnt lgkmcnt(0)\n"
"buffer_store_dwordx4 v[44:47], v19, s[76:79], s91 offen sc1\n"
"s_and_b32 s30, s90, 0x3ffffff\n"
"s_waitcnt vmcnt(2)\n"
"s_add_u32 s62, s40, s30\n"
"s_addc_u32 s63, s41, 0\n"
"v_cmp_ne_u64 vcc, s[84:85], v[92:93]\n"
"s_cbranch_vccnz .LJ_4t1k0s\n"
".LJ_4t1k0r:\n"
"global_load_dwordx4 v[46:49], v16, s[62:63] offset:-4096 sc1\n"
"global_load_dwordx4 v[50:53], v16, s[62:63] offset:-3072 sc1\n"
"global_load_dwordx4 v[54:57], v16, s[62:63] offset:-2048 sc1\n"
"global_load_dwordx4 v[58:61], v16, s[62:63] offset:-1024 sc1\n"
"global_load_dwordx4 v[62:65], v17, s[62:63] offset:0 sc1\n"
"global_load_dwordx4 v[70:73], v16, s[62:63] offset:0 sc1\n"
"global_load_dwordx4 v[74:77], v16, s[62:63] offset:1024 sc1\n"
"global_load_dwordx4 v[78:81], v16, s[62:63] offset:2048 sc1\n"
"global_load_dwordx4 v[82:85], v16, s[62:63] offset:3072 sc1\n"
"s_mov_b32 s28, s90\n"
"global_load_dwordx4 v[86:89], v17, s[62:63] offset:1024 sc1\n"
"v_mov_b32 v44, v26\n"
"v_mov_b32 v45, v27\n"
"v_mov_b32 v66, v28\n"
"v_mov_b32 v67, v29\n"
"s_waitcnt vmcnt(5)\n"
"v_mfma_f32_16x16x32_bf16 v[132:135], v[38:41], v[44:47], 0\n"
"s_cmp_ge_i32 s28, s80\n"
"s_cbranch_scc1 .LJ_wt1k0s\n"
".LJ_wt1k0r:\n"
"v_mfma_f32_16x16x32_bf16 v[132:135], v[38:41], v[52:55], v[132:135]\n"
"s_lshr_b32 s91, s28, s81\n"
"v_mfma_f32_16x16x32_bf16 v[132:135], v[38:41], v[60:63], v[132:135]\n"
"s_bitcmp1_b32 s91, 15\n"
"s_cselect_b32 s31, s83, s82\n"
"v_mfma_f32_16x16x32_bf16 v[128:131], v[38:41], v[48:51], 0\n"
"s_add_u32 s91, s91, s31\n"
"v_mfma_f32_16x16x32_bf16 v[128:131], v[38:41], v[56:59], v[128:131]\n"
"v_mfma_f32_16x16x32_bf16 v[128:131], v[38:41], v[64:67], v[128:131]\n"
"v_cvt_pk_bf16_f32 v144, v132, v133\n"
"v_cvt_pk_bf16_f32 v145, v134, v135\n"
"s_nop 5\n"
"v_cvt_pk_bf16_f32 v146, v128, v129\n"
"v_cvt_pk_bf16_f32 v147, v130, v131\n"
"v_mov_b32 v68, v30\n"
"v_mov_b32 v69, v31\n"
"v_mov_b32 v90, v32\n"
"ds_bpermute_b32 v48, v21, v144\n"
"ds_bpermute_b32 v49, v21, v145\n"
"v_mov_b32 v91, v33\n"
"ds_bpermute_b32 v50, v21, v146\n"
"ds_bpermute_b32 v51, v21, v147\n"
"s_waitcnt lgkmcnt(0)\n"
"buffer_store_dwordx4 v[48:51], v18, s[76:79], s91 offen sc1\n"
"s_waitcnt vmcnt(1)\n"
"v_mfma_f32_16x16x32_bf16 v[56:59], v[38:41], v[68:71], 0\n"
"v_mfma_f32_16x16x32_bf16 v[56:59], v[38:41], v[76:79], v[56:59]\n"
"v_mfma_f32_16x16x32_bf16 v[56:59], v[38:41], v[84:87], v[56:59]\n"
"v_mfma_f32_16x16x32_bf16 v[60:63], v[38:41], v[72:75], 0\n"
"v_mfma_f32_16x16x32_bf16 v[60:63], v[38:41], v[80:83], v[60:63]\n"
"v_mfma_f32_16x16x32_bf16 v[60:63], v[38:41], v[88:91], v[60:63]\n"
"v_cvt_pk_bf16_f32 v64, v56, v57\n"
"v_cvt_pk_bf16_f32 v65, v58, v59\n"
"s_nop 5\n"
"v_cvt_pk_bf16_f32 v66, v60, v61\n"
"v_cvt_pk_bf16_f32 v67, v62, v63\n"
"ds_bpermute_b32 v44, v21, v64\n"
"ds_bpermute_b32 v45, v21, v65\n"
"ds_bpermute_b32 v46, v21, v66\n"
"ds_bpermute_b32 v47, v21, v67\n"
"s_waitcnt lgkmcnt(0)\n"
"buffer_store_dwordx4 v[44:47], v19, s[76:79], s91 offen sc1\n"
"s_branch .LJ_end\n"
".LJ_t2:\n"
"v_mfma_f32_16x16x32_bf16 v[56:59], v[100:103], a[116:119], v[56:59]\n"
"ds_write_addtid_b32 v48 offset:768\n"
"v_mfma_f32_16x16x32_bf16 v[56:59], v[104:107], a[120:123], v[56:59]\n"
"ds_write_addtid_b32 v49 offset:5408\n"
"v_mfma_f32_16x16x32_bf16 v[56:59], v[108:111], a[124:127], v[56:59]\n"
"ds_write_addtid_b32 v50 offset:10112\n"
"v_mfma_f32_16x16x32_bf16 v[56:59], v[112:115], a[128:131], v[56:59]\n"
"ds_write_addtid_b32 v51 offset:14752\n"
"v_mfma_f32_16x16x32_bf16 v[56:59], v[116:119], a[132:135], v[56:59]\n"
"ds_bpermute_b32 v44, v21, v64\n"
"v_mfma_f32_16x16x32_bf16 v[56:59], v[120:123], a[136:139], v[56:59]\n"
"v_mfma_f32_16x16x32_bf16 v[48:51], v[96:99], a[140:143], 0\n"
"ds_bpermute_b32 v45, v21, v65\n"
"v_mfma_f32_16x16x32_bf16 v[48:51], v[100:103], a[144:147], v[48:51]\n"
"ds_write_addtid_b32 v56 offset:1024\n"
"v_mfma_f32_16x16x32_bf16 v[48:51], v[104:107], a[148:151], v[48:51]\n"
"ds_write_addtid_b32 v57 offset:5664\n"
"v_mfma_f32_16x16x32_bf16 v[48:51], v[108:111], a[152:155], v[48:51]\n"
"ds_write_addtid_b32 v58 offset:10368\n"
"v_mfma_f32_16x16x32_bf16 v[48:51], v[112:115], a[156:159], v[48:51]\n"
"ds_write_addtid_b32 v59 offset:15008\n"
"v_mfma_f32_16x16x32_bf16 v[48:51], v[116:119], a[160:163], v[48:51]\n"
"ds_bpermute_b32 v46, v21, v66\n"
"v_mfma_f32_16x16x32_bf16 v[48:51], v[120:123], a[164:167], v[48:51]\n"
"ds_bpermute_b32 v47, v21, v67\n"
"v_mfma_f32_16x16x32_bf16 v[68:71], a[168:171], v[96:99], 0\n"
"v_mfma_f32_16x16x32_bf16 v[68:71], a[172:175], v[100:103], v[68:71]\n"
"ds_write_addtid_b32 v48 offset:1280\n"
"v_mfma_f32_16x16x32_bf16 v[68:71], a[176:179], v[104:107], v[68:71]\n"
"ds_write_addtid_b32 v49 offset:5920\n"
"v_mfma_f32_16x16x32_bf16 v[68:71], a[180:183], v[108:111], v[68:71]\n"
"ds_write_addtid_b32 v50 offset:10624\n"
"v_mfma_f32_16x16x32_bf16 v[68:71], a[184:187], v[112:115], v[68:71]\n"
"ds_write_addtid_b32 v51 offset:15264\n"
"v_mfma_f32_16x16x32_bf16 v[68:71], a[188:191], v[116:119], v[68:71]\n"
"s_waitcnt lgkmcnt(4)\n"
"buffer_store_dwordx4 v[44:47], v19, s[76:79], s91 offen sc1\n"
"v_mfma_f32_16x16x32_bf16 v[68:71], a[192:195], v[120:123], v[68:71]\n"
"v_mfma_f32_16x16x32_bf16 v[72:75], a[196:199], v[96:99], 0\n"
"s_and_b32 s30, s90, 0x3ffffff\n"
"v_mfma_f32_16x16x32_bf16 v[72:75], a[200:203], v[100:103], v[72:75]\n"
"s_waitcnt lgkmcnt(0)\n"
"v_mfma_f32_16x16x32_bf16 v[72:75], a[204:207], v[104:107], v[72:75]\n"
"s_barrier\n"
"v_mfma_f32_16x16x32_bf16 v[72:75], a[208:211], v[108:111], v[72:75]\n"
"s_waitcnt vmcnt(5)\n"
"ds_read_b128 v[76:79], v2 offset:0\n"
"v_mfma_f32_16x16x32_bf16 v[72:75], a[212:215], v[112:115], v[72:75]\n"
"s_add_u32 s62, s40, s30\n"
"ds_read_b128 v[80:83], v3 offset:512\n"
"ds_read_b128 v[84:87], v4 offset:1024\n"
"v_mfma_f32_16x16x32_bf16 v[72:75], a[216:219], v[116:119], v[72:75]\n"
"s_addc_u32 s63, s41, 0\n"
"ds_read_b128 v[88:91], v2 offset:256\n"
"ds_read_b128 v[96:99], v3 offset:768\n"
"v_mfma_f32_16x16x32_bf16 v[72:75], a[220:223], v[120:123], v[72:75]\n"
"s_waitcnt vmcnt(10)\n"
"ds_read_b128 v[100:103], v4 offset:1280\n"
"v_mfma_f32_16x16x32_bf16 v[104:107], v[124:127], a[0:3], 0\n"
"v_cmp_ne_u64 vcc, s[84:85], v[92:93]\n"
"v_mfma_f32_16x16x32_bf16 v[104:107], v[128:131], a[4:7], v[104:107]\n"
"s_waitcnt vmcnt(8)\n"
"v_mfma_f32_16x16x32_bf16 v[104:107], v[132:135], a[8:11], v[104:107]\n"
"s_cbranch_vccnz .LJ_4t2k7s\n"
".LJ_4t2k7r:\n"
"v_mfma_f32_16x16x32_bf16 v[104:107], v[136:139], a[12:15], v[104:107]\n"
"s_waitcnt vmcnt(6)\n"
"global_load_dwordx4 v[46:49], v16, s[62:63] offset:-4096 sc1\n"
"v_mfma_f32_16x16x32_bf16 v[104:107], v[140:143], a[16:19], v[104:107]\n"
"s_waitcnt lgkmcnt(3)\n"
"v_add_f32 v76, v76, v80\n"
"v_add_f32 v77, v77, v81\n"
"v_mfma_f32_16x16x32_bf16 v[104:107], a[224:227], a[20:23], v[104:107]\n"
"s_waitcnt vmcnt(4)\n"
"v_add_f32 v78, v78, v82\n"
"v_add_f32 v79, v79, v83\n"
"v_mfma_f32_16x16x32_bf16 v[104:107], a[228:231], a[24:27], v[104:107]\n"
"v_add_f32 v76, v76, v84\n"
"v_mfma_f32_16x16x32_bf16 v[80:83], v[124:127], a[28:31], 0\n"
"v_add_f32 v77, v77, v85\n"
"v_add_f32 v78, v78, v86\n"
"v_mfma_f32_16x16x32_bf16 v[80:83], v[128:131], a[32:35], v[80:83]\n"
"ds_write_addtid_b32 v104 offset:1536\n"
"v_add_f32 v79, v79, v87\n"
"v_mfma_f32_16x16x32_bf16 v[80:83], v[132:135], a[36:39], v[80:83]\n"
"ds_write_addtid_b32 v105 offset:6176\n"
"v_add_f32 v76, v76, v68\n"
"v_mfma_f32_16x16x32_bf16 v[80:83], v[136:139], a[40:43], v[80:83]\n"
"ds_write_addtid_b32 v106 offset:10880\n"
"v_add_f32 v77, v77, v69\n"
"v_mfma_f32_16x16x32_bf16 v[80:83], v[140:143], a[44:47], v[80:83]\n"
"ds_write_addtid_b32 v107 offset:15520\n"
"v_add_f32 v78, v78, v70\n"
"v_mfma_f32_16x16x32_bf16 v[80:83], a[224:227], a[48:51], v[80:83]\n"
"global_load_dwordx4 v[50:53], v16, s[62:63] offset:-3072 sc1\n"
"v_add_f32 v79, v79, v71\n"
"v_mfma_f32_16x16x32_bf16 v[80:83], a[228:231], a[52:55], v[80:83]\n"
"s_add_u32 s30, s50, 0xffe80000\n"
"s_and_b32 s29, s30, 0xfbffffff\n"
"v_cvt_pk_bf16_f32 v84, v76, v77\n"
"v_mfma_f32_16x16x32_bf16 v[68:71], v[124:127], a[56:59], 0\n"
"v_cvt_pk_bf16_f32 v85, v78, v79\n"
"v_mfma_f32_16x16x32_bf16 v[68:71], v[128:131], a[60:63], v[68:71]\n"
"s_waitcnt lgkmcnt(4)\n"
"ds_write_addtid_b32 v80 offset:1792\n"
"v_add_f32 v88, v88, v96\n"
"v_mfma_f32_16x16x32_bf16 v[68:71], v[132:135], a[64:67], v[68:71]\n"
"ds_write_addtid_b32 v81 offset:6432\n"
"v_add_f32 v89, v89, v97\n"
"v_mfma_f32_16x16x32_bf16 v[68:71], v[136:139], a[68:71], v[68:71]\n"
"ds_write_addtid_b32 v82 offset:11136\n"
"v_add_f32 v90, v90, v98\n"
"v_mfma_f32_16x16x32_bf16 v[68:71], v[140:143], a[72:75], v[68:71]\n"
"ds_write_addtid_b32 v83 offset:15776\n"
"v_add_f32 v91, v91, v99\n"
"v_mfma_f32_16x16x32_bf16 v[68:71], a[224:227], a[76:79], v[68:71]\n"
"s_waitcnt vmcnt(3)\n"
"v_add_f32 v88, v88, v100\n"
"v_add_f32 v89, v89, v101\n"
"v_mfma_f32_16x16x32_bf16 v[68:71], a[228:231], a[80:83], v[68:71]\n"
"v_add_f32 v90, v90, v102\n"
"v_add_f32 v91, v91, v103\n"
"v_mfma_f32_16x16x32_bf16 v[76:79], v[124:127], a[84:87], 0\n"
"v_add_f32 v88, v88, v72\n"
"v_add_f32 v89, v89, v73\n"
"v_mfma_f32_16x16x32_bf16 v[76:79], v[128:131], a[88:91], v[76:79]\n"
"ds_write_addtid_b32 v68 offset:2048\n"
"v_add_f32 v90, v90, v74\n"
"v_mfma_f32_16x16x32_bf16 v[76:79], v[132:135], a[92:95], v[76:79]\n"
"ds_write_addtid_b32 v69 offset:6688\n"
"v_add_f32 v91, v91, v75\n"
"v_mfma_f32_16x16x32_bf16 v[76:79], v[136:139], a[96:99], v[76:79]\n"
"ds_write_addtid_b32 v70 offset:11392\n"
"v_cvt_pk_bf16_f32 v86, v88, v89\n"
"v_mfma_f32_16x16x32_bf16 v[76:79], v[140:143], a[100:103], v[76:79]\n"
"ds_write_addtid_b32 v71 offset:16032\n"
"v_cvt_pk_bf16_f32 v87, v90, v91\n"
"v_mfma_f32_16x16x32_bf16 v[76:79], a[224:227], a[104:107], v[76:79]\n"
"buffer_store_dwordx4 v[84:87], v13, s[40:43], s29 offen sc1\n"
"v_mfma_f32_16x16x32_bf16 v[76:79], a[228:231], a[108:111], v[76:79]\n"
"buffer_store_dwordx2 v[0:1], v14, s[40:43], s31 offen offset:0 sc1\n"
"v_mfma_f32_16x16x32_bf16 v[68:71], v[124:127], a[112:115], 0\n"
"v_mfma_f32_16x16x32_bf16 v[68:71], v[128:131], a[116:119], v[68:71]\n"
"ds_write_addtid_b32 v76 offset:2304\n"
"global_load_dwordx4 v[54:57], v16, s[62:63] offset:-2048 sc1\n"
"v_mfma_f32_16x16x32_bf16 v[68:71], v[132:135], a[120:123], v[68:71]\n"
"ds_write_addtid_b32 v77 offset:6944\n"
"v_mfma_f32_16x16x32_bf16 v[68:71], v[136:139], a[124:127], v[68:71]\n"
"ds_write_addtid_b32 v78 offset:11648\n"
"v_mfma_f32_16x16x32_bf16 v[68:71], v[140:143], a[128:131], v[68:71]\n"
"ds_write_addtid_b32 v79 offset:16288\n"
"v_mfma_f32_16x16x32_bf16 v[68:71], a[224:227], a[132:135], v[68:71]\n"
"v_mfma_f32_16x16x32_bf16 v[68:71], a[228:231], a[136:139], v[68:71]\n"
"v_mfma_f32_16x16x32_bf16 v[112:115], v[124:127], a[140:143], 0\n"
"global_load_dwordx4 v[58:61], v16, s[62:63] offset:-1024 sc1\n"
"v_mfma_f32_16x16x32_bf16 v[112:115], v[128:131], a[144:147], v[112:115]\n"
"ds_write_addtid_b32 v68 offset:2560\n"
"v_mfma_f32_16x16x32_bf16 v[112:115], v[132:135], a[148:151], v[112:115]\n"
"ds_write_addtid_b32 v69 offset:7200\n"
"v_mfma_f32_16x16x32_bf16 v[112:115], v[136:139], a[152:155], v[112:115]\n"
"ds_write_addtid_b32 v70 offset:11904\n"
"v_mfma_f32_16x16x32_bf16 v[112:115], v[140:143], a[156:159], v[112:115]\n"
"ds_write_addtid_b32 v71 offset:16544\n"
"v_mfma_f32_16x16x32_bf16 v[112:115], a[224:227], a[160:163], v[112:115]\n"
"global_load_dwordx4 v[62:65], v17, s[62:63] offset:0 sc1\n"
"v_mfma_f32_16x16x32_bf16 v[112:115], a[228:231], a[164:167], v[112:115]\n"
"v_mfma_f32_16x16x32_bf16 v[144:147], a[168:171], v[124:127], 0\n"
"v_mfma_f32_16x16x32_bf16 v[144:147], a[172:175], v[128:131], v[144:147]\n"
"ds_write_addtid_b32 v112 offset:2816\n"
"v_mfma_f32_16x16x32_bf16 v[144:147], a[176:179], v[132:135], v[144:147]\n"
"ds_write_addtid_b32 v113 offset:7456\n"
"v_mfma_f32_16x16x32_bf16 v[144:147], a[180:183], v[136:139], v[144:147]\n"
"ds_write_addtid_b32 v114 offset:12160\n"
"global_load_dwordx4 v[70:73], v16, s[62:63] offset:0 sc1\n"
"v_mfma_f32_16x16x32_bf16 v[144:147], a[184:187], v[140:143], v[144:147]\n"
"ds_write_addtid_b32 v115 offset:16800\n"
"v_mfma_f32_16x16x32_bf16 v[144:147], a[188:191], a[224:227], v[144:147]\n"
"v_mfma_f32_16x16x32_bf16 v[144:147], a[192:195], a[228:231], v[144:147]\n"
"v_mfma_f32_16x16x32_bf16 v[148:151], a[196:199], v[124:127], 0\n"
"v_mfma_f32_16x16x32_bf16 v[148:151], a[200:203], v[128:131], v[148:151]\n"
"s_waitcnt lgkmcnt(0)\n"
"global_load_dwordx4 v[74:77], v16, s[62:63] offset:1024 sc1\n"
"v_mfma_f32_16x16x32_bf16 v[148:151], a[204:207], v[132:135], v[148:151]\n"
"s_barrier\n"
"v_mfma_f32_16x16x32_bf16 v[148:151], a[208:211], v[136:139], v[148:151]\n"
"ds_read_b128 v[124:127], v2 offset:1536\n"
"v_mfma_f32_16x16x32_bf16 v[148:151], a[212:215], v[140:143], v[148:151]\n"
"ds_read_b128 v[128:131], v3 offset:2048\n"
"ds_read_b128 v[132:135], v4 offset:2560\n"
"v_mfma_f32_16x16x32_bf16 v[148:151], a[216:219], a[224:227], v[148:151]\n"
"ds_read_b128 v[136:139], v2 offset:1792\n"
"ds_read_b128 v[140:143], v3 offset:2304\n"
"v_mfma_f32_16x16x32_bf16 v[148:151], a[220:223], a[228:231], v[148:151]\n"
"ds_read_b128 v[152:155], v4 offset:2816\n"
"global_load_dwordx4 v[78:81], v16, s[62:63] offset:2048 sc1\n"
"s_waitcnt lgkmcnt(3)\n"
"v_add_f32 v124, v124, v128\n"
"v_add_f32 v125, v125, v129\n"
"v_add_f32 v126, v126, v130\n"
"v_add_f32 v127, v127, v131\n"
"v_add_f32 v124, v124, v132\n"
"v_add_f32 v125, v125, v133\n"
"v_add_f32 v126, v126, v134\n"
"v_add_f32 v127, v127, v135\n"
"v_add_f32 v124, v124, v144\n"
"v_add_f32 v125, v125, v145\n"
"v_add_f32 v126, v126, v146\n"
"global_load_dwordx4 v[82:85], v16, s[62:63] offset:3072 sc1\n"
"v_add_f32 v127, v127, v147\n"
"s_add_u32 s29, s50, 0x0\n"
"s_and_b32 s31, s29, 0xfbffffff\n"
"v_cvt_pk_bf16_f32 v132, v124, v125\n"
"v_cvt_pk_bf16_f32 v133, v126, v127\n"
"s_waitcnt lgkmcnt(0)\n"
"v_add_f32 v136, v136, v140\n"
"s_mov_b32 s28, s90\n"
"v_add_f32 v137, v137, v141\n"
"v_add_f32 v138, v138, v142\n"
"s_waitcnt vmcnt(8)\n"
"buffer_store_dwordx2 v[0:1], v14, s[40:43], s30 offen offset:0 sc1\n"
"v_add_f32 v139, v139, v143\n"
"v_add_f32 v136, v136, v152\n"
"v_add_f32 v137, v137, v153\n"
"v_add_f32 v138, v138, v154\n"
"v_add_f32 v139, v139, v155\n"
"v_add_f32 v136, v136, v148\n"
"v_add_f32 v137, v137, v149\n"
"v_add_f32 v138, v138, v150\n"
"v_add_f32 v139, v139, v151\n"
"v_cvt_pk_bf16_f32 v134, v136, v137\n"
"v_cvt_pk_bf16_f32 v135, v138, v139\n"
"buffer_store_dwordx4 v[132:135], v13, s[40:43], s31 offen sc1\n"
"global_load_dwordx4 v[86:89], v17, s[62:63] offset:1024 sc1\n"
"v_mov_b32 v44, v26\n"
"v_mov_b32 v45, v27\n"
"v_mov_b32 v66, v28\n"
"v_mov_b32 v67, v29\n"
"s_waitcnt vmcnt(7)\n"
"v_mfma_f32_16x16x32_bf16 v[132:135], v[38:41], v[44:47], 0\n"
"s_cmp_ge_i32 s28, s80\n"
"s_cbranch_scc1 .LJ_wt2k7s\n"
".LJ_wt2k7r:\n"
"v_mfma_f32_16x16x32_bf16 v[132:135], v[38:41], v[52:55], v[132:135]\n"
"s_lshr_b32 s91, s28, s81\n"
"v_mfma_f32_16x16x32_bf16 v[132:135], v[38:41], v[60:63], v[132:135]\n"
"s_bitcmp1_b32 s91, 15\n"
"s_cselect_b32 s31, s83, s82\n"
"v_mfma_f32_16x16x32_bf16 v[128:131], v[38:41], v[48:51], 0\n"
"s_add_u32 s91, s91, s31\n"
"v_mfma_f32_16x16x32_bf16 v[128:131], v[38:41], v[56:59], v[128:131]\n"
"v_mfma_f32_16x16x32_bf16 v[128:131], v[38:41], v[64:67], v[128:131]\n"
"v_cvt_pk_bf16_f32 v144, v132, v133\n"
"v_cvt_pk_bf16_f32 v145, v134, v135\n"
"s_nop 5\n"
"v_cvt_pk_bf16_f32 v146, v128, v129\n"
"v_cvt_pk_bf16_f32 v147, v130, v131\n"
"v_mov_b32 v68, v30\n"
"v_mov_b32 v69, v31\n"
"v_mov_b32 v90, v32\n"
"ds_bpermute_b32 v48, v21, v144\n"
"ds_bpermute_b32 v49, v21, v145\n"
"v_mov_b32 v91, v33\n"
"ds_bpermute_b32 v50, v21, v146\n"
"ds_bpermute_b32 v51, v21, v147\n"
"s_add_u32 s90, s50, 0xffe00000\n"
"s_cmp_lt_u32 s90, 0x8000000\n"
"s_cselect_b32 s90, 0x80000000, s90\n"
"s_waitcnt lgkmcnt(0)\n"
"buffer_load_dwordx2 v[92:93], v15, s[40:43], s90 offen offset:0 sc1\n"
"buffer_store_dwordx4 v[48:51], v18, s[76:79], s91 offen sc1\n"
"s_waitcnt vmcnt(2)\n"
"v_mfma_f32_16x16x32_bf16 v[56:59], v[38:41], v[68:71], 0\n"
"v_mfma_f32_16x16x32_bf16 v[56:59], v[38:41], v[76:79], v[56:59]\n"
"v_mfma_f32_16x16x32_bf16 v[56:59], v[38:41], v[84:87], v[56:59]\n"
"v_mfma_f32_16x16x32_bf16 v[60:63], v[38:41], v[72:75], 0\n"
"v_mfma_f32_16x16x32_bf16 v[60:63], v[38:41], v[80:83], v[60:63]\n"
"v_mfma_f32_16x16x32_bf16 v[60:63], v[38:41], v[88:91], v[60:63]\n"
"v_cvt_pk_bf16_f32 v64, v56, v57\n"
"v_cvt_pk_bf16_f32 v65, v58, v59\n"
"s_waitcnt vmcnt(3)\n"
"buffer_store_dwordx2 v[0:1], v14, s[40:43], s29 offen offset:0 sc1\n"
"s_nop 3\n"
"v_cvt_pk_bf16_f32 v66, v60, v61\n"
"v_cvt_pk_bf16_f32 v67, v62, v63\n"
"ds_bpermute_b32 v44, v21, v64\n"
"ds_bpermute_b32 v45, v21, v65\n"
"ds_bpermute_b32 v46, v21, v66\n"
"ds_bpermute_b32 v47, v21, v67\n"
"s_waitcnt lgkmcnt(0)\n"
"buffer_store_dwordx4 v[44:47], v19, s[76:79], s91 offen sc1\n"
"s_and_b32 s30, s90, 0x3ffffff\n"
"s_waitcnt vmcnt(3)\n"
"s_add_u32 s62, s40, s30\n"
"s_addc_u32 s63, s41, 0\n"
"v_cmp_ne_u64 vcc, s[84:85], v[92:93]\n"
"s_cbranch_vccnz .LJ_4t2k0s\n"
".LJ_4t2k0r:\n"
"global_load_dwordx4 v[46:49], v16, s[62:63] offset:-4096 sc1\n"
"global_load_dwordx4 v[50:53], v16, s[62:63] offset:-3072 sc1\n"
"global_load_dwordx4 v[54:57], v16, s[62:63] offset:-2048 sc1\n"
"global_load_dwordx4 v[58:61], v16, s[62:63] offset:-1024 sc1\n"
"global_load_dwordx4 v[62:65], v17, s[62:63] offset:0 sc1\n"
"global_load_dwordx4 v[70:73], v16, s[62:63] offset:0 sc1\n"
"global_load_dwordx4 v[74:77], v16, s[62:63] offset:1024 sc1\n"
"global_load_dwordx4 v[78:81], v16, s[62:63] offset:2048 sc1\n"
"global_load_dwordx4 v[82:85], v16, s[62:63] offset:3072 sc1\n"
"s_mov_b32 s28, s90\n"
"global_load_dwordx4 v[86:89], v17, s[62:63] offset:1024 sc1\n"
"v_mov_b32 v44, v26\n"
"v_mov_b32 v45, v27\n"
"v_mov_b32 v66, v28\n"
"v_mov_b32 v67, v29\n"
"s_waitcnt vmcnt(5)\n"
"v_mfma_f32_16x16x32_bf16 v[132:135], v[38:41], v[44:47], 0\n"
"s_cmp_ge_i32 s28, s80\n"
"s_cbranch_scc1 .LJ_wt2k0s\n"
".LJ_wt2k0r:\n"
"v_mfma_f32_16x16x32_bf16 v[132:135], v[38:41], v[52:55], v[132:135]\n"
"s_lshr_b32 s91, s28, s81\n"
"v_mfma_f32_16x16x32_bf16 v[132:135], v[38:41], v[60:63], v[132:135]\n"
"s_bitcmp1_b32 s91, 15\n"
"s_cselect_b32 s31, s83, s82\n"
"v_mfma_f32_16x16x32_bf16 v[128:131], v[38:41], v[48:51], 0\n"
"s_add_u32 s91, s91, s31\n"
"v_mfma_f32_16x16x32_bf16 v[128:131], v[38:41], v[56:59], v[128:131]\n"
"v_mfma_f32_16x16x32_bf16 v[128:131], v[38:41], v[64:67], v[128:131]\n"
"v_cvt_pk_bf16_f32 v144, v132, v133\n"
"v_cvt_pk_bf16_f32 v145, v134, v135\n"
"s_nop 5\n"
"v_cvt_pk_bf16_f32 v146, v128, v129\n"
"v_cvt_pk_bf16_f32 v147, v130, v131\n"
"v_mov_b32 v68, v30\n"
"v_mov_b32 v69, v31\n"
"v_mov_b32 v90, v32\n"
"ds_bpermute_b32 v48, v21, v144\n"
"ds_bpermute_b32 v49, v21, v145\n"
"v_mov_b32 v91, v33\n"
"ds_bpermute_b32 v50, v21, v146\n"
"ds_bpermute_b32 v51, v21, v147\n"
"s_waitcnt lgkmcnt(0)\n"
"buffer_store_dwordx4 v[48:51], v18, s[76:79], s91 offen sc1\n"
"s_waitcnt vmcnt(1)\n"
"v_mfma_f32_16x16x32_bf16 v[56:59], v[38:41], v[68:71], 0\n"
"v_mfma_f32_16x16x32_bf16 v[56:59], v[38:41], v[76:79], v[56:59]\n"
"v_mfma_f32_16x16x32_bf16 v[56:59], v[38:41], v[84:87], v[56:59]\n"
"v_mfma_f32_16x16x32_bf16 v[60:63], v[38:41], v[72:75], 0\n"
"v_mfma_f32_16x16x32_bf16 v[60:63], v[38:41], v[80:83], v[60:63]\n"
"v_mfma_f32_16x16x32_bf16 v[60:63], v[38:41], v[88:91], v[60:63]\n"
"v_cvt_pk_bf16_f32 v64, v56, v57\n"
"v_cvt_pk_bf16_f32 v65, v58, v59\n"
"s_nop 5\n"
"v_cvt_pk_bf16_f32 v66, v60, v61\n"
"v_cvt_pk_bf16_f32 v67, v62, v63\n"
"ds_bpermute_b32 v44, v21, v64\n"
"ds_bpermute_b32 v45, v21, v65\n"
"ds_bpermute_b32 v46, v21, v66\n"
"ds_bpermute_b32 v47, v21, v67\n"
"s_waitcnt lgkmcnt(0)\n"
"buffer_store_dwordx4 v[44:47], v19, s[76:79], s91 offen sc1\n"
"s_branch .LJ_end\n"
".LJ_end:\n"
"s_cmp_lt_u32 s50, s53\n"
"s_cbranch_scc1 .LJ_ne\n"
"s_cmp_eq_u64 s[72:73], 0\n"
"s_cbranch_scc1 .LJ_ne\n"
"v_mov_b32 v12, 0\n"
"global_store_dwordx2 v12, v[0:1], s[72:73] sc1\n"
".LJ_ne:\n"
"s_waitcnt vmcnt(0) lgkmcnt(0)\n"
"s_endpgm\n"
      :: "{v2}"((unsigned)(lds0 + ((wid + 3) & 3) * 18592 + trp)), "{v3}"((unsigned)(lds0 + ((wid + 2) & 3) * 18592 + trp)), "{v4}"((unsigned)(lds0 + ((wid + 1) & 3) * 18592 + trp)), "{v5}"((unsigned)(lane * 16 + f0 * 1024 + ((0 + rot) % 7) * 1024 + 134316032)), "{v6}"((unsigned)(lane * 16 + f0 * 1024 + ((1 + rot) % 7) * 1024 + 134316032)), "{v7}"((unsigned)(lane * 16 + f0 * 1024 + ((2 + rot) % 7) * 1024 + 134316032)), "{v8}"((unsigned)(lane * 16 + f0 * 1024 + ((3 + rot) % 7) * 1024 + 134316032)), "{v9}"((unsigned)(lane * 16 + f0 * 1024 + ((4 + rot) % 7) * 1024 + 134316032)), "{v10}"((unsigned)(lane * 16 + f0 * 1024 + ((5 + rot) % 7) * 1024 + 134316032)), "{v11}"((unsigned)(lane * 16 + f0 * 1024 + ((6 + rot) % 7) * 1024 + 134316032)), "{v12}"((unsigned)((f0 + lane % 7) * 8 + kq * 32 + 353024 + J_GL * 1572864)), "{v13}"((unsigned)(lane * 16 + pair * 4096 + kq * 1024 + 212992)), "{v14}"((unsigned)(lane ? DROP : pair * 64 + kq * 8 + 354304)), "{v15}"((unsigned)((v16 * 2 + ((lane >> 2) & 1)) * 64 + (lane & 3) * 8 + 354304)), "{v16}"((unsigned)(lane * 16 + v16 * 8192 + 134434816)), "{v17}"((unsigned)(lane * 16 + v16 * 2048 + 134283264)), "{v18}"((unsigned)(lane * 16 + v16 * 2048 + 0)), "{v19}"((unsigned)(lane * 16 + v16 * 2048 + 1024)), "{v20}"((unsigned)(((lane >> 4) + (lane & 12)) * 4096 + (lane & 3) * 16 + v16 * 128)), "{v21}"((unsigned)(lane * 4)), "{v22}"((unsigned)((lane & 3) * 64 + (lane & 60))), "{v23}"((unsigned)(lg == (li >> 2) ? v16 * 128 + li * 8 + 67457024 : DROP)), "{v24}"((unsigned)(lane ? DROP : v16 * 8 + 67462912)), "{v26}"((unsigned)(bia0[0])), "{v27}"((unsigned)(bia0[1])), "{v28}"((unsigned)(bia0[2])), "{v29}"((unsigned)(bia0[3])), "{v30}"((unsigned)(bia1[0])), "{v31}"((unsigned)(bia1[1])), "{v32}"((unsigned)(bia1[2])), "{v33}"((unsigned)(bia1[3])), "{v34}"((unsigned)(0x3f803f80)), "{v35}"((unsigned)(0x3f803f80)), "{v36}"((unsigned)(0x3f803f80)), "{v37}"((unsigned)(0x3f803f80)), "{v38}"((unsigned)(sel0)), "{v39}"((unsigned)(sel1)), "{v40}"((unsigned)(sel0)), "{v41}"((unsigned)(sel1)), "{v44}"((unsigned)(vT + tsw)), "{v45}"((unsigned)(vT + (tsw ^ 8))), "{v46}"((unsigned)(vD + ((0 + rot) % 7) * 65536)), "{v47}"((unsigned)(vD + ((1 + rot) % 7) * 65536)), "{v48}"((unsigned)(vD + ((2 + rot) % 7) * 65536)), "{v49}"((unsigned)(vD + ((3 + rot) % 7) * 65536)), "{v50}"((unsigned)(vD + ((4 + rot) % 7) * 65536)), "{v51}"((unsigned)(vD + ((5 + rot) % 7) * 65536)), "{v52}"((unsigned)(vD + ((6 + rot) % 7) * 65536)), "{s[40:43]}"(rS), "{s[44:47]}"(rW), "{s[48:49]}"(wsp), "{s50}"((unsigned)(sA)), "{s51}"((unsigned)(sLo)), "{s52}"((unsigned)(sHi)), "{s53}"((unsigned)(sEx)), "{s54}"((unsigned)(sEnt)), "{s55}"((unsigned)(sLd)), "{s[56:57]}"(wsb + (sLo & 0x3ffffff)), "{s59}"((unsigned)(lds0 + wid * 18592)), "{s[62:63]}"(wsb), "{s[64:65]}"(0xAAAAAAAAAAAAAAAAull), "{s[66:67]}"(0xCCCCCCCCCCCCCCCCull), "{s[68:71]}"(rO), "{s[72:73]}"(ep), "{s[76:79]}"(rS), "{s80}"((unsigned)(201326592)), "{s81}"((unsigned)(0)), "{s82}"((unsigned)(0)), "{s83}"((unsigned)(0)), "{s86}"((unsigned)(sW0 + 64 * ((wid + 1) & 3))), "{s87}"((unsigned)(sW0 + 64 * ((wid + 2) & 3))), "{s88}"((unsigned)(sW0 + 64 * ((wid + 3) & 3))), "{s89}"((unsigned)(sW0 + 64 * ((wid + 4) & 3)))
      : "v255", "a255", "s95", "vcc", "scc", "memory");

    }
  }
}

extern "C" void kernel_launch(void* const* d_in, const int* in_sizes, int n_in, void* d_out, int out_size,
                              void* d_ws, size_t ws_size, hipStream_t stream) {
  Args m{};
  m.x = (const u16*)d_in[0]; m.wqkv = (const u16*)d_in[1]; m.wo = (const u16*)d_in[2]; m.w1 = (const u16*)d_in[4];
  m.b1 = (const u16*)d_in[5]; m.w2 = (const u16*)d_in[6]; m.b2 = (const u16*)d_in[7];
  m.out = (u16*)d_out; m.ws = (char*)d_ws;
  (void)hipFuncSetAttribute((const void*)stream_kernel, hipFuncAttributeMaxDynamicSharedMemorySize, LDS_BYTES);
  hipLaunchKernelGGL(stream_kernel, dim3(256), dim3(256), LDS_BYTES, stream, m);
}
```

```cpp
#include <hip/hip_runtime.h>
#include <stdint.h>
typedef unsigned short u16;
typedef unsigned long long u64;
typedef __attribute__((ext_vector_type(4))) unsigned u32x4;
constexpr unsigned F_X = 0;
constexpr unsigned F_ATT = 32768;
constexpr unsigned F_X1 = 65536;
constexpr unsigned F_H = 98304;
constexpr unsigned F_P = 212992;
constexpr unsigned F_ST1 = 344064;
constexpr unsigned F_ST2 = 348160;
constexpr unsigned F_G = 352256;
constexpr unsigned F_FA = 352512;
constexpr unsigned F_FX1 = 352768;
constexpr unsigned F_FH = 353024;
constexpr unsigned F_FX = 354048;
constexpr unsigned F_FP = 354304;
constexpr unsigned RS = 524288;
constexpr unsigned PADF = 256;
constexpr unsigned LOWB = 64;
constexpr unsigned L_TAB = 98304;
constexpr unsigned LDS_BYTES = 163840;
constexpr int Q_GL = 2;
constexpr int Q_EX = 0;
constexpr int Q_TL = 1;
constexpr int O_GL = 1;
constexpr int O_EX = 0;
constexpr int O_TL = 1;
constexpr int F_GL = 1;
constexpr int F_EX = 0;
constexpr int F_TL = 1;
constexpr int G_GL = 1;
constexpr int G_EX = 0;
constexpr int G_TL = 1;
constexpr int H_GL = 1;
constexpr int H_EX = 0;
constexpr int H_TL = 1;
constexpr int J_GL = 1;
constexpr int J_EX = 0;
constexpr int J_TL = 1;
#define MAPV 4
#define FDLY 0
#define PF 42
#define KR 1
#define XP 2
#define OR 1
#define JJ 1
#define DLO 0
#define DLF 450
#define DLG 0
#define SLP 12
#define TW 1
#define DLW 250
#define DLB 300
#define EC 1
#define DLQ 0
#define FR0 0
#define PQ 3
#define QW 6
#define NFL 48
#define PSTX 0
#define CP 1
#define FN 4
#define XF 1
#define P2 0
#define DT 400
#define DU 0
#define C2 0
#define NT 20
#define DLY 300
#define DX 1
#define NE 20
#define DLX 300

constexpr unsigned DROP = 0x40000000u, WS0 = 1u << 20;
static_assert(WS0 + (PADF + 256 - LOWB) * RS <= (256u << 20), "workspace");
struct Args { const u16 *x, *wqkv, *wo, *w1, *b1, *w2, *b2; u16* out; char* ws;
};
__device__ __forceinline__ u32x4 desc(const void* p, unsigned n) { const u64 a = (u64)p; return (u32x4){(unsigned)a, (unsigned)(a >> 32) & 0xffffu, n, 0x00020000u}; }
__device__ __forceinline__ float lo16(unsigned v) { return __builtin_bit_cast(float, v << 16); }
__device__ __forceinline__ float hi16(unsigned v) { return __builtin_bit_cast(float, v & 0xffff0000u); }

__host__ __device__ inline void decode(int bid, int& role, int& set, int& idx) {
  const int xg = bid & 7, xj = bid >> 3;
  if (xg < 4) {
    if (xj < 16) { role = 0; set = xj & 1; idx = MAPV == 1 || MAPV == 4 ? 8 * xg + (xj >> 1) : MAPV == 2 ? 4 * (xj >> 1) + xg : 4 * (xj >> 1) + ((xg + 2 * set) & 3); }
    else if (MAPV == 4 && SLP && xj >= 30) { role = 2; set = 0; idx = (int)(0x1915110e0d090501ull >> (16 * xg + 8 * (xj & 1))) & 31; }
    else { role = 2; set = xj < 23 ? 1 : xj < 30 ? 2 : 0; idx = 7 * xg + (xj - 16) % 7; }
  } else if (xj < 21) { role = 3; set = xj / 7; idx = (xg - 4) * 7 + xj % 7; }
  else {
    const int k = xj - 21, no = xg == 4 ? 11 : xg == 5 ? 5 : 4;
    if (k < no) { role = 1; set = xg == 4 ? k >> 3 : xg == 5 ? 1 : 2; idx = xg == 4 ? k & 7 : xg == 5 ? 3 + k : (xg - 6) * 4 + k; }
    else if (SLP) { role = 2; set = 0; idx = (int)((xg == 5 ? 0x181716100f0000ull : xg == 6 ? 0x1b1a0a08070302ull : 0x14131206040c0bull) >> (8 * (k - 4))) & 31; }
    else { role = 2; set = 0; idx = (int)((xg == 5 ? 0x19181711100200ull : xg == 6 ? 0x1b1a0b0a090403ull : 0x14131206050d0cull) >> (8 * (k - 4))) & 31; }
  }
}

__global__ __launch_bounds__(256, 1) void stream_kernel(Args a) {
  extern __shared__ __attribute__((aligned(16))) char smem[];
  const int tid = threadIdx.x, lane = tid & 63, wid = __builtin_amdgcn_readfirstlane(tid >> 6), li = lane & 15, lg = lane >> 4;
  const unsigned lds0 = (unsigned)(size_t)smem;
  const u64 tz = __builtin_amdgcn_s_memrealtime();
  const int bp = (blockIdx.x + 2 * (XP - 1)) & 7, bid = (blockIdx.x & ~7) | ((bp & 1) << 2) | (bp >> 1);
  {
    u64 pc; unsigned len;
    asm volatile("s_getpc_b64 %0\n.Lc0:\n s_mov_b32 %1, .LO_end-.Lc0\n s_max_u32 %1, %1, .LH_end-.Lc0\n s_max_u32 %1, %1, .LJ_end-.Lc0\n s_max_u32 %1, %1, .LG_end-.Lc0" : "=s"(pc), "=s"(len)::"scc");
    asm volatile("global_load_dword v252, %0, %1" ::"v"(min(((blockIdx.x >> 3) * 256 + tid) * 16, len)), "s"(pc) : "v252");
  }
  const int pi = (li >> 2) + 4 * (li & 3), xj = bid >> 3, xo = (bid & 7) - 4;
  auto two = [&]() __attribute__((always_inline)) {
    const int hf = xo > 1;
#pragma unroll
    for (int i = 0; i < 191 / (84 - NT) + 1; ++i) {
      const int r0 = min(((i * (84 - NT) + xj * 4 + wid - NT) * 64 + lane) >> (4 - hf), 767);
      asm volatile("global_load_dword v252, %0, %1" ::"v"(((r0 & 3) * 256 + 64 + (r0 >> 2)) * 2048 + (hf ? (lane & 7) + (xo & 1) * 8 : xo ? max(li, 6) : li) * 128), "s"(a.wo) : "v252", "memory");
    }
  };
  auto tw2 = [&]() __attribute__((always_inline)) {
#pragma unroll
    for (int i = 0; i < 3; ++i)
      if (i * 21 + xj < 56) asm volatile("global_load_dword v252, %0, %1" ::"v"((xo * 896 + wid * 224 + (i * 21 + xj) * 4 + lg) * 2048 + li * 128), "s"(a.w2) : "v252", "memory");
  };
  auto conv = [&](int pr, bool early) __attribute__((always_inline)) {
    const __amdgpu_buffer_rsrc_t rb = __builtin_amdgcn_make_buffer_rsrc(a.ws + WS0 - LOWB * RS, 0, (PADF + 256) * RS, 0x00020000);
    const u32x4* src = (const u32x4*)(a.x + ((pr >> 1) * 32 + 2 * pi + (pr & 1)) * 1024 + wid * 256 + lg * 8);
    const u64 ev = __hip_atomic_load((const u64*)a.ws, __ATOMIC_RELAXED, __HIP_MEMORY_SCOPE_AGENT);
    u32x4 v[8];
#pragma unroll
    for (int k = 0; k < 8; ++k) v[k] = src[k * 4];
    u32x4 pf = {};
    if (early && (EC & 2)) asm volatile("global_load_dword v252, %0, %1 sc1" ::"v"(WS0 + (PADF - LOWB + pr) * RS + (lane & 1) * F_G + wid * 8192 * !(lane & 1)), "s"(a.ws) : "v252");
    if (!early && pr >= NE && pr < (MAPV == 4 ? 84 : 104) && (PF & 50) == 2) {
      const int hf = MAPV == 4 ? xo > 1 : xo, g = xj * 4 + wid - NE, n = MAPV == 4 ? (hf ? 767 / (84 - NE) + 1 : 1535 / (84 - NE) + 1) : hf ? 767 / (112 - NE) + 1 : 1535 / (80 - NE) + 1;
      for (int i = 0; i < n; ++i) {
        const int u = g * n + i, r0 = hf ? u : u >> 1, row = r0 < 768 ? (r0 / 192) * 256 + 64 + r0 % 192 : 0;
        asm volatile("global_load_dwordx4 %0, %1, off" : "+v"(pf) : "v"((const char*)a.wo + row * 2048 + (hf ? xo == (MAPV == 4 ? 3 : 2) : u & 1) * 1024 + lane * 16));
      }
    }
    float s = 0.f, q = 0.f;
#pragma unroll
    for (int k = 0; k < 8; ++k) __builtin_amdgcn_raw_buffer_store_b128(v[k], rb, lane * 16 + k * 1024, (PADF + pr) * RS + F_X + wid * 8192, 16);
    if (!early && pr >= NE && pr < 84 && (PF & 16)) two();
    __builtin_amdgcn_sched_barrier(0);
#pragma unroll
    for (int k = 0; k < 8; ++k) {
#pragma unroll
      for (int d = 0; d < 4; ++d) { const float l = lo16(v[k][d]), h = hi16(v[k][d]); s += l + h; q = fmaf(l, l, fmaf(h, h, q)); }
    }
    s += __shfl_xor(s, 16); q += __shfl_xor(q, 16); s += __shfl_xor(s, 32); q += __shfl_xor(q, 32);
    float2* t = (float2*)smem;
    if (lg == 0) t[wid * 16 + li] = make_float2(s, q);
    if (!early && pr >= NE && pr < 84 && (PF & 16)) {
      asm volatile("s_waitcnt vmcnt(%0)" ::"n"(191 / (84 - NE) + 1) : "memory");
    } else asm volatile("s_waitcnt vmcnt(0)" : "+v"(pf)::"memory");
    __syncthreads();
    if (tid < 16) {
      const float2 t0 = t[li], t1 = t[16 + li], t2 = t[32 + li], t3 = t[48 + li];
      const float nm = ((t0.x + t1.x) + (t2.x + t3.x)) * -0x1p-10f;
      const float var = fmaxf(fmaf(-nm, nm, fmaf((t0.y + t1.y) + (t2.y + t3.y), 0x1p-10f, 1e-5f)), 1e-5f);
      const u32x4 g = {__builtin_bit_cast(unsigned, __builtin_amdgcn_rsqf(var)), __builtin_bit_cast(unsigned, nm), ((unsigned)ev + 2u) | 1u, (unsigned)(ev >> 32) + 0x9E3779B9u};
      __builtin_amdgcn_raw_buffer_store_b128(g, rb, li * 16, (PADF + pr) * RS + F_G, 16);
    }
    __syncthreads();
  };
  auto conv2 = [&](int pa, int pb) __attribute__((always_inline)) {
    const __amdgpu_buffer_rsrc_t rb = __builtin_amdgcn_make_buffer_rsrc(a.ws + WS0 - LOWB * RS, 0, (PADF + 256) * RS, 0x00020000);
    const u64 ev = __hip_atomic_load((const u64*)a.ws, __ATOMIC_RELAXED, __HIP_MEMORY_SCOPE_AGENT);
    u32x4 v[2][8];
    float2* t = (float2*)smem;
#pragma unroll
    for (int r = 0; r < 2; ++r) {
      const int pr = r ? pb : pa;
      const u32x4* src = (const u32x4*)(a.x + ((pr >> 1) * 32 + 2 * pi + (pr & 1)) * 1024 + wid * 256 + lg * 8);
#pragma unroll
      for (int k = 0; k < 8; ++k) v[r][k] = src[k * 4];
    }
    __builtin_amdgcn_sched_barrier(0);
#pragma unroll
    for (int r = 0; r < 2; ++r)
#pragma unroll
      for (int k = 0; k < 8; ++k) __builtin_amdgcn_raw_buffer_store_b128(v[r][k], rb, lane * 16 + k * 1024, (PADF + (r ? pb : pa)) * RS + F_X + wid * 8192, 16);
    __builtin_amdgcn_sched_barrier(0);
#pragma unroll
    for (int r = 0; r < 2; ++r) {
      float s = 0.f, q = 0.f;
#pragma unroll
      for (int k = 0; k < 8; ++k) {
#pragma unroll
        for (int d = 0; d < 4; ++d) { const float l = lo16(v[r][k][d]), h = hi16(v[r][k][d]); s += l + h; q = fmaf(l, l, fmaf(h, h, q)); }
      }
      s += __shfl_xor(s, 16); q += __shfl_xor(q, 16); s += __shfl_xor(s, 32); q += __shfl_xor(q, 32);
      if (lg == 0) t[r * 64 + wid * 16 + li] = make_float2(s, q);
    }
    asm volatile("s_waitcnt vmcnt(0)" ::: "memory");
    __syncthreads();
    if (wid < 2 && lane < 16) {
      const float2 t0 = t[wid * 64 + li], t1 = t[wid * 64 + 16 + li], t2 = t[wid * 64 + 32 + li], t3 = t[wid * 64 + 48 + li];
      const float nm = ((t0.x + t1.x) + (t2.x + t3.x)) * -0x1p-10f;
      const float var = fmaxf(fmaf(-nm, nm, fmaf((t0.y + t1.y) + (t2.y + t3.y), 0x1p-10f, 1e-5f)), 1e-5f);
      const u32x4 g = {__builtin_bit_cast(unsigned, __builtin_amdgcn_rsqf(var)), __builtin_bit_cast(unsigned, nm), ((unsigned)ev + 2u) | 1u, (unsigned)(ev >> 32) + 0x9E3779B9u};
      __builtin_amdgcn_raw_buffer_store_b128(g, rb, li * 16, (PADF + (wid ? pb : pa)) * RS + F_G, 16);
    }
    __syncthreads();
  };
  if (!(bid & 4)) {
    if (DX == 1 && bid >= 128) asm volatile("global_load_dword v252, %0, %1" ::"v"((((bid >> 3) - 16) * 64 + (tid >> 2)) * 128), "s"(a.x) : "v252");
    const unsigned o = (wid * 256 + (lane >> 2)) * 6144 + (bid & 7) * 512 + (lane & 3) * 128;
#pragma unroll
    for (int i = 0; i < PQ - 1; ++i) {
      const int j = (bid >> 3) + (PQ == 3 ? i * 16 : i * 16 - 16);
      if ((PQ == 3 && !i) || bid >= 128) asm volatile("global_load_dword v252, %0, %1" ::"v"(o + j / 3 * 16 * 6144 + j % 3 * 2048), "s"(a.wqkv) : "v252");
    }
  }
  int xe = xo;
  asm volatile("" : "+s"(xe));
  if (EC && xe >= 0 && xj < NE / 4) conv(xj * 4 + xo, true);
  if (FN == 4) __builtin_amdgcn_s_barrier();
  int role, set, idx;
  decode(bid, role, set, idx);
  const u64 wsp = (u64)a.ws, wsb = wsp + WS0 - LOWB * RS;
  const u32x4 rS = desc(a.ws + WS0 - LOWB * RS, (PADF + 256) * RS);
  if (role == 2 && xo < 0 && (PF & 1)) {
    const int g = (xj - 16) * 4 + wid;
    const char* p = (const char*)a.wqkv + (((g >> 2) * 2 + 1) * 32 + (g & 3) * 8 + (lane >> 5)) * 6144 + (bid & 7) * 512 + (lane & 31) * 16;
    u32x4 t[12];
    unsigned fl = 0;
    if (PF & 4) fl = __hip_atomic_load((const unsigned*)(a.ws + WS0 + (size_t)(PADF - LOWB + g * 4 + (bid & 7)) * RS + F_ST2 + (lane < 56 ? lane : 0) * 128), __ATOMIC_RELAXED, __HIP_MEMORY_SCOPE_AGENT);
#pragma unroll
    for (int i = 0; i < 12; ++i) t[i] = *(const u32x4*)(p + (i & 3) * 12288 + (i >> 2) * 2048);
#pragma unroll
    for (int i = 0; i < 12; ++i) asm volatile("" ::"v"(t[i]));
    asm volatile("" ::"v"(fl));
  }
  if (role == 2 && xo < 0)
    asm volatile("global_load_dword v252, %0, %1 sc1" ::"v"(WS0 + (PADF - LOWB + (xj * 4 + wid - 64) * 4 + (bid & 7)) * RS + F_ST2 + (lane < 16 ? lane : lane < NFL ? lane + 16 : 0) * 128), "s"(a.ws) : "v252");
  if (role == 2 && xo < 0 && (SLP & 1)) {
    const unsigned c0 = 14 * (xo + 4), c = c0 + ((3 - c0 - (unsigned)((u64)a.w1 >> 7)) & 7) + (lane & 2) * 4;
    asm volatile("global_load_dword v252, %0, %1" ::"v"(((xj * 4 + wid - 64) * 16 + (lane >> 2)) * 7168 + (c < c0 + 14 ? c : c0) * 128 + (lane & 1) * 64), "s"(a.w1) : "v252");
  }
  if (role == 3 && xj * 4 + wid < 56 && (SLP & 2))
    asm volatile("global_load_dword v252, %0, %1" ::"v"((xo * 896 + (xj * 4 + wid) * 16 + (lane >> 2)) * 2048 + (((3 - (unsigned)((u64)a.w2 >> 7)) & 7) + (lane & 2) * 4) * 128 + (lane & 1) * 64), "s"(a.w2) : "v252");
  if (role == 0 || FN == 2 || (FN == 3 && role == 2)) asm volatile("s_waitcnt vmcnt(%0)" ::"n"(QW));
  if (role == 0 && DLQ) while (__builtin_amdgcn_s_memrealtime() - tz < DLQ) __builtin_amdgcn_s_sleep(1);
  if (FN == 3) __builtin_amdgcn_s_barrier();
  if (xo < 0 && (PF & 8) && !FN) {
    const int g = xj * 4 + wid;
    const char* p = (const char*)a.wqkv + (g * 8 + (lane >> 5)) * 6144 + (bid & 7) * 512 + (lane & 31) * 16;
    u32x4 t[12];
#pragma unroll
    for (int i = 0; i < 12; ++i) t[i] = *(const u32x4*)(p + (i & 3) * 12288 + (i >> 2) * 2048);
    unsigned fl = 0;
    if (role == 2) fl = __hip_atomic_load((const unsigned*)(a.ws + WS0 + (size_t)(PADF - LOWB + (g - 64) * 4 + (bid & 7)) * RS + F_ST2 + (lane < 56 ? lane : 0) * 128), __ATOMIC_RELAXED, __HIP_MEMORY_SCOPE_AGENT);
#pragma unroll
    for (int i = 0; i < 12; ++i) asm volatile("" ::"v"(t[i]));
    asm volatile("" ::"v"(fl));
  }
  if (role == 2 && xo < 0) for (int i = 0; i < FDLY; ++i) __builtin_amdgcn_s_sleep(16);
  const int p2 = xj < 4 ? 84 + xj * 4 + xo : P2 ? (xj % 7 < 4 ? 84 + (set * 4 + xj % 7) * 4 + xo : 128) : xj % 7 > 3 ? 84 + (set * 3 + xj % 7) * 4 + xo : 128;
  if (role == 3 && xj >= NT / 4 && (PF & 32)) {
    while (__builtin_amdgcn_s_memrealtime() - tz < DLW) __builtin_amdgcn_s_sleep(2);
    two();
  }
  for (int pr = role < 3 ? 128 : EC && xj < NE / 4 ? p2 : xj * 4 + xo; pr < 128; pr = pr < 84 ? p2 : 128) {
    if (pr >= NE) while (__builtin_amdgcn_s_memrealtime() - tz < (pr < NT ? DLY : DLX)) __builtin_amdgcn_s_sleep(4);
    if (pr >= 84 && (SLP & 32)) {
      while (__builtin_amdgcn_s_memrealtime() - tz < DLG) __builtin_amdgcn_s_sleep(4);
      tw2();
    }
    if (C2 && pr >= C2 && pr < 84 && p2 < 128) { conv2(pr, p2); break; }
    conv(pr, false);
  }
  const unsigned sLo = (PADF + set) * RS, sLd = lds0 + wid * 40960, vT = sLd + (8 * lg + (li >> 2)) * 64 + (li & 3) * 16;
  const unsigned trp = (li & 3) * 4608 + (li & 1) * 32 + (li & 2) * 64 + (li >> 2) * 64 + lg * 16;
  if (role == 0) {
    const unsigned sA = sLo - 2 * RS, sHi = sLo + 127 * 2 * RS, sEx = sHi + Q_EX * 2 * RS, sEnt = -wid & 3, tsw = 0, rot = KR * !PQ * (set + 2 * (idx & 1));
    const unsigned sW0 = wid * 256 * 6144 + idx * 64, vD = (lane >> 2) * 6144 + (lane & 3) * 16;
    const u32x4 rW = desc(a.wqkv, 1024 * 6144);
    auto ok = [&](int r, int prev) { const int k = lg + 4 * r; return prev ? k >= pi + 9 : (k <= pi && k >= pi - 7); };
    auto qmask = [&](int r, int prev) { return ok(r, prev) ? 0u : 0xff800000u; };
    auto pmask = [&](int d) { return (ok(2 * (d & 1), d >> 1) ? 0xffffu : 0u) | (ok(2 * (d & 1) + 1, d >> 1) ? 0xffff0000u : 0u); };
    asm volatile(
"s_nop 4\n"
"v_mov_b32 v186, 0\n"
"global_load_dwordx2 v[0:1], v186, s[48:49] sc1\n"
"v_mov_b32 v188, 0x3f803f80\n"
"v_mov_b32 v189, 0x3f803f80\n"
"v_mov_b32 v190, 0x3f803f80\n"
"v_mov_b32 v191, 0x3f803f80\n"
"v_xor_b32 v229, 64, v55\n"
"v_xor_b32 v230, 128, v55\n"
"v_xor_b32 v231, 192, v55\n"
"s_add_u32 m0, s55, 16384\n"
"s_add_u32 s60, s68, 0\n"
"buffer_load_dwordx4 v57, s[44:47], s60 offen lds\n"
"s_add_u32 m0, s55, 17408\n"
"s_add_u32 s60, s68, 98304\n"
"buffer_load_dwordx4 v57, s[44:47], s60 offen lds\n"
"s_add_u32 m0, s55, 18432\n"
"s_add_u32 s60, s69, 0\n"
"buffer_load_dwordx4 v57, s[44:47], s60 offen lds\n"
"s_add_u32 m0, s55, 19456\n"
"s_add_u32 s60, s69, 98304\n"
"buffer_load_dwordx4 v57, s[44:47], s60 offen lds\n"
"s_add_u32 m0, s55, 20480\n"
"s_add_u32 s60, s70, 0\n"
"buffer_load_dwordx4 v57, s[44:47], s60 offen lds\n"
"s_add_u32 m0, s55, 21504\n"
"s_add_u32 s60, s70, 98304\n"
"buffer_load_dwordx4 v57, s[44:47], s60 offen lds\n"
"s_add_u32 m0, s55, 22528\n"
"s_add_u32 s60, s68, 196608\n"
"buffer_load_dwordx4 v57, s[44:47], s60 offen lds\n"
"s_add_u32 m0, s55, 23552\n"
"s_add_u32 s60, s68, 294912\n"
"buffer_load_dwordx4 v57, s[44:47], s60 offen lds\n"
"s_add_u32 m0, s55, 0\n"
"s_mov_b32 s60, 0\n"
"v_xor_b32 v187, 0, v54\n"
"buffer_load_dwordx4 v187, s[64:67], s60 offen lds\n"
"s_add_u32 m0, s55, 1024\n"
"s_mov_b32 s60, 32768\n"
"v_xor_b32 v187, 32, v54\n"
"buffer_load_dwordx4 v187, s[64:67], s60 offen lds\n"
"s_add_u32 m0, s55, 2048\n"
"s_mov_b32 s60, 4096\n"
"v_xor_b32 v187, 64, v54\n"
"buffer_load_dwordx4 v187, s[64:67], s60 offen lds\n"
"s_add_u32 m0, s55, 3072\n"
"s_mov_b32 s60, 36864\n"
"v_xor_b32 v187, 96, v54\n"
"buffer_load_dwordx4 v187, s[64:67], s60 offen lds\n"
"s_add_u32 m0, s55, 4096\n"
"s_mov_b32 s60, 8192\n"
"v_xor_b32 v187, 128, v54\n"
"buffer_load_dwordx4 v187, s[64:67], s60 offen lds\n"
"s_add_u32 m0, s55, 5120\n"
"s_mov_b32 s60, 40960\n"
"v_xor_b32 v187, 160, v54\n"
"buffer_load_dwordx4 v187, s[64:67], s60 offen lds\n"
"s_add_u32 m0, s55, 6144\n"
"s_mov_b32 s60, 12288\n"
"v_xor_b32 v187, 192, v54\n"
"buffer_load_dwordx4 v187, s[64:67], s60 offen lds\n"
"s_add_u32 m0, s55, 7168\n"
"s_mov_b32 s60, 45056\n"
"v_xor_b32 v187, 224, v54\n"
"buffer_load_dwordx4 v187, s[64:67], s60 offen lds\n"
"s_add_u32 m0, s55, 8192\n"
"s_mov_b32 s60, 65536\n"
"v_xor_b32 v187, 0, v54\n"
"buffer_load_dwordx4 v187, s[64:67], s60 offen lds\n"
"s_add_u32 m0, s55, 9216\n"
"s_mov_b32 s60, 98304\n"
"v_xor_b32 v187, 32, v54\n"
"buffer_load_dwordx4 v187, s[64:67], s60 offen lds\n"
"s_add_u32 m0, s55, 10240\n"
"s_mov_b32 s60, 69632\n"
"v_xor_b32 v187, 64, v54\n"
"buffer_load_dwordx4 v187, s[64:67], s60 offen lds\n"
"s_add_u32 m0, s55, 11264\n"
"s_mov_b32 s60, 102400\n"
"v_xor_b32 v187, 96, v54\n"
"buffer_load_dwordx4 v187, s[64:67], s60 offen lds\n"
"s_add_u32 m0, s55, 12288\n"
"s_mov_b32 s60, 73728\n"
"v_xor_b32 v187, 128, v54\n"
"buffer_load_dwordx4 v187, s[64:67], s60 offen lds\n"
"s_add_u32 m0, s55, 13312\n"
"s_mov_b32 s60, 106496\n"
"v_xor_b32 v187, 160, v54\n"
"buffer_load_dwordx4 v187, s[64:67], s60 offen lds\n"
"s_add_u32 m0, s55, 14336\n"
"s_mov_b32 s60, 77824\n"
"v_xor_b32 v187, 192, v54\n"
"buffer_load_dwordx4 v187, s[64:67], s60 offen lds\n"
"s_add_u32 m0, s55, 15360\n"
"s_mov_b32 s60, 110592\n"
"v_xor_b32 v187, 224, v54\n"
"buffer_load_dwordx4 v187, s[64:67], s60 offen lds\n"
"s_add_u32 m0, s55, 24576\n"
"s_add_u32 s60, s69, 196608\n"
"buffer_load_dwordx4 v57, s[44:47], s60 offen lds\n"
"s_add_u32 m0, s55, 25600\n"
"s_add_u32 s60, s69, 294912\n"
"buffer_load_dwordx4 v57, s[44:47], s60 offen lds\n"
"s_add_u32 m0, s55, 26624\n"
"s_add_u32 s60, s70, 196608\n"
"buffer_load_dwordx4 v57, s[44:47], s60 offen lds\n"
"s_add_u32 m0, s55, 27648\n"
"s_add_u32 s60, s70, 294912\n"
"buffer_load_dwordx4 v57, s[44:47], s60 offen lds\n"
"s_add_u32 m0, s55, 28672\n"
"s_add_u32 s60, s68, 393216\n"
"buffer_load_dwordx4 v57, s[44:47], s60 offen lds\n"
"s_add_u32 m0, s55, 29696\n"
"s_add_u32 s60, s68, 491520\n"
"buffer_load_dwordx4 v57, s[44:47], s60 offen lds\n"
"s_add_u32 m0, s55, 30720\n"
"s_add_u32 s60, s69, 393216\n"
"buffer_load_dwordx4 v57, s[44:47], s60 offen lds\n"
"s_add_u32 m0, s55, 31744\n"
"s_add_u32 s60, s69, 491520\n"
"buffer_load_dwordx4 v57, s[44:47], s60 offen lds\n"
"s_waitcnt vmcnt(16)\n"
"s_cmp_lg_u32 s54, 0\n"
"s_cbranch_scc1 .LQ_v123_0\n"
"ds_read_b128 v[64:67], v55 offset:0\n"
"ds_read_b128 v[68:71], v229 offset:0\n"
"ds_read_b128 v[72:75], v230 offset:0\n"
"ds_read_b128 v[76:79], v231 offset:0\n"
"ds_read_b128 v[80:83], v55 offset:256\n"
"ds_read_b128 v[84:87], v229 offset:256\n"
"ds_read_b128 v[88:91], v230 offset:256\n"
"ds_read_b128 v[92:95], v231 offset:256\n"
"s_branch .LQ_v123_3\n"
".LQ_v123_0:\n"
"s_cmp_lg_u32 s54, 1\n"
"s_cbranch_scc1 .LQ_v123_1\n"
"ds_read_b128 v[96:99], v55 offset:0\n"
"ds_read_b128 v[100:103], v229 offset:0\n"
"ds_read_b128 v[104:107], v230 offset:0\n"
"ds_read_b128 v[108:111], v231 offset:0\n"
"ds_read_b128 v[112:115], v55 offset:256\n"
"ds_read_b128 v[116:119], v229 offset:256\n"
"ds_read_b128 v[120:123], v230 offset:256\n"
"ds_read_b128 v[124:127], v231 offset:256\n"
"s_branch .LQ_v123_3\n"
".LQ_v123_1:\n"
"s_cmp_lg_u32 s54, 2\n"
"s_cbranch_scc1 .LQ_v123_2\n"
"ds_read_b128 a[192:195], v55 offset:0\n"
"ds_read_b128 a[196:199], v229 offset:0\n"
"ds_read_b128 a[200:203], v230 offset:0\n"
"ds_read_b128 a[204:207], v231 offset:0\n"
"ds_read_b128 a[208:211], v55 offset:256\n"
"ds_read_b128 a[212:215], v229 offset:256\n"
"ds_read_b128 a[216:219], v230 offset:256\n"
"ds_read_b128 a[220:223], v231 offset:256\n"
"s_branch .LQ_v123_3\n"
".LQ_v123_2:\n"
"ds_read_b128 a[224:227], v55 offset:0\n"
"ds_read_b128 a[228:231], v229 offset:0\n"
"ds_read_b128 a[232:235], v230 offset:0\n"
"ds_read_b128 a[236:239], v231 offset:0\n"
"ds_read_b128 a[240:243], v55 offset:256\n"
"ds_read_b128 a[244:247], v229 offset:256\n"
"ds_read_b128 a[248:251], v230 offset:256\n"
"ds_read_b128 a[252:255], v231 offset:256\n"
".LQ_v123_3:\n"
"s_waitcnt lgkmcnt(0)\n"
"s_add_u32 m0, s55, 0\n"
"s_add_u32 s60, s70, 393216\n"
"buffer_load_dwordx4 v57, s[44:47], s60 offen lds\n"
"s_add_u32 m0, s55, 1024\n"
"s_add_u32 s60, s70, 491520\n"
"buffer_load_dwordx4 v57, s[44:47], s60 offen lds\n"
"s_add_u32 m0, s55, 2048\n"
"s_add_u32 s60, s68, 589824\n"
"buffer_load_dwordx4 v57, s[44:47], s60 offen lds\n"
"s_add_u32 m0, s55, 3072\n"
"s_add_u32 s60, s68, 688128\n"
"buffer_load_dwordx4 v57, s[44:47], s60 offen lds\n"
"s_add_u32 m0, s55, 4096\n"
"s_add_u32 s60, s69, 589824\n"
"buffer_load_dwordx4 v57, s[44:47], s60 offen lds\n"
"s_add_u32 m0, s55, 5120\n"
"s_add_u32 s60, s69, 688128\n"
"buffer_load_dwordx4 v57, s[44:47], s60 offen lds\n"
"s_add_u32 m0, s55, 6144\n"
"s_add_u32 s60, s70, 589824\n"
"buffer_load_dwordx4 v57, s[44:47], s60 offen lds\n"
"s_add_u32 m0, s55, 7168\n"
"s_add_u32 s60, s70, 688128\n"
"buffer_load_dwordx4 v57, s[44:47], s60 offen lds\n"
"s_waitcnt vmcnt(16)\n"
"s_cmp_lg_u32 s54, 0\n"
"s_cbranch_scc1 .LQ_v150_0\n"
"ds_read_b128 v[96:99], v55 offset:8192\n"
"ds_read_b128 v[100:103], v229 offset:8192\n"
"ds_read_b128 v[104:107], v230 offset:8192\n"
"ds_read_b128 v[108:111], v231 offset:8192\n"
"ds_read_b128 v[112:115], v55 offset:8448\n"
"ds_read_b128 v[116:119], v229 offset:8448\n"
"ds_read_b128 v[120:123], v230 offset:8448\n"
"ds_read_b128 v[124:127], v231 offset:8448\n"
"s_branch .LQ_v150_3\n"
".LQ_v150_0:\n"
"s_cmp_lg_u32 s54, 1\n"
"s_cbranch_scc1 .LQ_v150_1\n"
"ds_read_b128 a[192:195], v55 offset:8192\n"
"ds_read_b128 a[196:199], v229 offset:8192\n"
"ds_read_b128 a[200:203], v230 offset:8192\n"
"ds_read_b128 a[204:207], v231 offset:8192\n"
"ds_read_b128 a[208:211], v55 offset:8448\n"
"ds_read_b128 a[212:215], v229 offset:8448\n"
"ds_read_b128 a[216:219], v230 offset:8448\n"
"ds_read_b128 a[220:223], v231 offset:8448\n"
"s_branch .LQ_v150_3\n"
".LQ_v150_1:\n"
"s_cmp_lg_u32 s54, 2\n"
"s_cbranch_scc1 .LQ_v150_2\n"
"ds_read_b128 a[224:227], v55 offset:8192\n"
"ds_read_b128 a[228:231], v229 offset:8192\n"
"ds_read_b128 a[232:235], v230 offset:8192\n"
"ds_read_b128 a[236:239], v231 offset:8192\n"
"ds_read_b128 a[240:243], v55 offset:8448\n"
"ds_read_b128 a[244:247], v229 offset:8448\n"
"ds_read_b128 a[248:251], v230 offset:8448\n"
"ds_read_b128 a[252:255], v231 offset:8448\n"
"s_branch .LQ_v150_3\n"
".LQ_v150_2:\n"
"ds_read_b128 v[64:67], v55 offset:8192\n"
"ds_read_b128 v[68:71], v229 offset:8192\n"
"ds_read_b128 v[72:75], v230 offset:8192\n"
"ds_read_b128 v[76:79], v231 offset:8192\n"
"ds_read_b128 v[80:83], v55 offset:8448\n"
"ds_read_b128 v[84:87], v229 offset:8448\n"
"ds_read_b128 v[88:91], v230 offset:8448\n"
"ds_read_b128 v[92:95], v231 offset:8448\n"
".LQ_v150_3:\n"
"s_waitcnt lgkmcnt(0)\n"
"s_add_u32 m0, s55, 8192\n"
"s_add_u32 s60, s68, 786432\n"
"buffer_load_dwordx4 v57, s[44:47], s60 offen lds\n"
"s_add_u32 m0, s55, 9216\n"
"s_add_u32 s60, s68, 884736\n"
"buffer_load_dwordx4 v57, s[44:47], s60 offen lds\n"
"s_add_u32 m0, s55, 10240\n"
"s_add_u32 s60, s69, 786432\n"
"buffer_load_dwordx4 v57, s[44:47], s60 offen lds\n"
"s_add_u32 m0, s55, 11264\n"
"s_add_u32 s60, s69, 884736\n"
"buffer_load_dwordx4 v57, s[44:47], s60 offen lds\n"
"s_add_u32 m0, s55, 12288\n"
"s_add_u32 s60, s70, 786432\n"
"buffer_load_dwordx4 v57, s[44:47], s60 offen lds\n"
"s_add_u32 m0, s55, 13312\n"
"s_add_u32 s60, s70, 884736\n"
"buffer_load_dwordx4 v57, s[44:47], s60 offen lds\n"
"s_add_u32 m0, s55, 14336\n"
"s_add_u32 s60, s68, 983040\n"
"buffer_load_dwordx4 v57, s[44:47], s60 offen lds\n"
"s_add_u32 m0, s55, 15360\n"
"s_add_u32 s60, s68, 1081344\n"
"buffer_load_dwordx4 v57, s[44:47], s60 offen lds\n"
"s_waitcnt vmcnt(46)\n"
"ds_read_b64_tr_b16 a[0:1], v48 offset:16384\n"
"ds_read_b64_tr_b16 a[2:3], v48 offset:16640\n"
"ds_read_b64_tr_b16 a[32:33], v49 offset:16384\n"
"ds_read_b64_tr_b16 a[34:35], v49 offset:16640\n"
"v_add_u32 v0, 2, v0\n"
"v_add_u32 v1, 0x9e3779b9, v1\n"
"v_or_b32 v0, 1, v0\n"
"v_readfirstlane_b32 s49, v1\n"
"v_readfirstlane_b32 s48, v0\n"
"s_waitcnt vmcnt(44)\n"
"ds_read_b64_tr_b16 a[64:65], v48 offset:18432\n"
"ds_read_b64_tr_b16 a[66:67], v48 offset:18688\n"
"ds_read_b64_tr_b16 a[96:97], v49 offset:18432\n"
"ds_read_b64_tr_b16 a[98:99], v49 offset:18688\n"
"s_waitcnt lgkmcnt(4)\n"
"s_add_u32 m0, s55, 16384\n"
"s_add_u32 s60, s69, 983040\n"
"buffer_load_dwordx4 v57, s[44:47], s60 offen lds\n"
"s_add_u32 m0, s55, 17408\n"
"s_add_u32 s60, s69, 1081344\n"
"buffer_load_dwordx4 v57, s[44:47], s60 offen lds\n"
"v_mfma_f32_16x16x32_bf16 v[192:195], a[0:3], v[188:191], 0\n"
"v_mfma_f32_16x16x32_bf16 v[196:199], a[32:35], v[188:191], 0\n"
"s_waitcnt vmcnt(44)\n"
"ds_read_b64_tr_b16 a[128:129], v48 offset:20480\n"
"ds_read_b64_tr_b16 a[130:131], v48 offset:20736\n"
"ds_read_b64_tr_b16 a[160:161], v49 offset:20480\n"
"ds_read_b64_tr_b16 a[162:163], v49 offset:20736\n"
"s_waitcnt lgkmcnt(4)\n"
"s_add_u32 m0, s55, 18432\n"
"s_add_u32 s60, s70, 983040\n"
"buffer_load_dwordx4 v57, s[44:47], s60 offen lds\n"
"s_add_u32 m0, s55, 19456\n"
"s_add_u32 s60, s70, 1081344\n"
"buffer_load_dwordx4 v57, s[44:47], s60 offen lds\n"
"v_mfma_f32_16x16x32_bf16 v[200:203], a[64:67], v[188:191], 0\n"
"v_mfma_f32_16x16x32_bf16 v[204:207], a[96:99], v[188:191], 0\n"
"s_waitcnt vmcnt(44)\n"
"ds_read_b64_tr_b16 a[4:5], v48 offset:22528\n"
"ds_read_b64_tr_b16 a[6:7], v48 offset:22784\n"
"ds_read_b64_tr_b16 a[36:37], v49 offset:22528\n"
"ds_read_b64_tr_b16 a[38:39], v49 offset:22784\n"
"s_waitcnt lgkmcnt(4)\n"
"s_add_u32 m0, s55, 20480\n"
"s_add_u32 s60, s68, 1179648\n"
"buffer_load_dwordx4 v57, s[44:47], s60 offen lds\n"
"s_add_u32 m0, s55, 21504\n"
"s_add_u32 s60, s68, 1277952\n"
"buffer_load_dwordx4 v57, s[44:47], s60 offen lds\n"
"v_mfma_f32_16x16x32_bf16 v[208:211], v[188:191], a[128:131], 0\n"
"v_mfma_f32_16x16x32_bf16 v[212:215], v[188:191], a[160:163], 0\n"
"s_waitcnt vmcnt(28)\n"
"ds_read_b64_tr_b16 a[68:69], v48 offset:24576\n"
"ds_read_b64_tr_b16 a[70:71], v48 offset:24832\n"
"ds_read_b64_tr_b16 a[100:101], v49 offset:24576\n"
"ds_read_b64_tr_b16 a[102:103], v49 offset:24832\n"
"s_waitcnt lgkmcnt(4)\n"
"s_add_u32 m0, s55, 22528\n"
"s_add_u32 s60, s69, 1179648\n"
"buffer_load_dwordx4 v57, s[44:47], s60 offen lds\n"
"s_add_u32 m0, s55, 23552\n"
"s_add_u32 s60, s69, 1277952\n"
"buffer_load_dwordx4 v57, s[44:47], s60 offen lds\n"
"v_mfma_f32_16x16x32_bf16 v[192:195], a[4:7], v[188:191], v[192:195]\n"
"v_mfma_f32_16x16x32_bf16 v[196:199], a[36:39], v[188:191], v[196:199]\n"
"s_waitcnt vmcnt(28)\n"
"ds_read_b64_tr_b16 a[132:133], v48 offset:26624\n"
"ds_read_b64_tr_b16 a[134:135], v48 offset:26880\n"
"ds_read_b64_tr_b16 a[164:165], v49 offset:26624\n"
"ds_read_b64_tr_b16 a[166:167], v49 offset:26880\n"
"s_waitcnt lgkmcnt(4)\n"
"s_add_u32 m0, s55, 24576\n"
"s_add_u32 s60, s70, 1179648\n"
"buffer_load_dwordx4 v57, s[44:47], s60 offen lds\n"
"s_add_u32 m0, s55, 25600\n"
"s_add_u32 s60, s70, 1277952\n"
"buffer_load_dwordx4 v57, s[44:47], s60 offen lds\n"
"v_mfma_f32_16x16x32_bf16 v[200:203], a[68:71], v[188:191], v[200:203]\n"
"v_mfma_f32_16x16x32_bf16 v[204:207], a[100:103], v[188:191], v[204:207]\n"
"s_waitcnt vmcnt(28)\n"
"ds_read_b64_tr_b16 a[8:9], v48 offset:28672\n"
"ds_read_b64_tr_b16 a[10:11], v48 offset:28928\n"
"ds_read_b64_tr_b16 a[40:41], v49 offset:28672\n"
"ds_read_b64_tr_b16 a[42:43], v49 offset:28928\n"
"s_waitcnt lgkmcnt(4)\n"
"s_add_u32 m0, s55, 26624\n"
"s_add_u32 s60, s68, 1376256\n"
"buffer_load_dwordx4 v57, s[44:47], s60 offen lds\n"
"s_add_u32 m0, s55, 27648\n"
"s_add_u32 s60, s68, 1474560\n"
"buffer_load_dwordx4 v57, s[44:47], s60 offen lds\n"
"v_mfma_f32_16x16x32_bf16 v[208:211], v[188:191], a[132:135], v[208:211]\n"
"v_mfma_f32_16x16x32_bf16 v[212:215], v[188:191], a[164:167], v[212:215]\n"
"s_waitcnt vmcnt(28)\n"
"ds_read_b64_tr_b16 a[72:73], v48 offset:30720\n"
"ds_read_b64_tr_b16 a[74:75], v48 offset:30976\n"
"ds_read_b64_tr_b16 a[104:105], v49 offset:30720\n"
"ds_read_b64_tr_b16 a[106:107], v49 offset:30976\n"
"s_waitcnt lgkmcnt(4)\n"
"s_add_u32 m0, s55, 28672\n"
"s_add_u32 s60, s69, 1376256\n"
"buffer_load_dwordx4 v57, s[44:47], s60 offen lds\n"
"s_add_u32 m0, s55, 29696\n"
"s_add_u32 s60, s69, 1474560\n"
"buffer_load_dwordx4 v57, s[44:47], s60 offen lds\n"
"v_mfma_f32_16x16x32_bf16 v[192:195], a[8:11], v[188:191], v[192:195]\n"
"v_mfma_f32_16x16x32_bf16 v[196:199], a[40:43], v[188:191], v[196:199]\n"
"s_waitcnt vmcnt(28)\n"
"ds_read_b64_tr_b16 a[136:137], v48 offset:0\n"
"ds_read_b64_tr_b16 a[138:139], v48 offset:256\n"
"ds_read_b64_tr_b16 a[168:169], v49 offset:0\n"
"ds_read_b64_tr_b16 a[170:171], v49 offset:256\n"
"s_waitcnt lgkmcnt(4)\n"
"s_add_u32 m0, s55, 30720\n"
"s_add_u32 s60, s70, 1376256\n"
"buffer_load_dwordx4 v57, s[44:47], s60 offen lds\n"
"s_add_u32 m0, s55, 31744\n"
"s_add_u32 s60, s70, 1474560\n"
"buffer_load_dwordx4 v57, s[44:47], s60 offen lds\n"
"v_mfma_f32_16x16x32_bf16 v[200:203], a[72:75], v[188:191], v[200:203]\n"
"v_mfma_f32_16x16x32_bf16 v[204:207], a[104:107], v[188:191], v[204:207]\n"
"buffer_load_dwordx4 v[232:235], v50, s[40:43], s50 offen sc1\n"
"buffer_load_dwordx4 v[236:239], v51, s[40:43], s50 offen sc1\n"
"buffer_load_dwordx4 v[240:243], v52, s[40:43], s50 offen sc1\n"
"buffer_load_dwordx4 v[244:247], v53, s[40:43], s50 offen sc1\n"
"s_waitcnt vmcnt(32)\n"
"ds_read_b64_tr_b16 a[12:13], v48 offset:2048\n"
"ds_read_b64_tr_b16 a[14:15], v48 offset:2304\n"
"ds_read_b64_tr_b16 a[44:45], v49 offset:2048\n"
"ds_read_b64_tr_b16 a[46:47], v49 offset:2304\n"
"s_waitcnt lgkmcnt(4)\n"
"v_mfma_f32_16x16x32_bf16 v[208:211], v[188:191], a[136:139], v[208:211]\n"
"v_mfma_f32_16x16x32_bf16 v[212:215], v[188:191], a[168:171], v[212:215]\n"
"s_waitcnt vmcnt(30)\n"
"ds_read_b64_tr_b16 a[76:77], v48 offset:4096\n"
"ds_read_b64_tr_b16 a[78:79], v48 offset:4352\n"
"ds_read_b64_tr_b16 a[108:109], v49 offset:4096\n"
"ds_read_b64_tr_b16 a[110:111], v49 offset:4352\n"
"s_waitcnt lgkmcnt(4)\n"
"v_mfma_f32_16x16x32_bf16 v[192:195], a[12:15], v[188:191], v[192:195]\n"
"v_mfma_f32_16x16x32_bf16 v[196:199], a[44:47], v[188:191], v[196:199]\n"
"s_waitcnt vmcnt(28)\n"
"ds_read_b64_tr_b16 a[140:141], v48 offset:6144\n"
"ds_read_b64_tr_b16 a[142:143], v48 offset:6400\n"
"ds_read_b64_tr_b16 a[172:173], v49 offset:6144\n"
"ds_read_b64_tr_b16 a[174:175], v49 offset:6400\n"
"s_waitcnt lgkmcnt(4)\n"
"v_mfma_f32_16x16x32_bf16 v[200:203], a[76:79], v[188:191], v[200:203]\n"
"v_mfma_f32_16x16x32_bf16 v[204:207], a[108:111], v[188:191], v[204:207]\n"
"s_waitcnt vmcnt(26)\n"
"ds_read_b64_tr_b16 a[16:17], v48 offset:8192\n"
"ds_read_b64_tr_b16 a[18:19], v48 offset:8448\n"
"ds_read_b64_tr_b16 a[48:49], v49 offset:8192\n"
"ds_read_b64_tr_b16 a[50:51], v49 offset:8448\n"
"s_waitcnt lgkmcnt(4)\n"
"v_mfma_f32_16x16x32_bf16 v[208:211], v[188:191], a[140:143], v[208:211]\n"
"v_mfma_f32_16x16x32_bf16 v[212:215], v[188:191], a[172:175], v[212:215]\n"
"s_waitcnt vmcnt(24)\n"
"ds_read_b64_tr_b16 a[80:81], v48 offset:10240\n"
"ds_read_b64_tr_b16 a[82:83], v48 offset:10496\n"
"ds_read_b64_tr_b16 a[112:113], v49 offset:10240\n"
"ds_read_b64_tr_b16 a[114:115], v49 offset:10496\n"
"s_waitcnt lgkmcnt(4)\n"
"v_mfma_f32_16x16x32_bf16 v[192:195], a[16:19], v[188:191], v[192:195]\n"
"v_mfma_f32_16x16x32_bf16 v[196:199], a[48:51], v[188:191], v[196:199]\n"
"s_waitcnt vmcnt(22)\n"
"ds_read_b64_tr_b16 a[144:145], v48 offset:12288\n"
"ds_read_b64_tr_b16 a[146:147], v48 offset:12544\n"
"ds_read_b64_tr_b16 a[176:177], v49 offset:12288\n"
"ds_read_b64_tr_b16 a[178:179], v49 offset:12544\n"
"s_waitcnt lgkmcnt(4)\n"
"v_mfma_f32_16x16x32_bf16 v[200:203], a[80:83], v[188:191], v[200:203]\n"
"v_mfma_f32_16x16x32_bf16 v[204:207], a[112:115], v[188:191], v[204:207]\n"
"s_waitcnt vmcnt(20)\n"
"ds_read_b64_tr_b16 a[20:21], v48 offset:14336\n"
"ds_read_b64_tr_b16 a[22:23], v48 offset:14592\n"
"ds_read_b64_tr_b16 a[52:53], v49 offset:14336\n"
"ds_read_b64_tr_b16 a[54:55], v49 offset:14592\n"
"s_waitcnt lgkmcnt(4)\n"
"v_mfma_f32_16x16x32_bf16 v[208:211], v[188:191], a[144:147], v[208:211]\n"
"v_mfma_f32_16x16x32_bf16 v[212:215], v[188:191], a[176:179], v[212:215]\n"
"s_waitcnt vmcnt(18)\n"
"ds_read_b64_tr_b16 a[84:85], v48 offset:16384\n"
"ds_read_b64_tr_b16 a[86:87], v48 offset:16640\n"
"ds_read_b64_tr_b16 a[116:117], v49 offset:16384\n"
"ds_read_b64_tr_b16 a[118:119], v49 offset:16640\n"
"s_waitcnt lgkmcnt(4)\n"
"v_mfma_f32_16x16x32_bf16 v[192:195], a[20:23], v[188:191], v[192:195]\n"
"v_mfma_f32_16x16x32_bf16 v[196:199], a[52:55], v[188:191], v[196:199]\n"
"s_waitcnt vmcnt(16)\n"
"ds_read_b64_tr_b16 a[148:149], v48 offset:18432\n"
"ds_read_b64_tr_b16 a[150:151], v48 offset:18688\n"
"ds_read_b64_tr_b16 a[180:181], v49 offset:18432\n"
"ds_read_b64_tr_b16 a[182:183], v49 offset:18688\n"
"s_waitcnt lgkmcnt(4)\n"
"v_mfma_f32_16x16x32_bf16 v[200:203], a[84:87], v[188:191], v[200:203]\n"
"v_mfma_f32_16x16x32_bf16 v[204:207], a[116:119], v[188:191], v[204:207]\n"
"s_waitcnt vmcnt(14)\n"
"ds_read_b64_tr_b16 a[24:25], v48 offset:20480\n"
"ds_read_b64_tr_b16 a[26:27], v48 offset:20736\n"
"ds_read_b64_tr_b16 a[56:57], v49 offset:20480\n"
"ds_read_b64_tr_b16 a[58:59], v49 offset:20736\n"
"s_waitcnt lgkmcnt(4)\n"
"v_mfma_f32_16x16x32_bf16 v[208:211], v[188:191], a[148:151], v[208:211]\n"
"v_mfma_f32_16x16x32_bf16 v[212:215], v[188:191], a[180:183], v[212:215]\n"
"s_waitcnt vmcnt(12)\n"
"ds_read_b64_tr_b16 a[88:89], v48 offset:22528\n"
"ds_read_b64_tr_b16 a[90:91], v48 offset:22784\n"
"ds_read_b64_tr_b16 a[120:121], v49 offset:22528\n"
"ds_read_b64_tr_b16 a[122:123], v49 offset:22784\n"
"s_waitcnt lgkmcnt(4)\n"
"v_mfma_f32_16x16x32_bf16 v[192:195], a[24:27], v[188:191], v[192:195]\n"
"v_mfma_f32_16x16x32_bf16 v[196:199], a[56:59], v[188:191], v[196:199]\n"
"s_waitcnt vmcnt(10)\n"
"ds_read_b64_tr_b16 a[152:153], v48 offset:24576\n"
"ds_read_b64_tr_b16 a[154:155], v48 offset:24832\n"
"ds_read_b64_tr_b16 a[184:185], v49 offset:24576\n"
"ds_read_b64_tr_b16 a[186:187], v49 offset:24832\n"
"s_waitcnt lgkmcnt(4)\n"
"v_mfma_f32_16x16x32_bf16 v[200:203], a[88:91], v[188:191], v[200:203]\n"
"v_mfma_f32_16x16x32_bf16 v[204:207], a[120:123], v[188:191], v[204:207]\n"
"s_waitcnt vmcnt(8)\n"
"ds_read_b64_tr_b16 a[28:29], v48 offset:26624\n"
"ds_read_b64_tr_b16 a[30:31], v48 offset:26880\n"
"ds_read_b64_tr_b16 a[60:61], v49 offset:26624\n"
"ds_read_b64_tr_b16 a[62:63], v49 offset:26880\n"
"s_waitcnt lgkmcnt(4)\n"
"v_mfma_f32_16x16x32_bf16 v[208:211], v[188:191], a[152:155], v[208:211]\n"
"v_mfma_f32_16x16x32_bf16 v[212:215], v[188:191], a[184:187], v[212:215]\n"
"s_waitcnt vmcnt(6)\n"
"ds_read_b64_tr_b16 a[92:93], v48 offset:28672\n"
"ds_read_b64_tr_b16 a[94:95], v48 offset:28928\n"
"ds_read_b64_tr_b16 a[124:125], v49 offset:28672\n"
"ds_read_b64_tr_b16 a[126:127], v49 offset:28928\n"
"s_waitcnt lgkmcnt(4)\n"
"v_mfma_f32_16x16x32_bf16 v[192:195], a[28:31], v[188:191], v[192:195]\n"
"v_mfma_f32_16x16x32_bf16 v[196:199], a[60:63], v[188:191], v[196:199]\n"
"s_waitcnt vmcnt(4)\n"
"ds_read_b64_tr_b16 a[156:157], v48 offset:30720\n"
"ds_read_b64_tr_b16 a[158:159], v48 offset:30976\n"
"ds_read_b64_tr_b16 a[188:189], v49 offset:30720\n"
"ds_read_b64_tr_b16 a[190:191], v49 offset:30976\n"
"s_waitcnt lgkmcnt(4)\n"
"v_mfma_f32_16x16x32_bf16 v[200:203], a[92:95], v[188:191], v[200:203]\n"
"v_mfma_f32_16x16x32_bf16 v[204:207], a[124:127], v[188:191], v[204:207]\n"
"s_waitcnt lgkmcnt(0)\n"
"v_mfma_f32_16x16x32_bf16 v[208:211], v[188:191], a[156:159], v[208:211]\n"
"v_mfma_f32_16x16x32_bf16 v[212:215], v[188:191], a[188:191], v[212:215]\n"
"s_mov_b32 m0, s59\n"
"ds_write_b128 v58, v[192:195] offset:0\n"
"ds_write_b128 v58, v[196:199] offset:1024\n"
"ds_write_b128 v58, v[200:203] offset:2048\n"
"ds_write_b128 v58, v[204:207] offset:3072\n"
"s_nop 2\n"
"v_mov_b32 v209, v212\n"
"ds_write_b64 v58, v[208:209] offset:4096\n"
"s_waitcnt lgkmcnt(0)\n"
"s_barrier\n"
"ds_read_b128 v[128:131], v59 offset:0\n"
"ds_read_b128 v[132:135], v60 offset:0\n"
"ds_read_b128 v[136:139], v61 offset:0\n"
"ds_read_b128 v[140:143], v59 offset:1024\n"
"ds_read_b128 v[144:147], v60 offset:1024\n"
"ds_read_b128 v[148:151], v61 offset:1024\n"
"ds_read_b128 v[152:155], v59 offset:2048\n"
"ds_read_b128 v[156:159], v60 offset:2048\n"
"ds_read_b128 v[160:163], v61 offset:2048\n"
"ds_read_b128 v[164:167], v59 offset:3072\n"
"ds_read_b128 v[168:171], v60 offset:3072\n"
"ds_read_b128 v[172:175], v61 offset:3072\n"
"ds_read_b64 v[176:177], v59 offset:4096\n"
"ds_read_b64 v[180:181], v60 offset:4096\n"
"ds_read_b64 v[184:185], v61 offset:4096\n"
"s_waitcnt lgkmcnt(12)\n"
"v_add_f32 v128, v128, v136\n"
"v_add_f32 v129, v129, v137\n"
"v_add_f32 v130, v130, v138\n"
"v_add_f32 v131, v131, v139\n"
"v_add_f32 v132, v132, v192\n"
"v_add_f32 v133, v133, v193\n"
"v_add_f32 v134, v134, v194\n"
"v_add_f32 v135, v135, v195\n"
"v_add_f32 v28, v128, v132\n"
"v_add_f32 v29, v129, v133\n"
"v_add_f32 v30, v130, v134\n"
"v_add_f32 v31, v131, v135\n"
"s_waitcnt lgkmcnt(9)\n"
"v_add_f32 v140, v140, v148\n"
"v_add_f32 v141, v141, v149\n"
"v_add_f32 v142, v142, v150\n"
"v_add_f32 v143, v143, v151\n"
"v_add_f32 v144, v144, v196\n"
"v_add_f32 v145, v145, v197\n"
"v_add_f32 v146, v146, v198\n"
"v_add_f32 v147, v147, v199\n"
"v_add_f32 v32, v140, v144\n"
"v_add_f32 v33, v141, v145\n"
"v_add_f32 v34, v142, v146\n"
"v_add_f32 v35, v143, v147\n"
"s_waitcnt lgkmcnt(6)\n"
"v_add_f32 v152, v152, v160\n"
"v_add_f32 v153, v153, v161\n"
"v_add_f32 v154, v154, v162\n"
"v_add_f32 v155, v155, v163\n"
"v_add_f32 v156, v156, v200\n"
"v_add_f32 v157, v157, v201\n"
"v_add_f32 v158, v158, v202\n"
"v_add_f32 v159, v159, v203\n"
"v_add_f32 v36, v152, v156\n"
"v_add_f32 v37, v153, v157\n"
"v_add_f32 v38, v154, v158\n"
"v_add_f32 v39, v155, v159\n"
"s_waitcnt lgkmcnt(3)\n"
"v_add_f32 v164, v164, v172\n"
"v_add_f32 v165, v165, v173\n"
"v_add_f32 v166, v166, v174\n"
"v_add_f32 v167, v167, v175\n"
"v_add_f32 v168, v168, v204\n"
"v_add_f32 v169, v169, v205\n"
"v_add_f32 v170, v170, v206\n"
"v_add_f32 v171, v171, v207\n"
"v_add_f32 v40, v164, v168\n"
"v_add_f32 v41, v165, v169\n"
"v_add_f32 v42, v166, v170\n"
"v_add_f32 v43, v167, v171\n"
"s_waitcnt lgkmcnt(0)\n"
"v_add_f32 v176, v176, v184\n"
"v_add_f32 v177, v177, v185\n"
"v_add_f32 v180, v180, v208\n"
"v_add_f32 v181, v181, v209\n"
"v_add_f32 v44, v176, v180\n"
"v_add_f32 v45, v177, v181\n"
"s_mov_b32 s94, 0x80000000\n"
"s_mov_b32 s95, 0x80000000\n"
"v_mov_b32 v144, 0\n"
"v_mov_b32 v145, 0\n"
"v_mov_b32 v146, 0\n"
"v_mov_b32 v147, 0\n"
"v_mov_b32 v152, 0\n"
"v_mov_b32 v153, 0\n"
"v_mov_b32 v154, 0\n"
"v_mov_b32 v155, 0\n"
"v_mov_b32 v156, 0\n"
"v_mov_b32 v157, 0\n"
"v_mov_b32 v158, 0\n"
"v_mov_b32 v159, 0\n"
"v_mov_b32 v180, 0\n"
"v_mov_b32 v181, 0\n"
"v_mov_b32 v182, 0\n"
"v_mov_b32 v183, 0\n"
"s_mov_b32 s61, 200000\n"
"s_waitcnt vmcnt(0)\n"
".LQ_pc:\n"
"v_cmp_ne_u64 vcc, s[48:49], v[234:235]\n"
"v_cmp_ne_u64 s[28:29], s[48:49], v[238:239]\n"
"s_or_b64 vcc, vcc, s[28:29]\n"
"s_cbranch_scc1 .LQ_pp\n"
"s_add_u32 s56, s56, 0x200000\n"
"s_addc_u32 s57, s57, 0\n"
"s_add_u32 s50, s50, 0x200000\n"
"v_cmp_ne_u64 vcc, s[48:49], v[242:243]\n"
"s_cmp_lg_u32 s54, 0\n"
"s_cbranch_scc1 .LQ_v537_0\n"
"v_mov_b32 v144, v232\n"
"v_mov_b32 v145, v233\n"
"v_mov_b32 v152, v236\n"
"v_mov_b32 v153, v237\n"
"v_mov_b32 v156, v240\n"
"v_mov_b32 v157, v241\n"
"v_mov_b32 v158, v242\n"
"v_mov_b32 v159, v243\n"
"v_mov_b32 v180, v244\n"
"v_mov_b32 v181, v245\n"
"v_mov_b32 v182, v246\n"
"v_mov_b32 v183, v247\n"
"s_cbranch_vccnz .LQ_3s\n"
"s_branch .LQ_3r\n"
"s_branch .LQ_v537_3\n"
".LQ_v537_0:\n"
"s_cmp_lg_u32 s54, 1\n"
"s_cbranch_scc1 .LQ_v537_1\n"
"v_mov_b32 v152, v232\n"
"v_mov_b32 v153, v233\n"
"v_mov_b32 v156, v236\n"
"v_mov_b32 v157, v237\n"
"v_mov_b32 v180, v240\n"
"v_mov_b32 v181, v241\n"
"v_mov_b32 v182, v242\n"
"v_mov_b32 v183, v243\n"
"v_mov_b32 v144, v244\n"
"v_mov_b32 v145, v245\n"
"v_mov_b32 v146, v246\n"
"v_mov_b32 v147, v247\n"
"s_cbranch_vccnz .LQ_4s\n"
"s_branch .LQ_4r\n"
"s_branch .LQ_v537_3\n"
".LQ_v537_1:\n"
"s_cmp_lg_u32 s54, 2\n"
"s_cbranch_scc1 .LQ_v537_2\n"
"v_mov_b32 v156, v232\n"
"v_mov_b32 v157, v233\n"
"v_mov_b32 v180, v236\n"
"v_mov_b32 v181, v237\n"
"v_mov_b32 v144, v240\n"
"v_mov_b32 v145, v241\n"
"v_mov_b32 v146, v242\n"
"v_mov_b32 v147, v243\n"
"v_mov_b32 v152, v244\n"
"v_mov_b32 v153, v245\n"
"v_mov_b32 v154, v246\n"
"v_mov_b32 v155, v247\n"
"s_cbranch_vccnz .LQ_1s\n"
"s_branch .LQ_1r\n"
"s_branch .LQ_v537_3\n"
".LQ_v537_2:\n"
"v_mov_b32 v180, v232\n"
"v_mov_b32 v181, v233\n"
"v_mov_b32 v144, v236\n"
"v_mov_b32 v145, v237\n"
"v_mov_b32 v152, v240\n"
"v_mov_b32 v153, v241\n"
"v_mov_b32 v154, v242\n"
"v_mov_b32 v155, v243\n"
"v_mov_b32 v156, v244\n"
"v_mov_b32 v157, v245\n"
"v_mov_b32 v158, v246\n"
"v_mov_b32 v159, v247\n"
"v_mfma_f32_16x16x32_bf16 v[108:111], a[224:227], a[64:67], 0\n"
"s_cbranch_vccnz .LQ_2s\n"
"s_branch .LQ_2r\n"
".LQ_v537_3:\n"
".LQ_pp:\n"
"s_sleep 1\n"
"buffer_load_dwordx4 v[232:235], v50, s[40:43], s50 offen sc1\n"
"buffer_load_dwordx4 v[236:239], v51, s[40:43], s50 offen sc1\n"
"buffer_load_dwordx4 v[240:243], v52, s[40:43], s50 offen sc1\n"
"buffer_load_dwordx4 v[244:247], v53, s[40:43], s50 offen sc1\n"
"s_sub_u32 s61, s61, 1\n"
"s_waitcnt vmcnt(0)\n"
"s_cbranch_scc0 .LQ_pc\n"
"s_branch .LQ_end\n"
".p2align 6\n"
".LQ_top:\n"
"v_mfma_f32_16x16x32_bf16 v[128:131], a[128:131], v[64:67], 0\n"
"s_add_u32 s50, s50, 0x100000\n"
"global_load_dwordx4 a[192:195], v5, s[56:57] offset:0 sc1\n"
"v_mfma_f32_16x16x32_bf16 v[128:131], a[132:135], v[68:71], v[128:131]\n"
"ds_write_addtid_b32 v168 offset:4352\n"
"v_mfma_f32_16x16x32_bf16 v[128:131], a[136:139], v[72:75], v[128:131]\n"
"ds_write_addtid_b32 v169 offset:8992\n"
"v_mfma_f32_16x16x32_bf16 v[128:131], a[140:143], v[76:79], v[128:131]\n"
"s_waitcnt vmcnt(12)\n"
"ds_write_addtid_b32 v170 offset:13696\n"
"v_mfma_f32_16x16x32_bf16 v[128:131], a[144:147], v[80:83], v[128:131]\n"
"ds_write_addtid_b32 v171 offset:18336\n"
"v_mov_b32 v254, v144\n"
"v_mfma_f32_16x16x32_bf16 v[128:131], a[148:151], v[84:87], v[128:131]\n"
"s_waitcnt vmcnt(10)\n"
"global_load_dwordx4 a[196:199], v5, s[56:57] offset:1024 sc1\n"
"v_mul_f32 v135, v145, v39\n"
"v_mfma_f32_16x16x32_bf16 v[128:131], a[152:155], v[88:91], v[128:131]\n"
"v_mul_f32 v133, v145, v37\n"
"v_mul_f32 v134, v145, v38\n"
"v_mfma_f32_16x16x32_bf16 v[128:131], a[156:159], v[92:95], v[128:131]\n"
"v_mul_f32 v139, v145, v43\n"
"v_mul_f32 v132, v145, v36\n"
"v_mfma_f32_16x16x32_bf16 v[140:143], a[160:163], v[64:67], 0\n"
"v_mul_f32 v137, v145, v41\n"
"v_mul_f32 v138, v145, v42\n"
"v_mfma_f32_16x16x32_bf16 v[140:143], a[164:167], v[68:71], v[140:143]\n"
"ds_write_addtid_b32 v128 offset:0\n"
"v_mul_f32 v136, v145, v40\n"
"v_mfma_f32_16x16x32_bf16 v[140:143], a[168:171], v[72:75], v[140:143]\n"
"s_waitcnt lgkmcnt(1)\n"
"ds_write_addtid_b32 v129 offset:4640\n"
"global_load_dwordx4 a[200:203], v6, s[56:57] offset:0 sc1\n"
"v_mfma_f32_16x16x32_bf16 v[140:143], a[172:175], v[76:79], v[140:143]\n"
"s_barrier\n"
"v_mfma_f32_16x16x32_bf16 v[140:143], a[176:179], v[80:83], v[140:143]\n"
"ds_write_addtid_b32 v130 offset:9344\n"
"ds_read2st64_b32 v[144:145], v12 offset0:0 offset1:1\n"
"v_mfma_f32_16x16x32_bf16 v[140:143], a[180:183], v[84:87], v[140:143]\n"
"ds_write_addtid_b32 v131 offset:13984\n"
"ds_read2st64_b32 v[146:147], v12 offset0:2 offset1:3\n"
"v_mfma_f32_16x16x32_bf16 v[140:143], a[184:187], v[88:91], v[140:143]\n"
"ds_read2st64_b32 v[160:161], v11 offset0:4 offset1:5\n"
"ds_read2st64_b32 v[164:165], v11 offset0:6 offset1:7\n"
"v_mfma_f32_16x16x32_bf16 v[140:143], a[188:191], v[92:95], v[140:143]\n"
"s_add_u32 s94, s50, 0xffc00000\n"
"s_and_b32 s95, s94, 0xfbffffff\n"
"v_mfma_f32_16x16x32_bf16 v[128:131], v[64:67], a[0:3], 0\n"
"global_load_dwordx4 a[204:207], v6, s[56:57] offset:1024 sc1\n"
"v_mfma_f32_16x16x32_bf16 v[128:131], v[68:71], a[4:7], v[128:131]\n"
"ds_write_addtid_b32 v140 offset:256\n"
"v_mfma_f32_16x16x32_bf16 v[128:131], v[72:75], a[8:11], v[128:131]\n"
"s_waitcnt lgkmcnt(3)\n"
"ds_write_addtid_b32 v141 offset:4896\n"
"v_mfma_f32_16x16x32_bf16 v[148:151], v[144:147], v[184:187], v[16:19]\n"
"s_cmp_eq_u32 s94, s62\n"
"s_cbranch_scc1 .LQ_fs\n"
".LQ_fr:\n"
"v_mfma_f32_16x16x32_bf16 v[128:131], v[76:79], a[12:15], v[128:131]\n"
"ds_write_addtid_b32 v142 offset:9600\n"
"v_max_f32 v253, v172, v174\n"
"v_mfma_f32_16x16x32_bf16 v[128:131], v[80:83], a[16:19], v[128:131]\n"
"ds_write_addtid_b32 v143 offset:14240\n"
"global_load_dwordx4 a[208:211], v7, s[56:57] offset:0 sc1\n"
"v_mfma_f32_16x16x32_bf16 v[128:131], v[84:87], a[20:23], v[128:131]\n"
"v_max_f32 v252, v173, v175\n"
"v_max3_f32 v253, v148, v150, v253\n"
"v_mfma_f32_16x16x32_bf16 v[128:131], v[88:91], a[24:27], v[128:131]\n"
"v_max3_f32 v252, v149, v151, v252\n"
"v_max_f32 v251, v253, v252\n"
"v_mfma_f32_16x16x32_bf16 v[128:131], v[92:95], a[28:31], v[128:131]\n"
"v_mov_b32 v250, v251\n"
"v_mfma_f32_16x16x32_bf16 v[140:143], v[64:67], a[32:35], 0\n"
"buffer_load_dwordx4 v[144:147], v9, s[40:43], s50 offen sc1\n"
"v_mfma_f32_16x16x32_bf16 v[140:143], v[68:71], a[36:39], v[140:143]\n"
"ds_write_addtid_b32 v128 offset:512\n"
"global_load_dwordx4 a[212:215], v7, s[56:57] offset:1024 sc1\n"
"v_mfma_f32_16x16x32_bf16 v[140:143], v[72:75], a[40:43], v[140:143]\n"
"ds_write_addtid_b32 v129 offset:5152\n"
"v_mfma_f32_16x16x32_bf16 v[140:143], v[76:79], a[44:47], v[140:143]\n"
"ds_write_addtid_b32 v130 offset:9856\n"
"v_mfma_f32_16x16x32_bf16 v[140:143], v[80:83], a[48:51], v[140:143]\n"
"ds_write_addtid_b32 v131 offset:14496\n"
"v_mfma_f32_16x16x32_bf16 v[140:143], v[84:87], a[52:55], v[140:143]\n"
"v_permlane32_swap_b32 v250, v251\n"
"v_mfma_f32_16x16x32_bf16 v[140:143], v[88:91], a[56:59], v[140:143]\n"
"global_load_dwordx4 a[216:219], v8, s[56:57] offset:0 sc1\n"
"v_max_f32 v251, v250, v251\n"
"v_mfma_f32_16x16x32_bf16 v[140:143], v[92:95], a[60:63], v[140:143]\n"
"v_mov_b32 v250, v251\n"
"v_mfma_f32_16x16x32_bf16 v[132:135], a[64:67], v[64:67], v[132:135]\n"
"v_permlane16_swap_b32 v250, v251\n"
"v_mfma_f32_16x16x32_bf16 v[132:135], a[68:71], v[68:71], v[132:135]\n"
"ds_write_addtid_b32 v140 offset:768\n"
"v_max_f32 v251, v250, v251\n"
"v_mfma_f32_16x16x32_bf16 v[132:135], a[72:75], v[72:75], v[132:135]\n"
"ds_write_addtid_b32 v141 offset:5408\n"
"v_mul_f32 v251, v251, v249\n"
"v_mfma_f32_16x16x32_bf16 v[132:135], a[76:79], v[76:79], v[132:135]\n"
"ds_write_addtid_b32 v142 offset:10112\n"
"global_load_dwordx4 a[220:223], v8, s[56:57] offset:1024 sc1\n"
"v_mfma_f32_16x16x32_bf16 v[132:135], a[80:83], v[80:83], v[132:135]\n"
"ds_write_addtid_b32 v143 offset:14752\n"
"v_fma_f32 v253, v253, v249, -v251\n"
"v_mfma_f32_16x16x32_bf16 v[132:135], a[84:87], v[84:87], v[132:135]\n"
"v_fma_f32 v252, v252, v249, -v251\n"
"v_mfma_f32_16x16x32_bf16 v[132:135], a[88:91], v[88:91], v[132:135]\n"
"v_exp_f32 v253, v253\n"
"v_mfma_f32_16x16x32_bf16 v[132:135], a[92:95], v[92:95], v[132:135]\n"
"v_exp_f32 v252, v252\n"
"v_mfma_f32_16x16x32_bf16 v[136:139], a[96:99], v[64:67], v[136:139]\n"
"s_add_u32 s58, s50, 0x100000\n"
"v_add_f32 v251, v253, v252\n"
"v_mov_b32 v250, v251\n"
"v_mfma_f32_16x16x32_bf16 v[136:139], a[100:103], v[68:71], v[136:139]\n"
"s_and_b32 s58, s58, 0x3ffffff\n"
"v_permlane32_swap_b32 v250, v251\n"
"v_mfma_f32_16x16x32_bf16 v[136:139], a[104:107], v[72:75], v[136:139]\n"
"s_add_u32 s56, s40, s58\n"
"v_add_f32 v251, v250, v251\n"
"v_mov_b32 v250, v251\n"
"v_mfma_f32_16x16x32_bf16 v[136:139], a[108:111], v[76:79], v[136:139]\n"
"s_addc_u32 s57, s41, 0\n"
"v_permlane16_swap_b32 v250, v251\n"
"v_mfma_f32_16x16x32_bf16 v[136:139], a[112:115], v[80:83], v[136:139]\n"
"s_waitcnt vmcnt(12)\n"
"v_cmp_ne_u64 vcc, s[48:49], v[182:183]\n"
"v_add_f32 v251, v250, v251\n"
"v_mfma_f32_16x16x32_bf16 v[136:139], a[116:119], v[84:87], v[136:139]\n"
"v_rcp_f32 v251, v251\n"
"v_mfma_f32_16x16x32_bf16 v[136:139], a[120:123], v[88:91], v[136:139]\n"
"s_cbranch_vccnz .LQ_4s\n"
".LQ_4r:\n"
"v_mul_f32 v253, v253, v251\n"
"v_mfma_f32_16x16x32_bf16 v[136:139], a[124:127], v[92:95], v[136:139]\n"
"global_load_dwordx4 a[224:227], v5, s[56:57] offset:0 sc1\n"
"v_mul_f32 v252, v252, v251\n"
"v_mfma_f32_16x16x32_bf16 v[64:67], v[96:99], a[64:67], 0\n"
"s_add_u32 s50, s50, 0x100000\n"
"v_cvt_pk_bf16_f32 v250, v253, v252\n"
"v_mfma_f32_16x16x32_bf16 v[64:67], v[100:103], a[68:71], v[64:67]\n"
"v_mfma_f32_16x16x32_bf16 v[64:67], v[104:107], a[72:75], v[64:67]\n"
"s_waitcnt lgkmcnt(12)\n"
"v_and_b32 v68, v250, v24\n"
"v_and_b32 v69, v250, v25\n"
"v_mfma_f32_16x16x32_bf16 v[64:67], v[108:111], a[76:79], v[64:67]\n"
"s_waitcnt vmcnt(12)\n"
"v_and_b32 v70, v250, v26\n"
"v_and_b32 v71, v250, v27\n"
"v_mfma_f32_16x16x32_bf16 v[64:67], v[112:115], a[80:83], v[64:67]\n"
"global_load_dwordx4 a[228:231], v5, s[56:57] offset:1024 sc1\n"
"v_mfma_f32_16x16x32_bf16 v[72:75], v[160:163], v[68:71], 0\n"
"v_mul_f32_dpp v95, v153, v44 row_newbcast:3 row_mask:0xf bank_mask:0xf\n"
"v_mov_b32 v253, v152\n"
"v_mfma_f32_16x16x32_bf16 v[76:79], v[164:167], v[68:71], 0\n"
"v_mul_f32_dpp v93, v153, v44 row_newbcast:1 row_mask:0xf bank_mask:0xf\n"
"v_mul_f32_dpp v94, v153, v44 row_newbcast:2 row_mask:0xf bank_mask:0xf\n"
"v_mfma_f32_16x16x32_bf16 v[64:67], v[116:119], a[84:87], v[64:67]\n"
"s_waitcnt vmcnt(11)\n"
"v_cvt_pk_bf16_f32 v68, v72, v73\n"
"v_cvt_pk_bf16_f32 v69, v74, v75\n"
"v_mfma_f32_16x16x32_bf16 v[64:67], v[120:123], a[88:91], v[64:67]\n"
"v_cvt_pk_bf16_f32 v70, v76, v77\n"
"v_cvt_pk_bf16_f32 v71, v78, v79\n"
"v_mfma_f32_16x16x32_bf16 v[64:67], v[124:127], a[92:95], v[64:67]\n"
"buffer_store_dwordx4 v[68:71], v13, s[40:43], s95 offen sc1\n"
"v_mul_f32_dpp v130, v153, v45 row_newbcast:2 row_mask:0xf bank_mask:0xf\n"
"v_mul_f32_dpp v131, v153, v45 row_newbcast:3 row_mask:0xf bank_mask:0xf\n"
"v_mul_f32_dpp v92, v153, v44 row_newbcast:0 row_mask:0xf bank_mask:0xf\n"
"v_mfma_f32_16x16x32_bf16 v[68:71], v[96:99], a[96:99], 0\n"
"s_waitcnt lgkmcnt(0)\n"
"v_mul_f32_dpp v128, v153, v45 row_newbcast:0 row_mask:0xf bank_mask:0xf\n"
"v_mul_f32_dpp v129, v153, v45 row_newbcast:1 row_mask:0xf bank_mask:0xf\n"
"v_mfma_f32_16x16x32_bf16 v[68:71], v[100:103], a[100:103], v[68:71]\n"
"s_barrier\n"
"v_mfma_f32_16x16x32_bf16 v[68:71], v[104:107], a[104:107], v[68:71]\n"
"ds_write_addtid_b32 v64 offset:1024\n"
"ds_read_b128 v[72:75], v2 offset:1024\n"
"v_mfma_f32_16x16x32_bf16 v[68:71], v[108:111], a[108:111], v[68:71]\n"
"ds_write_addtid_b32 v65 offset:5664\n"
"ds_read_b128 v[76:79], v3 offset:2048\n"
"v_mfma_f32_16x16x32_bf16 v[68:71], v[112:115], a[112:115], v[68:71]\n"
"ds_write_addtid_b32 v66 offset:10368\n"
"global_load_dwordx4 a[232:235], v6, s[56:57] offset:0 sc1\n"
"v_mfma_f32_16x16x32_bf16 v[68:71], v[116:119], a[116:119], v[68:71]\n"
"ds_write_addtid_b32 v67 offset:15008\n"
"ds_read_b128 v[80:83], v4 offset:3072\n"
"v_mfma_f32_16x16x32_bf16 v[68:71], v[120:123], a[120:123], v[68:71]\n"
"ds_read_b128 v[84:87], v2 offset:1280\n"
"ds_read_b128 v[64:67], v3 offset:2304\n"
"v_mfma_f32_16x16x32_bf16 v[68:71], v[124:127], a[124:127], v[68:71]\n"
"ds_read_b128 v[88:91], v4 offset:3328\n"
"v_mfma_f32_16x16x32_bf16 v[140:143], v[96:99], a[0:3], 0\n"
"global_load_dwordx4 a[236:239], v6, s[56:57] offset:1024 sc1\n"
"v_mfma_f32_16x16x32_bf16 v[140:143], v[100:103], a[4:7], v[140:143]\n"
"ds_write_addtid_b32 v68 offset:1280\n"
"v_mfma_f32_16x16x32_bf16 v[140:143], v[104:107], a[8:11], v[140:143]\n"
"ds_write_addtid_b32 v69 offset:5920\n"
"v_mfma_f32_16x16x32_bf16 v[140:143], v[108:111], a[12:15], v[140:143]\n"
"s_waitcnt lgkmcnt(5)\n"
"ds_write_addtid_b32 v70 offset:10624\n"
"v_add_f32 v72, v72, v76\n"
"v_mfma_f32_16x16x32_bf16 v[140:143], v[112:115], a[16:19], v[140:143]\n"
"ds_write_addtid_b32 v71 offset:15264\n"
"v_add_f32 v73, v73, v77\n"
"v_mfma_f32_16x16x32_bf16 v[140:143], v[116:119], a[20:23], v[140:143]\n"
"global_load_dwordx4 a[240:243], v7, s[56:57] offset:0 sc1\n"
"v_add_f32 v74, v74, v78\n"
"v_mfma_f32_16x16x32_bf16 v[140:143], v[120:123], a[24:27], v[140:143]\n"
"v_add_f32 v75, v75, v79\n"
"v_add_f32 v72, v72, v80\n"
"v_mfma_f32_16x16x32_bf16 v[140:143], v[124:127], a[28:31], v[140:143]\n"
"v_add_f32 v73, v73, v81\n"
"v_add_f32 v74, v74, v82\n"
"v_mfma_f32_16x16x32_bf16 v[68:71], v[96:99], a[32:35], 0\n"
"v_add_f32 v75, v75, v83\n"
"v_add_f32 v72, v72, v132\n"
"v_mfma_f32_16x16x32_bf16 v[68:71], v[100:103], a[36:39], v[68:71]\n"
"ds_write_addtid_b32 v140 offset:1536\n"
"v_add_f32 v73, v73, v133\n"
"v_mfma_f32_16x16x32_bf16 v[68:71], v[104:107], a[40:43], v[68:71]\n"
"ds_write_addtid_b32 v141 offset:6176\n"
"global_load_dwordx4 a[244:247], v7, s[56:57] offset:1024 sc1\n"
"v_mfma_f32_16x16x32_bf16 v[68:71], v[108:111], a[44:47], v[68:71]\n"
"ds_write_addtid_b32 v142 offset:10880\n"
"v_add_f32 v74, v74, v134\n"
"v_mfma_f32_16x16x32_bf16 v[68:71], v[112:115], a[48:51], v[68:71]\n"
"ds_write_addtid_b32 v143 offset:15520\n"
"v_add_f32 v75, v75, v135\n"
"v_mfma_f32_16x16x32_bf16 v[68:71], v[116:119], a[52:55], v[68:71]\n"
"v_mul_f32 v72, v254, v72\n"
"v_mul_f32 v73, v254, v73\n"
"v_mfma_f32_16x16x32_bf16 v[68:71], v[120:123], a[56:59], v[68:71]\n"
"v_mul_f32 v74, v254, v74\n"
"v_mul_f32 v75, v254, v75\n"
"v_mfma_f32_16x16x32_bf16 v[68:71], v[124:127], a[60:63], v[68:71]\n"
"global_load_dwordx4 a[248:251], v8, s[56:57] offset:0 sc1\n"
"v_cvt_pk_bf16_f32 v76, v72, v73\n"
"v_mfma_f32_16x16x32_bf16 v[92:95], v[96:99], a[128:131], v[92:95]\n"
"buffer_load_dwordx4 v[152:155], v10, s[40:43], s50 offen sc1\n"
"v_mfma_f32_16x16x32_bf16 v[92:95], v[100:103], a[132:135], v[92:95]\n"
"ds_write_addtid_b32 v68 offset:1792\n"
"v_cvt_pk_bf16_f32 v77, v74, v75\n"
"v_mfma_f32_16x16x32_bf16 v[92:95], v[104:107], a[136:139], v[92:95]\n"
"ds_write_addtid_b32 v69 offset:6432\n"
"ds_write_addtid_b32 v76 offset:18592\n"
"v_mfma_f32_16x16x32_bf16 v[92:95], v[108:111], a[140:143], v[92:95]\n"
"ds_write_addtid_b32 v70 offset:11136\n"
"ds_write_addtid_b32 v77 offset:18848\n"
"v_mfma_f32_16x16x32_bf16 v[92:95], v[112:115], a[144:147], v[92:95]\n"
"s_waitcnt lgkmcnt(13)\n"
"ds_write_addtid_b32 v71 offset:15776\n"
"global_load_dwordx4 a[252:255], v8, s[56:57] offset:1024 sc1\n"
"v_mfma_f32_16x16x32_bf16 v[92:95], v[116:119], a[148:151], v[92:95]\n"
"v_add_f32 v84, v84, v64\n"
"v_add_f32 v85, v85, v65\n"
"v_mfma_f32_16x16x32_bf16 v[92:95], v[120:123], a[152:155], v[92:95]\n"
"v_add_f32 v86, v86, v66\n"
"v_add_f32 v87, v87, v67\n"
"v_mfma_f32_16x16x32_bf16 v[92:95], v[124:127], a[156:159], v[92:95]\n"
"v_add_f32 v84, v84, v88\n"
"v_add_f32 v85, v85, v89\n"
"v_mfma_f32_16x16x32_bf16 v[128:131], v[96:99], a[160:163], v[128:131]\n"
"s_add_u32 s58, s50, 0x100000\n"
"v_add_f32 v86, v86, v90\n"
"v_add_f32 v87, v87, v91\n"
"v_mfma_f32_16x16x32_bf16 v[128:131], v[100:103], a[164:167], v[128:131]\n"
"s_and_b32 s58, s58, 0x3ffffff\n"
"v_add_f32 v84, v84, v136\n"
"v_add_f32 v85, v85, v137\n"
"v_mfma_f32_16x16x32_bf16 v[128:131], v[104:107], a[168:171], v[128:131]\n"
"s_add_u32 s56, s40, s58\n"
"v_add_f32 v86, v86, v138\n"
"v_add_f32 v87, v87, v139\n"
"v_mfma_f32_16x16x32_bf16 v[128:131], v[108:111], a[172:175], v[128:131]\n"
"s_addc_u32 s57, s41, 0\n"
"v_mul_f32 v84, v254, v84\n"
"v_mul_f32 v85, v254, v85\n"
"v_mfma_f32_16x16x32_bf16 v[128:131], v[112:115], a[176:179], v[128:131]\n"
"s_waitcnt vmcnt(13)\n"
"v_cmp_ne_u64 vcc, s[48:49], v[146:147]\n"
"v_mul_f32 v86, v254, v86\n"
"v_mfma_f32_16x16x32_bf16 v[128:131], v[116:119], a[180:183], v[128:131]\n"
"v_mul_f32 v87, v254, v87\n"
"v_cvt_pk_bf16_f32 v78, v84, v85\n"
"v_mfma_f32_16x16x32_bf16 v[128:131], v[120:123], a[184:187], v[128:131]\n"
"s_cbranch_vccnz .LQ_1s\n"
".LQ_1r:\n"
"v_cvt_pk_bf16_f32 v79, v86, v87\n"
"v_mfma_f32_16x16x32_bf16 v[128:131], v[124:127], a[188:191], v[128:131]\n"
"ds_write_addtid_b32 v78 offset:19104\n"
"ds_write_addtid_b32 v79 offset:19360\n"
"v_mfma_f32_16x16x32_bf16 v[80:83], a[192:195], a[64:67], 0\n"
"s_add_u32 s50, s50, 0x100000\n"
"global_load_dwordx4 v[64:67], v5, s[56:57] offset:0 sc1\n"
"v_mfma_f32_16x16x32_bf16 v[80:83], a[196:199], a[68:71], v[80:83]\n"
"v_mfma_f32_16x16x32_bf16 v[80:83], a[200:203], a[72:75], v[80:83]\n"
"v_mfma_f32_16x16x32_bf16 v[80:83], a[204:207], a[76:79], v[80:83]\n"
"s_waitcnt vmcnt(13)\n"
"v_mul_f32 v249, 0x3e8293ee, v156\n"
"v_mfma_f32_16x16x32_bf16 v[80:83], a[208:211], a[80:83], v[80:83]\n"
"v_mul_f32 v126, v157, v30\n"
"v_mul_f32 v127, v157, v31\n"
"v_mfma_f32_16x16x32_bf16 v[80:83], a[212:215], a[84:87], v[80:83]\n"
"s_waitcnt vmcnt(11)\n"
"global_load_dwordx4 v[68:71], v5, s[56:57] offset:1024 sc1\n"
"v_mul_f32 v125, v157, v29\n"
"v_mfma_f32_16x16x32_bf16 v[80:83], a[216:219], a[88:91], v[80:83]\n"
"v_mul_f32 v135, v157, v35\n"
"v_mul_f32 v124, v157, v28\n"
"v_mfma_f32_16x16x32_bf16 v[80:83], a[220:223], a[92:95], v[80:83]\n"
"s_waitcnt vmcnt(9)\n"
"buffer_store_dwordx2 v[0:1], v14, s[40:43], s94 offen offset:0 sc1\n"
"v_mul_f32 v134, v157, v34\n"
"v_mfma_f32_16x16x32_bf16 v[84:87], a[192:195], a[96:99], 0\n"
"s_waitcnt lgkmcnt(0)\n"
"v_mul_f32 v132, v157, v32\n"
"v_mul_f32 v133, v157, v33\n"
"v_mfma_f32_16x16x32_bf16 v[84:87], a[196:199], a[100:103], v[84:87]\n"
"s_barrier\n"
"v_mfma_f32_16x16x32_bf16 v[84:87], a[200:203], a[104:107], v[84:87]\n"
"ds_write_addtid_b32 v80 offset:2048\n"
"global_load_dwordx4 v[72:75], v6, s[56:57] offset:0 sc1\n"
"v_mfma_f32_16x16x32_bf16 v[84:87], a[204:207], a[108:111], v[84:87]\n"
"ds_write_addtid_b32 v81 offset:6688\n"
"ds_read_b128 v[96:99], v2 offset:2560\n"
"v_mfma_f32_16x16x32_bf16 v[84:87], a[208:211], a[112:115], v[84:87]\n"
"ds_write_addtid_b32 v82 offset:11392\n"
"ds_read_b128 v[88:91], v3 offset:3584\n"
"v_mfma_f32_16x16x32_bf16 v[84:87], a[212:215], a[116:119], v[84:87]\n"
"ds_write_addtid_b32 v83 offset:16032\n"
"ds_read_b128 v[100:103], v4 offset:0\n"
"v_mfma_f32_16x16x32_bf16 v[84:87], a[216:219], a[120:123], v[84:87]\n"
"ds_read_b128 v[104:107], v2 offset:2816\n"
"ds_read_b128 v[108:111], v3 offset:3840\n"
"v_mfma_f32_16x16x32_bf16 v[84:87], a[220:223], a[124:127], v[84:87]\n"
"global_load_dwordx4 v[76:79], v6, s[56:57] offset:1024 sc1\n"
"ds_read_b128 v[112:115], v4 offset:256\n"
"v_mfma_f32_16x16x32_bf16 v[116:119], a[128:131], a[192:195], 0\n"
"v_mfma_f32_16x16x32_bf16 v[116:119], a[132:135], a[196:199], v[116:119]\n"
"ds_write_addtid_b32 v84 offset:2304\n"
"v_mfma_f32_16x16x32_bf16 v[116:119], a[136:139], a[200:203], v[116:119]\n"
"ds_write_addtid_b32 v85 offset:6944\n"
"v_mfma_f32_16x16x32_bf16 v[116:119], a[140:143], a[204:207], v[116:119]\n"
"s_waitcnt lgkmcnt(5)\n"
"ds_write_addtid_b32 v86 offset:11648\n"
"v_add_f32 v96, v96, v88\n"
"v_mfma_f32_16x16x32_bf16 v[116:119], a[144:147], a[208:211], v[116:119]\n"
"ds_write_addtid_b32 v87 offset:16288\n"
"global_load_dwordx4 v[80:83], v7, s[56:57] offset:0 sc1\n"
"v_mfma_f32_16x16x32_bf16 v[116:119], a[148:151], a[212:215], v[116:119]\n"
"v_add_f32 v97, v97, v89\n"
"v_add_f32 v98, v98, v90\n"
"v_mfma_f32_16x16x32_bf16 v[116:119], a[152:155], a[216:219], v[116:119]\n"
"v_add_f32 v99, v99, v91\n"
"v_add_f32 v96, v96, v100\n"
"v_mfma_f32_16x16x32_bf16 v[116:119], a[156:159], a[220:223], v[116:119]\n"
"v_add_f32 v97, v97, v101\n"
"v_add_f32 v98, v98, v102\n"
"v_mfma_f32_16x16x32_bf16 v[120:123], a[160:163], a[192:195], 0\n"
"buffer_load_dwordx4 v[156:159], v9, s[40:43], s50 offen sc1\n"
"v_mfma_f32_16x16x32_bf16 v[120:123], a[164:167], a[196:199], v[120:123]\n"
"ds_write_addtid_b32 v116 offset:2560\n"
"global_load_dwordx4 v[84:87], v7, s[56:57] offset:1024 sc1\n"
"v_mfma_f32_16x16x32_bf16 v[120:123], a[168:171], a[200:203], v[120:123]\n"
"ds_write_addtid_b32 v117 offset:7200\n"
"v_add_f32 v99, v99, v103\n"
"v_mfma_f32_16x16x32_bf16 v[120:123], a[172:175], a[204:207], v[120:123]\n"
"ds_write_addtid_b32 v118 offset:11904\n"
"v_add_f32 v96, v96, v92\n"
"v_mfma_f32_16x16x32_bf16 v[120:123], a[176:179], a[208:211], v[120:123]\n"
"ds_write_addtid_b32 v119 offset:16544\n"
"v_add_f32 v97, v97, v93\n"
"v_mfma_f32_16x16x32_bf16 v[120:123], a[180:183], a[212:215], v[120:123]\n"
"v_add_f32 v98, v98, v94\n"
"v_add_f32 v99, v99, v95\n"
"v_mfma_f32_16x16x32_bf16 v[120:123], a[184:187], a[216:219], v[120:123]\n"
"global_load_dwordx4 v[88:91], v8, s[56:57] offset:0 sc1\n"
"v_mul_f32_dpp v96, v253, v96 row_newbcast:0 row_mask:0xf bank_mask:0xf\n"
"v_mfma_f32_16x16x32_bf16 v[120:123], a[188:191], a[220:223], v[120:123]\n"
"v_mul_f32_dpp v97, v253, v97 row_newbcast:1 row_mask:0xf bank_mask:0xf\n"
"v_mul_f32_dpp v98, v253, v98 row_newbcast:2 row_mask:0xf bank_mask:0xf\n"
"v_mfma_f32_16x16x32_bf16 v[124:127], a[0:3], a[192:195], v[124:127]\n"
"v_mul_f32_dpp v99, v253, v99 row_newbcast:3 row_mask:0xf bank_mask:0xf\n"
"v_cvt_pk_bf16_f32 v162, v96, v97\n"
"v_mfma_f32_16x16x32_bf16 v[124:127], a[4:7], a[196:199], v[124:127]\n"
"ds_write_addtid_b32 v120 offset:2816\n"
"v_cvt_pk_bf16_f32 v163, v98, v99\n"
"v_mfma_f32_16x16x32_bf16 v[124:127], a[8:11], a[200:203], v[124:127]\n"
"ds_write_addtid_b32 v121 offset:7456\n"
"ds_write_addtid_b32 v162 offset:19616\n"
"v_mfma_f32_16x16x32_bf16 v[124:127], a[12:15], a[204:207], v[124:127]\n"
"ds_write_addtid_b32 v122 offset:12160\n"
"global_load_dwordx4 v[92:95], v8, s[56:57] offset:1024 sc1\n"
"v_mfma_f32_16x16x32_bf16 v[124:127], a[16:19], a[208:211], v[124:127]\n"
"ds_write_addtid_b32 v123 offset:16800\n"
"ds_write_addtid_b32 v163 offset:19872\n"
"v_mfma_f32_16x16x32_bf16 v[124:127], a[20:23], a[212:215], v[124:127]\n"
"s_waitcnt lgkmcnt(14)\n"
"v_add_f32 v104, v104, v108\n"
"v_add_f32 v105, v105, v109\n"
"v_mfma_f32_16x16x32_bf16 v[124:127], a[24:27], a[216:219], v[124:127]\n"
"v_add_f32 v106, v106, v110\n"
"v_add_f32 v107, v107, v111\n"
"v_mfma_f32_16x16x32_bf16 v[124:127], a[28:31], a[220:223], v[124:127]\n"
"v_add_f32 v104, v104, v112\n"
"v_add_f32 v105, v105, v113\n"
"v_mfma_f32_16x16x32_bf16 v[132:135], a[32:35], a[192:195], v[132:135]\n"
"v_add_f32 v106, v106, v114\n"
"v_add_f32 v107, v107, v115\n"
"v_mfma_f32_16x16x32_bf16 v[132:135], a[36:39], a[196:199], v[132:135]\n"
"v_add_f32 v104, v104, v128\n"
"v_add_f32 v105, v105, v129\n"
"v_mfma_f32_16x16x32_bf16 v[132:135], a[40:43], a[200:203], v[132:135]\n"
"s_add_u32 s58, s50, 0x100000\n"
"v_add_f32 v106, v106, v130\n"
"v_add_f32 v107, v107, v131\n"
"v_mfma_f32_16x16x32_bf16 v[132:135], a[44:47], a[204:207], v[132:135]\n"
"s_and_b32 s58, s58, 0x3ffffff\n"
"v_mul_f32_dpp v104, v253, v104 row_newbcast:0 row_mask:0xf bank_mask:0xf\n"
"v_mul_f32_dpp v105, v253, v105 row_newbcast:1 row_mask:0xf bank_mask:0xf\n"
"v_mfma_f32_16x16x32_bf16 v[132:135], a[48:51], a[208:211], v[132:135]\n"
"s_add_u32 s56, s40, s58\n"
"v_mul_f32_dpp v106, v253, v106 row_newbcast:2 row_mask:0xf bank_mask:0xf\n"
"v_mul_f32_dpp v107, v253, v107 row_newbcast:3 row_mask:0xf bank_mask:0xf\n"
"v_mfma_f32_16x16x32_bf16 v[132:135], a[52:55], a[212:215], v[132:135]\n"
"s_addc_u32 s57, s41, 0\n"
"v_cvt_pk_bf16_f32 v166, v104, v105\n"
"v_cvt_pk_bf16_f32 v167, v106, v107\n"
"v_mfma_f32_16x16x32_bf16 v[132:135], a[56:59], a[216:219], v[132:135]\n"
"s_waitcnt vmcnt(11)\n"
"ds_write_addtid_b32 v166 offset:20128\n"
"v_cmp_ne_u64 vcc, s[48:49], v[154:155]\n"
"v_mfma_f32_16x16x32_bf16 v[132:135], a[60:63], a[220:223], v[132:135]\n"
"ds_write_addtid_b32 v167 offset:20384\n"
"v_mfma_f32_16x16x32_bf16 v[108:111], a[224:227], a[64:67], 0\n"
"s_cbranch_vccnz .LQ_2s\n"
".LQ_2r:\n"
"v_mfma_f32_16x16x32_bf16 v[108:111], a[228:231], a[68:71], v[108:111]\n"
"global_load_dwordx4 v[96:99], v5, s[56:57] offset:0 sc1\n"
"v_mfma_f32_16x16x32_bf16 v[108:111], a[232:235], a[72:75], v[108:111]\n"
"s_add_u32 s50, s50, 0x100000\n"
"v_mfma_f32_16x16x32_bf16 v[108:111], a[236:239], a[76:79], v[108:111]\n"
"v_mfma_f32_16x16x32_bf16 v[108:111], a[240:243], a[80:83], v[108:111]\n"
"v_mfma_f32_16x16x32_bf16 v[108:111], a[244:247], a[84:87], v[108:111]\n"
"s_waitcnt vmcnt(11)\n"
"v_mfma_f32_16x16x32_bf16 v[108:111], a[248:251], a[88:91], v[108:111]\n"
"global_load_dwordx4 v[100:103], v5, s[56:57] offset:1024 sc1\n"
"v_mfma_f32_16x16x32_bf16 v[108:111], a[252:255], a[92:95], v[108:111]\n"
"v_mfma_f32_16x16x32_bf16 v[116:119], a[224:227], a[96:99], 0\n"
"v_mfma_f32_16x16x32_bf16 v[116:119], a[228:231], a[100:103], v[116:119]\n"
"ds_write_addtid_b32 v108 offset:3072\n"
"v_mfma_f32_16x16x32_bf16 v[116:119], a[232:235], a[104:107], v[116:119]\n"
"s_waitcnt lgkmcnt(1)\n"
"ds_write_addtid_b32 v109 offset:7712\n"
"v_mfma_f32_16x16x32_bf16 v[116:119], a[236:239], a[108:111], v[116:119]\n"
"s_barrier\n"
"v_mfma_f32_16x16x32_bf16 v[116:119], a[240:243], a[112:115], v[116:119]\n"
"ds_write_addtid_b32 v110 offset:12416\n"
"ds_read_b128 v[128:131], v2 offset:4096\n"
"v_mfma_f32_16x16x32_bf16 v[116:119], a[244:247], a[116:119], v[116:119]\n"
"ds_write_addtid_b32 v111 offset:17056\n"
"global_load_dwordx4 v[104:107], v6, s[56:57] offset:0 sc1\n"
"v_mfma_f32_16x16x32_bf16 v[116:119], a[248:251], a[120:123], v[116:119]\n"
"ds_read_b128 v[120:123], v3 offset:512\n"
"ds_read_b128 v[136:139], v4 offset:1536\n"
"v_mfma_f32_16x16x32_bf16 v[116:119], a[252:255], a[124:127], v[116:119]\n"
"global_load_dwordx4 v[108:111], v6, s[56:57] offset:1024 sc1\n"
"ds_read_b128 v[140:143], v2 offset:4352\n"
"v_mfma_f32_16x16x32_bf16 v[148:151], a[128:131], a[224:227], 0\n"
"ds_read_b128 v[168:171], v3 offset:768\n"
"ds_read_b128 v[172:175], v4 offset:1792\n"
"v_mfma_f32_16x16x32_bf16 v[148:151], a[132:135], a[228:231], v[148:151]\n"
"ds_write_addtid_b32 v116 offset:3328\n"
"v_mfma_f32_16x16x32_bf16 v[148:151], a[136:139], a[232:235], v[148:151]\n"
"ds_write_addtid_b32 v117 offset:7968\n"
"v_mfma_f32_16x16x32_bf16 v[148:151], a[140:143], a[236:239], v[148:151]\n"
"ds_write_addtid_b32 v118 offset:12672\n"
"v_mfma_f32_16x16x32_bf16 v[148:151], a[144:147], a[240:243], v[148:151]\n"
"s_waitcnt lgkmcnt(6)\n"
"ds_write_addtid_b32 v119 offset:17312\n"
"global_load_dwordx4 v[112:115], v7, s[56:57] offset:0 sc1\n"
"v_mfma_f32_16x16x32_bf16 v[148:151], a[148:151], a[244:247], v[148:151]\n"
"v_add_f32 v128, v128, v120\n"
"v_add_f32 v129, v129, v121\n"
"v_mfma_f32_16x16x32_bf16 v[148:151], a[152:155], a[248:251], v[148:151]\n"
"v_add_f32 v130, v130, v122\n"
"v_add_f32 v131, v131, v123\n"
"v_mfma_f32_16x16x32_bf16 v[148:151], a[156:159], a[252:255], v[148:151]\n"
"v_add_f32 v128, v128, v136\n"
"v_add_f32 v129, v129, v137\n"
"v_mfma_f32_16x16x32_bf16 v[176:179], a[160:163], a[224:227], 0\n"
"buffer_load_dwordx4 v[180:183], v9, s[40:43], s50 offen sc1\n"
"v_mfma_f32_16x16x32_bf16 v[176:179], a[164:167], a[228:231], v[176:179]\n"
"ds_write_addtid_b32 v148 offset:3584\n"
"global_load_dwordx4 v[116:119], v7, s[56:57] offset:1024 sc1\n"
"v_mfma_f32_16x16x32_bf16 v[176:179], a[168:171], a[232:235], v[176:179]\n"
"ds_write_addtid_b32 v149 offset:8224\n"
"v_add_f32 v130, v130, v138\n"
"v_mfma_f32_16x16x32_bf16 v[176:179], a[172:175], a[236:239], v[176:179]\n"
"ds_write_addtid_b32 v150 offset:12928\n"
"v_add_f32 v131, v131, v139\n"
"v_mfma_f32_16x16x32_bf16 v[176:179], a[176:179], a[240:243], v[176:179]\n"
"ds_write_addtid_b32 v151 offset:17568\n"
"v_add_f32 v128, v128, v124\n"
"v_mfma_f32_16x16x32_bf16 v[176:179], a[180:183], a[244:247], v[176:179]\n"
"v_add_f32 v129, v129, v125\n"
"v_add_f32 v130, v130, v126\n"
"v_mfma_f32_16x16x32_bf16 v[176:179], a[184:187], a[248:251], v[176:179]\n"
"global_load_dwordx4 v[120:123], v8, s[56:57] offset:0 sc1\n"
"v_add_f32 v131, v131, v127\n"
"v_mfma_f32_16x16x32_bf16 v[176:179], a[188:191], a[252:255], v[176:179]\n"
"v_cvt_pk_bf16_f32 v184, v128, v129\n"
"v_cvt_pk_bf16_f32 v185, v130, v131\n"
"v_mfma_f32_16x16x32_bf16 v[136:139], a[224:227], a[0:3], 0\n"
"ds_read2st64_b32 v[148:149], v11 offset0:0 offset1:1\n"
"ds_read2st64_b32 v[150:151], v11 offset0:2 offset1:3\n"
"v_mfma_f32_16x16x32_bf16 v[136:139], a[228:231], a[4:7], v[136:139]\n"
"s_waitcnt lgkmcnt(10)\n"
"ds_write_addtid_b32 v176 offset:3840\n"
"v_add_f32 v140, v140, v168\n"
"v_mfma_f32_16x16x32_bf16 v[136:139], a[232:235], a[8:11], v[136:139]\n"
"ds_write_addtid_b32 v177 offset:8480\n"
"v_add_f32 v141, v141, v169\n"
"v_mfma_f32_16x16x32_bf16 v[136:139], a[236:239], a[12:15], v[136:139]\n"
"ds_write_addtid_b32 v178 offset:13184\n"
"global_load_dwordx4 v[124:127], v8, s[56:57] offset:1024 sc1\n"
"v_mfma_f32_16x16x32_bf16 v[136:139], a[240:243], a[16:19], v[136:139]\n"
"ds_write_addtid_b32 v179 offset:17824\n"
"v_add_f32 v142, v142, v170\n"
"v_mfma_f32_16x16x32_bf16 v[136:139], a[244:247], a[20:23], v[136:139]\n"
"v_add_f32 v143, v143, v171\n"
"v_add_f32 v140, v140, v172\n"
"v_mfma_f32_16x16x32_bf16 v[136:139], a[248:251], a[24:27], v[136:139]\n"
"v_add_f32 v141, v141, v173\n"
"v_add_f32 v142, v142, v174\n"
"v_mfma_f32_16x16x32_bf16 v[136:139], a[252:255], a[28:31], v[136:139]\n"
"v_add_f32 v143, v143, v175\n"
"v_add_f32 v140, v140, v132\n"
"v_mfma_f32_16x16x32_bf16 v[168:171], a[224:227], a[32:35], 0\n"
"s_add_u32 s58, s50, 0x100000\n"
"v_add_f32 v141, v141, v133\n"
"v_add_f32 v142, v142, v134\n"
"v_mfma_f32_16x16x32_bf16 v[168:171], a[228:231], a[36:39], v[168:171]\n"
"s_and_b32 s58, s58, 0x3ffffff\n"
"ds_write_addtid_b32 v136 offset:4096\n"
"v_add_f32 v143, v143, v135\n"
"v_mfma_f32_16x16x32_bf16 v[168:171], a[232:235], a[40:43], v[168:171]\n"
"s_add_u32 s56, s40, s58\n"
"ds_write_addtid_b32 v137 offset:8736\n"
"v_cvt_pk_bf16_f32 v186, v140, v141\n"
"v_mfma_f32_16x16x32_bf16 v[168:171], a[236:239], a[44:47], v[168:171]\n"
"s_addc_u32 s57, s41, 0\n"
"ds_write_addtid_b32 v138 offset:13440\n"
"v_cvt_pk_bf16_f32 v187, v142, v143\n"
"v_mfma_f32_16x16x32_bf16 v[168:171], a[240:243], a[48:51], v[168:171]\n"
"s_waitcnt vmcnt(12)\n"
"ds_write_addtid_b32 v139 offset:18080\n"
"v_cmp_ne_u64 vcc, s[48:49], v[158:159]\n"
"v_mfma_f32_16x16x32_bf16 v[168:171], a[244:247], a[52:55], v[168:171]\n"
"s_waitcnt lgkmcnt(8)\n"
"v_mfma_f32_16x16x32_bf16 v[172:175], v[148:151], v[184:187], v[20:23]\n"
"v_mfma_f32_16x16x32_bf16 v[168:171], a[248:251], a[56:59], v[168:171]\n"
"s_cbranch_vccnz .LQ_3s\n"
".LQ_3r:\n"
"v_mfma_f32_16x16x32_bf16 v[168:171], a[252:255], a[60:63], v[168:171]\n"
"s_branch .LQ_top\n"
".LQ_1s:\n"
"s_cmp_ge_u32 s50, s53\n"
"s_cbranch_scc1 .LQ_t0\n"
"s_cmp_ge_u32 s50, s52\n"
"s_cbranch_scc1 .LQ_1r\n"
"s_sub_u32 s60, s50, 0x100000\n"
"s_mov_b32 s61, 200000\n"
"s_nop 7\n"
".LQ_1p:\n"
"buffer_load_dwordx4 v[144:147], v9, s[40:43], s60 offen sc1\n"
"s_waitcnt vmcnt(0)\n"
"v_cmp_ne_u64 vcc, s[48:49], v[146:147]\n"
"s_cbranch_vccz .LQ_1px\n"
"s_sleep 1\n"
"s_sub_u32 s61, s61, 1\n"
"s_cbranch_scc0 .LQ_1p\n"
"s_branch .LQ_end\n"
".LQ_1px:\n"
"s_branch .LQ_1r\n"
".LQ_2s:\n"
"s_cmp_ge_u32 s50, s53\n"
"s_cbranch_scc1 .LQ_t1\n"
"s_cmp_ge_u32 s50, s52\n"
"s_cbranch_scc1 .LQ_2r\n"
"s_sub_u32 s60, s50, 0x100000\n"
"s_mov_b32 s61, 200000\n"
"s_nop 7\n"
".LQ_2p:\n"
"buffer_load_dwordx4 v[152:155], v10, s[40:43], s60 offen sc1\n"
"s_waitcnt vmcnt(0)\n"
"v_cmp_ne_u64 vcc, s[48:49], v[154:155]\n"
"s_cbranch_vccz .LQ_2px\n"
"s_sleep 1\n"
"s_sub_u32 s61, s61, 1\n"
"s_cbranch_scc0 .LQ_2p\n"
"s_branch .LQ_end\n"
".LQ_2px:\n"
"s_branch .LQ_2r\n"
".LQ_3s:\n"
"s_cmp_ge_u32 s50, s53\n"
"s_cbranch_scc1 .LQ_t2\n"
"s_cmp_ge_u32 s50, s52\n"
"s_cbranch_scc1 .LQ_3r\n"
"s_sub_u32 s60, s50, 0x100000\n"
"s_mov_b32 s61, 200000\n"
"s_nop 7\n"
".LQ_3p:\n"
"buffer_load_dwordx4 v[156:159], v9, s[40:43], s60 offen sc1\n"
"s_waitcnt vmcnt(0)\n"
"v_cmp_ne_u64 vcc, s[48:49], v[158:159]\n"
"s_cbranch_vccz .LQ_3px\n"
"s_sleep 1\n"
"s_sub_u32 s61, s61, 1\n"
"s_cbranch_scc0 .LQ_3p\n"
"s_branch .LQ_end\n"
".LQ_3px:\n"
"s_branch .LQ_3r\n"
".LQ_4s:\n"
"s_cmp_ge_u32 s50, s53\n"
"s_cbranch_scc1 .LQ_t3\n"
"s_cmp_ge_u32 s50, s52\n"
"s_cbranch_scc1 .LQ_4r\n"
"s_sub_u32 s60, s50, 0x100000\n"
"s_mov_b32 s61, 200000\n"
"s_nop 7\n"
".LQ_4p:\n"
"buffer_load_dwordx4 v[180:183], v9, s[40:43], s60 offen sc1\n"
"s_waitcnt vmcnt(0)\n"
"v_cmp_ne_u64 vcc, s[48:49], v[182:183]\n"
"s_cbranch_vccz .LQ_4px\n"
"s_sleep 1\n"
"s_sub_u32 s61, s61, 1\n"
"s_cbranch_scc0 .LQ_4p\n"
"s_branch .LQ_end\n"
".LQ_4px:\n"
"s_branch .LQ_4r\n"
".LQ_fs:\n"
"s_nop 7\n"
"v_mov_b32 v172, 0xff800000\n"
"v_mov_b32 v173, 0xff800000\n"
"v_mov_b32 v174, 0xff800000\n"
"v_mov_b32 v175, 0xff800000\n"
"v_mov_b32 v162, 0\n"
"v_mov_b32 v163, 0\n"
"v_mov_b32 v166, 0\n"
"v_mov_b32 v167, 0\n"
"s_add_u32 s62, s62, 0x4000000\n"
"s_branch .LQ_fr\n"
".LQ_ft0k7s:\n"
"s_nop 7\n"
"v_mov_b32 v172, 0xff800000\n"
"v_mov_b32 v173, 0xff800000\n"
"v_mov_b32 v174, 0xff800000\n"
"v_mov_b32 v175, 0xff800000\n"
"v_mov_b32 v162, 0\n"
"v_mov_b32 v163, 0\n"
"v_mov_b32 v166, 0\n"
"v_mov_b32 v167, 0\n"
"s_add_u32 s62, s62, 0x4000000\n"
"s_branch .LQ_ft0k7r\n"
".LQ_ft1k7s:\n"
"s_nop 7\n"
"v_mov_b32 v172, 0xff800000\n"
"v_mov_b32 v173, 0xff800000\n"
"v_mov_b32 v174, 0xff800000\n"
"v_mov_b32 v175, 0xff800000\n"
"v_mov_b32 v162, 0\n"
"v_mov_b32 v163, 0\n"
"v_mov_b32 v166, 0\n"
"v_mov_b32 v167, 0\n"
"s_add_u32 s62, s62, 0x4000000\n"
"s_branch .LQ_ft1k7r\n"
".LQ_ft2k7s:\n"
"s_nop 7\n"
"v_mov_b32 v172, 0xff800000\n"
"v_mov_b32 v173, 0xff800000\n"
"v_mov_b32 v174, 0xff800000\n"
"v_mov_b32 v175, 0xff800000\n"
"v_mov_b32 v162, 0\n"
"v_mov_b32 v163, 0\n"
"v_mov_b32 v166, 0\n"
"v_mov_b32 v167, 0\n"
"s_add_u32 s62, s62, 0x4000000\n"
"s_branch .LQ_ft2k7r\n"
".LQ_ft3k0s:\n"
"s_nop 7\n"
"v_mov_b32 v172, 0xff800000\n"
"v_mov_b32 v173, 0xff800000\n"
"v_mov_b32 v174, 0xff800000\n"
"v_mov_b32 v175, 0xff800000\n"
"v_mov_b32 v162, 0\n"
"v_mov_b32 v163, 0\n"
"v_mov_b32 v166, 0\n"
"v_mov_b32 v167, 0\n"
"s_add_u32 s62, s62, 0x4000000\n"
"s_branch .LQ_ft3k0r\n"
".LQ_t0:\n"
"v_cvt_pk_bf16_f32 v79, v86, v87\n"
"v_mfma_f32_16x16x32_bf16 v[128:131], v[124:127], a[188:191], v[128:131]\n"
"s_waitcnt vmcnt(17)\n"
"ds_write_addtid_b32 v78 offset:19104\n"
"ds_write_addtid_b32 v79 offset:19360\n"
"v_mfma_f32_16x16x32_bf16 v[80:83], a[192:195], a[64:67], 0\n"
"v_mfma_f32_16x16x32_bf16 v[80:83], a[196:199], a[68:71], v[80:83]\n"
"s_waitcnt vmcnt(15)\n"
"v_mfma_f32_16x16x32_bf16 v[80:83], a[200:203], a[72:75], v[80:83]\n"
"v_mfma_f32_16x16x32_bf16 v[80:83], a[204:207], a[76:79], v[80:83]\n"
"s_waitcnt vmcnt(12)\n"
"v_mul_f32 v249, 0x3e8293ee, v156\n"
"v_mfma_f32_16x16x32_bf16 v[80:83], a[208:211], a[80:83], v[80:83]\n"
"v_mul_f32 v126, v157, v30\n"
"v_mul_f32 v127, v157, v31\n"
"v_mfma_f32_16x16x32_bf16 v[80:83], a[212:215], a[84:87], v[80:83]\n"
"s_waitcnt vmcnt(10)\n"
"v_mul_f32 v125, v157, v29\n"
"v_mfma_f32_16x16x32_bf16 v[80:83], a[216:219], a[88:91], v[80:83]\n"
"v_mul_f32 v135, v157, v35\n"
"v_mul_f32 v124, v157, v28\n"
"v_mfma_f32_16x16x32_bf16 v[80:83], a[220:223], a[92:95], v[80:83]\n"
"s_waitcnt vmcnt(7)\n"
"buffer_store_dwordx2 v[0:1], v14, s[40:43], s94 offen offset:0 sc1\n"
"v_mul_f32 v134, v157, v34\n"
"v_mfma_f32_16x16x32_bf16 v[84:87], a[192:195], a[96:99], 0\n"
"s_waitcnt lgkmcnt(0)\n"
"v_mul_f32 v132, v157, v32\n"
"v_mul_f32 v133, v157, v33\n"
"v_mfma_f32_16x16x32_bf16 v[84:87], a[196:199], a[100:103], v[84:87]\n"
"s_barrier\n"
"v_mfma_f32_16x16x32_bf16 v[84:87], a[200:203], a[104:107], v[84:87]\n"
"ds_write_addtid_b32 v80 offset:2048\n"
"v_mfma_f32_16x16x32_bf16 v[84:87], a[204:207], a[108:111], v[84:87]\n"
"ds_write_addtid_b32 v81 offset:6688\n"
"ds_read_b128 v[96:99], v2 offset:2560\n"
"v_mfma_f32_16x16x32_bf16 v[84:87], a[208:211], a[112:115], v[84:87]\n"
"ds_write_addtid_b32 v82 offset:11392\n"
"ds_read_b128 v[88:91], v3 offset:3584\n"
"v_mfma_f32_16x16x32_bf16 v[84:87], a[212:215], a[116:119], v[84:87]\n"
"ds_write_addtid_b32 v83 offset:16032\n"
"ds_read_b128 v[100:103], v4 offset:0\n"
"v_mfma_f32_16x16x32_bf16 v[84:87], a[216:219], a[120:123], v[84:87]\n"
"ds_read_b128 v[104:107], v2 offset:2816\n"
"ds_read_b128 v[108:111], v3 offset:3840\n"
"v_mfma_f32_16x16x32_bf16 v[84:87], a[220:223], a[124:127], v[84:87]\n"
"ds_read_b128 v[112:115], v4 offset:256\n"
"v_mfma_f32_16x16x32_bf16 v[116:119], a[128:131], a[192:195], 0\n"
"v_mfma_f32_16x16x32_bf16 v[116:119], a[132:135], a[196:199], v[116:119]\n"
"ds_write_addtid_b32 v84 offset:2304\n"
"v_mfma_f32_16x16x32_bf16 v[116:119], a[136:139], a[200:203], v[116:119]\n"
"ds_write_addtid_b32 v85 offset:6944\n"
"v_mfma_f32_16x16x32_bf16 v[116:119], a[140:143], a[204:207], v[116:119]\n"
"s_waitcnt lgkmcnt(5)\n"
"ds_write_addtid_b32 v86 offset:11648\n"
"v_add_f32 v96, v96, v88\n"
"v_mfma_f32_16x16x32_bf16 v[116:119], a[144:147], a[208:211], v[116:119]\n"
"ds_write_addtid_b32 v87 offset:16288\n"
"v_mfma_f32_16x16x32_bf16 v[116:119], a[148:151], a[212:215], v[116:119]\n"
"v_add_f32 v97, v97, v89\n"
"v_add_f32 v98, v98, v90\n"
"v_mfma_f32_16x16x32_bf16 v[116:119], a[152:155], a[216:219], v[116:119]\n"
"v_add_f32 v99, v99, v91\n"
"v_add_f32 v96, v96, v100\n"
"v_mfma_f32_16x16x32_bf16 v[116:119], a[156:159], a[220:223], v[116:119]\n"
"v_add_f32 v97, v97, v101\n"
"v_add_f32 v98, v98, v102\n"
"v_mfma_f32_16x16x32_bf16 v[120:123], a[160:163], a[192:195], 0\n"
"v_mfma_f32_16x16x32_bf16 v[120:123], a[164:167], a[196:199], v[120:123]\n"
"ds_write_addtid_b32 v116 offset:2560\n"
"v_mfma_f32_16x16x32_bf16 v[120:123], a[168:171], a[200:203], v[120:123]\n"
"ds_write_addtid_b32 v117 offset:7200\n"
"v_add_f32 v99, v99, v103\n"
"v_mfma_f32_16x16x32_bf16 v[120:123], a[172:175], a[204:207], v[120:123]\n"
"ds_write_addtid_b32 v118 offset:11904\n"
"v_add_f32 v96, v96, v92\n"
"v_mfma_f32_16x16x32_bf16 v[120:123], a[176:179], a[208:211], v[120:123]\n"
"ds_write_addtid_b32 v119 offset:16544\n"
"v_add_f32 v97, v97, v93\n"
"v_mfma_f32_16x16x32_bf16 v[120:123], a[180:183], a[212:215], v[120:123]\n"
"v_add_f32 v98, v98, v94\n"
"v_add_f32 v99, v99, v95\n"
"v_mfma_f32_16x16x32_bf16 v[120:123], a[184:187], a[216:219], v[120:123]\n"
"v_mul_f32_dpp v96, v253, v96 row_newbcast:0 row_mask:0xf bank_mask:0xf\n"
"v_mfma_f32_16x16x32_bf16 v[120:123], a[188:191], a[220:223], v[120:123]\n"
"v_mul_f32_dpp v97, v253, v97 row_newbcast:1 row_mask:0xf bank_mask:0xf\n"
"v_mul_f32_dpp v98, v253, v98 row_newbcast:2 row_mask:0xf bank_mask:0xf\n"
"v_mfma_f32_16x16x32_bf16 v[124:127], a[0:3], a[192:195], v[124:127]\n"
"v_mul_f32_dpp v99, v253, v99 row_newbcast:3 row_mask:0xf bank_mask:0xf\n"
"v_cvt_pk_bf16_f32 v162, v96, v97\n"
"v_mfma_f32_16x16x32_bf16 v[124:127], a[4:7], a[196:199], v[124:127]\n"
"ds_write_addtid_b32 v120 offset:2816\n"
"v_cvt_pk_bf16_f32 v163, v98, v99\n"
"v_mfma_f32_16x16x32_bf16 v[124:127], a[8:11], a[200:203], v[124:127]\n"
"ds_write_addtid_b32 v121 offset:7456\n"
"ds_write_addtid_b32 v162 offset:19616\n"
"v_mfma_f32_16x16x32_bf16 v[124:127], a[12:15], a[204:207], v[124:127]\n"
"ds_write_addtid_b32 v122 offset:12160\n"
"v_mfma_f32_16x16x32_bf16 v[124:127], a[16:19], a[208:211], v[124:127]\n"
"ds_write_addtid_b32 v123 offset:16800\n"
"ds_write_addtid_b32 v163 offset:19872\n"
"v_mfma_f32_16x16x32_bf16 v[124:127], a[20:23], a[212:215], v[124:127]\n"
"s_waitcnt lgkmcnt(14)\n"
"v_add_f32 v104, v104, v108\n"
"v_add_f32 v105, v105, v109\n"
"v_mfma_f32_16x16x32_bf16 v[124:127], a[24:27], a[216:219], v[124:127]\n"
"v_add_f32 v106, v106, v110\n"
"v_add_f32 v107, v107, v111\n"
"v_mfma_f32_16x16x32_bf16 v[124:127], a[28:31], a[220:223], v[124:127]\n"
"v_add_f32 v104, v104, v112\n"
"v_add_f32 v105, v105, v113\n"
"v_mfma_f32_16x16x32_bf16 v[132:135], a[32:35], a[192:195], v[132:135]\n"
"v_add_f32 v106, v106, v114\n"
"v_add_f32 v107, v107, v115\n"
"v_mfma_f32_16x16x32_bf16 v[132:135], a[36:39], a[196:199], v[132:135]\n"
"v_add_f32 v104, v104, v128\n"
"v_add_f32 v105, v105, v129\n"
"v_mfma_f32_16x16x32_bf16 v[132:135], a[40:43], a[200:203], v[132:135]\n"
"v_add_f32 v106, v106, v130\n"
"v_add_f32 v107, v107, v131\n"
"v_mfma_f32_16x16x32_bf16 v[132:135], a[44:47], a[204:207], v[132:135]\n"
"v_mul_f32_dpp v104, v253, v104 row_newbcast:0 row_mask:0xf bank_mask:0xf\n"
"v_mul_f32_dpp v105, v253, v105 row_newbcast:1 row_mask:0xf bank_mask:0xf\n"
"v_mfma_f32_16x16x32_bf16 v[132:135], a[48:51], a[208:211], v[132:135]\n"
"v_mul_f32_dpp v106, v253, v106 row_newbcast:2 row_mask:0xf bank_mask:0xf\n"
"v_mul_f32_dpp v107, v253, v107 row_newbcast:3 row_mask:0xf bank_mask:0xf\n"
"v_mfma_f32_16x16x32_bf16 v[132:135], a[52:55], a[212:215], v[132:135]\n"
"v_cvt_pk_bf16_f32 v166, v104, v105\n"
"v_cvt_pk_bf16_f32 v167, v106, v107\n"
"v_mfma_f32_16x16x32_bf16 v[132:135], a[56:59], a[216:219], v[132:135]\n"
"s_waitcnt vmcnt(2)\n"
"ds_write_addtid_b32 v166 offset:20128\n"
"v_mfma_f32_16x16x32_bf16 v[132:135], a[60:63], a[220:223], v[132:135]\n"
"s_waitcnt vmcnt(9)\n"
"ds_write_addtid_b32 v167 offset:20384\n"
"v_mfma_f32_16x16x32_bf16 v[108:111], a[224:227], a[64:67], 0\n"
"v_mfma_f32_16x16x32_bf16 v[108:111], a[228:231], a[68:71], v[108:111]\n"
"s_waitcnt vmcnt(6)\n"
"v_mfma_f32_16x16x32_bf16 v[108:111], a[232:235], a[72:75], v[108:111]\n"
"v_mfma_f32_16x16x32_bf16 v[108:111], a[236:239], a[76:79], v[108:111]\n"
"s_waitcnt vmcnt(4)\n"
"v_mfma_f32_16x16x32_bf16 v[108:111], a[240:243], a[80:83], v[108:111]\n"
"v_mfma_f32_16x16x32_bf16 v[108:111], a[244:247], a[84:87], v[108:111]\n"
"s_waitcnt vmcnt(1)\n"
"v_mfma_f32_16x16x32_bf16 v[108:111], a[248:251], a[88:91], v[108:111]\n"
"v_mfma_f32_16x16x32_bf16 v[108:111], a[252:255], a[92:95], v[108:111]\n"
"v_mfma_f32_16x16x32_bf16 v[116:119], a[224:227], a[96:99], 0\n"
"v_mfma_f32_16x16x32_bf16 v[116:119], a[228:231], a[100:103], v[116:119]\n"
"ds_write_addtid_b32 v108 offset:3072\n"
"v_mfma_f32_16x16x32_bf16 v[116:119], a[232:235], a[104:107], v[116:119]\n"
"s_waitcnt lgkmcnt(1)\n"
"ds_write_addtid_b32 v109 offset:7712\n"
"v_mfma_f32_16x16x32_bf16 v[116:119], a[236:239], a[108:111], v[116:119]\n"
"s_barrier\n"
"v_mfma_f32_16x16x32_bf16 v[116:119], a[240:243], a[112:115], v[116:119]\n"
"ds_write_addtid_b32 v110 offset:12416\n"
"ds_read_b128 v[128:131], v2 offset:4096\n"
"v_mfma_f32_16x16x32_bf16 v[116:119], a[244:247], a[116:119], v[116:119]\n"
"ds_write_addtid_b32 v111 offset:17056\n"
"v_mfma_f32_16x16x32_bf16 v[116:119], a[248:251], a[120:123], v[116:119]\n"
"ds_read_b128 v[120:123], v3 offset:512\n"
"ds_read_b128 v[136:139], v4 offset:1536\n"
"v_mfma_f32_16x16x32_bf16 v[116:119], a[252:255], a[124:127], v[116:119]\n"
"ds_read_b128 v[140:143], v2 offset:4352\n"
"v_mfma_f32_16x16x32_bf16 v[148:151], a[128:131], a[224:227], 0\n"
"ds_read_b128 v[168:171], v3 offset:768\n"
"ds_read_b128 v[172:175], v4 offset:1792\n"
"v_mfma_f32_16x16x32_bf16 v[148:151], a[132:135], a[228:231], v[148:151]\n"
"ds_write_addtid_b32 v116 offset:3328\n"
"v_mfma_f32_16x16x32_bf16 v[148:151], a[136:139], a[232:235], v[148:151]\n"
"ds_write_addtid_b32 v117 offset:7968\n"
"v_mfma_f32_16x16x32_bf16 v[148:151], a[140:143], a[236:239], v[148:151]\n"
"ds_write_addtid_b32 v118 offset:12672\n"
"v_mfma_f32_16x16x32_bf16 v[148:151], a[144:147], a[240:243], v[148:151]\n"
"s_waitcnt lgkmcnt(6)\n"
"ds_write_addtid_b32 v119 offset:17312\n"
"v_mfma_f32_16x16x32_bf16 v[148:151], a[148:151], a[244:247], v[148:151]\n"
"v_add_f32 v128, v128, v120\n"
"v_add_f32 v129, v129, v121\n"
"v_mfma_f32_16x16x32_bf16 v[148:151], a[152:155], a[248:251], v[148:151]\n"
"v_add_f32 v130, v130, v122\n"
"v_add_f32 v131, v131, v123\n"
"v_mfma_f32_16x16x32_bf16 v[148:151], a[156:159], a[252:255], v[148:151]\n"
"v_add_f32 v128, v128, v136\n"
"v_add_f32 v129, v129, v137\n"
"v_mfma_f32_16x16x32_bf16 v[176:179], a[160:163], a[224:227], 0\n"
"v_mfma_f32_16x16x32_bf16 v[176:179], a[164:167], a[228:231], v[176:179]\n"
"ds_write_addtid_b32 v148 offset:3584\n"
"v_mfma_f32_16x16x32_bf16 v[176:179], a[168:171], a[232:235], v[176:179]\n"
"ds_write_addtid_b32 v149 offset:8224\n"
"v_add_f32 v130, v130, v138\n"
"v_mfma_f32_16x16x32_bf16 v[176:179], a[172:175], a[236:239], v[176:179]\n"
"ds_write_addtid_b32 v150 offset:12928\n"
"v_add_f32 v131, v131, v139\n"
"v_mfma_f32_16x16x32_bf16 v[176:179], a[176:179], a[240:243], v[176:179]\n"
"ds_write_addtid_b32 v151 offset:17568\n"
"v_add_f32 v128, v128, v124\n"
"v_mfma_f32_16x16x32_bf16 v[176:179], a[180:183], a[244:247], v[176:179]\n"
"v_add_f32 v129, v129, v125\n"
"v_add_f32 v130, v130, v126\n"
"v_mfma_f32_16x16x32_bf16 v[176:179], a[184:187], a[248:251], v[176:179]\n"
"v_add_f32 v131, v131, v127\n"
"v_mfma_f32_16x16x32_bf16 v[176:179], a[188:191], a[252:255], v[176:179]\n"
"v_cvt_pk_bf16_f32 v184, v128, v129\n"
"v_cvt_pk_bf16_f32 v185, v130, v131\n"
"v_mfma_f32_16x16x32_bf16 v[136:139], a[224:227], a[0:3], 0\n"
"ds_read2st64_b32 v[148:149], v11 offset0:0 offset1:1\n"
"ds_read2st64_b32 v[150:151], v11 offset0:2 offset1:3\n"
"v_mfma_f32_16x16x32_bf16 v[136:139], a[228:231], a[4:7], v[136:139]\n"
"s_waitcnt lgkmcnt(10)\n"
"ds_write_addtid_b32 v176 offset:3840\n"
"v_add_f32 v140, v140, v168\n"
"v_mfma_f32_16x16x32_bf16 v[136:139], a[232:235], a[8:11], v[136:139]\n"
"ds_write_addtid_b32 v177 offset:8480\n"
"v_add_f32 v141, v141, v169\n"
"v_mfma_f32_16x16x32_bf16 v[136:139], a[236:239], a[12:15], v[136:139]\n"
"ds_write_addtid_b32 v178 offset:13184\n"
"v_mfma_f32_16x16x32_bf16 v[136:139], a[240:243], a[16:19], v[136:139]\n"
"ds_write_addtid_b32 v179 offset:17824\n"
"v_add_f32 v142, v142, v170\n"
"v_mfma_f32_16x16x32_bf16 v[136:139], a[244:247], a[20:23], v[136:139]\n"
"v_add_f32 v143, v143, v171\n"
"v_add_f32 v140, v140, v172\n"
"v_mfma_f32_16x16x32_bf16 v[136:139], a[248:251], a[24:27], v[136:139]\n"
"v_add_f32 v141, v141, v173\n"
"v_add_f32 v142, v142, v174\n"
"v_mfma_f32_16x16x32_bf16 v[136:139], a[252:255], a[28:31], v[136:139]\n"
"v_add_f32 v143, v143, v175\n"
"v_add_f32 v140, v140, v132\n"
"v_mfma_f32_16x16x32_bf16 v[168:171], a[224:227], a[32:35], 0\n"
"v_add_f32 v141, v141, v133\n"
"v_add_f32 v142, v142, v134\n"
"v_mfma_f32_16x16x32_bf16 v[168:171], a[228:231], a[36:39], v[168:171]\n"
"ds_write_addtid_b32 v136 offset:4096\n"
"v_add_f32 v143, v143, v135\n"
"v_mfma_f32_16x16x32_bf16 v[168:171], a[232:235], a[40:43], v[168:171]\n"
"ds_write_addtid_b32 v137 offset:8736\n"
"v_cvt_pk_bf16_f32 v186, v140, v141\n"
"v_mfma_f32_16x16x32_bf16 v[168:171], a[236:239], a[44:47], v[168:171]\n"
"ds_write_addtid_b32 v138 offset:13440\n"
"v_cvt_pk_bf16_f32 v187, v142, v143\n"
"v_mfma_f32_16x16x32_bf16 v[168:171], a[240:243], a[48:51], v[168:171]\n"
"ds_write_addtid_b32 v139 offset:18080\n"
"v_mfma_f32_16x16x32_bf16 v[168:171], a[244:247], a[52:55], v[168:171]\n"
"s_waitcnt lgkmcnt(8)\n"
"v_mfma_f32_16x16x32_bf16 v[172:175], v[148:151], v[184:187], v[20:23]\n"
"v_mfma_f32_16x16x32_bf16 v[168:171], a[248:251], a[56:59], v[168:171]\n"
"v_mfma_f32_16x16x32_bf16 v[168:171], a[252:255], a[60:63], v[168:171]\n"
"s_nop 7\n"
"ds_write_addtid_b32 v168 offset:4352\n"
"ds_write_addtid_b32 v169 offset:8992\n"
"ds_write_addtid_b32 v170 offset:13696\n"
"ds_write_addtid_b32 v171 offset:18336\n"
"s_waitcnt lgkmcnt(0)\n"
"s_barrier\n"
"ds_read2st64_b32 v[144:145], v12 offset0:0 offset1:1\n"
"ds_read2st64_b32 v[146:147], v12 offset0:2 offset1:3\n"
"ds_read2st64_b32 v[160:161], v11 offset0:4 offset1:5\n"
"ds_read2st64_b32 v[164:165], v11 offset0:6 offset1:7\n"
"s_add_u32 s94, s50, 0xfff00000\n"
"s_and_b32 s95, s94, 0xfbffffff\n"
"s_waitcnt lgkmcnt(2)\n"
"v_mfma_f32_16x16x32_bf16 v[148:151], v[144:147], v[184:187], v[16:19]\n"
"s_cmp_eq_u32 s94, s62\n"
"s_cbranch_scc1 .LQ_ft0k7s\n"
".LQ_ft0k7r:\n"
"v_max_f32 v253, v172, v174\n"
"v_max_f32 v252, v173, v175\n"
"s_nop 3\n"
"v_max3_f32 v253, v148, v150, v253\n"
"v_max3_f32 v252, v149, v151, v252\n"
"v_max_f32 v251, v253, v252\n"
"v_mov_b32 v250, v251\n"
"s_nop 1\n"
"v_permlane32_swap_b32 v250, v251\n"
"v_max_f32 v251, v250, v251\n"
"v_mov_b32 v250, v251\n"
"s_nop 1\n"
"v_permlane16_swap_b32 v250, v251\n"
"v_max_f32 v251, v250, v251\n"
"v_mul_f32 v251, v251, v249\n"
"v_fma_f32 v253, v253, v249, -v251\n"
"v_fma_f32 v252, v252, v249, -v251\n"
"v_exp_f32 v253, v253\n"
"v_exp_f32 v252, v252\n"
"s_nop 0\n"
"v_add_f32 v251, v253, v252\n"
"v_mov_b32 v250, v251\n"
"s_nop 1\n"
"v_permlane32_swap_b32 v250, v251\n"
"v_add_f32 v251, v250, v251\n"
"v_mov_b32 v250, v251\n"
"s_nop 1\n"
"v_permlane16_swap_b32 v250, v251\n"
"v_add_f32 v251, v250, v251\n"
"v_rcp_f32 v251, v251\n"
"s_nop 0\n"
"v_mul_f32 v253, v253, v251\n"
"v_mul_f32 v252, v252, v251\n"
"v_cvt_pk_bf16_f32 v250, v253, v252\n"
"s_waitcnt lgkmcnt(0)\n"
"v_and_b32 v68, v250, v24\n"
"v_and_b32 v69, v250, v25\n"
"v_and_b32 v70, v250, v26\n"
"v_and_b32 v71, v250, v27\n"
"s_nop 1\n"
"v_mfma_f32_16x16x32_bf16 v[72:75], v[160:163], v[68:71], 0\n"
"v_mfma_f32_16x16x32_bf16 v[76:79], v[164:167], v[68:71], 0\n"
"s_nop 6\n"
"v_cvt_pk_bf16_f32 v68, v72, v73\n"
"v_cvt_pk_bf16_f32 v69, v74, v75\n"
"v_cvt_pk_bf16_f32 v70, v76, v77\n"
"v_cvt_pk_bf16_f32 v71, v78, v79\n"
"buffer_store_dwordx4 v[68:71], v13, s[40:43], s95 offen sc1\n"
"s_barrier\n"
"s_waitcnt vmcnt(0)\n"
"buffer_store_dwordx2 v[0:1], v14, s[40:43], s94 offen offset:0 sc1\n"
"s_branch .LQ_end\n"
".LQ_t1:\n"
"v_mfma_f32_16x16x32_bf16 v[108:111], a[228:231], a[68:71], v[108:111]\n"
"s_waitcnt vmcnt(15)\n"
"v_mfma_f32_16x16x32_bf16 v[108:111], a[232:235], a[72:75], v[108:111]\n"
"v_mfma_f32_16x16x32_bf16 v[108:111], a[236:239], a[76:79], v[108:111]\n"
"s_waitcnt vmcnt(13)\n"
"v_mfma_f32_16x16x32_bf16 v[108:111], a[240:243], a[80:83], v[108:111]\n"
"v_mfma_f32_16x16x32_bf16 v[108:111], a[244:247], a[84:87], v[108:111]\n"
"s_waitcnt vmcnt(10)\n"
"v_mfma_f32_16x16x32_bf16 v[108:111], a[248:251], a[88:91], v[108:111]\n"
"v_mfma_f32_16x16x32_bf16 v[108:111], a[252:255], a[92:95], v[108:111]\n"
"v_mfma_f32_16x16x32_bf16 v[116:119], a[224:227], a[96:99], 0\n"
"v_mfma_f32_16x16x32_bf16 v[116:119], a[228:231], a[100:103], v[116:119]\n"
"ds_write_addtid_b32 v108 offset:3072\n"
"v_mfma_f32_16x16x32_bf16 v[116:119], a[232:235], a[104:107], v[116:119]\n"
"s_waitcnt lgkmcnt(1)\n"
"ds_write_addtid_b32 v109 offset:7712\n"
"v_mfma_f32_16x16x32_bf16 v[116:119], a[236:239], a[108:111], v[116:119]\n"
"s_barrier\n"
"v_mfma_f32_16x16x32_bf16 v[116:119], a[240:243], a[112:115], v[116:119]\n"
"ds_write_addtid_b32 v110 offset:12416\n"
"ds_read_b128 v[128:131], v2 offset:4096\n"
"v_mfma_f32_16x16x32_bf16 v[116:119], a[244:247], a[116:119], v[116:119]\n"
"ds_write_addtid_b32 v111 offset:17056\n"
"v_mfma_f32_16x16x32_bf16 v[116:119], a[248:251], a[120:123], v[116:119]\n"
"ds_read_b128 v[120:123], v3 offset:512\n"
"ds_read_b128 v[136:139], v4 offset:1536\n"
"v_mfma_f32_16x16x32_bf16 v[116:119], a[252:255], a[124:127], v[116:119]\n"
"ds_read_b128 v[140:143], v2 offset:4352\n"
"v_mfma_f32_16x16x32_bf16 v[148:151], a[128:131], a[224:227], 0\n"
"ds_read_b128 v[168:171], v3 offset:768\n"
"ds_read_b128 v[172:175], v4 offset:1792\n"
"v_mfma_f32_16x16x32_bf16 v[148:151], a[132:135], a[228:231], v[148:151]\n"
"ds_write_addtid_b32 v116 offset:3328\n"
"v_mfma_f32_16x16x32_bf16 v[148:151], a[136:139], a[232:235], v[148:151]\n"
"ds_write_addtid_b32 v117 offset:7968\n"
"v_mfma_f32_16x16x32_bf16 v[148:151], a[140:143], a[236:239], v[148:151]\n"
"ds_write_addtid_b32 v118 offset:12672\n"
"v_mfma_f32_16x16x32_bf16 v[148:151], a[144:147], a[240:243], v[148:151]\n"
"s_waitcnt lgkmcnt(6)\n"
"ds_write_addtid_b32 v119 offset:17312\n"
"v_mfma_f32_16x16x32_bf16 v[148:151], a[148:151], a[244:247], v[148:151]\n"
"v_add_f32 v128, v128, v120\n"
"v_add_f32 v129, v129, v121\n"
"v_mfma_f32_16x16x32_bf16 v[148:151], a[152:155], a[248:251], v[148:151]\n"
"v_add_f32 v130, v130, v122\n"
"v_add_f32 v131, v131, v123\n"
"v_mfma_f32_16x16x32_bf16 v[148:151], a[156:159], a[252:255], v[148:151]\n"
"v_add_f32 v128, v128, v136\n"
"v_add_f32 v129, v129, v137\n"
"v_mfma_f32_16x16x32_bf16 v[176:179], a[160:163], a[224:227], 0\n"
"v_mfma_f32_16x16x32_bf16 v[176:179], a[164:167], a[228:231], v[176:179]\n"
"ds_write_addtid_b32 v148 offset:3584\n"
"v_mfma_f32_16x16x32_bf16 v[176:179], a[168:171], a[232:235], v[176:179]\n"
"ds_write_addtid_b32 v149 offset:8224\n"
"v_add_f32 v130, v130, v138\n"
"v_mfma_f32_16x16x32_bf16 v[176:179], a[172:175], a[236:239], v[176:179]\n"
"ds_write_addtid_b32 v150 offset:12928\n"
"v_add_f32 v131, v131, v139\n"
"v_mfma_f32_16x16x32_bf16 v[176:179], a[176:179], a[240:243], v[176:179]\n"
"ds_write_addtid_b32 v151 offset:17568\n"
"v_add_f32 v128, v128, v124\n"
"v_mfma_f32_16x16x32_bf16 v[176:179], a[180:183], a[244:247], v[176:179]\n"
"v_add_f32 v129, v129, v125\n"
"v_add_f32 v130, v130, v126\n"
"v_mfma_f32_16x16x32_bf16 v[176:179], a[184:187], a[248:251], v[176:179]\n"
"v_add_f32 v131, v131, v127\n"
"v_mfma_f32_16x16x32_bf16 v[176:179], a[188:191], a[252:255], v[176:179]\n"
"v_cvt_pk_bf16_f32 v184, v128, v129\n"
"v_cvt_pk_bf16_f32 v185, v130, v131\n"
"v_mfma_f32_16x16x32_bf16 v[136:139], a[224:227], a[0:3], 0\n"
"ds_read2st64_b32 v[148:149], v11 offset0:0 offset1:1\n"
"ds_read2st64_b32 v[150:151], v11 offset0:2 offset1:3\n"
"v_mfma_f32_16x16x32_bf16 v[136:139], a[228:231], a[4:7], v[136:139]\n"
"s_waitcnt lgkmcnt(10)\n"
"ds_write_addtid_b32 v176 offset:3840\n"
"v_add_f32 v140, v140, v168\n"
"v_mfma_f32_16x16x32_bf16 v[136:139], a[232:235], a[8:11], v[136:139]\n"
"ds_write_addtid_b32 v177 offset:8480\n"
"v_add_f32 v141, v141, v169\n"
"v_mfma_f32_16x16x32_bf16 v[136:139], a[236:239], a[12:15], v[136:139]\n"
"ds_write_addtid_b32 v178 offset:13184\n"
"v_mfma_f32_16x16x32_bf16 v[136:139], a[240:243], a[16:19], v[136:139]\n"
"ds_write_addtid_b32 v179 offset:17824\n"
"v_add_f32 v142, v142, v170\n"
"v_mfma_f32_16x16x32_bf16 v[136:139], a[244:247], a[20:23], v[136:139]\n"
"v_add_f32 v143, v143, v171\n"
"v_add_f32 v140, v140, v172\n"
"v_mfma_f32_16x16x32_bf16 v[136:139], a[248:251], a[24:27], v[136:139]\n"
"v_add_f32 v141, v141, v173\n"
"v_add_f32 v142, v142, v174\n"
"v_mfma_f32_16x16x32_bf16 v[136:139], a[252:255], a[28:31], v[136:139]\n"
"v_add_f32 v143, v143, v175\n"
"v_add_f32 v140, v140, v132\n"
"v_mfma_f32_16x16x32_bf16 v[168:171], a[224:227], a[32:35], 0\n"
"v_add_f32 v141, v141, v133\n"
"v_add_f32 v142, v142, v134\n"
"v_mfma_f32_16x16x32_bf16 v[168:171], a[228:231], a[36:39], v[168:171]\n"
"ds_write_addtid_b32 v136 offset:4096\n"
"v_add_f32 v143, v143, v135\n"
"v_mfma_f32_16x16x32_bf16 v[168:171], a[232:235], a[40:43], v[168:171]\n"
"ds_write_addtid_b32 v137 offset:8736\n"
"v_cvt_pk_bf16_f32 v186, v140, v141\n"
"v_mfma_f32_16x16x32_bf16 v[168:171], a[236:239], a[44:47], v[168:171]\n"
"ds_write_addtid_b32 v138 offset:13440\n"
"v_cvt_pk_bf16_f32 v187, v142, v143\n"
"v_mfma_f32_16x16x32_bf16 v[168:171], a[240:243], a[48:51], v[168:171]\n"
"s_waitcnt vmcnt(3)\n"
"ds_write_addtid_b32 v139 offset:18080\n"
"v_mfma_f32_16x16x32_bf16 v[168:171], a[244:247], a[52:55], v[168:171]\n"
"s_waitcnt lgkmcnt(8)\n"
"v_mfma_f32_16x16x32_bf16 v[172:175], v[148:151], v[184:187], v[20:23]\n"
"v_mfma_f32_16x16x32_bf16 v[168:171], a[248:251], a[56:59], v[168:171]\n"
"v_mfma_f32_16x16x32_bf16 v[168:171], a[252:255], a[60:63], v[168:171]\n"
"s_waitcnt vmcnt(8)\n"
"v_mfma_f32_16x16x32_bf16 v[128:131], a[128:131], v[64:67], 0\n"
"v_mfma_f32_16x16x32_bf16 v[128:131], a[132:135], v[68:71], v[128:131]\n"
"s_waitcnt vmcnt(5)\n"
"ds_write_addtid_b32 v168 offset:4352\n"
"v_mfma_f32_16x16x32_bf16 v[128:131], a[136:139], v[72:75], v[128:131]\n"
"ds_write_addtid_b32 v169 offset:8992\n"
"v_mfma_f32_16x16x32_bf16 v[128:131], a[140:143], v[76:79], v[128:131]\n"
"s_waitcnt vmcnt(2)\n"
"ds_write_addtid_b32 v170 offset:13696\n"
"v_mfma_f32_16x16x32_bf16 v[128:131], a[144:147], v[80:83], v[128:131]\n"
"ds_write_addtid_b32 v171 offset:18336\n"
"v_mov_b32 v254, v144\n"
"v_mfma_f32_16x16x32_bf16 v[128:131], a[148:151], v[84:87], v[128:131]\n"
"s_waitcnt vmcnt(0)\n"
"v_mul_f32 v135, v145, v39\n"
"v_mfma_f32_16x16x32_bf16 v[128:131], a[152:155], v[88:91], v[128:131]\n"
"v_mul_f32 v133, v145, v37\n"
"v_mul_f32 v134, v145, v38\n"
"v_mfma_f32_16x16x32_bf16 v[128:131], a[156:159], v[92:95], v[128:131]\n"
"v_mul_f32 v139, v145, v43\n"
"v_mul_f32 v132, v145, v36\n"
"v_mfma_f32_16x16x32_bf16 v[140:143], a[160:163], v[64:67], 0\n"
"v_mul_f32 v137, v145, v41\n"
"v_mul_f32 v138, v145, v42\n"
"v_mfma_f32_16x16x32_bf16 v[140:143], a[164:167], v[68:71], v[140:143]\n"
"ds_write_addtid_b32 v128 offset:0\n"
"v_mul_f32 v136, v145, v40\n"
"v_mfma_f32_16x16x32_bf16 v[140:143], a[168:171], v[72:75], v[140:143]\n"
"s_waitcnt lgkmcnt(1)\n"
"ds_write_addtid_b32 v129 offset:4640\n"
"v_mfma_f32_16x16x32_bf16 v[140:143], a[172:175], v[76:79], v[140:143]\n"
"s_barrier\n"
"v_mfma_f32_16x16x32_bf16 v[140:143], a[176:179], v[80:83], v[140:143]\n"
"ds_write_addtid_b32 v130 offset:9344\n"
"ds_read2st64_b32 v[144:145], v12 offset0:0 offset1:1\n"
"v_mfma_f32_16x16x32_bf16 v[140:143], a[180:183], v[84:87], v[140:143]\n"
"ds_write_addtid_b32 v131 offset:13984\n"
"ds_read2st64_b32 v[146:147], v12 offset0:2 offset1:3\n"
"v_mfma_f32_16x16x32_bf16 v[140:143], a[184:187], v[88:91], v[140:143]\n"
"ds_read2st64_b32 v[160:161], v11 offset0:4 offset1:5\n"
"ds_read2st64_b32 v[164:165], v11 offset0:6 offset1:7\n"
"v_mfma_f32_16x16x32_bf16 v[140:143], a[188:191], v[92:95], v[140:143]\n"
"s_add_u32 s94, s50, 0xffe00000\n"
"s_and_b32 s95, s94, 0xfbffffff\n"
"v_mfma_f32_16x16x32_bf16 v[128:131], v[64:67], a[0:3], 0\n"
"v_mfma_f32_16x16x32_bf16 v[128:131], v[68:71], a[4:7], v[128:131]\n"
"ds_write_addtid_b32 v140 offset:256\n"
"v_mfma_f32_16x16x32_bf16 v[128:131], v[72:75], a[8:11], v[128:131]\n"
"s_waitcnt lgkmcnt(3)\n"
"ds_write_addtid_b32 v141 offset:4896\n"
"v_mfma_f32_16x16x32_bf16 v[148:151], v[144:147], v[184:187], v[16:19]\n"
"s_cmp_eq_u32 s94, s62\n"
"s_cbranch_scc1 .LQ_ft1k7s\n"
".LQ_ft1k7r:\n"
"v_mfma_f32_16x16x32_bf16 v[128:131], v[76:79], a[12:15], v[128:131]\n"
"ds_write_addtid_b32 v142 offset:9600\n"
"v_max_f32 v253, v172, v174\n"
"v_mfma_f32_16x16x32_bf16 v[128:131], v[80:83], a[16:19], v[128:131]\n"
"ds_write_addtid_b32 v143 offset:14240\n"
"v_mfma_f32_16x16x32_bf16 v[128:131], v[84:87], a[20:23], v[128:131]\n"
"v_max_f32 v252, v173, v175\n"
"v_max3_f32 v253, v148, v150, v253\n"
"v_mfma_f32_16x16x32_bf16 v[128:131], v[88:91], a[24:27], v[128:131]\n"
"v_max3_f32 v252, v149, v151, v252\n"
"v_max_f32 v251, v253, v252\n"
"v_mfma_f32_16x16x32_bf16 v[128:131], v[92:95], a[28:31], v[128:131]\n"
"v_mov_b32 v250, v251\n"
"v_mfma_f32_16x16x32_bf16 v[140:143], v[64:67], a[32:35], 0\n"
"v_mfma_f32_16x16x32_bf16 v[140:143], v[68:71], a[36:39], v[140:143]\n"
"ds_write_addtid_b32 v128 offset:512\n"
"v_mfma_f32_16x16x32_bf16 v[140:143], v[72:75], a[40:43], v[140:143]\n"
"ds_write_addtid_b32 v129 offset:5152\n"
"v_mfma_f32_16x16x32_bf16 v[140:143], v[76:79], a[44:47], v[140:143]\n"
"ds_write_addtid_b32 v130 offset:9856\n"
"v_mfma_f32_16x16x32_bf16 v[140:143], v[80:83], a[48:51], v[140:143]\n"
"ds_write_addtid_b32 v131 offset:14496\n"
"v_mfma_f32_16x16x32_bf16 v[140:143], v[84:87], a[52:55], v[140:143]\n"
"v_permlane32_swap_b32 v250, v251\n"
"v_mfma_f32_16x16x32_bf16 v[140:143], v[88:91], a[56:59], v[140:143]\n"
"v_max_f32 v251, v250, v251\n"
"v_mfma_f32_16x16x32_bf16 v[140:143], v[92:95], a[60:63], v[140:143]\n"
"v_mov_b32 v250, v251\n"
"v_mfma_f32_16x16x32_bf16 v[132:135], a[64:67], v[64:67], v[132:135]\n"
"v_permlane16_swap_b32 v250, v251\n"
"v_mfma_f32_16x16x32_bf16 v[132:135], a[68:71], v[68:71], v[132:135]\n"
"ds_write_addtid_b32 v140 offset:768\n"
"v_max_f32 v251, v250, v251\n"
"v_mfma_f32_16x16x32_bf16 v[132:135], a[72:75], v[72:75], v[132:135]\n"
"ds_write_addtid_b32 v141 offset:5408\n"
"v_mul_f32 v251, v251, v249\n"
"v_mfma_f32_16x16x32_bf16 v[132:135], a[76:79], v[76:79], v[132:135]\n"
"ds_write_addtid_b32 v142 offset:10112\n"
"v_mfma_f32_16x16x32_bf16 v[132:135], a[80:83], v[80:83], v[132:135]\n"
"ds_write_addtid_b32 v143 offset:14752\n"
"v_fma_f32 v253, v253, v249, -v251\n"
"v_mfma_f32_16x16x32_bf16 v[132:135], a[84:87], v[84:87], v[132:135]\n"
"v_fma_f32 v252, v252, v249, -v251\n"
"v_mfma_f32_16x16x32_bf16 v[132:135], a[88:91], v[88:91], v[132:135]\n"
"v_exp_f32 v253, v253\n"
"v_mfma_f32_16x16x32_bf16 v[132:135], a[92:95], v[92:95], v[132:135]\n"
"v_exp_f32 v252, v252\n"
"v_mfma_f32_16x16x32_bf16 v[136:139], a[96:99], v[64:67], v[136:139]\n"
"v_add_f32 v251, v253, v252\n"
"v_mov_b32 v250, v251\n"
"v_mfma_f32_16x16x32_bf16 v[136:139], a[100:103], v[68:71], v[136:139]\n"
"v_permlane32_swap_b32 v250, v251\n"
"v_mfma_f32_16x16x32_bf16 v[136:139], a[104:107], v[72:75], v[136:139]\n"
"v_add_f32 v251, v250, v251\n"
"v_mov_b32 v250, v251\n"
"v_mfma_f32_16x16x32_bf16 v[136:139], a[108:111], v[76:79], v[136:139]\n"
"v_permlane16_swap_b32 v250, v251\n"
"v_mfma_f32_16x16x32_bf16 v[136:139], a[112:115], v[80:83], v[136:139]\n"
"v_add_f32 v251, v250, v251\n"
"v_mfma_f32_16x16x32_bf16 v[136:139], a[116:119], v[84:87], v[136:139]\n"
"v_rcp_f32 v251, v251\n"
"v_mfma_f32_16x16x32_bf16 v[136:139], a[120:123], v[88:91], v[136:139]\n"
"v_mul_f32 v253, v253, v251\n"
"v_mfma_f32_16x16x32_bf16 v[136:139], a[124:127], v[92:95], v[136:139]\n"
"v_mul_f32 v252, v252, v251\n"
"v_cvt_pk_bf16_f32 v250, v253, v252\n"
"s_waitcnt lgkmcnt(12)\n"
"v_and_b32 v68, v250, v24\n"
"v_and_b32 v69, v250, v25\n"
"v_and_b32 v70, v250, v26\n"
"v_and_b32 v71, v250, v27\n"
"s_nop 1\n"
"v_mfma_f32_16x16x32_bf16 v[72:75], v[160:163], v[68:71], 0\n"
"v_mfma_f32_16x16x32_bf16 v[76:79], v[164:167], v[68:71], 0\n"
"s_nop 6\n"
"v_cvt_pk_bf16_f32 v68, v72, v73\n"
"v_cvt_pk_bf16_f32 v69, v74, v75\n"
"v_cvt_pk_bf16_f32 v70, v76, v77\n"
"v_cvt_pk_bf16_f32 v71, v78, v79\n"
"buffer_store_dwordx4 v[68:71], v13, s[40:43], s95 offen sc1\n"
"s_waitcnt lgkmcnt(0)\n"
"s_barrier\n"
"ds_read_b128 v[72:75], v2 offset:1024\n"
"ds_read_b128 v[76:79], v3 offset:2048\n"
"ds_read_b128 v[80:83], v4 offset:3072\n"
"ds_read_b128 v[84:87], v2 offset:1280\n"
"ds_read_b128 v[64:67], v3 offset:2304\n"
"ds_read_b128 v[88:91], v4 offset:3328\n"
"s_waitcnt lgkmcnt(3)\n"
"v_add_f32 v72, v72, v76\n"
"v_add_f32 v73, v73, v77\n"
"v_add_f32 v74, v74, v78\n"
"v_add_f32 v75, v75, v79\n"
"v_add_f32 v72, v72, v80\n"
"v_add_f32 v73, v73, v81\n"
"v_add_f32 v74, v74, v82\n"
"v_add_f32 v75, v75, v83\n"
"v_add_f32 v72, v72, v132\n"
"v_add_f32 v73, v73, v133\n"
"v_add_f32 v74, v74, v134\n"
"v_add_f32 v75, v75, v135\n"
"v_mul_f32 v72, v254, v72\n"
"v_mul_f32 v73, v254, v73\n"
"v_mul_f32 v74, v254, v74\n"
"v_mul_f32 v75, v254, v75\n"
"v_cvt_pk_bf16_f32 v76, v72, v73\n"
"v_cvt_pk_bf16_f32 v77, v74, v75\n"
"ds_write_addtid_b32 v76 offset:18592\n"
"ds_write_addtid_b32 v77 offset:18848\n"
"s_waitcnt lgkmcnt(2)\n"
"v_add_f32 v84, v84, v64\n"
"v_add_f32 v85, v85, v65\n"
"v_add_f32 v86, v86, v66\n"
"v_add_f32 v87, v87, v67\n"
"v_add_f32 v84, v84, v88\n"
"v_add_f32 v85, v85, v89\n"
"v_add_f32 v86, v86, v90\n"
"v_add_f32 v87, v87, v91\n"
"v_add_f32 v84, v84, v136\n"
"v_add_f32 v85, v85, v137\n"
"v_add_f32 v86, v86, v138\n"
"v_add_f32 v87, v87, v139\n"
"v_mul_f32 v84, v254, v84\n"
"v_mul_f32 v85, v254, v85\n"
"v_mul_f32 v86, v254, v86\n"
"v_mul_f32 v87, v254, v87\n"
"v_cvt_pk_bf16_f32 v78, v84, v85\n"
"v_cvt_pk_bf16_f32 v79, v86, v87\n"
"ds_write_addtid_b32 v78 offset:19104\n"
"ds_write_addtid_b32 v79 offset:19360\n"
"s_waitcnt vmcnt(0)\n"
"buffer_store_dwordx2 v[0:1], v14, s[40:43], s94 offen offset:0 sc1\n"
"s_waitcnt lgkmcnt(0)\n"
"s_barrier\n"
"s_branch .LQ_end\n"
".LQ_t2:\n"
"v_mfma_f32_16x16x32_bf16 v[168:171], a[252:255], a[60:63], v[168:171]\n"
"s_waitcnt vmcnt(17)\n"
"v_mfma_f32_16x16x32_bf16 v[128:131], a[128:131], v[64:67], 0\n"
"v_mfma_f32_16x16x32_bf16 v[128:131], a[132:135], v[68:71], v[128:131]\n"
"s_waitcnt vmcnt(14)\n"
"ds_write_addtid_b32 v168 offset:4352\n"
"v_mfma_f32_16x16x32_bf16 v[128:131], a[136:139], v[72:75], v[128:131]\n"
"ds_write_addtid_b32 v169 offset:8992\n"
"v_mfma_f32_16x16x32_bf16 v[128:131], a[140:143], v[76:79], v[128:131]\n"
"s_waitcnt vmcnt(11)\n"
"ds_write_addtid_b32 v170 offset:13696\n"
"v_mfma_f32_16x16x32_bf16 v[128:131], a[144:147], v[80:83], v[128:131]\n"
"ds_write_addtid_b32 v171 offset:18336\n"
"v_mov_b32 v254, v144\n"
"v_mfma_f32_16x16x32_bf16 v[128:131], a[148:151], v[84:87], v[128:131]\n"
"s_waitcnt vmcnt(9)\n"
"v_mul_f32 v135, v145, v39\n"
"v_mfma_f32_16x16x32_bf16 v[128:131], a[152:155], v[88:91], v[128:131]\n"
"v_mul_f32 v133, v145, v37\n"
"v_mul_f32 v134, v145, v38\n"
"v_mfma_f32_16x16x32_bf16 v[128:131], a[156:159], v[92:95], v[128:131]\n"
"v_mul_f32 v139, v145, v43\n"
"v_mul_f32 v132, v145, v36\n"
"v_mfma_f32_16x16x32_bf16 v[140:143], a[160:163], v[64:67], 0\n"
"v_mul_f32 v137, v145, v41\n"
"v_mul_f32 v138, v145, v42\n"
"v_mfma_f32_16x16x32_bf16 v[140:143], a[164:167], v[68:71], v[140:143]\n"
"ds_write_addtid_b32 v128 offset:0\n"
"v_mul_f32 v136, v145, v40\n"
"v_mfma_f32_16x16x32_bf16 v[140:143], a[168:171], v[72:75], v[140:143]\n"
"s_waitcnt lgkmcnt(1)\n"
"ds_write_addtid_b32 v129 offset:4640\n"
"v_mfma_f32_16x16x32_bf16 v[140:143], a[172:175], v[76:79], v[140:143]\n"
"s_barrier\n"
"v_mfma_f32_16x16x32_bf16 v[140:143], a[176:179], v[80:83], v[140:143]\n"
"ds_write_addtid_b32 v130 offset:9344\n"
"ds_read2st64_b32 v[144:145], v12 offset0:0 offset1:1\n"
"v_mfma_f32_16x16x32_bf16 v[140:143], a[180:183], v[84:87], v[140:143]\n"
"ds_write_addtid_b32 v131 offset:13984\n"
"ds_read2st64_b32 v[146:147], v12 offset0:2 offset1:3\n"
"v_mfma_f32_16x16x32_bf16 v[140:143], a[184:187], v[88:91], v[140:143]\n"
"ds_read2st64_b32 v[160:161], v11 offset0:4 offset1:5\n"
"ds_read2st64_b32 v[164:165], v11 offset0:6 offset1:7\n"
"v_mfma_f32_16x16x32_bf16 v[140:143], a[188:191], v[92:95], v[140:143]\n"
"s_add_u32 s94, s50, 0xffd00000\n"
"s_and_b32 s95, s94, 0xfbffffff\n"
"v_mfma_f32_16x16x32_bf16 v[128:131], v[64:67], a[0:3], 0\n"
"v_mfma_f32_16x16x32_bf16 v[128:131], v[68:71], a[4:7], v[128:131]\n"
"ds_write_addtid_b32 v140 offset:256\n"
"v_mfma_f32_16x16x32_bf16 v[128:131], v[72:75], a[8:11], v[128:131]\n"
"s_waitcnt lgkmcnt(3)\n"
"ds_write_addtid_b32 v141 offset:4896\n"
"v_mfma_f32_16x16x32_bf16 v[148:151], v[144:147], v[184:187], v[16:19]\n"
"s_cmp_eq_u32 s94, s62\n"
"s_cbranch_scc1 .LQ_ft2k7s\n"
".LQ_ft2k7r:\n"
"v_mfma_f32_16x16x32_bf16 v[128:131], v[76:79], a[12:15], v[128:131]\n"
"ds_write_addtid_b32 v142 offset:9600\n"
"v_max_f32 v253, v172, v174\n"
"v_mfma_f32_16x16x32_bf16 v[128:131], v[80:83], a[16:19], v[128:131]\n"
"ds_write_addtid_b32 v143 offset:14240\n"
"v_mfma_f32_16x16x32_bf16 v[128:131], v[84:87], a[20:23], v[128:131]\n"
"v_max_f32 v252, v173, v175\n"
"v_max3_f32 v253, v148, v150, v253\n"
"v_mfma_f32_16x16x32_bf16 v[128:131], v[88:91], a[24:27], v[128:131]\n"
"v_max3_f32 v252, v149, v151, v252\n"
"v_max_f32 v251, v253, v252\n"
"v_mfma_f32_16x16x32_bf16 v[128:131], v[92:95], a[28:31], v[128:131]\n"
"v_mov_b32 v250, v251\n"
"v_mfma_f32_16x16x32_bf16 v[140:143], v[64:67], a[32:35], 0\n"
"v_mfma_f32_16x16x32_bf16 v[140:143], v[68:71], a[36:39], v[140:143]\n"
"ds_write_addtid_b32 v128 offset:512\n"
"v_mfma_f32_16x16x32_bf16 v[140:143], v[72:75], a[40:43], v[140:143]\n"
"ds_write_addtid_b32 v129 offset:5152\n"
"v_mfma_f32_16x16x32_bf16 v[140:143], v[76:79], a[44:47], v[140:143]\n"
"ds_write_addtid_b32 v130 offset:9856\n"
"v_mfma_f32_16x16x32_bf16 v[140:143], v[80:83], a[48:51], v[140:143]\n"
"ds_write_addtid_b32 v131 offset:14496\n"
"v_mfma_f32_16x16x32_bf16 v[140:143], v[84:87], a[52:55], v[140:143]\n"
"v_permlane32_swap_b32 v250, v251\n"
"v_mfma_f32_16x16x32_bf16 v[140:143], v[88:91], a[56:59], v[140:143]\n"
"v_max_f32 v251, v250, v251\n"
"v_mfma_f32_16x16x32_bf16 v[140:143], v[92:95], a[60:63], v[140:143]\n"
"v_mov_b32 v250, v251\n"
"v_mfma_f32_16x16x32_bf16 v[132:135], a[64:67], v[64:67], v[132:135]\n"
"v_permlane16_swap_b32 v250, v251\n"
"v_mfma_f32_16x16x32_bf16 v[132:135], a[68:71], v[68:71], v[132:135]\n"
"ds_write_addtid_b32 v140 offset:768\n"
"v_max_f32 v251, v250, v251\n"
"v_mfma_f32_16x16x32_bf16 v[132:135], a[72:75], v[72:75], v[132:135]\n"
"ds_write_addtid_b32 v141 offset:5408\n"
"v_mul_f32 v251, v251, v249\n"
"v_mfma_f32_16x16x32_bf16 v[132:135], a[76:79], v[76:79], v[132:135]\n"
"ds_write_addtid_b32 v142 offset:10112\n"
"v_mfma_f32_16x16x32_bf16 v[132:135], a[80:83], v[80:83], v[132:135]\n"
"ds_write_addtid_b32 v143 offset:14752\n"
"v_fma_f32 v253, v253, v249, -v251\n"
"v_mfma_f32_16x16x32_bf16 v[132:135], a[84:87], v[84:87], v[132:135]\n"
"v_fma_f32 v252, v252, v249, -v251\n"
"v_mfma_f32_16x16x32_bf16 v[132:135], a[88:91], v[88:91], v[132:135]\n"
"v_exp_f32 v253, v253\n"
"v_mfma_f32_16x16x32_bf16 v[132:135], a[92:95], v[92:95], v[132:135]\n"
"v_exp_f32 v252, v252\n"
"v_mfma_f32_16x16x32_bf16 v[136:139], a[96:99], v[64:67], v[136:139]\n"
"v_add_f32 v251, v253, v252\n"
"v_mov_b32 v250, v251\n"
"v_mfma_f32_16x16x32_bf16 v[136:139], a[100:103], v[68:71], v[136:139]\n"
"v_permlane32_swap_b32 v250, v251\n"
"v_mfma_f32_16x16x32_bf16 v[136:139], a[104:107], v[72:75], v[136:139]\n"
"v_add_f32 v251, v250, v251\n"
"v_mov_b32 v250, v251\n"
"v_mfma_f32_16x16x32_bf16 v[136:139], a[108:111], v[76:79], v[136:139]\n"
"v_permlane16_swap_b32 v250, v251\n"
"v_mfma_f32_16x16x32_bf16 v[136:139], a[112:115], v[80:83], v[136:139]\n"
"s_waitcnt vmcnt(3)\n"
"v_add_f32 v251, v250, v251\n"
"v_mfma_f32_16x16x32_bf16 v[136:139], a[116:119], v[84:87], v[136:139]\n"
"v_rcp_f32 v251, v251\n"
"v_mfma_f32_16x16x32_bf16 v[136:139], a[120:123], v[88:91], v[136:139]\n"
"v_mul_f32 v253, v253, v251\n"
"v_mfma_f32_16x16x32_bf16 v[136:139], a[124:127], v[92:95], v[136:139]\n"
"s_waitcnt vmcnt(7)\n"
"v_mul_f32 v252, v252, v251\n"
"v_mfma_f32_16x16x32_bf16 v[64:67], v[96:99], a[64:67], 0\n"
"v_cvt_pk_bf16_f32 v250, v253, v252\n"
"v_mfma_f32_16x16x32_bf16 v[64:67], v[100:103], a[68:71], v[64:67]\n"
"s_waitcnt vmcnt(5)\n"
"v_mfma_f32_16x16x32_bf16 v[64:67], v[104:107], a[72:75], v[64:67]\n"
"s_waitcnt lgkmcnt(12)\n"
"v_and_b32 v68, v250, v24\n"
"v_and_b32 v69, v250, v25\n"
"v_mfma_f32_16x16x32_bf16 v[64:67], v[108:111], a[76:79], v[64:67]\n"
"s_waitcnt vmcnt(2)\n"
"v_and_b32 v70, v250, v26\n"
"v_and_b32 v71, v250, v27\n"
"v_mfma_f32_16x16x32_bf16 v[64:67], v[112:115], a[80:83], v[64:67]\n"
"v_mfma_f32_16x16x32_bf16 v[72:75], v[160:163], v[68:71], 0\n"
"v_mul_f32_dpp v95, v153, v44 row_newbcast:3 row_mask:0xf bank_mask:0xf\n"
"v_mov_b32 v253, v152\n"
"v_mfma_f32_16x16x32_bf16 v[76:79], v[164:167], v[68:71], 0\n"
"v_mul_f32_dpp v93, v153, v44 row_newbcast:1 row_mask:0xf bank_mask:0xf\n"
"v_mul_f32_dpp v94, v153, v44 row_newbcast:2 row_mask:0xf bank_mask:0xf\n"
"v_mfma_f32_16x16x32_bf16 v[64:67], v[116:119], a[84:87], v[64:67]\n"
"s_waitcnt vmcnt(0)\n"
"v_cvt_pk_bf16_f32 v68, v72, v73\n"
"v_cvt_pk_bf16_f32 v69, v74, v75\n"
"v_mfma_f32_16x16x32_bf16 v[64:67], v[120:123], a[88:91], v[64:67]\n"
"v_cvt_pk_bf16_f32 v70, v76, v77\n"
"v_cvt_pk_bf16_f32 v71, v78, v79\n"
"v_mfma_f32_16x16x32_bf16 v[64:67], v[124:127], a[92:95], v[64:67]\n"
"buffer_store_dwordx4 v[68:71], v13, s[40:43], s95 offen sc1\n"
"v_mul_f32_dpp v130, v153, v45 row_newbcast:2 row_mask:0xf bank_mask:0xf\n"
"v_mul_f32_dpp v131, v153, v45 row_newbcast:3 row_mask:0xf bank_mask:0xf\n"
"v_mul_f32_dpp v92, v153, v44 row_newbcast:0 row_mask:0xf bank_mask:0xf\n"
"v_mfma_f32_16x16x32_bf16 v[68:71], v[96:99], a[96:99], 0\n"
"s_waitcnt lgkmcnt(0)\n"
"v_mul_f32_dpp v128, v153, v45 row_newbcast:0 row_mask:0xf bank_mask:0xf\n"
"v_mul_f32_dpp v129, v153, v45 row_newbcast:1 row_mask:0xf bank_mask:0xf\n"
"v_mfma_f32_16x16x32_bf16 v[68:71], v[100:103], a[100:103], v[68:71]\n"
"s_barrier\n"
"v_mfma_f32_16x16x32_bf16 v[68:71], v[104:107], a[104:107], v[68:71]\n"
"ds_write_addtid_b32 v64 offset:1024\n"
"ds_read_b128 v[72:75], v2 offset:1024\n"
"v_mfma_f32_16x16x32_bf16 v[68:71], v[108:111], a[108:111], v[68:71]\n"
"ds_write_addtid_b32 v65 offset:5664\n"
"ds_read_b128 v[76:79], v3 offset:2048\n"
"v_mfma_f32_16x16x32_bf16 v[68:71], v[112:115], a[112:115], v[68:71]\n"
"ds_write_addtid_b32 v66 offset:10368\n"
"v_mfma_f32_16x16x32_bf16 v[68:71], v[116:119], a[116:119], v[68:71]\n"
"ds_write_addtid_b32 v67 offset:15008\n"
"ds_read_b128 v[80:83], v4 offset:3072\n"
"v_mfma_f32_16x16x32_bf16 v[68:71], v[120:123], a[120:123], v[68:71]\n"
"ds_read_b128 v[84:87], v2 offset:1280\n"
"ds_read_b128 v[64:67], v3 offset:2304\n"
"v_mfma_f32_16x16x32_bf16 v[68:71], v[124:127], a[124:127], v[68:71]\n"
"ds_read_b128 v[88:91], v4 offset:3328\n"
"v_mfma_f32_16x16x32_bf16 v[140:143], v[96:99], a[0:3], 0\n"
"v_mfma_f32_16x16x32_bf16 v[140:143], v[100:103], a[4:7], v[140:143]\n"
"ds_write_addtid_b32 v68 offset:1280\n"
"v_mfma_f32_16x16x32_bf16 v[140:143], v[104:107], a[8:11], v[140:143]\n"
"ds_write_addtid_b32 v69 offset:5920\n"
"v_mfma_f32_16x16x32_bf16 v[140:143], v[108:111], a[12:15], v[140:143]\n"
"s_waitcnt lgkmcnt(5)\n"
"ds_write_addtid_b32 v70 offset:10624\n"
"v_add_f32 v72, v72, v76\n"
"v_mfma_f32_16x16x32_bf16 v[140:143], v[112:115], a[16:19], v[140:143]\n"
"ds_write_addtid_b32 v71 offset:15264\n"
"v_add_f32 v73, v73, v77\n"
"v_mfma_f32_16x16x32_bf16 v[140:143], v[116:119], a[20:23], v[140:143]\n"
"v_add_f32 v74, v74, v78\n"
"v_mfma_f32_16x16x32_bf16 v[140:143], v[120:123], a[24:27], v[140:143]\n"
"v_add_f32 v75, v75, v79\n"
"v_add_f32 v72, v72, v80\n"
"v_mfma_f32_16x16x32_bf16 v[140:143], v[124:127], a[28:31], v[140:143]\n"
"v_add_f32 v73, v73, v81\n"
"v_add_f32 v74, v74, v82\n"
"v_mfma_f32_16x16x32_bf16 v[68:71], v[96:99], a[32:35], 0\n"
"v_add_f32 v75, v75, v83\n"
"v_add_f32 v72, v72, v132\n"
"v_mfma_f32_16x16x32_bf16 v[68:71], v[100:103], a[36:39], v[68:71]\n"
"ds_write_addtid_b32 v140 offset:1536\n"
"v_add_f32 v73, v73, v133\n"
"v_mfma_f32_16x16x32_bf16 v[68:71], v[104:107], a[40:43], v[68:71]\n"
"ds_write_addtid_b32 v141 offset:6176\n"
"v_mfma_f32_16x16x32_bf16 v[68:71], v[108:111], a[44:47], v[68:71]\n"
"ds_write_addtid_b32 v142 offset:10880\n"
"v_add_f32 v74, v74, v134\n"
"v_mfma_f32_16x16x32_bf16 v[68:71], v[112:115], a[48:51], v[68:71]\n"
"ds_write_addtid_b32 v143 offset:15520\n"
"v_add_f32 v75, v75, v135\n"
"v_mfma_f32_16x16x32_bf16 v[68:71], v[116:119], a[52:55], v[68:71]\n"
"v_mul_f32 v72, v254, v72\n"
"v_mul_f32 v73, v254, v73\n"
"v_mfma_f32_16x16x32_bf16 v[68:71], v[120:123], a[56:59], v[68:71]\n"
"v_mul_f32 v74, v254, v74\n"
"v_mul_f32 v75, v254, v75\n"
"v_mfma_f32_16x16x32_bf16 v[68:71], v[124:127], a[60:63], v[68:71]\n"
"v_cvt_pk_bf16_f32 v76, v72, v73\n"
"v_mfma_f32_16x16x32_bf16 v[92:95], v[96:99], a[128:131], v[92:95]\n"
"v_mfma_f32_16x16x32_bf16 v[92:95], v[100:103], a[132:135], v[92:95]\n"
"ds_write_addtid_b32 v68 offset:1792\n"
"v_cvt_pk_bf16_f32 v77, v74, v75\n"
"v_mfma_f32_16x16x32_bf16 v[92:95], v[104:107], a[136:139], v[92:95]\n"
"ds_write_addtid_b32 v69 offset:6432\n"
"ds_write_addtid_b32 v76 offset:18592\n"
"v_mfma_f32_16x16x32_bf16 v[92:95], v[108:111], a[140:143], v[92:95]\n"
"ds_write_addtid_b32 v70 offset:11136\n"
"ds_write_addtid_b32 v77 offset:18848\n"
"v_mfma_f32_16x16x32_bf16 v[92:95], v[112:115], a[144:147], v[92:95]\n"
"s_waitcnt lgkmcnt(13)\n"
"ds_write_addtid_b32 v71 offset:15776\n"
"v_mfma_f32_16x16x32_bf16 v[92:95], v[116:119], a[148:151], v[92:95]\n"
"v_add_f32 v84, v84, v64\n"
"v_add_f32 v85, v85, v65\n"
"v_mfma_f32_16x16x32_bf16 v[92:95], v[120:123], a[152:155], v[92:95]\n"
"v_add_f32 v86, v86, v66\n"
"v_add_f32 v87, v87, v67\n"
"v_mfma_f32_16x16x32_bf16 v[92:95], v[124:127], a[156:159], v[92:95]\n"
"v_add_f32 v84, v84, v88\n"
"v_add_f32 v85, v85, v89\n"
"v_mfma_f32_16x16x32_bf16 v[128:131], v[96:99], a[160:163], v[128:131]\n"
"v_add_f32 v86, v86, v90\n"
"v_add_f32 v87, v87, v91\n"
"v_mfma_f32_16x16x32_bf16 v[128:131], v[100:103], a[164:167], v[128:131]\n"
"v_add_f32 v84, v84, v136\n"
"v_add_f32 v85, v85, v137\n"
"v_mfma_f32_16x16x32_bf16 v[128:131], v[104:107], a[168:171], v[128:131]\n"
"v_add_f32 v86, v86, v138\n"
"v_add_f32 v87, v87, v139\n"
"v_mfma_f32_16x16x32_bf16 v[128:131], v[108:111], a[172:175], v[128:131]\n"
"v_mul_f32 v84, v254, v84\n"
"v_mul_f32 v85, v254, v85\n"
"v_mfma_f32_16x16x32_bf16 v[128:131], v[112:115], a[176:179], v[128:131]\n"
"v_mul_f32 v86, v254, v86\n"
"v_mfma_f32_16x16x32_bf16 v[128:131], v[116:119], a[180:183], v[128:131]\n"
"v_mul_f32 v87, v254, v87\n"
"v_cvt_pk_bf16_f32 v78, v84, v85\n"
"v_mfma_f32_16x16x32_bf16 v[128:131], v[120:123], a[184:187], v[128:131]\n"
"v_cvt_pk_bf16_f32 v79, v86, v87\n"
"v_mfma_f32_16x16x32_bf16 v[128:131], v[124:127], a[188:191], v[128:131]\n"
"ds_write_addtid_b32 v78 offset:19104\n"
"ds_write_addtid_b32 v79 offset:19360\n"
"s_waitcnt vmcnt(0)\n"
"buffer_store_dwordx2 v[0:1], v14, s[40:43], s94 offen offset:0 sc1\n"
"s_waitcnt lgkmcnt(0)\n"
"s_barrier\n"
"ds_read_b128 v[96:99], v2 offset:2560\n"
"ds_read_b128 v[88:91], v3 offset:3584\n"
"ds_read_b128 v[100:103], v4 offset:0\n"
"ds_read_b128 v[104:107], v2 offset:2816\n"
"ds_read_b128 v[108:111], v3 offset:3840\n"
"ds_read_b128 v[112:115], v4 offset:256\n"
"s_waitcnt lgkmcnt(3)\n"
"v_add_f32 v96, v96, v88\n"
"v_add_f32 v97, v97, v89\n"
"v_add_f32 v98, v98, v90\n"
"v_add_f32 v99, v99, v91\n"
"v_add_f32 v96, v96, v100\n"
"v_add_f32 v97, v97, v101\n"
"v_add_f32 v98, v98, v102\n"
"v_add_f32 v99, v99, v103\n"
"v_add_f32 v96, v96, v92\n"
"v_add_f32 v97, v97, v93\n"
"v_add_f32 v98, v98, v94\n"
"v_add_f32 v99, v99, v95\n"
"v_mul_f32_dpp v96, v253, v96 row_newbcast:0 row_mask:0xf bank_mask:0xf\n"
"v_mul_f32_dpp v97, v253, v97 row_newbcast:1 row_mask:0xf bank_mask:0xf\n"
"v_mul_f32_dpp v98, v253, v98 row_newbcast:2 row_mask:0xf bank_mask:0xf\n"
"v_mul_f32_dpp v99, v253, v99 row_newbcast:3 row_mask:0xf bank_mask:0xf\n"
"v_cvt_pk_bf16_f32 v162, v96, v97\n"
"v_cvt_pk_bf16_f32 v163, v98, v99\n"
"ds_write_addtid_b32 v162 offset:19616\n"
"ds_write_addtid_b32 v163 offset:19872\n"
"s_waitcnt lgkmcnt(2)\n"
"v_add_f32 v104, v104, v108\n"
"v_add_f32 v105, v105, v109\n"
"v_add_f32 v106, v106, v110\n"
"v_add_f32 v107, v107, v111\n"
"v_add_f32 v104, v104, v112\n"
"v_add_f32 v105, v105, v113\n"
"v_add_f32 v106, v106, v114\n"
"v_add_f32 v107, v107, v115\n"
"v_add_f32 v104, v104, v128\n"
"v_add_f32 v105, v105, v129\n"
"v_add_f32 v106, v106, v130\n"
"v_add_f32 v107, v107, v131\n"
"v_mul_f32_dpp v104, v253, v104 row_newbcast:0 row_mask:0xf bank_mask:0xf\n"
"v_mul_f32_dpp v105, v253, v105 row_newbcast:1 row_mask:0xf bank_mask:0xf\n"
"v_mul_f32_dpp v106, v253, v106 row_newbcast:2 row_mask:0xf bank_mask:0xf\n"
"v_mul_f32_dpp v107, v253, v107 row_newbcast:3 row_mask:0xf bank_mask:0xf\n"
"v_cvt_pk_bf16_f32 v166, v104, v105\n"
"v_cvt_pk_bf16_f32 v167, v106, v107\n"
"ds_write_addtid_b32 v166 offset:20128\n"
"ds_write_addtid_b32 v167 offset:20384\n"
"s_waitcnt lgkmcnt(0)\n"
"s_barrier\n"
"s_branch .LQ_end\n"
".LQ_t3:\n"
"v_mul_f32 v253, v253, v251\n"
"v_mfma_f32_16x16x32_bf16 v[136:139], a[124:127], v[92:95], v[136:139]\n"
"s_waitcnt vmcnt(16)\n"
"v_mul_f32 v252, v252, v251\n"
"v_mfma_f32_16x16x32_bf16 v[64:67], v[96:99], a[64:67], 0\n"
"v_cvt_pk_bf16_f32 v250, v253, v252\n"
"v_mfma_f32_16x16x32_bf16 v[64:67], v[100:103], a[68:71], v[64:67]\n"
"s_waitcnt vmcnt(14)\n"
"v_mfma_f32_16x16x32_bf16 v[64:67], v[104:107], a[72:75], v[64:67]\n"
"s_waitcnt lgkmcnt(12)\n"
"v_and_b32 v68, v250, v24\n"
"v_and_b32 v69, v250, v25\n"
"v_mfma_f32_16x16x32_bf16 v[64:67], v[108:111], a[76:79], v[64:67]\n"
"s_waitcnt vmcnt(11)\n"
"v_and_b32 v70, v250, v26\n"
"v_and_b32 v71, v250, v27\n"
"v_mfma_f32_16x16x32_bf16 v[64:67], v[112:115], a[80:83], v[64:67]\n"
"v_mfma_f32_16x16x32_bf16 v[72:75], v[160:163], v[68:71], 0\n"
"v_mul_f32_dpp v95, v153, v44 row_newbcast:3 row_mask:0xf bank_mask:0xf\n"
"v_mov_b32 v253, v152\n"
"v_mfma_f32_16x16x32_bf16 v[76:79], v[164:167], v[68:71], 0\n"
"v_mul_f32_dpp v93, v153, v44 row_newbcast:1 row_mask:0xf bank_mask:0xf\n"
"v_mul_f32_dpp v94, v153, v44 row_newbcast:2 row_mask:0xf bank_mask:0xf\n"
"v_mfma_f32_16x16x32_bf16 v[64:67], v[116:119], a[84:87], v[64:67]\n"
"s_waitcnt vmcnt(9)\n"
"v_cvt_pk_bf16_f32 v68, v72, v73\n"
"v_cvt_pk_bf16_f32 v69, v74, v75\n"
"v_mfma_f32_16x16x32_bf16 v[64:67], v[120:123], a[88:91], v[64:67]\n"
"v_cvt_pk_bf16_f32 v70, v76, v77\n"
"v_cvt_pk_bf16_f32 v71, v78, v79\n"
"v_mfma_f32_16x16x32_bf16 v[64:67], v[124:127], a[92:95], v[64:67]\n"
"buffer_store_dwordx4 v[68:71], v13, s[40:43], s95 offen sc1\n"
"v_mul_f32_dpp v130, v153, v45 row_newbcast:2 row_mask:0xf bank_mask:0xf\n"
"v_mul_f32_dpp v131, v153, v45 row_newbcast:3 row_mask:0xf bank_mask:0xf\n"
"v_mul_f32_dpp v92, v153, v44 row_newbcast:0 row_mask:0xf bank_mask:0xf\n"
"v_mfma_f32_16x16x32_bf16 v[68:71], v[96:99], a[96:99], 0\n"
"s_waitcnt lgkmcnt(0)\n"
"v_mul_f32_dpp v128, v153, v45 row_newbcast:0 row_mask:0xf bank_mask:0xf\n"
"v_mul_f32_dpp v129, v153, v45 row_newbcast:1 row_mask:0xf bank_mask:0xf\n"
"v_mfma_f32_16x16x32_bf16 v[68:71], v[100:103], a[100:103], v[68:71]\n"
"s_barrier\n"
"v_mfma_f32_16x16x32_bf16 v[68:71], v[104:107], a[104:107], v[68:71]\n"
"ds_write_addtid_b32 v64 offset:1024\n"
"ds_read_b128 v[72:75], v2 offset:1024\n"
"v_mfma_f32_16x16x32_bf16 v[68:71], v[108:111], a[108:111], v[68:71]\n"
"ds_write_addtid_b32 v65 offset:5664\n"
"ds_read_b128 v[76:79], v3 offset:2048\n"
"v_mfma_f32_16x16x32_bf16 v[68:71], v[112:115], a[112:115], v[68:71]\n"
"ds_write_addtid_b32 v66 offset:10368\n"
"v_mfma_f32_16x16x32_bf16 v[68:71], v[116:119], a[116:119], v[68:71]\n"
"ds_write_addtid_b32 v67 offset:15008\n"
"ds_read_b128 v[80:83], v4 offset:3072\n"
"v_mfma_f32_16x16x32_bf16 v[68:71], v[120:123], a[120:123], v[68:71]\n"
"ds_read_b128 v[84:87], v2 offset:1280\n"
"ds_read_b128 v[64:67], v3 offset:2304\n"
"v_mfma_f32_16x16x32_bf16 v[68:71], v[124:127], a[124:127], v[68:71]\n"
"ds_read_b128 v[88:91], v4 offset:3328\n"
"v_mfma_f32_16x16x32_bf16 v[140:143], v[96:99], a[0:3], 0\n"
"v_mfma_f32_16x16x32_bf16 v[140:143], v[100:103], a[4:7], v[140:143]\n"
"ds_write_addtid_b32 v68 offset:1280\n"
"v_mfma_f32_16x16x32_bf16 v[140:143], v[104:107], a[8:11], v[140:143]\n"
"ds_write_addtid_b32 v69 offset:5920\n"
"v_mfma_f32_16x16x32_bf16 v[140:143], v[108:111], a[12:15], v[140:143]\n"
"s_waitcnt lgkmcnt(5)\n"
"ds_write_addtid_b32 v70 offset:10624\n"
"v_add_f32 v72, v72, v76\n"
"v_mfma_f32_16x16x32_bf16 v[140:143], v[112:115], a[16:19], v[140:143]\n"
"ds_write_addtid_b32 v71 offset:15264\n"
"v_add_f32 v73, v73, v77\n"
"v_mfma_f32_16x16x32_bf16 v[140:143], v[116:119], a[20:23], v[140:143]\n"
"v_add_f32 v74, v74, v78\n"
"v_mfma_f32_16x16x32_bf16 v[140:143], v[120:123], a[24:27], v[140:143]\n"
"v_add_f32 v75, v75, v79\n"
"v_add_f32 v72, v72, v80\n"
"v_mfma_f32_16x16x32_bf16 v[140:143], v[124:127], a[28:31], v[140:143]\n"
"v_add_f32 v73, v73, v81\n"
"v_add_f32 v74, v74, v82\n"
"v_mfma_f32_16x16x32_bf16 v[68:71], v[96:99], a[32:35], 0\n"
"v_add_f32 v75, v75, v83\n"
"v_add_f32 v72, v72, v132\n"
"v_mfma_f32_16x16x32_bf16 v[68:71], v[100:103], a[36:39], v[68:71]\n"
"ds_write_addtid_b32 v140 offset:1536\n"
"v_add_f32 v73, v73, v133\n"
"v_mfma_f32_16x16x32_bf16 v[68:71], v[104:107], a[40:43], v[68:71]\n"
"ds_write_addtid_b32 v141 offset:6176\n"
"v_mfma_f32_16x16x32_bf16 v[68:71], v[108:111], a[44:47], v[68:71]\n"
"ds_write_addtid_b32 v142 offset:10880\n"
"v_add_f32 v74, v74, v134\n"
"v_mfma_f32_16x16x32_bf16 v[68:71], v[112:115], a[48:51], v[68:71]\n"
"ds_write_addtid_b32 v143 offset:15520\n"
"v_add_f32 v75, v75, v135\n"
"v_mfma_f32_16x16x32_bf16 v[68:71], v[116:119], a[52:55], v[68:71]\n"
"v_mul_f32 v72, v254, v72\n"
"v_mul_f32 v73, v254, v73\n"
"v_mfma_f32_16x16x32_bf16 v[68:71], v[120:123], a[56:59], v[68:71]\n"
"v_mul_f32 v74, v254, v74\n"
"v_mul_f32 v75, v254, v75\n"
"v_mfma_f32_16x16x32_bf16 v[68:71], v[124:127], a[60:63], v[68:71]\n"
"v_cvt_pk_bf16_f32 v76, v72, v73\n"
"v_mfma_f32_16x16x32_bf16 v[92:95], v[96:99], a[128:131], v[92:95]\n"
"v_mfma_f32_16x16x32_bf16 v[92:95], v[100:103], a[132:135], v[92:95]\n"
"ds_write_addtid_b32 v68 offset:1792\n"
"v_cvt_pk_bf16_f32 v77, v74, v75\n"
"v_mfma_f32_16x16x32_bf16 v[92:95], v[104:107], a[136:139], v[92:95]\n"
"ds_write_addtid_b32 v69 offset:6432\n"
"ds_write_addtid_b32 v76 offset:18592\n"
"v_mfma_f32_16x16x32_bf16 v[92:95], v[108:111], a[140:143], v[92:95]\n"
"ds_write_addtid_b32 v70 offset:11136\n"
"ds_write_addtid_b32 v77 offset:18848\n"
"v_mfma_f32_16x16x32_bf16 v[92:95], v[112:115], a[144:147], v[92:95]\n"
"s_waitcnt lgkmcnt(13)\n"
"ds_write_addtid_b32 v71 offset:15776\n"
"v_mfma_f32_16x16x32_bf16 v[92:95], v[116:119], a[148:151], v[92:95]\n"
"v_add_f32 v84, v84, v64\n"
"v_add_f32 v85, v85, v65\n"
"v_mfma_f32_16x16x32_bf16 v[92:95], v[120:123], a[152:155], v[92:95]\n"
"v_add_f32 v86, v86, v66\n"
"v_add_f32 v87, v87, v67\n"
"v_mfma_f32_16x16x32_bf16 v[92:95], v[124:127], a[156:159], v[92:95]\n"
"v_add_f32 v84, v84, v88\n"
"v_add_f32 v85, v85, v89\n"
"v_mfma_f32_16x16x32_bf16 v[128:131], v[96:99], a[160:163], v[128:131]\n"
"v_add_f32 v86, v86, v90\n"
"v_add_f32 v87, v87, v91\n"
"v_mfma_f32_16x16x32_bf16 v[128:131], v[100:103], a[164:167], v[128:131]\n"
"v_add_f32 v84, v84, v136\n"
"v_add_f32 v85, v85, v137\n"
"v_mfma_f32_16x16x32_bf16 v[128:131], v[104:107], a[168:171], v[128:131]\n"
"v_add_f32 v86, v86, v138\n"
"v_add_f32 v87, v87, v139\n"
"v_mfma_f32_16x16x32_bf16 v[128:131], v[108:111], a[172:175], v[128:131]\n"
"v_mul_f32 v84, v254, v84\n"
"v_mul_f32 v85, v254, v85\n"
"v_mfma_f32_16x16x32_bf16 v[128:131], v[112:115], a[176:179], v[128:131]\n"
"s_waitcnt vmcnt(4)\n"
"v_mul_f32 v86, v254, v86\n"
"v_mfma_f32_16x16x32_bf16 v[128:131], v[116:119], a[180:183], v[128:131]\n"
"v_mul_f32 v87, v254, v87\n"
"v_cvt_pk_bf16_f32 v78, v84, v85\n"
"v_mfma_f32_16x16x32_bf16 v[128:131], v[120:123], a[184:187], v[128:131]\n"
"v_cvt_pk_bf16_f32 v79, v86, v87\n"
"v_mfma_f32_16x16x32_bf16 v[128:131], v[124:127], a[188:191], v[128:131]\n"
"s_waitcnt vmcnt(8)\n"
"ds_write_addtid_b32 v78 offset:19104\n"
"ds_write_addtid_b32 v79 offset:19360\n"
"v_mfma_f32_16x16x32_bf16 v[80:83], a[192:195], a[64:67], 0\n"
"v_mfma_f32_16x16x32_bf16 v[80:83], a[196:199], a[68:71], v[80:83]\n"
"s_waitcnt vmcnt(6)\n"
"v_mfma_f32_16x16x32_bf16 v[80:83], a[200:203], a[72:75], v[80:83]\n"
"v_mfma_f32_16x16x32_bf16 v[80:83], a[204:207], a[76:79], v[80:83]\n"
"s_waitcnt vmcnt(3)\n"
"v_mul_f32 v249, 0x3e8293ee, v156\n"
"v_mfma_f32_16x16x32_bf16 v[80:83], a[208:211], a[80:83], v[80:83]\n"
"v_mul_f32 v126, v157, v30\n"
"v_mul_f32 v127, v157, v31\n"
"v_mfma_f32_16x16x32_bf16 v[80:83], a[212:215], a[84:87], v[80:83]\n"
"s_waitcnt vmcnt(1)\n"
"v_mul_f32 v125, v157, v29\n"
"v_mfma_f32_16x16x32_bf16 v[80:83], a[216:219], a[88:91], v[80:83]\n"
"v_mul_f32 v135, v157, v35\n"
"v_mul_f32 v124, v157, v28\n"
"v_mfma_f32_16x16x32_bf16 v[80:83], a[220:223], a[92:95], v[80:83]\n"
"s_waitcnt vmcnt(0)\n"
"buffer_store_dwordx2 v[0:1], v14, s[40:43], s94 offen offset:0 sc1\n"
"v_mul_f32 v134, v157, v34\n"
"v_mfma_f32_16x16x32_bf16 v[84:87], a[192:195], a[96:99], 0\n"
"s_waitcnt lgkmcnt(0)\n"
"v_mul_f32 v132, v157, v32\n"
"v_mul_f32 v133, v157, v33\n"
"v_mfma_f32_16x16x32_bf16 v[84:87], a[196:199], a[100:103], v[84:87]\n"
"s_barrier\n"
"v_mfma_f32_16x16x32_bf16 v[84:87], a[200:203], a[104:107], v[84:87]\n"
"ds_write_addtid_b32 v80 offset:2048\n"
"v_mfma_f32_16x16x32_bf16 v[84:87], a[204:207], a[108:111], v[84:87]\n"
"ds_write_addtid_b32 v81 offset:6688\n"
"ds_read_b128 v[96:99], v2 offset:2560\n"
"v_mfma_f32_16x16x32_bf16 v[84:87], a[208:211], a[112:115], v[84:87]\n"
"ds_write_addtid_b32 v82 offset:11392\n"
"ds_read_b128 v[88:91], v3 offset:3584\n"
"v_mfma_f32_16x16x32_bf16 v[84:87], a[212:215], a[116:119], v[84:87]\n"
"ds_write_addtid_b32 v83 offset:16032\n"
"ds_read_b128 v[100:103], v4 offset:0\n"
"v_mfma_f32_16x16x32_bf16 v[84:87], a[216:219], a[120:123], v[84:87]\n"
"ds_read_b128 v[104:107], v2 offset:2816\n"
"ds_read_b128 v[108:111], v3 offset:3840\n"
"v_mfma_f32_16x16x32_bf16 v[84:87], a[220:223], a[124:127], v[84:87]\n"
"ds_read_b128 v[112:115], v4 offset:256\n"
"v_mfma_f32_16x16x32_bf16 v[116:119], a[128:131], a[192:195], 0\n"
"v_mfma_f32_16x16x32_bf16 v[116:119], a[132:135], a[196:199], v[116:119]\n"
"ds_write_addtid_b32 v84 offset:2304\n"
"v_mfma_f32_16x16x32_bf16 v[116:119], a[136:139], a[200:203], v[116:119]\n"
"ds_write_addtid_b32 v85 offset:6944\n"
"v_mfma_f32_16x16x32_bf16 v[116:119], a[140:143], a[204:207], v[116:119]\n"
"s_waitcnt lgkmcnt(5)\n"
"ds_write_addtid_b32 v86 offset:11648\n"
"v_add_f32 v96, v96, v88\n"
"v_mfma_f32_16x16x32_bf16 v[116:119], a[144:147], a[208:211], v[116:119]\n"
"ds_write_addtid_b32 v87 offset:16288\n"
"v_mfma_f32_16x16x32_bf16 v[116:119], a[148:151], a[212:215], v[116:119]\n"
"v_add_f32 v97, v97, v89\n"
"v_add_f32 v98, v98, v90\n"
"v_mfma_f32_16x16x32_bf16 v[116:119], a[152:155], a[216:219], v[116:119]\n"
"v_add_f32 v99, v99, v91\n"
"v_add_f32 v96, v96, v100\n"
"v_mfma_f32_16x16x32_bf16 v[116:119], a[156:159], a[220:223], v[116:119]\n"
"v_add_f32 v97, v97, v101\n"
"v_add_f32 v98, v98, v102\n"
"v_mfma_f32_16x16x32_bf16 v[120:123], a[160:163], a[192:195], 0\n"
"v_mfma_f32_16x16x32_bf16 v[120:123], a[164:167], a[196:199], v[120:123]\n"
"ds_write_addtid_b32 v116 offset:2560\n"
"v_mfma_f32_16x16x32_bf16 v[120:123], a[168:171], a[200:203], v[120:123]\n"
"ds_write_addtid_b32 v117 offset:7200\n"
"v_add_f32 v99, v99, v103\n"
"v_mfma_f32_16x16x32_bf16 v[120:123], a[172:175], a[204:207], v[120:123]\n"
"ds_write_addtid_b32 v118 offset:11904\n"
"v_add_f32 v96, v96, v92\n"
"v_mfma_f32_16x16x32_bf16 v[120:123], a[176:179], a[208:211], v[120:123]\n"
"ds_write_addtid_b32 v119 offset:16544\n"
"v_add_f32 v97, v97, v93\n"
"v_mfma_f32_16x16x32_bf16 v[120:123], a[180:183], a[212:215], v[120:123]\n"
"v_add_f32 v98, v98, v94\n"
"v_add_f32 v99, v99, v95\n"
"v_mfma_f32_16x16x32_bf16 v[120:123], a[184:187], a[216:219], v[120:123]\n"
"v_mul_f32_dpp v96, v253, v96 row_newbcast:0 row_mask:0xf bank_mask:0xf\n"
"v_mfma_f32_16x16x32_bf16 v[120:123], a[188:191], a[220:223], v[120:123]\n"
"v_mul_f32_dpp v97, v253, v97 row_newbcast:1 row_mask:0xf bank_mask:0xf\n"
"v_mul_f32_dpp v98, v253, v98 row_newbcast:2 row_mask:0xf bank_mask:0xf\n"
"v_mfma_f32_16x16x32_bf16 v[124:127], a[0:3], a[192:195], v[124:127]\n"
"v_mul_f32_dpp v99, v253, v99 row_newbcast:3 row_mask:0xf bank_mask:0xf\n"
"v_cvt_pk_bf16_f32 v162, v96, v97\n"
"v_mfma_f32_16x16x32_bf16 v[124:127], a[4:7], a[196:199], v[124:127]\n"
"ds_write_addtid_b32 v120 offset:2816\n"
"v_cvt_pk_bf16_f32 v163, v98, v99\n"
"v_mfma_f32_16x16x32_bf16 v[124:127], a[8:11], a[200:203], v[124:127]\n"
"ds_write_addtid_b32 v121 offset:7456\n"
"ds_write_addtid_b32 v162 offset:19616\n"
"v_mfma_f32_16x16x32_bf16 v[124:127], a[12:15], a[204:207], v[124:127]\n"
"ds_write_addtid_b32 v122 offset:12160\n"
"v_mfma_f32_16x16x32_bf16 v[124:127], a[16:19], a[208:211], v[124:127]\n"
"ds_write_addtid_b32 v123 offset:16800\n"
"ds_write_addtid_b32 v163 offset:19872\n"
"v_mfma_f32_16x16x32_bf16 v[124:127], a[20:23], a[212:215], v[124:127]\n"
"s_waitcnt lgkmcnt(14)\n"
"v_add_f32 v104, v104, v108\n"
"v_add_f32 v105, v105, v109\n"
"v_mfma_f32_16x16x32_bf16 v[124:127], a[24:27], a[216:219], v[124:127]\n"
"v_add_f32 v106, v106, v110\n"
"v_add_f32 v107, v107, v111\n"
"v_mfma_f32_16x16x32_bf16 v[124:127], a[28:31], a[220:223], v[124:127]\n"
"v_add_f32 v104, v104, v112\n"
"v_add_f32 v105, v105, v113\n"
"v_mfma_f32_16x16x32_bf16 v[132:135], a[32:35], a[192:195], v[132:135]\n"
"v_add_f32 v106, v106, v114\n"
"v_add_f32 v107, v107, v115\n"
"v_mfma_f32_16x16x32_bf16 v[132:135], a[36:39], a[196:199], v[132:135]\n"
"v_add_f32 v104, v104, v128\n"
"v_add_f32 v105, v105, v129\n"
"v_mfma_f32_16x16x32_bf16 v[132:135], a[40:43], a[200:203], v[132:135]\n"
"v_add_f32 v106, v106, v130\n"
"v_add_f32 v107, v107, v131\n"
"v_mfma_f32_16x16x32_bf16 v[132:135], a[44:47], a[204:207], v[132:135]\n"
"v_mul_f32_dpp v104, v253, v104 row_newbcast:0 row_mask:0xf bank_mask:0xf\n"
"v_mul_f32_dpp v105, v253, v105 row_newbcast:1 row_mask:0xf bank_mask:0xf\n"
"v_mfma_f32_16x16x32_bf16 v[132:135], a[48:51], a[208:211], v[132:135]\n"
"v_mul_f32_dpp v106, v253, v106 row_newbcast:2 row_mask:0xf bank_mask:0xf\n"
"v_mul_f32_dpp v107, v253, v107 row_newbcast:3 row_mask:0xf bank_mask:0xf\n"
"v_mfma_f32_16x16x32_bf16 v[132:135], a[52:55], a[212:215], v[132:135]\n"
"v_cvt_pk_bf16_f32 v166, v104, v105\n"
"v_cvt_pk_bf16_f32 v167, v106, v107\n"
"v_mfma_f32_16x16x32_bf16 v[132:135], a[56:59], a[216:219], v[132:135]\n"
"ds_write_addtid_b32 v166 offset:20128\n"
"v_mfma_f32_16x16x32_bf16 v[132:135], a[60:63], a[220:223], v[132:135]\n"
"ds_write_addtid_b32 v167 offset:20384\n"
"s_waitcnt lgkmcnt(0)\n"
"s_barrier\n"
"ds_read_b128 v[128:131], v2 offset:4096\n"
"ds_read_b128 v[120:123], v3 offset:512\n"
"ds_read_b128 v[136:139], v4 offset:1536\n"
"ds_read_b128 v[140:143], v2 offset:4352\n"
"ds_read_b128 v[168:171], v3 offset:768\n"
"ds_read_b128 v[172:175], v4 offset:1792\n"
"s_waitcnt lgkmcnt(3)\n"
"v_add_f32 v128, v128, v120\n"
"v_add_f32 v129, v129, v121\n"
"v_add_f32 v130, v130, v122\n"
"v_add_f32 v131, v131, v123\n"
"v_add_f32 v128, v128, v136\n"
"v_add_f32 v129, v129, v137\n"
"v_add_f32 v130, v130, v138\n"
"v_add_f32 v131, v131, v139\n"
"v_add_f32 v128, v128, v124\n"
"v_add_f32 v129, v129, v125\n"
"v_add_f32 v130, v130, v126\n"
"v_add_f32 v131, v131, v127\n"
"v_cvt_pk_bf16_f32 v184, v128, v129\n"
"v_cvt_pk_bf16_f32 v185, v130, v131\n"
"ds_read2st64_b32 v[148:149], v11 offset0:0 offset1:1\n"
"ds_read2st64_b32 v[150:151], v11 offset0:2 offset1:3\n"
"s_waitcnt lgkmcnt(2)\n"
"v_add_f32 v140, v140, v168\n"
"v_add_f32 v141, v141, v169\n"
"v_add_f32 v142, v142, v170\n"
"v_add_f32 v143, v143, v171\n"
"v_add_f32 v140, v140, v172\n"
"v_add_f32 v141, v141, v173\n"
"v_add_f32 v142, v142, v174\n"
"v_add_f32 v143, v143, v175\n"
"v_add_f32 v140, v140, v132\n"
"v_add_f32 v141, v141, v133\n"
"v_add_f32 v142, v142, v134\n"
"v_add_f32 v143, v143, v135\n"
"v_cvt_pk_bf16_f32 v186, v140, v141\n"
"v_cvt_pk_bf16_f32 v187, v142, v143\n"
"s_waitcnt lgkmcnt(0)\n"
"s_nop 0\n"
"v_mfma_f32_16x16x32_bf16 v[172:175], v[148:151], v[184:187], v[20:23]\n"
"s_barrier\n"
"ds_read2st64_b32 v[144:145], v12 offset0:0 offset1:1\n"
"ds_read2st64_b32 v[146:147], v12 offset0:2 offset1:3\n"
"ds_read2st64_b32 v[160:161], v11 offset0:4 offset1:5\n"
"ds_read2st64_b32 v[164:165], v11 offset0:6 offset1:7\n"
"s_add_u32 s94, s50, 0x0\n"
"s_and_b32 s95, s94, 0xfbffffff\n"
"s_waitcnt lgkmcnt(2)\n"
"v_mfma_f32_16x16x32_bf16 v[148:151], v[144:147], v[184:187], v[16:19]\n"
"s_cmp_eq_u32 s94, s62\n"
"s_cbranch_scc1 .LQ_ft3k0s\n"
".LQ_ft3k0r:\n"
"v_max_f32 v253, v172, v174\n"
"v_max_f32 v252, v173, v175\n"
"s_nop 3\n"
"v_max3_f32 v253, v148, v150, v253\n"
"v_max3_f32 v252, v149, v151, v252\n"
"v_max_f32 v251, v253, v252\n"
"v_mov_b32 v250, v251\n"
"s_nop 1\n"
"v_permlane32_swap_b32 v250, v251\n"
"v_max_f32 v251, v250, v251\n"
"v_mov_b32 v250, v251\n"
"s_nop 1\n"
"v_permlane16_swap_b32 v250, v251\n"
"v_max_f32 v251, v250, v251\n"
"v_mul_f32 v251, v251, v249\n"
"v_fma_f32 v253, v253, v249, -v251\n"
"v_fma_f32 v252, v252, v249, -v251\n"
"v_exp_f32 v253, v253\n"
"v_exp_f32 v252, v252\n"
"s_nop 0\n"
"v_add_f32 v251, v253, v252\n"
"v_mov_b32 v250, v251\n"
"s_nop 1\n"
"v_permlane32_swap_b32 v250, v251\n"
"v_add_f32 v251, v250, v251\n"
"v_mov_b32 v250, v251\n"
"s_nop 1\n"
"v_permlane16_swap_b32 v250, v251\n"
"v_add_f32 v251, v250, v251\n"
"v_rcp_f32 v251, v251\n"
"s_nop 0\n"
"v_mul_f32 v253, v253, v251\n"
"v_mul_f32 v252, v252, v251\n"
"v_cvt_pk_bf16_f32 v250, v253, v252\n"
"s_waitcnt lgkmcnt(0)\n"
"v_and_b32 v68, v250, v24\n"
"v_and_b32 v69, v250, v25\n"
"v_and_b32 v70, v250, v26\n"
"v_and_b32 v71, v250, v27\n"
"s_nop 1\n"
"v_mfma_f32_16x16x32_bf16 v[72:75], v[160:163], v[68:71], 0\n"
"v_mfma_f32_16x16x32_bf16 v[76:79], v[164:167], v[68:71], 0\n"
"s_nop 6\n"
"v_cvt_pk_bf16_f32 v68, v72, v73\n"
"v_cvt_pk_bf16_f32 v69, v74, v75\n"
"v_cvt_pk_bf16_f32 v70, v76, v77\n"
"v_cvt_pk_bf16_f32 v71, v78, v79\n"
"buffer_store_dwordx4 v[68:71], v13, s[40:43], s95 offen sc1\n"
"s_waitcnt vmcnt(0)\n"
"buffer_store_dwordx2 v[0:1], v14, s[40:43], s94 offen offset:0 sc1\n"
"s_branch .LQ_end\n"
".LQ_end:\n"
"s_waitcnt vmcnt(0) lgkmcnt(0)\n"
"s_endpgm\n"
      :: "{v2}"((unsigned)(lds0 + ((wid + 3) & 3) * 20640 + trp)), "{v3}"((unsigned)(lds0 + ((wid + 2) & 3) * 20640 + trp)), "{v4}"((unsigned)(lds0 + ((wid + 1) & 3) * 20640 + trp)), "{v5}"((unsigned)(lane * 16 + wid * 8192 + ((0 + rot) % 4) * 2048 + 134217728)), "{v6}"((unsigned)(lane * 16 + wid * 8192 + ((1 + rot) % 4) * 2048 + 134217728)), "{v7}"((unsigned)(lane * 16 + wid * 8192 + ((2 + rot) % 4) * 2048 + 134217728)), "{v8}"((unsigned)(lane * 16 + wid * 8192 + ((3 + rot) % 4) * 2048 + 134217728)), "{v9}"((unsigned)(li * 16 + 352256 + Q_GL * 1048576)), "{v10}"((unsigned)(((li + 4 * lg) & 15) * 16 + 352256 + Q_GL * 1048576)), "{v11}"((unsigned)(lds0 + ((wid + 1) & 3) * 20640 + 18592 + lane * 4)), "{v12}"((unsigned)(lds0 + ((wid + 2) & 3) * 20640 + 18592 + lane * 4)), "{v13}"((unsigned)(lane * 16 + idx * 1024 + 32768)), "{v14}"((unsigned)(lane ? DROP : idx * 8 + 352512)), "{v16}"((unsigned)(qmask(0, 0))), "{v17}"((unsigned)(qmask(1, 0))), "{v18}"((unsigned)(qmask(2, 0))), "{v19}"((unsigned)(qmask(3, 0))), "{v20}"((unsigned)(qmask(0, 1))), "{v21}"((unsigned)(qmask(1, 1))), "{v22}"((unsigned)(qmask(2, 1))), "{v23}"((unsigned)(qmask(3, 1))), "{v24}"((unsigned)(pmask(0))), "{v25}"((unsigned)(pmask(1))), "{v26}"((unsigned)(pmask(2))), "{v27}"((unsigned)(pmask(3))), "{v48}"((unsigned)(vT + tsw - wid * 6144)), "{v49}"((unsigned)(vT + (tsw ^ 8) - wid * 6144)), "{v50}"((unsigned)((sEnt == 1 ? (li + 4 * lg) & 15 : li) * 16 + 1400832)), "{v51}"((unsigned)((sEnt == 0 ? (li + 4 * lg) & 15 : li) * 16 + 2449408)), "{v52}"((unsigned)((sEnt == 3 ? (li + 4 * lg) & 15 : li) * 16 + 3497984)), "{v53}"((unsigned)((sEnt == 2 ? (li + 4 * lg) & 15 : li) * 16 + 4546560)), "{v54}"((unsigned)(((lane >> 5) * 8 + set) * 2048 + wid * 512 + ((lane ^ (lane >> 5)) & 31) * 16)), "{v55}"((unsigned)(sLd - wid * 6144 + li * 512 + (lg ^ li) * 16)), "{v56}"((unsigned)(lane * 128 + wid * 8192 + (2 + (idx & 7) % 6) * 1048576 + 134217728)), "{v57}"((unsigned)(vD + ((0 + rot) % 4) * 393216)), "{v58}"((unsigned)(lds0 + 139264 + ((wid + 0) & 3) * 6144 + lane * 16)), "{v59}"((unsigned)(lds0 + 139264 + ((wid + 1) & 3) * 6144 + lane * 16)), "{v60}"((unsigned)(lds0 + 139264 + ((wid + 2) & 3) * 6144 + lane * 16)), "{v61}"((unsigned)(lds0 + 139264 + ((wid + 3) & 3) * 6144 + lane * 16)), "{s[40:43]}"(rS), "{s[44:47]}"(rW), "{s[48:49]}"(wsp), "{s50}"((unsigned)(sA)), "{s51}"((unsigned)(sLo)), "{s52}"((unsigned)(sHi)), "{s53}"((unsigned)(sEx)), "{s54}"((unsigned)(sEnt)), "{s55}"((unsigned)(sLd - wid * 6144)), "{s[56:57]}"(wsb + (sLo & 0x3ffffff)), "{s59}"((unsigned)(lds0 + wid * 20640)), "{s62}"((unsigned)(sLo)), "{s[64:67]}"(desc(a.x, 1 << 22)), "{s68}"((unsigned)(sW0 + 0)), "{s69}"((unsigned)(sW0 + 2048)), "{s70}"((unsigned)(sW0 + 4096))
      : "v255", "a255", "s95", "vcc", "scc", "memory");

  }
  if (DT && xo < 0 && xj < 12 + (DU ? NT : NE)) {
    while (__builtin_amdgcn_s_memrealtime() - tz < DT + (xj - 16) * DU) __builtin_amdgcn_s_sleep(2);
    asm volatile("global_load_dword v252, %0, %1 sc1" ::"v"(WS0 + (PADF - LOWB + xj - 12) * RS + F_X + tid * 128), "s"(a.ws) : "v252");
  }
  while (__builtin_amdgcn_s_memrealtime() - tz < (role == 1 ? DLO : role == 2 ? (xo < 0 ? DLF : DLB) : DLG)) __builtin_amdgcn_s_sleep(4);
  if (role == 2 && xo < 0 && (SLP & 4)) {
#pragma unroll
    for (int i = 0; i < 4 * TW; ++i)
      asm volatile("global_load_dword v252, %0, %1" ::"v"((wid * 256 + i / TW * 64 + (xj - 16) * 4 + lg) * 7168 + (14 * (xo + 4) + min(li, 13)) * 128 + i % TW * 64), "s"(a.w1) : "v252");
  }
  if (role == 3 && (SLP & 8) && !((SLP & 32) && p2 < 128)) tw2();
  const unsigned sA = sLo - 3 * RS, sHi = sLo + (set ? 84 : 85) * 3 * RS;
  if (role == 1) {
    const unsigned sEx = sHi + O_EX * 3 * RS, sEnt = OR ? -wid & 3 : 0, tsw = 0, pair = idx * 4 + wid, rot = 0;
    const unsigned sW0 = wid * 256 * 2048 + idx * 256, vD = (lane >> 2) * 2048 + (lane & 3) * 16;
    const u32x4 rW = desc(a.wo, 1024 * 2048);
    asm volatile(
"s_nop 4\n"
"v_mov_b32 v186, 0\n"
"global_load_dwordx2 v[0:1], v186, s[48:49] sc1\n"
"v_mov_b32 v188, 0x3f803f80\n"
"v_mov_b32 v189, 0x3f803f80\n"
"v_mov_b32 v190, 0x3f803f80\n"
"v_mov_b32 v191, 0x3f803f80\n"
"s_add_u32 m0, s55, 0\n"
"s_add_u32 s60, s66, 0\n"
"buffer_load_dwordx4 v22, s[44:47], s60 offen lds\n"
"s_add_u32 m0, s55, 1024\n"
"s_add_u32 s60, s66, 32768\n"
"buffer_load_dwordx4 v22, s[44:47], s60 offen lds\n"
"s_add_u32 m0, s55, 2048\n"
"s_add_u32 s60, s67, 0\n"
"buffer_load_dwordx4 v22, s[44:47], s60 offen lds\n"
"s_add_u32 m0, s55, 3072\n"
"s_add_u32 s60, s67, 32768\n"
"buffer_load_dwordx4 v22, s[44:47], s60 offen lds\n"
"s_add_u32 m0, s55, 4096\n"
"s_add_u32 s60, s68, 0\n"
"buffer_load_dwordx4 v22, s[44:47], s60 offen lds\n"
"s_add_u32 m0, s55, 5120\n"
"s_add_u32 s60, s68, 32768\n"
"buffer_load_dwordx4 v22, s[44:47], s60 offen lds\n"
"s_add_u32 m0, s55, 6144\n"
"s_add_u32 s60, s69, 0\n"
"buffer_load_dwordx4 v22, s[44:47], s60 offen lds\n"
"s_add_u32 m0, s55, 7168\n"
"s_add_u32 s60, s69, 32768\n"
"buffer_load_dwordx4 v22, s[44:47], s60 offen lds\n"
"s_add_u32 m0, s55, 8192\n"
"s_add_u32 s60, s66, 65536\n"
"buffer_load_dwordx4 v22, s[44:47], s60 offen lds\n"
"s_add_u32 m0, s55, 9216\n"
"s_add_u32 s60, s66, 98304\n"
"buffer_load_dwordx4 v22, s[44:47], s60 offen lds\n"
"s_add_u32 m0, s55, 10240\n"
"s_add_u32 s60, s67, 65536\n"
"buffer_load_dwordx4 v22, s[44:47], s60 offen lds\n"
"s_add_u32 m0, s55, 11264\n"
"s_add_u32 s60, s67, 98304\n"
"buffer_load_dwordx4 v22, s[44:47], s60 offen lds\n"
"s_add_u32 m0, s55, 12288\n"
"s_add_u32 s60, s68, 65536\n"
"buffer_load_dwordx4 v22, s[44:47], s60 offen lds\n"
"s_add_u32 m0, s55, 13312\n"
"s_add_u32 s60, s68, 98304\n"
"buffer_load_dwordx4 v22, s[44:47], s60 offen lds\n"
"s_add_u32 m0, s55, 14336\n"
"s_add_u32 s60, s69, 65536\n"
"buffer_load_dwordx4 v22, s[44:47], s60 offen lds\n"
"s_add_u32 m0, s55, 15360\n"
"s_add_u32 s60, s69, 98304\n"
"buffer_load_dwordx4 v22, s[44:47], s60 offen lds\n"
"s_add_u32 m0, s55, 16384\n"
"s_add_u32 s60, s66, 0\n"
"buffer_load_dwordx4 v23, s[44:47], s60 offen lds\n"
"s_add_u32 m0, s55, 17408\n"
"s_add_u32 s60, s66, 32768\n"
"buffer_load_dwordx4 v23, s[44:47], s60 offen lds\n"
"s_add_u32 m0, s55, 18432\n"
"s_add_u32 s60, s67, 0\n"
"buffer_load_dwordx4 v23, s[44:47], s60 offen lds\n"
"s_add_u32 m0, s55, 19456\n"
"s_add_u32 s60, s67, 32768\n"
"buffer_load_dwordx4 v23, s[44:47], s60 offen lds\n"
"s_add_u32 m0, s55, 20480\n"
"s_add_u32 s60, s68, 0\n"
"buffer_load_dwordx4 v23, s[44:47], s60 offen lds\n"
"s_add_u32 m0, s55, 21504\n"
"s_add_u32 s60, s68, 32768\n"
"buffer_load_dwordx4 v23, s[44:47], s60 offen lds\n"
"s_add_u32 m0, s55, 22528\n"
"s_add_u32 s60, s69, 0\n"
"buffer_load_dwordx4 v23, s[44:47], s60 offen lds\n"
"s_add_u32 m0, s55, 23552\n"
"s_add_u32 s60, s69, 32768\n"
"buffer_load_dwordx4 v23, s[44:47], s60 offen lds\n"
"s_add_u32 m0, s55, 24576\n"
"s_add_u32 s60, s66, 65536\n"
"buffer_load_dwordx4 v23, s[44:47], s60 offen lds\n"
"s_add_u32 m0, s55, 25600\n"
"s_add_u32 s60, s66, 98304\n"
"buffer_load_dwordx4 v23, s[44:47], s60 offen lds\n"
"s_add_u32 m0, s55, 26624\n"
"s_add_u32 s60, s67, 65536\n"
"buffer_load_dwordx4 v23, s[44:47], s60 offen lds\n"
"s_add_u32 m0, s55, 27648\n"
"s_add_u32 s60, s67, 98304\n"
"buffer_load_dwordx4 v23, s[44:47], s60 offen lds\n"
"s_add_u32 m0, s55, 28672\n"
"s_add_u32 s60, s68, 65536\n"
"buffer_load_dwordx4 v23, s[44:47], s60 offen lds\n"
"s_add_u32 m0, s55, 29696\n"
"s_add_u32 s60, s68, 98304\n"
"buffer_load_dwordx4 v23, s[44:47], s60 offen lds\n"
"s_add_u32 m0, s55, 30720\n"
"s_add_u32 s60, s69, 65536\n"
"buffer_load_dwordx4 v23, s[44:47], s60 offen lds\n"
"s_add_u32 m0, s55, 31744\n"
"s_add_u32 s60, s69, 98304\n"
"buffer_load_dwordx4 v23, s[44:47], s60 offen lds\n"
"s_add_u32 m0, s55, 32768\n"
"s_add_u32 s60, s66, 0\n"
"buffer_load_dwordx4 v24, s[44:47], s60 offen lds\n"
"s_add_u32 m0, s55, 33792\n"
"s_add_u32 s60, s66, 32768\n"
"buffer_load_dwordx4 v24, s[44:47], s60 offen lds\n"
"s_add_u32 m0, s55, 34816\n"
"s_add_u32 s60, s67, 0\n"
"buffer_load_dwordx4 v24, s[44:47], s60 offen lds\n"
"s_add_u32 m0, s55, 35840\n"
"s_add_u32 s60, s67, 32768\n"
"buffer_load_dwordx4 v24, s[44:47], s60 offen lds\n"
"s_add_u32 m0, s55, 36864\n"
"s_add_u32 s60, s68, 0\n"
"buffer_load_dwordx4 v24, s[44:47], s60 offen lds\n"
"s_add_u32 m0, s55, 37888\n"
"s_add_u32 s60, s68, 32768\n"
"buffer_load_dwordx4 v24, s[44:47], s60 offen lds\n"
"s_add_u32 m0, s55, 38912\n"
"s_add_u32 s60, s69, 0\n"
"buffer_load_dwordx4 v24, s[44:47], s60 offen lds\n"
"s_add_u32 m0, s55, 39936\n"
"s_add_u32 s60, s69, 32768\n"
"buffer_load_dwordx4 v24, s[44:47], s60 offen lds\n"
"s_waitcnt vmcnt(38)\n"
"ds_read_b64_tr_b16 a[0:1], v20 offset:0\n"
"ds_read_b64_tr_b16 a[2:3], v20 offset:256\n"
"ds_read_b64_tr_b16 a[32:33], v21 offset:0\n"
"ds_read_b64_tr_b16 a[34:35], v21 offset:256\n"
"v_add_u32 v0, 2, v0\n"
"v_add_u32 v1, 0x9e3779b9, v1\n"
"v_or_b32 v0, 1, v0\n"
"v_readfirstlane_b32 s49, v1\n"
"v_readfirstlane_b32 s48, v0\n"
"s_waitcnt vmcnt(36)\n"
"ds_read_b64_tr_b16 a[64:65], v20 offset:2048\n"
"ds_read_b64_tr_b16 a[66:67], v20 offset:2304\n"
"ds_read_b64_tr_b16 a[96:97], v21 offset:2048\n"
"ds_read_b64_tr_b16 a[98:99], v21 offset:2304\n"
"s_waitcnt lgkmcnt(4)\n"
"s_add_u32 m0, s55, 0\n"
"s_add_u32 s60, s66, 65536\n"
"buffer_load_dwordx4 v24, s[44:47], s60 offen lds\n"
"s_add_u32 m0, s55, 1024\n"
"s_add_u32 s60, s66, 98304\n"
"buffer_load_dwordx4 v24, s[44:47], s60 offen lds\n"
"s_waitcnt vmcnt(36)\n"
"ds_read_b64_tr_b16 a[128:129], v20 offset:4096\n"
"ds_read_b64_tr_b16 a[130:131], v20 offset:4352\n"
"ds_read_b64_tr_b16 a[160:161], v21 offset:4096\n"
"ds_read_b64_tr_b16 a[162:163], v21 offset:4352\n"
"s_waitcnt lgkmcnt(4)\n"
"s_add_u32 m0, s55, 2048\n"
"s_add_u32 s60, s67, 65536\n"
"buffer_load_dwordx4 v24, s[44:47], s60 offen lds\n"
"s_add_u32 m0, s55, 3072\n"
"s_add_u32 s60, s67, 98304\n"
"buffer_load_dwordx4 v24, s[44:47], s60 offen lds\n"
"s_waitcnt vmcnt(36)\n"
"ds_read_b64_tr_b16 a[192:193], v20 offset:6144\n"
"ds_read_b64_tr_b16 a[194:195], v20 offset:6400\n"
"ds_read_b64_tr_b16 a[224:225], v21 offset:6144\n"
"ds_read_b64_tr_b16 a[226:227], v21 offset:6400\n"
"s_waitcnt lgkmcnt(4)\n"
"s_add_u32 m0, s55, 4096\n"
"s_add_u32 s60, s68, 65536\n"
"buffer_load_dwordx4 v24, s[44:47], s60 offen lds\n"
"s_add_u32 m0, s55, 5120\n"
"s_add_u32 s60, s68, 98304\n"
"buffer_load_dwordx4 v24, s[44:47], s60 offen lds\n"
"s_waitcnt vmcnt(36)\n"
"ds_read_b64_tr_b16 a[4:5], v20 offset:8192\n"
"ds_read_b64_tr_b16 a[6:7], v20 offset:8448\n"
"ds_read_b64_tr_b16 a[36:37], v21 offset:8192\n"
"ds_read_b64_tr_b16 a[38:39], v21 offset:8448\n"
"s_waitcnt lgkmcnt(4)\n"
"s_add_u32 m0, s55, 6144\n"
"s_add_u32 s60, s69, 65536\n"
"buffer_load_dwordx4 v24, s[44:47], s60 offen lds\n"
"s_add_u32 m0, s55, 7168\n"
"s_add_u32 s60, s69, 98304\n"
"buffer_load_dwordx4 v24, s[44:47], s60 offen lds\n"
"s_waitcnt vmcnt(36)\n"
"ds_read_b64_tr_b16 a[68:69], v20 offset:10240\n"
"ds_read_b64_tr_b16 a[70:71], v20 offset:10496\n"
"ds_read_b64_tr_b16 a[100:101], v21 offset:10240\n"
"ds_read_b64_tr_b16 a[102:103], v21 offset:10496\n"
"s_waitcnt lgkmcnt(4)\n"
"s_add_u32 m0, s55, 8192\n"
"s_add_u32 s60, s66, 0\n"
"buffer_load_dwordx4 v25, s[44:47], s60 offen lds\n"
"s_add_u32 m0, s55, 9216\n"
"s_add_u32 s60, s66, 32768\n"
"buffer_load_dwordx4 v25, s[44:47], s60 offen lds\n"
"s_waitcnt vmcnt(36)\n"
"ds_read_b64_tr_b16 a[132:133], v20 offset:12288\n"
"ds_read_b64_tr_b16 a[134:135], v20 offset:12544\n"
"ds_read_b64_tr_b16 a[164:165], v21 offset:12288\n"
"ds_read_b64_tr_b16 a[166:167], v21 offset:12544\n"
"s_waitcnt lgkmcnt(4)\n"
"s_add_u32 m0, s55, 10240\n"
"s_add_u32 s60, s67, 0\n"
"buffer_load_dwordx4 v25, s[44:47], s60 offen lds\n"
"s_add_u32 m0, s55, 11264\n"
"s_add_u32 s60, s67, 32768\n"
"buffer_load_dwordx4 v25, s[44:47], s60 offen lds\n"
"s_waitcnt vmcnt(36)\n"
"ds_read_b64_tr_b16 a[196:197], v20 offset:14336\n"
"ds_read_b64_tr_b16 a[198:199], v20 offset:14592\n"
"ds_read_b64_tr_b16 a[228:229], v21 offset:14336\n"
"ds_read_b64_tr_b16 a[230:231], v21 offset:14592\n"
"s_waitcnt lgkmcnt(4)\n"
"s_add_u32 m0, s55, 12288\n"
"s_add_u32 s60, s68, 0\n"
"buffer_load_dwordx4 v25, s[44:47], s60 offen lds\n"
"s_add_u32 m0, s55, 13312\n"
"s_add_u32 s60, s68, 32768\n"
"buffer_load_dwordx4 v25, s[44:47], s60 offen lds\n"
"s_waitcnt vmcnt(36)\n"
"ds_read_b64_tr_b16 a[8:9], v20 offset:16384\n"
"ds_read_b64_tr_b16 a[10:11], v20 offset:16640\n"
"ds_read_b64_tr_b16 a[40:41], v21 offset:16384\n"
"ds_read_b64_tr_b16 a[42:43], v21 offset:16640\n"
"s_waitcnt lgkmcnt(4)\n"
"s_add_u32 m0, s55, 14336\n"
"s_add_u32 s60, s69, 0\n"
"buffer_load_dwordx4 v25, s[44:47], s60 offen lds\n"
"s_add_u32 m0, s55, 15360\n"
"s_add_u32 s60, s69, 32768\n"
"buffer_load_dwordx4 v25, s[44:47], s60 offen lds\n"
"s_waitcnt vmcnt(36)\n"
"ds_read_b64_tr_b16 a[72:73], v20 offset:18432\n"
"ds_read_b64_tr_b16 a[74:75], v20 offset:18688\n"
"ds_read_b64_tr_b16 a[104:105], v21 offset:18432\n"
"ds_read_b64_tr_b16 a[106:107], v21 offset:18688\n"
"s_waitcnt lgkmcnt(4)\n"
"s_add_u32 m0, s55, 16384\n"
"s_add_u32 s60, s66, 65536\n"
"buffer_load_dwordx4 v25, s[44:47], s60 offen lds\n"
"s_add_u32 m0, s55, 17408\n"
"s_add_u32 s60, s66, 98304\n"
"buffer_load_dwordx4 v25, s[44:47], s60 offen lds\n"
"s_waitcnt vmcnt(36)\n"
"ds_read_b64_tr_b16 a[136:137], v20 offset:20480\n"
"ds_read_b64_tr_b16 a[138:139], v20 offset:20736\n"
"ds_read_b64_tr_b16 a[168:169], v21 offset:20480\n"
"ds_read_b64_tr_b16 a[170:171], v21 offset:20736\n"
"s_waitcnt lgkmcnt(4)\n"
"s_add_u32 m0, s55, 18432\n"
"s_add_u32 s60, s67, 65536\n"
"buffer_load_dwordx4 v25, s[44:47], s60 offen lds\n"
"s_add_u32 m0, s55, 19456\n"
"s_add_u32 s60, s67, 98304\n"
"buffer_load_dwordx4 v25, s[44:47], s60 offen lds\n"
"s_waitcnt vmcnt(36)\n"
"ds_read_b64_tr_b16 a[200:201], v20 offset:22528\n"
"ds_read_b64_tr_b16 a[202:203], v20 offset:22784\n"
"ds_read_b64_tr_b16 a[232:233], v21 offset:22528\n"
"ds_read_b64_tr_b16 a[234:235], v21 offset:22784\n"
"s_waitcnt lgkmcnt(4)\n"
"s_add_u32 m0, s55, 20480\n"
"s_add_u32 s60, s68, 65536\n"
"buffer_load_dwordx4 v25, s[44:47], s60 offen lds\n"
"s_add_u32 m0, s55, 21504\n"
"s_add_u32 s60, s68, 98304\n"
"buffer_load_dwordx4 v25, s[44:47], s60 offen lds\n"
"s_waitcnt vmcnt(36)\n"
"ds_read_b64_tr_b16 a[12:13], v20 offset:24576\n"
"ds_read_b64_tr_b16 a[14:15], v20 offset:24832\n"
"ds_read_b64_tr_b16 a[44:45], v21 offset:24576\n"
"ds_read_b64_tr_b16 a[46:47], v21 offset:24832\n"
"s_waitcnt lgkmcnt(4)\n"
"s_add_u32 m0, s55, 22528\n"
"s_add_u32 s60, s69, 65536\n"
"buffer_load_dwordx4 v25, s[44:47], s60 offen lds\n"
"s_add_u32 m0, s55, 23552\n"
"s_add_u32 s60, s69, 98304\n"
"buffer_load_dwordx4 v25, s[44:47], s60 offen lds\n"
"s_waitcnt vmcnt(36)\n"
"ds_read_b64_tr_b16 a[76:77], v20 offset:26624\n"
"ds_read_b64_tr_b16 a[78:79], v20 offset:26880\n"
"ds_read_b64_tr_b16 a[108:109], v21 offset:26624\n"
"ds_read_b64_tr_b16 a[110:111], v21 offset:26880\n"
"s_waitcnt lgkmcnt(4)\n"
"s_waitcnt vmcnt(34)\n"
"ds_read_b64_tr_b16 a[140:141], v20 offset:28672\n"
"ds_read_b64_tr_b16 a[142:143], v20 offset:28928\n"
"ds_read_b64_tr_b16 a[172:173], v21 offset:28672\n"
"ds_read_b64_tr_b16 a[174:175], v21 offset:28928\n"
"s_waitcnt lgkmcnt(4)\n"
"s_waitcnt vmcnt(32)\n"
"ds_read_b64_tr_b16 a[204:205], v20 offset:30720\n"
"ds_read_b64_tr_b16 a[206:207], v20 offset:30976\n"
"ds_read_b64_tr_b16 a[236:237], v21 offset:30720\n"
"ds_read_b64_tr_b16 a[238:239], v21 offset:30976\n"
"s_waitcnt lgkmcnt(4)\n"
"s_waitcnt vmcnt(30)\n"
"ds_read_b64_tr_b16 a[16:17], v20 offset:32768\n"
"ds_read_b64_tr_b16 a[18:19], v20 offset:33024\n"
"ds_read_b64_tr_b16 a[48:49], v21 offset:32768\n"
"ds_read_b64_tr_b16 a[50:51], v21 offset:33024\n"
"s_waitcnt lgkmcnt(4)\n"
"s_waitcnt vmcnt(28)\n"
"ds_read_b64_tr_b16 a[80:81], v20 offset:34816\n"
"ds_read_b64_tr_b16 a[82:83], v20 offset:35072\n"
"ds_read_b64_tr_b16 a[112:113], v21 offset:34816\n"
"ds_read_b64_tr_b16 a[114:115], v21 offset:35072\n"
"s_waitcnt lgkmcnt(4)\n"
"s_waitcnt vmcnt(26)\n"
"ds_read_b64_tr_b16 a[144:145], v20 offset:36864\n"
"ds_read_b64_tr_b16 a[146:147], v20 offset:37120\n"
"ds_read_b64_tr_b16 a[176:177], v21 offset:36864\n"
"ds_read_b64_tr_b16 a[178:179], v21 offset:37120\n"
"s_waitcnt lgkmcnt(4)\n"
"s_waitcnt vmcnt(24)\n"
"ds_read_b64_tr_b16 a[208:209], v20 offset:38912\n"
"ds_read_b64_tr_b16 a[210:211], v20 offset:39168\n"
"ds_read_b64_tr_b16 a[240:241], v21 offset:38912\n"
"ds_read_b64_tr_b16 a[242:243], v21 offset:39168\n"
"s_waitcnt lgkmcnt(4)\n"
"s_waitcnt vmcnt(22)\n"
"ds_read_b64_tr_b16 a[20:21], v20 offset:0\n"
"ds_read_b64_tr_b16 a[22:23], v20 offset:256\n"
"ds_read_b64_tr_b16 a[52:53], v21 offset:0\n"
"ds_read_b64_tr_b16 a[54:55], v21 offset:256\n"
"s_waitcnt lgkmcnt(4)\n"
"s_waitcnt vmcnt(20)\n"
"ds_read_b64_tr_b16 a[84:85], v20 offset:2048\n"
"ds_read_b64_tr_b16 a[86:87], v20 offset:2304\n"
"ds_read_b64_tr_b16 a[116:117], v21 offset:2048\n"
"ds_read_b64_tr_b16 a[118:119], v21 offset:2304\n"
"s_waitcnt lgkmcnt(4)\n"
"s_waitcnt vmcnt(18)\n"
"ds_read_b64_tr_b16 a[148:149], v20 offset:4096\n"
"ds_read_b64_tr_b16 a[150:151], v20 offset:4352\n"
"ds_read_b64_tr_b16 a[180:181], v21 offset:4096\n"
"ds_read_b64_tr_b16 a[182:183], v21 offset:4352\n"
"s_waitcnt lgkmcnt(4)\n"
"s_waitcnt vmcnt(16)\n"
"ds_read_b64_tr_b16 a[212:213], v20 offset:6144\n"
"ds_read_b64_tr_b16 a[214:215], v20 offset:6400\n"
"ds_read_b64_tr_b16 a[244:245], v21 offset:6144\n"
"ds_read_b64_tr_b16 a[246:247], v21 offset:6400\n"
"s_waitcnt lgkmcnt(4)\n"
"s_waitcnt vmcnt(14)\n"
"ds_read_b64_tr_b16 a[24:25], v20 offset:8192\n"
"ds_read_b64_tr_b16 a[26:27], v20 offset:8448\n"
"ds_read_b64_tr_b16 a[56:57], v21 offset:8192\n"
"ds_read_b64_tr_b16 a[58:59], v21 offset:8448\n"
"s_waitcnt lgkmcnt(4)\n"
"s_waitcnt vmcnt(12)\n"
"ds_read_b64_tr_b16 a[88:89], v20 offset:10240\n"
"ds_read_b64_tr_b16 a[90:91], v20 offset:10496\n"
"ds_read_b64_tr_b16 a[120:121], v21 offset:10240\n"
"ds_read_b64_tr_b16 a[122:123], v21 offset:10496\n"
"s_waitcnt lgkmcnt(4)\n"
"s_waitcnt vmcnt(10)\n"
"ds_read_b64_tr_b16 a[152:153], v20 offset:12288\n"
"ds_read_b64_tr_b16 a[154:155], v20 offset:12544\n"
"ds_read_b64_tr_b16 a[184:185], v21 offset:12288\n"
"ds_read_b64_tr_b16 a[186:187], v21 offset:12544\n"
"s_waitcnt lgkmcnt(4)\n"
"s_waitcnt vmcnt(8)\n"
"ds_read_b64_tr_b16 a[216:217], v20 offset:14336\n"
"ds_read_b64_tr_b16 a[218:219], v20 offset:14592\n"
"ds_read_b64_tr_b16 a[248:249], v21 offset:14336\n"
"ds_read_b64_tr_b16 a[250:251], v21 offset:14592\n"
"s_waitcnt lgkmcnt(4)\n"
"s_waitcnt vmcnt(6)\n"
"ds_read_b64_tr_b16 a[28:29], v20 offset:16384\n"
"ds_read_b64_tr_b16 a[30:31], v20 offset:16640\n"
"ds_read_b64_tr_b16 a[60:61], v21 offset:16384\n"
"ds_read_b64_tr_b16 a[62:63], v21 offset:16640\n"
"s_waitcnt lgkmcnt(4)\n"
"s_waitcnt vmcnt(4)\n"
"ds_read_b64_tr_b16 a[92:93], v20 offset:18432\n"
"ds_read_b64_tr_b16 a[94:95], v20 offset:18688\n"
"ds_read_b64_tr_b16 a[124:125], v21 offset:18432\n"
"ds_read_b64_tr_b16 a[126:127], v21 offset:18688\n"
"s_waitcnt lgkmcnt(4)\n"
"s_waitcnt vmcnt(2)\n"
"ds_read_b64_tr_b16 a[156:157], v20 offset:20480\n"
"ds_read_b64_tr_b16 a[158:159], v20 offset:20736\n"
"ds_read_b64_tr_b16 a[188:189], v21 offset:20480\n"
"ds_read_b64_tr_b16 a[190:191], v21 offset:20736\n"
"s_waitcnt lgkmcnt(4)\n"
"s_waitcnt vmcnt(0)\n"
"ds_read_b64_tr_b16 a[220:221], v20 offset:22528\n"
"ds_read_b64_tr_b16 a[222:223], v20 offset:22784\n"
"ds_read_b64_tr_b16 a[252:253], v21 offset:22528\n"
"ds_read_b64_tr_b16 a[254:255], v21 offset:22784\n"
"s_waitcnt lgkmcnt(4)\n"
"s_waitcnt lgkmcnt(0)\n"
"s_mov_b32 m0, s59\n"
"s_barrier\n"
"s_mov_b32 s93, 0x80000000\n"
"s_mov_b32 s95, 0x80000000\n"
"s_mov_b32 s95, 0x80000000\n"
"s_mov_b32 s94, 0x80000000\n"
"s_mov_b32 s93, 0x80000000\n"
"s_mov_b32 s94, 0x80000000\n"
"s_mov_b32 s94, 0x80000000\n"
"s_mov_b32 s95, 0x80000000\n"
"v_mov_b32 v124, 0\n"
"v_mov_b32 v125, 0\n"
"v_mov_b32 v120, 0\n"
"v_mov_b32 v121, 0\n"
"v_mov_b32 v124, 0\n"
"v_mov_b32 v125, 0\n"
"v_mov_b32 v120, 0\n"
"v_mov_b32 v121, 0\n"
"s_cmp_eq_u32 s54, 0\n"
"s_cbranch_scc1 .LO_1s\n"
"s_cmp_eq_u32 s54, 1\n"
"s_cbranch_scc1 .LO_2s\n"
"s_cmp_eq_u32 s54, 2\n"
"s_cbranch_scc1 .LO_3s\n"
"s_cmp_eq_u32 s54, 3\n"
"s_cbranch_scc1 .LO_4s\n"
".p2align 6\n"
".LO_top:\n"
"v_mfma_f32_16x16x32_bf16 v[40:43], v[84:87], a[0:3], 0\n"
"global_load_dwordx4 v[20:23], v6, s[56:57] offset:1024 sc1\n"
"v_mfma_f32_16x16x32_bf16 v[40:43], v[88:91], a[4:7], v[40:43]\n"
"s_waitcnt vmcnt(13)\n"
"v_mfma_f32_16x16x32_bf16 v[40:43], v[92:95], a[8:11], v[40:43]\n"
"s_waitcnt lgkmcnt(3)\n"
"v_add_f32 v72, v72, v44\n"
"v_add_f32 v73, v73, v45\n"
"v_mfma_f32_16x16x32_bf16 v[40:43], v[52:55], a[12:15], v[40:43]\n"
"s_waitcnt vmcnt(11)\n"
"v_add_f32 v74, v74, v46\n"
"v_add_f32 v75, v75, v47\n"
"v_mfma_f32_16x16x32_bf16 v[40:43], v[56:59], a[16:19], v[40:43]\n"
"v_add_f32 v72, v72, v48\n"
"v_add_f32 v73, v73, v49\n"
"v_mfma_f32_16x16x32_bf16 v[40:43], v[60:63], a[20:23], v[40:43]\n"
"s_waitcnt vmcnt(8)\n"
"global_load_dwordx4 v[24:27], v7, s[56:57] offset:0 sc1\n"
"v_add_f32 v74, v74, v50\n"
"v_mfma_f32_16x16x32_bf16 v[40:43], v[64:67], a[24:27], v[40:43]\n"
"v_add_f32 v75, v75, v51\n"
"v_add_f32 v72, v72, v112\n"
"v_mfma_f32_16x16x32_bf16 v[40:43], v[68:71], a[28:31], v[40:43]\n"
"v_add_f32 v73, v73, v113\n"
"v_add_f32 v74, v74, v114\n"
"v_mfma_f32_16x16x32_bf16 v[44:47], v[84:87], a[32:35], 0\n"
"global_load_dwordx4 v[28:31], v7, s[56:57] offset:1024 sc1\n"
"v_add_f32 v75, v75, v115\n"
"v_mfma_f32_16x16x32_bf16 v[44:47], v[88:91], a[36:39], v[44:47]\n"
"s_add_u32 s94, s50, 0xffd00000\n"
"s_and_b32 s95, s94, 0xfbffffff\n"
"ds_write_addtid_b32 v40 offset:0\n"
"v_mfma_f32_16x16x32_bf16 v[44:47], v[92:95], a[40:43], v[44:47]\n"
"ds_write_addtid_b32 v41 offset:4640\n"
"v_cvt_pk_bf16_f32 v48, v72, v73\n"
"v_mfma_f32_16x16x32_bf16 v[44:47], v[52:55], a[44:47], v[44:47]\n"
"ds_write_addtid_b32 v42 offset:9344\n"
"v_cvt_pk_bf16_f32 v49, v74, v75\n"
"v_mfma_f32_16x16x32_bf16 v[44:47], v[56:59], a[48:51], v[44:47]\n"
"s_waitcnt lgkmcnt(3)\n"
"ds_write_addtid_b32 v43 offset:13984\n"
"v_add_f32 v76, v76, v80\n"
"v_mfma_f32_16x16x32_bf16 v[44:47], v[60:63], a[52:55], v[44:47]\n"
"global_load_dwordx4 v[32:35], v8, s[56:57] offset:0 sc1\n"
"v_add_f32 v77, v77, v81\n"
"v_mfma_f32_16x16x32_bf16 v[44:47], v[64:67], a[56:59], v[44:47]\n"
"v_add_f32 v78, v78, v82\n"
"v_add_f32 v79, v79, v83\n"
"v_mfma_f32_16x16x32_bf16 v[44:47], v[68:71], a[60:63], v[44:47]\n"
"v_add_f32 v76, v76, v120\n"
"v_add_f32 v77, v77, v121\n"
"v_mfma_f32_16x16x32_bf16 v[40:43], v[84:87], a[64:67], 0\n"
"v_add_f32 v78, v78, v122\n"
"v_add_f32 v79, v79, v123\n"
"v_mfma_f32_16x16x32_bf16 v[40:43], v[88:91], a[68:71], v[40:43]\n"
"ds_write_addtid_b32 v44 offset:256\n"
"v_add_f32 v76, v76, v108\n"
"v_mfma_f32_16x16x32_bf16 v[40:43], v[92:95], a[72:75], v[40:43]\n"
"s_waitcnt vmcnt(9)\n"
"buffer_store_dwordx2 v[0:1], v12, s[40:43], s93 offen offset:0 sc1\n"
"ds_write_addtid_b32 v45 offset:4896\n"
"v_mfma_f32_16x16x32_bf16 v[40:43], v[52:55], a[76:79], v[40:43]\n"
"ds_write_addtid_b32 v46 offset:9600\n"
"v_add_f32 v77, v77, v109\n"
"v_mfma_f32_16x16x32_bf16 v[40:43], v[56:59], a[80:83], v[40:43]\n"
"s_waitcnt vmcnt(9)\n"
"ds_write_addtid_b32 v47 offset:14240\n"
"global_load_dwordx4 v[36:39], v8, s[56:57] offset:1024 sc1\n"
"v_mfma_f32_16x16x32_bf16 v[40:43], v[60:63], a[84:87], v[40:43]\n"
"v_add_f32 v78, v78, v110\n"
"v_add_f32 v79, v79, v111\n"
"v_mfma_f32_16x16x32_bf16 v[40:43], v[64:67], a[88:91], v[40:43]\n"
"v_cvt_pk_bf16_f32 v50, v76, v77\n"
"v_cvt_pk_bf16_f32 v51, v78, v79\n"
"v_mfma_f32_16x16x32_bf16 v[40:43], v[68:71], a[92:95], v[40:43]\n"
"v_lshlrev_b32 v72, 16, v116\n"
"v_and_b32 v73, 0xffff0000, v116\n"
"v_mfma_f32_16x16x32_bf16 v[44:47], v[84:87], a[96:99], 0\n"
"v_lshlrev_b32 v74, 16, v117\n"
"v_and_b32 v75, 0xffff0000, v117\n"
"v_mfma_f32_16x16x32_bf16 v[44:47], v[88:91], a[100:103], v[44:47]\n"
"ds_write_addtid_b32 v40 offset:512\n"
"v_lshlrev_b32 v76, 16, v118\n"
"v_mfma_f32_16x16x32_bf16 v[44:47], v[92:95], a[104:107], v[44:47]\n"
"ds_write_addtid_b32 v41 offset:5152\n"
"v_and_b32 v77, 0xffff0000, v118\n"
"v_mfma_f32_16x16x32_bf16 v[44:47], v[52:55], a[108:111], v[44:47]\n"
"ds_write_addtid_b32 v42 offset:9856\n"
"v_lshlrev_b32 v78, 16, v119\n"
"v_mfma_f32_16x16x32_bf16 v[44:47], v[56:59], a[112:115], v[44:47]\n"
"ds_write_addtid_b32 v43 offset:14496\n"
"v_and_b32 v79, 0xffff0000, v119\n"
"v_mfma_f32_16x16x32_bf16 v[44:47], v[60:63], a[116:119], v[44:47]\n"
"v_mfma_f32_16x16x32_bf16 v[44:47], v[64:67], a[120:123], v[44:47]\n"
"v_mfma_f32_16x16x32_bf16 v[44:47], v[68:71], a[124:127], v[44:47]\n"
"v_mfma_f32_16x16x32_bf16 v[40:43], v[84:87], a[128:131], 0\n"
"v_mfma_f32_16x16x32_bf16 v[40:43], v[88:91], a[132:135], v[40:43]\n"
"ds_write_addtid_b32 v44 offset:768\n"
"v_mfma_f32_16x16x32_bf16 v[40:43], v[92:95], a[136:139], v[40:43]\n"
"buffer_store_dwordx4 v[48:51], v11, s[40:43], s95 offen sc1\n"
"ds_write_addtid_b32 v45 offset:5408\n"
"v_mfma_f32_16x16x32_bf16 v[80:83], v[48:51], v[48:51], 0\n"
"v_mfma_f32_16x16x32_bf16 v[108:111], v[14:17], v[48:51], 0\n"
"v_mfma_f32_16x16x32_bf16 v[40:43], v[52:55], a[140:143], v[40:43]\n"
"ds_write_addtid_b32 v46 offset:10112\n"
"v_cndmask_b32_e64 v80, v80, v81, s[62:63]\n"
"v_mfma_f32_16x16x32_bf16 v[40:43], v[56:59], a[144:147], v[40:43]\n"
"ds_write_addtid_b32 v47 offset:14752\n"
"v_cndmask_b32_e64 v82, v82, v83, s[62:63]\n"
"v_mfma_f32_16x16x32_bf16 v[40:43], v[60:63], a[148:151], v[40:43]\n"
"global_load_dwordx4 v[112:115], v10, s[56:57] sc1\n"
"v_cndmask_b32_e64 v109, v80, v82, s[64:65]\n"
"v_mfma_f32_16x16x32_bf16 v[40:43], v[64:67], a[152:155], v[40:43]\n"
"buffer_store_dwordx2 v[108:109], v13, s[40:43], s95 offen offset:0 sc1\n"
"v_mfma_f32_16x16x32_bf16 v[40:43], v[68:71], a[156:159], v[40:43]\n"
"v_mfma_f32_16x16x32_bf16 v[48:51], v[84:87], a[160:163], 0\n"
"s_add_u32 s58, s50, 0x180000\n"
"v_mfma_f32_16x16x32_bf16 v[48:51], v[88:91], a[164:167], v[48:51]\n"
"s_and_b32 s58, s58, 0x3ffffff\n"
"ds_write_addtid_b32 v40 offset:1024\n"
"v_mfma_f32_16x16x32_bf16 v[48:51], v[92:95], a[168:171], v[48:51]\n"
"s_add_u32 s56, s40, s58\n"
"ds_write_addtid_b32 v41 offset:5664\n"
"v_mfma_f32_16x16x32_bf16 v[48:51], v[52:55], a[172:175], v[48:51]\n"
"s_addc_u32 s57, s41, 0\n"
"ds_write_addtid_b32 v42 offset:10368\n"
"v_mfma_f32_16x16x32_bf16 v[48:51], v[56:59], a[176:179], v[48:51]\n"
"s_waitcnt vmcnt(10)\n"
"ds_write_addtid_b32 v43 offset:15008\n"
"v_cmp_ne_u64 vcc, s[48:49], v[124:125]\n"
"v_mfma_f32_16x16x32_bf16 v[48:51], v[60:63], a[180:183], v[48:51]\n"
"v_mfma_f32_16x16x32_bf16 v[48:51], v[64:67], a[184:187], v[48:51]\n"
"s_cbranch_vccnz .LO_3s\n"
".LO_3r:\n"
"v_mfma_f32_16x16x32_bf16 v[48:51], v[68:71], a[188:191], v[48:51]\n"
"s_add_u32 s50, s50, 0x180000\n"
"global_load_dwordx4 v[40:43], v5, s[56:57] offset:0 sc1\n"
"v_mfma_f32_16x16x32_bf16 v[72:75], a[192:195], v[84:87], v[72:75]\n"
"v_mfma_f32_16x16x32_bf16 v[72:75], a[196:199], v[88:91], v[72:75]\n"
"ds_write_addtid_b32 v48 offset:1280\n"
"v_mfma_f32_16x16x32_bf16 v[72:75], a[200:203], v[92:95], v[72:75]\n"
"ds_write_addtid_b32 v49 offset:5920\n"
"v_mfma_f32_16x16x32_bf16 v[72:75], a[204:207], v[52:55], v[72:75]\n"
"ds_write_addtid_b32 v50 offset:10624\n"
"v_mfma_f32_16x16x32_bf16 v[72:75], a[208:211], v[56:59], v[72:75]\n"
"ds_write_addtid_b32 v51 offset:15264\n"
"global_load_dwordx4 v[44:47], v5, s[56:57] offset:1024 sc1\n"
"v_mfma_f32_16x16x32_bf16 v[72:75], a[212:215], v[60:63], v[72:75]\n"
"v_mfma_f32_16x16x32_bf16 v[72:75], a[216:219], v[64:67], v[72:75]\n"
"v_mfma_f32_16x16x32_bf16 v[72:75], a[220:223], v[68:71], v[72:75]\n"
"v_mfma_f32_16x16x32_bf16 v[76:79], a[224:227], v[84:87], v[76:79]\n"
"s_waitcnt lgkmcnt(0)\n"
"v_mfma_f32_16x16x32_bf16 v[76:79], a[228:231], v[88:91], v[76:79]\n"
"s_barrier\n"
"v_mfma_f32_16x16x32_bf16 v[76:79], a[232:235], v[92:95], v[76:79]\n"
"ds_read_b128 v[80:83], v2 offset:1536\n"
"ds_read_b128 v[84:87], v3 offset:512\n"
"v_mfma_f32_16x16x32_bf16 v[76:79], a[236:239], v[52:55], v[76:79]\n"
"buffer_load_dwordx2 v[120:121], v9, s[40:43], s50 offen sc1\n"
"v_mfma_f32_16x16x32_bf16 v[76:79], a[240:243], v[56:59], v[76:79]\n"
"global_load_dwordx4 v[48:51], v6, s[56:57] offset:0 sc1\n"
"ds_read_b128 v[88:91], v4 offset:2560\n"
"v_mfma_f32_16x16x32_bf16 v[76:79], a[244:247], v[60:63], v[76:79]\n"
"ds_read_b128 v[92:95], v2 offset:1792\n"
"ds_read_b128 v[108:111], v3 offset:768\n"
"v_mfma_f32_16x16x32_bf16 v[76:79], a[248:251], v[64:67], v[76:79]\n"
"global_load_dwordx4 v[52:55], v6, s[56:57] offset:1024 sc1\n"
"ds_read_b128 v[116:119], v4 offset:2816\n"
"v_mfma_f32_16x16x32_bf16 v[76:79], a[252:255], v[68:71], v[76:79]\n"
"v_mfma_f32_16x16x32_bf16 v[64:67], v[96:99], a[0:3], 0\n"
"v_mfma_f32_16x16x32_bf16 v[64:67], v[100:103], a[4:7], v[64:67]\n"
"s_waitcnt vmcnt(13)\n"
"v_mfma_f32_16x16x32_bf16 v[64:67], v[104:107], a[8:11], v[64:67]\n"
"s_waitcnt lgkmcnt(3)\n"
"v_add_f32 v80, v80, v84\n"
"v_add_f32 v81, v81, v85\n"
"v_mfma_f32_16x16x32_bf16 v[64:67], v[20:23], a[12:15], v[64:67]\n"
"s_waitcnt vmcnt(11)\n"
"global_load_dwordx4 v[56:59], v7, s[56:57] offset:0 sc1\n"
"v_add_f32 v82, v82, v86\n"
"v_mfma_f32_16x16x32_bf16 v[64:67], v[24:27], a[16:19], v[64:67]\n"
"v_add_f32 v83, v83, v87\n"
"v_add_f32 v80, v80, v88\n"
"v_mfma_f32_16x16x32_bf16 v[64:67], v[28:31], a[20:23], v[64:67]\n"
"s_waitcnt vmcnt(9)\n"
"v_add_f32 v81, v81, v89\n"
"v_add_f32 v82, v82, v90\n"
"v_mfma_f32_16x16x32_bf16 v[64:67], v[32:35], a[24:27], v[64:67]\n"
"v_add_f32 v83, v83, v91\n"
"v_add_f32 v80, v80, v72\n"
"v_mfma_f32_16x16x32_bf16 v[64:67], v[36:39], a[28:31], v[64:67]\n"
"v_add_f32 v81, v81, v73\n"
"v_add_f32 v82, v82, v74\n"
"v_mfma_f32_16x16x32_bf16 v[84:87], v[96:99], a[32:35], 0\n"
"v_add_f32 v83, v83, v75\n"
"v_mfma_f32_16x16x32_bf16 v[84:87], v[100:103], a[36:39], v[84:87]\n"
"s_add_u32 s93, s50, 0xffd00000\n"
"s_and_b32 s95, s93, 0xfbffffff\n"
"ds_write_addtid_b32 v64 offset:1536\n"
"v_mfma_f32_16x16x32_bf16 v[84:87], v[104:107], a[40:43], v[84:87]\n"
"ds_write_addtid_b32 v65 offset:6176\n"
"v_cvt_pk_bf16_f32 v72, v80, v81\n"
"v_mfma_f32_16x16x32_bf16 v[84:87], v[20:23], a[44:47], v[84:87]\n"
"ds_write_addtid_b32 v66 offset:10880\n"
"global_load_dwordx4 v[60:63], v7, s[56:57] offset:1024 sc1\n"
"v_mfma_f32_16x16x32_bf16 v[84:87], v[24:27], a[48:51], v[84:87]\n"
"ds_write_addtid_b32 v67 offset:15520\n"
"v_cvt_pk_bf16_f32 v73, v82, v83\n"
"v_mfma_f32_16x16x32_bf16 v[84:87], v[28:31], a[52:55], v[84:87]\n"
"s_waitcnt lgkmcnt(4)\n"
"v_add_f32 v92, v92, v108\n"
"v_add_f32 v93, v93, v109\n"
"v_mfma_f32_16x16x32_bf16 v[84:87], v[32:35], a[56:59], v[84:87]\n"
"v_add_f32 v94, v94, v110\n"
"v_add_f32 v95, v95, v111\n"
"v_mfma_f32_16x16x32_bf16 v[84:87], v[36:39], a[60:63], v[84:87]\n"
"v_add_f32 v92, v92, v116\n"
"v_add_f32 v93, v93, v117\n"
"v_mfma_f32_16x16x32_bf16 v[80:83], v[96:99], a[64:67], 0\n"
"global_load_dwordx4 v[64:67], v8, s[56:57] offset:0 sc1\n"
"v_add_f32 v94, v94, v118\n"
"v_mfma_f32_16x16x32_bf16 v[80:83], v[100:103], a[68:71], v[80:83]\n"
"ds_write_addtid_b32 v84 offset:1792\n"
"global_load_dwordx4 v[68:71], v8, s[56:57] offset:1024 sc1\n"
"v_mfma_f32_16x16x32_bf16 v[80:83], v[104:107], a[72:75], v[80:83]\n"
"ds_write_addtid_b32 v85 offset:6432\n"
"v_add_f32 v95, v95, v119\n"
"v_mfma_f32_16x16x32_bf16 v[80:83], v[20:23], a[76:79], v[80:83]\n"
"ds_write_addtid_b32 v86 offset:11136\n"
"v_add_f32 v92, v92, v76\n"
"v_mfma_f32_16x16x32_bf16 v[80:83], v[24:27], a[80:83], v[80:83]\n"
"ds_write_addtid_b32 v87 offset:15776\n"
"v_add_f32 v93, v93, v77\n"
"v_mfma_f32_16x16x32_bf16 v[80:83], v[28:31], a[84:87], v[80:83]\n"
"s_waitcnt vmcnt(10)\n"
"v_add_f32 v94, v94, v78\n"
"v_add_f32 v95, v95, v79\n"
"v_mfma_f32_16x16x32_bf16 v[80:83], v[32:35], a[88:91], v[80:83]\n"
"s_waitcnt vmcnt(9)\n"
"v_cvt_pk_bf16_f32 v74, v92, v93\n"
"v_cvt_pk_bf16_f32 v75, v94, v95\n"
"v_mfma_f32_16x16x32_bf16 v[80:83], v[36:39], a[92:95], v[80:83]\n"
"buffer_store_dwordx4 v[72:75], v11, s[40:43], s95 offen sc1\n"
"v_lshlrev_b32 v84, 16, v112\n"
"v_and_b32 v85, 0xffff0000, v112\n"
"v_lshlrev_b32 v86, 16, v113\n"
"v_mfma_f32_16x16x32_bf16 v[76:79], v[72:75], v[72:75], 0\n"
"buffer_store_dwordx2 v[0:1], v12, s[40:43], s94 offen offset:0 sc1\n"
"v_and_b32 v87, 0xffff0000, v113\n"
"v_mfma_f32_16x16x32_bf16 v[88:91], v[14:17], v[72:75], 0\n"
"v_lshlrev_b32 v92, 16, v114\n"
"v_and_b32 v93, 0xffff0000, v114\n"
"v_mfma_f32_16x16x32_bf16 v[108:111], v[96:99], a[96:99], 0\n"
"v_cndmask_b32_e64 v76, v76, v77, s[62:63]\n"
"v_cndmask_b32_e64 v78, v78, v79, s[62:63]\n"
"v_mfma_f32_16x16x32_bf16 v[108:111], v[100:103], a[100:103], v[108:111]\n"
"ds_write_addtid_b32 v80 offset:2048\n"
"v_cndmask_b32_e64 v89, v76, v78, s[64:65]\n"
"v_mfma_f32_16x16x32_bf16 v[108:111], v[104:107], a[104:107], v[108:111]\n"
"buffer_store_dwordx2 v[88:89], v13, s[40:43], s95 offen offset:0 sc1\n"
"ds_write_addtid_b32 v81 offset:6688\n"
"v_mfma_f32_16x16x32_bf16 v[108:111], v[20:23], a[108:111], v[108:111]\n"
"ds_write_addtid_b32 v82 offset:11392\n"
"v_lshlrev_b32 v94, 16, v115\n"
"v_mfma_f32_16x16x32_bf16 v[108:111], v[24:27], a[112:115], v[108:111]\n"
"ds_write_addtid_b32 v83 offset:16032\n"
"v_and_b32 v95, 0xffff0000, v115\n"
"v_mfma_f32_16x16x32_bf16 v[108:111], v[28:31], a[116:119], v[108:111]\n"
"v_mfma_f32_16x16x32_bf16 v[108:111], v[32:35], a[120:123], v[108:111]\n"
"v_mfma_f32_16x16x32_bf16 v[108:111], v[36:39], a[124:127], v[108:111]\n"
"v_mfma_f32_16x16x32_bf16 v[72:75], v[96:99], a[128:131], 0\n"
"v_mfma_f32_16x16x32_bf16 v[72:75], v[100:103], a[132:135], v[72:75]\n"
"ds_write_addtid_b32 v108 offset:2304\n"
"v_mfma_f32_16x16x32_bf16 v[72:75], v[104:107], a[136:139], v[72:75]\n"
"ds_write_addtid_b32 v109 offset:6944\n"
"v_mfma_f32_16x16x32_bf16 v[72:75], v[20:23], a[140:143], v[72:75]\n"
"ds_write_addtid_b32 v110 offset:11648\n"
"v_mfma_f32_16x16x32_bf16 v[72:75], v[24:27], a[144:147], v[72:75]\n"
"ds_write_addtid_b32 v111 offset:16288\n"
"v_mfma_f32_16x16x32_bf16 v[72:75], v[28:31], a[148:151], v[72:75]\n"
"v_mfma_f32_16x16x32_bf16 v[72:75], v[32:35], a[152:155], v[72:75]\n"
"global_load_dwordx4 v[88:91], v10, s[56:57] sc1\n"
"v_mfma_f32_16x16x32_bf16 v[72:75], v[36:39], a[156:159], v[72:75]\n"
"v_mfma_f32_16x16x32_bf16 v[76:79], v[96:99], a[160:163], 0\n"
"v_mfma_f32_16x16x32_bf16 v[76:79], v[100:103], a[164:167], v[76:79]\n"
"ds_write_addtid_b32 v72 offset:2560\n"
"v_mfma_f32_16x16x32_bf16 v[76:79], v[104:107], a[168:171], v[76:79]\n"
"s_add_u32 s58, s50, 0x180000\n"
"ds_write_addtid_b32 v73 offset:7200\n"
"v_mfma_f32_16x16x32_bf16 v[76:79], v[20:23], a[172:175], v[76:79]\n"
"s_and_b32 s58, s58, 0x3ffffff\n"
"ds_write_addtid_b32 v74 offset:11904\n"
"v_mfma_f32_16x16x32_bf16 v[76:79], v[24:27], a[176:179], v[76:79]\n"
"s_add_u32 s56, s40, s58\n"
"ds_write_addtid_b32 v75 offset:16544\n"
"v_mfma_f32_16x16x32_bf16 v[76:79], v[28:31], a[180:183], v[76:79]\n"
"s_addc_u32 s57, s41, 0\n"
"v_mfma_f32_16x16x32_bf16 v[76:79], v[32:35], a[184:187], v[76:79]\n"
"s_waitcnt vmcnt(10)\n"
"v_cmp_ne_u64 vcc, s[48:49], v[120:121]\n"
"v_mfma_f32_16x16x32_bf16 v[76:79], v[36:39], a[188:191], v[76:79]\n"
"v_mfma_f32_16x16x32_bf16 v[84:87], a[192:195], v[96:99], v[84:87]\n"
"s_cbranch_vccnz .LO_4s\n"
".LO_4r:\n"
"v_mfma_f32_16x16x32_bf16 v[84:87], a[196:199], v[100:103], v[84:87]\n"
"s_add_u32 s50, s50, 0x180000\n"
"ds_write_addtid_b32 v76 offset:2816\n"
"v_mfma_f32_16x16x32_bf16 v[84:87], a[200:203], v[104:107], v[84:87]\n"
"ds_write_addtid_b32 v77 offset:7456\n"
"global_load_dwordx4 v[72:75], v5, s[56:57] offset:0 sc1\n"
"v_mfma_f32_16x16x32_bf16 v[84:87], a[204:207], v[20:23], v[84:87]\n"
"ds_write_addtid_b32 v78 offset:12160\n"
"v_mfma_f32_16x16x32_bf16 v[84:87], a[208:211], v[24:27], v[84:87]\n"
"ds_write_addtid_b32 v79 offset:16800\n"
"v_mfma_f32_16x16x32_bf16 v[84:87], a[212:215], v[28:31], v[84:87]\n"
"v_mfma_f32_16x16x32_bf16 v[84:87], a[216:219], v[32:35], v[84:87]\n"
"v_mfma_f32_16x16x32_bf16 v[84:87], a[220:223], v[36:39], v[84:87]\n"
"global_load_dwordx4 v[76:79], v5, s[56:57] offset:1024 sc1\n"
"v_mfma_f32_16x16x32_bf16 v[92:95], a[224:227], v[96:99], v[92:95]\n"
"s_waitcnt lgkmcnt(0)\n"
"global_load_dwordx4 v[80:83], v6, s[56:57] offset:0 sc1\n"
"v_mfma_f32_16x16x32_bf16 v[92:95], a[228:231], v[100:103], v[92:95]\n"
"s_barrier\n"
"v_mfma_f32_16x16x32_bf16 v[92:95], a[232:235], v[104:107], v[92:95]\n"
"ds_read_b128 v[96:99], v2 offset:0\n"
"ds_read_b128 v[100:103], v3 offset:2048\n"
"v_mfma_f32_16x16x32_bf16 v[92:95], a[236:239], v[20:23], v[92:95]\n"
"ds_read_b128 v[104:107], v4 offset:1024\n"
"ds_read_b128 v[108:111], v2 offset:256\n"
"v_mfma_f32_16x16x32_bf16 v[92:95], a[240:243], v[24:27], v[92:95]\n"
"ds_read_b128 v[112:115], v3 offset:2304\n"
"ds_read_b128 v[116:119], v4 offset:1280\n"
"v_mfma_f32_16x16x32_bf16 v[92:95], a[244:247], v[28:31], v[92:95]\n"
"buffer_load_dwordx2 v[124:125], v9, s[40:43], s50 offen sc1\n"
"v_mfma_f32_16x16x32_bf16 v[92:95], a[248:251], v[32:35], v[92:95]\n"
"v_mfma_f32_16x16x32_bf16 v[92:95], a[252:255], v[36:39], v[92:95]\n"
"v_mfma_f32_16x16x32_bf16 v[120:123], v[40:43], a[0:3], 0\n"
"v_mfma_f32_16x16x32_bf16 v[120:123], v[44:47], a[4:7], v[120:123]\n"
"s_waitcnt vmcnt(12)\n"
"global_load_dwordx4 v[20:23], v6, s[56:57] offset:1024 sc1\n"
"v_mfma_f32_16x16x32_bf16 v[120:123], v[48:51], a[8:11], v[120:123]\n"
"s_waitcnt lgkmcnt(3)\n"
"v_add_f32 v96, v96, v100\n"
"v_add_f32 v97, v97, v101\n"
"v_mfma_f32_16x16x32_bf16 v[120:123], v[52:55], a[12:15], v[120:123]\n"
"s_waitcnt vmcnt(11)\n"
"v_add_f32 v98, v98, v102\n"
"v_add_f32 v99, v99, v103\n"
"v_mfma_f32_16x16x32_bf16 v[120:123], v[56:59], a[16:19], v[120:123]\n"
"v_add_f32 v96, v96, v104\n"
"v_add_f32 v97, v97, v105\n"
"v_mfma_f32_16x16x32_bf16 v[120:123], v[60:63], a[20:23], v[120:123]\n"
"s_waitcnt vmcnt(9)\n"
"v_add_f32 v98, v98, v106\n"
"v_add_f32 v99, v99, v107\n"
"v_mfma_f32_16x16x32_bf16 v[120:123], v[64:67], a[24:27], v[120:123]\n"
"global_load_dwordx4 v[24:27], v7, s[56:57] offset:0 sc1\n"
"v_add_f32 v96, v96, v84\n"
"v_mfma_f32_16x16x32_bf16 v[120:123], v[68:71], a[28:31], v[120:123]\n"
"global_load_dwordx4 v[28:31], v7, s[56:57] offset:1024 sc1\n"
"v_add_f32 v97, v97, v85\n"
"v_mfma_f32_16x16x32_bf16 v[100:103], v[40:43], a[32:35], 0\n"
"v_add_f32 v98, v98, v86\n"
"v_add_f32 v99, v99, v87\n"
"v_mfma_f32_16x16x32_bf16 v[100:103], v[44:47], a[36:39], v[100:103]\n"
"s_add_u32 s95, s50, 0xffd00000\n"
"s_and_b32 s94, s95, 0xfbffffff\n"
"ds_write_addtid_b32 v120 offset:0\n"
"v_mfma_f32_16x16x32_bf16 v[100:103], v[48:51], a[40:43], v[100:103]\n"
"s_waitcnt vmcnt(8)\n"
"buffer_store_dwordx2 v[0:1], v12, s[40:43], s93 offen offset:0 sc1\n"
"ds_write_addtid_b32 v121 offset:4640\n"
"v_mfma_f32_16x16x32_bf16 v[100:103], v[52:55], a[44:47], v[100:103]\n"
"ds_write_addtid_b32 v122 offset:9344\n"
"v_cvt_pk_bf16_f32 v84, v96, v97\n"
"v_mfma_f32_16x16x32_bf16 v[100:103], v[56:59], a[48:51], v[100:103]\n"
"ds_write_addtid_b32 v123 offset:13984\n"
"global_load_dwordx4 v[32:35], v8, s[56:57] offset:0 sc1\n"
"v_mfma_f32_16x16x32_bf16 v[100:103], v[60:63], a[52:55], v[100:103]\n"
"v_cvt_pk_bf16_f32 v85, v98, v99\n"
"v_mfma_f32_16x16x32_bf16 v[100:103], v[64:67], a[56:59], v[100:103]\n"
"s_waitcnt lgkmcnt(4)\n"
"v_add_f32 v108, v108, v112\n"
"v_add_f32 v109, v109, v113\n"
"v_mfma_f32_16x16x32_bf16 v[100:103], v[68:71], a[60:63], v[100:103]\n"
"v_add_f32 v110, v110, v114\n"
"v_add_f32 v111, v111, v115\n"
"v_mfma_f32_16x16x32_bf16 v[96:99], v[40:43], a[64:67], 0\n"
"v_add_f32 v108, v108, v116\n"
"v_add_f32 v109, v109, v117\n"
"v_mfma_f32_16x16x32_bf16 v[96:99], v[44:47], a[68:71], v[96:99]\n"
"ds_write_addtid_b32 v100 offset:256\n"
"v_add_f32 v110, v110, v118\n"
"v_mfma_f32_16x16x32_bf16 v[96:99], v[48:51], a[72:75], v[96:99]\n"
"ds_write_addtid_b32 v101 offset:4896\n"
"v_add_f32 v111, v111, v119\n"
"v_mfma_f32_16x16x32_bf16 v[96:99], v[52:55], a[76:79], v[96:99]\n"
"ds_write_addtid_b32 v102 offset:9600\n"
"v_add_f32 v108, v108, v92\n"
"v_mfma_f32_16x16x32_bf16 v[96:99], v[56:59], a[80:83], v[96:99]\n"
"ds_write_addtid_b32 v103 offset:14240\n"
"v_add_f32 v109, v109, v93\n"
"v_mfma_f32_16x16x32_bf16 v[96:99], v[60:63], a[84:87], v[96:99]\n"
"global_load_dwordx4 v[36:39], v8, s[56:57] offset:1024 sc1\n"
"v_add_f32 v110, v110, v94\n"
"v_mfma_f32_16x16x32_bf16 v[96:99], v[64:67], a[88:91], v[96:99]\n"
"s_waitcnt vmcnt(10)\n"
"v_add_f32 v111, v111, v95\n"
"v_cvt_pk_bf16_f32 v86, v108, v109\n"
"v_mfma_f32_16x16x32_bf16 v[96:99], v[68:71], a[92:95], v[96:99]\n"
"v_cvt_pk_bf16_f32 v87, v110, v111\n"
"v_lshlrev_b32 v100, 16, v88\n"
"v_mfma_f32_16x16x32_bf16 v[92:95], v[40:43], a[96:99], 0\n"
"v_and_b32 v101, 0xffff0000, v88\n"
"v_lshlrev_b32 v102, 16, v89\n"
"v_mfma_f32_16x16x32_bf16 v[92:95], v[44:47], a[100:103], v[92:95]\n"
"ds_write_addtid_b32 v96 offset:512\n"
"v_and_b32 v103, 0xffff0000, v89\n"
"v_mfma_f32_16x16x32_bf16 v[92:95], v[48:51], a[104:107], v[92:95]\n"
"ds_write_addtid_b32 v97 offset:5152\n"
"v_lshlrev_b32 v104, 16, v90\n"
"v_mfma_f32_16x16x32_bf16 v[92:95], v[52:55], a[108:111], v[92:95]\n"
"buffer_store_dwordx4 v[84:87], v11, s[40:43], s94 offen sc1\n"
"ds_write_addtid_b32 v98 offset:9856\n"
"v_and_b32 v105, 0xffff0000, v90\n"
"v_lshlrev_b32 v106, 16, v91\n"
"v_mfma_f32_16x16x32_bf16 v[108:111], v[84:87], v[84:87], 0\n"
"v_and_b32 v107, 0xffff0000, v91\n"
"v_mfma_f32_16x16x32_bf16 v[112:115], v[14:17], v[84:87], 0\n"
"v_mfma_f32_16x16x32_bf16 v[92:95], v[56:59], a[112:115], v[92:95]\n"
"ds_write_addtid_b32 v99 offset:14496\n"
"v_cndmask_b32_e64 v108, v108, v109, s[62:63]\n"
"v_mfma_f32_16x16x32_bf16 v[92:95], v[60:63], a[116:119], v[92:95]\n"
"v_cndmask_b32_e64 v110, v110, v111, s[62:63]\n"
"v_cndmask_b32_e64 v113, v108, v110, s[64:65]\n"
"v_mfma_f32_16x16x32_bf16 v[92:95], v[64:67], a[120:123], v[92:95]\n"
"buffer_store_dwordx2 v[112:113], v13, s[40:43], s94 offen offset:0 sc1\n"
"v_mfma_f32_16x16x32_bf16 v[92:95], v[68:71], a[124:127], v[92:95]\n"
"v_mfma_f32_16x16x32_bf16 v[84:87], v[40:43], a[128:131], 0\n"
"v_mfma_f32_16x16x32_bf16 v[84:87], v[44:47], a[132:135], v[84:87]\n"
"ds_write_addtid_b32 v92 offset:768\n"
"v_mfma_f32_16x16x32_bf16 v[84:87], v[48:51], a[136:139], v[84:87]\n"
"ds_write_addtid_b32 v93 offset:5408\n"
"v_mfma_f32_16x16x32_bf16 v[84:87], v[52:55], a[140:143], v[84:87]\n"
"ds_write_addtid_b32 v94 offset:10112\n"
"v_mfma_f32_16x16x32_bf16 v[84:87], v[56:59], a[144:147], v[84:87]\n"
"ds_write_addtid_b32 v95 offset:14752\n"
"v_mfma_f32_16x16x32_bf16 v[84:87], v[60:63], a[148:151], v[84:87]\n"
"v_mfma_f32_16x16x32_bf16 v[84:87], v[64:67], a[152:155], v[84:87]\n"
"v_mfma_f32_16x16x32_bf16 v[84:87], v[68:71], a[156:159], v[84:87]\n"
"global_load_dwordx4 v[96:99], v10, s[56:57] sc1\n"
"v_mfma_f32_16x16x32_bf16 v[92:95], v[40:43], a[160:163], 0\n"
"v_mfma_f32_16x16x32_bf16 v[92:95], v[44:47], a[164:167], v[92:95]\n"
"s_add_u32 s58, s50, 0x180000\n"
"ds_write_addtid_b32 v84 offset:1024\n"
"v_mfma_f32_16x16x32_bf16 v[92:95], v[48:51], a[168:171], v[92:95]\n"
"s_and_b32 s58, s58, 0x3ffffff\n"
"ds_write_addtid_b32 v85 offset:5664\n"
"v_mfma_f32_16x16x32_bf16 v[92:95], v[52:55], a[172:175], v[92:95]\n"
"s_add_u32 s56, s40, s58\n"
"ds_write_addtid_b32 v86 offset:10368\n"
"v_mfma_f32_16x16x32_bf16 v[92:95], v[56:59], a[176:179], v[92:95]\n"
"s_addc_u32 s57, s41, 0\n"
"ds_write_addtid_b32 v87 offset:15008\n"
"v_mfma_f32_16x16x32_bf16 v[92:95], v[60:63], a[180:183], v[92:95]\n"
"s_waitcnt vmcnt(9)\n"
"v_cmp_ne_u64 vcc, s[48:49], v[124:125]\n"
"v_mfma_f32_16x16x32_bf16 v[92:95], v[64:67], a[184:187], v[92:95]\n"
"v_mfma_f32_16x16x32_bf16 v[92:95], v[68:71], a[188:191], v[92:95]\n"
"s_cbranch_vccnz .LO_1s\n"
".LO_1r:\n"
"v_mfma_f32_16x16x32_bf16 v[100:103], a[192:195], v[40:43], v[100:103]\n"
"s_add_u32 s50, s50, 0x180000\n"
"global_load_dwordx4 v[84:87], v5, s[56:57] offset:0 sc1\n"
"v_mfma_f32_16x16x32_bf16 v[100:103], a[196:199], v[44:47], v[100:103]\n"
"ds_write_addtid_b32 v92 offset:1280\n"
"v_mfma_f32_16x16x32_bf16 v[100:103], a[200:203], v[48:51], v[100:103]\n"
"ds_write_addtid_b32 v93 offset:5920\n"
"v_mfma_f32_16x16x32_bf16 v[100:103], a[204:207], v[52:55], v[100:103]\n"
"ds_write_addtid_b32 v94 offset:10624\n"
"v_mfma_f32_16x16x32_bf16 v[100:103], a[208:211], v[56:59], v[100:103]\n"
"ds_write_addtid_b32 v95 offset:15264\n"
"v_mfma_f32_16x16x32_bf16 v[100:103], a[212:215], v[60:63], v[100:103]\n"
"global_load_dwordx4 v[88:91], v5, s[56:57] offset:1024 sc1\n"
"v_mfma_f32_16x16x32_bf16 v[100:103], a[216:219], v[64:67], v[100:103]\n"
"v_mfma_f32_16x16x32_bf16 v[100:103], a[220:223], v[68:71], v[100:103]\n"
"v_mfma_f32_16x16x32_bf16 v[104:107], a[224:227], v[40:43], v[104:107]\n"
"s_waitcnt lgkmcnt(0)\n"
"v_mfma_f32_16x16x32_bf16 v[104:107], a[228:231], v[44:47], v[104:107]\n"
"s_barrier\n"
"v_mfma_f32_16x16x32_bf16 v[104:107], a[232:235], v[48:51], v[104:107]\n"
"global_load_dwordx4 v[92:95], v6, s[56:57] offset:0 sc1\n"
"ds_read_b128 v[40:43], v2 offset:1536\n"
"v_mfma_f32_16x16x32_bf16 v[104:107], a[236:239], v[52:55], v[104:107]\n"
"ds_read_b128 v[44:47], v3 offset:512\n"
"ds_read_b128 v[48:51], v4 offset:2560\n"
"v_mfma_f32_16x16x32_bf16 v[104:107], a[240:243], v[56:59], v[104:107]\n"
"ds_read_b128 v[108:111], v2 offset:1792\n"
"ds_read_b128 v[112:115], v3 offset:768\n"
"v_mfma_f32_16x16x32_bf16 v[104:107], a[244:247], v[60:63], v[104:107]\n"
"ds_read_b128 v[116:119], v4 offset:2816\n"
"v_mfma_f32_16x16x32_bf16 v[104:107], a[248:251], v[64:67], v[104:107]\n"
"buffer_load_dwordx2 v[120:121], v9, s[40:43], s50 offen sc1\n"
"v_mfma_f32_16x16x32_bf16 v[104:107], a[252:255], v[68:71], v[104:107]\n"
"global_load_dwordx4 v[52:55], v6, s[56:57] offset:1024 sc1\n"
"v_mfma_f32_16x16x32_bf16 v[64:67], v[72:75], a[0:3], 0\n"
"v_mfma_f32_16x16x32_bf16 v[64:67], v[76:79], a[4:7], v[64:67]\n"
"s_waitcnt vmcnt(13)\n"
"v_mfma_f32_16x16x32_bf16 v[64:67], v[80:83], a[8:11], v[64:67]\n"
"s_waitcnt lgkmcnt(3)\n"
"v_add_f32 v40, v40, v44\n"
"v_add_f32 v41, v41, v45\n"
"v_mfma_f32_16x16x32_bf16 v[64:67], v[20:23], a[12:15], v[64:67]\n"
"s_waitcnt vmcnt(11)\n"
"v_add_f32 v42, v42, v46\n"
"v_add_f32 v43, v43, v47\n"
"v_mfma_f32_16x16x32_bf16 v[64:67], v[24:27], a[16:19], v[64:67]\n"
"global_load_dwordx4 v[56:59], v7, s[56:57] offset:0 sc1\n"
"v_add_f32 v40, v40, v48\n"
"v_mfma_f32_16x16x32_bf16 v[64:67], v[28:31], a[20:23], v[64:67]\n"
"s_waitcnt vmcnt(9)\n"
"v_add_f32 v41, v41, v49\n"
"v_add_f32 v42, v42, v50\n"
"v_mfma_f32_16x16x32_bf16 v[64:67], v[32:35], a[24:27], v[64:67]\n"
"v_add_f32 v43, v43, v51\n"
"v_add_f32 v40, v40, v100\n"
"v_mfma_f32_16x16x32_bf16 v[64:67], v[36:39], a[28:31], v[64:67]\n"
"v_add_f32 v41, v41, v101\n"
"v_add_f32 v42, v42, v102\n"
"v_mfma_f32_16x16x32_bf16 v[44:47], v[72:75], a[32:35], 0\n"
"v_add_f32 v43, v43, v103\n"
"v_mfma_f32_16x16x32_bf16 v[44:47], v[76:79], a[36:39], v[44:47]\n"
"ds_write_addtid_b32 v64 offset:1536\n"
"global_load_dwordx4 v[60:63], v7, s[56:57] offset:1024 sc1\n"
"v_mfma_f32_16x16x32_bf16 v[44:47], v[80:83], a[40:43], v[44:47]\n"
"s_add_u32 s93, s50, 0xffd00000\n"
"s_and_b32 s94, s93, 0xfbffffff\n"
"ds_write_addtid_b32 v65 offset:6176\n"
"v_mfma_f32_16x16x32_bf16 v[44:47], v[20:23], a[44:47], v[44:47]\n"
"ds_write_addtid_b32 v66 offset:10880\n"
"v_cvt_pk_bf16_f32 v48, v40, v41\n"
"v_mfma_f32_16x16x32_bf16 v[44:47], v[24:27], a[48:51], v[44:47]\n"
"ds_write_addtid_b32 v67 offset:15520\n"
"v_cvt_pk_bf16_f32 v49, v42, v43\n"
"v_mfma_f32_16x16x32_bf16 v[44:47], v[28:31], a[52:55], v[44:47]\n"
"s_waitcnt lgkmcnt(4)\n"
"v_add_f32 v108, v108, v112\n"
"v_add_f32 v109, v109, v113\n"
"v_mfma_f32_16x16x32_bf16 v[44:47], v[32:35], a[56:59], v[44:47]\n"
"s_waitcnt vmcnt(8)\n"
"global_load_dwordx4 v[64:67], v8, s[56:57] offset:0 sc1\n"
"v_add_f32 v110, v110, v114\n"
"v_mfma_f32_16x16x32_bf16 v[44:47], v[36:39], a[60:63], v[44:47]\n"
"buffer_store_dwordx2 v[0:1], v12, s[40:43], s95 offen offset:0 sc1\n"
"v_add_f32 v111, v111, v115\n"
"v_mfma_f32_16x16x32_bf16 v[40:43], v[72:75], a[64:67], 0\n"
"v_add_f32 v108, v108, v116\n"
"v_add_f32 v109, v109, v117\n"
"v_mfma_f32_16x16x32_bf16 v[40:43], v[76:79], a[68:71], v[40:43]\n"
"ds_write_addtid_b32 v44 offset:1792\n"
"v_add_f32 v110, v110, v118\n"
"v_mfma_f32_16x16x32_bf16 v[40:43], v[80:83], a[72:75], v[40:43]\n"
"ds_write_addtid_b32 v45 offset:6432\n"
"v_add_f32 v111, v111, v119\n"
"v_mfma_f32_16x16x32_bf16 v[40:43], v[20:23], a[76:79], v[40:43]\n"
"ds_write_addtid_b32 v46 offset:11136\n"
"global_load_dwordx4 v[68:71], v8, s[56:57] offset:1024 sc1\n"
"v_mfma_f32_16x16x32_bf16 v[40:43], v[24:27], a[80:83], v[40:43]\n"
"ds_write_addtid_b32 v47 offset:15776\n"
"v_add_f32 v108, v108, v104\n"
"v_mfma_f32_16x16x32_bf16 v[40:43], v[28:31], a[84:87], v[40:43]\n"
"v_add_f32 v109, v109, v105\n"
"v_add_f32 v110, v110, v106\n"
"v_mfma_f32_16x16x32_bf16 v[40:43], v[32:35], a[88:91], v[40:43]\n"
"v_add_f32 v111, v111, v107\n"
"v_cvt_pk_bf16_f32 v50, v108, v109\n"
"v_mfma_f32_16x16x32_bf16 v[40:43], v[36:39], a[92:95], v[40:43]\n"
"s_waitcnt vmcnt(10)\n"
"v_cvt_pk_bf16_f32 v51, v110, v111\n"
"v_lshlrev_b32 v112, 16, v96\n"
"v_mfma_f32_16x16x32_bf16 v[44:47], v[72:75], a[96:99], 0\n"
"v_and_b32 v113, 0xffff0000, v96\n"
"v_lshlrev_b32 v114, 16, v97\n"
"v_mfma_f32_16x16x32_bf16 v[44:47], v[76:79], a[100:103], v[44:47]\n"
"ds_write_addtid_b32 v40 offset:2048\n"
"v_and_b32 v115, 0xffff0000, v97\n"
"v_mfma_f32_16x16x32_bf16 v[44:47], v[80:83], a[104:107], v[44:47]\n"
"ds_write_addtid_b32 v41 offset:6688\n"
"v_lshlrev_b32 v108, 16, v98\n"
"v_mfma_f32_16x16x32_bf16 v[44:47], v[20:23], a[108:111], v[44:47]\n"
"ds_write_addtid_b32 v42 offset:11392\n"
"v_and_b32 v109, 0xffff0000, v98\n"
"v_mfma_f32_16x16x32_bf16 v[44:47], v[24:27], a[112:115], v[44:47]\n"
"ds_write_addtid_b32 v43 offset:16032\n"
"v_lshlrev_b32 v110, 16, v99\n"
"v_mfma_f32_16x16x32_bf16 v[44:47], v[28:31], a[116:119], v[44:47]\n"
"v_and_b32 v111, 0xffff0000, v99\n"
"v_mfma_f32_16x16x32_bf16 v[44:47], v[32:35], a[120:123], v[44:47]\n"
"v_mfma_f32_16x16x32_bf16 v[44:47], v[36:39], a[124:127], v[44:47]\n"
"buffer_store_dwordx4 v[48:51], v11, s[40:43], s94 offen sc1\n"
"v_mfma_f32_16x16x32_bf16 v[40:43], v[48:51], v[48:51], 0\n"
"v_mfma_f32_16x16x32_bf16 v[96:99], v[14:17], v[48:51], 0\n"
"v_mfma_f32_16x16x32_bf16 v[100:103], v[72:75], a[128:131], 0\n"
"v_cndmask_b32_e64 v40, v40, v41, s[62:63]\n"
"v_cndmask_b32_e64 v42, v42, v43, s[62:63]\n"
"v_mfma_f32_16x16x32_bf16 v[100:103], v[76:79], a[132:135], v[100:103]\n"
"ds_write_addtid_b32 v44 offset:2304\n"
"v_cndmask_b32_e64 v97, v40, v42, s[64:65]\n"
"v_mfma_f32_16x16x32_bf16 v[100:103], v[80:83], a[136:139], v[100:103]\n"
"buffer_store_dwordx2 v[96:97], v13, s[40:43], s94 offen offset:0 sc1\n"
"ds_write_addtid_b32 v45 offset:6944\n"
"v_mfma_f32_16x16x32_bf16 v[100:103], v[20:23], a[140:143], v[100:103]\n"
"ds_write_addtid_b32 v46 offset:11648\n"
"v_mfma_f32_16x16x32_bf16 v[100:103], v[24:27], a[144:147], v[100:103]\n"
"ds_write_addtid_b32 v47 offset:16288\n"
"global_load_dwordx4 v[116:119], v10, s[56:57] sc1\n"
"v_mfma_f32_16x16x32_bf16 v[100:103], v[28:31], a[148:151], v[100:103]\n"
"v_mfma_f32_16x16x32_bf16 v[100:103], v[32:35], a[152:155], v[100:103]\n"
"v_mfma_f32_16x16x32_bf16 v[100:103], v[36:39], a[156:159], v[100:103]\n"
"v_mfma_f32_16x16x32_bf16 v[40:43], v[72:75], a[160:163], 0\n"
"v_mfma_f32_16x16x32_bf16 v[40:43], v[76:79], a[164:167], v[40:43]\n"
"ds_write_addtid_b32 v100 offset:2560\n"
"v_mfma_f32_16x16x32_bf16 v[40:43], v[80:83], a[168:171], v[40:43]\n"
"s_add_u32 s58, s50, 0x180000\n"
"ds_write_addtid_b32 v101 offset:7200\n"
"v_mfma_f32_16x16x32_bf16 v[40:43], v[20:23], a[172:175], v[40:43]\n"
"s_and_b32 s58, s58, 0x3ffffff\n"
"ds_write_addtid_b32 v102 offset:11904\n"
"v_mfma_f32_16x16x32_bf16 v[40:43], v[24:27], a[176:179], v[40:43]\n"
"s_add_u32 s56, s40, s58\n"
"ds_write_addtid_b32 v103 offset:16544\n"
"v_mfma_f32_16x16x32_bf16 v[40:43], v[28:31], a[180:183], v[40:43]\n"
"s_addc_u32 s57, s41, 0\n"
"v_mfma_f32_16x16x32_bf16 v[40:43], v[32:35], a[184:187], v[40:43]\n"
"s_waitcnt vmcnt(9)\n"
"v_cmp_ne_u64 vcc, s[48:49], v[120:121]\n"
"v_mfma_f32_16x16x32_bf16 v[40:43], v[36:39], a[188:191], v[40:43]\n"
"v_mfma_f32_16x16x32_bf16 v[112:115], a[192:195], v[72:75], v[112:115]\n"
"s_cbranch_vccnz .LO_2s\n"
".LO_2r:\n"
"v_mfma_f32_16x16x32_bf16 v[112:115], a[196:199], v[76:79], v[112:115]\n"
"s_add_u32 s50, s50, 0x180000\n"
"ds_write_addtid_b32 v40 offset:2816\n"
"global_load_dwordx4 v[96:99], v5, s[56:57] offset:0 sc1\n"
"v_mfma_f32_16x16x32_bf16 v[112:115], a[200:203], v[80:83], v[112:115]\n"
"ds_write_addtid_b32 v41 offset:7456\n"
"v_mfma_f32_16x16x32_bf16 v[112:115], a[204:207], v[20:23], v[112:115]\n"
"ds_write_addtid_b32 v42 offset:12160\n"
"v_mfma_f32_16x16x32_bf16 v[112:115], a[208:211], v[24:27], v[112:115]\n"
"ds_write_addtid_b32 v43 offset:16800\n"
"v_mfma_f32_16x16x32_bf16 v[112:115], a[212:215], v[28:31], v[112:115]\n"
"v_mfma_f32_16x16x32_bf16 v[112:115], a[216:219], v[32:35], v[112:115]\n"
"global_load_dwordx4 v[100:103], v5, s[56:57] offset:1024 sc1\n"
"v_mfma_f32_16x16x32_bf16 v[112:115], a[220:223], v[36:39], v[112:115]\n"
"v_mfma_f32_16x16x32_bf16 v[108:111], a[224:227], v[72:75], v[108:111]\n"
"s_waitcnt lgkmcnt(0)\n"
"v_mfma_f32_16x16x32_bf16 v[108:111], a[228:231], v[76:79], v[108:111]\n"
"s_barrier\n"
"v_mfma_f32_16x16x32_bf16 v[108:111], a[232:235], v[80:83], v[108:111]\n"
"buffer_load_dwordx2 v[124:125], v9, s[40:43], s50 offen sc1\n"
"v_mfma_f32_16x16x32_bf16 v[108:111], a[236:239], v[20:23], v[108:111]\n"
"global_load_dwordx4 v[104:107], v6, s[56:57] offset:0 sc1\n"
"ds_read_b128 v[72:75], v2 offset:0\n"
"v_mfma_f32_16x16x32_bf16 v[108:111], a[240:243], v[24:27], v[108:111]\n"
"ds_read_b128 v[44:47], v3 offset:2048\n"
"ds_read_b128 v[48:51], v4 offset:1024\n"
"v_mfma_f32_16x16x32_bf16 v[108:111], a[244:247], v[28:31], v[108:111]\n"
"ds_read_b128 v[76:79], v2 offset:256\n"
"ds_read_b128 v[80:83], v3 offset:2304\n"
"v_mfma_f32_16x16x32_bf16 v[108:111], a[248:251], v[32:35], v[108:111]\n"
"ds_read_b128 v[120:123], v4 offset:1280\n"
"v_mfma_f32_16x16x32_bf16 v[108:111], a[252:255], v[36:39], v[108:111]\n"
"s_branch .LO_top\n"
".LO_1s:\n"
"s_cmp_ge_u32 s50, s53\n"
"s_cbranch_scc1 .LO_t0\n"
"s_cmp_ge_u32 s50, s52\n"
"s_cbranch_scc1 .LO_1r\n"
"s_sub_u32 s60, s50, 0x0\n"
"s_mov_b32 s61, 200000\n"
"s_nop 7\n"
".LO_1p:\n"
"buffer_load_dwordx2 v[124:125], v9, s[40:43], s60 offen sc1\n"
"s_waitcnt vmcnt(0)\n"
"v_cmp_ne_u64 vcc, s[48:49], v[124:125]\n"
"s_cbranch_vccz .LO_1px\n"
"s_sleep 1\n"
"s_sub_u32 s61, s61, 1\n"
"s_cbranch_scc0 .LO_1p\n"
"s_branch .LO_end\n"
".LO_1px:\n"
"s_branch .LO_1r\n"
".LO_2s:\n"
"s_cmp_ge_u32 s50, s53\n"
"s_cbranch_scc1 .LO_t1\n"
"s_cmp_ge_u32 s50, s52\n"
"s_cbranch_scc1 .LO_2r\n"
"s_sub_u32 s60, s50, 0x0\n"
"s_mov_b32 s61, 200000\n"
"s_nop 7\n"
".LO_2p:\n"
"buffer_load_dwordx2 v[120:121], v9, s[40:43], s60 offen sc1\n"
"s_waitcnt vmcnt(0)\n"
"v_cmp_ne_u64 vcc, s[48:49], v[120:121]\n"
"s_cbranch_vccz .LO_2px\n"
"s_sleep 1\n"
"s_sub_u32 s61, s61, 1\n"
"s_cbranch_scc0 .LO_2p\n"
"s_branch .LO_end\n"
".LO_2px:\n"
"s_branch .LO_2r\n"
".LO_3s:\n"
"s_cmp_ge_u32 s50, s53\n"
"s_cbranch_scc1 .LO_t2\n"
"s_cmp_ge_u32 s50, s52\n"
"s_cbranch_scc1 .LO_3r\n"
"s_sub_u32 s60, s50, 0x0\n"
"s_mov_b32 s61, 200000\n"
"s_nop 7\n"
".LO_3p:\n"
"buffer_load_dwordx2 v[124:125], v9, s[40:43], s60 offen sc1\n"
"s_waitcnt vmcnt(0)\n"
"v_cmp_ne_u64 vcc, s[48:49], v[124:125]\n"
"s_cbranch_vccz .LO_3px\n"
"s_sleep 1\n"
"s_sub_u32 s61, s61, 1\n"
"s_cbranch_scc0 .LO_3p\n"
"s_branch .LO_end\n"
".LO_3px:\n"
"s_branch .LO_3r\n"
".LO_4s:\n"
"s_cmp_ge_u32 s50, s53\n"
"s_cbranch_scc1 .LO_t3\n"
"s_cmp_ge_u32 s50, s52\n"
"s_cbranch_scc1 .LO_4r\n"
"s_sub_u32 s60, s50, 0x0\n"
"s_mov_b32 s61, 200000\n"
"s_nop 7\n"
".LO_4p:\n"
"buffer_load_dwordx2 v[120:121], v9, s[40:43], s60 offen sc1\n"
"s_waitcnt vmcnt(0)\n"
"v_cmp_ne_u64 vcc, s[48:49], v[120:121]\n"
"s_cbranch_vccz .LO_4px\n"
"s_sleep 1\n"
"s_sub_u32 s61, s61, 1\n"
"s_cbranch_scc0 .LO_4p\n"
"s_branch .LO_end\n"
".LO_4px:\n"
"s_branch .LO_4r\n"
".LO_t0:\n"
"v_mfma_f32_16x16x32_bf16 v[100:103], a[192:195], v[40:43], v[100:103]\n"
"v_mfma_f32_16x16x32_bf16 v[100:103], a[196:199], v[44:47], v[100:103]\n"
"ds_write_addtid_b32 v92 offset:1280\n"
"v_mfma_f32_16x16x32_bf16 v[100:103], a[200:203], v[48:51], v[100:103]\n"
"ds_write_addtid_b32 v93 offset:5920\n"
"v_mfma_f32_16x16x32_bf16 v[100:103], a[204:207], v[52:55], v[100:103]\n"
"ds_write_addtid_b32 v94 offset:10624\n"
"v_mfma_f32_16x16x32_bf16 v[100:103], a[208:211], v[56:59], v[100:103]\n"
"ds_write_addtid_b32 v95 offset:15264\n"
"v_mfma_f32_16x16x32_bf16 v[100:103], a[212:215], v[60:63], v[100:103]\n"
"v_mfma_f32_16x16x32_bf16 v[100:103], a[216:219], v[64:67], v[100:103]\n"
"v_mfma_f32_16x16x32_bf16 v[100:103], a[220:223], v[68:71], v[100:103]\n"
"v_mfma_f32_16x16x32_bf16 v[104:107], a[224:227], v[40:43], v[104:107]\n"
"s_waitcnt lgkmcnt(0)\n"
"v_mfma_f32_16x16x32_bf16 v[104:107], a[228:231], v[44:47], v[104:107]\n"
"s_barrier\n"
"v_mfma_f32_16x16x32_bf16 v[104:107], a[232:235], v[48:51], v[104:107]\n"
"ds_read_b128 v[40:43], v2 offset:1536\n"
"v_mfma_f32_16x16x32_bf16 v[104:107], a[236:239], v[52:55], v[104:107]\n"
"ds_read_b128 v[44:47], v3 offset:512\n"
"ds_read_b128 v[48:51], v4 offset:2560\n"
"v_mfma_f32_16x16x32_bf16 v[104:107], a[240:243], v[56:59], v[104:107]\n"
"ds_read_b128 v[108:111], v2 offset:1792\n"
"ds_read_b128 v[112:115], v3 offset:768\n"
"v_mfma_f32_16x16x32_bf16 v[104:107], a[244:247], v[60:63], v[104:107]\n"
"ds_read_b128 v[116:119], v4 offset:2816\n"
"v_mfma_f32_16x16x32_bf16 v[104:107], a[248:251], v[64:67], v[104:107]\n"
"v_mfma_f32_16x16x32_bf16 v[104:107], a[252:255], v[68:71], v[104:107]\n"
"s_waitcnt vmcnt(11)\n"
"v_mfma_f32_16x16x32_bf16 v[64:67], v[72:75], a[0:3], 0\n"
"v_mfma_f32_16x16x32_bf16 v[64:67], v[76:79], a[4:7], v[64:67]\n"
"s_waitcnt vmcnt(8)\n"
"v_mfma_f32_16x16x32_bf16 v[64:67], v[80:83], a[8:11], v[64:67]\n"
"s_waitcnt lgkmcnt(3)\n"
"v_add_f32 v40, v40, v44\n"
"v_add_f32 v41, v41, v45\n"
"v_mfma_f32_16x16x32_bf16 v[64:67], v[20:23], a[12:15], v[64:67]\n"
"s_waitcnt vmcnt(6)\n"
"v_add_f32 v42, v42, v46\n"
"v_add_f32 v43, v43, v47\n"
"v_mfma_f32_16x16x32_bf16 v[64:67], v[24:27], a[16:19], v[64:67]\n"
"v_add_f32 v40, v40, v48\n"
"v_mfma_f32_16x16x32_bf16 v[64:67], v[28:31], a[20:23], v[64:67]\n"
"s_waitcnt vmcnt(3)\n"
"v_add_f32 v41, v41, v49\n"
"v_add_f32 v42, v42, v50\n"
"v_mfma_f32_16x16x32_bf16 v[64:67], v[32:35], a[24:27], v[64:67]\n"
"v_add_f32 v43, v43, v51\n"
"v_add_f32 v40, v40, v100\n"
"v_mfma_f32_16x16x32_bf16 v[64:67], v[36:39], a[28:31], v[64:67]\n"
"v_add_f32 v41, v41, v101\n"
"v_add_f32 v42, v42, v102\n"
"v_mfma_f32_16x16x32_bf16 v[44:47], v[72:75], a[32:35], 0\n"
"v_add_f32 v43, v43, v103\n"
"v_mfma_f32_16x16x32_bf16 v[44:47], v[76:79], a[36:39], v[44:47]\n"
"ds_write_addtid_b32 v64 offset:1536\n"
"v_mfma_f32_16x16x32_bf16 v[44:47], v[80:83], a[40:43], v[44:47]\n"
"s_add_u32 s93, s50, 0xffe80000\n"
"s_and_b32 s94, s93, 0xfbffffff\n"
"ds_write_addtid_b32 v65 offset:6176\n"
"v_mfma_f32_16x16x32_bf16 v[44:47], v[20:23], a[44:47], v[44:47]\n"
"ds_write_addtid_b32 v66 offset:10880\n"
"v_cvt_pk_bf16_f32 v48, v40, v41\n"
"v_mfma_f32_16x16x32_bf16 v[44:47], v[24:27], a[48:51], v[44:47]\n"
"ds_write_addtid_b32 v67 offset:15520\n"
"v_cvt_pk_bf16_f32 v49, v42, v43\n"
"v_mfma_f32_16x16x32_bf16 v[44:47], v[28:31], a[52:55], v[44:47]\n"
"s_waitcnt lgkmcnt(4)\n"
"v_add_f32 v108, v108, v112\n"
"v_add_f32 v109, v109, v113\n"
"v_mfma_f32_16x16x32_bf16 v[44:47], v[32:35], a[56:59], v[44:47]\n"
"s_waitcnt vmcnt(1)\n"
"v_add_f32 v110, v110, v114\n"
"v_mfma_f32_16x16x32_bf16 v[44:47], v[36:39], a[60:63], v[44:47]\n"
"buffer_store_dwordx2 v[0:1], v12, s[40:43], s95 offen offset:0 sc1\n"
"v_add_f32 v111, v111, v115\n"
"v_mfma_f32_16x16x32_bf16 v[40:43], v[72:75], a[64:67], 0\n"
"v_add_f32 v108, v108, v116\n"
"v_add_f32 v109, v109, v117\n"
"v_mfma_f32_16x16x32_bf16 v[40:43], v[76:79], a[68:71], v[40:43]\n"
"ds_write_addtid_b32 v44 offset:1792\n"
"v_add_f32 v110, v110, v118\n"
"v_mfma_f32_16x16x32_bf16 v[40:43], v[80:83], a[72:75], v[40:43]\n"
"ds_write_addtid_b32 v45 offset:6432\n"
"v_add_f32 v111, v111, v119\n"
"v_mfma_f32_16x16x32_bf16 v[40:43], v[20:23], a[76:79], v[40:43]\n"
"ds_write_addtid_b32 v46 offset:11136\n"
"v_mfma_f32_16x16x32_bf16 v[40:43], v[24:27], a[80:83], v[40:43]\n"
"ds_write_addtid_b32 v47 offset:15776\n"
"v_add_f32 v108, v108, v104\n"
"v_mfma_f32_16x16x32_bf16 v[40:43], v[28:31], a[84:87], v[40:43]\n"
"v_add_f32 v109, v109, v105\n"
"v_add_f32 v110, v110, v106\n"
"v_mfma_f32_16x16x32_bf16 v[40:43], v[32:35], a[88:91], v[40:43]\n"
"v_add_f32 v111, v111, v107\n"
"v_cvt_pk_bf16_f32 v50, v108, v109\n"
"v_mfma_f32_16x16x32_bf16 v[40:43], v[36:39], a[92:95], v[40:43]\n"
"s_waitcnt vmcnt(1)\n"
"v_cvt_pk_bf16_f32 v51, v110, v111\n"
"v_lshlrev_b32 v112, 16, v96\n"
"v_mfma_f32_16x16x32_bf16 v[44:47], v[72:75], a[96:99], 0\n"
"v_and_b32 v113, 0xffff0000, v96\n"
"v_lshlrev_b32 v114, 16, v97\n"
"v_mfma_f32_16x16x32_bf16 v[44:47], v[76:79], a[100:103], v[44:47]\n"
"ds_write_addtid_b32 v40 offset:2048\n"
"v_and_b32 v115, 0xffff0000, v97\n"
"v_mfma_f32_16x16x32_bf16 v[44:47], v[80:83], a[104:107], v[44:47]\n"
"ds_write_addtid_b32 v41 offset:6688\n"
"v_lshlrev_b32 v108, 16, v98\n"
"v_mfma_f32_16x16x32_bf16 v[44:47], v[20:23], a[108:111], v[44:47]\n"
"ds_write_addtid_b32 v42 offset:11392\n"
"v_and_b32 v109, 0xffff0000, v98\n"
"v_mfma_f32_16x16x32_bf16 v[44:47], v[24:27], a[112:115], v[44:47]\n"
"ds_write_addtid_b32 v43 offset:16032\n"
"v_lshlrev_b32 v110, 16, v99\n"
"v_mfma_f32_16x16x32_bf16 v[44:47], v[28:31], a[116:119], v[44:47]\n"
"v_and_b32 v111, 0xffff0000, v99\n"
"v_mfma_f32_16x16x32_bf16 v[44:47], v[32:35], a[120:123], v[44:47]\n"
"v_mfma_f32_16x16x32_bf16 v[44:47], v[36:39], a[124:127], v[44:47]\n"
"buffer_store_dwordx4 v[48:51], v11, s[40:43], s94 offen sc1\n"
"v_mfma_f32_16x16x32_bf16 v[40:43], v[48:51], v[48:51], 0\n"
"v_mfma_f32_16x16x32_bf16 v[96:99], v[14:17], v[48:51], 0\n"
"v_mfma_f32_16x16x32_bf16 v[100:103], v[72:75], a[128:131], 0\n"
"v_cndmask_b32_e64 v40, v40, v41, s[62:63]\n"
"v_cndmask_b32_e64 v42, v42, v43, s[62:63]\n"
"v_mfma_f32_16x16x32_bf16 v[100:103], v[76:79], a[132:135], v[100:103]\n"
"ds_write_addtid_b32 v44 offset:2304\n"
"v_cndmask_b32_e64 v97, v40, v42, s[64:65]\n"
"v_mfma_f32_16x16x32_bf16 v[100:103], v[80:83], a[136:139], v[100:103]\n"
"buffer_store_dwordx2 v[96:97], v13, s[40:43], s94 offen offset:0 sc1\n"
"ds_write_addtid_b32 v45 offset:6944\n"
"v_mfma_f32_16x16x32_bf16 v[100:103], v[20:23], a[140:143], v[100:103]\n"
"ds_write_addtid_b32 v46 offset:11648\n"
"v_mfma_f32_16x16x32_bf16 v[100:103], v[24:27], a[144:147], v[100:103]\n"
"ds_write_addtid_b32 v47 offset:16288\n"
"v_mfma_f32_16x16x32_bf16 v[100:103], v[28:31], a[148:151], v[100:103]\n"
"v_mfma_f32_16x16x32_bf16 v[100:103], v[32:35], a[152:155], v[100:103]\n"
"v_mfma_f32_16x16x32_bf16 v[100:103], v[36:39], a[156:159], v[100:103]\n"
"v_mfma_f32_16x16x32_bf16 v[40:43], v[72:75], a[160:163], 0\n"
"v_mfma_f32_16x16x32_bf16 v[40:43], v[76:79], a[164:167], v[40:43]\n"
"ds_write_addtid_b32 v100 offset:2560\n"
"v_mfma_f32_16x16x32_bf16 v[40:43], v[80:83], a[168:171], v[40:43]\n"
"ds_write_addtid_b32 v101 offset:7200\n"
"v_mfma_f32_16x16x32_bf16 v[40:43], v[20:23], a[172:175], v[40:43]\n"
"ds_write_addtid_b32 v102 offset:11904\n"
"v_mfma_f32_16x16x32_bf16 v[40:43], v[24:27], a[176:179], v[40:43]\n"
"ds_write_addtid_b32 v103 offset:16544\n"
"v_mfma_f32_16x16x32_bf16 v[40:43], v[28:31], a[180:183], v[40:43]\n"
"v_mfma_f32_16x16x32_bf16 v[40:43], v[32:35], a[184:187], v[40:43]\n"
"v_mfma_f32_16x16x32_bf16 v[40:43], v[36:39], a[188:191], v[40:43]\n"
"v_mfma_f32_16x16x32_bf16 v[112:115], a[192:195], v[72:75], v[112:115]\n"
"v_mfma_f32_16x16x32_bf16 v[112:115], a[196:199], v[76:79], v[112:115]\n"
"ds_write_addtid_b32 v40 offset:2816\n"
"v_mfma_f32_16x16x32_bf16 v[112:115], a[200:203], v[80:83], v[112:115]\n"
"ds_write_addtid_b32 v41 offset:7456\n"
"v_mfma_f32_16x16x32_bf16 v[112:115], a[204:207], v[20:23], v[112:115]\n"
"ds_write_addtid_b32 v42 offset:12160\n"
"v_mfma_f32_16x16x32_bf16 v[112:115], a[208:211], v[24:27], v[112:115]\n"
"ds_write_addtid_b32 v43 offset:16800\n"
"v_mfma_f32_16x16x32_bf16 v[112:115], a[212:215], v[28:31], v[112:115]\n"
"v_mfma_f32_16x16x32_bf16 v[112:115], a[216:219], v[32:35], v[112:115]\n"
"v_mfma_f32_16x16x32_bf16 v[112:115], a[220:223], v[36:39], v[112:115]\n"
"v_mfma_f32_16x16x32_bf16 v[108:111], a[224:227], v[72:75], v[108:111]\n"
"s_waitcnt lgkmcnt(0)\n"
"v_mfma_f32_16x16x32_bf16 v[108:111], a[228:231], v[76:79], v[108:111]\n"
"s_barrier\n"
"v_mfma_f32_16x16x32_bf16 v[108:111], a[232:235], v[80:83], v[108:111]\n"
"v_mfma_f32_16x16x32_bf16 v[108:111], a[236:239], v[20:23], v[108:111]\n"
"ds_read_b128 v[72:75], v2 offset:0\n"
"v_mfma_f32_16x16x32_bf16 v[108:111], a[240:243], v[24:27], v[108:111]\n"
"ds_read_b128 v[44:47], v3 offset:2048\n"
"ds_read_b128 v[48:51], v4 offset:1024\n"
"v_mfma_f32_16x16x32_bf16 v[108:111], a[244:247], v[28:31], v[108:111]\n"
"ds_read_b128 v[76:79], v2 offset:256\n"
"ds_read_b128 v[80:83], v3 offset:2304\n"
"v_mfma_f32_16x16x32_bf16 v[108:111], a[248:251], v[32:35], v[108:111]\n"
"ds_read_b128 v[120:123], v4 offset:1280\n"
"v_mfma_f32_16x16x32_bf16 v[108:111], a[252:255], v[36:39], v[108:111]\n"
"s_waitcnt lgkmcnt(3)\n"
"v_add_f32 v72, v72, v44\n"
"v_add_f32 v73, v73, v45\n"
"v_add_f32 v74, v74, v46\n"
"v_add_f32 v75, v75, v47\n"
"v_add_f32 v72, v72, v48\n"
"v_add_f32 v73, v73, v49\n"
"v_add_f32 v74, v74, v50\n"
"v_add_f32 v75, v75, v51\n"
"v_add_f32 v72, v72, v112\n"
"v_add_f32 v73, v73, v113\n"
"v_add_f32 v74, v74, v114\n"
"v_add_f32 v75, v75, v115\n"
"s_add_u32 s94, s50, 0x0\n"
"s_and_b32 s95, s94, 0xfbffffff\n"
"v_cvt_pk_bf16_f32 v48, v72, v73\n"
"v_cvt_pk_bf16_f32 v49, v74, v75\n"
"s_waitcnt lgkmcnt(0)\n"
"v_add_f32 v76, v76, v80\n"
"v_add_f32 v77, v77, v81\n"
"v_add_f32 v78, v78, v82\n"
"v_add_f32 v79, v79, v83\n"
"v_add_f32 v76, v76, v120\n"
"v_add_f32 v77, v77, v121\n"
"v_add_f32 v78, v78, v122\n"
"v_add_f32 v79, v79, v123\n"
"v_add_f32 v76, v76, v108\n"
"s_waitcnt vmcnt(0)\n"
"buffer_store_dwordx2 v[0:1], v12, s[40:43], s93 offen offset:0 sc1\n"
"v_add_f32 v77, v77, v109\n"
"v_add_f32 v78, v78, v110\n"
"v_add_f32 v79, v79, v111\n"
"v_cvt_pk_bf16_f32 v50, v76, v77\n"
"v_cvt_pk_bf16_f32 v51, v78, v79\n"
"buffer_store_dwordx4 v[48:51], v11, s[40:43], s95 offen sc1\n"
"s_nop 0\n"
"v_mfma_f32_16x16x32_bf16 v[80:83], v[48:51], v[48:51], 0\n"
"v_mfma_f32_16x16x32_bf16 v[108:111], v[14:17], v[48:51], 0\n"
"s_nop 6\n"
"v_cndmask_b32_e64 v80, v80, v81, s[62:63]\n"
"v_cndmask_b32_e64 v82, v82, v83, s[62:63]\n"
"v_cndmask_b32_e64 v109, v80, v82, s[64:65]\n"
"buffer_store_dwordx2 v[108:109], v13, s[40:43], s95 offen offset:0 sc1\n"
"s_waitcnt vmcnt(0)\n"
"buffer_store_dwordx2 v[0:1], v12, s[40:43], s94 offen offset:0 sc1\n"
"s_branch .LO_end\n"
".LO_t1:\n"
"v_mfma_f32_16x16x32_bf16 v[112:115], a[196:199], v[76:79], v[112:115]\n"
"ds_write_addtid_b32 v40 offset:2816\n"
"v_mfma_f32_16x16x32_bf16 v[112:115], a[200:203], v[80:83], v[112:115]\n"
"ds_write_addtid_b32 v41 offset:7456\n"
"v_mfma_f32_16x16x32_bf16 v[112:115], a[204:207], v[20:23], v[112:115]\n"
"ds_write_addtid_b32 v42 offset:12160\n"
"v_mfma_f32_16x16x32_bf16 v[112:115], a[208:211], v[24:27], v[112:115]\n"
"ds_write_addtid_b32 v43 offset:16800\n"
"v_mfma_f32_16x16x32_bf16 v[112:115], a[212:215], v[28:31], v[112:115]\n"
"v_mfma_f32_16x16x32_bf16 v[112:115], a[216:219], v[32:35], v[112:115]\n"
"v_mfma_f32_16x16x32_bf16 v[112:115], a[220:223], v[36:39], v[112:115]\n"
"v_mfma_f32_16x16x32_bf16 v[108:111], a[224:227], v[72:75], v[108:111]\n"
"s_waitcnt lgkmcnt(0)\n"
"v_mfma_f32_16x16x32_bf16 v[108:111], a[228:231], v[76:79], v[108:111]\n"
"s_barrier\n"
"v_mfma_f32_16x16x32_bf16 v[108:111], a[232:235], v[80:83], v[108:111]\n"
"v_mfma_f32_16x16x32_bf16 v[108:111], a[236:239], v[20:23], v[108:111]\n"
"ds_read_b128 v[72:75], v2 offset:0\n"
"v_mfma_f32_16x16x32_bf16 v[108:111], a[240:243], v[24:27], v[108:111]\n"
"ds_read_b128 v[44:47], v3 offset:2048\n"
"ds_read_b128 v[48:51], v4 offset:1024\n"
"v_mfma_f32_16x16x32_bf16 v[108:111], a[244:247], v[28:31], v[108:111]\n"
"ds_read_b128 v[76:79], v2 offset:256\n"
"ds_read_b128 v[80:83], v3 offset:2304\n"
"v_mfma_f32_16x16x32_bf16 v[108:111], a[248:251], v[32:35], v[108:111]\n"
"ds_read_b128 v[120:123], v4 offset:1280\n"
"v_mfma_f32_16x16x32_bf16 v[108:111], a[252:255], v[36:39], v[108:111]\n"
"s_waitcnt vmcnt(11)\n"
"v_mfma_f32_16x16x32_bf16 v[40:43], v[84:87], a[0:3], 0\n"
"v_mfma_f32_16x16x32_bf16 v[40:43], v[88:91], a[4:7], v[40:43]\n"
"s_waitcnt vmcnt(8)\n"
"v_mfma_f32_16x16x32_bf16 v[40:43], v[92:95], a[8:11], v[40:43]\n"
"s_waitcnt lgkmcnt(3)\n"
"v_add_f32 v72, v72, v44\n"
"v_add_f32 v73, v73, v45\n"
"v_mfma_f32_16x16x32_bf16 v[40:43], v[52:55], a[12:15], v[40:43]\n"
"s_waitcnt vmcnt(6)\n"
"v_add_f32 v74, v74, v46\n"
"v_add_f32 v75, v75, v47\n"
"v_mfma_f32_16x16x32_bf16 v[40:43], v[56:59], a[16:19], v[40:43]\n"
"v_add_f32 v72, v72, v48\n"
"v_add_f32 v73, v73, v49\n"
"v_mfma_f32_16x16x32_bf16 v[40:43], v[60:63], a[20:23], v[40:43]\n"
"s_waitcnt vmcnt(3)\n"
"v_add_f32 v74, v74, v50\n"
"v_mfma_f32_16x16x32_bf16 v[40:43], v[64:67], a[24:27], v[40:43]\n"
"v_add_f32 v75, v75, v51\n"
"v_add_f32 v72, v72, v112\n"
"v_mfma_f32_16x16x32_bf16 v[40:43], v[68:71], a[28:31], v[40:43]\n"
"v_add_f32 v73, v73, v113\n"
"v_add_f32 v74, v74, v114\n"
"v_mfma_f32_16x16x32_bf16 v[44:47], v[84:87], a[32:35], 0\n"
"v_add_f32 v75, v75, v115\n"
"v_mfma_f32_16x16x32_bf16 v[44:47], v[88:91], a[36:39], v[44:47]\n"
"s_add_u32 s94, s50, 0xffe80000\n"
"s_and_b32 s95, s94, 0xfbffffff\n"
"ds_write_addtid_b32 v40 offset:0\n"
"v_mfma_f32_16x16x32_bf16 v[44:47], v[92:95], a[40:43], v[44:47]\n"
"ds_write_addtid_b32 v41 offset:4640\n"
"v_cvt_pk_bf16_f32 v48, v72, v73\n"
"v_mfma_f32_16x16x32_bf16 v[44:47], v[52:55], a[44:47], v[44:47]\n"
"ds_write_addtid_b32 v42 offset:9344\n"
"v_cvt_pk_bf16_f32 v49, v74, v75\n"
"v_mfma_f32_16x16x32_bf16 v[44:47], v[56:59], a[48:51], v[44:47]\n"
"s_waitcnt lgkmcnt(3)\n"
"ds_write_addtid_b32 v43 offset:13984\n"
"v_add_f32 v76, v76, v80\n"
"v_mfma_f32_16x16x32_bf16 v[44:47], v[60:63], a[52:55], v[44:47]\n"
"v_add_f32 v77, v77, v81\n"
"v_mfma_f32_16x16x32_bf16 v[44:47], v[64:67], a[56:59], v[44:47]\n"
"v_add_f32 v78, v78, v82\n"
"v_add_f32 v79, v79, v83\n"
"v_mfma_f32_16x16x32_bf16 v[44:47], v[68:71], a[60:63], v[44:47]\n"
"v_add_f32 v76, v76, v120\n"
"v_add_f32 v77, v77, v121\n"
"v_mfma_f32_16x16x32_bf16 v[40:43], v[84:87], a[64:67], 0\n"
"v_add_f32 v78, v78, v122\n"
"v_add_f32 v79, v79, v123\n"
"v_mfma_f32_16x16x32_bf16 v[40:43], v[88:91], a[68:71], v[40:43]\n"
"ds_write_addtid_b32 v44 offset:256\n"
"v_add_f32 v76, v76, v108\n"
"v_mfma_f32_16x16x32_bf16 v[40:43], v[92:95], a[72:75], v[40:43]\n"
"s_waitcnt vmcnt(1)\n"
"buffer_store_dwordx2 v[0:1], v12, s[40:43], s93 offen offset:0 sc1\n"
"ds_write_addtid_b32 v45 offset:4896\n"
"v_mfma_f32_16x16x32_bf16 v[40:43], v[52:55], a[76:79], v[40:43]\n"
"ds_write_addtid_b32 v46 offset:9600\n"
"v_add_f32 v77, v77, v109\n"
"v_mfma_f32_16x16x32_bf16 v[40:43], v[56:59], a[80:83], v[40:43]\n"
"s_waitcnt vmcnt(1)\n"
"ds_write_addtid_b32 v47 offset:14240\n"
"v_mfma_f32_16x16x32_bf16 v[40:43], v[60:63], a[84:87], v[40:43]\n"
"v_add_f32 v78, v78, v110\n"
"v_add_f32 v79, v79, v111\n"
"v_mfma_f32_16x16x32_bf16 v[40:43], v[64:67], a[88:91], v[40:43]\n"
"v_cvt_pk_bf16_f32 v50, v76, v77\n"
"v_cvt_pk_bf16_f32 v51, v78, v79\n"
"v_mfma_f32_16x16x32_bf16 v[40:43], v[68:71], a[92:95], v[40:43]\n"
"v_lshlrev_b32 v72, 16, v116\n"
"v_and_b32 v73, 0xffff0000, v116\n"
"v_mfma_f32_16x16x32_bf16 v[44:47], v[84:87], a[96:99], 0\n"
"v_lshlrev_b32 v74, 16, v117\n"
"v_and_b32 v75, 0xffff0000, v117\n"
"v_mfma_f32_16x16x32_bf16 v[44:47], v[88:91], a[100:103], v[44:47]\n"
"ds_write_addtid_b32 v40 offset:512\n"
"v_lshlrev_b32 v76, 16, v118\n"
"v_mfma_f32_16x16x32_bf16 v[44:47], v[92:95], a[104:107], v[44:47]\n"
"ds_write_addtid_b32 v41 offset:5152\n"
"v_and_b32 v77, 0xffff0000, v118\n"
"v_mfma_f32_16x16x32_bf16 v[44:47], v[52:55], a[108:111], v[44:47]\n"
"ds_write_addtid_b32 v42 offset:9856\n"
"v_lshlrev_b32 v78, 16, v119\n"
"v_mfma_f32_16x16x32_bf16 v[44:47], v[56:59], a[112:115], v[44:47]\n"
"ds_write_addtid_b32 v43 offset:14496\n"
"v_and_b32 v79, 0xffff0000, v119\n"
"v_mfma_f32_16x16x32_bf16 v[44:47], v[60:63], a[116:119], v[44:47]\n"
"v_mfma_f32_16x16x32_bf16 v[44:47], v[64:67], a[120:123], v[44:47]\n"
"v_mfma_f32_16x16x32_bf16 v[44:47], v[68:71], a[124:127], v[44:47]\n"
"v_mfma_f32_16x16x32_bf16 v[40:43], v[84:87], a[128:131], 0\n"
"v_mfma_f32_16x16x32_bf16 v[40:43], v[88:91], a[132:135], v[40:43]\n"
"ds_write_addtid_b32 v44 offset:768\n"
"v_mfma_f32_16x16x32_bf16 v[40:43], v[92:95], a[136:139], v[40:43]\n"
"buffer_store_dwordx4 v[48:51], v11, s[40:43], s95 offen sc1\n"
"ds_write_addtid_b32 v45 offset:5408\n"
"v_mfma_f32_16x16x32_bf16 v[80:83], v[48:51], v[48:51], 0\n"
"v_mfma_f32_16x16x32_bf16 v[108:111], v[14:17], v[48:51], 0\n"
"v_mfma_f32_16x16x32_bf16 v[40:43], v[52:55], a[140:143], v[40:43]\n"
"ds_write_addtid_b32 v46 offset:10112\n"
"v_cndmask_b32_e64 v80, v80, v81, s[62:63]\n"
"v_mfma_f32_16x16x32_bf16 v[40:43], v[56:59], a[144:147], v[40:43]\n"
"ds_write_addtid_b32 v47 offset:14752\n"
"v_cndmask_b32_e64 v82, v82, v83, s[62:63]\n"
"v_mfma_f32_16x16x32_bf16 v[40:43], v[60:63], a[148:151], v[40:43]\n"
"v_cndmask_b32_e64 v109, v80, v82, s[64:65]\n"
"v_mfma_f32_16x16x32_bf16 v[40:43], v[64:67], a[152:155], v[40:43]\n"
"buffer_store_dwordx2 v[108:109], v13, s[40:43], s95 offen offset:0 sc1\n"
"v_mfma_f32_16x16x32_bf16 v[40:43], v[68:71], a[156:159], v[40:43]\n"
"v_mfma_f32_16x16x32_bf16 v[48:51], v[84:87], a[160:163], 0\n"
"v_mfma_f32_16x16x32_bf16 v[48:51], v[88:91], a[164:167], v[48:51]\n"
"ds_write_addtid_b32 v40 offset:1024\n"
"v_mfma_f32_16x16x32_bf16 v[48:51], v[92:95], a[168:171], v[48:51]\n"
"ds_write_addtid_b32 v41 offset:5664\n"
"v_mfma_f32_16x16x32_bf16 v[48:51], v[52:55], a[172:175], v[48:51]\n"
"ds_write_addtid_b32 v42 offset:10368\n"
"v_mfma_f32_16x16x32_bf16 v[48:51], v[56:59], a[176:179], v[48:51]\n"
"ds_write_addtid_b32 v43 offset:15008\n"
"v_mfma_f32_16x16x32_bf16 v[48:51], v[60:63], a[180:183], v[48:51]\n"
"v_mfma_f32_16x16x32_bf16 v[48:51], v[64:67], a[184:187], v[48:51]\n"
"v_mfma_f32_16x16x32_bf16 v[48:51], v[68:71], a[188:191], v[48:51]\n"
"v_mfma_f32_16x16x32_bf16 v[72:75], a[192:195], v[84:87], v[72:75]\n"
"v_mfma_f32_16x16x32_bf16 v[72:75], a[196:199], v[88:91], v[72:75]\n"
"ds_write_addtid_b32 v48 offset:1280\n"
"v_mfma_f32_16x16x32_bf16 v[72:75], a[200:203], v[92:95], v[72:75]\n"
"ds_write_addtid_b32 v49 offset:5920\n"
"v_mfma_f32_16x16x32_bf16 v[72:75], a[204:207], v[52:55], v[72:75]\n"
"ds_write_addtid_b32 v50 offset:10624\n"
"v_mfma_f32_16x16x32_bf16 v[72:75], a[208:211], v[56:59], v[72:75]\n"
"ds_write_addtid_b32 v51 offset:15264\n"
"v_mfma_f32_16x16x32_bf16 v[72:75], a[212:215], v[60:63], v[72:75]\n"
"v_mfma_f32_16x16x32_bf16 v[72:75], a[216:219], v[64:67], v[72:75]\n"
"v_mfma_f32_16x16x32_bf16 v[72:75], a[220:223], v[68:71], v[72:75]\n"
"v_mfma_f32_16x16x32_bf16 v[76:79], a[224:227], v[84:87], v[76:79]\n"
"s_waitcnt lgkmcnt(0)\n"
"v_mfma_f32_16x16x32_bf16 v[76:79], a[228:231], v[88:91], v[76:79]\n"
"s_barrier\n"
"v_mfma_f32_16x16x32_bf16 v[76:79], a[232:235], v[92:95], v[76:79]\n"
"ds_read_b128 v[80:83], v2 offset:1536\n"
"ds_read_b128 v[84:87], v3 offset:512\n"
"v_mfma_f32_16x16x32_bf16 v[76:79], a[236:239], v[52:55], v[76:79]\n"
"v_mfma_f32_16x16x32_bf16 v[76:79], a[240:243], v[56:59], v[76:79]\n"
"ds_read_b128 v[88:91], v4 offset:2560\n"
"v_mfma_f32_16x16x32_bf16 v[76:79], a[244:247], v[60:63], v[76:79]\n"
"ds_read_b128 v[92:95], v2 offset:1792\n"
"ds_read_b128 v[108:111], v3 offset:768\n"
"v_mfma_f32_16x16x32_bf16 v[76:79], a[248:251], v[64:67], v[76:79]\n"
"ds_read_b128 v[116:119], v4 offset:2816\n"
"v_mfma_f32_16x16x32_bf16 v[76:79], a[252:255], v[68:71], v[76:79]\n"
"s_waitcnt lgkmcnt(3)\n"
"v_add_f32 v80, v80, v84\n"
"v_add_f32 v81, v81, v85\n"
"v_add_f32 v82, v82, v86\n"
"v_add_f32 v83, v83, v87\n"
"v_add_f32 v80, v80, v88\n"
"v_add_f32 v81, v81, v89\n"
"v_add_f32 v82, v82, v90\n"
"v_add_f32 v83, v83, v91\n"
"v_add_f32 v80, v80, v72\n"
"v_add_f32 v81, v81, v73\n"
"v_add_f32 v82, v82, v74\n"
"v_add_f32 v83, v83, v75\n"
"s_add_u32 s93, s50, 0x0\n"
"s_and_b32 s95, s93, 0xfbffffff\n"
"v_cvt_pk_bf16_f32 v72, v80, v81\n"
"v_cvt_pk_bf16_f32 v73, v82, v83\n"
"s_waitcnt lgkmcnt(0)\n"
"v_add_f32 v92, v92, v108\n"
"v_add_f32 v93, v93, v109\n"
"v_add_f32 v94, v94, v110\n"
"v_add_f32 v95, v95, v111\n"
"v_add_f32 v92, v92, v116\n"
"v_add_f32 v93, v93, v117\n"
"v_add_f32 v94, v94, v118\n"
"v_add_f32 v95, v95, v119\n"
"v_add_f32 v92, v92, v76\n"
"v_add_f32 v93, v93, v77\n"
"v_add_f32 v94, v94, v78\n"
"v_add_f32 v95, v95, v79\n"
"s_waitcnt vmcnt(0)\n"
"v_cvt_pk_bf16_f32 v74, v92, v93\n"
"v_cvt_pk_bf16_f32 v75, v94, v95\n"
"buffer_store_dwordx4 v[72:75], v11, s[40:43], s95 offen sc1\n"
"s_nop 0\n"
"v_mfma_f32_16x16x32_bf16 v[76:79], v[72:75], v[72:75], 0\n"
"buffer_store_dwordx2 v[0:1], v12, s[40:43], s94 offen offset:0 sc1\n"
"v_mfma_f32_16x16x32_bf16 v[88:91], v[14:17], v[72:75], 0\n"
"s_nop 5\n"
"v_cndmask_b32_e64 v76, v76, v77, s[62:63]\n"
"v_cndmask_b32_e64 v78, v78, v79, s[62:63]\n"
"v_cndmask_b32_e64 v89, v76, v78, s[64:65]\n"
"buffer_store_dwordx2 v[88:89], v13, s[40:43], s95 offen offset:0 sc1\n"
"s_waitcnt vmcnt(0)\n"
"buffer_store_dwordx2 v[0:1], v12, s[40:43], s93 offen offset:0 sc1\n"
"s_branch .LO_end\n"
".LO_t2:\n"
"v_mfma_f32_16x16x32_bf16 v[48:51], v[68:71], a[188:191], v[48:51]\n"
"v_mfma_f32_16x16x32_bf16 v[72:75], a[192:195], v[84:87], v[72:75]\n"
"v_mfma_f32_16x16x32_bf16 v[72:75], a[196:199], v[88:91], v[72:75]\n"
"ds_write_addtid_b32 v48 offset:1280\n"
"v_mfma_f32_16x16x32_bf16 v[72:75], a[200:203], v[92:95], v[72:75]\n"
"ds_write_addtid_b32 v49 offset:5920\n"
"v_mfma_f32_16x16x32_bf16 v[72:75], a[204:207], v[52:55], v[72:75]\n"
"ds_write_addtid_b32 v50 offset:10624\n"
"v_mfma_f32_16x16x32_bf16 v[72:75], a[208:211], v[56:59], v[72:75]\n"
"ds_write_addtid_b32 v51 offset:15264\n"
"v_mfma_f32_16x16x32_bf16 v[72:75], a[212:215], v[60:63], v[72:75]\n"
"v_mfma_f32_16x16x32_bf16 v[72:75], a[216:219], v[64:67], v[72:75]\n"
"v_mfma_f32_16x16x32_bf16 v[72:75], a[220:223], v[68:71], v[72:75]\n"
"v_mfma_f32_16x16x32_bf16 v[76:79], a[224:227], v[84:87], v[76:79]\n"
"s_waitcnt lgkmcnt(0)\n"
"v_mfma_f32_16x16x32_bf16 v[76:79], a[228:231], v[88:91], v[76:79]\n"
"s_barrier\n"
"v_mfma_f32_16x16x32_bf16 v[76:79], a[232:235], v[92:95], v[76:79]\n"
"ds_read_b128 v[80:83], v2 offset:1536\n"
"ds_read_b128 v[84:87], v3 offset:512\n"
"v_mfma_f32_16x16x32_bf16 v[76:79], a[236:239], v[52:55], v[76:79]\n"
"v_mfma_f32_16x16x32_bf16 v[76:79], a[240:243], v[56:59], v[76:79]\n"
"ds_read_b128 v[88:91], v4 offset:2560\n"
"v_mfma_f32_16x16x32_bf16 v[76:79], a[244:247], v[60:63], v[76:79]\n"
"ds_read_b128 v[92:95], v2 offset:1792\n"
"ds_read_b128 v[108:111], v3 offset:768\n"
"v_mfma_f32_16x16x32_bf16 v[76:79], a[248:251], v[64:67], v[76:79]\n"
"ds_read_b128 v[116:119], v4 offset:2816\n"
"v_mfma_f32_16x16x32_bf16 v[76:79], a[252:255], v[68:71], v[76:79]\n"
"s_waitcnt vmcnt(11)\n"
"v_mfma_f32_16x16x32_bf16 v[64:67], v[96:99], a[0:3], 0\n"
"v_mfma_f32_16x16x32_bf16 v[64:67], v[100:103], a[4:7], v[64:67]\n"
"s_waitcnt vmcnt(8)\n"
"v_mfma_f32_16x16x32_bf16 v[64:67], v[104:107], a[8:11], v[64:67]\n"
"s_waitcnt lgkmcnt(3)\n"
"v_add_f32 v80, v80, v84\n"
"v_add_f32 v81, v81, v85\n"
"v_mfma_f32_16x16x32_bf16 v[64:67], v[20:23], a[12:15], v[64:67]\n"
"s_waitcnt vmcnt(6)\n"
"v_add_f32 v82, v82, v86\n"
"v_mfma_f32_16x16x32_bf16 v[64:67], v[24:27], a[16:19], v[64:67]\n"
"v_add_f32 v83, v83, v87\n"
"v_add_f32 v80, v80, v88\n"
"v_mfma_f32_16x16x32_bf16 v[64:67], v[28:31], a[20:23], v[64:67]\n"
"s_waitcnt vmcnt(3)\n"
"v_add_f32 v81, v81, v89\n"
"v_add_f32 v82, v82, v90\n"
"v_mfma_f32_16x16x32_bf16 v[64:67], v[32:35], a[24:27], v[64:67]\n"
"v_add_f32 v83, v83, v91\n"
"v_add_f32 v80, v80, v72\n"
"v_mfma_f32_16x16x32_bf16 v[64:67], v[36:39], a[28:31], v[64:67]\n"
"v_add_f32 v81, v81, v73\n"
"v_add_f32 v82, v82, v74\n"
"v_mfma_f32_16x16x32_bf16 v[84:87], v[96:99], a[32:35], 0\n"
"v_add_f32 v83, v83, v75\n"
"v_mfma_f32_16x16x32_bf16 v[84:87], v[100:103], a[36:39], v[84:87]\n"
"s_add_u32 s93, s50, 0xffe80000\n"
"s_and_b32 s95, s93, 0xfbffffff\n"
"ds_write_addtid_b32 v64 offset:1536\n"
"v_mfma_f32_16x16x32_bf16 v[84:87], v[104:107], a[40:43], v[84:87]\n"
"ds_write_addtid_b32 v65 offset:6176\n"
"v_cvt_pk_bf16_f32 v72, v80, v81\n"
"v_mfma_f32_16x16x32_bf16 v[84:87], v[20:23], a[44:47], v[84:87]\n"
"ds_write_addtid_b32 v66 offset:10880\n"
"v_mfma_f32_16x16x32_bf16 v[84:87], v[24:27], a[48:51], v[84:87]\n"
"ds_write_addtid_b32 v67 offset:15520\n"
"v_cvt_pk_bf16_f32 v73, v82, v83\n"
"v_mfma_f32_16x16x32_bf16 v[84:87], v[28:31], a[52:55], v[84:87]\n"
"s_waitcnt lgkmcnt(4)\n"
"v_add_f32 v92, v92, v108\n"
"v_add_f32 v93, v93, v109\n"
"v_mfma_f32_16x16x32_bf16 v[84:87], v[32:35], a[56:59], v[84:87]\n"
"v_add_f32 v94, v94, v110\n"
"v_add_f32 v95, v95, v111\n"
"v_mfma_f32_16x16x32_bf16 v[84:87], v[36:39], a[60:63], v[84:87]\n"
"v_add_f32 v92, v92, v116\n"
"v_add_f32 v93, v93, v117\n"
"v_mfma_f32_16x16x32_bf16 v[80:83], v[96:99], a[64:67], 0\n"
"v_add_f32 v94, v94, v118\n"
"v_mfma_f32_16x16x32_bf16 v[80:83], v[100:103], a[68:71], v[80:83]\n"
"ds_write_addtid_b32 v84 offset:1792\n"
"v_mfma_f32_16x16x32_bf16 v[80:83], v[104:107], a[72:75], v[80:83]\n"
"ds_write_addtid_b32 v85 offset:6432\n"
"v_add_f32 v95, v95, v119\n"
"v_mfma_f32_16x16x32_bf16 v[80:83], v[20:23], a[76:79], v[80:83]\n"
"ds_write_addtid_b32 v86 offset:11136\n"
"v_add_f32 v92, v92, v76\n"
"v_mfma_f32_16x16x32_bf16 v[80:83], v[24:27], a[80:83], v[80:83]\n"
"ds_write_addtid_b32 v87 offset:15776\n"
"v_add_f32 v93, v93, v77\n"
"v_mfma_f32_16x16x32_bf16 v[80:83], v[28:31], a[84:87], v[80:83]\n"
"s_waitcnt vmcnt(1)\n"
"v_add_f32 v94, v94, v78\n"
"v_add_f32 v95, v95, v79\n"
"v_mfma_f32_16x16x32_bf16 v[80:83], v[32:35], a[88:91], v[80:83]\n"
"s_waitcnt vmcnt(0)\n"
"v_cvt_pk_bf16_f32 v74, v92, v93\n"
"v_cvt_pk_bf16_f32 v75, v94, v95\n"
"v_mfma_f32_16x16x32_bf16 v[80:83], v[36:39], a[92:95], v[80:83]\n"
"buffer_store_dwordx4 v[72:75], v11, s[40:43], s95 offen sc1\n"
"v_lshlrev_b32 v84, 16, v112\n"
"v_and_b32 v85, 0xffff0000, v112\n"
"v_lshlrev_b32 v86, 16, v113\n"
"v_mfma_f32_16x16x32_bf16 v[76:79], v[72:75], v[72:75], 0\n"
"buffer_store_dwordx2 v[0:1], v12, s[40:43], s94 offen offset:0 sc1\n"
"v_and_b32 v87, 0xffff0000, v113\n"
"v_mfma_f32_16x16x32_bf16 v[88:91], v[14:17], v[72:75], 0\n"
"v_lshlrev_b32 v92, 16, v114\n"
"v_and_b32 v93, 0xffff0000, v114\n"
"v_mfma_f32_16x16x32_bf16 v[108:111], v[96:99], a[96:99], 0\n"
"v_cndmask_b32_e64 v76, v76, v77, s[62:63]\n"
"v_cndmask_b32_e64 v78, v78, v79, s[62:63]\n"
"v_mfma_f32_16x16x32_bf16 v[108:111], v[100:103], a[100:103], v[108:111]\n"
"ds_write_addtid_b32 v80 offset:2048\n"
"v_cndmask_b32_e64 v89, v76, v78, s[64:65]\n"
"v_mfma_f32_16x16x32_bf16 v[108:111], v[104:107], a[104:107], v[108:111]\n"
"buffer_store_dwordx2 v[88:89], v13, s[40:43], s95 offen offset:0 sc1\n"
"ds_write_addtid_b32 v81 offset:6688\n"
"v_mfma_f32_16x16x32_bf16 v[108:111], v[20:23], a[108:111], v[108:111]\n"
"ds_write_addtid_b32 v82 offset:11392\n"
"v_lshlrev_b32 v94, 16, v115\n"
"v_mfma_f32_16x16x32_bf16 v[108:111], v[24:27], a[112:115], v[108:111]\n"
"ds_write_addtid_b32 v83 offset:16032\n"
"v_and_b32 v95, 0xffff0000, v115\n"
"v_mfma_f32_16x16x32_bf16 v[108:111], v[28:31], a[116:119], v[108:111]\n"
"v_mfma_f32_16x16x32_bf16 v[108:111], v[32:35], a[120:123], v[108:111]\n"
"v_mfma_f32_16x16x32_bf16 v[108:111], v[36:39], a[124:127], v[108:111]\n"
"v_mfma_f32_16x16x32_bf16 v[72:75], v[96:99], a[128:131], 0\n"
"v_mfma_f32_16x16x32_bf16 v[72:75], v[100:103], a[132:135], v[72:75]\n"
"ds_write_addtid_b32 v108 offset:2304\n"
"v_mfma_f32_16x16x32_bf16 v[72:75], v[104:107], a[136:139], v[72:75]\n"
"ds_write_addtid_b32 v109 offset:6944\n"
"v_mfma_f32_16x16x32_bf16 v[72:75], v[20:23], a[140:143], v[72:75]\n"
"ds_write_addtid_b32 v110 offset:11648\n"
"v_mfma_f32_16x16x32_bf16 v[72:75], v[24:27], a[144:147], v[72:75]\n"
"ds_write_addtid_b32 v111 offset:16288\n"
"v_mfma_f32_16x16x32_bf16 v[72:75], v[28:31], a[148:151], v[72:75]\n"
"v_mfma_f32_16x16x32_bf16 v[72:75], v[32:35], a[152:155], v[72:75]\n"
"v_mfma_f32_16x16x32_bf16 v[72:75], v[36:39], a[156:159], v[72:75]\n"
"v_mfma_f32_16x16x32_bf16 v[76:79], v[96:99], a[160:163], 0\n"
"v_mfma_f32_16x16x32_bf16 v[76:79], v[100:103], a[164:167], v[76:79]\n"
"ds_write_addtid_b32 v72 offset:2560\n"
"v_mfma_f32_16x16x32_bf16 v[76:79], v[104:107], a[168:171], v[76:79]\n"
"ds_write_addtid_b32 v73 offset:7200\n"
"v_mfma_f32_16x16x32_bf16 v[76:79], v[20:23], a[172:175], v[76:79]\n"
"ds_write_addtid_b32 v74 offset:11904\n"
"v_mfma_f32_16x16x32_bf16 v[76:79], v[24:27], a[176:179], v[76:79]\n"
"ds_write_addtid_b32 v75 offset:16544\n"
"v_mfma_f32_16x16x32_bf16 v[76:79], v[28:31], a[180:183], v[76:79]\n"
"v_mfma_f32_16x16x32_bf16 v[76:79], v[32:35], a[184:187], v[76:79]\n"
"v_mfma_f32_16x16x32_bf16 v[76:79], v[36:39], a[188:191], v[76:79]\n"
"v_mfma_f32_16x16x32_bf16 v[84:87], a[192:195], v[96:99], v[84:87]\n"
"v_mfma_f32_16x16x32_bf16 v[84:87], a[196:199], v[100:103], v[84:87]\n"
"ds_write_addtid_b32 v76 offset:2816\n"
"v_mfma_f32_16x16x32_bf16 v[84:87], a[200:203], v[104:107], v[84:87]\n"
"ds_write_addtid_b32 v77 offset:7456\n"
"v_mfma_f32_16x16x32_bf16 v[84:87], a[204:207], v[20:23], v[84:87]\n"
"ds_write_addtid_b32 v78 offset:12160\n"
"v_mfma_f32_16x16x32_bf16 v[84:87], a[208:211], v[24:27], v[84:87]\n"
"ds_write_addtid_b32 v79 offset:16800\n"
"v_mfma_f32_16x16x32_bf16 v[84:87], a[212:215], v[28:31], v[84:87]\n"
"v_mfma_f32_16x16x32_bf16 v[84:87], a[216:219], v[32:35], v[84:87]\n"
"v_mfma_f32_16x16x32_bf16 v[84:87], a[220:223], v[36:39], v[84:87]\n"
"v_mfma_f32_16x16x32_bf16 v[92:95], a[224:227], v[96:99], v[92:95]\n"
"s_waitcnt lgkmcnt(0)\n"
"v_mfma_f32_16x16x32_bf16 v[92:95], a[228:231], v[100:103], v[92:95]\n"
"s_barrier\n"
"v_mfma_f32_16x16x32_bf16 v[92:95], a[232:235], v[104:107], v[92:95]\n"
"ds_read_b128 v[96:99], v2 offset:0\n"
"ds_read_b128 v[100:103], v3 offset:2048\n"
"v_mfma_f32_16x16x32_bf16 v[92:95], a[236:239], v[20:23], v[92:95]\n"
"ds_read_b128 v[104:107], v4 offset:1024\n"
"ds_read_b128 v[108:111], v2 offset:256\n"
"v_mfma_f32_16x16x32_bf16 v[92:95], a[240:243], v[24:27], v[92:95]\n"
"ds_read_b128 v[112:115], v3 offset:2304\n"
"ds_read_b128 v[116:119], v4 offset:1280\n"
"v_mfma_f32_16x16x32_bf16 v[92:95], a[244:247], v[28:31], v[92:95]\n"
"v_mfma_f32_16x16x32_bf16 v[92:95], a[248:251], v[32:35], v[92:95]\n"
"v_mfma_f32_16x16x32_bf16 v[92:95], a[252:255], v[36:39], v[92:95]\n"
"s_waitcnt lgkmcnt(3)\n"
"v_add_f32 v96, v96, v100\n"
"v_add_f32 v97, v97, v101\n"
"v_add_f32 v98, v98, v102\n"
"v_add_f32 v99, v99, v103\n"
"v_add_f32 v96, v96, v104\n"
"v_add_f32 v97, v97, v105\n"
"v_add_f32 v98, v98, v106\n"
"v_add_f32 v99, v99, v107\n"
"v_add_f32 v96, v96, v84\n"
"v_add_f32 v97, v97, v85\n"
"v_add_f32 v98, v98, v86\n"
"v_add_f32 v99, v99, v87\n"
"s_add_u32 s95, s50, 0x0\n"
"s_and_b32 s94, s95, 0xfbffffff\n"
"s_waitcnt vmcnt(0)\n"
"buffer_store_dwordx2 v[0:1], v12, s[40:43], s93 offen offset:0 sc1\n"
"v_cvt_pk_bf16_f32 v84, v96, v97\n"
"v_cvt_pk_bf16_f32 v85, v98, v99\n"
"s_waitcnt lgkmcnt(0)\n"
"v_add_f32 v108, v108, v112\n"
"v_add_f32 v109, v109, v113\n"
"v_add_f32 v110, v110, v114\n"
"v_add_f32 v111, v111, v115\n"
"v_add_f32 v108, v108, v116\n"
"v_add_f32 v109, v109, v117\n"
"v_add_f32 v110, v110, v118\n"
"v_add_f32 v111, v111, v119\n"
"v_add_f32 v108, v108, v92\n"
"v_add_f32 v109, v109, v93\n"
"v_add_f32 v110, v110, v94\n"
"v_add_f32 v111, v111, v95\n"
"v_cvt_pk_bf16_f32 v86, v108, v109\n"
"v_cvt_pk_bf16_f32 v87, v110, v111\n"
"buffer_store_dwordx4 v[84:87], v11, s[40:43], s94 offen sc1\n"
"s_nop 0\n"
"v_mfma_f32_16x16x32_bf16 v[108:111], v[84:87], v[84:87], 0\n"
"v_mfma_f32_16x16x32_bf16 v[112:115], v[14:17], v[84:87], 0\n"
"s_nop 6\n"
"v_cndmask_b32_e64 v108, v108, v109, s[62:63]\n"
"v_cndmask_b32_e64 v110, v110, v111, s[62:63]\n"
"v_cndmask_b32_e64 v113, v108, v110, s[64:65]\n"
"buffer_store_dwordx2 v[112:113], v13, s[40:43], s94 offen offset:0 sc1\n"
"s_waitcnt vmcnt(0)\n"
"buffer_store_dwordx2 v[0:1], v12, s[40:43], s95 offen offset:0 sc1\n"
"s_branch .LO_end\n"
".LO_t3:\n"
"v_mfma_f32_16x16x32_bf16 v[84:87], a[196:199], v[100:103], v[84:87]\n"
"ds_write_addtid_b32 v76 offset:2816\n"
"v_mfma_f32_16x16x32_bf16 v[84:87], a[200:203], v[104:107], v[84:87]\n"
"ds_write_addtid_b32 v77 offset:7456\n"
"v_mfma_f32_16x16x32_bf16 v[84:87], a[204:207], v[20:23], v[84:87]\n"
"ds_write_addtid_b32 v78 offset:12160\n"
"v_mfma_f32_16x16x32_bf16 v[84:87], a[208:211], v[24:27], v[84:87]\n"
"ds_write_addtid_b32 v79 offset:16800\n"
"v_mfma_f32_16x16x32_bf16 v[84:87], a[212:215], v[28:31], v[84:87]\n"
"v_mfma_f32_16x16x32_bf16 v[84:87], a[216:219], v[32:35], v[84:87]\n"
"v_mfma_f32_16x16x32_bf16 v[84:87], a[220:223], v[36:39], v[84:87]\n"
"v_mfma_f32_16x16x32_bf16 v[92:95], a[224:227], v[96:99], v[92:95]\n"
"s_waitcnt lgkmcnt(0)\n"
"v_mfma_f32_16x16x32_bf16 v[92:95], a[228:231], v[100:103], v[92:95]\n"
"s_barrier\n"
"v_mfma_f32_16x16x32_bf16 v[92:95], a[232:235], v[104:107], v[92:95]\n"
"ds_read_b128 v[96:99], v2 offset:0\n"
"ds_read_b128 v[100:103], v3 offset:2048\n"
"v_mfma_f32_16x16x32_bf16 v[92:95], a[236:239], v[20:23], v[92:95]\n"
"ds_read_b128 v[104:107], v4 offset:1024\n"
"ds_read_b128 v[108:111], v2 offset:256\n"
"v_mfma_f32_16x16x32_bf16 v[92:95], a[240:243], v[24:27], v[92:95]\n"
"ds_read_b128 v[112:115], v3 offset:2304\n"
"ds_read_b128 v[116:119], v4 offset:1280\n"
"v_mfma_f32_16x16x32_bf16 v[92:95], a[244:247], v[28:31], v[92:95]\n"
"v_mfma_f32_16x16x32_bf16 v[92:95], a[248:251], v[32:35], v[92:95]\n"
"v_mfma_f32_16x16x32_bf16 v[92:95], a[252:255], v[36:39], v[92:95]\n"
"s_waitcnt vmcnt(11)\n"
"v_mfma_f32_16x16x32_bf16 v[120:123], v[40:43], a[0:3], 0\n"
"v_mfma_f32_16x16x32_bf16 v[120:123], v[44:47], a[4:7], v[120:123]\n"
"s_waitcnt vmcnt(8)\n"
"v_mfma_f32_16x16x32_bf16 v[120:123], v[48:51], a[8:11], v[120:123]\n"
"s_waitcnt lgkmcnt(3)\n"
"v_add_f32 v96, v96, v100\n"
"v_add_f32 v97, v97, v101\n"
"v_mfma_f32_16x16x32_bf16 v[120:123], v[52:55], a[12:15], v[120:123]\n"
"s_waitcnt vmcnt(6)\n"
"v_add_f32 v98, v98, v102\n"
"v_add_f32 v99, v99, v103\n"
"v_mfma_f32_16x16x32_bf16 v[120:123], v[56:59], a[16:19], v[120:123]\n"
"v_add_f32 v96, v96, v104\n"
"v_add_f32 v97, v97, v105\n"
"v_mfma_f32_16x16x32_bf16 v[120:123], v[60:63], a[20:23], v[120:123]\n"
"s_waitcnt vmcnt(4)\n"
"v_add_f32 v98, v98, v106\n"
"v_add_f32 v99, v99, v107\n"
"v_mfma_f32_16x16x32_bf16 v[120:123], v[64:67], a[24:27], v[120:123]\n"
"v_add_f32 v96, v96, v84\n"
"v_mfma_f32_16x16x32_bf16 v[120:123], v[68:71], a[28:31], v[120:123]\n"
"v_add_f32 v97, v97, v85\n"
"v_mfma_f32_16x16x32_bf16 v[100:103], v[40:43], a[32:35], 0\n"
"v_add_f32 v98, v98, v86\n"
"v_add_f32 v99, v99, v87\n"
"v_mfma_f32_16x16x32_bf16 v[100:103], v[44:47], a[36:39], v[100:103]\n"
"s_add_u32 s95, s50, 0xffe80000\n"
"s_and_b32 s94, s95, 0xfbffffff\n"
"ds_write_addtid_b32 v120 offset:0\n"
"v_mfma_f32_16x16x32_bf16 v[100:103], v[48:51], a[40:43], v[100:103]\n"
"s_waitcnt vmcnt(1)\n"
"buffer_store_dwordx2 v[0:1], v12, s[40:43], s93 offen offset:0 sc1\n"
"ds_write_addtid_b32 v121 offset:4640\n"
"v_mfma_f32_16x16x32_bf16 v[100:103], v[52:55], a[44:47], v[100:103]\n"
"ds_write_addtid_b32 v122 offset:9344\n"
"v_cvt_pk_bf16_f32 v84, v96, v97\n"
"v_mfma_f32_16x16x32_bf16 v[100:103], v[56:59], a[48:51], v[100:103]\n"
"ds_write_addtid_b32 v123 offset:13984\n"
"v_mfma_f32_16x16x32_bf16 v[100:103], v[60:63], a[52:55], v[100:103]\n"
"v_cvt_pk_bf16_f32 v85, v98, v99\n"
"v_mfma_f32_16x16x32_bf16 v[100:103], v[64:67], a[56:59], v[100:103]\n"
"s_waitcnt lgkmcnt(4)\n"
"v_add_f32 v108, v108, v112\n"
"v_add_f32 v109, v109, v113\n"
"v_mfma_f32_16x16x32_bf16 v[100:103], v[68:71], a[60:63], v[100:103]\n"
"v_add_f32 v110, v110, v114\n"
"v_add_f32 v111, v111, v115\n"
"v_mfma_f32_16x16x32_bf16 v[96:99], v[40:43], a[64:67], 0\n"
"v_add_f32 v108, v108, v116\n"
"v_add_f32 v109, v109, v117\n"
"v_mfma_f32_16x16x32_bf16 v[96:99], v[44:47], a[68:71], v[96:99]\n"
"ds_write_addtid_b32 v100 offset:256\n"
"v_add_f32 v110, v110, v118\n"
"v_mfma_f32_16x16x32_bf16 v[96:99], v[48:51], a[72:75], v[96:99]\n"
"ds_write_addtid_b32 v101 offset:4896\n"
"v_add_f32 v111, v111, v119\n"
"v_mfma_f32_16x16x32_bf16 v[96:99], v[52:55], a[76:79], v[96:99]\n"
"ds_write_addtid_b32 v102 offset:9600\n"
"v_add_f32 v108, v108, v92\n"
"v_mfma_f32_16x16x32_bf16 v[96:99], v[56:59], a[80:83], v[96:99]\n"
"ds_write_addtid_b32 v103 offset:14240\n"
"v_add_f32 v109, v109, v93\n"
"v_mfma_f32_16x16x32_bf16 v[96:99], v[60:63], a[84:87], v[96:99]\n"
"v_add_f32 v110, v110, v94\n"
"v_mfma_f32_16x16x32_bf16 v[96:99], v[64:67], a[88:91], v[96:99]\n"
"s_waitcnt vmcnt(1)\n"
"v_add_f32 v111, v111, v95\n"
"v_cvt_pk_bf16_f32 v86, v108, v109\n"
"v_mfma_f32_16x16x32_bf16 v[96:99], v[68:71], a[92:95], v[96:99]\n"
"v_cvt_pk_bf16_f32 v87, v110, v111\n"
"v_lshlrev_b32 v100, 16, v88\n"
"v_mfma_f32_16x16x32_bf16 v[92:95], v[40:43], a[96:99], 0\n"
"v_and_b32 v101, 0xffff0000, v88\n"
"v_lshlrev_b32 v102, 16, v89\n"
"v_mfma_f32_16x16x32_bf16 v[92:95], v[44:47], a[100:103], v[92:95]\n"
"ds_write_addtid_b32 v96 offset:512\n"
"v_and_b32 v103, 0xffff0000, v89\n"
"v_mfma_f32_16x16x32_bf16 v[92:95], v[48:51], a[104:107], v[92:95]\n"
"ds_write_addtid_b32 v97 offset:5152\n"
"v_lshlrev_b32 v104, 16, v90\n"
"v_mfma_f32_16x16x32_bf16 v[92:95], v[52:55], a[108:111], v[92:95]\n"
"buffer_store_dwordx4 v[84:87], v11, s[40:43], s94 offen sc1\n"
"ds_write_addtid_b32 v98 offset:9856\n"
"v_and_b32 v105, 0xffff0000, v90\n"
"v_lshlrev_b32 v106, 16, v91\n"
"v_mfma_f32_16x16x32_bf16 v[108:111], v[84:87], v[84:87], 0\n"
"v_and_b32 v107, 0xffff0000, v91\n"
"v_mfma_f32_16x16x32_bf16 v[112:115], v[14:17], v[84:87], 0\n"
"v_mfma_f32_16x16x32_bf16 v[92:95], v[56:59], a[112:115], v[92:95]\n"
"ds_write_addtid_b32 v99 offset:14496\n"
"v_cndmask_b32_e64 v108, v108, v109, s[62:63]\n"
"v_mfma_f32_16x16x32_bf16 v[92:95], v[60:63], a[116:119], v[92:95]\n"
"v_cndmask_b32_e64 v110, v110, v111, s[62:63]\n"
"v_cndmask_b32_e64 v113, v108, v110, s[64:65]\n"
"v_mfma_f32_16x16x32_bf16 v[92:95], v[64:67], a[120:123], v[92:95]\n"
"buffer_store_dwordx2 v[112:113], v13, s[40:43], s94 offen offset:0 sc1\n"
"v_mfma_f32_16x16x32_bf16 v[92:95], v[68:71], a[124:127], v[92:95]\n"
"v_mfma_f32_16x16x32_bf16 v[84:87], v[40:43], a[128:131], 0\n"
"v_mfma_f32_16x16x32_bf16 v[84:87], v[44:47], a[132:135], v[84:87]\n"
"ds_write_addtid_b32 v92 offset:768\n"
"v_mfma_f32_16x16x32_bf16 v[84:87], v[48:51], a[136:139], v[84:87]\n"
"ds_write_addtid_b32 v93 offset:5408\n"
"v_mfma_f32_16x16x32_bf16 v[84:87], v[52:55], a[140:143], v[84:87]\n"
"ds_write_addtid_b32 v94 offset:10112\n"
"v_mfma_f32_16x16x32_bf16 v[84:87], v[56:59], a[144:147], v[84:87]\n"
"ds_write_addtid_b32 v95 offset:14752\n"
"v_mfma_f32_16x16x32_bf16 v[84:87], v[60:63], a[148:151], v[84:87]\n"
"v_mfma_f32_16x16x32_bf16 v[84:87], v[64:67], a[152:155], v[84:87]\n"
"v_mfma_f32_16x16x32_bf16 v[84:87], v[68:71], a[156:159], v[84:87]\n"
"v_mfma_f32_16x16x32_bf16 v[92:95], v[40:43], a[160:163], 0\n"
"v_mfma_f32_16x16x32_bf16 v[92:95], v[44:47], a[164:167], v[92:95]\n"
"ds_write_addtid_b32 v84 offset:1024\n"
"v_mfma_f32_16x16x32_bf16 v[92:95], v[48:51], a[168:171], v[92:95]\n"
"ds_write_addtid_b32 v85 offset:5664\n"
"v_mfma_f32_16x16x32_bf16 v[92:95], v[52:55], a[172:175], v[92:95]\n"
"ds_write_addtid_b32 v86 offset:10368\n"
"v_mfma_f32_16x16x32_bf16 v[92:95], v[56:59], a[176:179], v[92:95]\n"
"ds_write_addtid_b32 v87 offset:15008\n"
"v_mfma_f32_16x16x32_bf16 v[92:95], v[60:63], a[180:183], v[92:95]\n"
"v_mfma_f32_16x16x32_bf16 v[92:95], v[64:67], a[184:187], v[92:95]\n"
"v_mfma_f32_16x16x32_bf16 v[92:95], v[68:71], a[188:191], v[92:95]\n"
"v_mfma_f32_16x16x32_bf16 v[100:103], a[192:195], v[40:43], v[100:103]\n"
"v_mfma_f32_16x16x32_bf16 v[100:103], a[196:199], v[44:47], v[100:103]\n"
"ds_write_addtid_b32 v92 offset:1280\n"
"v_mfma_f32_16x16x32_bf16 v[100:103], a[200:203], v[48:51], v[100:103]\n"
"ds_write_addtid_b32 v93 offset:5920\n"
"v_mfma_f32_16x16x32_bf16 v[100:103], a[204:207], v[52:55], v[100:103]\n"
"ds_write_addtid_b32 v94 offset:10624\n"
"v_mfma_f32_16x16x32_bf16 v[100:103], a[208:211], v[56:59], v[100:103]\n"
"ds_write_addtid_b32 v95 offset:15264\n"
"v_mfma_f32_16x16x32_bf16 v[100:103], a[212:215], v[60:63], v[100:103]\n"
"v_mfma_f32_16x16x32_bf16 v[100:103], a[216:219], v[64:67], v[100:103]\n"
"v_mfma_f32_16x16x32_bf16 v[100:103], a[220:223], v[68:71], v[100:103]\n"
"v_mfma_f32_16x16x32_bf16 v[104:107], a[224:227], v[40:43], v[104:107]\n"
"s_waitcnt lgkmcnt(0)\n"
"v_mfma_f32_16x16x32_bf16 v[104:107], a[228:231], v[44:47], v[104:107]\n"
"s_barrier\n"
"v_mfma_f32_16x16x32_bf16 v[104:107], a[232:235], v[48:51], v[104:107]\n"
"ds_read_b128 v[40:43], v2 offset:1536\n"
"v_mfma_f32_16x16x32_bf16 v[104:107], a[236:239], v[52:55], v[104:107]\n"
"ds_read_b128 v[44:47], v3 offset:512\n"
"ds_read_b128 v[48:51], v4 offset:2560\n"
"v_mfma_f32_16x16x32_bf16 v[104:107], a[240:243], v[56:59], v[104:107]\n"
"ds_read_b128 v[108:111], v2 offset:1792\n"
"ds_read_b128 v[112:115], v3 offset:768\n"
"v_mfma_f32_16x16x32_bf16 v[104:107], a[244:247], v[60:63], v[104:107]\n"
"ds_read_b128 v[116:119], v4 offset:2816\n"
"v_mfma_f32_16x16x32_bf16 v[104:107], a[248:251], v[64:67], v[104:107]\n"
"v_mfma_f32_16x16x32_bf16 v[104:107], a[252:255], v[68:71], v[104:107]\n"
"s_waitcnt lgkmcnt(3)\n"
"v_add_f32 v40, v40, v44\n"
"v_add_f32 v41, v41, v45\n"
"v_add_f32 v42, v42, v46\n"
"v_add_f32 v43, v43, v47\n"
"v_add_f32 v40, v40, v48\n"
"v_add_f32 v41, v41, v49\n"
"v_add_f32 v42, v42, v50\n"
"v_add_f32 v43, v43, v51\n"
"v_add_f32 v40, v40, v100\n"
"v_add_f32 v41, v41, v101\n"
"v_add_f32 v42, v42, v102\n"
"v_add_f32 v43, v43, v103\n"
"s_add_u32 s93, s50, 0x0\n"
"s_and_b32 s94, s93, 0xfbffffff\n"
"v_cvt_pk_bf16_f32 v48, v40, v41\n"
"v_cvt_pk_bf16_f32 v49, v42, v43\n"
"s_waitcnt lgkmcnt(0)\n"
"v_add_f32 v108, v108, v112\n"
"v_add_f32 v109, v109, v113\n"
"s_waitcnt vmcnt(0)\n"
"v_add_f32 v110, v110, v114\n"
"buffer_store_dwordx2 v[0:1], v12, s[40:43], s95 offen offset:0 sc1\n"
"v_add_f32 v111, v111, v115\n"
"v_add_f32 v108, v108, v116\n"
"v_add_f32 v109, v109, v117\n"
"v_add_f32 v110, v110, v118\n"
"v_add_f32 v111, v111, v119\n"
"v_add_f32 v108, v108, v104\n"
"v_add_f32 v109, v109, v105\n"
"v_add_f32 v110, v110, v106\n"
"v_add_f32 v111, v111, v107\n"
"v_cvt_pk_bf16_f32 v50, v108, v109\n"
"v_cvt_pk_bf16_f32 v51, v110, v111\n"
"buffer_store_dwordx4 v[48:51], v11, s[40:43], s94 offen sc1\n"
"s_nop 0\n"
"v_mfma_f32_16x16x32_bf16 v[40:43], v[48:51], v[48:51], 0\n"
"v_mfma_f32_16x16x32_bf16 v[96:99], v[14:17], v[48:51], 0\n"
"s_nop 6\n"
"v_cndmask_b32_e64 v40, v40, v41, s[62:63]\n"
"v_cndmask_b32_e64 v42, v42, v43, s[62:63]\n"
"v_cndmask_b32_e64 v97, v40, v42, s[64:65]\n"
"buffer_store_dwordx2 v[96:97], v13, s[40:43], s94 offen offset:0 sc1\n"
"s_waitcnt vmcnt(0)\n"
"buffer_store_dwordx2 v[0:1], v12, s[40:43], s93 offen offset:0 sc1\n"
"s_branch .LO_end\n"
".LO_end:\n"
"s_waitcnt vmcnt(0) lgkmcnt(0)\n"
"s_endpgm\n"
      :: "{v2}"((unsigned)(lds0 + ((wid + 3) & 3) * 18592 + trp)), "{v3}"((unsigned)(lds0 + ((wid + 2) & 3) * 18592 + trp)), "{v4}"((unsigned)(lds0 + ((wid + 1) & 3) * 18592 + trp)), "{v5}"((unsigned)(lane * 16 + wid * 8192 + ((0 + rot) % 4) * 2048 + 134250496)), "{v6}"((unsigned)(lane * 16 + wid * 8192 + ((1 + rot) % 4) * 2048 + 134250496)), "{v7}"((unsigned)(lane * 16 + wid * 8192 + ((2 + rot) % 4) * 2048 + 134250496)), "{v8}"((unsigned)(lane * 16 + wid * 8192 + ((3 + rot) % 4) * 2048 + 134250496)), "{v9}"((unsigned)((wid * 8 + (lane & 7)) * 8 + 352512 + O_GL * 1572864)), "{v10}"((unsigned)(lane * 16 + pair * 1024 + 134217728)), "{v11}"((unsigned)(lane * 16 + pair * 1024 + 65536)), "{v12}"((unsigned)(lane ? DROP : pair * 8 + 352768)), "{v13}"((unsigned)(lg == (li >> 2) ? pair * 128 + li * 8 + 344064 : DROP)), "{v14}"((unsigned)(0x3f803f80)), "{v15}"((unsigned)(0x3f803f80)), "{v16}"((unsigned)(0x3f803f80)), "{v17}"((unsigned)(0x3f803f80)), "{v20}"((unsigned)(vT + tsw)), "{v21}"((unsigned)(vT + (tsw ^ 8))), "{v22}"((unsigned)(vD + ((0 + rot) % 4) * 131072)), "{v23}"((unsigned)(vD + ((1 + rot) % 4) * 131072)), "{v24}"((unsigned)(vD + ((2 + rot) % 4) * 131072)), "{v25}"((unsigned)(vD + ((3 + rot) % 4) * 131072)), "{s[40:43]}"(rS), "{s[44:47]}"(rW), "{s[48:49]}"(wsp), "{s50}"((unsigned)(sA)), "{s51}"((unsigned)(sLo)), "{s52}"((unsigned)(sHi)), "{s53}"((unsigned)(sEx)), "{s54}"((unsigned)(sEnt)), "{s55}"((unsigned)(sLd)), "{s[56:57]}"(wsb + (sLo & 0x3ffffff)), "{s59}"((unsigned)(lds0 + wid * 18592)), "{s[62:63]}"(0xAAAAAAAAAAAAAAAAull), "{s[64:65]}"(0xCCCCCCCCCCCCCCCCull), "{s66}"((unsigned)(sW0 + 64 * ((wid + 1) & 3))), "{s67}"((unsigned)(sW0 + 64 * ((wid + 2) & 3))), "{s68}"((unsigned)(sW0 + 64 * ((wid + 3) & 3))), "{s69}"((unsigned)(sW0 + 64 * ((wid + 4) & 3)))
      : "v255", "a255", "s95", "vcc", "scc", "memory");

  } else if (role == 2) {
    const unsigned sEx = sHi + F_EX * 3 * RS, sEnt = -wid & 3, tsw = 0, pair = idx * 4 + wid, rot = SLP & 4 ? FR0 * (xo >= 0) : KR * (set ? set & 2 : 1);
    const unsigned sW0 = wid * 256 * 7168 + idx * 256, vD = (lane >> 2) * 7168 + (lane & 3) * 16;
    const u32x4 rW = desc(a.w1, 1024 * 7168);
    asm volatile(
"s_nop 4\n"
"v_mov_b32 v186, 0\n"
"global_load_dwordx2 v[0:1], v186, s[48:49] sc1\n"
"global_load_dwordx4 v[30:33], v38, s[64:65]\n"
"v_mov_b32 v188, 0x3f803f80\n"
"v_mov_b32 v189, 0x3f803f80\n"
"v_mov_b32 v190, 0x3f803f80\n"
"v_mov_b32 v191, 0x3f803f80\n"
"s_add_u32 m0, s55, 0\n"
"s_add_u32 s60, s66, 0\n"
"buffer_load_dwordx4 v39, s[44:47], s60 offen lds\n"
"s_add_u32 m0, s55, 1024\n"
"s_add_u32 s60, s66, 114688\n"
"buffer_load_dwordx4 v39, s[44:47], s60 offen lds\n"
"s_add_u32 m0, s55, 2048\n"
"s_add_u32 s60, s67, 0\n"
"buffer_load_dwordx4 v39, s[44:47], s60 offen lds\n"
"s_add_u32 m0, s55, 3072\n"
"s_add_u32 s60, s67, 114688\n"
"buffer_load_dwordx4 v39, s[44:47], s60 offen lds\n"
"s_add_u32 m0, s55, 4096\n"
"s_add_u32 s60, s68, 0\n"
"buffer_load_dwordx4 v39, s[44:47], s60 offen lds\n"
"s_add_u32 m0, s55, 5120\n"
"s_add_u32 s60, s68, 114688\n"
"buffer_load_dwordx4 v39, s[44:47], s60 offen lds\n"
"s_add_u32 m0, s55, 6144\n"
"s_add_u32 s60, s69, 0\n"
"buffer_load_dwordx4 v39, s[44:47], s60 offen lds\n"
"s_add_u32 m0, s55, 7168\n"
"s_add_u32 s60, s69, 114688\n"
"buffer_load_dwordx4 v39, s[44:47], s60 offen lds\n"
"s_add_u32 m0, s55, 8192\n"
"s_add_u32 s60, s66, 229376\n"
"buffer_load_dwordx4 v39, s[44:47], s60 offen lds\n"
"s_add_u32 m0, s55, 9216\n"
"s_add_u32 s60, s66, 344064\n"
"buffer_load_dwordx4 v39, s[44:47], s60 offen lds\n"
"s_add_u32 m0, s55, 10240\n"
"s_add_u32 s60, s67, 229376\n"
"buffer_load_dwordx4 v39, s[44:47], s60 offen lds\n"
"s_add_u32 m0, s55, 11264\n"
"s_add_u32 s60, s67, 344064\n"
"buffer_load_dwordx4 v39, s[44:47], s60 offen lds\n"
"s_add_u32 m0, s55, 12288\n"
"s_add_u32 s60, s68, 229376\n"
"buffer_load_dwordx4 v39, s[44:47], s60 offen lds\n"
"s_add_u32 m0, s55, 13312\n"
"s_add_u32 s60, s68, 344064\n"
"buffer_load_dwordx4 v39, s[44:47], s60 offen lds\n"
"s_add_u32 m0, s55, 14336\n"
"s_add_u32 s60, s69, 229376\n"
"buffer_load_dwordx4 v39, s[44:47], s60 offen lds\n"
"s_add_u32 m0, s55, 15360\n"
"s_add_u32 s60, s69, 344064\n"
"buffer_load_dwordx4 v39, s[44:47], s60 offen lds\n"
"s_add_u32 m0, s55, 16384\n"
"s_add_u32 s60, s66, 0\n"
"buffer_load_dwordx4 v40, s[44:47], s60 offen lds\n"
"s_add_u32 m0, s55, 17408\n"
"s_add_u32 s60, s66, 114688\n"
"buffer_load_dwordx4 v40, s[44:47], s60 offen lds\n"
"s_add_u32 m0, s55, 18432\n"
"s_add_u32 s60, s67, 0\n"
"buffer_load_dwordx4 v40, s[44:47], s60 offen lds\n"
"s_add_u32 m0, s55, 19456\n"
"s_add_u32 s60, s67, 114688\n"
"buffer_load_dwordx4 v40, s[44:47], s60 offen lds\n"
"s_add_u32 m0, s55, 20480\n"
"s_add_u32 s60, s68, 0\n"
"buffer_load_dwordx4 v40, s[44:47], s60 offen lds\n"
"s_add_u32 m0, s55, 21504\n"
"s_add_u32 s60, s68, 114688\n"
"buffer_load_dwordx4 v40, s[44:47], s60 offen lds\n"
"s_add_u32 m0, s55, 22528\n"
"s_add_u32 s60, s69, 0\n"
"buffer_load_dwordx4 v40, s[44:47], s60 offen lds\n"
"s_add_u32 m0, s55, 23552\n"
"s_add_u32 s60, s69, 114688\n"
"buffer_load_dwordx4 v40, s[44:47], s60 offen lds\n"
"s_add_u32 m0, s55, 24576\n"
"s_add_u32 s60, s66, 229376\n"
"buffer_load_dwordx4 v40, s[44:47], s60 offen lds\n"
"s_add_u32 m0, s55, 25600\n"
"s_add_u32 s60, s66, 344064\n"
"buffer_load_dwordx4 v40, s[44:47], s60 offen lds\n"
"s_add_u32 m0, s55, 26624\n"
"s_add_u32 s60, s67, 229376\n"
"buffer_load_dwordx4 v40, s[44:47], s60 offen lds\n"
"s_add_u32 m0, s55, 27648\n"
"s_add_u32 s60, s67, 344064\n"
"buffer_load_dwordx4 v40, s[44:47], s60 offen lds\n"
"s_add_u32 m0, s55, 28672\n"
"s_add_u32 s60, s68, 229376\n"
"buffer_load_dwordx4 v40, s[44:47], s60 offen lds\n"
"s_add_u32 m0, s55, 29696\n"
"s_add_u32 s60, s68, 344064\n"
"buffer_load_dwordx4 v40, s[44:47], s60 offen lds\n"
"s_add_u32 m0, s55, 30720\n"
"s_add_u32 s60, s69, 229376\n"
"buffer_load_dwordx4 v40, s[44:47], s60 offen lds\n"
"s_add_u32 m0, s55, 31744\n"
"s_add_u32 s60, s69, 344064\n"
"buffer_load_dwordx4 v40, s[44:47], s60 offen lds\n"
"s_add_u32 m0, s55, 32768\n"
"s_add_u32 s60, s66, 0\n"
"buffer_load_dwordx4 v41, s[44:47], s60 offen lds\n"
"s_add_u32 m0, s55, 33792\n"
"s_add_u32 s60, s66, 114688\n"
"buffer_load_dwordx4 v41, s[44:47], s60 offen lds\n"
"s_add_u32 m0, s55, 34816\n"
"s_add_u32 s60, s67, 0\n"
"buffer_load_dwordx4 v41, s[44:47], s60 offen lds\n"
"s_add_u32 m0, s55, 35840\n"
"s_add_u32 s60, s67, 114688\n"
"buffer_load_dwordx4 v41, s[44:47], s60 offen lds\n"
"s_add_u32 m0, s55, 36864\n"
"s_add_u32 s60, s68, 0\n"
"buffer_load_dwordx4 v41, s[44:47], s60 offen lds\n"
"s_add_u32 m0, s55, 37888\n"
"s_add_u32 s60, s68, 114688\n"
"buffer_load_dwordx4 v41, s[44:47], s60 offen lds\n"
"s_add_u32 m0, s55, 38912\n"
"s_add_u32 s60, s69, 0\n"
"buffer_load_dwordx4 v41, s[44:47], s60 offen lds\n"
"s_add_u32 m0, s55, 39936\n"
"s_add_u32 s60, s69, 114688\n"
"buffer_load_dwordx4 v41, s[44:47], s60 offen lds\n"
"s_waitcnt vmcnt(38)\n"
"ds_read_b64_tr_b16 a[0:1], v36 offset:0\n"
"ds_read_b64_tr_b16 a[2:3], v36 offset:256\n"
"ds_read_b64_tr_b16 a[32:33], v37 offset:0\n"
"ds_read_b64_tr_b16 a[34:35], v37 offset:256\n"
"v_add_u32 v0, 2, v0\n"
"v_add_u32 v1, 0x9e3779b9, v1\n"
"v_or_b32 v0, 1, v0\n"
"v_readfirstlane_b32 s49, v1\n"
"v_readfirstlane_b32 s48, v0\n"
"s_waitcnt vmcnt(36)\n"
"ds_read_b64_tr_b16 a[64:65], v36 offset:2048\n"
"ds_read_b64_tr_b16 a[66:67], v36 offset:2304\n"
"ds_read_b64_tr_b16 a[96:97], v37 offset:2048\n"
"ds_read_b64_tr_b16 a[98:99], v37 offset:2304\n"
"s_waitcnt lgkmcnt(4)\n"
"s_add_u32 m0, s55, 0\n"
"s_add_u32 s60, s66, 229376\n"
"buffer_load_dwordx4 v41, s[44:47], s60 offen lds\n"
"s_add_u32 m0, s55, 1024\n"
"s_add_u32 s60, s66, 344064\n"
"buffer_load_dwordx4 v41, s[44:47], s60 offen lds\n"
"v_mfma_f32_16x16x32_bf16 v[192:195], v[188:191], a[0:3], 0\n"
"v_mfma_f32_16x16x32_bf16 v[196:199], v[188:191], a[32:35], 0\n"
"s_waitcnt vmcnt(36)\n"
"ds_read_b64_tr_b16 a[128:129], v36 offset:4096\n"
"ds_read_b64_tr_b16 a[130:131], v36 offset:4352\n"
"ds_read_b64_tr_b16 a[160:161], v37 offset:4096\n"
"ds_read_b64_tr_b16 a[162:163], v37 offset:4352\n"
"s_waitcnt lgkmcnt(4)\n"
"s_add_u32 m0, s55, 2048\n"
"s_add_u32 s60, s67, 229376\n"
"buffer_load_dwordx4 v41, s[44:47], s60 offen lds\n"
"s_add_u32 m0, s55, 3072\n"
"s_add_u32 s60, s67, 344064\n"
"buffer_load_dwordx4 v41, s[44:47], s60 offen lds\n"
"v_mfma_f32_16x16x32_bf16 v[200:203], v[188:191], a[64:67], 0\n"
"v_mfma_f32_16x16x32_bf16 v[204:207], v[188:191], a[96:99], 0\n"
"s_waitcnt vmcnt(36)\n"
"ds_read_b64_tr_b16 a[192:193], v36 offset:6144\n"
"ds_read_b64_tr_b16 a[194:195], v36 offset:6400\n"
"ds_read_b64_tr_b16 a[224:225], v37 offset:6144\n"
"ds_read_b64_tr_b16 a[226:227], v37 offset:6400\n"
"s_waitcnt lgkmcnt(4)\n"
"s_add_u32 m0, s55, 4096\n"
"s_add_u32 s60, s68, 229376\n"
"buffer_load_dwordx4 v41, s[44:47], s60 offen lds\n"
"s_add_u32 m0, s55, 5120\n"
"s_add_u32 s60, s68, 344064\n"
"buffer_load_dwordx4 v41, s[44:47], s60 offen lds\n"
"v_mfma_f32_16x16x32_bf16 v[208:211], v[188:191], a[128:131], 0\n"
"v_mfma_f32_16x16x32_bf16 v[212:215], v[188:191], a[160:163], 0\n"
"s_waitcnt vmcnt(36)\n"
"ds_read_b64_tr_b16 a[4:5], v36 offset:8192\n"
"ds_read_b64_tr_b16 a[6:7], v36 offset:8448\n"
"ds_read_b64_tr_b16 a[36:37], v37 offset:8192\n"
"ds_read_b64_tr_b16 a[38:39], v37 offset:8448\n"
"s_waitcnt lgkmcnt(4)\n"
"s_add_u32 m0, s55, 6144\n"
"s_add_u32 s60, s69, 229376\n"
"buffer_load_dwordx4 v41, s[44:47], s60 offen lds\n"
"s_add_u32 m0, s55, 7168\n"
"s_add_u32 s60, s69, 344064\n"
"buffer_load_dwordx4 v41, s[44:47], s60 offen lds\n"
"v_mfma_f32_16x16x32_bf16 v[216:219], v[188:191], a[192:195], 0\n"
"v_mfma_f32_16x16x32_bf16 v[220:223], v[188:191], a[224:227], 0\n"
"s_waitcnt vmcnt(36)\n"
"ds_read_b64_tr_b16 a[68:69], v36 offset:10240\n"
"ds_read_b64_tr_b16 a[70:71], v36 offset:10496\n"
"ds_read_b64_tr_b16 a[100:101], v37 offset:10240\n"
"ds_read_b64_tr_b16 a[102:103], v37 offset:10496\n"
"s_waitcnt lgkmcnt(4)\n"
"s_add_u32 m0, s55, 8192\n"
"s_add_u32 s60, s66, 0\n"
"buffer_load_dwordx4 v42, s[44:47], s60 offen lds\n"
"s_add_u32 m0, s55, 9216\n"
"s_add_u32 s60, s66, 114688\n"
"buffer_load_dwordx4 v42, s[44:47], s60 offen lds\n"
"v_mfma_f32_16x16x32_bf16 v[192:195], v[188:191], a[4:7], v[192:195]\n"
"v_mfma_f32_16x16x32_bf16 v[196:199], v[188:191], a[36:39], v[196:199]\n"
"s_waitcnt vmcnt(36)\n"
"ds_read_b64_tr_b16 a[132:133], v36 offset:12288\n"
"ds_read_b64_tr_b16 a[134:135], v36 offset:12544\n"
"ds_read_b64_tr_b16 a[164:165], v37 offset:12288\n"
"ds_read_b64_tr_b16 a[166:167], v37 offset:12544\n"
"s_waitcnt lgkmcnt(4)\n"
"s_add_u32 m0, s55, 10240\n"
"s_add_u32 s60, s67, 0\n"
"buffer_load_dwordx4 v42, s[44:47], s60 offen lds\n"
"s_add_u32 m0, s55, 11264\n"
"s_add_u32 s60, s67, 114688\n"
"buffer_load_dwordx4 v42, s[44:47], s60 offen lds\n"
"v_mfma_f32_16x16x32_bf16 v[200:203], v[188:191], a[68:71], v[200:203]\n"
"v_mfma_f32_16x16x32_bf16 v[204:207], v[188:191], a[100:103], v[204:207]\n"
"s_waitcnt vmcnt(36)\n"
"ds_read_b64_tr_b16 a[196:197], v36 offset:14336\n"
"ds_read_b64_tr_b16 a[198:199], v36 offset:14592\n"
"ds_read_b64_tr_b16 a[228:229], v37 offset:14336\n"
"ds_read_b64_tr_b16 a[230:231], v37 offset:14592\n"
"s_waitcnt lgkmcnt(4)\n"
"s_add_u32 m0, s55, 12288\n"
"s_add_u32 s60, s68, 0\n"
"buffer_load_dwordx4 v42, s[44:47], s60 offen lds\n"
"s_add_u32 m0, s55, 13312\n"
"s_add_u32 s60, s68, 114688\n"
"buffer_load_dwordx4 v42, s[44:47], s60 offen lds\n"
"v_mfma_f32_16x16x32_bf16 v[208:211], v[188:191], a[132:135], v[208:211]\n"
"v_mfma_f32_16x16x32_bf16 v[212:215], v[188:191], a[164:167], v[212:215]\n"
"s_waitcnt vmcnt(36)\n"
"ds_read_b64_tr_b16 a[8:9], v36 offset:16384\n"
"ds_read_b64_tr_b16 a[10:11], v36 offset:16640\n"
"ds_read_b64_tr_b16 a[40:41], v37 offset:16384\n"
"ds_read_b64_tr_b16 a[42:43], v37 offset:16640\n"
"s_waitcnt lgkmcnt(4)\n"
"s_add_u32 m0, s55, 14336\n"
"s_add_u32 s60, s69, 0\n"
"buffer_load_dwordx4 v42, s[44:47], s60 offen lds\n"
"s_add_u32 m0, s55, 15360\n"
"s_add_u32 s60, s69, 114688\n"
"buffer_load_dwordx4 v42, s[44:47], s60 offen lds\n"
"v_mfma_f32_16x16x32_bf16 v[216:219], v[188:191], a[196:199], v[216:219]\n"
"v_mfma_f32_16x16x32_bf16 v[220:223], v[188:191], a[228:231], v[220:223]\n"
"s_waitcnt vmcnt(36)\n"
"ds_read_b64_tr_b16 a[72:73], v36 offset:18432\n"
"ds_read_b64_tr_b16 a[74:75], v36 offset:18688\n"
"ds_read_b64_tr_b16 a[104:105], v37 offset:18432\n"
"ds_read_b64_tr_b16 a[106:107], v37 offset:18688\n"
"s_waitcnt lgkmcnt(4)\n"
"s_add_u32 m0, s55, 16384\n"
"s_add_u32 s60, s66, 229376\n"
"buffer_load_dwordx4 v42, s[44:47], s60 offen lds\n"
"s_add_u32 m0, s55, 17408\n"
"s_add_u32 s60, s66, 344064\n"
"buffer_load_dwordx4 v42, s[44:47], s60 offen lds\n"
"v_mfma_f32_16x16x32_bf16 v[192:195], v[188:191], a[8:11], v[192:195]\n"
"v_mfma_f32_16x16x32_bf16 v[196:199], v[188:191], a[40:43], v[196:199]\n"
"s_waitcnt vmcnt(36)\n"
"ds_read_b64_tr_b16 a[136:137], v36 offset:20480\n"
"ds_read_b64_tr_b16 a[138:139], v36 offset:20736\n"
"ds_read_b64_tr_b16 a[168:169], v37 offset:20480\n"
"ds_read_b64_tr_b16 a[170:171], v37 offset:20736\n"
"s_waitcnt lgkmcnt(4)\n"
"s_add_u32 m0, s55, 18432\n"
"s_add_u32 s60, s67, 229376\n"
"buffer_load_dwordx4 v42, s[44:47], s60 offen lds\n"
"s_add_u32 m0, s55, 19456\n"
"s_add_u32 s60, s67, 344064\n"
"buffer_load_dwordx4 v42, s[44:47], s60 offen lds\n"
"v_mfma_f32_16x16x32_bf16 v[200:203], v[188:191], a[72:75], v[200:203]\n"
"v_mfma_f32_16x16x32_bf16 v[204:207], v[188:191], a[104:107], v[204:207]\n"
"s_waitcnt vmcnt(36)\n"
"ds_read_b64_tr_b16 a[200:201], v36 offset:22528\n"
"ds_read_b64_tr_b16 a[202:203], v36 offset:22784\n"
"ds_read_b64_tr_b16 a[232:233], v37 offset:22528\n"
"ds_read_b64_tr_b16 a[234:235], v37 offset:22784\n"
"s_waitcnt lgkmcnt(4)\n"
"s_add_u32 m0, s55, 20480\n"
"s_add_u32 s60, s68, 229376\n"
"buffer_load_dwordx4 v42, s[44:47], s60 offen lds\n"
"s_add_u32 m0, s55, 21504\n"
"s_add_u32 s60, s68, 344064\n"
"buffer_load_dwordx4 v42, s[44:47], s60 offen lds\n"
"v_mfma_f32_16x16x32_bf16 v[208:211], v[188:191], a[136:139], v[208:211]\n"
"v_mfma_f32_16x16x32_bf16 v[212:215], v[188:191], a[168:171], v[212:215]\n"
"s_waitcnt vmcnt(36)\n"
"ds_read_b64_tr_b16 a[12:13], v36 offset:24576\n"
"ds_read_b64_tr_b16 a[14:15], v36 offset:24832\n"
"ds_read_b64_tr_b16 a[44:45], v37 offset:24576\n"
"ds_read_b64_tr_b16 a[46:47], v37 offset:24832\n"
"s_waitcnt lgkmcnt(4)\n"
"s_add_u32 m0, s55, 22528\n"
"s_add_u32 s60, s69, 229376\n"
"buffer_load_dwordx4 v42, s[44:47], s60 offen lds\n"
"s_add_u32 m0, s55, 23552\n"
"s_add_u32 s60, s69, 344064\n"
"buffer_load_dwordx4 v42, s[44:47], s60 offen lds\n"
"v_mfma_f32_16x16x32_bf16 v[216:219], v[188:191], a[200:203], v[216:219]\n"
"v_mfma_f32_16x16x32_bf16 v[220:223], v[188:191], a[232:235], v[220:223]\n"
"s_waitcnt vmcnt(36)\n"
"ds_read_b64_tr_b16 a[76:77], v36 offset:26624\n"
"ds_read_b64_tr_b16 a[78:79], v36 offset:26880\n"
"ds_read_b64_tr_b16 a[108:109], v37 offset:26624\n"
"ds_read_b64_tr_b16 a[110:111], v37 offset:26880\n"
"s_waitcnt lgkmcnt(4)\n"
"v_mfma_f32_16x16x32_bf16 v[192:195], v[188:191], a[12:15], v[192:195]\n"
"v_mfma_f32_16x16x32_bf16 v[196:199], v[188:191], a[44:47], v[196:199]\n"
"s_waitcnt vmcnt(34)\n"
"ds_read_b64_tr_b16 a[140:141], v36 offset:28672\n"
"ds_read_b64_tr_b16 a[142:143], v36 offset:28928\n"
"ds_read_b64_tr_b16 a[172:173], v37 offset:28672\n"
"ds_read_b64_tr_b16 a[174:175], v37 offset:28928\n"
"s_waitcnt lgkmcnt(4)\n"
"v_mfma_f32_16x16x32_bf16 v[200:203], v[188:191], a[76:79], v[200:203]\n"
"v_mfma_f32_16x16x32_bf16 v[204:207], v[188:191], a[108:111], v[204:207]\n"
"s_waitcnt vmcnt(32)\n"
"ds_read_b64_tr_b16 a[204:205], v36 offset:30720\n"
"ds_read_b64_tr_b16 a[206:207], v36 offset:30976\n"
"ds_read_b64_tr_b16 a[236:237], v37 offset:30720\n"
"ds_read_b64_tr_b16 a[238:239], v37 offset:30976\n"
"s_waitcnt lgkmcnt(4)\n"
"v_mfma_f32_16x16x32_bf16 v[208:211], v[188:191], a[140:143], v[208:211]\n"
"v_mfma_f32_16x16x32_bf16 v[212:215], v[188:191], a[172:175], v[212:215]\n"
"s_waitcnt vmcnt(30)\n"
"ds_read_b64_tr_b16 a[16:17], v36 offset:32768\n"
"ds_read_b64_tr_b16 a[18:19], v36 offset:33024\n"
"ds_read_b64_tr_b16 a[48:49], v37 offset:32768\n"
"ds_read_b64_tr_b16 a[50:51], v37 offset:33024\n"
"s_waitcnt lgkmcnt(4)\n"
"v_mfma_f32_16x16x32_bf16 v[216:219], v[188:191], a[204:207], v[216:219]\n"
"v_mfma_f32_16x16x32_bf16 v[220:223], v[188:191], a[236:239], v[220:223]\n"
"s_waitcnt vmcnt(28)\n"
"ds_read_b64_tr_b16 a[80:81], v36 offset:34816\n"
"ds_read_b64_tr_b16 a[82:83], v36 offset:35072\n"
"ds_read_b64_tr_b16 a[112:113], v37 offset:34816\n"
"ds_read_b64_tr_b16 a[114:115], v37 offset:35072\n"
"s_waitcnt lgkmcnt(4)\n"
"v_mfma_f32_16x16x32_bf16 v[192:195], v[188:191], a[16:19], v[192:195]\n"
"v_mfma_f32_16x16x32_bf16 v[196:199], v[188:191], a[48:51], v[196:199]\n"
"s_waitcnt vmcnt(26)\n"
"ds_read_b64_tr_b16 a[144:145], v36 offset:36864\n"
"ds_read_b64_tr_b16 a[146:147], v36 offset:37120\n"
"ds_read_b64_tr_b16 a[176:177], v37 offset:36864\n"
"ds_read_b64_tr_b16 a[178:179], v37 offset:37120\n"
"s_waitcnt lgkmcnt(4)\n"
"v_mfma_f32_16x16x32_bf16 v[200:203], v[188:191], a[80:83], v[200:203]\n"
"v_mfma_f32_16x16x32_bf16 v[204:207], v[188:191], a[112:115], v[204:207]\n"
"s_waitcnt vmcnt(24)\n"
"ds_read_b64_tr_b16 a[208:209], v36 offset:38912\n"
"ds_read_b64_tr_b16 a[210:211], v36 offset:39168\n"
"ds_read_b64_tr_b16 a[240:241], v37 offset:38912\n"
"ds_read_b64_tr_b16 a[242:243], v37 offset:39168\n"
"s_waitcnt lgkmcnt(4)\n"
"v_mfma_f32_16x16x32_bf16 v[208:211], v[188:191], a[144:147], v[208:211]\n"
"v_mfma_f32_16x16x32_bf16 v[212:215], v[188:191], a[176:179], v[212:215]\n"
"s_waitcnt vmcnt(22)\n"
"ds_read_b64_tr_b16 a[20:21], v36 offset:0\n"
"ds_read_b64_tr_b16 a[22:23], v36 offset:256\n"
"ds_read_b64_tr_b16 a[52:53], v37 offset:0\n"
"ds_read_b64_tr_b16 a[54:55], v37 offset:256\n"
"s_waitcnt lgkmcnt(4)\n"
"v_mfma_f32_16x16x32_bf16 v[216:219], v[188:191], a[208:211], v[216:219]\n"
"v_mfma_f32_16x16x32_bf16 v[220:223], v[188:191], a[240:243], v[220:223]\n"
"s_waitcnt vmcnt(20)\n"
"ds_read_b64_tr_b16 a[84:85], v36 offset:2048\n"
"ds_read_b64_tr_b16 a[86:87], v36 offset:2304\n"
"ds_read_b64_tr_b16 a[116:117], v37 offset:2048\n"
"ds_read_b64_tr_b16 a[118:119], v37 offset:2304\n"
"s_waitcnt lgkmcnt(4)\n"
"v_mfma_f32_16x16x32_bf16 v[192:195], v[188:191], a[20:23], v[192:195]\n"
"v_mfma_f32_16x16x32_bf16 v[196:199], v[188:191], a[52:55], v[196:199]\n"
"s_waitcnt vmcnt(18)\n"
"ds_read_b64_tr_b16 a[148:149], v36 offset:4096\n"
"ds_read_b64_tr_b16 a[150:151], v36 offset:4352\n"
"ds_read_b64_tr_b16 a[180:181], v37 offset:4096\n"
"ds_read_b64_tr_b16 a[182:183], v37 offset:4352\n"
"s_waitcnt lgkmcnt(4)\n"
"v_mfma_f32_16x16x32_bf16 v[200:203], v[188:191], a[84:87], v[200:203]\n"
"v_mfma_f32_16x16x32_bf16 v[204:207], v[188:191], a[116:119], v[204:207]\n"
"s_waitcnt vmcnt(16)\n"
"ds_read_b64_tr_b16 a[212:213], v36 offset:6144\n"
"ds_read_b64_tr_b16 a[214:215], v36 offset:6400\n"
"ds_read_b64_tr_b16 a[244:245], v37 offset:6144\n"
"ds_read_b64_tr_b16 a[246:247], v37 offset:6400\n"
"s_waitcnt lgkmcnt(4)\n"
"v_mfma_f32_16x16x32_bf16 v[208:211], v[188:191], a[148:151], v[208:211]\n"
"v_mfma_f32_16x16x32_bf16 v[212:215], v[188:191], a[180:183], v[212:215]\n"
"s_waitcnt vmcnt(14)\n"
"ds_read_b64_tr_b16 a[24:25], v36 offset:8192\n"
"ds_read_b64_tr_b16 a[26:27], v36 offset:8448\n"
"ds_read_b64_tr_b16 a[56:57], v37 offset:8192\n"
"ds_read_b64_tr_b16 a[58:59], v37 offset:8448\n"
"s_waitcnt lgkmcnt(4)\n"
"v_mfma_f32_16x16x32_bf16 v[216:219], v[188:191], a[212:215], v[216:219]\n"
"v_mfma_f32_16x16x32_bf16 v[220:223], v[188:191], a[244:247], v[220:223]\n"
"s_waitcnt vmcnt(12)\n"
"ds_read_b64_tr_b16 a[88:89], v36 offset:10240\n"
"ds_read_b64_tr_b16 a[90:91], v36 offset:10496\n"
"ds_read_b64_tr_b16 a[120:121], v37 offset:10240\n"
"ds_read_b64_tr_b16 a[122:123], v37 offset:10496\n"
"s_waitcnt lgkmcnt(4)\n"
"v_mfma_f32_16x16x32_bf16 v[192:195], v[188:191], a[24:27], v[192:195]\n"
"v_mfma_f32_16x16x32_bf16 v[196:199], v[188:191], a[56:59], v[196:199]\n"
"s_waitcnt vmcnt(10)\n"
"ds_read_b64_tr_b16 a[152:153], v36 offset:12288\n"
"ds_read_b64_tr_b16 a[154:155], v36 offset:12544\n"
"ds_read_b64_tr_b16 a[184:185], v37 offset:12288\n"
"ds_read_b64_tr_b16 a[186:187], v37 offset:12544\n"
"s_waitcnt lgkmcnt(4)\n"
"v_mfma_f32_16x16x32_bf16 v[200:203], v[188:191], a[88:91], v[200:203]\n"
"v_mfma_f32_16x16x32_bf16 v[204:207], v[188:191], a[120:123], v[204:207]\n"
"s_waitcnt vmcnt(8)\n"
"ds_read_b64_tr_b16 a[216:217], v36 offset:14336\n"
"ds_read_b64_tr_b16 a[218:219], v36 offset:14592\n"
"ds_read_b64_tr_b16 a[248:249], v37 offset:14336\n"
"ds_read_b64_tr_b16 a[250:251], v37 offset:14592\n"
"s_waitcnt lgkmcnt(4)\n"
"v_mfma_f32_16x16x32_bf16 v[208:211], v[188:191], a[152:155], v[208:211]\n"
"v_mfma_f32_16x16x32_bf16 v[212:215], v[188:191], a[184:187], v[212:215]\n"
"s_waitcnt vmcnt(6)\n"
"ds_read_b64_tr_b16 a[28:29], v36 offset:16384\n"
"ds_read_b64_tr_b16 a[30:31], v36 offset:16640\n"
"ds_read_b64_tr_b16 a[60:61], v37 offset:16384\n"
"ds_read_b64_tr_b16 a[62:63], v37 offset:16640\n"
"s_waitcnt lgkmcnt(4)\n"
"v_mfma_f32_16x16x32_bf16 v[216:219], v[188:191], a[216:219], v[216:219]\n"
"v_mfma_f32_16x16x32_bf16 v[220:223], v[188:191], a[248:251], v[220:223]\n"
"s_waitcnt vmcnt(4)\n"
"ds_read_b64_tr_b16 a[92:93], v36 offset:18432\n"
"ds_read_b64_tr_b16 a[94:95], v36 offset:18688\n"
"ds_read_b64_tr_b16 a[124:125], v37 offset:18432\n"
"ds_read_b64_tr_b16 a[126:127], v37 offset:18688\n"
"s_waitcnt lgkmcnt(4)\n"
"v_mfma_f32_16x16x32_bf16 v[192:195], v[188:191], a[28:31], v[192:195]\n"
"v_mfma_f32_16x16x32_bf16 v[196:199], v[188:191], a[60:63], v[196:199]\n"
"s_waitcnt vmcnt(2)\n"
"ds_read_b64_tr_b16 a[156:157], v36 offset:20480\n"
"ds_read_b64_tr_b16 a[158:159], v36 offset:20736\n"
"ds_read_b64_tr_b16 a[188:189], v37 offset:20480\n"
"ds_read_b64_tr_b16 a[190:191], v37 offset:20736\n"
"s_waitcnt lgkmcnt(4)\n"
"v_mfma_f32_16x16x32_bf16 v[200:203], v[188:191], a[92:95], v[200:203]\n"
"v_mfma_f32_16x16x32_bf16 v[204:207], v[188:191], a[124:127], v[204:207]\n"
"s_waitcnt vmcnt(0)\n"
"ds_read_b64_tr_b16 a[220:221], v36 offset:22528\n"
"ds_read_b64_tr_b16 a[222:223], v36 offset:22784\n"
"ds_read_b64_tr_b16 a[252:253], v37 offset:22528\n"
"ds_read_b64_tr_b16 a[254:255], v37 offset:22784\n"
"s_waitcnt lgkmcnt(4)\n"
"v_mfma_f32_16x16x32_bf16 v[208:211], v[188:191], a[156:159], v[208:211]\n"
"v_mfma_f32_16x16x32_bf16 v[212:215], v[188:191], a[188:191], v[212:215]\n"
"s_waitcnt lgkmcnt(0)\n"
"v_mfma_f32_16x16x32_bf16 v[216:219], v[188:191], a[220:223], v[216:219]\n"
"v_mfma_f32_16x16x32_bf16 v[220:223], v[188:191], a[252:255], v[220:223]\n"
"s_mov_b32 m0, s59\n"
"s_barrier\n"
"ds_write_addtid_b32 v192 offset:0\n"
"ds_write_addtid_b32 v193 offset:4640\n"
"ds_write_addtid_b32 v194 offset:9344\n"
"ds_write_addtid_b32 v195 offset:13984\n"
"ds_write_addtid_b32 v196 offset:256\n"
"ds_write_addtid_b32 v197 offset:4896\n"
"ds_write_addtid_b32 v198 offset:9600\n"
"ds_write_addtid_b32 v199 offset:14240\n"
"ds_write_addtid_b32 v200 offset:512\n"
"ds_write_addtid_b32 v201 offset:5152\n"
"ds_write_addtid_b32 v202 offset:9856\n"
"ds_write_addtid_b32 v203 offset:14496\n"
"ds_write_addtid_b32 v204 offset:768\n"
"ds_write_addtid_b32 v205 offset:5408\n"
"ds_write_addtid_b32 v206 offset:10112\n"
"ds_write_addtid_b32 v207 offset:14752\n"
"ds_write_addtid_b32 v208 offset:1024\n"
"ds_write_addtid_b32 v209 offset:5664\n"
"ds_write_addtid_b32 v210 offset:10368\n"
"ds_write_addtid_b32 v211 offset:15008\n"
"ds_write_addtid_b32 v212 offset:1280\n"
"ds_write_addtid_b32 v213 offset:5920\n"
"ds_write_addtid_b32 v214 offset:10624\n"
"ds_write_addtid_b32 v215 offset:15264\n"
"ds_write_addtid_b32 v216 offset:1536\n"
"ds_write_addtid_b32 v217 offset:6176\n"
"ds_write_addtid_b32 v218 offset:10880\n"
"ds_write_addtid_b32 v219 offset:15520\n"
"ds_write_addtid_b32 v220 offset:1792\n"
"ds_write_addtid_b32 v221 offset:6432\n"
"ds_write_addtid_b32 v222 offset:11136\n"
"ds_write_addtid_b32 v223 offset:15776\n"
"s_waitcnt lgkmcnt(0)\n"
"s_barrier\n"
"ds_read_b128 v[96:99], v2 offset:0\n"
"ds_read_b128 v[100:103], v3 offset:512\n"
"ds_read_b128 v[104:107], v4 offset:1024\n"
"ds_read_b128 v[108:111], v43 offset:1536\n"
"ds_read_b128 v[112:115], v2 offset:256\n"
"ds_read_b128 v[116:119], v3 offset:768\n"
"ds_read_b128 v[120:123], v4 offset:1280\n"
"ds_read_b128 v[124:127], v43 offset:1792\n"
"s_waitcnt lgkmcnt(0)\n"
"s_barrier\n"
"v_add_f32 v96, v96, v100\n"
"v_add_f32 v104, v104, v108\n"
"v_add_f32 v18, v96, v104\n"
"v_add_f32 v97, v97, v101\n"
"v_add_f32 v105, v105, v109\n"
"v_add_f32 v19, v97, v105\n"
"v_add_f32 v98, v98, v102\n"
"v_add_f32 v106, v106, v110\n"
"v_add_f32 v20, v98, v106\n"
"v_add_f32 v99, v99, v103\n"
"v_add_f32 v107, v107, v111\n"
"v_add_f32 v21, v99, v107\n"
"v_add_f32 v112, v112, v116\n"
"v_add_f32 v120, v120, v124\n"
"v_add_f32 v22, v112, v120\n"
"v_add_f32 v113, v113, v117\n"
"v_add_f32 v121, v121, v125\n"
"v_add_f32 v23, v113, v121\n"
"v_add_f32 v114, v114, v118\n"
"v_add_f32 v122, v122, v126\n"
"v_add_f32 v24, v114, v122\n"
"v_add_f32 v115, v115, v119\n"
"v_add_f32 v123, v123, v127\n"
"v_add_f32 v25, v115, v123\n"
"v_lshlrev_b32 v26, 16, v30\n"
"v_and_b32 v27, 0xffff0000, v30\n"
"v_lshlrev_b32 v28, 16, v31\n"
"v_and_b32 v29, 0xffff0000, v31\n"
"v_lshlrev_b32 v30, 16, v32\n"
"v_and_b32 v31, 0xffff0000, v32\n"
"v_lshlrev_b32 v32, 16, v33\n"
"v_and_b32 v33, 0xffff0000, v33\n"
"s_mov_b32 s93, 0x80000000\n"
"s_mov_b32 s95, 0x80000000\n"
"s_mov_b32 s95, 0x80000000\n"
"s_mov_b32 s94, 0x80000000\n"
"s_mov_b32 s93, 0x80000000\n"
"s_mov_b32 s94, 0x80000000\n"
"s_mov_b32 s94, 0x80000000\n"
"s_mov_b32 s95, 0x80000000\n"
"v_mov_b32 v134, 0\n"
"v_mov_b32 v135, 0\n"
"v_mov_b32 v154, 0\n"
"v_mov_b32 v155, 0\n"
"v_mov_b32 v144, 0\n"
"v_mov_b32 v145, 0\n"
"v_mov_b32 v138, 0\n"
"v_mov_b32 v139, 0\n"
"s_cmp_eq_u32 s54, 0\n"
"s_cbranch_scc1 .LF_1s\n"
"s_cmp_eq_u32 s54, 1\n"
"s_cbranch_scc1 .LF_2s\n"
"s_cmp_eq_u32 s54, 2\n"
"s_cbranch_scc1 .LF_3s\n"
"s_cmp_eq_u32 s54, 3\n"
"s_cbranch_scc1 .LF_4s\n"
".p2align 6\n"
".LF_top:\n"
"v_mfma_f32_16x16x32_bf16 v[48:51], v[100:103], a[0:3], 0\n"
"global_load_dwordx4 v[36:39], v6, s[56:57] offset:1024 sc1\n"
"v_mul_f32 v125, v125, v124\n"
"v_mfma_f32_16x16x32_bf16 v[48:51], v[104:107], a[4:7], v[48:51]\n"
"s_waitcnt vmcnt(10)\n"
"ds_write_addtid_b32 v124 offset:18592\n"
"ds_write_addtid_b32 v125 offset:18848\n"
"v_mfma_f32_16x16x32_bf16 v[48:51], v[108:111], a[8:11], v[48:51]\n"
"v_cvt_pk_bf16_f32 v139, v142, v143\n"
"v_pk_max_i16 v138, v138, 0\n"
"v_mfma_f32_16x16x32_bf16 v[48:51], v[64:67], a[12:15], v[48:51]\n"
"s_waitcnt vmcnt(8)\n"
"ds_read2st64_b32 v[140:141], v14 offset0:0 offset1:1\n"
"v_pk_max_i16 v139, v139, 0\n"
"v_mfma_f32_16x16x32_bf16 v[48:51], v[68:71], a[16:19], v[48:51]\n"
"ds_read_b128 v[56:59], v2 offset:0\n"
"ds_read_b128 v[52:55], v3 offset:2048\n"
"v_mfma_f32_16x16x32_bf16 v[48:51], v[72:75], a[20:23], v[48:51]\n"
"s_waitcnt vmcnt(6)\n"
"global_load_dwordx4 v[40:43], v7, s[56:57] offset:0 sc1\n"
"ds_read_b128 v[60:63], v4 offset:1024\n"
"v_mfma_f32_16x16x32_bf16 v[48:51], v[76:79], a[24:27], v[48:51]\n"
"ds_read_b128 v[84:87], v2 offset:256\n"
"ds_read_b128 v[88:91], v3 offset:2304\n"
"v_mfma_f32_16x16x32_bf16 v[48:51], v[80:83], a[28:31], v[48:51]\n"
"ds_read_b128 v[92:95], v4 offset:1280\n"
"v_mfma_f32_16x16x32_bf16 v[96:99], v[100:103], a[32:35], 0\n"
"v_mfma_f32_16x16x32_bf16 v[96:99], v[104:107], a[36:39], v[96:99]\n"
"ds_write_addtid_b32 v48 offset:0\n"
"v_mfma_f32_16x16x32_bf16 v[96:99], v[108:111], a[40:43], v[96:99]\n"
"ds_write_addtid_b32 v49 offset:4640\n"
"global_load_dwordx4 v[44:47], v7, s[56:57] offset:1024 sc1\n"
"v_mfma_f32_16x16x32_bf16 v[96:99], v[64:67], a[44:47], v[96:99]\n"
"s_waitcnt lgkmcnt(5)\n"
"buffer_store_dwordx4 v[136:139], v15, s[40:43], s94 offen sc1\n"
"ds_write_addtid_b32 v50 offset:9344\n"
"v_add_f32 v56, v56, v52\n"
"v_add_f32 v57, v57, v53\n"
"v_mfma_f32_16x16x32_bf16 v[96:99], v[68:71], a[48:51], v[96:99]\n"
"ds_write_addtid_b32 v51 offset:13984\n"
"v_add_f32 v58, v58, v54\n"
"v_mfma_f32_16x16x32_bf16 v[96:99], v[72:75], a[52:55], v[96:99]\n"
"v_add_f32 v59, v59, v55\n"
"v_add_f32 v56, v56, v60\n"
"v_mfma_f32_16x16x32_bf16 v[96:99], v[76:79], a[56:59], v[96:99]\n"
"v_add_f32 v57, v57, v61\n"
"v_add_f32 v58, v58, v62\n"
"v_mfma_f32_16x16x32_bf16 v[96:99], v[80:83], a[60:63], v[96:99]\n"
"v_add_f32 v59, v59, v63\n"
"v_add_f32 v56, v56, v128\n"
"v_mfma_f32_16x16x32_bf16 v[124:127], v[100:103], a[64:67], 0\n"
"global_load_dwordx4 v[48:51], v8, s[56:57] offset:0 sc1\n"
"v_add_f32 v57, v57, v129\n"
"v_mfma_f32_16x16x32_bf16 v[124:127], v[104:107], a[68:71], v[124:127]\n"
"ds_write_addtid_b32 v96 offset:256\n"
"v_add_f32 v58, v58, v130\n"
"v_mfma_f32_16x16x32_bf16 v[124:127], v[108:111], a[72:75], v[124:127]\n"
"ds_write_addtid_b32 v97 offset:4896\n"
"v_add_f32 v59, v59, v131\n"
"v_mfma_f32_16x16x32_bf16 v[124:127], v[64:67], a[76:79], v[124:127]\n"
"s_add_u32 s94, s50, 0xffd00000\n"
"s_and_b32 s95, s94, 0xfbffffff\n"
"ds_write_addtid_b32 v98 offset:9600\n"
"v_mfma_f32_16x16x32_bf16 v[124:127], v[68:71], a[80:83], v[124:127]\n"
"ds_write_addtid_b32 v99 offset:14240\n"
"global_load_dwordx4 v[52:55], v8, s[56:57] offset:1024 sc1\n"
"v_mfma_f32_16x16x32_bf16 v[124:127], v[72:75], a[84:87], v[124:127]\n"
"v_fma_f32 v60, v18, v141, v26\n"
"v_fma_f32 v61, v19, v141, v27\n"
"v_mfma_f32_16x16x32_bf16 v[124:127], v[76:79], a[88:91], v[124:127]\n"
"v_fma_f32 v62, v20, v141, v28\n"
"v_fma_f32 v63, v21, v141, v29\n"
"v_mfma_f32_16x16x32_bf16 v[124:127], v[80:83], a[92:95], v[124:127]\n"
"v_fma_f32 v56, v56, v140, v60\n"
"v_fma_f32 v57, v57, v140, v61\n"
"v_mfma_f32_16x16x32_bf16 v[96:99], v[100:103], a[96:99], 0\n"
"v_fma_f32 v58, v58, v140, v62\n"
"v_fma_f32 v59, v59, v140, v63\n"
"v_mfma_f32_16x16x32_bf16 v[96:99], v[104:107], a[100:103], v[96:99]\n"
"ds_write_addtid_b32 v124 offset:512\n"
"v_mul_f32_e64 v56, v56, |v56|\n"
"v_mfma_f32_16x16x32_bf16 v[96:99], v[108:111], a[104:107], v[96:99]\n"
"ds_write_addtid_b32 v125 offset:5152\n"
"v_mul_f32_e64 v57, v57, |v57|\n"
"v_mfma_f32_16x16x32_bf16 v[96:99], v[64:67], a[108:111], v[96:99]\n"
"ds_write_addtid_b32 v126 offset:9856\n"
"v_mul_f32_e64 v58, v58, |v58|\n"
"v_mfma_f32_16x16x32_bf16 v[96:99], v[68:71], a[112:115], v[96:99]\n"
"ds_write_addtid_b32 v127 offset:14496\n"
"v_mul_f32_e64 v59, v59, |v59|\n"
"v_mfma_f32_16x16x32_bf16 v[96:99], v[72:75], a[116:119], v[96:99]\n"
"v_cvt_pk_bf16_f32 v128, v56, v57\n"
"v_cvt_pk_bf16_f32 v129, v58, v59\n"
"v_mfma_f32_16x16x32_bf16 v[96:99], v[76:79], a[120:123], v[96:99]\n"
"v_pk_max_i16 v128, v128, 0\n"
"v_pk_max_i16 v129, v129, 0\n"
"v_mfma_f32_16x16x32_bf16 v[96:99], v[80:83], a[124:127], v[96:99]\n"
"s_waitcnt lgkmcnt(12)\n"
"v_add_f32 v84, v84, v88\n"
"v_add_f32 v85, v85, v89\n"
"v_mfma_f32_16x16x32_bf16 v[56:59], v[100:103], a[128:131], 0\n"
"v_add_f32 v86, v86, v90\n"
"v_add_f32 v87, v87, v91\n"
"v_mfma_f32_16x16x32_bf16 v[56:59], v[104:107], a[132:135], v[56:59]\n"
"ds_write_addtid_b32 v96 offset:768\n"
"v_add_f32 v84, v84, v92\n"
"v_mfma_f32_16x16x32_bf16 v[56:59], v[108:111], a[136:139], v[56:59]\n"
"ds_write_addtid_b32 v97 offset:5408\n"
"v_add_f32 v85, v85, v93\n"
"v_mfma_f32_16x16x32_bf16 v[56:59], v[64:67], a[140:143], v[56:59]\n"
"ds_write_addtid_b32 v98 offset:10112\n"
"v_add_f32 v86, v86, v94\n"
"v_mfma_f32_16x16x32_bf16 v[56:59], v[68:71], a[144:147], v[56:59]\n"
"ds_write_addtid_b32 v99 offset:14752\n"
"v_add_f32 v87, v87, v95\n"
"v_mfma_f32_16x16x32_bf16 v[56:59], v[72:75], a[148:151], v[56:59]\n"
"v_add_f32 v84, v84, v132\n"
"v_add_f32 v85, v85, v133\n"
"v_mfma_f32_16x16x32_bf16 v[56:59], v[76:79], a[152:155], v[56:59]\n"
"v_add_f32 v86, v86, v134\n"
"v_add_f32 v87, v87, v135\n"
"v_mfma_f32_16x16x32_bf16 v[56:59], v[80:83], a[156:159], v[56:59]\n"
"v_fma_f32 v60, v22, v141, v30\n"
"v_fma_f32 v61, v23, v141, v31\n"
"v_mfma_f32_16x16x32_bf16 v[88:91], v[100:103], a[160:163], 0\n"
"v_fma_f32 v62, v24, v141, v32\n"
"v_fma_f32 v63, v25, v141, v33\n"
"v_mfma_f32_16x16x32_bf16 v[88:91], v[104:107], a[164:167], v[88:91]\n"
"ds_write_addtid_b32 v56 offset:1024\n"
"v_fma_f32 v84, v84, v140, v60\n"
"v_mfma_f32_16x16x32_bf16 v[88:91], v[108:111], a[168:171], v[88:91]\n"
"s_add_u32 s58, s50, 0x180000\n"
"ds_write_addtid_b32 v57 offset:5664\n"
"v_fma_f32 v85, v85, v140, v61\n"
"v_mfma_f32_16x16x32_bf16 v[88:91], v[64:67], a[172:175], v[88:91]\n"
"s_and_b32 s58, s58, 0x3ffffff\n"
"ds_write_addtid_b32 v58 offset:10368\n"
"v_fma_f32 v86, v86, v140, v62\n"
"v_mfma_f32_16x16x32_bf16 v[88:91], v[68:71], a[176:179], v[88:91]\n"
"s_add_u32 s56, s40, s58\n"
"ds_write_addtid_b32 v59 offset:15008\n"
"v_fma_f32 v87, v87, v140, v63\n"
"v_mfma_f32_16x16x32_bf16 v[88:91], v[72:75], a[180:183], v[88:91]\n"
"s_addc_u32 s57, s41, 0\n"
"v_mul_f32_e64 v84, v84, |v84|\n"
"v_mul_f32_e64 v85, v85, |v85|\n"
"v_mfma_f32_16x16x32_bf16 v[88:91], v[76:79], a[184:187], v[88:91]\n"
"s_waitcnt vmcnt(6)\n"
"v_cmp_ne_u64 vcc, s[48:49], v[144:145]\n"
"v_mul_f32_e64 v86, v86, |v86|\n"
"v_mfma_f32_16x16x32_bf16 v[88:91], v[80:83], a[188:191], v[88:91]\n"
"v_mul_f32_e64 v87, v87, |v87|\n"
"v_cvt_pk_bf16_f32 v130, v84, v85\n"
"v_mfma_f32_16x16x32_bf16 v[92:95], a[192:195], v[100:103], 0\n"
"s_cbranch_vccnz .LF_3s\n"
".LF_3r:\n"
"v_cvt_pk_bf16_f32 v131, v86, v87\n"
"v_mfma_f32_16x16x32_bf16 v[92:95], a[196:199], v[104:107], v[92:95]\n"
"s_add_u32 s50, s50, 0x180000\n"
"ds_write_addtid_b32 v88 offset:1280\n"
"global_load_dwordx4 v[56:59], v5, s[56:57] offset:0 sc1\n"
"v_mfma_f32_16x16x32_bf16 v[92:95], a[200:203], v[108:111], v[92:95]\n"
"ds_write_addtid_b32 v89 offset:5920\n"
"v_pk_max_i16 v130, v130, 0\n"
"v_mfma_f32_16x16x32_bf16 v[92:95], a[204:207], v[64:67], v[92:95]\n"
"ds_write_addtid_b32 v90 offset:10624\n"
"v_pk_max_i16 v131, v131, 0\n"
"v_mfma_f32_16x16x32_bf16 v[92:95], a[208:211], v[68:71], v[92:95]\n"
"ds_write_addtid_b32 v91 offset:15264\n"
"v_mfma_f32_16x16x32_bf16 v[92:95], a[212:215], v[72:75], v[92:95]\n"
"v_mfma_f32_16x16x32_bf16 v[92:95], a[216:219], v[76:79], v[92:95]\n"
"v_mfma_f32_16x16x32_bf16 v[92:95], a[220:223], v[80:83], v[92:95]\n"
"v_mfma_f32_16x16x32_bf16 v[88:91], a[224:227], v[100:103], 0\n"
"s_waitcnt lgkmcnt(0)\n"
"global_load_dwordx4 v[60:63], v5, s[56:57] offset:1024 sc1\n"
"v_mfma_f32_16x16x32_bf16 v[88:91], a[228:231], v[104:107], v[88:91]\n"
"s_barrier\n"
"v_mfma_f32_16x16x32_bf16 v[88:91], a[232:235], v[108:111], v[88:91]\n"
"ds_read2st64_b32 v[136:137], v11 offset0:0 offset1:1\n"
"ds_read_b128 v[96:99], v2 offset:1536\n"
"v_mfma_f32_16x16x32_bf16 v[88:91], a[236:239], v[64:67], v[88:91]\n"
"s_waitcnt vmcnt(4)\n"
"buffer_load_dwordx2 v[138:139], v9, s[40:43], s50 offen sc1\n"
"v_mfma_f32_16x16x32_bf16 v[88:91], a[240:243], v[68:71], v[88:91]\n"
"global_load_dwordx4 v[64:67], v6, s[56:57] offset:0 sc1\n"
"ds_read_b128 v[84:87], v3 offset:512\n"
"v_mfma_f32_16x16x32_bf16 v[88:91], a[244:247], v[72:75], v[88:91]\n"
"ds_read_b128 v[100:103], v4 offset:2560\n"
"ds_read_b128 v[104:107], v2 offset:1792\n"
"v_mfma_f32_16x16x32_bf16 v[88:91], a[248:251], v[76:79], v[88:91]\n"
"ds_read_b128 v[108:111], v3 offset:768\n"
"ds_read_b128 v[124:127], v4 offset:2816\n"
"v_mfma_f32_16x16x32_bf16 v[88:91], a[252:255], v[80:83], v[88:91]\n"
"buffer_store_dwordx2 v[0:1], v16, s[40:43], s93 offen offset:0 sc1\n"
"v_mfma_f32_16x16x32_bf16 v[132:135], v[112:115], a[0:3], 0\n"
"buffer_store_dwordx4 v[128:131], v15, s[40:43], s95 offen sc1\n"
"v_mfma_f32_16x16x32_bf16 v[132:135], v[116:119], a[4:7], v[132:135]\n"
"v_mfma_f32_16x16x32_bf16 v[132:135], v[120:123], a[8:11], v[132:135]\n"
"global_load_dwordx4 v[68:71], v6, s[56:57] offset:1024 sc1\n"
"v_mfma_f32_16x16x32_bf16 v[132:135], v[36:39], a[12:15], v[132:135]\n"
"v_mfma_f32_16x16x32_bf16 v[132:135], v[40:43], a[16:19], v[132:135]\n"
"s_waitcnt lgkmcnt(3)\n"
"v_add_f32 v96, v96, v84\n"
"v_add_f32 v97, v97, v85\n"
"v_mfma_f32_16x16x32_bf16 v[132:135], v[44:47], a[20:23], v[132:135]\n"
"s_waitcnt vmcnt(7)\n"
"global_load_dwordx4 v[72:75], v7, s[56:57] offset:0 sc1\n"
"v_add_f32 v98, v98, v86\n"
"v_mfma_f32_16x16x32_bf16 v[132:135], v[48:51], a[24:27], v[132:135]\n"
"v_add_f32 v99, v99, v87\n"
"v_add_f32 v96, v96, v100\n"
"v_mfma_f32_16x16x32_bf16 v[132:135], v[52:55], a[28:31], v[132:135]\n"
"global_load_dwordx4 v[76:79], v7, s[56:57] offset:1024 sc1\n"
"v_add_f32 v97, v97, v101\n"
"v_mfma_f32_16x16x32_bf16 v[128:131], v[112:115], a[32:35], 0\n"
"v_add_f32 v98, v98, v102\n"
"v_add_f32 v99, v99, v103\n"
"v_mfma_f32_16x16x32_bf16 v[128:131], v[116:119], a[36:39], v[128:131]\n"
"ds_write_addtid_b32 v132 offset:1536\n"
"v_add_f32 v96, v96, v92\n"
"v_mfma_f32_16x16x32_bf16 v[128:131], v[120:123], a[40:43], v[128:131]\n"
"ds_write_addtid_b32 v133 offset:6176\n"
"v_add_f32 v97, v97, v93\n"
"v_mfma_f32_16x16x32_bf16 v[128:131], v[36:39], a[44:47], v[128:131]\n"
"ds_write_addtid_b32 v134 offset:10880\n"
"v_add_f32 v98, v98, v94\n"
"v_mfma_f32_16x16x32_bf16 v[128:131], v[40:43], a[48:51], v[128:131]\n"
"ds_write_addtid_b32 v135 offset:15520\n"
"v_add_f32 v99, v99, v95\n"
"v_mfma_f32_16x16x32_bf16 v[128:131], v[44:47], a[52:55], v[128:131]\n"
"s_add_u32 s93, s50, 0xffd00000\n"
"s_and_b32 s95, s93, 0xfbffffff\n"
"v_fma_f32 v84, v18, v137, v26\n"
"v_mfma_f32_16x16x32_bf16 v[128:131], v[48:51], a[56:59], v[128:131]\n"
"v_fma_f32 v85, v19, v137, v27\n"
"v_fma_f32 v86, v20, v137, v28\n"
"v_mfma_f32_16x16x32_bf16 v[128:131], v[52:55], a[60:63], v[128:131]\n"
"v_fma_f32 v87, v21, v137, v29\n"
"v_fma_f32 v96, v96, v136, v84\n"
"v_mfma_f32_16x16x32_bf16 v[92:95], v[112:115], a[64:67], 0\n"
"v_fma_f32 v97, v97, v136, v85\n"
"v_fma_f32 v98, v98, v136, v86\n"
"v_mfma_f32_16x16x32_bf16 v[92:95], v[116:119], a[68:71], v[92:95]\n"
"ds_write_addtid_b32 v128 offset:1792\n"
"global_load_dwordx4 v[80:83], v8, s[56:57] offset:0 sc1\n"
"v_mfma_f32_16x16x32_bf16 v[92:95], v[120:123], a[72:75], v[92:95]\n"
"ds_write_addtid_b32 v129 offset:6432\n"
"v_fma_f32 v99, v99, v136, v87\n"
"v_mfma_f32_16x16x32_bf16 v[92:95], v[36:39], a[76:79], v[92:95]\n"
"ds_write_addtid_b32 v130 offset:11136\n"
"v_mul_f32_e64 v96, v96, |v96|\n"
"v_mfma_f32_16x16x32_bf16 v[92:95], v[40:43], a[80:83], v[92:95]\n"
"ds_write_addtid_b32 v131 offset:15776\n"
"v_mul_f32_e64 v97, v97, |v97|\n"
"v_mfma_f32_16x16x32_bf16 v[92:95], v[44:47], a[84:87], v[92:95]\n"
"global_load_dwordx4 v[84:87], v8, s[56:57] offset:1024 sc1\n"
"v_mul_f32_e64 v98, v98, |v98|\n"
"v_mfma_f32_16x16x32_bf16 v[92:95], v[48:51], a[88:91], v[92:95]\n"
"v_mul_f32_e64 v99, v99, |v99|\n"
"v_cvt_pk_bf16_f32 v100, v96, v97\n"
"v_mfma_f32_16x16x32_bf16 v[92:95], v[52:55], a[92:95], v[92:95]\n"
"v_cvt_pk_bf16_f32 v101, v98, v99\n"
"v_pk_max_i16 v100, v100, 0\n"
"v_mfma_f32_16x16x32_bf16 v[128:131], v[112:115], a[96:99], 0\n"
"v_pk_max_i16 v101, v101, 0\n"
"v_mfma_f32_16x16x32_bf16 v[128:131], v[116:119], a[100:103], v[128:131]\n"
"s_waitcnt lgkmcnt(8)\n"
"ds_write_addtid_b32 v92 offset:2048\n"
"v_add_f32 v104, v104, v108\n"
"v_mfma_f32_16x16x32_bf16 v[128:131], v[120:123], a[104:107], v[128:131]\n"
"ds_write_addtid_b32 v93 offset:6688\n"
"v_add_f32 v105, v105, v109\n"
"v_mfma_f32_16x16x32_bf16 v[128:131], v[36:39], a[108:111], v[128:131]\n"
"ds_write_addtid_b32 v94 offset:11392\n"
"v_add_f32 v106, v106, v110\n"
"v_mfma_f32_16x16x32_bf16 v[128:131], v[40:43], a[112:115], v[128:131]\n"
"ds_write_addtid_b32 v95 offset:16032\n"
"v_add_f32 v107, v107, v111\n"
"v_mfma_f32_16x16x32_bf16 v[128:131], v[44:47], a[116:119], v[128:131]\n"
"v_add_f32 v104, v104, v124\n"
"v_add_f32 v105, v105, v125\n"
"v_mfma_f32_16x16x32_bf16 v[128:131], v[48:51], a[120:123], v[128:131]\n"
"v_add_f32 v106, v106, v126\n"
"v_add_f32 v107, v107, v127\n"
"v_mfma_f32_16x16x32_bf16 v[128:131], v[52:55], a[124:127], v[128:131]\n"
"v_add_f32 v104, v104, v88\n"
"v_add_f32 v105, v105, v89\n"
"v_mfma_f32_16x16x32_bf16 v[92:95], v[112:115], a[128:131], 0\n"
"v_add_f32 v106, v106, v90\n"
"v_add_f32 v107, v107, v91\n"
"v_mfma_f32_16x16x32_bf16 v[92:95], v[116:119], a[132:135], v[92:95]\n"
"ds_write_addtid_b32 v128 offset:2304\n"
"v_fma_f32 v96, v22, v137, v30\n"
"v_mfma_f32_16x16x32_bf16 v[92:95], v[120:123], a[136:139], v[92:95]\n"
"ds_write_addtid_b32 v129 offset:6944\n"
"v_fma_f32 v97, v23, v137, v31\n"
"v_mfma_f32_16x16x32_bf16 v[92:95], v[36:39], a[140:143], v[92:95]\n"
"ds_write_addtid_b32 v130 offset:11648\n"
"v_fma_f32 v98, v24, v137, v32\n"
"v_mfma_f32_16x16x32_bf16 v[92:95], v[40:43], a[144:147], v[92:95]\n"
"ds_write_addtid_b32 v131 offset:16288\n"
"v_fma_f32 v99, v25, v137, v33\n"
"v_mfma_f32_16x16x32_bf16 v[92:95], v[44:47], a[148:151], v[92:95]\n"
"v_fma_f32 v104, v104, v136, v96\n"
"v_fma_f32 v105, v105, v136, v97\n"
"v_mfma_f32_16x16x32_bf16 v[92:95], v[48:51], a[152:155], v[92:95]\n"
"v_fma_f32 v106, v106, v136, v98\n"
"v_fma_f32 v107, v107, v136, v99\n"
"v_mfma_f32_16x16x32_bf16 v[92:95], v[52:55], a[156:159], v[92:95]\n"
"v_mul_f32_e64 v104, v104, |v104|\n"
"v_mul_f32_e64 v105, v105, |v105|\n"
"v_mfma_f32_16x16x32_bf16 v[96:99], v[112:115], a[160:163], 0\n"
"v_mul_f32_e64 v106, v106, |v106|\n"
"v_mul_f32_e64 v107, v107, |v107|\n"
"v_mfma_f32_16x16x32_bf16 v[96:99], v[116:119], a[164:167], v[96:99]\n"
"ds_write_addtid_b32 v92 offset:2560\n"
"v_cvt_pk_bf16_f32 v102, v104, v105\n"
"v_mfma_f32_16x16x32_bf16 v[96:99], v[120:123], a[168:171], v[96:99]\n"
"s_add_u32 s58, s50, 0x180000\n"
"ds_write_addtid_b32 v93 offset:7200\n"
"v_cvt_pk_bf16_f32 v103, v106, v107\n"
"v_mfma_f32_16x16x32_bf16 v[96:99], v[36:39], a[172:175], v[96:99]\n"
"s_and_b32 s58, s58, 0x3ffffff\n"
"ds_write_addtid_b32 v94 offset:11904\n"
"v_pk_max_i16 v102, v102, 0\n"
"v_mfma_f32_16x16x32_bf16 v[96:99], v[40:43], a[176:179], v[96:99]\n"
"s_add_u32 s56, s40, s58\n"
"ds_write_addtid_b32 v95 offset:16544\n"
"v_pk_max_i16 v103, v103, 0\n"
"v_mfma_f32_16x16x32_bf16 v[96:99], v[44:47], a[180:183], v[96:99]\n"
"s_addc_u32 s57, s41, 0\n"
"buffer_store_dwordx4 v[100:103], v15, s[40:43], s95 offen sc1\n"
"v_mfma_f32_16x16x32_bf16 v[96:99], v[48:51], a[184:187], v[96:99]\n"
"s_waitcnt vmcnt(9)\n"
"v_cmp_ne_u64 vcc, s[48:49], v[138:139]\n"
"v_mfma_f32_16x16x32_bf16 v[96:99], v[52:55], a[188:191], v[96:99]\n"
"v_mfma_f32_16x16x32_bf16 v[100:103], a[192:195], v[112:115], 0\n"
"s_cbranch_vccnz .LF_4s\n"
".LF_4r:\n"
"v_mfma_f32_16x16x32_bf16 v[100:103], a[196:199], v[116:119], v[100:103]\n"
"s_add_u32 s50, s50, 0x180000\n"
"ds_write_addtid_b32 v96 offset:2816\n"
"v_mfma_f32_16x16x32_bf16 v[100:103], a[200:203], v[120:123], v[100:103]\n"
"s_waitcnt vmcnt(6)\n"
"buffer_store_dwordx2 v[0:1], v16, s[40:43], s94 offen offset:0 sc1\n"
"ds_write_addtid_b32 v97 offset:7456\n"
"v_mfma_f32_16x16x32_bf16 v[100:103], a[204:207], v[36:39], v[100:103]\n"
"ds_write_addtid_b32 v98 offset:12160\n"
"global_load_dwordx4 v[88:91], v5, s[56:57] offset:0 sc1\n"
"v_mfma_f32_16x16x32_bf16 v[100:103], a[208:211], v[40:43], v[100:103]\n"
"ds_write_addtid_b32 v99 offset:16800\n"
"v_mfma_f32_16x16x32_bf16 v[100:103], a[212:215], v[44:47], v[100:103]\n"
"v_mfma_f32_16x16x32_bf16 v[100:103], a[216:219], v[48:51], v[100:103]\n"
"v_mfma_f32_16x16x32_bf16 v[100:103], a[220:223], v[52:55], v[100:103]\n"
"v_mfma_f32_16x16x32_bf16 v[104:107], a[224:227], v[112:115], 0\n"
"s_waitcnt lgkmcnt(0)\n"
"global_load_dwordx4 v[92:95], v5, s[56:57] offset:1024 sc1\n"
"v_mfma_f32_16x16x32_bf16 v[104:107], a[228:231], v[116:119], v[104:107]\n"
"s_barrier\n"
"v_mfma_f32_16x16x32_bf16 v[104:107], a[232:235], v[120:123], v[104:107]\n"
"ds_read2st64_b32 v[132:133], v12 offset0:0 offset1:1\n"
"ds_read_b128 v[108:111], v2 offset:0\n"
"v_mfma_f32_16x16x32_bf16 v[104:107], a[236:239], v[36:39], v[104:107]\n"
"ds_read_b128 v[96:99], v3 offset:2048\n"
"ds_read_b128 v[112:115], v4 offset:1024\n"
"v_mfma_f32_16x16x32_bf16 v[104:107], a[240:243], v[40:43], v[104:107]\n"
"buffer_load_dwordx2 v[134:135], v9, s[40:43], s50 offen sc1\n"
"v_mfma_f32_16x16x32_bf16 v[104:107], a[244:247], v[44:47], v[104:107]\n"
"global_load_dwordx4 v[36:39], v6, s[56:57] offset:0 sc1\n"
"ds_read_b128 v[116:119], v2 offset:256\n"
"v_mfma_f32_16x16x32_bf16 v[104:107], a[248:251], v[48:51], v[104:107]\n"
"ds_read_b128 v[120:123], v3 offset:2304\n"
"ds_read_b128 v[124:127], v4 offset:1280\n"
"v_mfma_f32_16x16x32_bf16 v[104:107], a[252:255], v[52:55], v[104:107]\n"
"global_load_dwordx4 v[40:43], v6, s[56:57] offset:1024 sc1\n"
"v_mfma_f32_16x16x32_bf16 v[128:131], v[56:59], a[0:3], 0\n"
"v_mfma_f32_16x16x32_bf16 v[128:131], v[60:63], a[4:7], v[128:131]\n"
"s_waitcnt vmcnt(11)\n"
"v_mfma_f32_16x16x32_bf16 v[128:131], v[64:67], a[8:11], v[128:131]\n"
"s_waitcnt lgkmcnt(3)\n"
"v_add_f32 v108, v108, v96\n"
"v_add_f32 v109, v109, v97\n"
"v_mfma_f32_16x16x32_bf16 v[128:131], v[68:71], a[12:15], v[128:131]\n"
"s_waitcnt vmcnt(9)\n"
"v_add_f32 v110, v110, v98\n"
"v_add_f32 v111, v111, v99\n"
"v_mfma_f32_16x16x32_bf16 v[128:131], v[72:75], a[16:19], v[128:131]\n"
"v_add_f32 v108, v108, v112\n"
"v_add_f32 v109, v109, v113\n"
"v_mfma_f32_16x16x32_bf16 v[128:131], v[76:79], a[20:23], v[128:131]\n"
"s_waitcnt vmcnt(7)\n"
"v_add_f32 v110, v110, v114\n"
"v_add_f32 v111, v111, v115\n"
"v_mfma_f32_16x16x32_bf16 v[128:131], v[80:83], a[24:27], v[128:131]\n"
"v_add_f32 v108, v108, v100\n"
"v_add_f32 v109, v109, v101\n"
"v_mfma_f32_16x16x32_bf16 v[128:131], v[84:87], a[28:31], v[128:131]\n"
"global_load_dwordx4 v[44:47], v7, s[56:57] offset:0 sc1\n"
"v_add_f32 v110, v110, v102\n"
"v_mfma_f32_16x16x32_bf16 v[112:115], v[56:59], a[32:35], 0\n"
"v_add_f32 v111, v111, v103\n"
"v_mfma_f32_16x16x32_bf16 v[112:115], v[60:63], a[36:39], v[112:115]\n"
"ds_write_addtid_b32 v128 offset:0\n"
"global_load_dwordx4 v[48:51], v7, s[56:57] offset:1024 sc1\n"
"v_mfma_f32_16x16x32_bf16 v[112:115], v[64:67], a[40:43], v[112:115]\n"
"s_add_u32 s95, s50, 0xffd00000\n"
"s_and_b32 s94, s95, 0xfbffffff\n"
"ds_write_addtid_b32 v129 offset:4640\n"
"v_mfma_f32_16x16x32_bf16 v[112:115], v[68:71], a[44:47], v[112:115]\n"
"ds_write_addtid_b32 v130 offset:9344\n"
"v_fma_f32 v96, v18, v133, v26\n"
"v_mfma_f32_16x16x32_bf16 v[112:115], v[72:75], a[48:51], v[112:115]\n"
"ds_write_addtid_b32 v131 offset:13984\n"
"v_fma_f32 v97, v19, v133, v27\n"
"v_mfma_f32_16x16x32_bf16 v[112:115], v[76:79], a[52:55], v[112:115]\n"
"v_fma_f32 v98, v20, v133, v28\n"
"v_fma_f32 v99, v21, v133, v29\n"
"v_mfma_f32_16x16x32_bf16 v[112:115], v[80:83], a[56:59], v[112:115]\n"
"v_fma_f32 v108, v108, v132, v96\n"
"v_fma_f32 v109, v109, v132, v97\n"
"v_mfma_f32_16x16x32_bf16 v[112:115], v[84:87], a[60:63], v[112:115]\n"
"v_fma_f32 v110, v110, v132, v98\n"
"v_fma_f32 v111, v111, v132, v99\n"
"v_mfma_f32_16x16x32_bf16 v[100:103], v[56:59], a[64:67], 0\n"
"global_load_dwordx4 v[52:55], v8, s[56:57] offset:0 sc1\n"
"v_mul_f32_e64 v108, v108, |v108|\n"
"v_mfma_f32_16x16x32_bf16 v[100:103], v[60:63], a[68:71], v[100:103]\n"
"ds_write_addtid_b32 v112 offset:256\n"
"v_mul_f32_e64 v109, v109, |v109|\n"
"v_mfma_f32_16x16x32_bf16 v[100:103], v[64:67], a[72:75], v[100:103]\n"
"ds_write_addtid_b32 v113 offset:4896\n"
"v_mul_f32_e64 v110, v110, |v110|\n"
"v_mfma_f32_16x16x32_bf16 v[100:103], v[68:71], a[76:79], v[100:103]\n"
"ds_write_addtid_b32 v114 offset:9600\n"
"v_mul_f32_e64 v111, v111, |v111|\n"
"v_mfma_f32_16x16x32_bf16 v[100:103], v[72:75], a[80:83], v[100:103]\n"
"ds_write_addtid_b32 v115 offset:14240\n"
"global_load_dwordx4 v[96:99], v8, s[56:57] offset:1024 sc1\n"
"v_mfma_f32_16x16x32_bf16 v[100:103], v[76:79], a[84:87], v[100:103]\n"
"v_cvt_pk_bf16_f32 v128, v108, v109\n"
"v_cvt_pk_bf16_f32 v129, v110, v111\n"
"v_mfma_f32_16x16x32_bf16 v[100:103], v[80:83], a[88:91], v[100:103]\n"
"v_pk_max_i16 v128, v128, 0\n"
"v_pk_max_i16 v129, v129, 0\n"
"v_mfma_f32_16x16x32_bf16 v[100:103], v[84:87], a[92:95], v[100:103]\n"
"s_waitcnt lgkmcnt(8)\n"
"v_add_f32 v116, v116, v120\n"
"v_add_f32 v117, v117, v121\n"
"v_mfma_f32_16x16x32_bf16 v[108:111], v[56:59], a[96:99], 0\n"
"v_add_f32 v118, v118, v122\n"
"v_add_f32 v119, v119, v123\n"
"v_mfma_f32_16x16x32_bf16 v[108:111], v[60:63], a[100:103], v[108:111]\n"
"ds_write_addtid_b32 v100 offset:512\n"
"v_add_f32 v116, v116, v124\n"
"v_mfma_f32_16x16x32_bf16 v[108:111], v[64:67], a[104:107], v[108:111]\n"
"ds_write_addtid_b32 v101 offset:5152\n"
"v_add_f32 v117, v117, v125\n"
"v_mfma_f32_16x16x32_bf16 v[108:111], v[68:71], a[108:111], v[108:111]\n"
"ds_write_addtid_b32 v102 offset:9856\n"
"v_add_f32 v118, v118, v126\n"
"v_mfma_f32_16x16x32_bf16 v[108:111], v[72:75], a[112:115], v[108:111]\n"
"ds_write_addtid_b32 v103 offset:14496\n"
"v_add_f32 v119, v119, v127\n"
"v_mfma_f32_16x16x32_bf16 v[108:111], v[76:79], a[116:119], v[108:111]\n"
"s_waitcnt vmcnt(10)\n"
"buffer_store_dwordx2 v[0:1], v16, s[40:43], s93 offen offset:0 sc1\n"
"v_add_f32 v116, v116, v104\n"
"v_mfma_f32_16x16x32_bf16 v[108:111], v[80:83], a[120:123], v[108:111]\n"
"v_add_f32 v117, v117, v105\n"
"v_add_f32 v118, v118, v106\n"
"v_mfma_f32_16x16x32_bf16 v[108:111], v[84:87], a[124:127], v[108:111]\n"
"v_add_f32 v119, v119, v107\n"
"v_fma_f32 v100, v22, v133, v30\n"
"v_mfma_f32_16x16x32_bf16 v[112:115], v[56:59], a[128:131], 0\n"
"v_fma_f32 v101, v23, v133, v31\n"
"v_fma_f32 v102, v24, v133, v32\n"
"v_mfma_f32_16x16x32_bf16 v[112:115], v[60:63], a[132:135], v[112:115]\n"
"ds_write_addtid_b32 v108 offset:768\n"
"v_fma_f32 v103, v25, v133, v33\n"
"v_mfma_f32_16x16x32_bf16 v[112:115], v[64:67], a[136:139], v[112:115]\n"
"ds_write_addtid_b32 v109 offset:5408\n"
"v_fma_f32 v116, v116, v132, v100\n"
"v_mfma_f32_16x16x32_bf16 v[112:115], v[68:71], a[140:143], v[112:115]\n"
"ds_write_addtid_b32 v110 offset:10112\n"
"v_fma_f32 v117, v117, v132, v101\n"
"v_mfma_f32_16x16x32_bf16 v[112:115], v[72:75], a[144:147], v[112:115]\n"
"ds_write_addtid_b32 v111 offset:14752\n"
"v_fma_f32 v118, v118, v132, v102\n"
"v_mfma_f32_16x16x32_bf16 v[112:115], v[76:79], a[148:151], v[112:115]\n"
"v_fma_f32 v119, v119, v132, v103\n"
"v_mul_f32_e64 v116, v116, |v116|\n"
"v_mfma_f32_16x16x32_bf16 v[112:115], v[80:83], a[152:155], v[112:115]\n"
"v_mul_f32_e64 v117, v117, |v117|\n"
"v_mul_f32_e64 v118, v118, |v118|\n"
"v_mfma_f32_16x16x32_bf16 v[112:115], v[84:87], a[156:159], v[112:115]\n"
"v_mul_f32_e64 v119, v119, |v119|\n"
"v_cvt_pk_bf16_f32 v130, v116, v117\n"
"v_mfma_f32_16x16x32_bf16 v[108:111], v[56:59], a[160:163], 0\n"
"v_cvt_pk_bf16_f32 v131, v118, v119\n"
"v_pk_max_i16 v130, v130, 0\n"
"v_mfma_f32_16x16x32_bf16 v[108:111], v[60:63], a[164:167], v[108:111]\n"
"s_add_u32 s58, s50, 0x180000\n"
"ds_write_addtid_b32 v112 offset:1024\n"
"v_pk_max_i16 v131, v131, 0\n"
"v_mfma_f32_16x16x32_bf16 v[108:111], v[64:67], a[168:171], v[108:111]\n"
"s_and_b32 s58, s58, 0x3ffffff\n"
"ds_write_addtid_b32 v113 offset:5664\n"
"v_mfma_f32_16x16x32_bf16 v[108:111], v[68:71], a[172:175], v[108:111]\n"
"s_add_u32 s56, s40, s58\n"
"ds_write_addtid_b32 v114 offset:10368\n"
"v_mfma_f32_16x16x32_bf16 v[108:111], v[72:75], a[176:179], v[108:111]\n"
"s_addc_u32 s57, s41, 0\n"
"ds_write_addtid_b32 v115 offset:15008\n"
"v_mfma_f32_16x16x32_bf16 v[108:111], v[76:79], a[180:183], v[108:111]\n"
"s_waitcnt vmcnt(7)\n"
"buffer_store_dwordx4 v[128:131], v15, s[40:43], s94 offen sc1\n"
"v_cmp_ne_u64 vcc, s[48:49], v[134:135]\n"
"v_mfma_f32_16x16x32_bf16 v[108:111], v[80:83], a[184:187], v[108:111]\n"
"v_mfma_f32_16x16x32_bf16 v[108:111], v[84:87], a[188:191], v[108:111]\n"
"s_cbranch_vccnz .LF_1s\n"
".LF_1r:\n"
"v_mfma_f32_16x16x32_bf16 v[112:115], a[192:195], v[56:59], 0\n"
"s_add_u32 s50, s50, 0x180000\n"
"global_load_dwordx4 v[100:103], v5, s[56:57] offset:0 sc1\n"
"v_mfma_f32_16x16x32_bf16 v[112:115], a[196:199], v[60:63], v[112:115]\n"
"ds_write_addtid_b32 v108 offset:1280\n"
"global_load_dwordx4 v[124:127], v10, s[56:57] offset:0 sc1\n"
"v_mfma_f32_16x16x32_bf16 v[112:115], a[200:203], v[64:67], v[112:115]\n"
"ds_write_addtid_b32 v109 offset:5920\n"
"global_load_dwordx4 v[116:119], v10, s[56:57] offset:1024 sc1\n"
"v_mfma_f32_16x16x32_bf16 v[112:115], a[204:207], v[68:71], v[112:115]\n"
"ds_write_addtid_b32 v110 offset:10624\n"
"global_load_dwordx4 v[120:123], v10, s[56:57] offset:2048 sc1\n"
"v_mfma_f32_16x16x32_bf16 v[112:115], a[208:211], v[72:75], v[112:115]\n"
"ds_write_addtid_b32 v111 offset:15264\n"
"global_load_dwordx4 v[128:131], v10, s[56:57] offset:3072 sc1\n"
"v_mfma_f32_16x16x32_bf16 v[112:115], a[212:215], v[76:79], v[112:115]\n"
"global_load_dwordx4 v[104:107], v5, s[56:57] offset:1024 sc1\n"
"v_mfma_f32_16x16x32_bf16 v[112:115], a[216:219], v[80:83], v[112:115]\n"
"v_mfma_f32_16x16x32_bf16 v[112:115], a[220:223], v[84:87], v[112:115]\n"
"v_mfma_f32_16x16x32_bf16 v[132:135], a[224:227], v[56:59], 0\n"
"s_waitcnt lgkmcnt(0)\n"
"v_mfma_f32_16x16x32_bf16 v[132:135], a[228:231], v[60:63], v[132:135]\n"
"s_barrier\n"
"v_mfma_f32_16x16x32_bf16 v[132:135], a[232:235], v[64:67], v[132:135]\n"
"global_load_dwordx4 v[108:111], v6, s[56:57] offset:0 sc1\n"
"ds_read2st64_b32 v[152:153], v13 offset0:0 offset1:1\n"
"v_mfma_f32_16x16x32_bf16 v[132:135], a[236:239], v[68:71], v[132:135]\n"
"ds_read_b128 v[56:59], v2 offset:1536\n"
"ds_read_b128 v[60:63], v3 offset:512\n"
"v_mfma_f32_16x16x32_bf16 v[132:135], a[240:243], v[72:75], v[132:135]\n"
"ds_read_b128 v[136:139], v4 offset:2560\n"
"ds_read_b128 v[140:143], v2 offset:1792\n"
"v_mfma_f32_16x16x32_bf16 v[132:135], a[244:247], v[76:79], v[132:135]\n"
"buffer_load_dwordx2 v[154:155], v9, s[40:43], s50 offen sc1\n"
"v_mfma_f32_16x16x32_bf16 v[132:135], a[248:251], v[80:83], v[132:135]\n"
"global_load_dwordx4 v[64:67], v6, s[56:57] offset:1024 sc1\n"
"ds_read_b128 v[144:147], v3 offset:768\n"
"v_mfma_f32_16x16x32_bf16 v[132:135], a[252:255], v[84:87], v[132:135]\n"
"ds_read_b128 v[148:151], v4 offset:2816\n"
"v_mfma_f32_16x16x32_bf16 v[76:79], v[88:91], a[0:3], 0\n"
"v_mfma_f32_16x16x32_bf16 v[76:79], v[92:95], a[4:7], v[76:79]\n"
"s_waitcnt vmcnt(15)\n"
"v_mfma_f32_16x16x32_bf16 v[76:79], v[36:39], a[8:11], v[76:79]\n"
"s_waitcnt lgkmcnt(3)\n"
"v_add_f32 v56, v56, v60\n"
"v_add_f32 v57, v57, v61\n"
"v_mfma_f32_16x16x32_bf16 v[76:79], v[40:43], a[12:15], v[76:79]\n"
"s_waitcnt vmcnt(13)\n"
"v_add_f32 v58, v58, v62\n"
"v_add_f32 v59, v59, v63\n"
"v_mfma_f32_16x16x32_bf16 v[76:79], v[44:47], a[16:19], v[76:79]\n"
"global_load_dwordx4 v[68:71], v7, s[56:57] offset:0 sc1\n"
"v_add_f32 v56, v56, v136\n"
"v_mfma_f32_16x16x32_bf16 v[76:79], v[48:51], a[20:23], v[76:79]\n"
"s_waitcnt vmcnt(12)\n"
"v_add_f32 v57, v57, v137\n"
"v_add_f32 v58, v58, v138\n"
"v_mfma_f32_16x16x32_bf16 v[76:79], v[52:55], a[24:27], v[76:79]\n"
"v_add_f32 v59, v59, v139\n"
"v_add_f32 v56, v56, v112\n"
"v_mfma_f32_16x16x32_bf16 v[76:79], v[96:99], a[28:31], v[76:79]\n"
"v_add_f32 v57, v57, v113\n"
"v_add_f32 v58, v58, v114\n"
"v_mfma_f32_16x16x32_bf16 v[60:63], v[88:91], a[32:35], 0\n"
"v_add_f32 v59, v59, v115\n"
"v_mfma_f32_16x16x32_bf16 v[60:63], v[92:95], a[36:39], v[60:63]\n"
"ds_write_addtid_b32 v76 offset:1536\n"
"global_load_dwordx4 v[72:75], v7, s[56:57] offset:1024 sc1\n"
"v_mfma_f32_16x16x32_bf16 v[60:63], v[36:39], a[40:43], v[60:63]\n"
"s_add_u32 s93, s50, 0xffd00000\n"
"s_and_b32 s94, s93, 0xfbffffff\n"
"ds_write_addtid_b32 v77 offset:6176\n"
"v_mfma_f32_16x16x32_bf16 v[60:63], v[40:43], a[44:47], v[60:63]\n"
"ds_write_addtid_b32 v78 offset:10880\n"
"v_fma_f32 v80, v18, v153, v26\n"
"v_mfma_f32_16x16x32_bf16 v[60:63], v[44:47], a[48:51], v[60:63]\n"
"ds_write_addtid_b32 v79 offset:15520\n"
"v_fma_f32 v81, v19, v153, v27\n"
"v_mfma_f32_16x16x32_bf16 v[60:63], v[48:51], a[52:55], v[60:63]\n"
"v_fma_f32 v82, v20, v153, v28\n"
"v_fma_f32 v83, v21, v153, v29\n"
"v_mfma_f32_16x16x32_bf16 v[60:63], v[52:55], a[56:59], v[60:63]\n"
"v_fma_f32 v56, v56, v152, v80\n"
"v_fma_f32 v57, v57, v152, v81\n"
"v_mfma_f32_16x16x32_bf16 v[60:63], v[96:99], a[60:63], v[60:63]\n"
"global_load_dwordx4 v[76:79], v8, s[56:57] offset:0 sc1\n"
"v_fma_f32 v58, v58, v152, v82\n"
"v_mfma_f32_16x16x32_bf16 v[84:87], v[88:91], a[64:67], 0\n"
"v_fma_f32 v59, v59, v152, v83\n"
"v_mul_f32_e64 v56, v56, |v56|\n"
"v_mfma_f32_16x16x32_bf16 v[84:87], v[92:95], a[68:71], v[84:87]\n"
"ds_write_addtid_b32 v60 offset:1792\n"
"v_mul_f32_e64 v57, v57, |v57|\n"
"v_mfma_f32_16x16x32_bf16 v[84:87], v[36:39], a[72:75], v[84:87]\n"
"ds_write_addtid_b32 v61 offset:6432\n"
"v_mul_f32_e64 v58, v58, |v58|\n"
"v_mfma_f32_16x16x32_bf16 v[84:87], v[40:43], a[76:79], v[84:87]\n"
"ds_write_addtid_b32 v62 offset:11136\n"
"global_load_dwordx4 v[80:83], v8, s[56:57] offset:1024 sc1\n"
"v_mfma_f32_16x16x32_bf16 v[84:87], v[44:47], a[80:83], v[84:87]\n"
"ds_write_addtid_b32 v63 offset:15776\n"
"v_mul_f32_e64 v59, v59, |v59|\n"
"v_mfma_f32_16x16x32_bf16 v[84:87], v[48:51], a[84:87], v[84:87]\n"
"v_cvt_pk_bf16_f32 v136, v56, v57\n"
"v_cvt_pk_bf16_f32 v137, v58, v59\n"
"v_mfma_f32_16x16x32_bf16 v[84:87], v[52:55], a[88:91], v[84:87]\n"
"v_pk_max_i16 v136, v136, 0\n"
"v_pk_max_i16 v137, v137, 0\n"
"v_mfma_f32_16x16x32_bf16 v[84:87], v[96:99], a[92:95], v[84:87]\n"
"s_waitcnt lgkmcnt(8)\n"
"v_add_f32 v140, v140, v144\n"
"v_add_f32 v141, v141, v145\n"
"v_mfma_f32_16x16x32_bf16 v[56:59], v[88:91], a[96:99], 0\n"
"v_add_f32 v142, v142, v146\n"
"v_add_f32 v143, v143, v147\n"
"v_mfma_f32_16x16x32_bf16 v[56:59], v[92:95], a[100:103], v[56:59]\n"
"ds_write_addtid_b32 v84 offset:2048\n"
"v_add_f32 v140, v140, v148\n"
"v_mfma_f32_16x16x32_bf16 v[56:59], v[36:39], a[104:107], v[56:59]\n"
"ds_write_addtid_b32 v85 offset:6688\n"
"v_add_f32 v141, v141, v149\n"
"v_mfma_f32_16x16x32_bf16 v[56:59], v[40:43], a[108:111], v[56:59]\n"
"ds_write_addtid_b32 v86 offset:11392\n"
"v_add_f32 v142, v142, v150\n"
"v_mfma_f32_16x16x32_bf16 v[56:59], v[44:47], a[112:115], v[56:59]\n"
"ds_write_addtid_b32 v87 offset:16032\n"
"v_add_f32 v143, v143, v151\n"
"v_mfma_f32_16x16x32_bf16 v[56:59], v[48:51], a[116:119], v[56:59]\n"
"s_waitcnt vmcnt(13)\n"
"buffer_store_dwordx2 v[0:1], v16, s[40:43], s95 offen offset:0 sc1\n"
"v_add_f32 v140, v140, v132\n"
"v_mfma_f32_16x16x32_bf16 v[56:59], v[52:55], a[120:123], v[56:59]\n"
"v_add_f32 v141, v141, v133\n"
"v_add_f32 v142, v142, v134\n"
"v_mfma_f32_16x16x32_bf16 v[56:59], v[96:99], a[124:127], v[56:59]\n"
"v_add_f32 v143, v143, v135\n"
"v_fma_f32 v60, v22, v153, v30\n"
"v_mfma_f32_16x16x32_bf16 v[84:87], v[88:91], a[128:131], 0\n"
"v_fma_f32 v61, v23, v153, v31\n"
"v_fma_f32 v62, v24, v153, v32\n"
"v_mfma_f32_16x16x32_bf16 v[84:87], v[92:95], a[132:135], v[84:87]\n"
"ds_write_addtid_b32 v56 offset:2304\n"
"v_fma_f32 v63, v25, v153, v33\n"
"v_mfma_f32_16x16x32_bf16 v[84:87], v[36:39], a[136:139], v[84:87]\n"
"ds_write_addtid_b32 v57 offset:6944\n"
"v_fma_f32 v140, v140, v152, v60\n"
"v_mfma_f32_16x16x32_bf16 v[84:87], v[40:43], a[140:143], v[84:87]\n"
"ds_write_addtid_b32 v58 offset:11648\n"
"v_fma_f32 v141, v141, v152, v61\n"
"v_mfma_f32_16x16x32_bf16 v[84:87], v[44:47], a[144:147], v[84:87]\n"
"s_waitcnt vmcnt(9)\n"
"ds_write_addtid_b32 v59 offset:16288\n"
"v_add_f32 v124, v124, v116\n"
"v_mfma_f32_16x16x32_bf16 v[84:87], v[48:51], a[148:151], v[84:87]\n"
"v_add_f32 v125, v125, v117\n"
"v_add_f32 v126, v126, v118\n"
"v_mfma_f32_16x16x32_bf16 v[84:87], v[52:55], a[152:155], v[84:87]\n"
"v_add_f32 v127, v127, v119\n"
"v_add_f32 v120, v120, v128\n"
"v_mfma_f32_16x16x32_bf16 v[84:87], v[96:99], a[156:159], v[84:87]\n"
"v_add_f32 v121, v121, v129\n"
"v_add_f32 v122, v122, v130\n"
"v_mfma_f32_16x16x32_bf16 v[56:59], v[88:91], a[160:163], 0\n"
"v_add_f32 v123, v123, v131\n"
"v_add_f32 v124, v124, v120\n"
"v_mfma_f32_16x16x32_bf16 v[56:59], v[92:95], a[164:167], v[56:59]\n"
"ds_write_addtid_b32 v84 offset:2560\n"
"v_add_f32 v125, v125, v121\n"
"v_mfma_f32_16x16x32_bf16 v[56:59], v[36:39], a[168:171], v[56:59]\n"
"s_add_u32 s58, s50, 0x180000\n"
"ds_write_addtid_b32 v85 offset:7200\n"
"v_add_f32 v126, v126, v122\n"
"v_mfma_f32_16x16x32_bf16 v[56:59], v[40:43], a[172:175], v[56:59]\n"
"s_and_b32 s58, s58, 0x3ffffff\n"
"ds_write_addtid_b32 v86 offset:11904\n"
"v_add_f32 v127, v127, v123\n"
"v_mfma_f32_16x16x32_bf16 v[56:59], v[44:47], a[176:179], v[56:59]\n"
"s_add_u32 s56, s40, s58\n"
"ds_write_addtid_b32 v87 offset:16544\n"
"v_fma_f32 v142, v142, v152, v62\n"
"v_mfma_f32_16x16x32_bf16 v[56:59], v[48:51], a[180:183], v[56:59]\n"
"s_addc_u32 s57, s41, 0\n"
"v_permlane32_swap_b32 v124, v126\n"
"v_mfma_f32_16x16x32_bf16 v[56:59], v[52:55], a[184:187], v[56:59]\n"
"s_waitcnt vmcnt(6)\n"
"v_cmp_ne_u64 vcc, s[48:49], v[154:155]\n"
"v_fma_f32 v143, v143, v152, v63\n"
"v_mfma_f32_16x16x32_bf16 v[56:59], v[96:99], a[188:191], v[56:59]\n"
"v_permlane32_swap_b32 v125, v127\n"
"v_mfma_f32_16x16x32_bf16 v[128:131], a[192:195], v[88:91], 0\n"
"s_cbranch_vccnz .LF_2s\n"
".LF_2r:\n"
"v_add_f32 v124, v124, v126\n"
"v_mfma_f32_16x16x32_bf16 v[128:131], a[196:199], v[92:95], v[128:131]\n"
"s_add_u32 s50, s50, 0x180000\n"
"ds_write_addtid_b32 v56 offset:2816\n"
"v_add_f32 v125, v125, v127\n"
"v_mfma_f32_16x16x32_bf16 v[128:131], a[200:203], v[36:39], v[128:131]\n"
"ds_write_addtid_b32 v57 offset:7456\n"
"global_load_dwordx4 v[112:115], v5, s[56:57] offset:0 sc1\n"
"v_mfma_f32_16x16x32_bf16 v[128:131], a[204:207], v[40:43], v[128:131]\n"
"ds_write_addtid_b32 v58 offset:12160\n"
"v_mul_f32_e64 v140, v140, |v140|\n"
"v_mfma_f32_16x16x32_bf16 v[128:131], a[208:211], v[44:47], v[128:131]\n"
"ds_write_addtid_b32 v59 offset:16800\n"
"v_mul_f32_e64 v141, v141, |v141|\n"
"v_mfma_f32_16x16x32_bf16 v[128:131], a[212:215], v[48:51], v[128:131]\n"
"v_permlane16_swap_b32 v124, v125\n"
"v_mfma_f32_16x16x32_bf16 v[128:131], a[216:219], v[52:55], v[128:131]\n"
"v_add_f32 v124, v124, v125\n"
"v_mul_f32_e64 v142, v142, |v142|\n"
"v_mfma_f32_16x16x32_bf16 v[128:131], a[220:223], v[96:99], v[128:131]\n"
"global_load_dwordx4 v[116:119], v5, s[56:57] offset:1024 sc1\n"
"v_add_f32_dpp v125, v124, v124 row_ror:8 row_mask:0xf bank_mask:0xf\n"
"v_mfma_f32_16x16x32_bf16 v[132:135], a[224:227], v[88:91], 0\n"
"s_waitcnt lgkmcnt(0)\n"
"v_mov_b32 v124, v125\n"
"v_mul_f32_e64 v143, v143, |v143|\n"
"v_mfma_f32_16x16x32_bf16 v[132:135], a[228:231], v[92:95], v[132:135]\n"
"s_barrier\n"
"v_mfma_f32_16x16x32_bf16 v[132:135], a[232:235], v[36:39], v[132:135]\n"
"v_permlane16_swap_b32 v125, v124\n"
"v_mfma_f32_16x16x32_bf16 v[132:135], a[236:239], v[40:43], v[132:135]\n"
"global_load_dwordx4 v[120:123], v6, s[56:57] offset:0 sc1\n"
"v_mul_f32 v125, 0xba800000, v125\n"
"v_mfma_f32_16x16x32_bf16 v[132:135], a[240:243], v[44:47], v[132:135]\n"
"buffer_load_dwordx2 v[144:145], v9, s[40:43], s50 offen sc1\n"
"v_mfma_f32_16x16x32_bf16 v[132:135], a[244:247], v[48:51], v[132:135]\n"
"v_fma_f32 v124, v124, s62, v17\n"
"v_fma_f32 v124, -v125, v125, v124\n"
"v_mfma_f32_16x16x32_bf16 v[132:135], a[248:251], v[52:55], v[132:135]\n"
"v_max_f32 v124, v17, v124\n"
"v_cvt_pk_bf16_f32 v138, v140, v141\n"
"v_mfma_f32_16x16x32_bf16 v[132:135], a[252:255], v[96:99], v[132:135]\n"
"v_rsq_f32 v124, v124\n"
"s_branch .LF_top\n"
".LF_1s:\n"
"s_cmp_ge_u32 s50, s53\n"
"s_cbranch_scc1 .LF_t0\n"
"s_cmp_ge_u32 s50, s52\n"
"s_cbranch_scc1 .LF_1r\n"
"s_sub_u32 s60, s50, 0x0\n"
"s_mov_b32 s61, 200000\n"
"s_nop 7\n"
".LF_1p:\n"
"buffer_load_dwordx2 v[134:135], v9, s[40:43], s60 offen sc1\n"
"s_waitcnt vmcnt(0)\n"
"v_cmp_ne_u64 vcc, s[48:49], v[134:135]\n"
"s_cbranch_vccz .LF_1px\n"
"s_sleep 1\n"
"s_sub_u32 s61, s61, 1\n"
"s_cbranch_scc0 .LF_1p\n"
"s_branch .LF_end\n"
".LF_1px:\n"
"s_branch .LF_1r\n"
".LF_2s:\n"
"s_cmp_ge_u32 s50, s53\n"
"s_cbranch_scc1 .LF_t1\n"
"s_cmp_ge_u32 s50, s52\n"
"s_cbranch_scc1 .LF_2r\n"
"s_sub_u32 s60, s50, 0x0\n"
"s_mov_b32 s61, 200000\n"
"s_nop 7\n"
".LF_2p:\n"
"buffer_load_dwordx2 v[154:155], v9, s[40:43], s60 offen sc1\n"
"s_waitcnt vmcnt(0)\n"
"v_cmp_ne_u64 vcc, s[48:49], v[154:155]\n"
"s_cbranch_vccz .LF_2px\n"
"s_sleep 1\n"
"s_sub_u32 s61, s61, 1\n"
"s_cbranch_scc0 .LF_2p\n"
"s_branch .LF_end\n"
".LF_2px:\n"
"s_branch .LF_2r\n"
".LF_3s:\n"
"s_cmp_ge_u32 s50, s53\n"
"s_cbranch_scc1 .LF_t2\n"
"s_cmp_ge_u32 s50, s52\n"
"s_cbranch_scc1 .LF_3r\n"
"s_sub_u32 s60, s50, 0x0\n"
"s_mov_b32 s61, 200000\n"
"s_nop 7\n"
".LF_3p:\n"
"buffer_load_dwordx2 v[144:145], v9, s[40:43], s60 offen sc1\n"
"s_waitcnt vmcnt(0)\n"
"v_cmp_ne_u64 vcc, s[48:49], v[144:145]\n"
"s_cbranch_vccz .LF_3px\n"
"s_sleep 1\n"
"s_sub_u32 s61, s61, 1\n"
"s_cbranch_scc0 .LF_3p\n"
"s_branch .LF_end\n"
".LF_3px:\n"
"s_branch .LF_3r\n"
".LF_4s:\n"
"s_cmp_ge_u32 s50, s53\n"
"s_cbranch_scc1 .LF_t3\n"
"s_cmp_ge_u32 s50, s52\n"
"s_cbranch_scc1 .LF_4r\n"
"s_sub_u32 s60, s50, 0x0\n"
"s_mov_b32 s61, 200000\n"
"s_nop 7\n"
".LF_4p:\n"
"buffer_load_dwordx2 v[138:139], v9, s[40:43], s60 offen sc1\n"
"s_waitcnt vmcnt(0)\n"
"v_cmp_ne_u64 vcc, s[48:49], v[138:139]\n"
"s_cbranch_vccz .LF_4px\n"
"s_sleep 1\n"
"s_sub_u32 s61, s61, 1\n"
"s_cbranch_scc0 .LF_4p\n"
"s_branch .LF_end\n"
".LF_4px:\n"
"s_branch .LF_4r\n"
".LF_t0:\n"
"v_mfma_f32_16x16x32_bf16 v[112:115], a[192:195], v[56:59], 0\n"
"v_mfma_f32_16x16x32_bf16 v[112:115], a[196:199], v[60:63], v[112:115]\n"
"ds_write_addtid_b32 v108 offset:1280\n"
"v_mfma_f32_16x16x32_bf16 v[112:115], a[200:203], v[64:67], v[112:115]\n"
"ds_write_addtid_b32 v109 offset:5920\n"
"v_mfma_f32_16x16x32_bf16 v[112:115], a[204:207], v[68:71], v[112:115]\n"
"ds_write_addtid_b32 v110 offset:10624\n"
"v_mfma_f32_16x16x32_bf16 v[112:115], a[208:211], v[72:75], v[112:115]\n"
"ds_write_addtid_b32 v111 offset:15264\n"
"v_mfma_f32_16x16x32_bf16 v[112:115], a[212:215], v[76:79], v[112:115]\n"
"v_mfma_f32_16x16x32_bf16 v[112:115], a[216:219], v[80:83], v[112:115]\n"
"v_mfma_f32_16x16x32_bf16 v[112:115], a[220:223], v[84:87], v[112:115]\n"
"v_mfma_f32_16x16x32_bf16 v[132:135], a[224:227], v[56:59], 0\n"
"s_waitcnt lgkmcnt(0)\n"
"v_mfma_f32_16x16x32_bf16 v[132:135], a[228:231], v[60:63], v[132:135]\n"
"s_barrier\n"
"v_mfma_f32_16x16x32_bf16 v[132:135], a[232:235], v[64:67], v[132:135]\n"
"ds_read2st64_b32 v[152:153], v13 offset0:0 offset1:1\n"
"v_mfma_f32_16x16x32_bf16 v[132:135], a[236:239], v[68:71], v[132:135]\n"
"ds_read_b128 v[56:59], v2 offset:1536\n"
"ds_read_b128 v[60:63], v3 offset:512\n"
"v_mfma_f32_16x16x32_bf16 v[132:135], a[240:243], v[72:75], v[132:135]\n"
"ds_read_b128 v[136:139], v4 offset:2560\n"
"ds_read_b128 v[140:143], v2 offset:1792\n"
"v_mfma_f32_16x16x32_bf16 v[132:135], a[244:247], v[76:79], v[132:135]\n"
"v_mfma_f32_16x16x32_bf16 v[132:135], a[248:251], v[80:83], v[132:135]\n"
"ds_read_b128 v[144:147], v3 offset:768\n"
"v_mfma_f32_16x16x32_bf16 v[132:135], a[252:255], v[84:87], v[132:135]\n"
"s_waitcnt vmcnt(9)\n"
"ds_read_b128 v[148:151], v4 offset:2816\n"
"v_mfma_f32_16x16x32_bf16 v[76:79], v[88:91], a[0:3], 0\n"
"v_mfma_f32_16x16x32_bf16 v[76:79], v[92:95], a[4:7], v[76:79]\n"
"s_waitcnt vmcnt(6)\n"
"v_mfma_f32_16x16x32_bf16 v[76:79], v[36:39], a[8:11], v[76:79]\n"
"s_waitcnt lgkmcnt(3)\n"
"v_add_f32 v56, v56, v60\n"
"v_add_f32 v57, v57, v61\n"
"v_mfma_f32_16x16x32_bf16 v[76:79], v[40:43], a[12:15], v[76:79]\n"
"s_waitcnt vmcnt(4)\n"
"v_add_f32 v58, v58, v62\n"
"v_add_f32 v59, v59, v63\n"
"v_mfma_f32_16x16x32_bf16 v[76:79], v[44:47], a[16:19], v[76:79]\n"
"v_add_f32 v56, v56, v136\n"
"v_mfma_f32_16x16x32_bf16 v[76:79], v[48:51], a[20:23], v[76:79]\n"
"s_waitcnt vmcnt(2)\n"
"v_add_f32 v57, v57, v137\n"
"v_add_f32 v58, v58, v138\n"
"v_mfma_f32_16x16x32_bf16 v[76:79], v[52:55], a[24:27], v[76:79]\n"
"v_add_f32 v59, v59, v139\n"
"v_add_f32 v56, v56, v112\n"
"v_mfma_f32_16x16x32_bf16 v[76:79], v[96:99], a[28:31], v[76:79]\n"
"v_add_f32 v57, v57, v113\n"
"v_add_f32 v58, v58, v114\n"
"v_mfma_f32_16x16x32_bf16 v[60:63], v[88:91], a[32:35], 0\n"
"v_add_f32 v59, v59, v115\n"
"v_mfma_f32_16x16x32_bf16 v[60:63], v[92:95], a[36:39], v[60:63]\n"
"ds_write_addtid_b32 v76 offset:1536\n"
"v_mfma_f32_16x16x32_bf16 v[60:63], v[36:39], a[40:43], v[60:63]\n"
"s_add_u32 s93, s50, 0xffe80000\n"
"s_and_b32 s94, s93, 0xfbffffff\n"
"ds_write_addtid_b32 v77 offset:6176\n"
"v_mfma_f32_16x16x32_bf16 v[60:63], v[40:43], a[44:47], v[60:63]\n"
"ds_write_addtid_b32 v78 offset:10880\n"
"v_fma_f32 v80, v18, v153, v26\n"
"v_mfma_f32_16x16x32_bf16 v[60:63], v[44:47], a[48:51], v[60:63]\n"
"ds_write_addtid_b32 v79 offset:15520\n"
"v_fma_f32 v81, v19, v153, v27\n"
"v_mfma_f32_16x16x32_bf16 v[60:63], v[48:51], a[52:55], v[60:63]\n"
"v_fma_f32 v82, v20, v153, v28\n"
"v_fma_f32 v83, v21, v153, v29\n"
"v_mfma_f32_16x16x32_bf16 v[60:63], v[52:55], a[56:59], v[60:63]\n"
"v_fma_f32 v56, v56, v152, v80\n"
"v_fma_f32 v57, v57, v152, v81\n"
"v_mfma_f32_16x16x32_bf16 v[60:63], v[96:99], a[60:63], v[60:63]\n"
"v_fma_f32 v58, v58, v152, v82\n"
"v_mfma_f32_16x16x32_bf16 v[84:87], v[88:91], a[64:67], 0\n"
"v_fma_f32 v59, v59, v152, v83\n"
"v_mul_f32_e64 v56, v56, |v56|\n"
"v_mfma_f32_16x16x32_bf16 v[84:87], v[92:95], a[68:71], v[84:87]\n"
"ds_write_addtid_b32 v60 offset:1792\n"
"v_mul_f32_e64 v57, v57, |v57|\n"
"v_mfma_f32_16x16x32_bf16 v[84:87], v[36:39], a[72:75], v[84:87]\n"
"ds_write_addtid_b32 v61 offset:6432\n"
"v_mul_f32_e64 v58, v58, |v58|\n"
"v_mfma_f32_16x16x32_bf16 v[84:87], v[40:43], a[76:79], v[84:87]\n"
"ds_write_addtid_b32 v62 offset:11136\n"
"v_mfma_f32_16x16x32_bf16 v[84:87], v[44:47], a[80:83], v[84:87]\n"
"ds_write_addtid_b32 v63 offset:15776\n"
"v_mul_f32_e64 v59, v59, |v59|\n"
"v_mfma_f32_16x16x32_bf16 v[84:87], v[48:51], a[84:87], v[84:87]\n"
"v_cvt_pk_bf16_f32 v136, v56, v57\n"
"v_cvt_pk_bf16_f32 v137, v58, v59\n"
"v_mfma_f32_16x16x32_bf16 v[84:87], v[52:55], a[88:91], v[84:87]\n"
"v_pk_max_i16 v136, v136, 0\n"
"v_pk_max_i16 v137, v137, 0\n"
"v_mfma_f32_16x16x32_bf16 v[84:87], v[96:99], a[92:95], v[84:87]\n"
"s_waitcnt lgkmcnt(8)\n"
"v_add_f32 v140, v140, v144\n"
"v_add_f32 v141, v141, v145\n"
"v_mfma_f32_16x16x32_bf16 v[56:59], v[88:91], a[96:99], 0\n"
"v_add_f32 v142, v142, v146\n"
"v_add_f32 v143, v143, v147\n"
"v_mfma_f32_16x16x32_bf16 v[56:59], v[92:95], a[100:103], v[56:59]\n"
"ds_write_addtid_b32 v84 offset:2048\n"
"v_add_f32 v140, v140, v148\n"
"v_mfma_f32_16x16x32_bf16 v[56:59], v[36:39], a[104:107], v[56:59]\n"
"ds_write_addtid_b32 v85 offset:6688\n"
"v_add_f32 v141, v141, v149\n"
"v_mfma_f32_16x16x32_bf16 v[56:59], v[40:43], a[108:111], v[56:59]\n"
"ds_write_addtid_b32 v86 offset:11392\n"
"v_add_f32 v142, v142, v150\n"
"v_mfma_f32_16x16x32_bf16 v[56:59], v[44:47], a[112:115], v[56:59]\n"
"ds_write_addtid_b32 v87 offset:16032\n"
"v_add_f32 v143, v143, v151\n"
"v_mfma_f32_16x16x32_bf16 v[56:59], v[48:51], a[116:119], v[56:59]\n"
"s_waitcnt vmcnt(0)\n"
"buffer_store_dwordx2 v[0:1], v16, s[40:43], s95 offen offset:0 sc1\n"
"v_add_f32 v140, v140, v132\n"
"v_mfma_f32_16x16x32_bf16 v[56:59], v[52:55], a[120:123], v[56:59]\n"
"v_add_f32 v141, v141, v133\n"
"v_add_f32 v142, v142, v134\n"
"v_mfma_f32_16x16x32_bf16 v[56:59], v[96:99], a[124:127], v[56:59]\n"
"v_add_f32 v143, v143, v135\n"
"v_fma_f32 v60, v22, v153, v30\n"
"v_mfma_f32_16x16x32_bf16 v[84:87], v[88:91], a[128:131], 0\n"
"v_fma_f32 v61, v23, v153, v31\n"
"v_fma_f32 v62, v24, v153, v32\n"
"v_mfma_f32_16x16x32_bf16 v[84:87], v[92:95], a[132:135], v[84:87]\n"
"ds_write_addtid_b32 v56 offset:2304\n"
"v_fma_f32 v63, v25, v153, v33\n"
"v_mfma_f32_16x16x32_bf16 v[84:87], v[36:39], a[136:139], v[84:87]\n"
"ds_write_addtid_b32 v57 offset:6944\n"
"v_fma_f32 v140, v140, v152, v60\n"
"v_mfma_f32_16x16x32_bf16 v[84:87], v[40:43], a[140:143], v[84:87]\n"
"ds_write_addtid_b32 v58 offset:11648\n"
"v_fma_f32 v141, v141, v152, v61\n"
"v_mfma_f32_16x16x32_bf16 v[84:87], v[44:47], a[144:147], v[84:87]\n"
"ds_write_addtid_b32 v59 offset:16288\n"
"v_mfma_f32_16x16x32_bf16 v[84:87], v[48:51], a[148:151], v[84:87]\n"
"v_mfma_f32_16x16x32_bf16 v[84:87], v[52:55], a[152:155], v[84:87]\n"
"v_mfma_f32_16x16x32_bf16 v[84:87], v[96:99], a[156:159], v[84:87]\n"
"v_mfma_f32_16x16x32_bf16 v[56:59], v[88:91], a[160:163], 0\n"
"v_mfma_f32_16x16x32_bf16 v[56:59], v[92:95], a[164:167], v[56:59]\n"
"ds_write_addtid_b32 v84 offset:2560\n"
"v_mfma_f32_16x16x32_bf16 v[56:59], v[36:39], a[168:171], v[56:59]\n"
"ds_write_addtid_b32 v85 offset:7200\n"
"v_mfma_f32_16x16x32_bf16 v[56:59], v[40:43], a[172:175], v[56:59]\n"
"ds_write_addtid_b32 v86 offset:11904\n"
"v_mfma_f32_16x16x32_bf16 v[56:59], v[44:47], a[176:179], v[56:59]\n"
"ds_write_addtid_b32 v87 offset:16544\n"
"v_fma_f32 v142, v142, v152, v62\n"
"v_mfma_f32_16x16x32_bf16 v[56:59], v[48:51], a[180:183], v[56:59]\n"
"v_mfma_f32_16x16x32_bf16 v[56:59], v[52:55], a[184:187], v[56:59]\n"
"v_fma_f32 v143, v143, v152, v63\n"
"v_mfma_f32_16x16x32_bf16 v[56:59], v[96:99], a[188:191], v[56:59]\n"
"v_mfma_f32_16x16x32_bf16 v[128:131], a[192:195], v[88:91], 0\n"
"v_mfma_f32_16x16x32_bf16 v[128:131], a[196:199], v[92:95], v[128:131]\n"
"ds_write_addtid_b32 v56 offset:2816\n"
"v_mfma_f32_16x16x32_bf16 v[128:131], a[200:203], v[36:39], v[128:131]\n"
"ds_write_addtid_b32 v57 offset:7456\n"
"v_mfma_f32_16x16x32_bf16 v[128:131], a[204:207], v[40:43], v[128:131]\n"
"ds_write_addtid_b32 v58 offset:12160\n"
"v_mul_f32_e64 v140, v140, |v140|\n"
"v_mfma_f32_16x16x32_bf16 v[128:131], a[208:211], v[44:47], v[128:131]\n"
"ds_write_addtid_b32 v59 offset:16800\n"
"v_mul_f32_e64 v141, v141, |v141|\n"
"v_mfma_f32_16x16x32_bf16 v[128:131], a[212:215], v[48:51], v[128:131]\n"
"v_mfma_f32_16x16x32_bf16 v[128:131], a[216:219], v[52:55], v[128:131]\n"
"v_mul_f32_e64 v142, v142, |v142|\n"
"v_mfma_f32_16x16x32_bf16 v[128:131], a[220:223], v[96:99], v[128:131]\n"
"v_mfma_f32_16x16x32_bf16 v[132:135], a[224:227], v[88:91], 0\n"
"s_waitcnt lgkmcnt(0)\n"
"v_mul_f32_e64 v143, v143, |v143|\n"
"v_mfma_f32_16x16x32_bf16 v[132:135], a[228:231], v[92:95], v[132:135]\n"
"s_barrier\n"
"v_mfma_f32_16x16x32_bf16 v[132:135], a[232:235], v[36:39], v[132:135]\n"
"v_mfma_f32_16x16x32_bf16 v[132:135], a[236:239], v[40:43], v[132:135]\n"
"v_mfma_f32_16x16x32_bf16 v[132:135], a[240:243], v[44:47], v[132:135]\n"
"v_mfma_f32_16x16x32_bf16 v[132:135], a[244:247], v[48:51], v[132:135]\n"
"v_mfma_f32_16x16x32_bf16 v[132:135], a[248:251], v[52:55], v[132:135]\n"
"v_cvt_pk_bf16_f32 v138, v140, v141\n"
"v_mfma_f32_16x16x32_bf16 v[132:135], a[252:255], v[96:99], v[132:135]\n"
"v_cvt_pk_bf16_f32 v139, v142, v143\n"
"v_pk_max_i16 v138, v138, 0\n"
"ds_read2st64_b32 v[140:141], v14 offset0:0 offset1:1\n"
"v_pk_max_i16 v139, v139, 0\n"
"ds_read_b128 v[56:59], v2 offset:0\n"
"ds_read_b128 v[52:55], v3 offset:2048\n"
"ds_read_b128 v[60:63], v4 offset:1024\n"
"ds_read_b128 v[84:87], v2 offset:256\n"
"ds_read_b128 v[88:91], v3 offset:2304\n"
"ds_read_b128 v[92:95], v4 offset:1280\n"
"s_waitcnt lgkmcnt(3)\n"
"buffer_store_dwordx4 v[136:139], v15, s[40:43], s94 offen sc1\n"
"v_add_f32 v56, v56, v52\n"
"v_add_f32 v57, v57, v53\n"
"v_add_f32 v58, v58, v54\n"
"v_add_f32 v59, v59, v55\n"
"v_add_f32 v56, v56, v60\n"
"v_add_f32 v57, v57, v61\n"
"v_add_f32 v58, v58, v62\n"
"v_add_f32 v59, v59, v63\n"
"v_add_f32 v56, v56, v128\n"
"v_add_f32 v57, v57, v129\n"
"v_add_f32 v58, v58, v130\n"
"v_add_f32 v59, v59, v131\n"
"s_add_u32 s94, s50, 0x0\n"
"s_and_b32 s95, s94, 0xfbffffff\n"
"v_fma_f32 v60, v18, v141, v26\n"
"v_fma_f32 v61, v19, v141, v27\n"
"v_fma_f32 v62, v20, v141, v28\n"
"v_fma_f32 v63, v21, v141, v29\n"
"v_fma_f32 v56, v56, v140, v60\n"
"v_fma_f32 v57, v57, v140, v61\n"
"v_fma_f32 v58, v58, v140, v62\n"
"v_fma_f32 v59, v59, v140, v63\n"
"v_mul_f32_e64 v56, v56, |v56|\n"
"v_mul_f32_e64 v57, v57, |v57|\n"
"v_mul_f32_e64 v58, v58, |v58|\n"
"v_mul_f32_e64 v59, v59, |v59|\n"
"v_cvt_pk_bf16_f32 v128, v56, v57\n"
"v_cvt_pk_bf16_f32 v129, v58, v59\n"
"v_pk_max_i16 v128, v128, 0\n"
"v_pk_max_i16 v129, v129, 0\n"
"s_waitcnt lgkmcnt(0)\n"
"v_add_f32 v84, v84, v88\n"
"v_add_f32 v85, v85, v89\n"
"v_add_f32 v86, v86, v90\n"
"v_add_f32 v87, v87, v91\n"
"v_add_f32 v84, v84, v92\n"
"v_add_f32 v85, v85, v93\n"
"v_add_f32 v86, v86, v94\n"
"v_add_f32 v87, v87, v95\n"
"v_add_f32 v84, v84, v132\n"
"v_add_f32 v85, v85, v133\n"
"v_add_f32 v86, v86, v134\n"
"v_add_f32 v87, v87, v135\n"
"v_fma_f32 v60, v22, v141, v30\n"
"v_fma_f32 v61, v23, v141, v31\n"
"v_fma_f32 v62, v24, v141, v32\n"
"v_fma_f32 v63, v25, v141, v33\n"
"v_fma_f32 v84, v84, v140, v60\n"
"v_fma_f32 v85, v85, v140, v61\n"
"v_fma_f32 v86, v86, v140, v62\n"
"v_fma_f32 v87, v87, v140, v63\n"
"v_mul_f32_e64 v84, v84, |v84|\n"
"v_mul_f32_e64 v85, v85, |v85|\n"
"v_mul_f32_e64 v86, v86, |v86|\n"
"v_mul_f32_e64 v87, v87, |v87|\n"
"v_cvt_pk_bf16_f32 v130, v84, v85\n"
"v_cvt_pk_bf16_f32 v131, v86, v87\n"
"v_pk_max_i16 v130, v130, 0\n"
"v_pk_max_i16 v131, v131, 0\n"
"s_waitcnt vmcnt(0)\n"
"buffer_store_dwordx2 v[0:1], v16, s[40:43], s93 offen offset:0 sc1\n"
"buffer_store_dwordx4 v[128:131], v15, s[40:43], s95 offen sc1\n"
"s_waitcnt vmcnt(0)\n"
"buffer_store_dwordx2 v[0:1], v16, s[40:43], s94 offen offset:0 sc1\n"
"s_branch .LF_end\n"
".LF_t1:\n"
"v_add_f32 v124, v124, v126\n"
"v_mfma_f32_16x16x32_bf16 v[128:131], a[196:199], v[92:95], v[128:131]\n"
"ds_write_addtid_b32 v56 offset:2816\n"
"v_add_f32 v125, v125, v127\n"
"v_mfma_f32_16x16x32_bf16 v[128:131], a[200:203], v[36:39], v[128:131]\n"
"ds_write_addtid_b32 v57 offset:7456\n"
"v_mfma_f32_16x16x32_bf16 v[128:131], a[204:207], v[40:43], v[128:131]\n"
"ds_write_addtid_b32 v58 offset:12160\n"
"v_mul_f32_e64 v140, v140, |v140|\n"
"v_mfma_f32_16x16x32_bf16 v[128:131], a[208:211], v[44:47], v[128:131]\n"
"ds_write_addtid_b32 v59 offset:16800\n"
"v_mul_f32_e64 v141, v141, |v141|\n"
"v_mfma_f32_16x16x32_bf16 v[128:131], a[212:215], v[48:51], v[128:131]\n"
"v_permlane16_swap_b32 v124, v125\n"
"v_mfma_f32_16x16x32_bf16 v[128:131], a[216:219], v[52:55], v[128:131]\n"
"v_add_f32 v124, v124, v125\n"
"v_mul_f32_e64 v142, v142, |v142|\n"
"v_mfma_f32_16x16x32_bf16 v[128:131], a[220:223], v[96:99], v[128:131]\n"
"v_add_f32_dpp v125, v124, v124 row_ror:8 row_mask:0xf bank_mask:0xf\n"
"v_mfma_f32_16x16x32_bf16 v[132:135], a[224:227], v[88:91], 0\n"
"s_waitcnt lgkmcnt(0)\n"
"v_mov_b32 v124, v125\n"
"v_mul_f32_e64 v143, v143, |v143|\n"
"v_mfma_f32_16x16x32_bf16 v[132:135], a[228:231], v[92:95], v[132:135]\n"
"s_barrier\n"
"v_mfma_f32_16x16x32_bf16 v[132:135], a[232:235], v[36:39], v[132:135]\n"
"v_permlane16_swap_b32 v125, v124\n"
"v_mfma_f32_16x16x32_bf16 v[132:135], a[236:239], v[40:43], v[132:135]\n"
"v_mul_f32 v125, 0xba800000, v125\n"
"v_mfma_f32_16x16x32_bf16 v[132:135], a[240:243], v[44:47], v[132:135]\n"
"v_mfma_f32_16x16x32_bf16 v[132:135], a[244:247], v[48:51], v[132:135]\n"
"v_fma_f32 v124, v124, s62, v17\n"
"v_fma_f32 v124, -v125, v125, v124\n"
"v_mfma_f32_16x16x32_bf16 v[132:135], a[248:251], v[52:55], v[132:135]\n"
"v_max_f32 v124, v17, v124\n"
"v_cvt_pk_bf16_f32 v138, v140, v141\n"
"v_mfma_f32_16x16x32_bf16 v[132:135], a[252:255], v[96:99], v[132:135]\n"
"s_waitcnt vmcnt(8)\n"
"v_rsq_f32 v124, v124\n"
"v_mfma_f32_16x16x32_bf16 v[48:51], v[100:103], a[0:3], 0\n"
"v_mul_f32 v125, v125, v124\n"
"v_mfma_f32_16x16x32_bf16 v[48:51], v[104:107], a[4:7], v[48:51]\n"
"s_waitcnt vmcnt(5)\n"
"ds_write_addtid_b32 v124 offset:18592\n"
"ds_write_addtid_b32 v125 offset:18848\n"
"v_mfma_f32_16x16x32_bf16 v[48:51], v[108:111], a[8:11], v[48:51]\n"
"v_cvt_pk_bf16_f32 v139, v142, v143\n"
"v_pk_max_i16 v138, v138, 0\n"
"v_mfma_f32_16x16x32_bf16 v[48:51], v[64:67], a[12:15], v[48:51]\n"
"s_waitcnt vmcnt(3)\n"
"ds_read2st64_b32 v[140:141], v14 offset0:0 offset1:1\n"
"v_pk_max_i16 v139, v139, 0\n"
"v_mfma_f32_16x16x32_bf16 v[48:51], v[68:71], a[16:19], v[48:51]\n"
"ds_read_b128 v[56:59], v2 offset:0\n"
"ds_read_b128 v[52:55], v3 offset:2048\n"
"v_mfma_f32_16x16x32_bf16 v[48:51], v[72:75], a[20:23], v[48:51]\n"
"s_waitcnt vmcnt(1)\n"
"ds_read_b128 v[60:63], v4 offset:1024\n"
"v_mfma_f32_16x16x32_bf16 v[48:51], v[76:79], a[24:27], v[48:51]\n"
"ds_read_b128 v[84:87], v2 offset:256\n"
"ds_read_b128 v[88:91], v3 offset:2304\n"
"v_mfma_f32_16x16x32_bf16 v[48:51], v[80:83], a[28:31], v[48:51]\n"
"ds_read_b128 v[92:95], v4 offset:1280\n"
"v_mfma_f32_16x16x32_bf16 v[96:99], v[100:103], a[32:35], 0\n"
"v_mfma_f32_16x16x32_bf16 v[96:99], v[104:107], a[36:39], v[96:99]\n"
"ds_write_addtid_b32 v48 offset:0\n"
"v_mfma_f32_16x16x32_bf16 v[96:99], v[108:111], a[40:43], v[96:99]\n"
"ds_write_addtid_b32 v49 offset:4640\n"
"v_mfma_f32_16x16x32_bf16 v[96:99], v[64:67], a[44:47], v[96:99]\n"
"s_waitcnt lgkmcnt(5)\n"
"buffer_store_dwordx4 v[136:139], v15, s[40:43], s94 offen sc1\n"
"ds_write_addtid_b32 v50 offset:9344\n"
"v_add_f32 v56, v56, v52\n"
"v_add_f32 v57, v57, v53\n"
"v_mfma_f32_16x16x32_bf16 v[96:99], v[68:71], a[48:51], v[96:99]\n"
"ds_write_addtid_b32 v51 offset:13984\n"
"v_add_f32 v58, v58, v54\n"
"v_mfma_f32_16x16x32_bf16 v[96:99], v[72:75], a[52:55], v[96:99]\n"
"v_add_f32 v59, v59, v55\n"
"v_add_f32 v56, v56, v60\n"
"v_mfma_f32_16x16x32_bf16 v[96:99], v[76:79], a[56:59], v[96:99]\n"
"v_add_f32 v57, v57, v61\n"
"v_add_f32 v58, v58, v62\n"
"v_mfma_f32_16x16x32_bf16 v[96:99], v[80:83], a[60:63], v[96:99]\n"
"v_add_f32 v59, v59, v63\n"
"v_add_f32 v56, v56, v128\n"
"v_mfma_f32_16x16x32_bf16 v[124:127], v[100:103], a[64:67], 0\n"
"v_add_f32 v57, v57, v129\n"
"v_mfma_f32_16x16x32_bf16 v[124:127], v[104:107], a[68:71], v[124:127]\n"
"ds_write_addtid_b32 v96 offset:256\n"
"v_add_f32 v58, v58, v130\n"
"v_mfma_f32_16x16x32_bf16 v[124:127], v[108:111], a[72:75], v[124:127]\n"
"ds_write_addtid_b32 v97 offset:4896\n"
"v_add_f32 v59, v59, v131\n"
"v_mfma_f32_16x16x32_bf16 v[124:127], v[64:67], a[76:79], v[124:127]\n"
"s_add_u32 s94, s50, 0xffe80000\n"
"s_and_b32 s95, s94, 0xfbffffff\n"
"ds_write_addtid_b32 v98 offset:9600\n"
"v_mfma_f32_16x16x32_bf16 v[124:127], v[68:71], a[80:83], v[124:127]\n"
"ds_write_addtid_b32 v99 offset:14240\n"
"v_mfma_f32_16x16x32_bf16 v[124:127], v[72:75], a[84:87], v[124:127]\n"
"v_fma_f32 v60, v18, v141, v26\n"
"v_fma_f32 v61, v19, v141, v27\n"
"v_mfma_f32_16x16x32_bf16 v[124:127], v[76:79], a[88:91], v[124:127]\n"
"v_fma_f32 v62, v20, v141, v28\n"
"v_fma_f32 v63, v21, v141, v29\n"
"v_mfma_f32_16x16x32_bf16 v[124:127], v[80:83], a[92:95], v[124:127]\n"
"v_fma_f32 v56, v56, v140, v60\n"
"v_fma_f32 v57, v57, v140, v61\n"
"v_mfma_f32_16x16x32_bf16 v[96:99], v[100:103], a[96:99], 0\n"
"v_fma_f32 v58, v58, v140, v62\n"
"v_fma_f32 v59, v59, v140, v63\n"
"v_mfma_f32_16x16x32_bf16 v[96:99], v[104:107], a[100:103], v[96:99]\n"
"ds_write_addtid_b32 v124 offset:512\n"
"v_mul_f32_e64 v56, v56, |v56|\n"
"v_mfma_f32_16x16x32_bf16 v[96:99], v[108:111], a[104:107], v[96:99]\n"
"ds_write_addtid_b32 v125 offset:5152\n"
"v_mul_f32_e64 v57, v57, |v57|\n"
"v_mfma_f32_16x16x32_bf16 v[96:99], v[64:67], a[108:111], v[96:99]\n"
"ds_write_addtid_b32 v126 offset:9856\n"
"v_mul_f32_e64 v58, v58, |v58|\n"
"v_mfma_f32_16x16x32_bf16 v[96:99], v[68:71], a[112:115], v[96:99]\n"
"ds_write_addtid_b32 v127 offset:14496\n"
"v_mul_f32_e64 v59, v59, |v59|\n"
"v_mfma_f32_16x16x32_bf16 v[96:99], v[72:75], a[116:119], v[96:99]\n"
"v_cvt_pk_bf16_f32 v128, v56, v57\n"
"v_cvt_pk_bf16_f32 v129, v58, v59\n"
"v_mfma_f32_16x16x32_bf16 v[96:99], v[76:79], a[120:123], v[96:99]\n"
"v_pk_max_i16 v128, v128, 0\n"
"v_pk_max_i16 v129, v129, 0\n"
"v_mfma_f32_16x16x32_bf16 v[96:99], v[80:83], a[124:127], v[96:99]\n"
"s_waitcnt lgkmcnt(12)\n"
"v_add_f32 v84, v84, v88\n"
"v_add_f32 v85, v85, v89\n"
"v_mfma_f32_16x16x32_bf16 v[56:59], v[100:103], a[128:131], 0\n"
"v_add_f32 v86, v86, v90\n"
"v_add_f32 v87, v87, v91\n"
"v_mfma_f32_16x16x32_bf16 v[56:59], v[104:107], a[132:135], v[56:59]\n"
"ds_write_addtid_b32 v96 offset:768\n"
"v_add_f32 v84, v84, v92\n"
"v_mfma_f32_16x16x32_bf16 v[56:59], v[108:111], a[136:139], v[56:59]\n"
"ds_write_addtid_b32 v97 offset:5408\n"
"v_add_f32 v85, v85, v93\n"
"v_mfma_f32_16x16x32_bf16 v[56:59], v[64:67], a[140:143], v[56:59]\n"
"ds_write_addtid_b32 v98 offset:10112\n"
"v_add_f32 v86, v86, v94\n"
"v_mfma_f32_16x16x32_bf16 v[56:59], v[68:71], a[144:147], v[56:59]\n"
"ds_write_addtid_b32 v99 offset:14752\n"
"v_add_f32 v87, v87, v95\n"
"v_mfma_f32_16x16x32_bf16 v[56:59], v[72:75], a[148:151], v[56:59]\n"
"v_add_f32 v84, v84, v132\n"
"v_add_f32 v85, v85, v133\n"
"v_mfma_f32_16x16x32_bf16 v[56:59], v[76:79], a[152:155], v[56:59]\n"
"v_add_f32 v86, v86, v134\n"
"v_add_f32 v87, v87, v135\n"
"v_mfma_f32_16x16x32_bf16 v[56:59], v[80:83], a[156:159], v[56:59]\n"
"v_fma_f32 v60, v22, v141, v30\n"
"v_fma_f32 v61, v23, v141, v31\n"
"v_mfma_f32_16x16x32_bf16 v[88:91], v[100:103], a[160:163], 0\n"
"v_fma_f32 v62, v24, v141, v32\n"
"v_fma_f32 v63, v25, v141, v33\n"
"v_mfma_f32_16x16x32_bf16 v[88:91], v[104:107], a[164:167], v[88:91]\n"
"ds_write_addtid_b32 v56 offset:1024\n"
"v_fma_f32 v84, v84, v140, v60\n"
"v_mfma_f32_16x16x32_bf16 v[88:91], v[108:111], a[168:171], v[88:91]\n"
"ds_write_addtid_b32 v57 offset:5664\n"
"v_fma_f32 v85, v85, v140, v61\n"
"v_mfma_f32_16x16x32_bf16 v[88:91], v[64:67], a[172:175], v[88:91]\n"
"ds_write_addtid_b32 v58 offset:10368\n"
"v_fma_f32 v86, v86, v140, v62\n"
"v_mfma_f32_16x16x32_bf16 v[88:91], v[68:71], a[176:179], v[88:91]\n"
"ds_write_addtid_b32 v59 offset:15008\n"
"v_fma_f32 v87, v87, v140, v63\n"
"v_mfma_f32_16x16x32_bf16 v[88:91], v[72:75], a[180:183], v[88:91]\n"
"v_mul_f32_e64 v84, v84, |v84|\n"
"v_mul_f32_e64 v85, v85, |v85|\n"
"v_mfma_f32_16x16x32_bf16 v[88:91], v[76:79], a[184:187], v[88:91]\n"
"v_mul_f32_e64 v86, v86, |v86|\n"
"v_mfma_f32_16x16x32_bf16 v[88:91], v[80:83], a[188:191], v[88:91]\n"
"v_mul_f32_e64 v87, v87, |v87|\n"
"v_cvt_pk_bf16_f32 v130, v84, v85\n"
"v_mfma_f32_16x16x32_bf16 v[92:95], a[192:195], v[100:103], 0\n"
"v_cvt_pk_bf16_f32 v131, v86, v87\n"
"v_mfma_f32_16x16x32_bf16 v[92:95], a[196:199], v[104:107], v[92:95]\n"
"ds_write_addtid_b32 v88 offset:1280\n"
"v_mfma_f32_16x16x32_bf16 v[92:95], a[200:203], v[108:111], v[92:95]\n"
"ds_write_addtid_b32 v89 offset:5920\n"
"v_pk_max_i16 v130, v130, 0\n"
"v_mfma_f32_16x16x32_bf16 v[92:95], a[204:207], v[64:67], v[92:95]\n"
"ds_write_addtid_b32 v90 offset:10624\n"
"v_pk_max_i16 v131, v131, 0\n"
"v_mfma_f32_16x16x32_bf16 v[92:95], a[208:211], v[68:71], v[92:95]\n"
"ds_write_addtid_b32 v91 offset:15264\n"
"v_mfma_f32_16x16x32_bf16 v[92:95], a[212:215], v[72:75], v[92:95]\n"
"v_mfma_f32_16x16x32_bf16 v[92:95], a[216:219], v[76:79], v[92:95]\n"
"v_mfma_f32_16x16x32_bf16 v[92:95], a[220:223], v[80:83], v[92:95]\n"
"v_mfma_f32_16x16x32_bf16 v[88:91], a[224:227], v[100:103], 0\n"
"s_waitcnt lgkmcnt(0)\n"
"v_mfma_f32_16x16x32_bf16 v[88:91], a[228:231], v[104:107], v[88:91]\n"
"s_barrier\n"
"v_mfma_f32_16x16x32_bf16 v[88:91], a[232:235], v[108:111], v[88:91]\n"
"ds_read2st64_b32 v[136:137], v11 offset0:0 offset1:1\n"
"ds_read_b128 v[96:99], v2 offset:1536\n"
"v_mfma_f32_16x16x32_bf16 v[88:91], a[236:239], v[64:67], v[88:91]\n"
"s_waitcnt vmcnt(0)\n"
"v_mfma_f32_16x16x32_bf16 v[88:91], a[240:243], v[68:71], v[88:91]\n"
"ds_read_b128 v[84:87], v3 offset:512\n"
"v_mfma_f32_16x16x32_bf16 v[88:91], a[244:247], v[72:75], v[88:91]\n"
"ds_read_b128 v[100:103], v4 offset:2560\n"
"ds_read_b128 v[104:107], v2 offset:1792\n"
"v_mfma_f32_16x16x32_bf16 v[88:91], a[248:251], v[76:79], v[88:91]\n"
"ds_read_b128 v[108:111], v3 offset:768\n"
"ds_read_b128 v[124:127], v4 offset:2816\n"
"v_mfma_f32_16x16x32_bf16 v[88:91], a[252:255], v[80:83], v[88:91]\n"
"buffer_store_dwordx2 v[0:1], v16, s[40:43], s93 offen offset:0 sc1\n"
"buffer_store_dwordx4 v[128:131], v15, s[40:43], s95 offen sc1\n"
"s_waitcnt lgkmcnt(3)\n"
"v_add_f32 v96, v96, v84\n"
"v_add_f32 v97, v97, v85\n"
"v_add_f32 v98, v98, v86\n"
"v_add_f32 v99, v99, v87\n"
"v_add_f32 v96, v96, v100\n"
"v_add_f32 v97, v97, v101\n"
"v_add_f32 v98, v98, v102\n"
"v_add_f32 v99, v99, v103\n"
"v_add_f32 v96, v96, v92\n"
"v_add_f32 v97, v97, v93\n"
"v_add_f32 v98, v98, v94\n"
"v_add_f32 v99, v99, v95\n"
"s_add_u32 s93, s50, 0x0\n"
"s_and_b32 s95, s93, 0xfbffffff\n"
"v_fma_f32 v84, v18, v137, v26\n"
"v_fma_f32 v85, v19, v137, v27\n"
"v_fma_f32 v86, v20, v137, v28\n"
"v_fma_f32 v87, v21, v137, v29\n"
"v_fma_f32 v96, v96, v136, v84\n"
"v_fma_f32 v97, v97, v136, v85\n"
"v_fma_f32 v98, v98, v136, v86\n"
"v_fma_f32 v99, v99, v136, v87\n"
"v_mul_f32_e64 v96, v96, |v96|\n"
"v_mul_f32_e64 v97, v97, |v97|\n"
"v_mul_f32_e64 v98, v98, |v98|\n"
"v_mul_f32_e64 v99, v99, |v99|\n"
"v_cvt_pk_bf16_f32 v100, v96, v97\n"
"v_cvt_pk_bf16_f32 v101, v98, v99\n"
"v_pk_max_i16 v100, v100, 0\n"
"v_pk_max_i16 v101, v101, 0\n"
"s_waitcnt lgkmcnt(0)\n"
"v_add_f32 v104, v104, v108\n"
"v_add_f32 v105, v105, v109\n"
"v_add_f32 v106, v106, v110\n"
"v_add_f32 v107, v107, v111\n"
"v_add_f32 v104, v104, v124\n"
"v_add_f32 v105, v105, v125\n"
"v_add_f32 v106, v106, v126\n"
"v_add_f32 v107, v107, v127\n"
"v_add_f32 v104, v104, v88\n"
"v_add_f32 v105, v105, v89\n"
"v_add_f32 v106, v106, v90\n"
"v_add_f32 v107, v107, v91\n"
"v_fma_f32 v96, v22, v137, v30\n"
"v_fma_f32 v97, v23, v137, v31\n"
"v_fma_f32 v98, v24, v137, v32\n"
"v_fma_f32 v99, v25, v137, v33\n"
"v_fma_f32 v104, v104, v136, v96\n"
"v_fma_f32 v105, v105, v136, v97\n"
"v_fma_f32 v106, v106, v136, v98\n"
"v_fma_f32 v107, v107, v136, v99\n"
"v_mul_f32_e64 v104, v104, |v104|\n"
"v_mul_f32_e64 v105, v105, |v105|\n"
"v_mul_f32_e64 v106, v106, |v106|\n"
"v_mul_f32_e64 v107, v107, |v107|\n"
"v_cvt_pk_bf16_f32 v102, v104, v105\n"
"v_cvt_pk_bf16_f32 v103, v106, v107\n"
"v_pk_max_i16 v102, v102, 0\n"
"v_pk_max_i16 v103, v103, 0\n"
"buffer_store_dwordx4 v[100:103], v15, s[40:43], s95 offen sc1\n"
"s_waitcnt vmcnt(1)\n"
"buffer_store_dwordx2 v[0:1], v16, s[40:43], s94 offen offset:0 sc1\n"
"s_waitcnt vmcnt(1)\n"
"buffer_store_dwordx2 v[0:1], v16, s[40:43], s93 offen offset:0 sc1\n"
"s_branch .LF_end\n"
".LF_t2:\n"
"v_cvt_pk_bf16_f32 v131, v86, v87\n"
"v_mfma_f32_16x16x32_bf16 v[92:95], a[196:199], v[104:107], v[92:95]\n"
"ds_write_addtid_b32 v88 offset:1280\n"
"v_mfma_f32_16x16x32_bf16 v[92:95], a[200:203], v[108:111], v[92:95]\n"
"ds_write_addtid_b32 v89 offset:5920\n"
"v_pk_max_i16 v130, v130, 0\n"
"v_mfma_f32_16x16x32_bf16 v[92:95], a[204:207], v[64:67], v[92:95]\n"
"ds_write_addtid_b32 v90 offset:10624\n"
"v_pk_max_i16 v131, v131, 0\n"
"v_mfma_f32_16x16x32_bf16 v[92:95], a[208:211], v[68:71], v[92:95]\n"
"ds_write_addtid_b32 v91 offset:15264\n"
"v_mfma_f32_16x16x32_bf16 v[92:95], a[212:215], v[72:75], v[92:95]\n"
"v_mfma_f32_16x16x32_bf16 v[92:95], a[216:219], v[76:79], v[92:95]\n"
"v_mfma_f32_16x16x32_bf16 v[92:95], a[220:223], v[80:83], v[92:95]\n"
"v_mfma_f32_16x16x32_bf16 v[88:91], a[224:227], v[100:103], 0\n"
"s_waitcnt lgkmcnt(0)\n"
"v_mfma_f32_16x16x32_bf16 v[88:91], a[228:231], v[104:107], v[88:91]\n"
"s_barrier\n"
"v_mfma_f32_16x16x32_bf16 v[88:91], a[232:235], v[108:111], v[88:91]\n"
"ds_read2st64_b32 v[136:137], v11 offset0:0 offset1:1\n"
"ds_read_b128 v[96:99], v2 offset:1536\n"
"v_mfma_f32_16x16x32_bf16 v[88:91], a[236:239], v[64:67], v[88:91]\n"
"s_waitcnt vmcnt(2)\n"
"v_mfma_f32_16x16x32_bf16 v[88:91], a[240:243], v[68:71], v[88:91]\n"
"ds_read_b128 v[84:87], v3 offset:512\n"
"v_mfma_f32_16x16x32_bf16 v[88:91], a[244:247], v[72:75], v[88:91]\n"
"ds_read_b128 v[100:103], v4 offset:2560\n"
"ds_read_b128 v[104:107], v2 offset:1792\n"
"v_mfma_f32_16x16x32_bf16 v[88:91], a[248:251], v[76:79], v[88:91]\n"
"ds_read_b128 v[108:111], v3 offset:768\n"
"ds_read_b128 v[124:127], v4 offset:2816\n"
"v_mfma_f32_16x16x32_bf16 v[88:91], a[252:255], v[80:83], v[88:91]\n"
"s_waitcnt vmcnt(8)\n"
"buffer_store_dwordx2 v[0:1], v16, s[40:43], s93 offen offset:0 sc1\n"
"v_mfma_f32_16x16x32_bf16 v[132:135], v[112:115], a[0:3], 0\n"
"buffer_store_dwordx4 v[128:131], v15, s[40:43], s95 offen sc1\n"
"v_mfma_f32_16x16x32_bf16 v[132:135], v[116:119], a[4:7], v[132:135]\n"
"s_waitcnt vmcnt(7)\n"
"v_mfma_f32_16x16x32_bf16 v[132:135], v[120:123], a[8:11], v[132:135]\n"
"v_mfma_f32_16x16x32_bf16 v[132:135], v[36:39], a[12:15], v[132:135]\n"
"s_waitcnt vmcnt(5)\n"
"v_mfma_f32_16x16x32_bf16 v[132:135], v[40:43], a[16:19], v[132:135]\n"
"s_waitcnt lgkmcnt(3)\n"
"v_add_f32 v96, v96, v84\n"
"v_add_f32 v97, v97, v85\n"
"v_mfma_f32_16x16x32_bf16 v[132:135], v[44:47], a[20:23], v[132:135]\n"
"s_waitcnt vmcnt(2)\n"
"v_add_f32 v98, v98, v86\n"
"v_mfma_f32_16x16x32_bf16 v[132:135], v[48:51], a[24:27], v[132:135]\n"
"v_add_f32 v99, v99, v87\n"
"v_add_f32 v96, v96, v100\n"
"v_mfma_f32_16x16x32_bf16 v[132:135], v[52:55], a[28:31], v[132:135]\n"
"v_add_f32 v97, v97, v101\n"
"v_mfma_f32_16x16x32_bf16 v[128:131], v[112:115], a[32:35], 0\n"
"v_add_f32 v98, v98, v102\n"
"v_add_f32 v99, v99, v103\n"
"v_mfma_f32_16x16x32_bf16 v[128:131], v[116:119], a[36:39], v[128:131]\n"
"ds_write_addtid_b32 v132 offset:1536\n"
"v_add_f32 v96, v96, v92\n"
"v_mfma_f32_16x16x32_bf16 v[128:131], v[120:123], a[40:43], v[128:131]\n"
"ds_write_addtid_b32 v133 offset:6176\n"
"v_add_f32 v97, v97, v93\n"
"v_mfma_f32_16x16x32_bf16 v[128:131], v[36:39], a[44:47], v[128:131]\n"
"ds_write_addtid_b32 v134 offset:10880\n"
"v_add_f32 v98, v98, v94\n"
"v_mfma_f32_16x16x32_bf16 v[128:131], v[40:43], a[48:51], v[128:131]\n"
"ds_write_addtid_b32 v135 offset:15520\n"
"v_add_f32 v99, v99, v95\n"
"v_mfma_f32_16x16x32_bf16 v[128:131], v[44:47], a[52:55], v[128:131]\n"
"s_add_u32 s93, s50, 0xffe80000\n"
"s_and_b32 s95, s93, 0xfbffffff\n"
"v_fma_f32 v84, v18, v137, v26\n"
"v_mfma_f32_16x16x32_bf16 v[128:131], v[48:51], a[56:59], v[128:131]\n"
"v_fma_f32 v85, v19, v137, v27\n"
"v_fma_f32 v86, v20, v137, v28\n"
"v_mfma_f32_16x16x32_bf16 v[128:131], v[52:55], a[60:63], v[128:131]\n"
"v_fma_f32 v87, v21, v137, v29\n"
"v_fma_f32 v96, v96, v136, v84\n"
"v_mfma_f32_16x16x32_bf16 v[92:95], v[112:115], a[64:67], 0\n"
"v_fma_f32 v97, v97, v136, v85\n"
"v_fma_f32 v98, v98, v136, v86\n"
"v_mfma_f32_16x16x32_bf16 v[92:95], v[116:119], a[68:71], v[92:95]\n"
"ds_write_addtid_b32 v128 offset:1792\n"
"v_mfma_f32_16x16x32_bf16 v[92:95], v[120:123], a[72:75], v[92:95]\n"
"ds_write_addtid_b32 v129 offset:6432\n"
"v_fma_f32 v99, v99, v136, v87\n"
"v_mfma_f32_16x16x32_bf16 v[92:95], v[36:39], a[76:79], v[92:95]\n"
"ds_write_addtid_b32 v130 offset:11136\n"
"v_mul_f32_e64 v96, v96, |v96|\n"
"v_mfma_f32_16x16x32_bf16 v[92:95], v[40:43], a[80:83], v[92:95]\n"
"ds_write_addtid_b32 v131 offset:15776\n"
"v_mul_f32_e64 v97, v97, |v97|\n"
"v_mfma_f32_16x16x32_bf16 v[92:95], v[44:47], a[84:87], v[92:95]\n"
"v_mul_f32_e64 v98, v98, |v98|\n"
"v_mfma_f32_16x16x32_bf16 v[92:95], v[48:51], a[88:91], v[92:95]\n"
"v_mul_f32_e64 v99, v99, |v99|\n"
"v_cvt_pk_bf16_f32 v100, v96, v97\n"
"v_mfma_f32_16x16x32_bf16 v[92:95], v[52:55], a[92:95], v[92:95]\n"
"v_cvt_pk_bf16_f32 v101, v98, v99\n"
"v_pk_max_i16 v100, v100, 0\n"
"v_mfma_f32_16x16x32_bf16 v[128:131], v[112:115], a[96:99], 0\n"
"v_pk_max_i16 v101, v101, 0\n"
"v_mfma_f32_16x16x32_bf16 v[128:131], v[116:119], a[100:103], v[128:131]\n"
"s_waitcnt lgkmcnt(8)\n"
"ds_write_addtid_b32 v92 offset:2048\n"
"v_add_f32 v104, v104, v108\n"
"v_mfma_f32_16x16x32_bf16 v[128:131], v[120:123], a[104:107], v[128:131]\n"
"ds_write_addtid_b32 v93 offset:6688\n"
"v_add_f32 v105, v105, v109\n"
"v_mfma_f32_16x16x32_bf16 v[128:131], v[36:39], a[108:111], v[128:131]\n"
"ds_write_addtid_b32 v94 offset:11392\n"
"v_add_f32 v106, v106, v110\n"
"v_mfma_f32_16x16x32_bf16 v[128:131], v[40:43], a[112:115], v[128:131]\n"
"ds_write_addtid_b32 v95 offset:16032\n"
"v_add_f32 v107, v107, v111\n"
"v_mfma_f32_16x16x32_bf16 v[128:131], v[44:47], a[116:119], v[128:131]\n"
"v_add_f32 v104, v104, v124\n"
"v_add_f32 v105, v105, v125\n"
"v_mfma_f32_16x16x32_bf16 v[128:131], v[48:51], a[120:123], v[128:131]\n"
"v_add_f32 v106, v106, v126\n"
"v_add_f32 v107, v107, v127\n"
"v_mfma_f32_16x16x32_bf16 v[128:131], v[52:55], a[124:127], v[128:131]\n"
"v_add_f32 v104, v104, v88\n"
"v_add_f32 v105, v105, v89\n"
"v_mfma_f32_16x16x32_bf16 v[92:95], v[112:115], a[128:131], 0\n"
"v_add_f32 v106, v106, v90\n"
"v_add_f32 v107, v107, v91\n"
"v_mfma_f32_16x16x32_bf16 v[92:95], v[116:119], a[132:135], v[92:95]\n"
"ds_write_addtid_b32 v128 offset:2304\n"
"v_fma_f32 v96, v22, v137, v30\n"
"v_mfma_f32_16x16x32_bf16 v[92:95], v[120:123], a[136:139], v[92:95]\n"
"ds_write_addtid_b32 v129 offset:6944\n"
"v_fma_f32 v97, v23, v137, v31\n"
"v_mfma_f32_16x16x32_bf16 v[92:95], v[36:39], a[140:143], v[92:95]\n"
"ds_write_addtid_b32 v130 offset:11648\n"
"v_fma_f32 v98, v24, v137, v32\n"
"v_mfma_f32_16x16x32_bf16 v[92:95], v[40:43], a[144:147], v[92:95]\n"
"ds_write_addtid_b32 v131 offset:16288\n"
"v_fma_f32 v99, v25, v137, v33\n"
"v_mfma_f32_16x16x32_bf16 v[92:95], v[44:47], a[148:151], v[92:95]\n"
"v_fma_f32 v104, v104, v136, v96\n"
"v_fma_f32 v105, v105, v136, v97\n"
"v_mfma_f32_16x16x32_bf16 v[92:95], v[48:51], a[152:155], v[92:95]\n"
"v_fma_f32 v106, v106, v136, v98\n"
"v_fma_f32 v107, v107, v136, v99\n"
"v_mfma_f32_16x16x32_bf16 v[92:95], v[52:55], a[156:159], v[92:95]\n"
"v_mul_f32_e64 v104, v104, |v104|\n"
"v_mul_f32_e64 v105, v105, |v105|\n"
"v_mfma_f32_16x16x32_bf16 v[96:99], v[112:115], a[160:163], 0\n"
"v_mul_f32_e64 v106, v106, |v106|\n"
"v_mul_f32_e64 v107, v107, |v107|\n"
"v_mfma_f32_16x16x32_bf16 v[96:99], v[116:119], a[164:167], v[96:99]\n"
"ds_write_addtid_b32 v92 offset:2560\n"
"v_cvt_pk_bf16_f32 v102, v104, v105\n"
"v_mfma_f32_16x16x32_bf16 v[96:99], v[120:123], a[168:171], v[96:99]\n"
"ds_write_addtid_b32 v93 offset:7200\n"
"v_cvt_pk_bf16_f32 v103, v106, v107\n"
"v_mfma_f32_16x16x32_bf16 v[96:99], v[36:39], a[172:175], v[96:99]\n"
"ds_write_addtid_b32 v94 offset:11904\n"
"v_pk_max_i16 v102, v102, 0\n"
"v_mfma_f32_16x16x32_bf16 v[96:99], v[40:43], a[176:179], v[96:99]\n"
"ds_write_addtid_b32 v95 offset:16544\n"
"v_pk_max_i16 v103, v103, 0\n"
"v_mfma_f32_16x16x32_bf16 v[96:99], v[44:47], a[180:183], v[96:99]\n"
"buffer_store_dwordx4 v[100:103], v15, s[40:43], s95 offen sc1\n"
"v_mfma_f32_16x16x32_bf16 v[96:99], v[48:51], a[184:187], v[96:99]\n"
"v_mfma_f32_16x16x32_bf16 v[96:99], v[52:55], a[188:191], v[96:99]\n"
"v_mfma_f32_16x16x32_bf16 v[100:103], a[192:195], v[112:115], 0\n"
"v_mfma_f32_16x16x32_bf16 v[100:103], a[196:199], v[116:119], v[100:103]\n"
"ds_write_addtid_b32 v96 offset:2816\n"
"v_mfma_f32_16x16x32_bf16 v[100:103], a[200:203], v[120:123], v[100:103]\n"
"s_waitcnt vmcnt(1)\n"
"buffer_store_dwordx2 v[0:1], v16, s[40:43], s94 offen offset:0 sc1\n"
"ds_write_addtid_b32 v97 offset:7456\n"
"v_mfma_f32_16x16x32_bf16 v[100:103], a[204:207], v[36:39], v[100:103]\n"
"ds_write_addtid_b32 v98 offset:12160\n"
"v_mfma_f32_16x16x32_bf16 v[100:103], a[208:211], v[40:43], v[100:103]\n"
"ds_write_addtid_b32 v99 offset:16800\n"
"v_mfma_f32_16x16x32_bf16 v[100:103], a[212:215], v[44:47], v[100:103]\n"
"v_mfma_f32_16x16x32_bf16 v[100:103], a[216:219], v[48:51], v[100:103]\n"
"v_mfma_f32_16x16x32_bf16 v[100:103], a[220:223], v[52:55], v[100:103]\n"
"v_mfma_f32_16x16x32_bf16 v[104:107], a[224:227], v[112:115], 0\n"
"s_waitcnt lgkmcnt(0)\n"
"v_mfma_f32_16x16x32_bf16 v[104:107], a[228:231], v[116:119], v[104:107]\n"
"s_barrier\n"
"v_mfma_f32_16x16x32_bf16 v[104:107], a[232:235], v[120:123], v[104:107]\n"
"ds_read2st64_b32 v[132:133], v12 offset0:0 offset1:1\n"
"ds_read_b128 v[108:111], v2 offset:0\n"
"v_mfma_f32_16x16x32_bf16 v[104:107], a[236:239], v[36:39], v[104:107]\n"
"ds_read_b128 v[96:99], v3 offset:2048\n"
"ds_read_b128 v[112:115], v4 offset:1024\n"
"v_mfma_f32_16x16x32_bf16 v[104:107], a[240:243], v[40:43], v[104:107]\n"
"v_mfma_f32_16x16x32_bf16 v[104:107], a[244:247], v[44:47], v[104:107]\n"
"ds_read_b128 v[116:119], v2 offset:256\n"
"v_mfma_f32_16x16x32_bf16 v[104:107], a[248:251], v[48:51], v[104:107]\n"
"ds_read_b128 v[120:123], v3 offset:2304\n"
"ds_read_b128 v[124:127], v4 offset:1280\n"
"v_mfma_f32_16x16x32_bf16 v[104:107], a[252:255], v[52:55], v[104:107]\n"
"s_waitcnt lgkmcnt(3)\n"
"v_add_f32 v108, v108, v96\n"
"v_add_f32 v109, v109, v97\n"
"v_add_f32 v110, v110, v98\n"
"v_add_f32 v111, v111, v99\n"
"v_add_f32 v108, v108, v112\n"
"v_add_f32 v109, v109, v113\n"
"v_add_f32 v110, v110, v114\n"
"v_add_f32 v111, v111, v115\n"
"v_add_f32 v108, v108, v100\n"
"v_add_f32 v109, v109, v101\n"
"v_add_f32 v110, v110, v102\n"
"v_add_f32 v111, v111, v103\n"
"s_add_u32 s95, s50, 0x0\n"
"s_and_b32 s94, s95, 0xfbffffff\n"
"v_fma_f32 v96, v18, v133, v26\n"
"v_fma_f32 v97, v19, v133, v27\n"
"v_fma_f32 v98, v20, v133, v28\n"
"v_fma_f32 v99, v21, v133, v29\n"
"v_fma_f32 v108, v108, v132, v96\n"
"v_fma_f32 v109, v109, v132, v97\n"
"v_fma_f32 v110, v110, v132, v98\n"
"v_fma_f32 v111, v111, v132, v99\n"
"v_mul_f32_e64 v108, v108, |v108|\n"
"v_mul_f32_e64 v109, v109, |v109|\n"
"v_mul_f32_e64 v110, v110, |v110|\n"
"v_mul_f32_e64 v111, v111, |v111|\n"
"v_cvt_pk_bf16_f32 v128, v108, v109\n"
"v_cvt_pk_bf16_f32 v129, v110, v111\n"
"v_pk_max_i16 v128, v128, 0\n"
"v_pk_max_i16 v129, v129, 0\n"
"s_waitcnt lgkmcnt(0)\n"
"v_add_f32 v116, v116, v120\n"
"v_add_f32 v117, v117, v121\n"
"v_add_f32 v118, v118, v122\n"
"v_add_f32 v119, v119, v123\n"
"v_add_f32 v116, v116, v124\n"
"v_add_f32 v117, v117, v125\n"
"v_add_f32 v118, v118, v126\n"
"v_add_f32 v119, v119, v127\n"
"s_waitcnt vmcnt(1)\n"
"buffer_store_dwordx2 v[0:1], v16, s[40:43], s93 offen offset:0 sc1\n"
"v_add_f32 v116, v116, v104\n"
"v_add_f32 v117, v117, v105\n"
"v_add_f32 v118, v118, v106\n"
"v_add_f32 v119, v119, v107\n"
"v_fma_f32 v100, v22, v133, v30\n"
"v_fma_f32 v101, v23, v133, v31\n"
"v_fma_f32 v102, v24, v133, v32\n"
"v_fma_f32 v103, v25, v133, v33\n"
"v_fma_f32 v116, v116, v132, v100\n"
"v_fma_f32 v117, v117, v132, v101\n"
"v_fma_f32 v118, v118, v132, v102\n"
"v_fma_f32 v119, v119, v132, v103\n"
"v_mul_f32_e64 v116, v116, |v116|\n"
"v_mul_f32_e64 v117, v117, |v117|\n"
"v_mul_f32_e64 v118, v118, |v118|\n"
"v_mul_f32_e64 v119, v119, |v119|\n"
"v_cvt_pk_bf16_f32 v130, v116, v117\n"
"v_cvt_pk_bf16_f32 v131, v118, v119\n"
"v_pk_max_i16 v130, v130, 0\n"
"v_pk_max_i16 v131, v131, 0\n"
"buffer_store_dwordx4 v[128:131], v15, s[40:43], s94 offen sc1\n"
"s_waitcnt vmcnt(0)\n"
"buffer_store_dwordx2 v[0:1], v16, s[40:43], s95 offen offset:0 sc1\n"
"s_branch .LF_end\n"
".LF_t3:\n"
"v_mfma_f32_16x16x32_bf16 v[100:103], a[196:199], v[116:119], v[100:103]\n"
"ds_write_addtid_b32 v96 offset:2816\n"
"v_mfma_f32_16x16x32_bf16 v[100:103], a[200:203], v[120:123], v[100:103]\n"
"s_waitcnt vmcnt(6)\n"
"buffer_store_dwordx2 v[0:1], v16, s[40:43], s94 offen offset:0 sc1\n"
"ds_write_addtid_b32 v97 offset:7456\n"
"v_mfma_f32_16x16x32_bf16 v[100:103], a[204:207], v[36:39], v[100:103]\n"
"ds_write_addtid_b32 v98 offset:12160\n"
"v_mfma_f32_16x16x32_bf16 v[100:103], a[208:211], v[40:43], v[100:103]\n"
"ds_write_addtid_b32 v99 offset:16800\n"
"v_mfma_f32_16x16x32_bf16 v[100:103], a[212:215], v[44:47], v[100:103]\n"
"v_mfma_f32_16x16x32_bf16 v[100:103], a[216:219], v[48:51], v[100:103]\n"
"v_mfma_f32_16x16x32_bf16 v[100:103], a[220:223], v[52:55], v[100:103]\n"
"v_mfma_f32_16x16x32_bf16 v[104:107], a[224:227], v[112:115], 0\n"
"s_waitcnt lgkmcnt(0)\n"
"v_mfma_f32_16x16x32_bf16 v[104:107], a[228:231], v[116:119], v[104:107]\n"
"s_barrier\n"
"v_mfma_f32_16x16x32_bf16 v[104:107], a[232:235], v[120:123], v[104:107]\n"
"ds_read2st64_b32 v[132:133], v12 offset0:0 offset1:1\n"
"ds_read_b128 v[108:111], v2 offset:0\n"
"v_mfma_f32_16x16x32_bf16 v[104:107], a[236:239], v[36:39], v[104:107]\n"
"ds_read_b128 v[96:99], v3 offset:2048\n"
"ds_read_b128 v[112:115], v4 offset:1024\n"
"v_mfma_f32_16x16x32_bf16 v[104:107], a[240:243], v[40:43], v[104:107]\n"
"v_mfma_f32_16x16x32_bf16 v[104:107], a[244:247], v[44:47], v[104:107]\n"
"ds_read_b128 v[116:119], v2 offset:256\n"
"v_mfma_f32_16x16x32_bf16 v[104:107], a[248:251], v[48:51], v[104:107]\n"
"ds_read_b128 v[120:123], v3 offset:2304\n"
"ds_read_b128 v[124:127], v4 offset:1280\n"
"v_mfma_f32_16x16x32_bf16 v[104:107], a[252:255], v[52:55], v[104:107]\n"
"s_waitcnt vmcnt(11)\n"
"v_mfma_f32_16x16x32_bf16 v[128:131], v[56:59], a[0:3], 0\n"
"v_mfma_f32_16x16x32_bf16 v[128:131], v[60:63], a[4:7], v[128:131]\n"
"s_waitcnt vmcnt(6)\n"
"v_mfma_f32_16x16x32_bf16 v[128:131], v[64:67], a[8:11], v[128:131]\n"
"s_waitcnt lgkmcnt(3)\n"
"v_add_f32 v108, v108, v96\n"
"v_add_f32 v109, v109, v97\n"
"v_mfma_f32_16x16x32_bf16 v[128:131], v[68:71], a[12:15], v[128:131]\n"
"s_waitcnt vmcnt(4)\n"
"v_add_f32 v110, v110, v98\n"
"v_add_f32 v111, v111, v99\n"
"v_mfma_f32_16x16x32_bf16 v[128:131], v[72:75], a[16:19], v[128:131]\n"
"v_add_f32 v108, v108, v112\n"
"v_add_f32 v109, v109, v113\n"
"v_mfma_f32_16x16x32_bf16 v[128:131], v[76:79], a[20:23], v[128:131]\n"
"s_waitcnt vmcnt(2)\n"
"v_add_f32 v110, v110, v114\n"
"v_add_f32 v111, v111, v115\n"
"v_mfma_f32_16x16x32_bf16 v[128:131], v[80:83], a[24:27], v[128:131]\n"
"v_add_f32 v108, v108, v100\n"
"v_add_f32 v109, v109, v101\n"
"v_mfma_f32_16x16x32_bf16 v[128:131], v[84:87], a[28:31], v[128:131]\n"
"v_add_f32 v110, v110, v102\n"
"v_mfma_f32_16x16x32_bf16 v[112:115], v[56:59], a[32:35], 0\n"
"v_add_f32 v111, v111, v103\n"
"v_mfma_f32_16x16x32_bf16 v[112:115], v[60:63], a[36:39], v[112:115]\n"
"ds_write_addtid_b32 v128 offset:0\n"
"v_mfma_f32_16x16x32_bf16 v[112:115], v[64:67], a[40:43], v[112:115]\n"
"s_add_u32 s95, s50, 0xffe80000\n"
"s_and_b32 s94, s95, 0xfbffffff\n"
"ds_write_addtid_b32 v129 offset:4640\n"
"v_mfma_f32_16x16x32_bf16 v[112:115], v[68:71], a[44:47], v[112:115]\n"
"ds_write_addtid_b32 v130 offset:9344\n"
"v_fma_f32 v96, v18, v133, v26\n"
"v_mfma_f32_16x16x32_bf16 v[112:115], v[72:75], a[48:51], v[112:115]\n"
"ds_write_addtid_b32 v131 offset:13984\n"
"v_fma_f32 v97, v19, v133, v27\n"
"v_mfma_f32_16x16x32_bf16 v[112:115], v[76:79], a[52:55], v[112:115]\n"
"v_fma_f32 v98, v20, v133, v28\n"
"v_fma_f32 v99, v21, v133, v29\n"
"v_mfma_f32_16x16x32_bf16 v[112:115], v[80:83], a[56:59], v[112:115]\n"
"v_fma_f32 v108, v108, v132, v96\n"
"v_fma_f32 v109, v109, v132, v97\n"
"v_mfma_f32_16x16x32_bf16 v[112:115], v[84:87], a[60:63], v[112:115]\n"
"v_fma_f32 v110, v110, v132, v98\n"
"v_fma_f32 v111, v111, v132, v99\n"
"v_mfma_f32_16x16x32_bf16 v[100:103], v[56:59], a[64:67], 0\n"
"v_mul_f32_e64 v108, v108, |v108|\n"
"v_mfma_f32_16x16x32_bf16 v[100:103], v[60:63], a[68:71], v[100:103]\n"
"ds_write_addtid_b32 v112 offset:256\n"
"v_mul_f32_e64 v109, v109, |v109|\n"
"v_mfma_f32_16x16x32_bf16 v[100:103], v[64:67], a[72:75], v[100:103]\n"
"ds_write_addtid_b32 v113 offset:4896\n"
"v_mul_f32_e64 v110, v110, |v110|\n"
"v_mfma_f32_16x16x32_bf16 v[100:103], v[68:71], a[76:79], v[100:103]\n"
"ds_write_addtid_b32 v114 offset:9600\n"
"v_mul_f32_e64 v111, v111, |v111|\n"
"v_mfma_f32_16x16x32_bf16 v[100:103], v[72:75], a[80:83], v[100:103]\n"
"ds_write_addtid_b32 v115 offset:14240\n"
"v_mfma_f32_16x16x32_bf16 v[100:103], v[76:79], a[84:87], v[100:103]\n"
"v_cvt_pk_bf16_f32 v128, v108, v109\n"
"v_cvt_pk_bf16_f32 v129, v110, v111\n"
"v_mfma_f32_16x16x32_bf16 v[100:103], v[80:83], a[88:91], v[100:103]\n"
"v_pk_max_i16 v128, v128, 0\n"
"v_pk_max_i16 v129, v129, 0\n"
"v_mfma_f32_16x16x32_bf16 v[100:103], v[84:87], a[92:95], v[100:103]\n"
"s_waitcnt lgkmcnt(8)\n"
"v_add_f32 v116, v116, v120\n"
"v_add_f32 v117, v117, v121\n"
"v_mfma_f32_16x16x32_bf16 v[108:111], v[56:59], a[96:99], 0\n"
"v_add_f32 v118, v118, v122\n"
"v_add_f32 v119, v119, v123\n"
"v_mfma_f32_16x16x32_bf16 v[108:111], v[60:63], a[100:103], v[108:111]\n"
"ds_write_addtid_b32 v100 offset:512\n"
"v_add_f32 v116, v116, v124\n"
"v_mfma_f32_16x16x32_bf16 v[108:111], v[64:67], a[104:107], v[108:111]\n"
"ds_write_addtid_b32 v101 offset:5152\n"
"v_add_f32 v117, v117, v125\n"
"v_mfma_f32_16x16x32_bf16 v[108:111], v[68:71], a[108:111], v[108:111]\n"
"ds_write_addtid_b32 v102 offset:9856\n"
"v_add_f32 v118, v118, v126\n"
"v_mfma_f32_16x16x32_bf16 v[108:111], v[72:75], a[112:115], v[108:111]\n"
"ds_write_addtid_b32 v103 offset:14496\n"
"v_add_f32 v119, v119, v127\n"
"v_mfma_f32_16x16x32_bf16 v[108:111], v[76:79], a[116:119], v[108:111]\n"
"s_waitcnt vmcnt(1)\n"
"buffer_store_dwordx2 v[0:1], v16, s[40:43], s93 offen offset:0 sc1\n"
"v_add_f32 v116, v116, v104\n"
"v_mfma_f32_16x16x32_bf16 v[108:111], v[80:83], a[120:123], v[108:111]\n"
"v_add_f32 v117, v117, v105\n"
"v_add_f32 v118, v118, v106\n"
"v_mfma_f32_16x16x32_bf16 v[108:111], v[84:87], a[124:127], v[108:111]\n"
"v_add_f32 v119, v119, v107\n"
"v_fma_f32 v100, v22, v133, v30\n"
"v_mfma_f32_16x16x32_bf16 v[112:115], v[56:59], a[128:131], 0\n"
"v_fma_f32 v101, v23, v133, v31\n"
"v_fma_f32 v102, v24, v133, v32\n"
"v_mfma_f32_16x16x32_bf16 v[112:115], v[60:63], a[132:135], v[112:115]\n"
"ds_write_addtid_b32 v108 offset:768\n"
"v_fma_f32 v103, v25, v133, v33\n"
"v_mfma_f32_16x16x32_bf16 v[112:115], v[64:67], a[136:139], v[112:115]\n"
"ds_write_addtid_b32 v109 offset:5408\n"
"v_fma_f32 v116, v116, v132, v100\n"
"v_mfma_f32_16x16x32_bf16 v[112:115], v[68:71], a[140:143], v[112:115]\n"
"ds_write_addtid_b32 v110 offset:10112\n"
"v_fma_f32 v117, v117, v132, v101\n"
"v_mfma_f32_16x16x32_bf16 v[112:115], v[72:75], a[144:147], v[112:115]\n"
"ds_write_addtid_b32 v111 offset:14752\n"
"v_fma_f32 v118, v118, v132, v102\n"
"v_mfma_f32_16x16x32_bf16 v[112:115], v[76:79], a[148:151], v[112:115]\n"
"v_fma_f32 v119, v119, v132, v103\n"
"v_mul_f32_e64 v116, v116, |v116|\n"
"v_mfma_f32_16x16x32_bf16 v[112:115], v[80:83], a[152:155], v[112:115]\n"
"v_mul_f32_e64 v117, v117, |v117|\n"
"v_mul_f32_e64 v118, v118, |v118|\n"
"v_mfma_f32_16x16x32_bf16 v[112:115], v[84:87], a[156:159], v[112:115]\n"
"v_mul_f32_e64 v119, v119, |v119|\n"
"v_cvt_pk_bf16_f32 v130, v116, v117\n"
"v_mfma_f32_16x16x32_bf16 v[108:111], v[56:59], a[160:163], 0\n"
"v_cvt_pk_bf16_f32 v131, v118, v119\n"
"v_pk_max_i16 v130, v130, 0\n"
"v_mfma_f32_16x16x32_bf16 v[108:111], v[60:63], a[164:167], v[108:111]\n"
"ds_write_addtid_b32 v112 offset:1024\n"
"v_pk_max_i16 v131, v131, 0\n"
"v_mfma_f32_16x16x32_bf16 v[108:111], v[64:67], a[168:171], v[108:111]\n"
"ds_write_addtid_b32 v113 offset:5664\n"
"v_mfma_f32_16x16x32_bf16 v[108:111], v[68:71], a[172:175], v[108:111]\n"
"ds_write_addtid_b32 v114 offset:10368\n"
"v_mfma_f32_16x16x32_bf16 v[108:111], v[72:75], a[176:179], v[108:111]\n"
"ds_write_addtid_b32 v115 offset:15008\n"
"v_mfma_f32_16x16x32_bf16 v[108:111], v[76:79], a[180:183], v[108:111]\n"
"buffer_store_dwordx4 v[128:131], v15, s[40:43], s94 offen sc1\n"
"v_mfma_f32_16x16x32_bf16 v[108:111], v[80:83], a[184:187], v[108:111]\n"
"v_mfma_f32_16x16x32_bf16 v[108:111], v[84:87], a[188:191], v[108:111]\n"
"v_mfma_f32_16x16x32_bf16 v[112:115], a[192:195], v[56:59], 0\n"
"v_mfma_f32_16x16x32_bf16 v[112:115], a[196:199], v[60:63], v[112:115]\n"
"ds_write_addtid_b32 v108 offset:1280\n"
"v_mfma_f32_16x16x32_bf16 v[112:115], a[200:203], v[64:67], v[112:115]\n"
"ds_write_addtid_b32 v109 offset:5920\n"
"v_mfma_f32_16x16x32_bf16 v[112:115], a[204:207], v[68:71], v[112:115]\n"
"ds_write_addtid_b32 v110 offset:10624\n"
"v_mfma_f32_16x16x32_bf16 v[112:115], a[208:211], v[72:75], v[112:115]\n"
"ds_write_addtid_b32 v111 offset:15264\n"
"v_mfma_f32_16x16x32_bf16 v[112:115], a[212:215], v[76:79], v[112:115]\n"
"v_mfma_f32_16x16x32_bf16 v[112:115], a[216:219], v[80:83], v[112:115]\n"
"v_mfma_f32_16x16x32_bf16 v[112:115], a[220:223], v[84:87], v[112:115]\n"
"v_mfma_f32_16x16x32_bf16 v[132:135], a[224:227], v[56:59], 0\n"
"s_waitcnt lgkmcnt(0)\n"
"v_mfma_f32_16x16x32_bf16 v[132:135], a[228:231], v[60:63], v[132:135]\n"
"s_barrier\n"
"v_mfma_f32_16x16x32_bf16 v[132:135], a[232:235], v[64:67], v[132:135]\n"
"ds_read2st64_b32 v[152:153], v13 offset0:0 offset1:1\n"
"v_mfma_f32_16x16x32_bf16 v[132:135], a[236:239], v[68:71], v[132:135]\n"
"ds_read_b128 v[56:59], v2 offset:1536\n"
"ds_read_b128 v[60:63], v3 offset:512\n"
"v_mfma_f32_16x16x32_bf16 v[132:135], a[240:243], v[72:75], v[132:135]\n"
"ds_read_b128 v[136:139], v4 offset:2560\n"
"ds_read_b128 v[140:143], v2 offset:1792\n"
"v_mfma_f32_16x16x32_bf16 v[132:135], a[244:247], v[76:79], v[132:135]\n"
"v_mfma_f32_16x16x32_bf16 v[132:135], a[248:251], v[80:83], v[132:135]\n"
"ds_read_b128 v[144:147], v3 offset:768\n"
"v_mfma_f32_16x16x32_bf16 v[132:135], a[252:255], v[84:87], v[132:135]\n"
"ds_read_b128 v[148:151], v4 offset:2816\n"
"s_waitcnt lgkmcnt(3)\n"
"v_add_f32 v56, v56, v60\n"
"v_add_f32 v57, v57, v61\n"
"v_add_f32 v58, v58, v62\n"
"v_add_f32 v59, v59, v63\n"
"v_add_f32 v56, v56, v136\n"
"v_add_f32 v57, v57, v137\n"
"v_add_f32 v58, v58, v138\n"
"v_add_f32 v59, v59, v139\n"
"v_add_f32 v56, v56, v112\n"
"v_add_f32 v57, v57, v113\n"
"v_add_f32 v58, v58, v114\n"
"v_add_f32 v59, v59, v115\n"
"s_add_u32 s93, s50, 0x0\n"
"s_and_b32 s94, s93, 0xfbffffff\n"
"v_fma_f32 v80, v18, v153, v26\n"
"v_fma_f32 v81, v19, v153, v27\n"
"v_fma_f32 v82, v20, v153, v28\n"
"v_fma_f32 v83, v21, v153, v29\n"
"v_fma_f32 v56, v56, v152, v80\n"
"v_fma_f32 v57, v57, v152, v81\n"
"v_fma_f32 v58, v58, v152, v82\n"
"v_fma_f32 v59, v59, v152, v83\n"
"v_mul_f32_e64 v56, v56, |v56|\n"
"v_mul_f32_e64 v57, v57, |v57|\n"
"v_mul_f32_e64 v58, v58, |v58|\n"
"v_mul_f32_e64 v59, v59, |v59|\n"
"v_cvt_pk_bf16_f32 v136, v56, v57\n"
"v_cvt_pk_bf16_f32 v137, v58, v59\n"
"v_pk_max_i16 v136, v136, 0\n"
"v_pk_max_i16 v137, v137, 0\n"
"s_waitcnt lgkmcnt(0)\n"
"v_add_f32 v140, v140, v144\n"
"v_add_f32 v141, v141, v145\n"
"v_add_f32 v142, v142, v146\n"
"v_add_f32 v143, v143, v147\n"
"v_add_f32 v140, v140, v148\n"
"v_add_f32 v141, v141, v149\n"
"v_add_f32 v142, v142, v150\n"
"v_add_f32 v143, v143, v151\n"
"s_waitcnt vmcnt(0)\n"
"buffer_store_dwordx2 v[0:1], v16, s[40:43], s95 offen offset:0 sc1\n"
"v_add_f32 v140, v140, v132\n"
"v_add_f32 v141, v141, v133\n"
"v_add_f32 v142, v142, v134\n"
"v_add_f32 v143, v143, v135\n"
"v_fma_f32 v60, v22, v153, v30\n"
"v_fma_f32 v61, v23, v153, v31\n"
"v_fma_f32 v62, v24, v153, v32\n"
"v_fma_f32 v63, v25, v153, v33\n"
"v_fma_f32 v140, v140, v152, v60\n"
"v_fma_f32 v141, v141, v152, v61\n"
"v_fma_f32 v142, v142, v152, v62\n"
"v_fma_f32 v143, v143, v152, v63\n"
"v_mul_f32_e64 v140, v140, |v140|\n"
"v_mul_f32_e64 v141, v141, |v141|\n"
"v_mul_f32_e64 v142, v142, |v142|\n"
"v_mul_f32_e64 v143, v143, |v143|\n"
"v_cvt_pk_bf16_f32 v138, v140, v141\n"
"v_cvt_pk_bf16_f32 v139, v142, v143\n"
"v_pk_max_i16 v138, v138, 0\n"
"v_pk_max_i16 v139, v139, 0\n"
"buffer_store_dwordx4 v[136:139], v15, s[40:43], s94 offen sc1\n"
"s_waitcnt vmcnt(0)\n"
"buffer_store_dwordx2 v[0:1], v16, s[40:43], s93 offen offset:0 sc1\n"
"s_branch .LF_end\n"
".LF_end:\n"
"s_waitcnt vmcnt(0) lgkmcnt(0)\n"
"s_endpgm\n"
      :: "{v2}"((unsigned)(lds0 + ((wid + 3) & 3) * 19104 + trp)), "{v3}"((unsigned)(lds0 + ((wid + 2) & 3) * 19104 + trp)), "{v4}"((unsigned)(lds0 + ((wid + 1) & 3) * 19104 + trp)), "{v5}"((unsigned)(lane * 16 + wid * 8192 + ((0 + rot) % 4) * 2048 + 134283264)), "{v6}"((unsigned)(lane * 16 + wid * 8192 + ((1 + rot) % 4) * 2048 + 134283264)), "{v7}"((unsigned)(lane * 16 + wid * 8192 + ((2 + rot) % 4) * 2048 + 134283264)), "{v8}"((unsigned)(lane * 16 + wid * 8192 + ((3 + rot) % 4) * 2048 + 134283264)), "{v9}"((unsigned)((lane & 31) * 8 + 352768 + F_GL * 1572864)), "{v10}"((unsigned)(lane * 16 + 134561792)), "{v11}"((unsigned)(lds0 + ((wid + 0) & 3) * 19104 + 18592 + ((li >> 1) + 32 * (li & 1)) * 4)), "{v12}"((unsigned)(lds0 + ((wid + 1) & 3) * 19104 + 18592 + ((li >> 1) + 32 * (li & 1)) * 4)), "{v13}"((unsigned)(lds0 + ((wid + 2) & 3) * 19104 + 18592 + ((li >> 1) + 32 * (li & 1)) * 4)), "{v14}"((unsigned)(lds0 + ((wid + 3) & 3) * 19104 + 18592 + ((li >> 1) + 32 * (li & 1)) * 4)), "{v15}"((unsigned)(lane * 16 + pair * 1024 + 98304)), "{v16}"((unsigned)(lane ? DROP : pair * 8 + idx / 7 * 32 + 353024)), "{v17}"((unsigned)(0x3727c5ac)), "{v36}"((unsigned)(vT + tsw)), "{v37}"((unsigned)(vT + (tsw ^ 8))), "{v38}"((unsigned)(pair * 64 + lg * 16)), "{v39}"((unsigned)(vD + ((0 + rot) % 4) * 458752)), "{v40}"((unsigned)(vD + ((1 + rot) % 4) * 458752)), "{v41}"((unsigned)(vD + ((2 + rot) % 4) * 458752)), "{v42}"((unsigned)(vD + ((3 + rot) % 4) * 458752)), "{v43}"((unsigned)(lds0 + wid * 19104 + trp)), "{s[40:43]}"(rS), "{s[44:47]}"(rW), "{s[48:49]}"(wsp), "{s50}"((unsigned)(sA)), "{s51}"((unsigned)(sLo)), "{s52}"((unsigned)(sHi)), "{s53}"((unsigned)(sEx)), "{s54}"((unsigned)(sEnt)), "{s55}"((unsigned)(sLd)), "{s[56:57]}"(wsb + (sLo & 0x3ffffff)), "{s59}"((unsigned)(lds0 + wid * 19104)), "{s62}"((unsigned)(0x3a800000)), "{s[64:65]}"((u64)a.b1), "{s66}"((unsigned)(sW0 + 64 * ((wid + 1) & 3))), "{s67}"((unsigned)(sW0 + 64 * ((wid + 2) & 3))), "{s68}"((unsigned)(sW0 + 64 * ((wid + 3) & 3))), "{s69}"((unsigned)(sW0 + 64 * ((wid + 4) & 3)))
      : "v255", "a255", "s95", "vcc", "scc", "memory");

  } else {
    const int kq = idx / 7, grp = idx % 7, f0 = kq * 28 + wid * 7, pg0 = grp < 4 ? grp * 5 : 4 + grp * 4, pair = pg0 + wid;
    const unsigned rot = SLP & 8 ? 0 : (set * 5) >> 1, tsw = (kq & 1) * 8, sW0 = f0 * 32 * 2048 + pg0 * 64, vD = (lane >> 2) * 2048 + (lane & 3) * 16;
    const u32x4 rW = desc(a.w2, 3584 * 2048);
    if (idx == 0) {
      const unsigned sEx = sHi + H_EX * 3 * RS, sEnt = -wid & 3;
    asm volatile(
"s_nop 4\n"
"v_mov_b32 v186, 0\n"
"global_load_dwordx2 v[0:1], v186, s[48:49] sc1\n"
"v_mov_b32 v188, 0x3f803f80\n"
"v_mov_b32 v189, 0x3f803f80\n"
"v_mov_b32 v190, 0x3f803f80\n"
"v_mov_b32 v191, 0x3f803f80\n"
"s_add_u32 m0, s55, 0\n"
"s_add_u32 s60, s66, 0\n"
"buffer_load_dwordx4 v50, s[44:47], s60 offen lds\n"
"s_add_u32 m0, s55, 1024\n"
"s_add_u32 s60, s66, 32768\n"
"buffer_load_dwordx4 v50, s[44:47], s60 offen lds\n"
"s_add_u32 m0, s55, 2048\n"
"s_add_u32 s60, s67, 0\n"
"buffer_load_dwordx4 v50, s[44:47], s60 offen lds\n"
"s_add_u32 m0, s55, 3072\n"
"s_add_u32 s60, s67, 32768\n"
"buffer_load_dwordx4 v50, s[44:47], s60 offen lds\n"
"s_add_u32 m0, s55, 4096\n"
"s_add_u32 s60, s68, 0\n"
"buffer_load_dwordx4 v50, s[44:47], s60 offen lds\n"
"s_add_u32 m0, s55, 5120\n"
"s_add_u32 s60, s68, 32768\n"
"buffer_load_dwordx4 v50, s[44:47], s60 offen lds\n"
"s_add_u32 m0, s55, 6144\n"
"s_add_u32 s60, s69, 0\n"
"buffer_load_dwordx4 v50, s[44:47], s60 offen lds\n"
"s_add_u32 m0, s55, 7168\n"
"s_add_u32 s60, s69, 32768\n"
"buffer_load_dwordx4 v50, s[44:47], s60 offen lds\n"
"s_add_u32 m0, s55, 8192\n"
"s_add_u32 s60, s70, 0\n"
"buffer_load_dwordx4 v50, s[44:47], s60 offen lds\n"
"s_add_u32 m0, s55, 9216\n"
"s_add_u32 s60, s70, 32768\n"
"buffer_load_dwordx4 v50, s[44:47], s60 offen lds\n"
"s_add_u32 m0, s55, 10240\n"
"s_add_u32 s60, s66, 0\n"
"buffer_load_dwordx4 v51, s[44:47], s60 offen lds\n"
"s_add_u32 m0, s55, 11264\n"
"s_add_u32 s60, s66, 32768\n"
"buffer_load_dwordx4 v51, s[44:47], s60 offen lds\n"
"s_add_u32 m0, s55, 12288\n"
"s_add_u32 s60, s67, 0\n"
"buffer_load_dwordx4 v51, s[44:47], s60 offen lds\n"
"s_add_u32 m0, s55, 13312\n"
"s_add_u32 s60, s67, 32768\n"
"buffer_load_dwordx4 v51, s[44:47], s60 offen lds\n"
"s_add_u32 m0, s55, 14336\n"
"s_add_u32 s60, s68, 0\n"
"buffer_load_dwordx4 v51, s[44:47], s60 offen lds\n"
"s_add_u32 m0, s55, 15360\n"
"s_add_u32 s60, s68, 32768\n"
"buffer_load_dwordx4 v51, s[44:47], s60 offen lds\n"
"s_add_u32 m0, s55, 16384\n"
"s_add_u32 s60, s69, 0\n"
"buffer_load_dwordx4 v51, s[44:47], s60 offen lds\n"
"s_add_u32 m0, s55, 17408\n"
"s_add_u32 s60, s69, 32768\n"
"buffer_load_dwordx4 v51, s[44:47], s60 offen lds\n"
"s_add_u32 m0, s55, 18432\n"
"s_add_u32 s60, s70, 0\n"
"buffer_load_dwordx4 v51, s[44:47], s60 offen lds\n"
"s_add_u32 m0, s55, 19456\n"
"s_add_u32 s60, s70, 32768\n"
"buffer_load_dwordx4 v51, s[44:47], s60 offen lds\n"
"s_add_u32 m0, s55, 20480\n"
"s_add_u32 s60, s66, 0\n"
"buffer_load_dwordx4 v52, s[44:47], s60 offen lds\n"
"s_add_u32 m0, s55, 21504\n"
"s_add_u32 s60, s66, 32768\n"
"buffer_load_dwordx4 v52, s[44:47], s60 offen lds\n"
"s_add_u32 m0, s55, 22528\n"
"s_add_u32 s60, s67, 0\n"
"buffer_load_dwordx4 v52, s[44:47], s60 offen lds\n"
"s_add_u32 m0, s55, 23552\n"
"s_add_u32 s60, s67, 32768\n"
"buffer_load_dwordx4 v52, s[44:47], s60 offen lds\n"
"s_add_u32 m0, s55, 24576\n"
"s_add_u32 s60, s68, 0\n"
"buffer_load_dwordx4 v52, s[44:47], s60 offen lds\n"
"s_add_u32 m0, s55, 25600\n"
"s_add_u32 s60, s68, 32768\n"
"buffer_load_dwordx4 v52, s[44:47], s60 offen lds\n"
"s_add_u32 m0, s55, 26624\n"
"s_add_u32 s60, s69, 0\n"
"buffer_load_dwordx4 v52, s[44:47], s60 offen lds\n"
"s_add_u32 m0, s55, 27648\n"
"s_add_u32 s60, s69, 32768\n"
"buffer_load_dwordx4 v52, s[44:47], s60 offen lds\n"
"s_add_u32 m0, s55, 28672\n"
"s_add_u32 s60, s70, 0\n"
"buffer_load_dwordx4 v52, s[44:47], s60 offen lds\n"
"s_add_u32 m0, s55, 29696\n"
"s_add_u32 s60, s70, 32768\n"
"buffer_load_dwordx4 v52, s[44:47], s60 offen lds\n"
"s_add_u32 m0, s55, 30720\n"
"s_add_u32 s60, s66, 0\n"
"buffer_load_dwordx4 v53, s[44:47], s60 offen lds\n"
"s_add_u32 m0, s55, 31744\n"
"s_add_u32 s60, s66, 32768\n"
"buffer_load_dwordx4 v53, s[44:47], s60 offen lds\n"
"s_add_u32 m0, s55, 32768\n"
"s_add_u32 s60, s67, 0\n"
"buffer_load_dwordx4 v53, s[44:47], s60 offen lds\n"
"s_add_u32 m0, s55, 33792\n"
"s_add_u32 s60, s67, 32768\n"
"buffer_load_dwordx4 v53, s[44:47], s60 offen lds\n"
"s_add_u32 m0, s55, 34816\n"
"s_add_u32 s60, s68, 0\n"
"buffer_load_dwordx4 v53, s[44:47], s60 offen lds\n"
"s_add_u32 m0, s55, 35840\n"
"s_add_u32 s60, s68, 32768\n"
"buffer_load_dwordx4 v53, s[44:47], s60 offen lds\n"
"s_add_u32 m0, s55, 36864\n"
"s_add_u32 s60, s69, 0\n"
"buffer_load_dwordx4 v53, s[44:47], s60 offen lds\n"
"s_add_u32 m0, s55, 37888\n"
"s_add_u32 s60, s69, 32768\n"
"buffer_load_dwordx4 v53, s[44:47], s60 offen lds\n"
"s_add_u32 m0, s55, 38912\n"
"s_add_u32 s60, s70, 0\n"
"buffer_load_dwordx4 v53, s[44:47], s60 offen lds\n"
"s_add_u32 m0, s55, 39936\n"
"s_add_u32 s60, s70, 32768\n"
"buffer_load_dwordx4 v53, s[44:47], s60 offen lds\n"
"s_waitcnt vmcnt(38)\n"
"ds_read_b64_tr_b16 a[0:1], v48 offset:0\n"
"ds_read_b64_tr_b16 a[2:3], v48 offset:256\n"
"ds_read_b64_tr_b16 a[28:29], v49 offset:0\n"
"ds_read_b64_tr_b16 a[30:31], v49 offset:256\n"
"v_add_u32 v0, 2, v0\n"
"v_add_u32 v1, 0x9e3779b9, v1\n"
"v_or_b32 v0, 1, v0\n"
"v_readfirstlane_b32 s49, v1\n"
"v_readfirstlane_b32 s48, v0\n"
"s_waitcnt vmcnt(36)\n"
"ds_read_b64_tr_b16 a[56:57], v48 offset:2048\n"
"ds_read_b64_tr_b16 a[58:59], v48 offset:2304\n"
"ds_read_b64_tr_b16 a[84:85], v49 offset:2048\n"
"ds_read_b64_tr_b16 a[86:87], v49 offset:2304\n"
"s_waitcnt lgkmcnt(4)\n"
"s_add_u32 m0, s55, 0\n"
"s_add_u32 s60, s66, 0\n"
"buffer_load_dwordx4 v54, s[44:47], s60 offen lds\n"
"s_add_u32 m0, s55, 1024\n"
"s_add_u32 s60, s66, 32768\n"
"buffer_load_dwordx4 v54, s[44:47], s60 offen lds\n"
"s_waitcnt vmcnt(36)\n"
"ds_read_b64_tr_b16 a[112:113], v48 offset:4096\n"
"ds_read_b64_tr_b16 a[114:115], v48 offset:4352\n"
"ds_read_b64_tr_b16 a[140:141], v49 offset:4096\n"
"ds_read_b64_tr_b16 a[142:143], v49 offset:4352\n"
"s_waitcnt lgkmcnt(4)\n"
"s_add_u32 m0, s55, 2048\n"
"s_add_u32 s60, s67, 0\n"
"buffer_load_dwordx4 v54, s[44:47], s60 offen lds\n"
"s_add_u32 m0, s55, 3072\n"
"s_add_u32 s60, s67, 32768\n"
"buffer_load_dwordx4 v54, s[44:47], s60 offen lds\n"
"s_waitcnt vmcnt(36)\n"
"ds_read_b64_tr_b16 a[168:169], v48 offset:6144\n"
"ds_read_b64_tr_b16 a[170:171], v48 offset:6400\n"
"ds_read_b64_tr_b16 a[196:197], v49 offset:6144\n"
"ds_read_b64_tr_b16 a[198:199], v49 offset:6400\n"
"s_waitcnt lgkmcnt(4)\n"
"s_add_u32 m0, s55, 4096\n"
"s_add_u32 s60, s68, 0\n"
"buffer_load_dwordx4 v54, s[44:47], s60 offen lds\n"
"s_add_u32 m0, s55, 5120\n"
"s_add_u32 s60, s68, 32768\n"
"buffer_load_dwordx4 v54, s[44:47], s60 offen lds\n"
"s_waitcnt vmcnt(36)\n"
"ds_read_b64_tr_b16 a[224:225], v48 offset:8192\n"
"ds_read_b64_tr_b16 a[226:227], v48 offset:8448\n"
"ds_read_b64_tr_b16 a[252:253], v49 offset:8192\n"
"ds_read_b64_tr_b16 a[254:255], v49 offset:8448\n"
"s_waitcnt lgkmcnt(4)\n"
"s_add_u32 m0, s55, 6144\n"
"s_add_u32 s60, s69, 0\n"
"buffer_load_dwordx4 v54, s[44:47], s60 offen lds\n"
"s_add_u32 m0, s55, 7168\n"
"s_add_u32 s60, s69, 32768\n"
"buffer_load_dwordx4 v54, s[44:47], s60 offen lds\n"
"s_waitcnt vmcnt(36)\n"
"ds_read_b64_tr_b16 a[4:5], v48 offset:10240\n"
"ds_read_b64_tr_b16 a[6:7], v48 offset:10496\n"
"ds_read_b64_tr_b16 a[32:33], v49 offset:10240\n"
"ds_read_b64_tr_b16 a[34:35], v49 offset:10496\n"
"s_waitcnt lgkmcnt(4)\n"
"s_add_u32 m0, s55, 8192\n"
"s_add_u32 s60, s70, 0\n"
"buffer_load_dwordx4 v54, s[44:47], s60 offen lds\n"
"s_add_u32 m0, s55, 9216\n"
"s_add_u32 s60, s70, 32768\n"
"buffer_load_dwordx4 v54, s[44:47], s60 offen lds\n"
"s_waitcnt vmcnt(36)\n"
"ds_read_b64_tr_b16 a[60:61], v48 offset:12288\n"
"ds_read_b64_tr_b16 a[62:63], v48 offset:12544\n"
"ds_read_b64_tr_b16 a[88:89], v49 offset:12288\n"
"ds_read_b64_tr_b16 a[90:91], v49 offset:12544\n"
"s_waitcnt lgkmcnt(4)\n"
"s_add_u32 m0, s55, 10240\n"
"s_add_u32 s60, s66, 0\n"
"buffer_load_dwordx4 v55, s[44:47], s60 offen lds\n"
"s_add_u32 m0, s55, 11264\n"
"s_add_u32 s60, s66, 32768\n"
"buffer_load_dwordx4 v55, s[44:47], s60 offen lds\n"
"s_waitcnt vmcnt(36)\n"
"ds_read_b64_tr_b16 a[116:117], v48 offset:14336\n"
"ds_read_b64_tr_b16 a[118:119], v48 offset:14592\n"
"ds_read_b64_tr_b16 a[144:145], v49 offset:14336\n"
"ds_read_b64_tr_b16 a[146:147], v49 offset:14592\n"
"s_waitcnt lgkmcnt(4)\n"
"s_add_u32 m0, s55, 12288\n"
"s_add_u32 s60, s67, 0\n"
"buffer_load_dwordx4 v55, s[44:47], s60 offen lds\n"
"s_add_u32 m0, s55, 13312\n"
"s_add_u32 s60, s67, 32768\n"
"buffer_load_dwordx4 v55, s[44:47], s60 offen lds\n"
"s_waitcnt vmcnt(36)\n"
"ds_read_b64_tr_b16 a[172:173], v48 offset:16384\n"
"ds_read_b64_tr_b16 a[174:175], v48 offset:16640\n"
"ds_read_b64_tr_b16 a[200:201], v49 offset:16384\n"
"ds_read_b64_tr_b16 a[202:203], v49 offset:16640\n"
"s_waitcnt lgkmcnt(4)\n"
"s_add_u32 m0, s55, 14336\n"
"s_add_u32 s60, s68, 0\n"
"buffer_load_dwordx4 v55, s[44:47], s60 offen lds\n"
"s_add_u32 m0, s55, 15360\n"
"s_add_u32 s60, s68, 32768\n"
"buffer_load_dwordx4 v55, s[44:47], s60 offen lds\n"
"s_waitcnt vmcnt(36)\n"
"ds_read_b64_tr_b16 a[228:229], v48 offset:18432\n"
"ds_read_b64_tr_b16 a[230:231], v48 offset:18688\n"
"ds_read_b64_tr_b16 v[24:25], v49 offset:18432\n"
"ds_read_b64_tr_b16 v[26:27], v49 offset:18688\n"
"s_waitcnt lgkmcnt(4)\n"
"s_add_u32 m0, s55, 16384\n"
"s_add_u32 s60, s69, 0\n"
"buffer_load_dwordx4 v55, s[44:47], s60 offen lds\n"
"s_add_u32 m0, s55, 17408\n"
"s_add_u32 s60, s69, 32768\n"
"buffer_load_dwordx4 v55, s[44:47], s60 offen lds\n"
"s_waitcnt vmcnt(36)\n"
"ds_read_b64_tr_b16 a[8:9], v48 offset:20480\n"
"ds_read_b64_tr_b16 a[10:11], v48 offset:20736\n"
"ds_read_b64_tr_b16 a[36:37], v49 offset:20480\n"
"ds_read_b64_tr_b16 a[38:39], v49 offset:20736\n"
"s_waitcnt lgkmcnt(4)\n"
"s_add_u32 m0, s55, 18432\n"
"s_add_u32 s60, s70, 0\n"
"buffer_load_dwordx4 v55, s[44:47], s60 offen lds\n"
"s_add_u32 m0, s55, 19456\n"
"s_add_u32 s60, s70, 32768\n"
"buffer_load_dwordx4 v55, s[44:47], s60 offen lds\n"
"s_waitcnt vmcnt(36)\n"
"ds_read_b64_tr_b16 a[64:65], v48 offset:22528\n"
"ds_read_b64_tr_b16 a[66:67], v48 offset:22784\n"
"ds_read_b64_tr_b16 a[92:93], v49 offset:22528\n"
"ds_read_b64_tr_b16 a[94:95], v49 offset:22784\n"
"s_waitcnt lgkmcnt(4)\n"
"s_add_u32 m0, s55, 20480\n"
"s_add_u32 s60, s66, 0\n"
"buffer_load_dwordx4 v56, s[44:47], s60 offen lds\n"
"s_add_u32 m0, s55, 21504\n"
"s_add_u32 s60, s66, 32768\n"
"buffer_load_dwordx4 v56, s[44:47], s60 offen lds\n"
"s_waitcnt vmcnt(36)\n"
"ds_read_b64_tr_b16 a[120:121], v48 offset:24576\n"
"ds_read_b64_tr_b16 a[122:123], v48 offset:24832\n"
"ds_read_b64_tr_b16 a[148:149], v49 offset:24576\n"
"ds_read_b64_tr_b16 a[150:151], v49 offset:24832\n"
"s_waitcnt lgkmcnt(4)\n"
"s_add_u32 m0, s55, 22528\n"
"s_add_u32 s60, s67, 0\n"
"buffer_load_dwordx4 v56, s[44:47], s60 offen lds\n"
"s_add_u32 m0, s55, 23552\n"
"s_add_u32 s60, s67, 32768\n"
"buffer_load_dwordx4 v56, s[44:47], s60 offen lds\n"
"s_waitcnt vmcnt(36)\n"
"ds_read_b64_tr_b16 a[176:177], v48 offset:26624\n"
"ds_read_b64_tr_b16 a[178:179], v48 offset:26880\n"
"ds_read_b64_tr_b16 a[204:205], v49 offset:26624\n"
"ds_read_b64_tr_b16 a[206:207], v49 offset:26880\n"
"s_waitcnt lgkmcnt(4)\n"
"s_add_u32 m0, s55, 24576\n"
"s_add_u32 s60, s68, 0\n"
"buffer_load_dwordx4 v56, s[44:47], s60 offen lds\n"
"s_add_u32 m0, s55, 25600\n"
"s_add_u32 s60, s68, 32768\n"
"buffer_load_dwordx4 v56, s[44:47], s60 offen lds\n"
"s_waitcnt vmcnt(36)\n"
"ds_read_b64_tr_b16 a[232:233], v48 offset:28672\n"
"ds_read_b64_tr_b16 a[234:235], v48 offset:28928\n"
"ds_read_b64_tr_b16 v[28:29], v49 offset:28672\n"
"ds_read_b64_tr_b16 v[30:31], v49 offset:28928\n"
"s_waitcnt lgkmcnt(4)\n"
"s_add_u32 m0, s55, 26624\n"
"s_add_u32 s60, s69, 0\n"
"buffer_load_dwordx4 v56, s[44:47], s60 offen lds\n"
"s_add_u32 m0, s55, 27648\n"
"s_add_u32 s60, s69, 32768\n"
"buffer_load_dwordx4 v56, s[44:47], s60 offen lds\n"
"s_waitcnt vmcnt(36)\n"
"ds_read_b64_tr_b16 a[12:13], v48 offset:30720\n"
"ds_read_b64_tr_b16 a[14:15], v48 offset:30976\n"
"ds_read_b64_tr_b16 a[40:41], v49 offset:30720\n"
"ds_read_b64_tr_b16 a[42:43], v49 offset:30976\n"
"s_waitcnt lgkmcnt(4)\n"
"s_add_u32 m0, s55, 28672\n"
"s_add_u32 s60, s70, 0\n"
"buffer_load_dwordx4 v56, s[44:47], s60 offen lds\n"
"s_add_u32 m0, s55, 29696\n"
"s_add_u32 s60, s70, 32768\n"
"buffer_load_dwordx4 v56, s[44:47], s60 offen lds\n"
"s_waitcnt vmcnt(36)\n"
"ds_read_b64_tr_b16 a[68:69], v48 offset:32768\n"
"ds_read_b64_tr_b16 a[70:71], v48 offset:33024\n"
"ds_read_b64_tr_b16 a[96:97], v49 offset:32768\n"
"ds_read_b64_tr_b16 a[98:99], v49 offset:33024\n"
"s_waitcnt lgkmcnt(4)\n"
"s_waitcnt vmcnt(34)\n"
"ds_read_b64_tr_b16 a[124:125], v48 offset:34816\n"
"ds_read_b64_tr_b16 a[126:127], v48 offset:35072\n"
"ds_read_b64_tr_b16 a[152:153], v49 offset:34816\n"
"ds_read_b64_tr_b16 a[154:155], v49 offset:35072\n"
"s_waitcnt lgkmcnt(4)\n"
"s_waitcnt vmcnt(32)\n"
"ds_read_b64_tr_b16 a[180:181], v48 offset:36864\n"
"ds_read_b64_tr_b16 a[182:183], v48 offset:37120\n"
"ds_read_b64_tr_b16 a[208:209], v49 offset:36864\n"
"ds_read_b64_tr_b16 a[210:211], v49 offset:37120\n"
"s_waitcnt lgkmcnt(4)\n"
"s_waitcnt vmcnt(30)\n"
"ds_read_b64_tr_b16 a[236:237], v48 offset:38912\n"
"ds_read_b64_tr_b16 a[238:239], v48 offset:39168\n"
"ds_read_b64_tr_b16 v[32:33], v49 offset:38912\n"
"ds_read_b64_tr_b16 v[34:35], v49 offset:39168\n"
"s_waitcnt lgkmcnt(4)\n"
"s_waitcnt vmcnt(28)\n"
"ds_read_b64_tr_b16 a[16:17], v48 offset:0\n"
"ds_read_b64_tr_b16 a[18:19], v48 offset:256\n"
"ds_read_b64_tr_b16 a[44:45], v49 offset:0\n"
"ds_read_b64_tr_b16 a[46:47], v49 offset:256\n"
"s_waitcnt lgkmcnt(4)\n"
"s_waitcnt vmcnt(26)\n"
"ds_read_b64_tr_b16 a[72:73], v48 offset:2048\n"
"ds_read_b64_tr_b16 a[74:75], v48 offset:2304\n"
"ds_read_b64_tr_b16 a[100:101], v49 offset:2048\n"
"ds_read_b64_tr_b16 a[102:103], v49 offset:2304\n"
"s_waitcnt lgkmcnt(4)\n"
"s_waitcnt vmcnt(24)\n"
"ds_read_b64_tr_b16 a[128:129], v48 offset:4096\n"
"ds_read_b64_tr_b16 a[130:131], v48 offset:4352\n"
"ds_read_b64_tr_b16 a[156:157], v49 offset:4096\n"
"ds_read_b64_tr_b16 a[158:159], v49 offset:4352\n"
"s_waitcnt lgkmcnt(4)\n"
"s_waitcnt vmcnt(22)\n"
"ds_read_b64_tr_b16 a[184:185], v48 offset:6144\n"
"ds_read_b64_tr_b16 a[186:187], v48 offset:6400\n"
"ds_read_b64_tr_b16 a[212:213], v49 offset:6144\n"
"ds_read_b64_tr_b16 a[214:215], v49 offset:6400\n"
"s_waitcnt lgkmcnt(4)\n"
"s_waitcnt vmcnt(20)\n"
"ds_read_b64_tr_b16 a[240:241], v48 offset:8192\n"
"ds_read_b64_tr_b16 a[242:243], v48 offset:8448\n"
"ds_read_b64_tr_b16 v[36:37], v49 offset:8192\n"
"ds_read_b64_tr_b16 v[38:39], v49 offset:8448\n"
"s_waitcnt lgkmcnt(4)\n"
"s_waitcnt vmcnt(18)\n"
"ds_read_b64_tr_b16 a[20:21], v48 offset:10240\n"
"ds_read_b64_tr_b16 a[22:23], v48 offset:10496\n"
"ds_read_b64_tr_b16 a[48:49], v49 offset:10240\n"
"ds_read_b64_tr_b16 a[50:51], v49 offset:10496\n"
"s_waitcnt lgkmcnt(4)\n"
"s_waitcnt vmcnt(16)\n"
"ds_read_b64_tr_b16 a[76:77], v48 offset:12288\n"
"ds_read_b64_tr_b16 a[78:79], v48 offset:12544\n"
"ds_read_b64_tr_b16 a[104:105], v49 offset:12288\n"
"ds_read_b64_tr_b16 a[106:107], v49 offset:12544\n"
"s_waitcnt lgkmcnt(4)\n"
"s_waitcnt vmcnt(14)\n"
"ds_read_b64_tr_b16 a[132:133], v48 offset:14336\n"
"ds_read_b64_tr_b16 a[134:135], v48 offset:14592\n"
"ds_read_b64_tr_b16 a[160:161], v49 offset:14336\n"
"ds_read_b64_tr_b16 a[162:163], v49 offset:14592\n"
"s_waitcnt lgkmcnt(4)\n"
"s_waitcnt vmcnt(12)\n"
"ds_read_b64_tr_b16 a[188:189], v48 offset:16384\n"
"ds_read_b64_tr_b16 a[190:191], v48 offset:16640\n"
"ds_read_b64_tr_b16 a[216:217], v49 offset:16384\n"
"ds_read_b64_tr_b16 a[218:219], v49 offset:16640\n"
"s_waitcnt lgkmcnt(4)\n"
"s_waitcnt vmcnt(10)\n"
"ds_read_b64_tr_b16 a[244:245], v48 offset:18432\n"
"ds_read_b64_tr_b16 a[246:247], v48 offset:18688\n"
"ds_read_b64_tr_b16 v[40:41], v49 offset:18432\n"
"ds_read_b64_tr_b16 v[42:43], v49 offset:18688\n"
"s_waitcnt lgkmcnt(4)\n"
"s_waitcnt vmcnt(8)\n"
"ds_read_b64_tr_b16 a[24:25], v48 offset:20480\n"
"ds_read_b64_tr_b16 a[26:27], v48 offset:20736\n"
"ds_read_b64_tr_b16 a[52:53], v49 offset:20480\n"
"ds_read_b64_tr_b16 a[54:55], v49 offset:20736\n"
"s_waitcnt lgkmcnt(4)\n"
"s_waitcnt vmcnt(6)\n"
"ds_read_b64_tr_b16 a[80:81], v48 offset:22528\n"
"ds_read_b64_tr_b16 a[82:83], v48 offset:22784\n"
"ds_read_b64_tr_b16 a[108:109], v49 offset:22528\n"
"ds_read_b64_tr_b16 a[110:111], v49 offset:22784\n"
"s_waitcnt lgkmcnt(4)\n"
"s_waitcnt vmcnt(4)\n"
"ds_read_b64_tr_b16 a[136:137], v48 offset:24576\n"
"ds_read_b64_tr_b16 a[138:139], v48 offset:24832\n"
"ds_read_b64_tr_b16 a[164:165], v49 offset:24576\n"
"ds_read_b64_tr_b16 a[166:167], v49 offset:24832\n"
"s_waitcnt lgkmcnt(4)\n"
"s_waitcnt vmcnt(2)\n"
"ds_read_b64_tr_b16 a[192:193], v48 offset:26624\n"
"ds_read_b64_tr_b16 a[194:195], v48 offset:26880\n"
"ds_read_b64_tr_b16 a[220:221], v49 offset:26624\n"
"ds_read_b64_tr_b16 a[222:223], v49 offset:26880\n"
"s_waitcnt lgkmcnt(4)\n"
"s_waitcnt vmcnt(0)\n"
"ds_read_b64_tr_b16 a[248:249], v48 offset:28672\n"
"ds_read_b64_tr_b16 a[250:251], v48 offset:28928\n"
"ds_read_b64_tr_b16 v[44:45], v49 offset:28672\n"
"ds_read_b64_tr_b16 v[46:47], v49 offset:28928\n"
"s_waitcnt lgkmcnt(4)\n"
"s_waitcnt vmcnt(0)\n"
"s_waitcnt lgkmcnt(0)\n"
"s_mov_b32 m0, s59\n"
"s_barrier\n"
"s_mov_b64 s[64:65], s[48:49]\n"
"s_mov_b32 s92, 0x80000000\n"
"s_mov_b32 s93, 0x80000000\n"
"s_mov_b32 s94, 0x80000000\n"
"s_mov_b32 s93, 0x80000000\n"
"s_mov_b32 s94, 0x80000000\n"
"s_mov_b32 s93, 0x80000000\n"
"s_mov_b32 s94, 0x80000000\n"
"s_mov_b32 s95, 0x80000000\n"
"s_mov_b32 s95, 0x80000000\n"
"v_mov_b32 v142, 0\n"
"v_mov_b32 v143, 0\n"
"v_mov_b32 v148, 0\n"
"v_mov_b32 v149, 0\n"
"v_mov_b32 v144, 0\n"
"v_mov_b32 v145, 0\n"
"v_mov_b32 v172, 0\n"
"v_mov_b32 v173, 0\n"
"v_mov_b32 v140, 0\n"
"v_mov_b32 v141, 0\n"
"s_cmp_eq_u32 s54, 0\n"
"s_cbranch_scc1 .LH_1s\n"
"s_cmp_eq_u32 s54, 1\n"
"s_cbranch_scc1 .LH_2s\n"
"s_cmp_eq_u32 s54, 2\n"
"s_cbranch_scc1 .LH_3s\n"
"s_cmp_eq_u32 s54, 3\n"
"s_cbranch_scc1 .LH_4s\n"
".p2align 6\n"
".LH_top:\n"
"v_mfma_f32_16x16x32_bf16 v[60:63], v[104:107], a[0:3], 0\n"
"v_mfma_f32_16x16x32_bf16 v[60:63], v[108:111], a[4:7], v[60:63]\n"
"v_mfma_f32_16x16x32_bf16 v[60:63], v[68:71], a[8:11], v[60:63]\n"
"v_mfma_f32_16x16x32_bf16 v[60:63], v[72:75], a[12:15], v[60:63]\n"
"s_waitcnt vmcnt(7)\n"
"global_load_dwordx4 v[48:51], v8, s[56:57] offset:0 sc1\n"
"v_mfma_f32_16x16x32_bf16 v[60:63], v[76:79], a[16:19], v[60:63]\n"
"s_waitcnt lgkmcnt(3)\n"
"v_add_f32 v96, v96, v52\n"
"v_add_f32 v97, v97, v53\n"
"v_mfma_f32_16x16x32_bf16 v[60:63], v[80:83], a[20:23], v[60:63]\n"
"s_waitcnt vmcnt(6)\n"
"v_add_f32 v98, v98, v54\n"
"v_add_f32 v99, v99, v55\n"
"v_mfma_f32_16x16x32_bf16 v[60:63], v[84:87], a[24:27], v[60:63]\n"
"buffer_load_dwordx2 v[144:145], v12, s[40:43], s50 offen sc1\n"
"v_mfma_f32_16x16x32_bf16 v[64:67], v[104:107], a[28:31], 0\n"
"global_load_dwordx4 v[52:55], v9, s[56:57] offset:0 sc1\n"
"v_add_f32 v96, v96, v88\n"
"v_mfma_f32_16x16x32_bf16 v[64:67], v[108:111], a[32:35], v[64:67]\n"
"ds_write_addtid_b32 v60 offset:0\n"
"v_add_f32 v97, v97, v89\n"
"v_mfma_f32_16x16x32_bf16 v[64:67], v[68:71], a[36:39], v[64:67]\n"
"ds_write_addtid_b32 v61 offset:4640\n"
"v_add_f32 v98, v98, v90\n"
"v_mfma_f32_16x16x32_bf16 v[64:67], v[72:75], a[40:43], v[64:67]\n"
"ds_write_addtid_b32 v62 offset:9344\n"
"v_add_f32 v99, v99, v91\n"
"v_mfma_f32_16x16x32_bf16 v[64:67], v[76:79], a[44:47], v[64:67]\n"
"ds_write_addtid_b32 v63 offset:13984\n"
"v_add_f32 v96, v96, v124\n"
"v_mfma_f32_16x16x32_bf16 v[64:67], v[80:83], a[48:51], v[64:67]\n"
"global_load_dwordx4 v[56:59], v10, s[56:57] offset:0 sc1\n"
"v_add_f32 v97, v97, v125\n"
"v_mfma_f32_16x16x32_bf16 v[64:67], v[84:87], a[52:55], v[64:67]\n"
"s_waitcnt vmcnt(8)\n"
"buffer_store_dwordx2 v[0:1], v14, s[40:43], s94 offen offset:0 sc1\n"
"v_add_f32 v98, v98, v126\n"
"v_mfma_f32_16x16x32_bf16 v[88:91], v[104:107], a[56:59], 0\n"
"v_add_f32 v99, v99, v127\n"
"v_mfma_f32_16x16x32_bf16 v[88:91], v[108:111], a[60:63], v[88:91]\n"
"ds_write_addtid_b32 v64 offset:256\n"
"global_load_dwordx4 v[60:63], v11, s[56:57] offset:0 sc1\n"
"v_mfma_f32_16x16x32_bf16 v[88:91], v[68:71], a[64:67], v[88:91]\n"
"s_add_u32 s94, s50, 0xffd00000\n"
"s_and_b32 s95, s94, 0xfbffffff\n"
"ds_write_addtid_b32 v65 offset:4896\n"
"v_mfma_f32_16x16x32_bf16 v[88:91], v[72:75], a[68:71], v[88:91]\n"
"ds_write_addtid_b32 v66 offset:9600\n"
"v_cvt_pk_bf16_f32 v92, v96, v97\n"
"v_mfma_f32_16x16x32_bf16 v[88:91], v[76:79], a[72:75], v[88:91]\n"
"ds_write_addtid_b32 v67 offset:14240\n"
"v_cvt_pk_bf16_f32 v93, v98, v99\n"
"v_mfma_f32_16x16x32_bf16 v[88:91], v[80:83], a[76:79], v[88:91]\n"
"s_waitcnt lgkmcnt(8)\n"
"v_add_f32 v132, v132, v136\n"
"v_add_f32 v133, v133, v137\n"
"v_mfma_f32_16x16x32_bf16 v[88:91], v[84:87], a[80:83], v[88:91]\n"
"v_add_f32 v134, v134, v138\n"
"v_add_f32 v135, v135, v139\n"
"v_mfma_f32_16x16x32_bf16 v[64:67], v[104:107], a[84:87], 0\n"
"v_add_f32 v132, v132, v140\n"
"v_add_f32 v133, v133, v141\n"
"v_mfma_f32_16x16x32_bf16 v[64:67], v[108:111], a[88:91], v[64:67]\n"
"ds_write_addtid_b32 v88 offset:512\n"
"v_add_f32 v134, v134, v142\n"
"v_mfma_f32_16x16x32_bf16 v[64:67], v[68:71], a[92:95], v[64:67]\n"
"ds_write_addtid_b32 v89 offset:5152\n"
"v_add_f32 v135, v135, v143\n"
"v_mfma_f32_16x16x32_bf16 v[64:67], v[72:75], a[96:99], v[64:67]\n"
"ds_write_addtid_b32 v90 offset:9856\n"
"v_add_f32 v132, v132, v128\n"
"v_mfma_f32_16x16x32_bf16 v[64:67], v[76:79], a[100:103], v[64:67]\n"
"ds_write_addtid_b32 v91 offset:14496\n"
"v_add_f32 v133, v133, v129\n"
"v_mfma_f32_16x16x32_bf16 v[64:67], v[80:83], a[104:107], v[64:67]\n"
"v_add_f32 v134, v134, v130\n"
"v_add_f32 v135, v135, v131\n"
"v_mfma_f32_16x16x32_bf16 v[64:67], v[84:87], a[108:111], v[64:67]\n"
"v_cvt_pk_bf16_f32 v94, v132, v133\n"
"v_cvt_pk_bf16_f32 v95, v134, v135\n"
"v_mfma_f32_16x16x32_bf16 v[88:91], v[104:107], a[112:115], 0\n"
"v_mfma_f32_16x16x32_bf16 v[88:91], v[108:111], a[116:119], v[88:91]\n"
"ds_write_addtid_b32 v64 offset:768\n"
"v_mfma_f32_16x16x32_bf16 v[88:91], v[68:71], a[120:123], v[88:91]\n"
"ds_write_addtid_b32 v65 offset:5408\n"
"v_mfma_f32_16x16x32_bf16 v[88:91], v[72:75], a[124:127], v[88:91]\n"
"ds_write_addtid_b32 v66 offset:10112\n"
"v_mfma_f32_16x16x32_bf16 v[88:91], v[76:79], a[128:131], v[88:91]\n"
"ds_write_addtid_b32 v67 offset:14752\n"
"v_mfma_f32_16x16x32_bf16 v[88:91], v[80:83], a[132:135], v[88:91]\n"
"v_mfma_f32_16x16x32_bf16 v[88:91], v[84:87], a[136:139], v[88:91]\n"
"v_mfma_f32_16x16x32_bf16 v[96:99], v[104:107], a[140:143], 0\n"
"v_mfma_f32_16x16x32_bf16 v[96:99], v[108:111], a[144:147], v[96:99]\n"
"ds_write_addtid_b32 v88 offset:1024\n"
"v_mfma_f32_16x16x32_bf16 v[96:99], v[68:71], a[148:151], v[96:99]\n"
"ds_write_addtid_b32 v89 offset:5664\n"
"v_mfma_f32_16x16x32_bf16 v[96:99], v[72:75], a[152:155], v[96:99]\n"
"ds_write_addtid_b32 v90 offset:10368\n"
"v_mfma_f32_16x16x32_bf16 v[96:99], v[76:79], a[156:159], v[96:99]\n"
"buffer_store_dwordx4 v[92:95], v13, s[40:43], s95 offen sc1\n"
"ds_write_addtid_b32 v91 offset:15008\n"
"v_mfma_f32_16x16x32_bf16 v[96:99], v[80:83], a[160:163], v[96:99]\n"
"v_mfma_f32_16x16x32_bf16 v[96:99], v[84:87], a[164:167], v[96:99]\n"
"v_mfma_f32_16x16x32_bf16 v[92:95], a[168:171], v[104:107], 0\n"
"v_mfma_f32_16x16x32_bf16 v[92:95], a[172:175], v[108:111], v[92:95]\n"
"ds_write_addtid_b32 v96 offset:1280\n"
"v_mfma_f32_16x16x32_bf16 v[92:95], a[176:179], v[68:71], v[92:95]\n"
"ds_write_addtid_b32 v97 offset:5920\n"
"v_mfma_f32_16x16x32_bf16 v[92:95], a[180:183], v[72:75], v[92:95]\n"
"ds_write_addtid_b32 v98 offset:10624\n"
"v_mfma_f32_16x16x32_bf16 v[92:95], a[184:187], v[76:79], v[92:95]\n"
"ds_write_addtid_b32 v99 offset:15264\n"
"v_mfma_f32_16x16x32_bf16 v[92:95], a[188:191], v[80:83], v[92:95]\n"
"v_mfma_f32_16x16x32_bf16 v[92:95], a[192:195], v[84:87], v[92:95]\n"
"v_mfma_f32_16x16x32_bf16 v[100:103], a[196:199], v[104:107], 0\n"
"v_mfma_f32_16x16x32_bf16 v[100:103], a[200:203], v[108:111], v[100:103]\n"
"v_mfma_f32_16x16x32_bf16 v[100:103], a[204:207], v[68:71], v[100:103]\n"
"v_mfma_f32_16x16x32_bf16 v[100:103], a[208:211], v[72:75], v[100:103]\n"
"v_mfma_f32_16x16x32_bf16 v[100:103], a[212:215], v[76:79], v[100:103]\n"
"v_mfma_f32_16x16x32_bf16 v[100:103], a[216:219], v[80:83], v[100:103]\n"
"v_mfma_f32_16x16x32_bf16 v[100:103], a[220:223], v[84:87], v[100:103]\n"
"v_mfma_f32_16x16x32_bf16 v[96:99], a[224:227], v[104:107], 0\n"
"s_add_u32 s58, s50, 0x180000\n"
"v_mfma_f32_16x16x32_bf16 v[96:99], a[228:231], v[108:111], v[96:99]\n"
"s_and_b32 s58, s58, 0x3ffffff\n"
"v_mfma_f32_16x16x32_bf16 v[96:99], a[232:235], v[68:71], v[96:99]\n"
"s_add_u32 s56, s40, s58\n"
"v_mfma_f32_16x16x32_bf16 v[96:99], a[236:239], v[72:75], v[96:99]\n"
"s_addc_u32 s57, s41, 0\n"
"v_mfma_f32_16x16x32_bf16 v[96:99], a[240:243], v[76:79], v[96:99]\n"
"s_waitcnt vmcnt(5)\n"
"v_cmp_ne_u64 vcc, s[48:49], v[144:145]\n"
"v_mfma_f32_16x16x32_bf16 v[96:99], a[244:247], v[80:83], v[96:99]\n"
"v_mfma_f32_16x16x32_bf16 v[96:99], a[248:251], v[84:87], v[96:99]\n"
"s_cbranch_vccnz .LH_3s\n"
".LH_3r:\n"
"v_mfma_f32_16x16x32_bf16 v[124:127], a[252:255], v[104:107], 0\n"
"s_add_u32 s50, s50, 0x180000\n"
"global_load_dwordx4 v[64:67], v5, s[56:57] offset:0 sc1\n"
"v_mfma_f32_16x16x32_bf16 v[124:127], v[24:27], v[108:111], v[124:127]\n"
"s_waitcnt lgkmcnt(0)\n"
"v_mfma_f32_16x16x32_bf16 v[124:127], v[28:31], v[68:71], v[124:127]\n"
"s_barrier\n"
"v_mfma_f32_16x16x32_bf16 v[124:127], v[32:35], v[72:75], v[124:127]\n"
"ds_read_b128 v[104:107], v2 offset:2048\n"
"ds_read_b128 v[88:91], v3 offset:512\n"
"v_mfma_f32_16x16x32_bf16 v[124:127], v[36:39], v[76:79], v[124:127]\n"
"global_load_dwordx4 v[68:71], v6, s[56:57] offset:0 sc1\n"
"ds_read_b128 v[108:111], v4 offset:3072\n"
"v_mfma_f32_16x16x32_bf16 v[124:127], v[40:43], v[80:83], v[124:127]\n"
"ds_read_b128 v[128:131], v2 offset:2304\n"
"ds_read_b128 v[132:135], v3 offset:768\n"
"v_mfma_f32_16x16x32_bf16 v[124:127], v[44:47], v[84:87], v[124:127]\n"
"ds_read_b128 v[136:139], v4 offset:3328\n"
"ds_read_b128 v[140:143], v2 offset:3584\n"
"v_mfma_f32_16x16x32_bf16 v[144:147], v[112:115], a[0:3], 0\n"
"global_load_dwordx4 v[72:75], v7, s[56:57] offset:0 sc1\n"
"ds_read_b128 v[148:151], v3 offset:1536\n"
"v_mfma_f32_16x16x32_bf16 v[144:147], v[116:119], a[4:7], v[144:147]\n"
"ds_read_b128 v[152:155], v4 offset:3584\n"
"ds_read_b128 v[156:159], v2 offset:3840\n"
"v_mfma_f32_16x16x32_bf16 v[144:147], v[120:123], a[8:11], v[144:147]\n"
"ds_read_b128 v[160:163], v3 offset:1792\n"
"ds_read_b128 v[164:167], v4 offset:3840\n"
"v_mfma_f32_16x16x32_bf16 v[144:147], v[48:51], a[12:15], v[144:147]\n"
"s_waitcnt vmcnt(6)\n"
"v_mfma_f32_16x16x32_bf16 v[144:147], v[52:55], a[16:19], v[144:147]\n"
"s_waitcnt lgkmcnt(9)\n"
"global_load_dwordx4 v[76:79], v8, s[56:57] offset:0 sc1\n"
"v_add_f32 v104, v104, v88\n"
"v_mfma_f32_16x16x32_bf16 v[144:147], v[56:59], a[20:23], v[144:147]\n"
"s_waitcnt vmcnt(5)\n"
"v_add_f32 v105, v105, v89\n"
"v_add_f32 v106, v106, v90\n"
"v_mfma_f32_16x16x32_bf16 v[144:147], v[60:63], a[24:27], v[144:147]\n"
"s_add_u32 s95, s50, 0xff000000\n"
"buffer_load_dwordx2 v[172:173], v12, s[40:43], s50 offen sc1\n"
"v_mfma_f32_16x16x32_bf16 v[168:171], v[112:115], a[28:31], 0\n"
"v_add_f32 v107, v107, v91\n"
"v_add_f32 v104, v104, v108\n"
"v_mfma_f32_16x16x32_bf16 v[168:171], v[116:119], a[32:35], v[168:171]\n"
"ds_write_addtid_b32 v144 offset:2048\n"
"global_load_dwordx4 v[80:83], v9, s[56:57] offset:0 sc1\n"
"v_mfma_f32_16x16x32_bf16 v[168:171], v[120:123], a[36:39], v[168:171]\n"
"ds_write_addtid_b32 v145 offset:6688\n"
"v_add_f32 v105, v105, v109\n"
"v_mfma_f32_16x16x32_bf16 v[168:171], v[48:51], a[40:43], v[168:171]\n"
"ds_write_addtid_b32 v146 offset:11392\n"
"global_load_dwordx4 v[84:87], v10, s[56:57] offset:0 sc1\n"
"v_mfma_f32_16x16x32_bf16 v[168:171], v[52:55], a[44:47], v[168:171]\n"
"ds_write_addtid_b32 v147 offset:16032\n"
"v_add_f32 v106, v106, v110\n"
"v_mfma_f32_16x16x32_bf16 v[168:171], v[56:59], a[48:51], v[168:171]\n"
"v_add_f32 v107, v107, v111\n"
"v_add_f32 v104, v104, v92\n"
"v_mfma_f32_16x16x32_bf16 v[168:171], v[60:63], a[52:55], v[168:171]\n"
"v_add_f32 v105, v105, v93\n"
"v_add_f32 v106, v106, v94\n"
"v_mfma_f32_16x16x32_bf16 v[108:111], v[112:115], a[56:59], 0\n"
"s_cmp_lt_u32 s95, 0x8000000\n"
"s_cselect_b32 s95, 0x80000000, s95\n"
"v_add_f32 v107, v107, v95\n"
"v_mfma_f32_16x16x32_bf16 v[108:111], v[116:119], a[60:63], v[108:111]\n"
"s_add_u32 s92, s50, 0xffd00000\n"
"s_and_b32 s93, s92, 0xfbffffff\n"
"ds_write_addtid_b32 v168 offset:2304\n"
"v_mfma_f32_16x16x32_bf16 v[108:111], v[120:123], a[64:67], v[108:111]\n"
"ds_write_addtid_b32 v169 offset:6944\n"
"v_cvt_pk_bf16_f32 v92, v104, v105\n"
"v_mfma_f32_16x16x32_bf16 v[108:111], v[48:51], a[68:71], v[108:111]\n"
"s_waitcnt vmcnt(7)\n"
"ds_write_addtid_b32 v170 offset:11648\n"
"global_load_dwordx4 v[88:91], v11, s[56:57] offset:0 sc1\n"
"v_mfma_f32_16x16x32_bf16 v[108:111], v[52:55], a[72:75], v[108:111]\n"
"ds_write_addtid_b32 v171 offset:16288\n"
"v_cvt_pk_bf16_f32 v93, v106, v107\n"
"v_mfma_f32_16x16x32_bf16 v[108:111], v[56:59], a[76:79], v[108:111]\n"
"s_waitcnt lgkmcnt(14)\n"
"v_add_f32 v128, v128, v132\n"
"v_add_f32 v129, v129, v133\n"
"v_mfma_f32_16x16x32_bf16 v[108:111], v[60:63], a[80:83], v[108:111]\n"
"v_add_f32 v130, v130, v134\n"
"v_add_f32 v131, v131, v135\n"
"v_mfma_f32_16x16x32_bf16 v[104:107], v[112:115], a[84:87], 0\n"
"v_add_f32 v128, v128, v136\n"
"v_add_f32 v129, v129, v137\n"
"v_mfma_f32_16x16x32_bf16 v[104:107], v[116:119], a[88:91], v[104:107]\n"
"ds_write_addtid_b32 v108 offset:2560\n"
"v_add_f32 v130, v130, v138\n"
"v_mfma_f32_16x16x32_bf16 v[104:107], v[120:123], a[92:95], v[104:107]\n"
"ds_write_addtid_b32 v109 offset:7200\n"
"v_add_f32 v131, v131, v139\n"
"v_mfma_f32_16x16x32_bf16 v[104:107], v[48:51], a[96:99], v[104:107]\n"
"ds_write_addtid_b32 v110 offset:11904\n"
"v_add_f32 v128, v128, v100\n"
"v_mfma_f32_16x16x32_bf16 v[104:107], v[52:55], a[100:103], v[104:107]\n"
"ds_write_addtid_b32 v111 offset:16544\n"
"v_add_f32 v129, v129, v101\n"
"v_mfma_f32_16x16x32_bf16 v[104:107], v[56:59], a[104:107], v[104:107]\n"
"v_add_f32 v130, v130, v102\n"
"v_add_f32 v131, v131, v103\n"
"v_mfma_f32_16x16x32_bf16 v[104:107], v[60:63], a[108:111], v[104:107]\n"
"v_cvt_pk_bf16_f32 v94, v128, v129\n"
"v_cvt_pk_bf16_f32 v95, v130, v131\n"
"v_mfma_f32_16x16x32_bf16 v[100:103], v[112:115], a[112:115], 0\n"
"buffer_store_dwordx4 v[92:95], v13, s[40:43], s93 offen sc1\n"
"v_mfma_f32_16x16x32_bf16 v[100:103], v[116:119], a[116:119], v[100:103]\n"
"s_waitcnt lgkmcnt(15)\n"
"ds_write_addtid_b32 v104 offset:2816\n"
"v_add_f32 v140, v140, v148\n"
"v_mfma_f32_16x16x32_bf16 v[100:103], v[120:123], a[120:123], v[100:103]\n"
"ds_write_addtid_b32 v105 offset:7456\n"
"v_add_f32 v141, v141, v149\n"
"v_mfma_f32_16x16x32_bf16 v[100:103], v[48:51], a[124:127], v[100:103]\n"
"ds_write_addtid_b32 v106 offset:12160\n"
"v_add_f32 v142, v142, v150\n"
"v_mfma_f32_16x16x32_bf16 v[100:103], v[52:55], a[128:131], v[100:103]\n"
"ds_write_addtid_b32 v107 offset:16800\n"
"v_add_f32 v143, v143, v151\n"
"v_mfma_f32_16x16x32_bf16 v[100:103], v[56:59], a[132:135], v[100:103]\n"
"v_add_f32 v140, v140, v152\n"
"v_add_f32 v141, v141, v153\n"
"v_mfma_f32_16x16x32_bf16 v[100:103], v[60:63], a[136:139], v[100:103]\n"
"v_add_f32 v142, v142, v154\n"
"v_add_f32 v143, v143, v155\n"
"v_mfma_f32_16x16x32_bf16 v[92:95], v[112:115], a[140:143], 0\n"
"v_add_f32 v140, v140, v96\n"
"v_add_f32 v141, v141, v97\n"
"v_mfma_f32_16x16x32_bf16 v[92:95], v[116:119], a[144:147], v[92:95]\n"
"ds_write_addtid_b32 v100 offset:3072\n"
"v_add_f32 v142, v142, v98\n"
"v_mfma_f32_16x16x32_bf16 v[92:95], v[120:123], a[148:151], v[92:95]\n"
"ds_write_addtid_b32 v101 offset:7712\n"
"v_add_f32 v143, v143, v99\n"
"v_mfma_f32_16x16x32_bf16 v[92:95], v[48:51], a[152:155], v[92:95]\n"
"ds_write_addtid_b32 v102 offset:12416\n"
"v_cvt_pk_bf16_f32 v104, v140, v141\n"
"v_mfma_f32_16x16x32_bf16 v[92:95], v[52:55], a[156:159], v[92:95]\n"
"ds_write_addtid_b32 v103 offset:17056\n"
"v_cvt_pk_bf16_f32 v105, v142, v143\n"
"v_mfma_f32_16x16x32_bf16 v[92:95], v[56:59], a[160:163], v[92:95]\n"
"s_waitcnt lgkmcnt(15)\n"
"v_add_f32 v156, v156, v160\n"
"v_add_f32 v157, v157, v161\n"
"v_mfma_f32_16x16x32_bf16 v[92:95], v[60:63], a[164:167], v[92:95]\n"
"v_add_f32 v158, v158, v162\n"
"v_add_f32 v159, v159, v163\n"
"v_mfma_f32_16x16x32_bf16 v[96:99], v[112:115], a[224:227], 0\n"
"v_add_f32 v156, v156, v164\n"
"v_add_f32 v157, v157, v165\n"
"v_mfma_f32_16x16x32_bf16 v[96:99], v[116:119], a[228:231], v[96:99]\n"
"ds_write_addtid_b32 v92 offset:3328\n"
"v_add_f32 v158, v158, v166\n"
"v_mfma_f32_16x16x32_bf16 v[96:99], v[120:123], a[232:235], v[96:99]\n"
"ds_write_addtid_b32 v93 offset:7968\n"
"v_add_f32 v159, v159, v167\n"
"v_mfma_f32_16x16x32_bf16 v[96:99], v[48:51], a[236:239], v[96:99]\n"
"ds_write_addtid_b32 v94 offset:12672\n"
"v_add_f32 v156, v156, v124\n"
"v_mfma_f32_16x16x32_bf16 v[96:99], v[52:55], a[240:243], v[96:99]\n"
"ds_write_addtid_b32 v95 offset:17312\n"
"v_add_f32 v157, v157, v125\n"
"v_mfma_f32_16x16x32_bf16 v[96:99], v[56:59], a[244:247], v[96:99]\n"
"v_add_f32 v158, v158, v126\n"
"v_add_f32 v159, v159, v127\n"
"v_mfma_f32_16x16x32_bf16 v[96:99], v[60:63], a[248:251], v[96:99]\n"
"v_cvt_pk_bf16_f32 v106, v156, v157\n"
"v_cvt_pk_bf16_f32 v107, v158, v159\n"
"v_mfma_f32_16x16x32_bf16 v[100:103], v[112:115], a[252:255], 0\n"
"buffer_store_dwordx4 v[104:107], v15, s[40:43], s93 offen sc1\n"
"v_mfma_f32_16x16x32_bf16 v[100:103], v[116:119], v[24:27], v[100:103]\n"
"s_add_u32 s58, s50, 0x180000\n"
"buffer_store_dwordx2 v[0:1], v14, s[40:43], s94 offen offset:0 sc1\n"
"ds_write_addtid_b32 v96 offset:3584\n"
"v_mfma_f32_16x16x32_bf16 v[100:103], v[120:123], v[28:31], v[100:103]\n"
"s_and_b32 s58, s58, 0x3ffffff\n"
"ds_write_addtid_b32 v97 offset:8224\n"
"v_mfma_f32_16x16x32_bf16 v[100:103], v[48:51], v[32:35], v[100:103]\n"
"s_add_u32 s56, s40, s58\n"
"ds_write_addtid_b32 v98 offset:12928\n"
"v_mfma_f32_16x16x32_bf16 v[100:103], v[52:55], v[36:39], v[100:103]\n"
"s_addc_u32 s57, s41, 0\n"
"ds_write_addtid_b32 v99 offset:17568\n"
"v_mfma_f32_16x16x32_bf16 v[100:103], v[56:59], v[40:43], v[100:103]\n"
"s_waitcnt vmcnt(6)\n"
"v_cmp_ne_u64 vcc, s[48:49], v[172:173]\n"
"v_mfma_f32_16x16x32_bf16 v[100:103], v[60:63], v[44:47], v[100:103]\n"
"buffer_load_dwordx2 v[140:141], v17, s[40:43], s95 offen offset:0 sc1\n"
"v_mfma_f32_16x16x32_bf16 v[104:107], a[168:171], v[112:115], 0\n"
"s_cbranch_vccnz .LH_4s\n"
".LH_4r:\n"
"v_mfma_f32_16x16x32_bf16 v[104:107], a[172:175], v[116:119], v[104:107]\n"
"s_add_u32 s50, s50, 0x180000\n"
"ds_write_addtid_b32 v100 offset:3840\n"
"v_mfma_f32_16x16x32_bf16 v[104:107], a[176:179], v[120:123], v[104:107]\n"
"ds_write_addtid_b32 v101 offset:8480\n"
"v_mfma_f32_16x16x32_bf16 v[104:107], a[180:183], v[48:51], v[104:107]\n"
"ds_write_addtid_b32 v102 offset:13184\n"
"v_mfma_f32_16x16x32_bf16 v[104:107], a[184:187], v[52:55], v[104:107]\n"
"ds_write_addtid_b32 v103 offset:17824\n"
"global_load_dwordx4 v[92:95], v5, s[56:57] offset:0 sc1\n"
"v_mfma_f32_16x16x32_bf16 v[104:107], a[188:191], v[56:59], v[104:107]\n"
"v_mfma_f32_16x16x32_bf16 v[104:107], a[192:195], v[60:63], v[104:107]\n"
"v_mfma_f32_16x16x32_bf16 v[108:111], a[196:199], v[112:115], 0\n"
"v_mfma_f32_16x16x32_bf16 v[108:111], a[200:203], v[116:119], v[108:111]\n"
"s_waitcnt lgkmcnt(0)\n"
"global_load_dwordx4 v[96:99], v6, s[56:57] offset:0 sc1\n"
"v_mfma_f32_16x16x32_bf16 v[108:111], a[204:207], v[120:123], v[108:111]\n"
"s_barrier\n"
"v_mfma_f32_16x16x32_bf16 v[108:111], a[208:211], v[48:51], v[108:111]\n"
"ds_read_b128 v[112:115], v2 offset:0\n"
"ds_read_b128 v[100:103], v3 offset:2560\n"
"v_mfma_f32_16x16x32_bf16 v[108:111], a[212:215], v[52:55], v[108:111]\n"
"ds_read_b128 v[116:119], v4 offset:1024\n"
"ds_read_b128 v[120:123], v2 offset:256\n"
"v_mfma_f32_16x16x32_bf16 v[108:111], a[216:219], v[56:59], v[108:111]\n"
"ds_read_b128 v[124:127], v3 offset:2816\n"
"ds_read_b128 v[128:131], v4 offset:1280\n"
"v_mfma_f32_16x16x32_bf16 v[108:111], a[220:223], v[60:63], v[108:111]\n"
"v_mfma_f32_16x16x32_bf16 v[132:135], v[64:67], a[0:3], 0\n"
"v_mfma_f32_16x16x32_bf16 v[132:135], v[68:71], a[4:7], v[132:135]\n"
"global_load_dwordx4 v[48:51], v7, s[56:57] offset:0 sc1\n"
"v_mfma_f32_16x16x32_bf16 v[132:135], v[72:75], a[8:11], v[132:135]\n"
"v_mfma_f32_16x16x32_bf16 v[132:135], v[76:79], a[12:15], v[132:135]\n"
"s_waitcnt vmcnt(8)\n"
"v_mfma_f32_16x16x32_bf16 v[132:135], v[80:83], a[16:19], v[132:135]\n"
"s_waitcnt lgkmcnt(3)\n"
"v_add_f32 v112, v112, v100\n"
"v_add_f32 v113, v113, v101\n"
"v_mfma_f32_16x16x32_bf16 v[132:135], v[84:87], a[20:23], v[132:135]\n"
"s_waitcnt vmcnt(7)\n"
"global_load_dwordx4 v[52:55], v8, s[56:57] offset:0 sc1\n"
"v_add_f32 v114, v114, v102\n"
"v_mfma_f32_16x16x32_bf16 v[132:135], v[88:91], a[24:27], v[132:135]\n"
"v_add_f32 v115, v115, v103\n"
"v_add_f32 v112, v112, v116\n"
"v_mfma_f32_16x16x32_bf16 v[136:139], v[64:67], a[28:31], 0\n"
"buffer_load_dwordx2 v[142:143], v12, s[40:43], s50 offen sc1\n"
"v_mfma_f32_16x16x32_bf16 v[136:139], v[68:71], a[32:35], v[136:139]\n"
"ds_write_addtid_b32 v132 offset:0\n"
"v_add_f32 v113, v113, v117\n"
"v_mfma_f32_16x16x32_bf16 v[136:139], v[72:75], a[36:39], v[136:139]\n"
"ds_write_addtid_b32 v133 offset:4640\n"
"global_load_dwordx4 v[56:59], v9, s[56:57] offset:0 sc1\n"
"v_mfma_f32_16x16x32_bf16 v[136:139], v[76:79], a[40:43], v[136:139]\n"
"ds_write_addtid_b32 v134 offset:9344\n"
"v_add_f32 v114, v114, v118\n"
"v_mfma_f32_16x16x32_bf16 v[136:139], v[80:83], a[44:47], v[136:139]\n"
"ds_write_addtid_b32 v135 offset:13984\n"
"global_load_dwordx4 v[60:63], v10, s[56:57] offset:0 sc1\n"
"v_mfma_f32_16x16x32_bf16 v[136:139], v[84:87], a[48:51], v[136:139]\n"
"v_add_f32 v115, v115, v119\n"
"v_add_f32 v112, v112, v104\n"
"v_mfma_f32_16x16x32_bf16 v[136:139], v[88:91], a[52:55], v[136:139]\n"
"v_add_f32 v113, v113, v105\n"
"v_add_f32 v114, v114, v106\n"
"v_mfma_f32_16x16x32_bf16 v[116:119], v[64:67], a[56:59], 0\n"
"v_add_f32 v115, v115, v107\n"
"v_mfma_f32_16x16x32_bf16 v[116:119], v[68:71], a[60:63], v[116:119]\n"
"s_add_u32 s94, s50, 0xffd00000\n"
"s_and_b32 s93, s94, 0xfbffffff\n"
"ds_write_addtid_b32 v136 offset:256\n"
"v_mfma_f32_16x16x32_bf16 v[116:119], v[72:75], a[64:67], v[116:119]\n"
"ds_write_addtid_b32 v137 offset:4896\n"
"global_load_dwordx4 v[100:103], v11, s[56:57] offset:0 sc1\n"
"v_mfma_f32_16x16x32_bf16 v[116:119], v[76:79], a[68:71], v[116:119]\n"
"ds_write_addtid_b32 v138 offset:9600\n"
"v_cvt_pk_bf16_f32 v104, v112, v113\n"
"v_mfma_f32_16x16x32_bf16 v[116:119], v[80:83], a[72:75], v[116:119]\n"
"ds_write_addtid_b32 v139 offset:14240\n"
"v_cvt_pk_bf16_f32 v105, v114, v115\n"
"v_mfma_f32_16x16x32_bf16 v[116:119], v[84:87], a[76:79], v[116:119]\n"
"s_waitcnt lgkmcnt(8)\n"
"v_add_f32 v120, v120, v124\n"
"v_add_f32 v121, v121, v125\n"
"v_mfma_f32_16x16x32_bf16 v[116:119], v[88:91], a[80:83], v[116:119]\n"
"v_add_f32 v122, v122, v126\n"
"v_add_f32 v123, v123, v127\n"
"v_mfma_f32_16x16x32_bf16 v[112:115], v[64:67], a[84:87], 0\n"
"v_add_f32 v120, v120, v128\n"
"v_add_f32 v121, v121, v129\n"
"v_mfma_f32_16x16x32_bf16 v[112:115], v[68:71], a[88:91], v[112:115]\n"
"ds_write_addtid_b32 v116 offset:512\n"
"v_add_f32 v122, v122, v130\n"
"v_mfma_f32_16x16x32_bf16 v[112:115], v[72:75], a[92:95], v[112:115]\n"
"ds_write_addtid_b32 v117 offset:5152\n"
"v_add_f32 v123, v123, v131\n"
"v_mfma_f32_16x16x32_bf16 v[112:115], v[76:79], a[96:99], v[112:115]\n"
"ds_write_addtid_b32 v118 offset:9856\n"
"v_add_f32 v120, v120, v108\n"
"v_mfma_f32_16x16x32_bf16 v[112:115], v[80:83], a[100:103], v[112:115]\n"
"ds_write_addtid_b32 v119 offset:14496\n"
"v_add_f32 v121, v121, v109\n"
"v_mfma_f32_16x16x32_bf16 v[112:115], v[84:87], a[104:107], v[112:115]\n"
"v_add_f32 v122, v122, v110\n"
"v_add_f32 v123, v123, v111\n"
"v_mfma_f32_16x16x32_bf16 v[112:115], v[88:91], a[108:111], v[112:115]\n"
"s_waitcnt vmcnt(10)\n"
"buffer_store_dwordx2 v[0:1], v16, s[40:43], s92 offen offset:0 sc1\n"
"v_cvt_pk_bf16_f32 v106, v120, v121\n"
"v_mfma_f32_16x16x32_bf16 v[108:111], v[64:67], a[112:115], 0\n"
"v_cvt_pk_bf16_f32 v107, v122, v123\n"
"v_mfma_f32_16x16x32_bf16 v[108:111], v[68:71], a[116:119], v[108:111]\n"
"ds_write_addtid_b32 v112 offset:768\n"
"v_mfma_f32_16x16x32_bf16 v[108:111], v[72:75], a[120:123], v[108:111]\n"
"ds_write_addtid_b32 v113 offset:5408\n"
"v_mfma_f32_16x16x32_bf16 v[108:111], v[76:79], a[124:127], v[108:111]\n"
"ds_write_addtid_b32 v114 offset:10112\n"
"v_mfma_f32_16x16x32_bf16 v[108:111], v[80:83], a[128:131], v[108:111]\n"
"buffer_store_dwordx4 v[104:107], v13, s[40:43], s93 offen sc1\n"
"ds_write_addtid_b32 v115 offset:14752\n"
"v_mfma_f32_16x16x32_bf16 v[108:111], v[84:87], a[132:135], v[108:111]\n"
"s_waitcnt vmcnt(10)\n"
"v_cmp_ne_u64 vcc, s[64:65], v[140:141]\n"
"v_mfma_f32_16x16x32_bf16 v[108:111], v[88:91], a[136:139], v[108:111]\n"
"v_mfma_f32_16x16x32_bf16 v[104:107], v[64:67], a[140:143], 0\n"
"s_cbranch_vccnz .LH_5s\n"
".LH_5r:\n"
"v_mfma_f32_16x16x32_bf16 v[104:107], v[68:71], a[144:147], v[104:107]\n"
"ds_write_addtid_b32 v108 offset:1024\n"
"v_mfma_f32_16x16x32_bf16 v[104:107], v[72:75], a[148:151], v[104:107]\n"
"ds_write_addtid_b32 v109 offset:5664\n"
"v_mfma_f32_16x16x32_bf16 v[104:107], v[76:79], a[152:155], v[104:107]\n"
"ds_write_addtid_b32 v110 offset:10368\n"
"v_mfma_f32_16x16x32_bf16 v[104:107], v[80:83], a[156:159], v[104:107]\n"
"ds_write_addtid_b32 v111 offset:15008\n"
"v_mfma_f32_16x16x32_bf16 v[104:107], v[84:87], a[160:163], v[104:107]\n"
"buffer_load_dwordx4 v[112:115], v18, s[40:43], s95 offen offset:0 sc1\n"
"v_mfma_f32_16x16x32_bf16 v[104:107], v[88:91], a[164:167], v[104:107]\n"
"buffer_load_dwordx4 v[116:119], v18, s[40:43], s95 offen offset:1024 sc1\n"
"v_mfma_f32_16x16x32_bf16 v[108:111], v[64:67], a[224:227], 0\n"
"v_mfma_f32_16x16x32_bf16 v[108:111], v[68:71], a[228:231], v[108:111]\n"
"ds_write_addtid_b32 v104 offset:1280\n"
"v_mfma_f32_16x16x32_bf16 v[108:111], v[72:75], a[232:235], v[108:111]\n"
"ds_write_addtid_b32 v105 offset:5920\n"
"v_mfma_f32_16x16x32_bf16 v[108:111], v[76:79], a[236:239], v[108:111]\n"
"ds_write_addtid_b32 v106 offset:10624\n"
"v_mfma_f32_16x16x32_bf16 v[108:111], v[80:83], a[240:243], v[108:111]\n"
"ds_write_addtid_b32 v107 offset:15264\n"
"v_mfma_f32_16x16x32_bf16 v[108:111], v[84:87], a[244:247], v[108:111]\n"
"v_mfma_f32_16x16x32_bf16 v[108:111], v[88:91], a[248:251], v[108:111]\n"
"v_mfma_f32_16x16x32_bf16 v[120:123], v[64:67], a[252:255], 0\n"
"v_mfma_f32_16x16x32_bf16 v[120:123], v[68:71], v[24:27], v[120:123]\n"
"s_add_u32 s58, s50, 0x180000\n"
"ds_write_addtid_b32 v108 offset:1536\n"
"v_mfma_f32_16x16x32_bf16 v[120:123], v[72:75], v[28:31], v[120:123]\n"
"s_and_b32 s58, s58, 0x3ffffff\n"
"ds_write_addtid_b32 v109 offset:6176\n"
"v_mfma_f32_16x16x32_bf16 v[120:123], v[76:79], v[32:35], v[120:123]\n"
"s_add_u32 s56, s40, s58\n"
"ds_write_addtid_b32 v110 offset:10880\n"
"v_mfma_f32_16x16x32_bf16 v[120:123], v[80:83], v[36:39], v[120:123]\n"
"s_addc_u32 s57, s41, 0\n"
"ds_write_addtid_b32 v111 offset:15520\n"
"v_mfma_f32_16x16x32_bf16 v[120:123], v[84:87], v[40:43], v[120:123]\n"
"s_waitcnt vmcnt(7)\n"
"v_cmp_ne_u64 vcc, s[48:49], v[142:143]\n"
"v_mfma_f32_16x16x32_bf16 v[120:123], v[88:91], v[44:47], v[120:123]\n"
"v_mfma_f32_16x16x32_bf16 v[124:127], a[168:171], v[64:67], 0\n"
"s_cbranch_vccnz .LH_1s\n"
".LH_1r:\n"
"v_mfma_f32_16x16x32_bf16 v[124:127], a[172:175], v[68:71], v[124:127]\n"
"s_add_u32 s50, s50, 0x180000\n"
"ds_write_addtid_b32 v120 offset:1792\n"
"v_mfma_f32_16x16x32_bf16 v[124:127], a[176:179], v[72:75], v[124:127]\n"
"ds_write_addtid_b32 v121 offset:6432\n"
"v_mfma_f32_16x16x32_bf16 v[124:127], a[180:183], v[76:79], v[124:127]\n"
"ds_write_addtid_b32 v122 offset:11136\n"
"v_mfma_f32_16x16x32_bf16 v[124:127], a[184:187], v[80:83], v[124:127]\n"
"ds_write_addtid_b32 v123 offset:15776\n"
"global_load_dwordx4 v[104:107], v5, s[56:57] offset:0 sc1\n"
"v_mfma_f32_16x16x32_bf16 v[124:127], a[188:191], v[84:87], v[124:127]\n"
"v_mfma_f32_16x16x32_bf16 v[124:127], a[192:195], v[88:91], v[124:127]\n"
"v_mfma_f32_16x16x32_bf16 v[120:123], a[196:199], v[64:67], 0\n"
"v_mfma_f32_16x16x32_bf16 v[120:123], a[200:203], v[68:71], v[120:123]\n"
"s_waitcnt lgkmcnt(0)\n"
"global_load_dwordx4 v[108:111], v6, s[56:57] offset:0 sc1\n"
"v_mfma_f32_16x16x32_bf16 v[120:123], a[204:207], v[72:75], v[120:123]\n"
"s_barrier\n"
"v_mfma_f32_16x16x32_bf16 v[120:123], a[208:211], v[76:79], v[120:123]\n"
"ds_read_b128 v[64:67], v2 offset:2048\n"
"ds_read_b128 v[128:131], v3 offset:512\n"
"v_mfma_f32_16x16x32_bf16 v[120:123], a[212:215], v[80:83], v[120:123]\n"
"ds_read_b128 v[132:135], v4 offset:3072\n"
"ds_read_b128 v[136:139], v2 offset:2304\n"
"v_mfma_f32_16x16x32_bf16 v[120:123], a[216:219], v[84:87], v[120:123]\n"
"global_load_dwordx4 v[68:71], v7, s[56:57] offset:0 sc1\n"
"ds_read_b128 v[140:143], v3 offset:768\n"
"v_mfma_f32_16x16x32_bf16 v[120:123], a[220:223], v[88:91], v[120:123]\n"
"ds_read_b128 v[144:147], v4 offset:3328\n"
"v_mfma_f32_16x16x32_bf16 v[84:87], v[92:95], a[0:3], 0\n"
"v_mfma_f32_16x16x32_bf16 v[84:87], v[96:99], a[4:7], v[84:87]\n"
"v_mfma_f32_16x16x32_bf16 v[84:87], v[48:51], a[8:11], v[84:87]\n"
"global_load_dwordx4 v[72:75], v8, s[56:57] offset:0 sc1\n"
"v_mfma_f32_16x16x32_bf16 v[84:87], v[52:55], a[12:15], v[84:87]\n"
"s_waitcnt vmcnt(9)\n"
"v_mfma_f32_16x16x32_bf16 v[84:87], v[56:59], a[16:19], v[84:87]\n"
"s_waitcnt lgkmcnt(3)\n"
"v_add_f32 v64, v64, v128\n"
"v_add_f32 v65, v65, v129\n"
"v_mfma_f32_16x16x32_bf16 v[84:87], v[60:63], a[20:23], v[84:87]\n"
"s_waitcnt vmcnt(8)\n"
"buffer_load_dwordx2 v[148:149], v12, s[40:43], s50 offen sc1\n"
"v_mfma_f32_16x16x32_bf16 v[84:87], v[100:103], a[24:27], v[84:87]\n"
"global_load_dwordx4 v[76:79], v9, s[56:57] offset:0 sc1\n"
"v_add_f32 v66, v66, v130\n"
"v_mfma_f32_16x16x32_bf16 v[88:91], v[92:95], a[28:31], 0\n"
"v_add_f32 v67, v67, v131\n"
"v_add_f32 v64, v64, v132\n"
"v_mfma_f32_16x16x32_bf16 v[88:91], v[96:99], a[32:35], v[88:91]\n"
"ds_write_addtid_b32 v84 offset:2048\n"
"v_add_f32 v65, v65, v133\n"
"v_mfma_f32_16x16x32_bf16 v[88:91], v[48:51], a[36:39], v[88:91]\n"
"ds_write_addtid_b32 v85 offset:6688\n"
"v_add_f32 v66, v66, v134\n"
"v_mfma_f32_16x16x32_bf16 v[88:91], v[52:55], a[40:43], v[88:91]\n"
"s_waitcnt vmcnt(8)\n"
"ds_write_addtid_b32 v86 offset:11392\n"
"global_load_dwordx4 v[80:83], v10, s[56:57] offset:0 sc1\n"
"v_mfma_f32_16x16x32_bf16 v[88:91], v[56:59], a[44:47], v[88:91]\n"
"buffer_store_dwordx2 v[0:1], v14, s[40:43], s94 offen offset:0 sc1\n"
"ds_write_addtid_b32 v87 offset:16032\n"
"v_mfma_f32_16x16x32_bf16 v[88:91], v[60:63], a[48:51], v[88:91]\n"
"v_add_f32 v67, v67, v135\n"
"v_add_f32 v64, v64, v124\n"
"v_mfma_f32_16x16x32_bf16 v[88:91], v[100:103], a[52:55], v[88:91]\n"
"v_add_f32 v65, v65, v125\n"
"v_add_f32 v66, v66, v126\n"
"v_mfma_f32_16x16x32_bf16 v[128:131], v[92:95], a[56:59], 0\n"
"global_load_dwordx4 v[84:87], v11, s[56:57] offset:0 sc1\n"
"v_add_f32 v67, v67, v127\n"
"v_mfma_f32_16x16x32_bf16 v[128:131], v[96:99], a[60:63], v[128:131]\n"
"s_add_u32 s94, s50, 0xffd00000\n"
"s_and_b32 s93, s94, 0xfbffffff\n"
"ds_write_addtid_b32 v88 offset:2304\n"
"v_mfma_f32_16x16x32_bf16 v[128:131], v[48:51], a[64:67], v[128:131]\n"
"ds_write_addtid_b32 v89 offset:6944\n"
"v_cvt_pk_bf16_f32 v124, v64, v65\n"
"v_mfma_f32_16x16x32_bf16 v[128:131], v[52:55], a[68:71], v[128:131]\n"
"ds_write_addtid_b32 v90 offset:11648\n"
"v_cvt_pk_bf16_f32 v125, v66, v67\n"
"v_mfma_f32_16x16x32_bf16 v[128:131], v[56:59], a[72:75], v[128:131]\n"
"s_waitcnt lgkmcnt(7)\n"
"ds_write_addtid_b32 v91 offset:16288\n"
"v_add_f32 v136, v136, v140\n"
"v_mfma_f32_16x16x32_bf16 v[128:131], v[60:63], a[76:79], v[128:131]\n"
"s_waitcnt vmcnt(9)\n"
"v_add_f32 v137, v137, v141\n"
"v_add_f32 v138, v138, v142\n"
"v_mfma_f32_16x16x32_bf16 v[128:131], v[100:103], a[80:83], v[128:131]\n"
"v_add_f32 v139, v139, v143\n"
"v_add_f32 v136, v136, v144\n"
"v_mfma_f32_16x16x32_bf16 v[64:67], v[92:95], a[84:87], 0\n"
"v_add_f32 v137, v137, v145\n"
"v_add_f32 v138, v138, v146\n"
"v_mfma_f32_16x16x32_bf16 v[64:67], v[96:99], a[88:91], v[64:67]\n"
"ds_write_addtid_b32 v128 offset:2560\n"
"v_add_f32 v139, v139, v147\n"
"v_mfma_f32_16x16x32_bf16 v[64:67], v[48:51], a[92:95], v[64:67]\n"
"ds_write_addtid_b32 v129 offset:7200\n"
"v_add_f32 v136, v136, v120\n"
"v_mfma_f32_16x16x32_bf16 v[64:67], v[52:55], a[96:99], v[64:67]\n"
"ds_write_addtid_b32 v130 offset:11904\n"
"v_add_f32 v137, v137, v121\n"
"v_mfma_f32_16x16x32_bf16 v[64:67], v[56:59], a[100:103], v[64:67]\n"
"ds_write_addtid_b32 v131 offset:16544\n"
"v_add_f32 v138, v138, v122\n"
"v_mfma_f32_16x16x32_bf16 v[64:67], v[60:63], a[104:107], v[64:67]\n"
"v_add_f32 v139, v139, v123\n"
"v_cvt_pk_bf16_f32 v126, v136, v137\n"
"v_mfma_f32_16x16x32_bf16 v[64:67], v[100:103], a[108:111], v[64:67]\n"
"v_cvt_pk_bf16_f32 v127, v138, v139\n"
"v_add_f32 v112, v112, v116\n"
"v_mfma_f32_16x16x32_bf16 v[88:91], v[92:95], a[112:115], 0\n"
"v_add_f32 v113, v113, v117\n"
"v_add_f32 v114, v114, v118\n"
"v_mfma_f32_16x16x32_bf16 v[88:91], v[96:99], a[116:119], v[88:91]\n"
"ds_write_addtid_b32 v64 offset:2816\n"
"v_add_f32 v115, v115, v119\n"
"v_mfma_f32_16x16x32_bf16 v[88:91], v[48:51], a[120:123], v[88:91]\n"
"ds_write_addtid_b32 v65 offset:7456\n"
"v_mfma_f32_16x16x32_bf16 v[88:91], v[52:55], a[124:127], v[88:91]\n"
"ds_write_addtid_b32 v66 offset:12160\n"
"v_mfma_f32_16x16x32_bf16 v[88:91], v[56:59], a[128:131], v[88:91]\n"
"ds_write_addtid_b32 v67 offset:16800\n"
"v_mfma_f32_16x16x32_bf16 v[88:91], v[60:63], a[132:135], v[88:91]\n"
"v_permlane32_swap_b32 v112, v114\n"
"v_mfma_f32_16x16x32_bf16 v[88:91], v[100:103], a[136:139], v[88:91]\n"
"v_permlane32_swap_b32 v113, v115\n"
"v_mfma_f32_16x16x32_bf16 v[64:67], v[92:95], a[140:143], 0\n"
"buffer_store_dwordx4 v[124:127], v13, s[40:43], s93 offen sc1\n"
"v_add_f32 v112, v112, v114\n"
"v_add_f32 v113, v113, v115\n"
"v_mfma_f32_16x16x32_bf16 v[64:67], v[96:99], a[144:147], v[64:67]\n"
"ds_write_addtid_b32 v88 offset:3072\n"
"v_mfma_f32_16x16x32_bf16 v[64:67], v[48:51], a[148:151], v[64:67]\n"
"ds_write_addtid_b32 v89 offset:7712\n"
"v_mfma_f32_16x16x32_bf16 v[64:67], v[52:55], a[152:155], v[64:67]\n"
"ds_write_addtid_b32 v90 offset:12416\n"
"v_mfma_f32_16x16x32_bf16 v[64:67], v[56:59], a[156:159], v[64:67]\n"
"ds_write_addtid_b32 v91 offset:17056\n"
"v_mfma_f32_16x16x32_bf16 v[64:67], v[60:63], a[160:163], v[64:67]\n"
"v_permlane16_swap_b32 v112, v113\n"
"v_mfma_f32_16x16x32_bf16 v[64:67], v[100:103], a[164:167], v[64:67]\n"
"v_add_f32 v112, v112, v113\n"
"v_mfma_f32_16x16x32_bf16 v[88:91], v[92:95], a[224:227], 0\n"
"v_add_f32_dpp v113, v112, v112 row_ror:8 row_mask:0xf bank_mask:0xf\n"
"v_mov_b32 v112, v113\n"
"v_mfma_f32_16x16x32_bf16 v[88:91], v[96:99], a[228:231], v[88:91]\n"
"ds_write_addtid_b32 v64 offset:3328\n"
"v_mfma_f32_16x16x32_bf16 v[88:91], v[48:51], a[232:235], v[88:91]\n"
"ds_write_addtid_b32 v65 offset:7968\n"
"v_mfma_f32_16x16x32_bf16 v[88:91], v[52:55], a[236:239], v[88:91]\n"
"ds_write_addtid_b32 v66 offset:12672\n"
"v_mfma_f32_16x16x32_bf16 v[88:91], v[56:59], a[240:243], v[88:91]\n"
"ds_write_addtid_b32 v67 offset:17312\n"
"v_mfma_f32_16x16x32_bf16 v[88:91], v[60:63], a[244:247], v[88:91]\n"
"v_permlane16_swap_b32 v113, v112\n"
"v_mfma_f32_16x16x32_bf16 v[88:91], v[100:103], a[248:251], v[88:91]\n"
"v_mul_f32 v113, 0xba800000, v113\n"
"v_fma_f32 v112, v112, s62, v20\n"
"v_mfma_f32_16x16x32_bf16 v[64:67], v[92:95], a[252:255], 0\n"
"v_fma_f32 v112, -v113, v113, v112\n"
"v_max_f32 v112, v20, v112\n"
"v_mfma_f32_16x16x32_bf16 v[64:67], v[96:99], v[24:27], v[64:67]\n"
"s_add_u32 s58, s50, 0x180000\n"
"ds_write_addtid_b32 v88 offset:3584\n"
"v_mfma_f32_16x16x32_bf16 v[64:67], v[48:51], v[28:31], v[64:67]\n"
"s_and_b32 s58, s58, 0x3ffffff\n"
"ds_write_addtid_b32 v89 offset:8224\n"
"v_mfma_f32_16x16x32_bf16 v[64:67], v[52:55], v[32:35], v[64:67]\n"
"s_add_u32 s56, s40, s58\n"
"ds_write_addtid_b32 v90 offset:12928\n"
"v_mfma_f32_16x16x32_bf16 v[64:67], v[56:59], v[36:39], v[64:67]\n"
"s_addc_u32 s57, s41, 0\n"
"ds_write_addtid_b32 v91 offset:17568\n"
"v_mfma_f32_16x16x32_bf16 v[64:67], v[60:63], v[40:43], v[64:67]\n"
"s_waitcnt vmcnt(5)\n"
"v_cmp_ne_u64 vcc, s[48:49], v[148:149]\n"
"v_mfma_f32_16x16x32_bf16 v[64:67], v[100:103], v[44:47], v[64:67]\n"
"v_rsq_f32 v112, v112\n"
"v_mfma_f32_16x16x32_bf16 v[124:127], a[168:171], v[92:95], 0\n"
"s_cbranch_vccnz .LH_2s\n"
".LH_2r:\n"
"v_mov_b32 v114, v0\n"
"v_mfma_f32_16x16x32_bf16 v[124:127], a[172:175], v[96:99], v[124:127]\n"
"s_add_u32 s50, s50, 0x180000\n"
"ds_write_addtid_b32 v64 offset:3840\n"
"v_mov_b32 v115, v1\n"
"v_mfma_f32_16x16x32_bf16 v[124:127], a[176:179], v[48:51], v[124:127]\n"
"buffer_store_dwordx4 v[112:115], v19, s[40:43], s95 offen sc1\n"
"ds_write_addtid_b32 v65 offset:8480\n"
"v_mfma_f32_16x16x32_bf16 v[124:127], a[180:183], v[52:55], v[124:127]\n"
"ds_write_addtid_b32 v66 offset:13184\n"
"v_mfma_f32_16x16x32_bf16 v[124:127], a[184:187], v[56:59], v[124:127]\n"
"ds_write_addtid_b32 v67 offset:17824\n"
"v_mfma_f32_16x16x32_bf16 v[124:127], a[188:191], v[60:63], v[124:127]\n"
"global_load_dwordx4 v[112:115], v5, s[56:57] offset:0 sc1\n"
"v_mfma_f32_16x16x32_bf16 v[124:127], a[192:195], v[100:103], v[124:127]\n"
"v_mfma_f32_16x16x32_bf16 v[128:131], a[196:199], v[92:95], 0\n"
"v_mfma_f32_16x16x32_bf16 v[128:131], a[200:203], v[96:99], v[128:131]\n"
"s_waitcnt lgkmcnt(0)\n"
"v_mfma_f32_16x16x32_bf16 v[128:131], a[204:207], v[48:51], v[128:131]\n"
"s_barrier\n"
"v_mfma_f32_16x16x32_bf16 v[128:131], a[208:211], v[52:55], v[128:131]\n"
"global_load_dwordx4 v[116:119], v6, s[56:57] offset:0 sc1\n"
"ds_read_b128 v[96:99], v2 offset:0\n"
"v_mfma_f32_16x16x32_bf16 v[128:131], a[212:215], v[56:59], v[128:131]\n"
"ds_read_b128 v[52:55], v3 offset:2560\n"
"ds_read_b128 v[88:91], v4 offset:1024\n"
"v_mfma_f32_16x16x32_bf16 v[128:131], a[216:219], v[60:63], v[128:131]\n"
"ds_read_b128 v[132:135], v2 offset:256\n"
"ds_read_b128 v[136:139], v3 offset:2816\n"
"v_mfma_f32_16x16x32_bf16 v[128:131], a[220:223], v[100:103], v[128:131]\n"
"global_load_dwordx4 v[120:123], v7, s[56:57] offset:0 sc1\n"
"ds_read_b128 v[140:143], v4 offset:1280\n"
"s_branch .LH_top\n"
".LH_1s:\n"
"s_cmp_ge_u32 s50, s53\n"
"s_cbranch_scc1 .LH_t0\n"
"s_cmp_ge_u32 s50, s52\n"
"s_cbranch_scc1 .LH_1r\n"
"s_sub_u32 s60, s50, 0x0\n"
"s_mov_b32 s61, 200000\n"
"s_nop 7\n"
".LH_1p:\n"
"buffer_load_dwordx2 v[142:143], v12, s[40:43], s60 offen sc1\n"
"s_waitcnt vmcnt(0)\n"
"v_cmp_ne_u64 vcc, s[48:49], v[142:143]\n"
"s_cbranch_vccz .LH_1px\n"
"s_sleep 1\n"
"s_sub_u32 s61, s61, 1\n"
"s_cbranch_scc0 .LH_1p\n"
"s_branch .LH_end\n"
".LH_1px:\n"
"s_branch .LH_1r\n"
".LH_2s:\n"
"s_cmp_ge_u32 s50, s53\n"
"s_cbranch_scc1 .LH_t1\n"
"s_cmp_ge_u32 s50, s52\n"
"s_cbranch_scc1 .LH_2r\n"
"s_sub_u32 s60, s50, 0x0\n"
"s_mov_b32 s61, 200000\n"
"s_nop 7\n"
".LH_2p:\n"
"buffer_load_dwordx2 v[148:149], v12, s[40:43], s60 offen sc1\n"
"s_waitcnt vmcnt(0)\n"
"v_cmp_ne_u64 vcc, s[48:49], v[148:149]\n"
"s_cbranch_vccz .LH_2px\n"
"s_sleep 1\n"
"s_sub_u32 s61, s61, 1\n"
"s_cbranch_scc0 .LH_2p\n"
"s_branch .LH_end\n"
".LH_2px:\n"
"s_branch .LH_2r\n"
".LH_3s:\n"
"s_cmp_ge_u32 s50, s53\n"
"s_cbranch_scc1 .LH_t2\n"
"s_cmp_ge_u32 s50, s52\n"
"s_cbranch_scc1 .LH_3r\n"
"s_sub_u32 s60, s50, 0x0\n"
"s_mov_b32 s61, 200000\n"
"s_nop 7\n"
".LH_3p:\n"
"buffer_load_dwordx2 v[144:145], v12, s[40:43], s60 offen sc1\n"
"s_waitcnt vmcnt(0)\n"
"v_cmp_ne_u64 vcc, s[48:49], v[144:145]\n"
"s_cbranch_vccz .LH_3px\n"
"s_sleep 1\n"
"s_sub_u32 s61, s61, 1\n"
"s_cbranch_scc0 .LH_3p\n"
"s_branch .LH_end\n"
".LH_3px:\n"
"s_branch .LH_3r\n"
".LH_4s:\n"
"s_cmp_ge_u32 s50, s53\n"
"s_cbranch_scc1 .LH_t3\n"
"s_cmp_ge_u32 s50, s52\n"
"s_cbranch_scc1 .LH_4r\n"
"s_sub_u32 s60, s50, 0x0\n"
"s_mov_b32 s61, 200000\n"
"s_nop 7\n"
".LH_4p:\n"
"buffer_load_dwordx2 v[172:173], v12, s[40:43], s60 offen sc1\n"
"s_waitcnt vmcnt(0)\n"
"v_cmp_ne_u64 vcc, s[48:49], v[172:173]\n"
"s_cbranch_vccz .LH_4px\n"
"s_sleep 1\n"
"s_sub_u32 s61, s61, 1\n"
"s_cbranch_scc0 .LH_4p\n"
"s_branch .LH_end\n"
".LH_4px:\n"
"s_branch .LH_4r\n"
".LH_5s:\n"
"s_cmp_ge_i32 s95, 0xc000000\n"
"s_cselect_b64 s[64:65], 0, s[64:65]\n"
"s_cmp_ge_u32 s95, 0xc000000\n"
"s_cbranch_scc1 .LH_5r\n"
"s_mov_b32 s61, 200000\n"
"s_nop 7\n"
".LH_5p:\n"
"buffer_load_dwordx2 v[140:141], v17, s[40:43], s95 offen offset:0 sc1\n"
"s_waitcnt vmcnt(0)\n"
"v_cmp_ne_u64 vcc, s[64:65], v[140:141]\n"
"s_cbranch_vccz .LH_5px\n"
"s_sleep 1\n"
"s_sub_u32 s61, s61, 1\n"
"s_cbranch_scc0 .LH_5p\n"
"s_branch .LH_end\n"
".LH_5px:\n"
"s_branch .LH_5r\n"
".LH_t0:\n"
"v_mfma_f32_16x16x32_bf16 v[124:127], a[172:175], v[68:71], v[124:127]\n"
"ds_write_addtid_b32 v120 offset:1792\n"
"v_mfma_f32_16x16x32_bf16 v[124:127], a[176:179], v[72:75], v[124:127]\n"
"ds_write_addtid_b32 v121 offset:6432\n"
"v_mfma_f32_16x16x32_bf16 v[124:127], a[180:183], v[76:79], v[124:127]\n"
"ds_write_addtid_b32 v122 offset:11136\n"
"v_mfma_f32_16x16x32_bf16 v[124:127], a[184:187], v[80:83], v[124:127]\n"
"ds_write_addtid_b32 v123 offset:15776\n"
"v_mfma_f32_16x16x32_bf16 v[124:127], a[188:191], v[84:87], v[124:127]\n"
"v_mfma_f32_16x16x32_bf16 v[124:127], a[192:195], v[88:91], v[124:127]\n"
"v_mfma_f32_16x16x32_bf16 v[120:123], a[196:199], v[64:67], 0\n"
"v_mfma_f32_16x16x32_bf16 v[120:123], a[200:203], v[68:71], v[120:123]\n"
"s_waitcnt lgkmcnt(0)\n"
"v_mfma_f32_16x16x32_bf16 v[120:123], a[204:207], v[72:75], v[120:123]\n"
"s_barrier\n"
"v_mfma_f32_16x16x32_bf16 v[120:123], a[208:211], v[76:79], v[120:123]\n"
"ds_read_b128 v[64:67], v2 offset:2048\n"
"ds_read_b128 v[128:131], v3 offset:512\n"
"v_mfma_f32_16x16x32_bf16 v[120:123], a[212:215], v[80:83], v[120:123]\n"
"ds_read_b128 v[132:135], v4 offset:3072\n"
"ds_read_b128 v[136:139], v2 offset:2304\n"
"v_mfma_f32_16x16x32_bf16 v[120:123], a[216:219], v[84:87], v[120:123]\n"
"ds_read_b128 v[140:143], v3 offset:768\n"
"v_mfma_f32_16x16x32_bf16 v[120:123], a[220:223], v[88:91], v[120:123]\n"
"s_waitcnt vmcnt(10)\n"
"ds_read_b128 v[144:147], v4 offset:3328\n"
"v_mfma_f32_16x16x32_bf16 v[84:87], v[92:95], a[0:3], 0\n"
"v_mfma_f32_16x16x32_bf16 v[84:87], v[96:99], a[4:7], v[84:87]\n"
"s_waitcnt vmcnt(8)\n"
"v_mfma_f32_16x16x32_bf16 v[84:87], v[48:51], a[8:11], v[84:87]\n"
"v_mfma_f32_16x16x32_bf16 v[84:87], v[52:55], a[12:15], v[84:87]\n"
"s_waitcnt vmcnt(5)\n"
"v_mfma_f32_16x16x32_bf16 v[84:87], v[56:59], a[16:19], v[84:87]\n"
"s_waitcnt lgkmcnt(3)\n"
"v_add_f32 v64, v64, v128\n"
"v_add_f32 v65, v65, v129\n"
"v_mfma_f32_16x16x32_bf16 v[84:87], v[60:63], a[20:23], v[84:87]\n"
"s_waitcnt vmcnt(4)\n"
"v_mfma_f32_16x16x32_bf16 v[84:87], v[100:103], a[24:27], v[84:87]\n"
"v_add_f32 v66, v66, v130\n"
"v_mfma_f32_16x16x32_bf16 v[88:91], v[92:95], a[28:31], 0\n"
"v_add_f32 v67, v67, v131\n"
"v_add_f32 v64, v64, v132\n"
"v_mfma_f32_16x16x32_bf16 v[88:91], v[96:99], a[32:35], v[88:91]\n"
"ds_write_addtid_b32 v84 offset:2048\n"
"v_add_f32 v65, v65, v133\n"
"v_mfma_f32_16x16x32_bf16 v[88:91], v[48:51], a[36:39], v[88:91]\n"
"ds_write_addtid_b32 v85 offset:6688\n"
"v_add_f32 v66, v66, v134\n"
"v_mfma_f32_16x16x32_bf16 v[88:91], v[52:55], a[40:43], v[88:91]\n"
"s_waitcnt vmcnt(2)\n"
"ds_write_addtid_b32 v86 offset:11392\n"
"v_mfma_f32_16x16x32_bf16 v[88:91], v[56:59], a[44:47], v[88:91]\n"
"buffer_store_dwordx2 v[0:1], v14, s[40:43], s94 offen offset:0 sc1\n"
"ds_write_addtid_b32 v87 offset:16032\n"
"v_mfma_f32_16x16x32_bf16 v[88:91], v[60:63], a[48:51], v[88:91]\n"
"v_add_f32 v67, v67, v135\n"
"v_add_f32 v64, v64, v124\n"
"v_mfma_f32_16x16x32_bf16 v[88:91], v[100:103], a[52:55], v[88:91]\n"
"v_add_f32 v65, v65, v125\n"
"v_add_f32 v66, v66, v126\n"
"v_mfma_f32_16x16x32_bf16 v[128:131], v[92:95], a[56:59], 0\n"
"v_add_f32 v67, v67, v127\n"
"v_mfma_f32_16x16x32_bf16 v[128:131], v[96:99], a[60:63], v[128:131]\n"
"s_add_u32 s94, s50, 0xffe80000\n"
"s_and_b32 s93, s94, 0xfbffffff\n"
"ds_write_addtid_b32 v88 offset:2304\n"
"v_mfma_f32_16x16x32_bf16 v[128:131], v[48:51], a[64:67], v[128:131]\n"
"ds_write_addtid_b32 v89 offset:6944\n"
"v_cvt_pk_bf16_f32 v124, v64, v65\n"
"v_mfma_f32_16x16x32_bf16 v[128:131], v[52:55], a[68:71], v[128:131]\n"
"ds_write_addtid_b32 v90 offset:11648\n"
"v_cvt_pk_bf16_f32 v125, v66, v67\n"
"v_mfma_f32_16x16x32_bf16 v[128:131], v[56:59], a[72:75], v[128:131]\n"
"s_waitcnt lgkmcnt(7)\n"
"ds_write_addtid_b32 v91 offset:16288\n"
"v_add_f32 v136, v136, v140\n"
"v_mfma_f32_16x16x32_bf16 v[128:131], v[60:63], a[76:79], v[128:131]\n"
"s_waitcnt vmcnt(1)\n"
"v_add_f32 v137, v137, v141\n"
"v_add_f32 v138, v138, v142\n"
"v_mfma_f32_16x16x32_bf16 v[128:131], v[100:103], a[80:83], v[128:131]\n"
"v_add_f32 v139, v139, v143\n"
"v_add_f32 v136, v136, v144\n"
"v_mfma_f32_16x16x32_bf16 v[64:67], v[92:95], a[84:87], 0\n"
"v_add_f32 v137, v137, v145\n"
"v_add_f32 v138, v138, v146\n"
"v_mfma_f32_16x16x32_bf16 v[64:67], v[96:99], a[88:91], v[64:67]\n"
"ds_write_addtid_b32 v128 offset:2560\n"
"v_add_f32 v139, v139, v147\n"
"v_mfma_f32_16x16x32_bf16 v[64:67], v[48:51], a[92:95], v[64:67]\n"
"ds_write_addtid_b32 v129 offset:7200\n"
"v_add_f32 v136, v136, v120\n"
"v_mfma_f32_16x16x32_bf16 v[64:67], v[52:55], a[96:99], v[64:67]\n"
"ds_write_addtid_b32 v130 offset:11904\n"
"v_add_f32 v137, v137, v121\n"
"v_mfma_f32_16x16x32_bf16 v[64:67], v[56:59], a[100:103], v[64:67]\n"
"ds_write_addtid_b32 v131 offset:16544\n"
"v_add_f32 v138, v138, v122\n"
"v_mfma_f32_16x16x32_bf16 v[64:67], v[60:63], a[104:107], v[64:67]\n"
"v_add_f32 v139, v139, v123\n"
"v_cvt_pk_bf16_f32 v126, v136, v137\n"
"v_mfma_f32_16x16x32_bf16 v[64:67], v[100:103], a[108:111], v[64:67]\n"
"v_cvt_pk_bf16_f32 v127, v138, v139\n"
"v_mfma_f32_16x16x32_bf16 v[88:91], v[92:95], a[112:115], 0\n"
"v_mfma_f32_16x16x32_bf16 v[88:91], v[96:99], a[116:119], v[88:91]\n"
"ds_write_addtid_b32 v64 offset:2816\n"
"v_mfma_f32_16x16x32_bf16 v[88:91], v[48:51], a[120:123], v[88:91]\n"
"ds_write_addtid_b32 v65 offset:7456\n"
"v_mfma_f32_16x16x32_bf16 v[88:91], v[52:55], a[124:127], v[88:91]\n"
"ds_write_addtid_b32 v66 offset:12160\n"
"v_mfma_f32_16x16x32_bf16 v[88:91], v[56:59], a[128:131], v[88:91]\n"
"ds_write_addtid_b32 v67 offset:16800\n"
"v_mfma_f32_16x16x32_bf16 v[88:91], v[60:63], a[132:135], v[88:91]\n"
"v_mfma_f32_16x16x32_bf16 v[88:91], v[100:103], a[136:139], v[88:91]\n"
"v_mfma_f32_16x16x32_bf16 v[64:67], v[92:95], a[140:143], 0\n"
"buffer_store_dwordx4 v[124:127], v13, s[40:43], s93 offen sc1\n"
"v_mfma_f32_16x16x32_bf16 v[64:67], v[96:99], a[144:147], v[64:67]\n"
"ds_write_addtid_b32 v88 offset:3072\n"
"v_mfma_f32_16x16x32_bf16 v[64:67], v[48:51], a[148:151], v[64:67]\n"
"ds_write_addtid_b32 v89 offset:7712\n"
"v_mfma_f32_16x16x32_bf16 v[64:67], v[52:55], a[152:155], v[64:67]\n"
"ds_write_addtid_b32 v90 offset:12416\n"
"v_mfma_f32_16x16x32_bf16 v[64:67], v[56:59], a[156:159], v[64:67]\n"
"ds_write_addtid_b32 v91 offset:17056\n"
"v_mfma_f32_16x16x32_bf16 v[64:67], v[60:63], a[160:163], v[64:67]\n"
"v_mfma_f32_16x16x32_bf16 v[64:67], v[100:103], a[164:167], v[64:67]\n"
"v_mfma_f32_16x16x32_bf16 v[88:91], v[92:95], a[224:227], 0\n"
"v_mfma_f32_16x16x32_bf16 v[88:91], v[96:99], a[228:231], v[88:91]\n"
"ds_write_addtid_b32 v64 offset:3328\n"
"v_mfma_f32_16x16x32_bf16 v[88:91], v[48:51], a[232:235], v[88:91]\n"
"ds_write_addtid_b32 v65 offset:7968\n"
"v_mfma_f32_16x16x32_bf16 v[88:91], v[52:55], a[236:239], v[88:91]\n"
"ds_write_addtid_b32 v66 offset:12672\n"
"v_mfma_f32_16x16x32_bf16 v[88:91], v[56:59], a[240:243], v[88:91]\n"
"ds_write_addtid_b32 v67 offset:17312\n"
"v_mfma_f32_16x16x32_bf16 v[88:91], v[60:63], a[244:247], v[88:91]\n"
"v_mfma_f32_16x16x32_bf16 v[88:91], v[100:103], a[248:251], v[88:91]\n"
"v_mfma_f32_16x16x32_bf16 v[64:67], v[92:95], a[252:255], 0\n"
"v_mfma_f32_16x16x32_bf16 v[64:67], v[96:99], v[24:27], v[64:67]\n"
"ds_write_addtid_b32 v88 offset:3584\n"
"v_mfma_f32_16x16x32_bf16 v[64:67], v[48:51], v[28:31], v[64:67]\n"
"ds_write_addtid_b32 v89 offset:8224\n"
"v_mfma_f32_16x16x32_bf16 v[64:67], v[52:55], v[32:35], v[64:67]\n"
"ds_write_addtid_b32 v90 offset:12928\n"
"v_mfma_f32_16x16x32_bf16 v[64:67], v[56:59], v[36:39], v[64:67]\n"
"ds_write_addtid_b32 v91 offset:17568\n"
"v_mfma_f32_16x16x32_bf16 v[64:67], v[60:63], v[40:43], v[64:67]\n"
"v_mfma_f32_16x16x32_bf16 v[64:67], v[100:103], v[44:47], v[64:67]\n"
"v_mfma_f32_16x16x32_bf16 v[124:127], a[168:171], v[92:95], 0\n"
"v_mfma_f32_16x16x32_bf16 v[124:127], a[172:175], v[96:99], v[124:127]\n"
"ds_write_addtid_b32 v64 offset:3840\n"
"v_mfma_f32_16x16x32_bf16 v[124:127], a[176:179], v[48:51], v[124:127]\n"
"ds_write_addtid_b32 v65 offset:8480\n"
"v_mfma_f32_16x16x32_bf16 v[124:127], a[180:183], v[52:55], v[124:127]\n"
"ds_write_addtid_b32 v66 offset:13184\n"
"v_mfma_f32_16x16x32_bf16 v[124:127], a[184:187], v[56:59], v[124:127]\n"
"ds_write_addtid_b32 v67 offset:17824\n"
"v_mfma_f32_16x16x32_bf16 v[124:127], a[188:191], v[60:63], v[124:127]\n"
"v_mfma_f32_16x16x32_bf16 v[124:127], a[192:195], v[100:103], v[124:127]\n"
"v_mfma_f32_16x16x32_bf16 v[128:131], a[196:199], v[92:95], 0\n"
"v_mfma_f32_16x16x32_bf16 v[128:131], a[200:203], v[96:99], v[128:131]\n"
"s_waitcnt lgkmcnt(0)\n"
"v_mfma_f32_16x16x32_bf16 v[128:131], a[204:207], v[48:51], v[128:131]\n"
"s_barrier\n"
"v_mfma_f32_16x16x32_bf16 v[128:131], a[208:211], v[52:55], v[128:131]\n"
"ds_read_b128 v[96:99], v2 offset:0\n"
"v_mfma_f32_16x16x32_bf16 v[128:131], a[212:215], v[56:59], v[128:131]\n"
"ds_read_b128 v[52:55], v3 offset:2560\n"
"ds_read_b128 v[88:91], v4 offset:1024\n"
"v_mfma_f32_16x16x32_bf16 v[128:131], a[216:219], v[60:63], v[128:131]\n"
"ds_read_b128 v[132:135], v2 offset:256\n"
"ds_read_b128 v[136:139], v3 offset:2816\n"
"v_mfma_f32_16x16x32_bf16 v[128:131], a[220:223], v[100:103], v[128:131]\n"
"ds_read_b128 v[140:143], v4 offset:1280\n"
"s_waitcnt lgkmcnt(3)\n"
"v_add_f32 v96, v96, v52\n"
"v_add_f32 v97, v97, v53\n"
"v_add_f32 v98, v98, v54\n"
"v_add_f32 v99, v99, v55\n"
"v_add_f32 v96, v96, v88\n"
"v_add_f32 v97, v97, v89\n"
"v_add_f32 v98, v98, v90\n"
"v_add_f32 v99, v99, v91\n"
"v_add_f32 v96, v96, v124\n"
"v_add_f32 v97, v97, v125\n"
"s_waitcnt vmcnt(0)\n"
"buffer_store_dwordx2 v[0:1], v14, s[40:43], s94 offen offset:0 sc1\n"
"v_add_f32 v98, v98, v126\n"
"v_add_f32 v99, v99, v127\n"
"s_add_u32 s94, s50, 0x0\n"
"s_and_b32 s95, s94, 0xfbffffff\n"
"v_cvt_pk_bf16_f32 v92, v96, v97\n"
"v_cvt_pk_bf16_f32 v93, v98, v99\n"
"s_waitcnt lgkmcnt(0)\n"
"v_add_f32 v132, v132, v136\n"
"v_add_f32 v133, v133, v137\n"
"v_add_f32 v134, v134, v138\n"
"v_add_f32 v135, v135, v139\n"
"v_add_f32 v132, v132, v140\n"
"v_add_f32 v133, v133, v141\n"
"v_add_f32 v134, v134, v142\n"
"v_add_f32 v135, v135, v143\n"
"v_add_f32 v132, v132, v128\n"
"v_add_f32 v133, v133, v129\n"
"v_add_f32 v134, v134, v130\n"
"v_add_f32 v135, v135, v131\n"
"v_cvt_pk_bf16_f32 v94, v132, v133\n"
"v_cvt_pk_bf16_f32 v95, v134, v135\n"
"buffer_store_dwordx4 v[92:95], v13, s[40:43], s95 offen sc1\n"
"s_waitcnt vmcnt(0)\n"
"buffer_store_dwordx2 v[0:1], v14, s[40:43], s94 offen offset:0 sc1\n"
"s_branch .LH_end\n"
".LH_t1:\n"
"v_mfma_f32_16x16x32_bf16 v[124:127], a[172:175], v[96:99], v[124:127]\n"
"ds_write_addtid_b32 v64 offset:3840\n"
"v_mfma_f32_16x16x32_bf16 v[124:127], a[176:179], v[48:51], v[124:127]\n"
"ds_write_addtid_b32 v65 offset:8480\n"
"v_mfma_f32_16x16x32_bf16 v[124:127], a[180:183], v[52:55], v[124:127]\n"
"ds_write_addtid_b32 v66 offset:13184\n"
"v_mfma_f32_16x16x32_bf16 v[124:127], a[184:187], v[56:59], v[124:127]\n"
"ds_write_addtid_b32 v67 offset:17824\n"
"v_mfma_f32_16x16x32_bf16 v[124:127], a[188:191], v[60:63], v[124:127]\n"
"v_mfma_f32_16x16x32_bf16 v[124:127], a[192:195], v[100:103], v[124:127]\n"
"v_mfma_f32_16x16x32_bf16 v[128:131], a[196:199], v[92:95], 0\n"
"v_mfma_f32_16x16x32_bf16 v[128:131], a[200:203], v[96:99], v[128:131]\n"
"s_waitcnt lgkmcnt(0)\n"
"v_mfma_f32_16x16x32_bf16 v[128:131], a[204:207], v[48:51], v[128:131]\n"
"s_barrier\n"
"v_mfma_f32_16x16x32_bf16 v[128:131], a[208:211], v[52:55], v[128:131]\n"
"ds_read_b128 v[96:99], v2 offset:0\n"
"v_mfma_f32_16x16x32_bf16 v[128:131], a[212:215], v[56:59], v[128:131]\n"
"ds_read_b128 v[52:55], v3 offset:2560\n"
"ds_read_b128 v[88:91], v4 offset:1024\n"
"v_mfma_f32_16x16x32_bf16 v[128:131], a[216:219], v[60:63], v[128:131]\n"
"ds_read_b128 v[132:135], v2 offset:256\n"
"ds_read_b128 v[136:139], v3 offset:2816\n"
"v_mfma_f32_16x16x32_bf16 v[128:131], a[220:223], v[100:103], v[128:131]\n"
"s_waitcnt vmcnt(8)\n"
"ds_read_b128 v[140:143], v4 offset:1280\n"
"v_mfma_f32_16x16x32_bf16 v[60:63], v[104:107], a[0:3], 0\n"
"v_mfma_f32_16x16x32_bf16 v[60:63], v[108:111], a[4:7], v[60:63]\n"
"s_waitcnt vmcnt(6)\n"
"v_mfma_f32_16x16x32_bf16 v[60:63], v[68:71], a[8:11], v[60:63]\n"
"v_mfma_f32_16x16x32_bf16 v[60:63], v[72:75], a[12:15], v[60:63]\n"
"s_waitcnt vmcnt(3)\n"
"v_mfma_f32_16x16x32_bf16 v[60:63], v[76:79], a[16:19], v[60:63]\n"
"s_waitcnt lgkmcnt(3)\n"
"v_add_f32 v96, v96, v52\n"
"v_add_f32 v97, v97, v53\n"
"v_mfma_f32_16x16x32_bf16 v[60:63], v[80:83], a[20:23], v[60:63]\n"
"s_waitcnt vmcnt(1)\n"
"v_add_f32 v98, v98, v54\n"
"v_add_f32 v99, v99, v55\n"
"v_mfma_f32_16x16x32_bf16 v[60:63], v[84:87], a[24:27], v[60:63]\n"
"v_mfma_f32_16x16x32_bf16 v[64:67], v[104:107], a[28:31], 0\n"
"v_add_f32 v96, v96, v88\n"
"v_mfma_f32_16x16x32_bf16 v[64:67], v[108:111], a[32:35], v[64:67]\n"
"ds_write_addtid_b32 v60 offset:0\n"
"v_add_f32 v97, v97, v89\n"
"v_mfma_f32_16x16x32_bf16 v[64:67], v[68:71], a[36:39], v[64:67]\n"
"ds_write_addtid_b32 v61 offset:4640\n"
"v_add_f32 v98, v98, v90\n"
"v_mfma_f32_16x16x32_bf16 v[64:67], v[72:75], a[40:43], v[64:67]\n"
"ds_write_addtid_b32 v62 offset:9344\n"
"v_add_f32 v99, v99, v91\n"
"v_mfma_f32_16x16x32_bf16 v[64:67], v[76:79], a[44:47], v[64:67]\n"
"ds_write_addtid_b32 v63 offset:13984\n"
"v_add_f32 v96, v96, v124\n"
"v_mfma_f32_16x16x32_bf16 v[64:67], v[80:83], a[48:51], v[64:67]\n"
"v_add_f32 v97, v97, v125\n"
"v_mfma_f32_16x16x32_bf16 v[64:67], v[84:87], a[52:55], v[64:67]\n"
"s_waitcnt vmcnt(0)\n"
"buffer_store_dwordx2 v[0:1], v14, s[40:43], s94 offen offset:0 sc1\n"
"v_add_f32 v98, v98, v126\n"
"v_mfma_f32_16x16x32_bf16 v[88:91], v[104:107], a[56:59], 0\n"
"v_add_f32 v99, v99, v127\n"
"v_mfma_f32_16x16x32_bf16 v[88:91], v[108:111], a[60:63], v[88:91]\n"
"ds_write_addtid_b32 v64 offset:256\n"
"v_mfma_f32_16x16x32_bf16 v[88:91], v[68:71], a[64:67], v[88:91]\n"
"s_add_u32 s94, s50, 0xffe80000\n"
"s_and_b32 s95, s94, 0xfbffffff\n"
"ds_write_addtid_b32 v65 offset:4896\n"
"v_mfma_f32_16x16x32_bf16 v[88:91], v[72:75], a[68:71], v[88:91]\n"
"ds_write_addtid_b32 v66 offset:9600\n"
"v_cvt_pk_bf16_f32 v92, v96, v97\n"
"v_mfma_f32_16x16x32_bf16 v[88:91], v[76:79], a[72:75], v[88:91]\n"
"ds_write_addtid_b32 v67 offset:14240\n"
"v_cvt_pk_bf16_f32 v93, v98, v99\n"
"v_mfma_f32_16x16x32_bf16 v[88:91], v[80:83], a[76:79], v[88:91]\n"
"s_waitcnt lgkmcnt(8)\n"
"v_add_f32 v132, v132, v136\n"
"v_add_f32 v133, v133, v137\n"
"v_mfma_f32_16x16x32_bf16 v[88:91], v[84:87], a[80:83], v[88:91]\n"
"v_add_f32 v134, v134, v138\n"
"v_add_f32 v135, v135, v139\n"
"v_mfma_f32_16x16x32_bf16 v[64:67], v[104:107], a[84:87], 0\n"
"v_add_f32 v132, v132, v140\n"
"v_add_f32 v133, v133, v141\n"
"v_mfma_f32_16x16x32_bf16 v[64:67], v[108:111], a[88:91], v[64:67]\n"
"ds_write_addtid_b32 v88 offset:512\n"
"v_add_f32 v134, v134, v142\n"
"v_mfma_f32_16x16x32_bf16 v[64:67], v[68:71], a[92:95], v[64:67]\n"
"ds_write_addtid_b32 v89 offset:5152\n"
"v_add_f32 v135, v135, v143\n"
"v_mfma_f32_16x16x32_bf16 v[64:67], v[72:75], a[96:99], v[64:67]\n"
"ds_write_addtid_b32 v90 offset:9856\n"
"v_add_f32 v132, v132, v128\n"
"v_mfma_f32_16x16x32_bf16 v[64:67], v[76:79], a[100:103], v[64:67]\n"
"ds_write_addtid_b32 v91 offset:14496\n"
"v_add_f32 v133, v133, v129\n"
"v_mfma_f32_16x16x32_bf16 v[64:67], v[80:83], a[104:107], v[64:67]\n"
"v_add_f32 v134, v134, v130\n"
"v_add_f32 v135, v135, v131\n"
"v_mfma_f32_16x16x32_bf16 v[64:67], v[84:87], a[108:111], v[64:67]\n"
"v_cvt_pk_bf16_f32 v94, v132, v133\n"
"v_cvt_pk_bf16_f32 v95, v134, v135\n"
"v_mfma_f32_16x16x32_bf16 v[88:91], v[104:107], a[112:115], 0\n"
"v_mfma_f32_16x16x32_bf16 v[88:91], v[108:111], a[116:119], v[88:91]\n"
"ds_write_addtid_b32 v64 offset:768\n"
"v_mfma_f32_16x16x32_bf16 v[88:91], v[68:71], a[120:123], v[88:91]\n"
"ds_write_addtid_b32 v65 offset:5408\n"
"v_mfma_f32_16x16x32_bf16 v[88:91], v[72:75], a[124:127], v[88:91]\n"
"ds_write_addtid_b32 v66 offset:10112\n"
"v_mfma_f32_16x16x32_bf16 v[88:91], v[76:79], a[128:131], v[88:91]\n"
"ds_write_addtid_b32 v67 offset:14752\n"
"v_mfma_f32_16x16x32_bf16 v[88:91], v[80:83], a[132:135], v[88:91]\n"
"v_mfma_f32_16x16x32_bf16 v[88:91], v[84:87], a[136:139], v[88:91]\n"
"v_mfma_f32_16x16x32_bf16 v[96:99], v[104:107], a[140:143], 0\n"
"v_mfma_f32_16x16x32_bf16 v[96:99], v[108:111], a[144:147], v[96:99]\n"
"ds_write_addtid_b32 v88 offset:1024\n"
"v_mfma_f32_16x16x32_bf16 v[96:99], v[68:71], a[148:151], v[96:99]\n"
"ds_write_addtid_b32 v89 offset:5664\n"
"v_mfma_f32_16x16x32_bf16 v[96:99], v[72:75], a[152:155], v[96:99]\n"
"ds_write_addtid_b32 v90 offset:10368\n"
"v_mfma_f32_16x16x32_bf16 v[96:99], v[76:79], a[156:159], v[96:99]\n"
"buffer_store_dwordx4 v[92:95], v13, s[40:43], s95 offen sc1\n"
"ds_write_addtid_b32 v91 offset:15008\n"
"v_mfma_f32_16x16x32_bf16 v[96:99], v[80:83], a[160:163], v[96:99]\n"
"v_mfma_f32_16x16x32_bf16 v[96:99], v[84:87], a[164:167], v[96:99]\n"
"v_mfma_f32_16x16x32_bf16 v[92:95], a[168:171], v[104:107], 0\n"
"v_mfma_f32_16x16x32_bf16 v[92:95], a[172:175], v[108:111], v[92:95]\n"
"ds_write_addtid_b32 v96 offset:1280\n"
"v_mfma_f32_16x16x32_bf16 v[92:95], a[176:179], v[68:71], v[92:95]\n"
"ds_write_addtid_b32 v97 offset:5920\n"
"v_mfma_f32_16x16x32_bf16 v[92:95], a[180:183], v[72:75], v[92:95]\n"
"ds_write_addtid_b32 v98 offset:10624\n"
"v_mfma_f32_16x16x32_bf16 v[92:95], a[184:187], v[76:79], v[92:95]\n"
"ds_write_addtid_b32 v99 offset:15264\n"
"v_mfma_f32_16x16x32_bf16 v[92:95], a[188:191], v[80:83], v[92:95]\n"
"v_mfma_f32_16x16x32_bf16 v[92:95], a[192:195], v[84:87], v[92:95]\n"
"v_mfma_f32_16x16x32_bf16 v[100:103], a[196:199], v[104:107], 0\n"
"v_mfma_f32_16x16x32_bf16 v[100:103], a[200:203], v[108:111], v[100:103]\n"
"v_mfma_f32_16x16x32_bf16 v[100:103], a[204:207], v[68:71], v[100:103]\n"
"v_mfma_f32_16x16x32_bf16 v[100:103], a[208:211], v[72:75], v[100:103]\n"
"v_mfma_f32_16x16x32_bf16 v[100:103], a[212:215], v[76:79], v[100:103]\n"
"v_mfma_f32_16x16x32_bf16 v[100:103], a[216:219], v[80:83], v[100:103]\n"
"v_mfma_f32_16x16x32_bf16 v[100:103], a[220:223], v[84:87], v[100:103]\n"
"v_mfma_f32_16x16x32_bf16 v[96:99], a[224:227], v[104:107], 0\n"
"v_mfma_f32_16x16x32_bf16 v[96:99], a[228:231], v[108:111], v[96:99]\n"
"v_mfma_f32_16x16x32_bf16 v[96:99], a[232:235], v[68:71], v[96:99]\n"
"v_mfma_f32_16x16x32_bf16 v[96:99], a[236:239], v[72:75], v[96:99]\n"
"v_mfma_f32_16x16x32_bf16 v[96:99], a[240:243], v[76:79], v[96:99]\n"
"v_mfma_f32_16x16x32_bf16 v[96:99], a[244:247], v[80:83], v[96:99]\n"
"v_mfma_f32_16x16x32_bf16 v[96:99], a[248:251], v[84:87], v[96:99]\n"
"v_mfma_f32_16x16x32_bf16 v[124:127], a[252:255], v[104:107], 0\n"
"v_mfma_f32_16x16x32_bf16 v[124:127], v[24:27], v[108:111], v[124:127]\n"
"s_waitcnt lgkmcnt(0)\n"
"v_mfma_f32_16x16x32_bf16 v[124:127], v[28:31], v[68:71], v[124:127]\n"
"s_barrier\n"
"v_mfma_f32_16x16x32_bf16 v[124:127], v[32:35], v[72:75], v[124:127]\n"
"ds_read_b128 v[104:107], v2 offset:2048\n"
"ds_read_b128 v[88:91], v3 offset:512\n"
"v_mfma_f32_16x16x32_bf16 v[124:127], v[36:39], v[76:79], v[124:127]\n"
"ds_read_b128 v[108:111], v4 offset:3072\n"
"v_mfma_f32_16x16x32_bf16 v[124:127], v[40:43], v[80:83], v[124:127]\n"
"ds_read_b128 v[128:131], v2 offset:2304\n"
"ds_read_b128 v[132:135], v3 offset:768\n"
"v_mfma_f32_16x16x32_bf16 v[124:127], v[44:47], v[84:87], v[124:127]\n"
"ds_read_b128 v[136:139], v4 offset:3328\n"
"ds_read_b128 v[140:143], v2 offset:3584\n"
"ds_read_b128 v[148:151], v3 offset:1536\n"
"ds_read_b128 v[152:155], v4 offset:3584\n"
"ds_read_b128 v[156:159], v2 offset:3840\n"
"ds_read_b128 v[160:163], v3 offset:1792\n"
"ds_read_b128 v[164:167], v4 offset:3840\n"
"s_waitcnt lgkmcnt(9)\n"
"v_add_f32 v104, v104, v88\n"
"v_add_f32 v105, v105, v89\n"
"v_add_f32 v106, v106, v90\n"
"v_add_f32 v107, v107, v91\n"
"v_add_f32 v104, v104, v108\n"
"v_add_f32 v105, v105, v109\n"
"v_add_f32 v106, v106, v110\n"
"v_add_f32 v107, v107, v111\n"
"v_add_f32 v104, v104, v92\n"
"v_add_f32 v105, v105, v93\n"
"v_add_f32 v106, v106, v94\n"
"v_add_f32 v107, v107, v95\n"
"s_add_u32 s92, s50, 0x0\n"
"s_and_b32 s93, s92, 0xfbffffff\n"
"v_cvt_pk_bf16_f32 v92, v104, v105\n"
"s_waitcnt vmcnt(0)\n"
"v_cvt_pk_bf16_f32 v93, v106, v107\n"
"s_waitcnt lgkmcnt(6)\n"
"v_add_f32 v128, v128, v132\n"
"v_add_f32 v129, v129, v133\n"
"v_add_f32 v130, v130, v134\n"
"v_add_f32 v131, v131, v135\n"
"v_add_f32 v128, v128, v136\n"
"v_add_f32 v129, v129, v137\n"
"v_add_f32 v130, v130, v138\n"
"v_add_f32 v131, v131, v139\n"
"v_add_f32 v128, v128, v100\n"
"v_add_f32 v129, v129, v101\n"
"v_add_f32 v130, v130, v102\n"
"v_add_f32 v131, v131, v103\n"
"v_cvt_pk_bf16_f32 v94, v128, v129\n"
"v_cvt_pk_bf16_f32 v95, v130, v131\n"
"buffer_store_dwordx4 v[92:95], v13, s[40:43], s93 offen sc1\n"
"s_waitcnt lgkmcnt(3)\n"
"v_add_f32 v140, v140, v148\n"
"v_add_f32 v141, v141, v149\n"
"v_add_f32 v142, v142, v150\n"
"v_add_f32 v143, v143, v151\n"
"v_add_f32 v140, v140, v152\n"
"v_add_f32 v141, v141, v153\n"
"v_add_f32 v142, v142, v154\n"
"v_add_f32 v143, v143, v155\n"
"v_add_f32 v140, v140, v96\n"
"v_add_f32 v141, v141, v97\n"
"v_add_f32 v142, v142, v98\n"
"v_add_f32 v143, v143, v99\n"
"v_cvt_pk_bf16_f32 v104, v140, v141\n"
"v_cvt_pk_bf16_f32 v105, v142, v143\n"
"s_waitcnt lgkmcnt(0)\n"
"v_add_f32 v156, v156, v160\n"
"v_add_f32 v157, v157, v161\n"
"v_add_f32 v158, v158, v162\n"
"v_add_f32 v159, v159, v163\n"
"v_add_f32 v156, v156, v164\n"
"v_add_f32 v157, v157, v165\n"
"v_add_f32 v158, v158, v166\n"
"v_add_f32 v159, v159, v167\n"
"v_add_f32 v156, v156, v124\n"
"v_add_f32 v157, v157, v125\n"
"v_add_f32 v158, v158, v126\n"
"v_add_f32 v159, v159, v127\n"
"v_cvt_pk_bf16_f32 v106, v156, v157\n"
"v_cvt_pk_bf16_f32 v107, v158, v159\n"
"buffer_store_dwordx4 v[104:107], v15, s[40:43], s93 offen sc1\n"
"buffer_store_dwordx2 v[0:1], v14, s[40:43], s94 offen offset:0 sc1\n"
"s_waitcnt vmcnt(1)\n"
"buffer_store_dwordx2 v[0:1], v16, s[40:43], s92 offen offset:0 sc1\n"
"s_branch .LH_end\n"
".LH_t2:\n"
"v_mfma_f32_16x16x32_bf16 v[124:127], a[252:255], v[104:107], 0\n"
"v_mfma_f32_16x16x32_bf16 v[124:127], v[24:27], v[108:111], v[124:127]\n"
"s_waitcnt lgkmcnt(0)\n"
"v_mfma_f32_16x16x32_bf16 v[124:127], v[28:31], v[68:71], v[124:127]\n"
"s_barrier\n"
"v_mfma_f32_16x16x32_bf16 v[124:127], v[32:35], v[72:75], v[124:127]\n"
"ds_read_b128 v[104:107], v2 offset:2048\n"
"ds_read_b128 v[88:91], v3 offset:512\n"
"v_mfma_f32_16x16x32_bf16 v[124:127], v[36:39], v[76:79], v[124:127]\n"
"ds_read_b128 v[108:111], v4 offset:3072\n"
"v_mfma_f32_16x16x32_bf16 v[124:127], v[40:43], v[80:83], v[124:127]\n"
"ds_read_b128 v[128:131], v2 offset:2304\n"
"ds_read_b128 v[132:135], v3 offset:768\n"
"v_mfma_f32_16x16x32_bf16 v[124:127], v[44:47], v[84:87], v[124:127]\n"
"s_waitcnt vmcnt(8)\n"
"ds_read_b128 v[136:139], v4 offset:3328\n"
"ds_read_b128 v[140:143], v2 offset:3584\n"
"v_mfma_f32_16x16x32_bf16 v[144:147], v[112:115], a[0:3], 0\n"
"ds_read_b128 v[148:151], v3 offset:1536\n"
"v_mfma_f32_16x16x32_bf16 v[144:147], v[116:119], a[4:7], v[144:147]\n"
"s_waitcnt vmcnt(6)\n"
"ds_read_b128 v[152:155], v4 offset:3584\n"
"ds_read_b128 v[156:159], v2 offset:3840\n"
"v_mfma_f32_16x16x32_bf16 v[144:147], v[120:123], a[8:11], v[144:147]\n"
"ds_read_b128 v[160:163], v3 offset:1792\n"
"ds_read_b128 v[164:167], v4 offset:3840\n"
"v_mfma_f32_16x16x32_bf16 v[144:147], v[48:51], a[12:15], v[144:147]\n"
"s_waitcnt vmcnt(3)\n"
"v_mfma_f32_16x16x32_bf16 v[144:147], v[52:55], a[16:19], v[144:147]\n"
"s_waitcnt lgkmcnt(9)\n"
"v_add_f32 v104, v104, v88\n"
"v_mfma_f32_16x16x32_bf16 v[144:147], v[56:59], a[20:23], v[144:147]\n"
"s_waitcnt vmcnt(1)\n"
"v_add_f32 v105, v105, v89\n"
"v_add_f32 v106, v106, v90\n"
"v_mfma_f32_16x16x32_bf16 v[144:147], v[60:63], a[24:27], v[144:147]\n"
"v_mfma_f32_16x16x32_bf16 v[168:171], v[112:115], a[28:31], 0\n"
"v_add_f32 v107, v107, v91\n"
"v_add_f32 v104, v104, v108\n"
"v_mfma_f32_16x16x32_bf16 v[168:171], v[116:119], a[32:35], v[168:171]\n"
"ds_write_addtid_b32 v144 offset:2048\n"
"v_mfma_f32_16x16x32_bf16 v[168:171], v[120:123], a[36:39], v[168:171]\n"
"ds_write_addtid_b32 v145 offset:6688\n"
"v_add_f32 v105, v105, v109\n"
"v_mfma_f32_16x16x32_bf16 v[168:171], v[48:51], a[40:43], v[168:171]\n"
"ds_write_addtid_b32 v146 offset:11392\n"
"v_mfma_f32_16x16x32_bf16 v[168:171], v[52:55], a[44:47], v[168:171]\n"
"ds_write_addtid_b32 v147 offset:16032\n"
"v_add_f32 v106, v106, v110\n"
"v_mfma_f32_16x16x32_bf16 v[168:171], v[56:59], a[48:51], v[168:171]\n"
"v_add_f32 v107, v107, v111\n"
"v_add_f32 v104, v104, v92\n"
"v_mfma_f32_16x16x32_bf16 v[168:171], v[60:63], a[52:55], v[168:171]\n"
"v_add_f32 v105, v105, v93\n"
"v_add_f32 v106, v106, v94\n"
"v_mfma_f32_16x16x32_bf16 v[108:111], v[112:115], a[56:59], 0\n"
"v_add_f32 v107, v107, v95\n"
"v_mfma_f32_16x16x32_bf16 v[108:111], v[116:119], a[60:63], v[108:111]\n"
"s_add_u32 s92, s50, 0xffe80000\n"
"s_and_b32 s93, s92, 0xfbffffff\n"
"ds_write_addtid_b32 v168 offset:2304\n"
"v_mfma_f32_16x16x32_bf16 v[108:111], v[120:123], a[64:67], v[108:111]\n"
"ds_write_addtid_b32 v169 offset:6944\n"
"v_cvt_pk_bf16_f32 v92, v104, v105\n"
"v_mfma_f32_16x16x32_bf16 v[108:111], v[48:51], a[68:71], v[108:111]\n"
"s_waitcnt vmcnt(0)\n"
"ds_write_addtid_b32 v170 offset:11648\n"
"v_mfma_f32_16x16x32_bf16 v[108:111], v[52:55], a[72:75], v[108:111]\n"
"ds_write_addtid_b32 v171 offset:16288\n"
"v_cvt_pk_bf16_f32 v93, v106, v107\n"
"v_mfma_f32_16x16x32_bf16 v[108:111], v[56:59], a[76:79], v[108:111]\n"
"s_waitcnt lgkmcnt(14)\n"
"v_add_f32 v128, v128, v132\n"
"v_add_f32 v129, v129, v133\n"
"v_mfma_f32_16x16x32_bf16 v[108:111], v[60:63], a[80:83], v[108:111]\n"
"v_add_f32 v130, v130, v134\n"
"v_add_f32 v131, v131, v135\n"
"v_mfma_f32_16x16x32_bf16 v[104:107], v[112:115], a[84:87], 0\n"
"v_add_f32 v128, v128, v136\n"
"v_add_f32 v129, v129, v137\n"
"v_mfma_f32_16x16x32_bf16 v[104:107], v[116:119], a[88:91], v[104:107]\n"
"ds_write_addtid_b32 v108 offset:2560\n"
"v_add_f32 v130, v130, v138\n"
"v_mfma_f32_16x16x32_bf16 v[104:107], v[120:123], a[92:95], v[104:107]\n"
"ds_write_addtid_b32 v109 offset:7200\n"
"v_add_f32 v131, v131, v139\n"
"v_mfma_f32_16x16x32_bf16 v[104:107], v[48:51], a[96:99], v[104:107]\n"
"ds_write_addtid_b32 v110 offset:11904\n"
"v_add_f32 v128, v128, v100\n"
"v_mfma_f32_16x16x32_bf16 v[104:107], v[52:55], a[100:103], v[104:107]\n"
"ds_write_addtid_b32 v111 offset:16544\n"
"v_add_f32 v129, v129, v101\n"
"v_mfma_f32_16x16x32_bf16 v[104:107], v[56:59], a[104:107], v[104:107]\n"
"v_add_f32 v130, v130, v102\n"
"v_add_f32 v131, v131, v103\n"
"v_mfma_f32_16x16x32_bf16 v[104:107], v[60:63], a[108:111], v[104:107]\n"
"v_cvt_pk_bf16_f32 v94, v128, v129\n"
"v_cvt_pk_bf16_f32 v95, v130, v131\n"
"v_mfma_f32_16x16x32_bf16 v[100:103], v[112:115], a[112:115], 0\n"
"buffer_store_dwordx4 v[92:95], v13, s[40:43], s93 offen sc1\n"
"v_mfma_f32_16x16x32_bf16 v[100:103], v[116:119], a[116:119], v[100:103]\n"
"s_waitcnt lgkmcnt(15)\n"
"ds_write_addtid_b32 v104 offset:2816\n"
"v_add_f32 v140, v140, v148\n"
"v_mfma_f32_16x16x32_bf16 v[100:103], v[120:123], a[120:123], v[100:103]\n"
"ds_write_addtid_b32 v105 offset:7456\n"
"v_add_f32 v141, v141, v149\n"
"v_mfma_f32_16x16x32_bf16 v[100:103], v[48:51], a[124:127], v[100:103]\n"
"ds_write_addtid_b32 v106 offset:12160\n"
"v_add_f32 v142, v142, v150\n"
"v_mfma_f32_16x16x32_bf16 v[100:103], v[52:55], a[128:131], v[100:103]\n"
"ds_write_addtid_b32 v107 offset:16800\n"
"v_add_f32 v143, v143, v151\n"
"v_mfma_f32_16x16x32_bf16 v[100:103], v[56:59], a[132:135], v[100:103]\n"
"v_add_f32 v140, v140, v152\n"
"v_add_f32 v141, v141, v153\n"
"v_mfma_f32_16x16x32_bf16 v[100:103], v[60:63], a[136:139], v[100:103]\n"
"v_add_f32 v142, v142, v154\n"
"v_add_f32 v143, v143, v155\n"
"v_mfma_f32_16x16x32_bf16 v[92:95], v[112:115], a[140:143], 0\n"
"v_add_f32 v140, v140, v96\n"
"v_add_f32 v141, v141, v97\n"
"v_mfma_f32_16x16x32_bf16 v[92:95], v[116:119], a[144:147], v[92:95]\n"
"ds_write_addtid_b32 v100 offset:3072\n"
"v_add_f32 v142, v142, v98\n"
"v_mfma_f32_16x16x32_bf16 v[92:95], v[120:123], a[148:151], v[92:95]\n"
"ds_write_addtid_b32 v101 offset:7712\n"
"v_add_f32 v143, v143, v99\n"
"v_mfma_f32_16x16x32_bf16 v[92:95], v[48:51], a[152:155], v[92:95]\n"
"ds_write_addtid_b32 v102 offset:12416\n"
"v_cvt_pk_bf16_f32 v104, v140, v141\n"
"v_mfma_f32_16x16x32_bf16 v[92:95], v[52:55], a[156:159], v[92:95]\n"
"ds_write_addtid_b32 v103 offset:17056\n"
"v_cvt_pk_bf16_f32 v105, v142, v143\n"
"v_mfma_f32_16x16x32_bf16 v[92:95], v[56:59], a[160:163], v[92:95]\n"
"s_waitcnt lgkmcnt(15)\n"
"v_add_f32 v156, v156, v160\n"
"v_add_f32 v157, v157, v161\n"
"v_mfma_f32_16x16x32_bf16 v[92:95], v[60:63], a[164:167], v[92:95]\n"
"v_add_f32 v158, v158, v162\n"
"v_add_f32 v159, v159, v163\n"
"v_mfma_f32_16x16x32_bf16 v[96:99], v[112:115], a[224:227], 0\n"
"v_add_f32 v156, v156, v164\n"
"v_add_f32 v157, v157, v165\n"
"v_mfma_f32_16x16x32_bf16 v[96:99], v[116:119], a[228:231], v[96:99]\n"
"ds_write_addtid_b32 v92 offset:3328\n"
"v_add_f32 v158, v158, v166\n"
"v_mfma_f32_16x16x32_bf16 v[96:99], v[120:123], a[232:235], v[96:99]\n"
"ds_write_addtid_b32 v93 offset:7968\n"
"v_add_f32 v159, v159, v167\n"
"v_mfma_f32_16x16x32_bf16 v[96:99], v[48:51], a[236:239], v[96:99]\n"
"ds_write_addtid_b32 v94 offset:12672\n"
"v_add_f32 v156, v156, v124\n"
"v_mfma_f32_16x16x32_bf16 v[96:99], v[52:55], a[240:243], v[96:99]\n"
"ds_write_addtid_b32 v95 offset:17312\n"
"v_add_f32 v157, v157, v125\n"
"v_mfma_f32_16x16x32_bf16 v[96:99], v[56:59], a[244:247], v[96:99]\n"
"v_add_f32 v158, v158, v126\n"
"v_add_f32 v159, v159, v127\n"
"v_mfma_f32_16x16x32_bf16 v[96:99], v[60:63], a[248:251], v[96:99]\n"
"v_cvt_pk_bf16_f32 v106, v156, v157\n"
"v_cvt_pk_bf16_f32 v107, v158, v159\n"
"v_mfma_f32_16x16x32_bf16 v[100:103], v[112:115], a[252:255], 0\n"
"buffer_store_dwordx4 v[104:107], v15, s[40:43], s93 offen sc1\n"
"v_mfma_f32_16x16x32_bf16 v[100:103], v[116:119], v[24:27], v[100:103]\n"
"buffer_store_dwordx2 v[0:1], v14, s[40:43], s94 offen offset:0 sc1\n"
"ds_write_addtid_b32 v96 offset:3584\n"
"v_mfma_f32_16x16x32_bf16 v[100:103], v[120:123], v[28:31], v[100:103]\n"
"ds_write_addtid_b32 v97 offset:8224\n"
"v_mfma_f32_16x16x32_bf16 v[100:103], v[48:51], v[32:35], v[100:103]\n"
"ds_write_addtid_b32 v98 offset:12928\n"
"v_mfma_f32_16x16x32_bf16 v[100:103], v[52:55], v[36:39], v[100:103]\n"
"ds_write_addtid_b32 v99 offset:17568\n"
"v_mfma_f32_16x16x32_bf16 v[100:103], v[56:59], v[40:43], v[100:103]\n"
"v_mfma_f32_16x16x32_bf16 v[100:103], v[60:63], v[44:47], v[100:103]\n"
"v_mfma_f32_16x16x32_bf16 v[104:107], a[168:171], v[112:115], 0\n"
"v_mfma_f32_16x16x32_bf16 v[104:107], a[172:175], v[116:119], v[104:107]\n"
"ds_write_addtid_b32 v100 offset:3840\n"
"v_mfma_f32_16x16x32_bf16 v[104:107], a[176:179], v[120:123], v[104:107]\n"
"ds_write_addtid_b32 v101 offset:8480\n"
"v_mfma_f32_16x16x32_bf16 v[104:107], a[180:183], v[48:51], v[104:107]\n"
"ds_write_addtid_b32 v102 offset:13184\n"
"v_mfma_f32_16x16x32_bf16 v[104:107], a[184:187], v[52:55], v[104:107]\n"
"ds_write_addtid_b32 v103 offset:17824\n"
"v_mfma_f32_16x16x32_bf16 v[104:107], a[188:191], v[56:59], v[104:107]\n"
"v_mfma_f32_16x16x32_bf16 v[104:107], a[192:195], v[60:63], v[104:107]\n"
"v_mfma_f32_16x16x32_bf16 v[108:111], a[196:199], v[112:115], 0\n"
"v_mfma_f32_16x16x32_bf16 v[108:111], a[200:203], v[116:119], v[108:111]\n"
"s_waitcnt lgkmcnt(0)\n"
"v_mfma_f32_16x16x32_bf16 v[108:111], a[204:207], v[120:123], v[108:111]\n"
"s_barrier\n"
"v_mfma_f32_16x16x32_bf16 v[108:111], a[208:211], v[48:51], v[108:111]\n"
"ds_read_b128 v[112:115], v2 offset:0\n"
"ds_read_b128 v[100:103], v3 offset:2560\n"
"v_mfma_f32_16x16x32_bf16 v[108:111], a[212:215], v[52:55], v[108:111]\n"
"ds_read_b128 v[116:119], v4 offset:1024\n"
"ds_read_b128 v[120:123], v2 offset:256\n"
"v_mfma_f32_16x16x32_bf16 v[108:111], a[216:219], v[56:59], v[108:111]\n"
"ds_read_b128 v[124:127], v3 offset:2816\n"
"ds_read_b128 v[128:131], v4 offset:1280\n"
"v_mfma_f32_16x16x32_bf16 v[108:111], a[220:223], v[60:63], v[108:111]\n"
"s_waitcnt lgkmcnt(3)\n"
"v_add_f32 v112, v112, v100\n"
"v_add_f32 v113, v113, v101\n"
"v_add_f32 v114, v114, v102\n"
"v_add_f32 v115, v115, v103\n"
"v_add_f32 v112, v112, v116\n"
"v_add_f32 v113, v113, v117\n"
"v_add_f32 v114, v114, v118\n"
"v_add_f32 v115, v115, v119\n"
"v_add_f32 v112, v112, v104\n"
"v_add_f32 v113, v113, v105\n"
"v_add_f32 v114, v114, v106\n"
"v_add_f32 v115, v115, v107\n"
"s_add_u32 s94, s50, 0x0\n"
"s_and_b32 s93, s94, 0xfbffffff\n"
"v_cvt_pk_bf16_f32 v104, v112, v113\n"
"v_cvt_pk_bf16_f32 v105, v114, v115\n"
"s_waitcnt lgkmcnt(0)\n"
"v_add_f32 v120, v120, v124\n"
"v_add_f32 v121, v121, v125\n"
"v_add_f32 v122, v122, v126\n"
"v_add_f32 v123, v123, v127\n"
"v_add_f32 v120, v120, v128\n"
"v_add_f32 v121, v121, v129\n"
"v_add_f32 v122, v122, v130\n"
"v_add_f32 v123, v123, v131\n"
"v_add_f32 v120, v120, v108\n"
"v_add_f32 v121, v121, v109\n"
"v_add_f32 v122, v122, v110\n"
"v_add_f32 v123, v123, v111\n"
"s_waitcnt vmcnt(1)\n"
"buffer_store_dwordx2 v[0:1], v16, s[40:43], s92 offen offset:0 sc1\n"
"v_cvt_pk_bf16_f32 v106, v120, v121\n"
"v_cvt_pk_bf16_f32 v107, v122, v123\n"
"buffer_store_dwordx4 v[104:107], v13, s[40:43], s93 offen sc1\n"
"s_waitcnt vmcnt(0)\n"
"buffer_store_dwordx2 v[0:1], v14, s[40:43], s94 offen offset:0 sc1\n"
"s_branch .LH_end\n"
".LH_t3:\n"
"v_mfma_f32_16x16x32_bf16 v[104:107], a[172:175], v[116:119], v[104:107]\n"
"ds_write_addtid_b32 v100 offset:3840\n"
"v_mfma_f32_16x16x32_bf16 v[104:107], a[176:179], v[120:123], v[104:107]\n"
"ds_write_addtid_b32 v101 offset:8480\n"
"v_mfma_f32_16x16x32_bf16 v[104:107], a[180:183], v[48:51], v[104:107]\n"
"ds_write_addtid_b32 v102 offset:13184\n"
"v_mfma_f32_16x16x32_bf16 v[104:107], a[184:187], v[52:55], v[104:107]\n"
"ds_write_addtid_b32 v103 offset:17824\n"
"v_mfma_f32_16x16x32_bf16 v[104:107], a[188:191], v[56:59], v[104:107]\n"
"v_mfma_f32_16x16x32_bf16 v[104:107], a[192:195], v[60:63], v[104:107]\n"
"v_mfma_f32_16x16x32_bf16 v[108:111], a[196:199], v[112:115], 0\n"
"v_mfma_f32_16x16x32_bf16 v[108:111], a[200:203], v[116:119], v[108:111]\n"
"s_waitcnt lgkmcnt(0)\n"
"v_mfma_f32_16x16x32_bf16 v[108:111], a[204:207], v[120:123], v[108:111]\n"
"s_barrier\n"
"v_mfma_f32_16x16x32_bf16 v[108:111], a[208:211], v[48:51], v[108:111]\n"
"ds_read_b128 v[112:115], v2 offset:0\n"
"ds_read_b128 v[100:103], v3 offset:2560\n"
"v_mfma_f32_16x16x32_bf16 v[108:111], a[212:215], v[52:55], v[108:111]\n"
"ds_read_b128 v[116:119], v4 offset:1024\n"
"ds_read_b128 v[120:123], v2 offset:256\n"
"v_mfma_f32_16x16x32_bf16 v[108:111], a[216:219], v[56:59], v[108:111]\n"
"ds_read_b128 v[124:127], v3 offset:2816\n"
"ds_read_b128 v[128:131], v4 offset:1280\n"
"v_mfma_f32_16x16x32_bf16 v[108:111], a[220:223], v[60:63], v[108:111]\n"
"s_waitcnt vmcnt(10)\n"
"v_mfma_f32_16x16x32_bf16 v[132:135], v[64:67], a[0:3], 0\n"
"v_mfma_f32_16x16x32_bf16 v[132:135], v[68:71], a[4:7], v[132:135]\n"
"s_waitcnt vmcnt(8)\n"
"v_mfma_f32_16x16x32_bf16 v[132:135], v[72:75], a[8:11], v[132:135]\n"
"v_mfma_f32_16x16x32_bf16 v[132:135], v[76:79], a[12:15], v[132:135]\n"
"s_waitcnt vmcnt(5)\n"
"v_mfma_f32_16x16x32_bf16 v[132:135], v[80:83], a[16:19], v[132:135]\n"
"s_waitcnt lgkmcnt(3)\n"
"v_add_f32 v112, v112, v100\n"
"v_add_f32 v113, v113, v101\n"
"v_mfma_f32_16x16x32_bf16 v[132:135], v[84:87], a[20:23], v[132:135]\n"
"s_waitcnt vmcnt(4)\n"
"v_add_f32 v114, v114, v102\n"
"v_mfma_f32_16x16x32_bf16 v[132:135], v[88:91], a[24:27], v[132:135]\n"
"v_add_f32 v115, v115, v103\n"
"v_add_f32 v112, v112, v116\n"
"v_mfma_f32_16x16x32_bf16 v[136:139], v[64:67], a[28:31], 0\n"
"v_mfma_f32_16x16x32_bf16 v[136:139], v[68:71], a[32:35], v[136:139]\n"
"ds_write_addtid_b32 v132 offset:0\n"
"v_add_f32 v113, v113, v117\n"
"v_mfma_f32_16x16x32_bf16 v[136:139], v[72:75], a[36:39], v[136:139]\n"
"ds_write_addtid_b32 v133 offset:4640\n"
"v_mfma_f32_16x16x32_bf16 v[136:139], v[76:79], a[40:43], v[136:139]\n"
"ds_write_addtid_b32 v134 offset:9344\n"
"v_add_f32 v114, v114, v118\n"
"v_mfma_f32_16x16x32_bf16 v[136:139], v[80:83], a[44:47], v[136:139]\n"
"ds_write_addtid_b32 v135 offset:13984\n"
"v_mfma_f32_16x16x32_bf16 v[136:139], v[84:87], a[48:51], v[136:139]\n"
"v_add_f32 v115, v115, v119\n"
"v_add_f32 v112, v112, v104\n"
"v_mfma_f32_16x16x32_bf16 v[136:139], v[88:91], a[52:55], v[136:139]\n"
"v_add_f32 v113, v113, v105\n"
"v_add_f32 v114, v114, v106\n"
"v_mfma_f32_16x16x32_bf16 v[116:119], v[64:67], a[56:59], 0\n"
"v_add_f32 v115, v115, v107\n"
"v_mfma_f32_16x16x32_bf16 v[116:119], v[68:71], a[60:63], v[116:119]\n"
"s_add_u32 s94, s50, 0xffe80000\n"
"s_and_b32 s93, s94, 0xfbffffff\n"
"ds_write_addtid_b32 v136 offset:256\n"
"v_mfma_f32_16x16x32_bf16 v[116:119], v[72:75], a[64:67], v[116:119]\n"
"ds_write_addtid_b32 v137 offset:4896\n"
"v_mfma_f32_16x16x32_bf16 v[116:119], v[76:79], a[68:71], v[116:119]\n"
"ds_write_addtid_b32 v138 offset:9600\n"
"v_cvt_pk_bf16_f32 v104, v112, v113\n"
"v_mfma_f32_16x16x32_bf16 v[116:119], v[80:83], a[72:75], v[116:119]\n"
"ds_write_addtid_b32 v139 offset:14240\n"
"v_cvt_pk_bf16_f32 v105, v114, v115\n"
"v_mfma_f32_16x16x32_bf16 v[116:119], v[84:87], a[76:79], v[116:119]\n"
"s_waitcnt lgkmcnt(8)\n"
"v_add_f32 v120, v120, v124\n"
"v_add_f32 v121, v121, v125\n"
"v_mfma_f32_16x16x32_bf16 v[116:119], v[88:91], a[80:83], v[116:119]\n"
"v_add_f32 v122, v122, v126\n"
"v_add_f32 v123, v123, v127\n"
"v_mfma_f32_16x16x32_bf16 v[112:115], v[64:67], a[84:87], 0\n"
"v_add_f32 v120, v120, v128\n"
"v_add_f32 v121, v121, v129\n"
"v_mfma_f32_16x16x32_bf16 v[112:115], v[68:71], a[88:91], v[112:115]\n"
"ds_write_addtid_b32 v116 offset:512\n"
"v_add_f32 v122, v122, v130\n"
"v_mfma_f32_16x16x32_bf16 v[112:115], v[72:75], a[92:95], v[112:115]\n"
"ds_write_addtid_b32 v117 offset:5152\n"
"v_add_f32 v123, v123, v131\n"
"v_mfma_f32_16x16x32_bf16 v[112:115], v[76:79], a[96:99], v[112:115]\n"
"ds_write_addtid_b32 v118 offset:9856\n"
"v_add_f32 v120, v120, v108\n"
"v_mfma_f32_16x16x32_bf16 v[112:115], v[80:83], a[100:103], v[112:115]\n"
"ds_write_addtid_b32 v119 offset:14496\n"
"v_add_f32 v121, v121, v109\n"
"v_mfma_f32_16x16x32_bf16 v[112:115], v[84:87], a[104:107], v[112:115]\n"
"v_add_f32 v122, v122, v110\n"
"v_add_f32 v123, v123, v111\n"
"v_mfma_f32_16x16x32_bf16 v[112:115], v[88:91], a[108:111], v[112:115]\n"
"s_waitcnt vmcnt(2)\n"
"buffer_store_dwordx2 v[0:1], v16, s[40:43], s92 offen offset:0 sc1\n"
"v_cvt_pk_bf16_f32 v106, v120, v121\n"
"v_mfma_f32_16x16x32_bf16 v[108:111], v[64:67], a[112:115], 0\n"
"v_cvt_pk_bf16_f32 v107, v122, v123\n"
"v_mfma_f32_16x16x32_bf16 v[108:111], v[68:71], a[116:119], v[108:111]\n"
"ds_write_addtid_b32 v112 offset:768\n"
"v_mfma_f32_16x16x32_bf16 v[108:111], v[72:75], a[120:123], v[108:111]\n"
"ds_write_addtid_b32 v113 offset:5408\n"
"v_mfma_f32_16x16x32_bf16 v[108:111], v[76:79], a[124:127], v[108:111]\n"
"ds_write_addtid_b32 v114 offset:10112\n"
"v_mfma_f32_16x16x32_bf16 v[108:111], v[80:83], a[128:131], v[108:111]\n"
"buffer_store_dwordx4 v[104:107], v13, s[40:43], s93 offen sc1\n"
"ds_write_addtid_b32 v115 offset:14752\n"
"v_mfma_f32_16x16x32_bf16 v[108:111], v[84:87], a[132:135], v[108:111]\n"
"s_waitcnt vmcnt(2)\n"
"v_mfma_f32_16x16x32_bf16 v[108:111], v[88:91], a[136:139], v[108:111]\n"
"v_mfma_f32_16x16x32_bf16 v[104:107], v[64:67], a[140:143], 0\n"
"v_mfma_f32_16x16x32_bf16 v[104:107], v[68:71], a[144:147], v[104:107]\n"
"ds_write_addtid_b32 v108 offset:1024\n"
"v_mfma_f32_16x16x32_bf16 v[104:107], v[72:75], a[148:151], v[104:107]\n"
"ds_write_addtid_b32 v109 offset:5664\n"
"v_mfma_f32_16x16x32_bf16 v[104:107], v[76:79], a[152:155], v[104:107]\n"
"ds_write_addtid_b32 v110 offset:10368\n"
"v_mfma_f32_16x16x32_bf16 v[104:107], v[80:83], a[156:159], v[104:107]\n"
"ds_write_addtid_b32 v111 offset:15008\n"
"v_mfma_f32_16x16x32_bf16 v[104:107], v[84:87], a[160:163], v[104:107]\n"
"v_mfma_f32_16x16x32_bf16 v[104:107], v[88:91], a[164:167], v[104:107]\n"
"v_mfma_f32_16x16x32_bf16 v[108:111], v[64:67], a[224:227], 0\n"
"v_mfma_f32_16x16x32_bf16 v[108:111], v[68:71], a[228:231], v[108:111]\n"
"ds_write_addtid_b32 v104 offset:1280\n"
"v_mfma_f32_16x16x32_bf16 v[108:111], v[72:75], a[232:235], v[108:111]\n"
"ds_write_addtid_b32 v105 offset:5920\n"
"v_mfma_f32_16x16x32_bf16 v[108:111], v[76:79], a[236:239], v[108:111]\n"
"ds_write_addtid_b32 v106 offset:10624\n"
"v_mfma_f32_16x16x32_bf16 v[108:111], v[80:83], a[240:243], v[108:111]\n"
"ds_write_addtid_b32 v107 offset:15264\n"
"v_mfma_f32_16x16x32_bf16 v[108:111], v[84:87], a[244:247], v[108:111]\n"
"v_mfma_f32_16x16x32_bf16 v[108:111], v[88:91], a[248:251], v[108:111]\n"
"v_mfma_f32_16x16x32_bf16 v[120:123], v[64:67], a[252:255], 0\n"
"v_mfma_f32_16x16x32_bf16 v[120:123], v[68:71], v[24:27], v[120:123]\n"
"ds_write_addtid_b32 v108 offset:1536\n"
"v_mfma_f32_16x16x32_bf16 v[120:123], v[72:75], v[28:31], v[120:123]\n"
"ds_write_addtid_b32 v109 offset:6176\n"
"v_mfma_f32_16x16x32_bf16 v[120:123], v[76:79], v[32:35], v[120:123]\n"
"ds_write_addtid_b32 v110 offset:10880\n"
"v_mfma_f32_16x16x32_bf16 v[120:123], v[80:83], v[36:39], v[120:123]\n"
"ds_write_addtid_b32 v111 offset:15520\n"
"v_mfma_f32_16x16x32_bf16 v[120:123], v[84:87], v[40:43], v[120:123]\n"
"v_mfma_f32_16x16x32_bf16 v[120:123], v[88:91], v[44:47], v[120:123]\n"
"v_mfma_f32_16x16x32_bf16 v[124:127], a[168:171], v[64:67], 0\n"
"v_mfma_f32_16x16x32_bf16 v[124:127], a[172:175], v[68:71], v[124:127]\n"
"ds_write_addtid_b32 v120 offset:1792\n"
"v_mfma_f32_16x16x32_bf16 v[124:127], a[176:179], v[72:75], v[124:127]\n"
"ds_write_addtid_b32 v121 offset:6432\n"
"v_mfma_f32_16x16x32_bf16 v[124:127], a[180:183], v[76:79], v[124:127]\n"
"ds_write_addtid_b32 v122 offset:11136\n"
"v_mfma_f32_16x16x32_bf16 v[124:127], a[184:187], v[80:83], v[124:127]\n"
"ds_write_addtid_b32 v123 offset:15776\n"
"v_mfma_f32_16x16x32_bf16 v[124:127], a[188:191], v[84:87], v[124:127]\n"
"v_mfma_f32_16x16x32_bf16 v[124:127], a[192:195], v[88:91], v[124:127]\n"
"v_mfma_f32_16x16x32_bf16 v[120:123], a[196:199], v[64:67], 0\n"
"v_mfma_f32_16x16x32_bf16 v[120:123], a[200:203], v[68:71], v[120:123]\n"
"s_waitcnt lgkmcnt(0)\n"
"v_mfma_f32_16x16x32_bf16 v[120:123], a[204:207], v[72:75], v[120:123]\n"
"s_barrier\n"
"v_mfma_f32_16x16x32_bf16 v[120:123], a[208:211], v[76:79], v[120:123]\n"
"ds_read_b128 v[64:67], v2 offset:2048\n"
"ds_read_b128 v[128:131], v3 offset:512\n"
"v_mfma_f32_16x16x32_bf16 v[120:123], a[212:215], v[80:83], v[120:123]\n"
"ds_read_b128 v[132:135], v4 offset:3072\n"
"ds_read_b128 v[136:139], v2 offset:2304\n"
"v_mfma_f32_16x16x32_bf16 v[120:123], a[216:219], v[84:87], v[120:123]\n"
"ds_read_b128 v[140:143], v3 offset:768\n"
"v_mfma_f32_16x16x32_bf16 v[120:123], a[220:223], v[88:91], v[120:123]\n"
"ds_read_b128 v[144:147], v4 offset:3328\n"
"s_waitcnt lgkmcnt(3)\n"
"v_add_f32 v64, v64, v128\n"
"v_add_f32 v65, v65, v129\n"
"v_add_f32 v66, v66, v130\n"
"v_add_f32 v67, v67, v131\n"
"v_add_f32 v64, v64, v132\n"
"v_add_f32 v65, v65, v133\n"
"v_add_f32 v66, v66, v134\n"
"s_waitcnt vmcnt(0)\n"
"buffer_store_dwordx2 v[0:1], v14, s[40:43], s94 offen offset:0 sc1\n"
"v_add_f32 v67, v67, v135\n"
"v_add_f32 v64, v64, v124\n"
"v_add_f32 v65, v65, v125\n"
"v_add_f32 v66, v66, v126\n"
"v_add_f32 v67, v67, v127\n"
"s_add_u32 s94, s50, 0x0\n"
"s_and_b32 s93, s94, 0xfbffffff\n"
"v_cvt_pk_bf16_f32 v124, v64, v65\n"
"v_cvt_pk_bf16_f32 v125, v66, v67\n"
"s_waitcnt lgkmcnt(0)\n"
"v_add_f32 v136, v136, v140\n"
"v_add_f32 v137, v137, v141\n"
"v_add_f32 v138, v138, v142\n"
"v_add_f32 v139, v139, v143\n"
"v_add_f32 v136, v136, v144\n"
"v_add_f32 v137, v137, v145\n"
"v_add_f32 v138, v138, v146\n"
"v_add_f32 v139, v139, v147\n"
"v_add_f32 v136, v136, v120\n"
"v_add_f32 v137, v137, v121\n"
"v_add_f32 v138, v138, v122\n"
"v_add_f32 v139, v139, v123\n"
"v_cvt_pk_bf16_f32 v126, v136, v137\n"
"v_cvt_pk_bf16_f32 v127, v138, v139\n"
"buffer_store_dwordx4 v[124:127], v13, s[40:43], s93 offen sc1\n"
"s_waitcnt vmcnt(0)\n"
"buffer_store_dwordx2 v[0:1], v14, s[40:43], s94 offen offset:0 sc1\n"
"s_branch .LH_end\n"
".LH_end:\n"
"s_waitcnt vmcnt(0) lgkmcnt(0)\n"
"s_endpgm\n"
      :: "{v2}"((unsigned)(lds0 + ((wid + 3) & 3) * 18592 + trp)), "{v3}"((unsigned)(lds0 + ((wid + 2) & 3) * 18592 + trp)), "{v4}"((unsigned)(lds0 + ((wid + 1) & 3) * 18592 + trp)), "{v5}"((unsigned)(lane * 16 + f0 * 1024 + ((0 + rot) % 7) * 1024 + 134316032)), "{v6}"((unsigned)(lane * 16 + f0 * 1024 + ((1 + rot) % 7) * 1024 + 134316032)), "{v7}"((unsigned)(lane * 16 + f0 * 1024 + ((2 + rot) % 7) * 1024 + 134316032)), "{v8}"((unsigned)(lane * 16 + f0 * 1024 + ((3 + rot) % 7) * 1024 + 134316032)), "{v9}"((unsigned)(lane * 16 + f0 * 1024 + ((4 + rot) % 7) * 1024 + 134316032)), "{v10}"((unsigned)(lane * 16 + f0 * 1024 + ((5 + rot) % 7) * 1024 + 134316032)), "{v11}"((unsigned)(lane * 16 + f0 * 1024 + ((6 + rot) % 7) * 1024 + 134316032)), "{v12}"((unsigned)((f0 + lane % 7) * 8 + kq * 32 + 353024 + H_GL * 1572864)), "{v13}"((unsigned)(lane * 16 + pair * 4096 + kq * 1024 + 212992)), "{v14}"((unsigned)(lane ? DROP : pair * 64 + kq * 8 + 354304)), "{v15}"((unsigned)(lane * 16 + (pg0 + 4) * 4096 + kq * 1024 + 212992)), "{v16}"((unsigned)(lane > 1 ? DROP : (lane ? pg0 + 4 : pair) * 64 + kq * 8 + 354304)), "{v17}"((unsigned)((lane & 15) * 8 + 67462912)), "{v18}"((unsigned)(lane * 16 + 67457024)), "{v19}"((unsigned)((lane & 24) ? DROP : (2 * (lane & 7) + (lane >> 5)) * 16 + 67461120)), "{v20}"((unsigned)(0x3727c5ac)), "{v48}"((unsigned)(vT + tsw)), "{v49}"((unsigned)(vT + (tsw ^ 8))), "{v50}"((unsigned)(vD + ((0 + rot) % 7) * 65536)), "{v51}"((unsigned)(vD + ((1 + rot) % 7) * 65536)), "{v52}"((unsigned)(vD + ((2 + rot) % 7) * 65536)), "{v53}"((unsigned)(vD + ((3 + rot) % 7) * 65536)), "{v54}"((unsigned)(vD + ((4 + rot) % 7) * 65536)), "{v55}"((unsigned)(vD + ((5 + rot) % 7) * 65536)), "{v56}"((unsigned)(vD + ((6 + rot) % 7) * 65536)), "{s[40:43]}"(rS), "{s[44:47]}"(rW), "{s[48:49]}"(wsp), "{s50}"((unsigned)(sA)), "{s51}"((unsigned)(sLo)), "{s52}"((unsigned)(sHi)), "{s53}"((unsigned)(sEx)), "{s54}"((unsigned)(sEnt)), "{s55}"((unsigned)(sLd)), "{s[56:57]}"(wsb + (sLo & 0x3ffffff)), "{s59}"((unsigned)(lds0 + wid * 18592)), "{s62}"((unsigned)(0x3a800000)), "{s66}"((unsigned)(sW0 + 64 * ((wid + 1) & 3))), "{s67}"((unsigned)(sW0 + 64 * ((wid + 2) & 3))), "{s68}"((unsigned)(sW0 + 64 * ((wid + 3) & 3))), "{s69}"((unsigned)(sW0 + 64 * ((wid + 4) & 3))), "{s70}"((unsigned)(sW0 + 256))
      : "v255", "a255", "s95", "vcc", "scc", "memory");

    } else if (grp < 4) {
      const unsigned sEx = sHi + G_EX * 3 * RS, sEnt = -wid & 3;
    asm volatile(
"s_nop 4\n"
"v_mov_b32 v186, 0\n"
"global_load_dwordx2 v[0:1], v186, s[48:49] sc1\n"
"v_mov_b32 v188, 0x3f803f80\n"
"v_mov_b32 v189, 0x3f803f80\n"
"v_mov_b32 v190, 0x3f803f80\n"
"v_mov_b32 v191, 0x3f803f80\n"
"s_add_u32 m0, s55, 0\n"
"s_add_u32 s60, s62, 0\n"
"buffer_load_dwordx4 v46, s[44:47], s60 offen lds\n"
"s_add_u32 m0, s55, 1024\n"
"s_add_u32 s60, s62, 32768\n"
"buffer_load_dwordx4 v46, s[44:47], s60 offen lds\n"
"s_add_u32 m0, s55, 2048\n"
"s_add_u32 s60, s63, 0\n"
"buffer_load_dwordx4 v46, s[44:47], s60 offen lds\n"
"s_add_u32 m0, s55, 3072\n"
"s_add_u32 s60, s63, 32768\n"
"buffer_load_dwordx4 v46, s[44:47], s60 offen lds\n"
"s_add_u32 m0, s55, 4096\n"
"s_add_u32 s60, s64, 0\n"
"buffer_load_dwordx4 v46, s[44:47], s60 offen lds\n"
"s_add_u32 m0, s55, 5120\n"
"s_add_u32 s60, s64, 32768\n"
"buffer_load_dwordx4 v46, s[44:47], s60 offen lds\n"
"s_add_u32 m0, s55, 6144\n"
"s_add_u32 s60, s65, 0\n"
"buffer_load_dwordx4 v46, s[44:47], s60 offen lds\n"
"s_add_u32 m0, s55, 7168\n"
"s_add_u32 s60, s65, 32768\n"
"buffer_load_dwordx4 v46, s[44:47], s60 offen lds\n"
"s_add_u32 m0, s55, 8192\n"
"s_add_u32 s60, s66, 0\n"
"buffer_load_dwordx4 v46, s[44:47], s60 offen lds\n"
"s_add_u32 m0, s55, 9216\n"
"s_add_u32 s60, s66, 32768\n"
"buffer_load_dwordx4 v46, s[44:47], s60 offen lds\n"
"s_add_u32 m0, s55, 10240\n"
"s_add_u32 s60, s62, 0\n"
"buffer_load_dwordx4 v47, s[44:47], s60 offen lds\n"
"s_add_u32 m0, s55, 11264\n"
"s_add_u32 s60, s62, 32768\n"
"buffer_load_dwordx4 v47, s[44:47], s60 offen lds\n"
"s_add_u32 m0, s55, 12288\n"
"s_add_u32 s60, s63, 0\n"
"buffer_load_dwordx4 v47, s[44:47], s60 offen lds\n"
"s_add_u32 m0, s55, 13312\n"
"s_add_u32 s60, s63, 32768\n"
"buffer_load_dwordx4 v47, s[44:47], s60 offen lds\n"
"s_add_u32 m0, s55, 14336\n"
"s_add_u32 s60, s64, 0\n"
"buffer_load_dwordx4 v47, s[44:47], s60 offen lds\n"
"s_add_u32 m0, s55, 15360\n"
"s_add_u32 s60, s64, 32768\n"
"buffer_load_dwordx4 v47, s[44:47], s60 offen lds\n"
"s_add_u32 m0, s55, 16384\n"
"s_add_u32 s60, s65, 0\n"
"buffer_load_dwordx4 v47, s[44:47], s60 offen lds\n"
"s_add_u32 m0, s55, 17408\n"
"s_add_u32 s60, s65, 32768\n"
"buffer_load_dwordx4 v47, s[44:47], s60 offen lds\n"
"s_add_u32 m0, s55, 18432\n"
"s_add_u32 s60, s66, 0\n"
"buffer_load_dwordx4 v47, s[44:47], s60 offen lds\n"
"s_add_u32 m0, s55, 19456\n"
"s_add_u32 s60, s66, 32768\n"
"buffer_load_dwordx4 v47, s[44:47], s60 offen lds\n"
"s_add_u32 m0, s55, 20480\n"
"s_add_u32 s60, s62, 0\n"
"buffer_load_dwordx4 v48, s[44:47], s60 offen lds\n"
"s_add_u32 m0, s55, 21504\n"
"s_add_u32 s60, s62, 32768\n"
"buffer_load_dwordx4 v48, s[44:47], s60 offen lds\n"
"s_add_u32 m0, s55, 22528\n"
"s_add_u32 s60, s63, 0\n"
"buffer_load_dwordx4 v48, s[44:47], s60 offen lds\n"
"s_add_u32 m0, s55, 23552\n"
"s_add_u32 s60, s63, 32768\n"
"buffer_load_dwordx4 v48, s[44:47], s60 offen lds\n"
"s_add_u32 m0, s55, 24576\n"
"s_add_u32 s60, s64, 0\n"
"buffer_load_dwordx4 v48, s[44:47], s60 offen lds\n"
"s_add_u32 m0, s55, 25600\n"
"s_add_u32 s60, s64, 32768\n"
"buffer_load_dwordx4 v48, s[44:47], s60 offen lds\n"
"s_add_u32 m0, s55, 26624\n"
"s_add_u32 s60, s65, 0\n"
"buffer_load_dwordx4 v48, s[44:47], s60 offen lds\n"
"s_add_u32 m0, s55, 27648\n"
"s_add_u32 s60, s65, 32768\n"
"buffer_load_dwordx4 v48, s[44:47], s60 offen lds\n"
"s_add_u32 m0, s55, 28672\n"
"s_add_u32 s60, s66, 0\n"
"buffer_load_dwordx4 v48, s[44:47], s60 offen lds\n"
"s_add_u32 m0, s55, 29696\n"
"s_add_u32 s60, s66, 32768\n"
"buffer_load_dwordx4 v48, s[44:47], s60 offen lds\n"
"s_add_u32 m0, s55, 30720\n"
"s_add_u32 s60, s62, 0\n"
"buffer_load_dwordx4 v49, s[44:47], s60 offen lds\n"
"s_add_u32 m0, s55, 31744\n"
"s_add_u32 s60, s62, 32768\n"
"buffer_load_dwordx4 v49, s[44:47], s60 offen lds\n"
"s_add_u32 m0, s55, 32768\n"
"s_add_u32 s60, s63, 0\n"
"buffer_load_dwordx4 v49, s[44:47], s60 offen lds\n"
"s_add_u32 m0, s55, 33792\n"
"s_add_u32 s60, s63, 32768\n"
"buffer_load_dwordx4 v49, s[44:47], s60 offen lds\n"
"s_add_u32 m0, s55, 34816\n"
"s_add_u32 s60, s64, 0\n"
"buffer_load_dwordx4 v49, s[44:47], s60 offen lds\n"
"s_add_u32 m0, s55, 35840\n"
"s_add_u32 s60, s64, 32768\n"
"buffer_load_dwordx4 v49, s[44:47], s60 offen lds\n"
"s_add_u32 m0, s55, 36864\n"
"s_add_u32 s60, s65, 0\n"
"buffer_load_dwordx4 v49, s[44:47], s60 offen lds\n"
"s_add_u32 m0, s55, 37888\n"
"s_add_u32 s60, s65, 32768\n"
"buffer_load_dwordx4 v49, s[44:47], s60 offen lds\n"
"s_add_u32 m0, s55, 38912\n"
"s_add_u32 s60, s66, 0\n"
"buffer_load_dwordx4 v49, s[44:47], s60 offen lds\n"
"s_add_u32 m0, s55, 39936\n"
"s_add_u32 s60, s66, 32768\n"
"buffer_load_dwordx4 v49, s[44:47], s60 offen lds\n"
"s_waitcnt vmcnt(38)\n"
"ds_read_b64_tr_b16 a[0:1], v44 offset:0\n"
"ds_read_b64_tr_b16 a[2:3], v44 offset:256\n"
"ds_read_b64_tr_b16 a[28:29], v45 offset:0\n"
"ds_read_b64_tr_b16 a[30:31], v45 offset:256\n"
"v_add_u32 v0, 2, v0\n"
"v_add_u32 v1, 0x9e3779b9, v1\n"
"v_or_b32 v0, 1, v0\n"
"v_readfirstlane_b32 s49, v1\n"
"v_readfirstlane_b32 s48, v0\n"
"s_waitcnt vmcnt(36)\n"
"ds_read_b64_tr_b16 a[56:57], v44 offset:2048\n"
"ds_read_b64_tr_b16 a[58:59], v44 offset:2304\n"
"ds_read_b64_tr_b16 a[84:85], v45 offset:2048\n"
"ds_read_b64_tr_b16 a[86:87], v45 offset:2304\n"
"s_waitcnt lgkmcnt(4)\n"
"s_add_u32 m0, s55, 0\n"
"s_add_u32 s60, s62, 0\n"
"buffer_load_dwordx4 v50, s[44:47], s60 offen lds\n"
"s_add_u32 m0, s55, 1024\n"
"s_add_u32 s60, s62, 32768\n"
"buffer_load_dwordx4 v50, s[44:47], s60 offen lds\n"
"s_waitcnt vmcnt(36)\n"
"ds_read_b64_tr_b16 a[112:113], v44 offset:4096\n"
"ds_read_b64_tr_b16 a[114:115], v44 offset:4352\n"
"ds_read_b64_tr_b16 a[140:141], v45 offset:4096\n"
"ds_read_b64_tr_b16 a[142:143], v45 offset:4352\n"
"s_waitcnt lgkmcnt(4)\n"
"s_add_u32 m0, s55, 2048\n"
"s_add_u32 s60, s63, 0\n"
"buffer_load_dwordx4 v50, s[44:47], s60 offen lds\n"
"s_add_u32 m0, s55, 3072\n"
"s_add_u32 s60, s63, 32768\n"
"buffer_load_dwordx4 v50, s[44:47], s60 offen lds\n"
"s_waitcnt vmcnt(36)\n"
"ds_read_b64_tr_b16 a[168:169], v44 offset:6144\n"
"ds_read_b64_tr_b16 a[170:171], v44 offset:6400\n"
"ds_read_b64_tr_b16 a[196:197], v45 offset:6144\n"
"ds_read_b64_tr_b16 a[198:199], v45 offset:6400\n"
"s_waitcnt lgkmcnt(4)\n"
"s_add_u32 m0, s55, 4096\n"
"s_add_u32 s60, s64, 0\n"
"buffer_load_dwordx4 v50, s[44:47], s60 offen lds\n"
"s_add_u32 m0, s55, 5120\n"
"s_add_u32 s60, s64, 32768\n"
"buffer_load_dwordx4 v50, s[44:47], s60 offen lds\n"
"s_waitcnt vmcnt(36)\n"
"ds_read_b64_tr_b16 a[224:225], v44 offset:8192\n"
"ds_read_b64_tr_b16 a[226:227], v44 offset:8448\n"
"ds_read_b64_tr_b16 a[252:253], v45 offset:8192\n"
"ds_read_b64_tr_b16 a[254:255], v45 offset:8448\n"
"s_waitcnt lgkmcnt(4)\n"
"s_add_u32 m0, s55, 6144\n"
"s_add_u32 s60, s65, 0\n"
"buffer_load_dwordx4 v50, s[44:47], s60 offen lds\n"
"s_add_u32 m0, s55, 7168\n"
"s_add_u32 s60, s65, 32768\n"
"buffer_load_dwordx4 v50, s[44:47], s60 offen lds\n"
"s_waitcnt vmcnt(36)\n"
"ds_read_b64_tr_b16 a[4:5], v44 offset:10240\n"
"ds_read_b64_tr_b16 a[6:7], v44 offset:10496\n"
"ds_read_b64_tr_b16 a[32:33], v45 offset:10240\n"
"ds_read_b64_tr_b16 a[34:35], v45 offset:10496\n"
"s_waitcnt lgkmcnt(4)\n"
"s_add_u32 m0, s55, 8192\n"
"s_add_u32 s60, s66, 0\n"
"buffer_load_dwordx4 v50, s[44:47], s60 offen lds\n"
"s_add_u32 m0, s55, 9216\n"
"s_add_u32 s60, s66, 32768\n"
"buffer_load_dwordx4 v50, s[44:47], s60 offen lds\n"
"s_waitcnt vmcnt(36)\n"
"ds_read_b64_tr_b16 a[60:61], v44 offset:12288\n"
"ds_read_b64_tr_b16 a[62:63], v44 offset:12544\n"
"ds_read_b64_tr_b16 a[88:89], v45 offset:12288\n"
"ds_read_b64_tr_b16 a[90:91], v45 offset:12544\n"
"s_waitcnt lgkmcnt(4)\n"
"s_add_u32 m0, s55, 10240\n"
"s_add_u32 s60, s62, 0\n"
"buffer_load_dwordx4 v51, s[44:47], s60 offen lds\n"
"s_add_u32 m0, s55, 11264\n"
"s_add_u32 s60, s62, 32768\n"
"buffer_load_dwordx4 v51, s[44:47], s60 offen lds\n"
"s_waitcnt vmcnt(36)\n"
"ds_read_b64_tr_b16 a[116:117], v44 offset:14336\n"
"ds_read_b64_tr_b16 a[118:119], v44 offset:14592\n"
"ds_read_b64_tr_b16 a[144:145], v45 offset:14336\n"
"ds_read_b64_tr_b16 a[146:147], v45 offset:14592\n"
"s_waitcnt lgkmcnt(4)\n"
"s_add_u32 m0, s55, 12288\n"
"s_add_u32 s60, s63, 0\n"
"buffer_load_dwordx4 v51, s[44:47], s60 offen lds\n"
"s_add_u32 m0, s55, 13312\n"
"s_add_u32 s60, s63, 32768\n"
"buffer_load_dwordx4 v51, s[44:47], s60 offen lds\n"
"s_waitcnt vmcnt(36)\n"
"ds_read_b64_tr_b16 a[172:173], v44 offset:16384\n"
"ds_read_b64_tr_b16 a[174:175], v44 offset:16640\n"
"ds_read_b64_tr_b16 a[200:201], v45 offset:16384\n"
"ds_read_b64_tr_b16 a[202:203], v45 offset:16640\n"
"s_waitcnt lgkmcnt(4)\n"
"s_add_u32 m0, s55, 14336\n"
"s_add_u32 s60, s64, 0\n"
"buffer_load_dwordx4 v51, s[44:47], s60 offen lds\n"
"s_add_u32 m0, s55, 15360\n"
"s_add_u32 s60, s64, 32768\n"
"buffer_load_dwordx4 v51, s[44:47], s60 offen lds\n"
"s_waitcnt vmcnt(36)\n"
"ds_read_b64_tr_b16 a[228:229], v44 offset:18432\n"
"ds_read_b64_tr_b16 a[230:231], v44 offset:18688\n"
"ds_read_b64_tr_b16 v[20:21], v45 offset:18432\n"
"ds_read_b64_tr_b16 v[22:23], v45 offset:18688\n"
"s_waitcnt lgkmcnt(4)\n"
"s_add_u32 m0, s55, 16384\n"
"s_add_u32 s60, s65, 0\n"
"buffer_load_dwordx4 v51, s[44:47], s60 offen lds\n"
"s_add_u32 m0, s55, 17408\n"
"s_add_u32 s60, s65, 32768\n"
"buffer_load_dwordx4 v51, s[44:47], s60 offen lds\n"
"s_waitcnt vmcnt(36)\n"
"ds_read_b64_tr_b16 a[8:9], v44 offset:20480\n"
"ds_read_b64_tr_b16 a[10:11], v44 offset:20736\n"
"ds_read_b64_tr_b16 a[36:37], v45 offset:20480\n"
"ds_read_b64_tr_b16 a[38:39], v45 offset:20736\n"
"s_waitcnt lgkmcnt(4)\n"
"s_add_u32 m0, s55, 18432\n"
"s_add_u32 s60, s66, 0\n"
"buffer_load_dwordx4 v51, s[44:47], s60 offen lds\n"
"s_add_u32 m0, s55, 19456\n"
"s_add_u32 s60, s66, 32768\n"
"buffer_load_dwordx4 v51, s[44:47], s60 offen lds\n"
"s_waitcnt vmcnt(36)\n"
"ds_read_b64_tr_b16 a[64:65], v44 offset:22528\n"
"ds_read_b64_tr_b16 a[66:67], v44 offset:22784\n"
"ds_read_b64_tr_b16 a[92:93], v45 offset:22528\n"
"ds_read_b64_tr_b16 a[94:95], v45 offset:22784\n"
"s_waitcnt lgkmcnt(4)\n"
"s_add_u32 m0, s55, 20480\n"
"s_add_u32 s60, s62, 0\n"
"buffer_load_dwordx4 v52, s[44:47], s60 offen lds\n"
"s_add_u32 m0, s55, 21504\n"
"s_add_u32 s60, s62, 32768\n"
"buffer_load_dwordx4 v52, s[44:47], s60 offen lds\n"
"s_waitcnt vmcnt(36)\n"
"ds_read_b64_tr_b16 a[120:121], v44 offset:24576\n"
"ds_read_b64_tr_b16 a[122:123], v44 offset:24832\n"
"ds_read_b64_tr_b16 a[148:149], v45 offset:24576\n"
"ds_read_b64_tr_b16 a[150:151], v45 offset:24832\n"
"s_waitcnt lgkmcnt(4)\n"
"s_add_u32 m0, s55, 22528\n"
"s_add_u32 s60, s63, 0\n"
"buffer_load_dwordx4 v52, s[44:47], s60 offen lds\n"
"s_add_u32 m0, s55, 23552\n"
"s_add_u32 s60, s63, 32768\n"
"buffer_load_dwordx4 v52, s[44:47], s60 offen lds\n"
"s_waitcnt vmcnt(36)\n"
"ds_read_b64_tr_b16 a[176:177], v44 offset:26624\n"
"ds_read_b64_tr_b16 a[178:179], v44 offset:26880\n"
"ds_read_b64_tr_b16 a[204:205], v45 offset:26624\n"
"ds_read_b64_tr_b16 a[206:207], v45 offset:26880\n"
"s_waitcnt lgkmcnt(4)\n"
"s_add_u32 m0, s55, 24576\n"
"s_add_u32 s60, s64, 0\n"
"buffer_load_dwordx4 v52, s[44:47], s60 offen lds\n"
"s_add_u32 m0, s55, 25600\n"
"s_add_u32 s60, s64, 32768\n"
"buffer_load_dwordx4 v52, s[44:47], s60 offen lds\n"
"s_waitcnt vmcnt(36)\n"
"ds_read_b64_tr_b16 a[232:233], v44 offset:28672\n"
"ds_read_b64_tr_b16 a[234:235], v44 offset:28928\n"
"ds_read_b64_tr_b16 v[24:25], v45 offset:28672\n"
"ds_read_b64_tr_b16 v[26:27], v45 offset:28928\n"
"s_waitcnt lgkmcnt(4)\n"
"s_add_u32 m0, s55, 26624\n"
"s_add_u32 s60, s65, 0\n"
"buffer_load_dwordx4 v52, s[44:47], s60 offen lds\n"
"s_add_u32 m0, s55, 27648\n"
"s_add_u32 s60, s65, 32768\n"
"buffer_load_dwordx4 v52, s[44:47], s60 offen lds\n"
"s_waitcnt vmcnt(36)\n"
"ds_read_b64_tr_b16 a[12:13], v44 offset:30720\n"
"ds_read_b64_tr_b16 a[14:15], v44 offset:30976\n"
"ds_read_b64_tr_b16 a[40:41], v45 offset:30720\n"
"ds_read_b64_tr_b16 a[42:43], v45 offset:30976\n"
"s_waitcnt lgkmcnt(4)\n"
"s_add_u32 m0, s55, 28672\n"
"s_add_u32 s60, s66, 0\n"
"buffer_load_dwordx4 v52, s[44:47], s60 offen lds\n"
"s_add_u32 m0, s55, 29696\n"
"s_add_u32 s60, s66, 32768\n"
"buffer_load_dwordx4 v52, s[44:47], s60 offen lds\n"
"s_waitcnt vmcnt(36)\n"
"ds_read_b64_tr_b16 a[68:69], v44 offset:32768\n"
"ds_read_b64_tr_b16 a[70:71], v44 offset:33024\n"
"ds_read_b64_tr_b16 a[96:97], v45 offset:32768\n"
"ds_read_b64_tr_b16 a[98:99], v45 offset:33024\n"
"s_waitcnt lgkmcnt(4)\n"
"s_waitcnt vmcnt(34)\n"
"ds_read_b64_tr_b16 a[124:125], v44 offset:34816\n"
"ds_read_b64_tr_b16 a[126:127], v44 offset:35072\n"
"ds_read_b64_tr_b16 a[152:153], v45 offset:34816\n"
"ds_read_b64_tr_b16 a[154:155], v45 offset:35072\n"
"s_waitcnt lgkmcnt(4)\n"
"s_waitcnt vmcnt(32)\n"
"ds_read_b64_tr_b16 a[180:181], v44 offset:36864\n"
"ds_read_b64_tr_b16 a[182:183], v44 offset:37120\n"
"ds_read_b64_tr_b16 a[208:209], v45 offset:36864\n"
"ds_read_b64_tr_b16 a[210:211], v45 offset:37120\n"
"s_waitcnt lgkmcnt(4)\n"
"s_waitcnt vmcnt(30)\n"
"ds_read_b64_tr_b16 a[236:237], v44 offset:38912\n"
"ds_read_b64_tr_b16 a[238:239], v44 offset:39168\n"
"ds_read_b64_tr_b16 v[28:29], v45 offset:38912\n"
"ds_read_b64_tr_b16 v[30:31], v45 offset:39168\n"
"s_waitcnt lgkmcnt(4)\n"
"s_waitcnt vmcnt(28)\n"
"ds_read_b64_tr_b16 a[16:17], v44 offset:0\n"
"ds_read_b64_tr_b16 a[18:19], v44 offset:256\n"
"ds_read_b64_tr_b16 a[44:45], v45 offset:0\n"
"ds_read_b64_tr_b16 a[46:47], v45 offset:256\n"
"s_waitcnt lgkmcnt(4)\n"
"s_waitcnt vmcnt(26)\n"
"ds_read_b64_tr_b16 a[72:73], v44 offset:2048\n"
"ds_read_b64_tr_b16 a[74:75], v44 offset:2304\n"
"ds_read_b64_tr_b16 a[100:101], v45 offset:2048\n"
"ds_read_b64_tr_b16 a[102:103], v45 offset:2304\n"
"s_waitcnt lgkmcnt(4)\n"
"s_waitcnt vmcnt(24)\n"
"ds_read_b64_tr_b16 a[128:129], v44 offset:4096\n"
"ds_read_b64_tr_b16 a[130:131], v44 offset:4352\n"
"ds_read_b64_tr_b16 a[156:157], v45 offset:4096\n"
"ds_read_b64_tr_b16 a[158:159], v45 offset:4352\n"
"s_waitcnt lgkmcnt(4)\n"
"s_waitcnt vmcnt(22)\n"
"ds_read_b64_tr_b16 a[184:185], v44 offset:6144\n"
"ds_read_b64_tr_b16 a[186:187], v44 offset:6400\n"
"ds_read_b64_tr_b16 a[212:213], v45 offset:6144\n"
"ds_read_b64_tr_b16 a[214:215], v45 offset:6400\n"
"s_waitcnt lgkmcnt(4)\n"
"s_waitcnt vmcnt(20)\n"
"ds_read_b64_tr_b16 a[240:241], v44 offset:8192\n"
"ds_read_b64_tr_b16 a[242:243], v44 offset:8448\n"
"ds_read_b64_tr_b16 v[32:33], v45 offset:8192\n"
"ds_read_b64_tr_b16 v[34:35], v45 offset:8448\n"
"s_waitcnt lgkmcnt(4)\n"
"s_waitcnt vmcnt(18)\n"
"ds_read_b64_tr_b16 a[20:21], v44 offset:10240\n"
"ds_read_b64_tr_b16 a[22:23], v44 offset:10496\n"
"ds_read_b64_tr_b16 a[48:49], v45 offset:10240\n"
"ds_read_b64_tr_b16 a[50:51], v45 offset:10496\n"
"s_waitcnt lgkmcnt(4)\n"
"s_waitcnt vmcnt(16)\n"
"ds_read_b64_tr_b16 a[76:77], v44 offset:12288\n"
"ds_read_b64_tr_b16 a[78:79], v44 offset:12544\n"
"ds_read_b64_tr_b16 a[104:105], v45 offset:12288\n"
"ds_read_b64_tr_b16 a[106:107], v45 offset:12544\n"
"s_waitcnt lgkmcnt(4)\n"
"s_waitcnt vmcnt(14)\n"
"ds_read_b64_tr_b16 a[132:133], v44 offset:14336\n"
"ds_read_b64_tr_b16 a[134:135], v44 offset:14592\n"
"ds_read_b64_tr_b16 a[160:161], v45 offset:14336\n"
"ds_read_b64_tr_b16 a[162:163], v45 offset:14592\n"
"s_waitcnt lgkmcnt(4)\n"
"s_waitcnt vmcnt(12)\n"
"ds_read_b64_tr_b16 a[188:189], v44 offset:16384\n"
"ds_read_b64_tr_b16 a[190:191], v44 offset:16640\n"
"ds_read_b64_tr_b16 a[216:217], v45 offset:16384\n"
"ds_read_b64_tr_b16 a[218:219], v45 offset:16640\n"
"s_waitcnt lgkmcnt(4)\n"
"s_waitcnt vmcnt(10)\n"
"ds_read_b64_tr_b16 a[244:245], v44 offset:18432\n"
"ds_read_b64_tr_b16 a[246:247], v44 offset:18688\n"
"ds_read_b64_tr_b16 v[36:37], v45 offset:18432\n"
"ds_read_b64_tr_b16 v[38:39], v45 offset:18688\n"
"s_waitcnt lgkmcnt(4)\n"
"s_waitcnt vmcnt(8)\n"
"ds_read_b64_tr_b16 a[24:25], v44 offset:20480\n"
"ds_read_b64_tr_b16 a[26:27], v44 offset:20736\n"
"ds_read_b64_tr_b16 a[52:53], v45 offset:20480\n"
"ds_read_b64_tr_b16 a[54:55], v45 offset:20736\n"
"s_waitcnt lgkmcnt(4)\n"
"s_waitcnt vmcnt(6)\n"
"ds_read_b64_tr_b16 a[80:81], v44 offset:22528\n"
"ds_read_b64_tr_b16 a[82:83], v44 offset:22784\n"
"ds_read_b64_tr_b16 a[108:109], v45 offset:22528\n"
"ds_read_b64_tr_b16 a[110:111], v45 offset:22784\n"
"s_waitcnt lgkmcnt(4)\n"
"s_waitcnt vmcnt(4)\n"
"ds_read_b64_tr_b16 a[136:137], v44 offset:24576\n"
"ds_read_b64_tr_b16 a[138:139], v44 offset:24832\n"
"ds_read_b64_tr_b16 a[164:165], v45 offset:24576\n"
"ds_read_b64_tr_b16 a[166:167], v45 offset:24832\n"
"s_waitcnt lgkmcnt(4)\n"
"s_waitcnt vmcnt(2)\n"
"ds_read_b64_tr_b16 a[192:193], v44 offset:26624\n"
"ds_read_b64_tr_b16 a[194:195], v44 offset:26880\n"
"ds_read_b64_tr_b16 a[220:221], v45 offset:26624\n"
"ds_read_b64_tr_b16 a[222:223], v45 offset:26880\n"
"s_waitcnt lgkmcnt(4)\n"
"s_waitcnt vmcnt(0)\n"
"ds_read_b64_tr_b16 a[248:249], v44 offset:28672\n"
"ds_read_b64_tr_b16 a[250:251], v44 offset:28928\n"
"ds_read_b64_tr_b16 v[40:41], v45 offset:28672\n"
"ds_read_b64_tr_b16 v[42:43], v45 offset:28928\n"
"s_waitcnt lgkmcnt(4)\n"
"s_waitcnt vmcnt(0)\n"
"s_waitcnt lgkmcnt(0)\n"
"s_mov_b32 m0, s59\n"
"s_barrier\n"
"s_mov_b32 s93, 0x80000000\n"
"s_mov_b32 s95, 0x80000000\n"
"s_mov_b32 s95, 0x80000000\n"
"s_mov_b32 s94, 0x80000000\n"
"s_mov_b32 s95, 0x80000000\n"
"s_mov_b32 s94, 0x80000000\n"
"s_mov_b32 s94, 0x80000000\n"
"s_mov_b32 s95, 0x80000000\n"
"v_mov_b32 v136, 0\n"
"v_mov_b32 v137, 0\n"
"v_mov_b32 v136, 0\n"
"v_mov_b32 v137, 0\n"
"v_mov_b32 v140, 0\n"
"v_mov_b32 v141, 0\n"
"v_mov_b32 v168, 0\n"
"v_mov_b32 v169, 0\n"
"s_cmp_eq_u32 s54, 0\n"
"s_cbranch_scc1 .LG_1s\n"
"s_cmp_eq_u32 s54, 1\n"
"s_cbranch_scc1 .LG_2s\n"
"s_cmp_eq_u32 s54, 2\n"
"s_cbranch_scc1 .LG_3s\n"
"s_cmp_eq_u32 s54, 3\n"
"s_cbranch_scc1 .LG_4s\n"
".p2align 6\n"
".LG_top:\n"
"v_mfma_f32_16x16x32_bf16 v[56:59], v[100:103], a[0:3], 0\n"
"v_mfma_f32_16x16x32_bf16 v[56:59], v[104:107], a[4:7], v[56:59]\n"
"v_mfma_f32_16x16x32_bf16 v[56:59], v[64:67], a[8:11], v[56:59]\n"
"v_mfma_f32_16x16x32_bf16 v[56:59], v[68:71], a[12:15], v[56:59]\n"
"s_waitcnt vmcnt(6)\n"
"global_load_dwordx4 v[44:47], v8, s[56:57] offset:0 sc1\n"
"v_mfma_f32_16x16x32_bf16 v[56:59], v[72:75], a[16:19], v[56:59]\n"
"s_waitcnt lgkmcnt(3)\n"
"v_add_f32 v92, v92, v48\n"
"v_add_f32 v93, v93, v49\n"
"v_mfma_f32_16x16x32_bf16 v[56:59], v[76:79], a[20:23], v[56:59]\n"
"s_waitcnt vmcnt(5)\n"
"v_add_f32 v94, v94, v50\n"
"v_add_f32 v95, v95, v51\n"
"v_mfma_f32_16x16x32_bf16 v[56:59], v[80:83], a[24:27], v[56:59]\n"
"buffer_load_dwordx2 v[140:141], v12, s[40:43], s50 offen sc1\n"
"v_mfma_f32_16x16x32_bf16 v[60:63], v[100:103], a[28:31], 0\n"
"global_load_dwordx4 v[48:51], v9, s[56:57] offset:0 sc1\n"
"v_add_f32 v92, v92, v84\n"
"v_mfma_f32_16x16x32_bf16 v[60:63], v[104:107], a[32:35], v[60:63]\n"
"ds_write_addtid_b32 v56 offset:0\n"
"v_add_f32 v93, v93, v85\n"
"v_mfma_f32_16x16x32_bf16 v[60:63], v[64:67], a[36:39], v[60:63]\n"
"ds_write_addtid_b32 v57 offset:4640\n"
"v_add_f32 v94, v94, v86\n"
"v_mfma_f32_16x16x32_bf16 v[60:63], v[68:71], a[40:43], v[60:63]\n"
"ds_write_addtid_b32 v58 offset:9344\n"
"v_add_f32 v95, v95, v87\n"
"v_mfma_f32_16x16x32_bf16 v[60:63], v[72:75], a[44:47], v[60:63]\n"
"ds_write_addtid_b32 v59 offset:13984\n"
"v_add_f32 v92, v92, v120\n"
"v_mfma_f32_16x16x32_bf16 v[60:63], v[76:79], a[48:51], v[60:63]\n"
"global_load_dwordx4 v[52:55], v10, s[56:57] offset:0 sc1\n"
"v_add_f32 v93, v93, v121\n"
"v_mfma_f32_16x16x32_bf16 v[60:63], v[80:83], a[52:55], v[60:63]\n"
"s_waitcnt vmcnt(7)\n"
"buffer_store_dwordx2 v[0:1], v14, s[40:43], s95 offen offset:0 sc1\n"
"v_add_f32 v94, v94, v122\n"
"v_mfma_f32_16x16x32_bf16 v[84:87], v[100:103], a[56:59], 0\n"
"v_add_f32 v95, v95, v123\n"
"v_mfma_f32_16x16x32_bf16 v[84:87], v[104:107], a[60:63], v[84:87]\n"
"ds_write_addtid_b32 v60 offset:256\n"
"global_load_dwordx4 v[56:59], v11, s[56:57] offset:0 sc1\n"
"v_mfma_f32_16x16x32_bf16 v[84:87], v[64:67], a[64:67], v[84:87]\n"
"s_add_u32 s94, s50, 0xffd00000\n"
"s_and_b32 s95, s94, 0xfbffffff\n"
"ds_write_addtid_b32 v61 offset:4896\n"
"v_mfma_f32_16x16x32_bf16 v[84:87], v[68:71], a[68:71], v[84:87]\n"
"ds_write_addtid_b32 v62 offset:9600\n"
"v_cvt_pk_bf16_f32 v88, v92, v93\n"
"v_mfma_f32_16x16x32_bf16 v[84:87], v[72:75], a[72:75], v[84:87]\n"
"ds_write_addtid_b32 v63 offset:14240\n"
"v_cvt_pk_bf16_f32 v89, v94, v95\n"
"v_mfma_f32_16x16x32_bf16 v[84:87], v[76:79], a[76:79], v[84:87]\n"
"s_waitcnt lgkmcnt(8)\n"
"v_add_f32 v128, v128, v132\n"
"v_add_f32 v129, v129, v133\n"
"v_mfma_f32_16x16x32_bf16 v[84:87], v[80:83], a[80:83], v[84:87]\n"
"v_add_f32 v130, v130, v134\n"
"v_add_f32 v131, v131, v135\n"
"v_mfma_f32_16x16x32_bf16 v[60:63], v[100:103], a[84:87], 0\n"
"v_add_f32 v128, v128, v136\n"
"v_add_f32 v129, v129, v137\n"
"v_mfma_f32_16x16x32_bf16 v[60:63], v[104:107], a[88:91], v[60:63]\n"
"ds_write_addtid_b32 v84 offset:512\n"
"v_add_f32 v130, v130, v138\n"
"v_mfma_f32_16x16x32_bf16 v[60:63], v[64:67], a[92:95], v[60:63]\n"
"ds_write_addtid_b32 v85 offset:5152\n"
"v_add_f32 v131, v131, v139\n"
"v_mfma_f32_16x16x32_bf16 v[60:63], v[68:71], a[96:99], v[60:63]\n"
"ds_write_addtid_b32 v86 offset:9856\n"
"v_add_f32 v128, v128, v124\n"
"v_mfma_f32_16x16x32_bf16 v[60:63], v[72:75], a[100:103], v[60:63]\n"
"ds_write_addtid_b32 v87 offset:14496\n"
"v_add_f32 v129, v129, v125\n"
"v_mfma_f32_16x16x32_bf16 v[60:63], v[76:79], a[104:107], v[60:63]\n"
"v_add_f32 v130, v130, v126\n"
"v_add_f32 v131, v131, v127\n"
"v_mfma_f32_16x16x32_bf16 v[60:63], v[80:83], a[108:111], v[60:63]\n"
"v_cvt_pk_bf16_f32 v90, v128, v129\n"
"v_cvt_pk_bf16_f32 v91, v130, v131\n"
"v_mfma_f32_16x16x32_bf16 v[84:87], v[100:103], a[112:115], 0\n"
"v_mfma_f32_16x16x32_bf16 v[84:87], v[104:107], a[116:119], v[84:87]\n"
"ds_write_addtid_b32 v60 offset:768\n"
"v_mfma_f32_16x16x32_bf16 v[84:87], v[64:67], a[120:123], v[84:87]\n"
"ds_write_addtid_b32 v61 offset:5408\n"
"v_mfma_f32_16x16x32_bf16 v[84:87], v[68:71], a[124:127], v[84:87]\n"
"ds_write_addtid_b32 v62 offset:10112\n"
"v_mfma_f32_16x16x32_bf16 v[84:87], v[72:75], a[128:131], v[84:87]\n"
"ds_write_addtid_b32 v63 offset:14752\n"
"v_mfma_f32_16x16x32_bf16 v[84:87], v[76:79], a[132:135], v[84:87]\n"
"v_mfma_f32_16x16x32_bf16 v[84:87], v[80:83], a[136:139], v[84:87]\n"
"v_mfma_f32_16x16x32_bf16 v[92:95], v[100:103], a[140:143], 0\n"
"v_mfma_f32_16x16x32_bf16 v[92:95], v[104:107], a[144:147], v[92:95]\n"
"ds_write_addtid_b32 v84 offset:1024\n"
"v_mfma_f32_16x16x32_bf16 v[92:95], v[64:67], a[148:151], v[92:95]\n"
"ds_write_addtid_b32 v85 offset:5664\n"
"v_mfma_f32_16x16x32_bf16 v[92:95], v[68:71], a[152:155], v[92:95]\n"
"ds_write_addtid_b32 v86 offset:10368\n"
"v_mfma_f32_16x16x32_bf16 v[92:95], v[72:75], a[156:159], v[92:95]\n"
"buffer_store_dwordx4 v[88:91], v13, s[40:43], s95 offen sc1\n"
"ds_write_addtid_b32 v87 offset:15008\n"
"v_mfma_f32_16x16x32_bf16 v[92:95], v[76:79], a[160:163], v[92:95]\n"
"v_mfma_f32_16x16x32_bf16 v[92:95], v[80:83], a[164:167], v[92:95]\n"
"v_mfma_f32_16x16x32_bf16 v[88:91], a[168:171], v[100:103], 0\n"
"v_mfma_f32_16x16x32_bf16 v[88:91], a[172:175], v[104:107], v[88:91]\n"
"ds_write_addtid_b32 v92 offset:1280\n"
"v_mfma_f32_16x16x32_bf16 v[88:91], a[176:179], v[64:67], v[88:91]\n"
"ds_write_addtid_b32 v93 offset:5920\n"
"v_mfma_f32_16x16x32_bf16 v[88:91], a[180:183], v[68:71], v[88:91]\n"
"ds_write_addtid_b32 v94 offset:10624\n"
"v_mfma_f32_16x16x32_bf16 v[88:91], a[184:187], v[72:75], v[88:91]\n"
"ds_write_addtid_b32 v95 offset:15264\n"
"v_mfma_f32_16x16x32_bf16 v[88:91], a[188:191], v[76:79], v[88:91]\n"
"v_mfma_f32_16x16x32_bf16 v[88:91], a[192:195], v[80:83], v[88:91]\n"
"v_mfma_f32_16x16x32_bf16 v[96:99], a[196:199], v[100:103], 0\n"
"v_mfma_f32_16x16x32_bf16 v[96:99], a[200:203], v[104:107], v[96:99]\n"
"v_mfma_f32_16x16x32_bf16 v[96:99], a[204:207], v[64:67], v[96:99]\n"
"v_mfma_f32_16x16x32_bf16 v[96:99], a[208:211], v[68:71], v[96:99]\n"
"s_add_u32 s58, s50, 0x180000\n"
"v_mfma_f32_16x16x32_bf16 v[96:99], a[212:215], v[72:75], v[96:99]\n"
"s_and_b32 s58, s58, 0x3ffffff\n"
"v_mfma_f32_16x16x32_bf16 v[96:99], a[216:219], v[76:79], v[96:99]\n"
"s_add_u32 s56, s40, s58\n"
"v_mfma_f32_16x16x32_bf16 v[96:99], a[220:223], v[80:83], v[96:99]\n"
"s_addc_u32 s57, s41, 0\n"
"v_mfma_f32_16x16x32_bf16 v[92:95], a[224:227], v[100:103], 0\n"
"s_waitcnt vmcnt(5)\n"
"v_cmp_ne_u64 vcc, s[48:49], v[140:141]\n"
"v_mfma_f32_16x16x32_bf16 v[92:95], a[228:231], v[104:107], v[92:95]\n"
"v_mfma_f32_16x16x32_bf16 v[92:95], a[232:235], v[64:67], v[92:95]\n"
"s_cbranch_vccnz .LG_3s\n"
".LG_3r:\n"
"v_mfma_f32_16x16x32_bf16 v[92:95], a[236:239], v[68:71], v[92:95]\n"
"s_add_u32 s50, s50, 0x180000\n"
"global_load_dwordx4 v[60:63], v5, s[56:57] offset:0 sc1\n"
"v_mfma_f32_16x16x32_bf16 v[92:95], a[240:243], v[72:75], v[92:95]\n"
"v_mfma_f32_16x16x32_bf16 v[92:95], a[244:247], v[76:79], v[92:95]\n"
"v_mfma_f32_16x16x32_bf16 v[92:95], a[248:251], v[80:83], v[92:95]\n"
"v_mfma_f32_16x16x32_bf16 v[120:123], a[252:255], v[100:103], 0\n"
"v_mfma_f32_16x16x32_bf16 v[120:123], v[20:23], v[104:107], v[120:123]\n"
"s_waitcnt lgkmcnt(0)\n"
"v_mfma_f32_16x16x32_bf16 v[120:123], v[24:27], v[64:67], v[120:123]\n"
"s_barrier\n"
"v_mfma_f32_16x16x32_bf16 v[120:123], v[28:31], v[68:71], v[120:123]\n"
"ds_read_b128 v[100:103], v2 offset:2048\n"
"ds_read_b128 v[84:87], v3 offset:512\n"
"v_mfma_f32_16x16x32_bf16 v[120:123], v[32:35], v[72:75], v[120:123]\n"
"global_load_dwordx4 v[64:67], v6, s[56:57] offset:0 sc1\n"
"ds_read_b128 v[104:107], v4 offset:3072\n"
"v_mfma_f32_16x16x32_bf16 v[120:123], v[36:39], v[76:79], v[120:123]\n"
"ds_read_b128 v[124:127], v2 offset:2304\n"
"ds_read_b128 v[128:131], v3 offset:768\n"
"v_mfma_f32_16x16x32_bf16 v[120:123], v[40:43], v[80:83], v[120:123]\n"
"ds_read_b128 v[132:135], v4 offset:3328\n"
"ds_read_b128 v[136:139], v2 offset:3584\n"
"v_mfma_f32_16x16x32_bf16 v[140:143], v[108:111], a[0:3], 0\n"
"global_load_dwordx4 v[68:71], v7, s[56:57] offset:0 sc1\n"
"ds_read_b128 v[144:147], v3 offset:1536\n"
"v_mfma_f32_16x16x32_bf16 v[140:143], v[112:115], a[4:7], v[140:143]\n"
"ds_read_b128 v[148:151], v4 offset:3584\n"
"ds_read_b128 v[152:155], v2 offset:3840\n"
"v_mfma_f32_16x16x32_bf16 v[140:143], v[116:119], a[8:11], v[140:143]\n"
"ds_read_b128 v[156:159], v3 offset:1792\n"
"ds_read_b128 v[160:163], v4 offset:3840\n"
"v_mfma_f32_16x16x32_bf16 v[140:143], v[44:47], a[12:15], v[140:143]\n"
"s_waitcnt vmcnt(6)\n"
"v_mfma_f32_16x16x32_bf16 v[140:143], v[48:51], a[16:19], v[140:143]\n"
"s_waitcnt lgkmcnt(9)\n"
"global_load_dwordx4 v[72:75], v8, s[56:57] offset:0 sc1\n"
"v_add_f32 v100, v100, v84\n"
"v_mfma_f32_16x16x32_bf16 v[140:143], v[52:55], a[20:23], v[140:143]\n"
"s_waitcnt vmcnt(5)\n"
"v_add_f32 v101, v101, v85\n"
"v_add_f32 v102, v102, v86\n"
"v_mfma_f32_16x16x32_bf16 v[140:143], v[56:59], a[24:27], v[140:143]\n"
"v_add_f32 v103, v103, v87\n"
"v_add_f32 v100, v100, v104\n"
"v_mfma_f32_16x16x32_bf16 v[164:167], v[108:111], a[28:31], 0\n"
"buffer_load_dwordx2 v[168:169], v12, s[40:43], s50 offen sc1\n"
"v_mfma_f32_16x16x32_bf16 v[164:167], v[112:115], a[32:35], v[164:167]\n"
"ds_write_addtid_b32 v140 offset:2048\n"
"global_load_dwordx4 v[76:79], v9, s[56:57] offset:0 sc1\n"
"v_mfma_f32_16x16x32_bf16 v[164:167], v[116:119], a[36:39], v[164:167]\n"
"ds_write_addtid_b32 v141 offset:6688\n"
"v_add_f32 v101, v101, v105\n"
"v_mfma_f32_16x16x32_bf16 v[164:167], v[44:47], a[40:43], v[164:167]\n"
"ds_write_addtid_b32 v142 offset:11392\n"
"global_load_dwordx4 v[80:83], v10, s[56:57] offset:0 sc1\n"
"v_mfma_f32_16x16x32_bf16 v[164:167], v[48:51], a[44:47], v[164:167]\n"
"ds_write_addtid_b32 v143 offset:16032\n"
"v_add_f32 v102, v102, v106\n"
"v_mfma_f32_16x16x32_bf16 v[164:167], v[52:55], a[48:51], v[164:167]\n"
"v_add_f32 v103, v103, v107\n"
"v_add_f32 v100, v100, v88\n"
"v_mfma_f32_16x16x32_bf16 v[164:167], v[56:59], a[52:55], v[164:167]\n"
"v_add_f32 v101, v101, v89\n"
"v_add_f32 v102, v102, v90\n"
"v_mfma_f32_16x16x32_bf16 v[104:107], v[108:111], a[56:59], 0\n"
"v_add_f32 v103, v103, v91\n"
"v_mfma_f32_16x16x32_bf16 v[104:107], v[112:115], a[60:63], v[104:107]\n"
"s_add_u32 s93, s50, 0xffd00000\n"
"s_and_b32 s95, s93, 0xfbffffff\n"
"ds_write_addtid_b32 v164 offset:2304\n"
"v_mfma_f32_16x16x32_bf16 v[104:107], v[116:119], a[64:67], v[104:107]\n"
"ds_write_addtid_b32 v165 offset:6944\n"
"v_cvt_pk_bf16_f32 v88, v100, v101\n"
"v_mfma_f32_16x16x32_bf16 v[104:107], v[44:47], a[68:71], v[104:107]\n"
"s_waitcnt vmcnt(7)\n"
"ds_write_addtid_b32 v166 offset:11648\n"
"global_load_dwordx4 v[84:87], v11, s[56:57] offset:0 sc1\n"
"v_mfma_f32_16x16x32_bf16 v[104:107], v[48:51], a[72:75], v[104:107]\n"
"ds_write_addtid_b32 v167 offset:16288\n"
"v_cvt_pk_bf16_f32 v89, v102, v103\n"
"v_mfma_f32_16x16x32_bf16 v[104:107], v[52:55], a[76:79], v[104:107]\n"
"s_waitcnt lgkmcnt(14)\n"
"v_add_f32 v124, v124, v128\n"
"v_add_f32 v125, v125, v129\n"
"v_mfma_f32_16x16x32_bf16 v[104:107], v[56:59], a[80:83], v[104:107]\n"
"v_add_f32 v126, v126, v130\n"
"v_add_f32 v127, v127, v131\n"
"v_mfma_f32_16x16x32_bf16 v[100:103], v[108:111], a[84:87], 0\n"
"v_add_f32 v124, v124, v132\n"
"v_add_f32 v125, v125, v133\n"
"v_mfma_f32_16x16x32_bf16 v[100:103], v[112:115], a[88:91], v[100:103]\n"
"ds_write_addtid_b32 v104 offset:2560\n"
"v_add_f32 v126, v126, v134\n"
"v_mfma_f32_16x16x32_bf16 v[100:103], v[116:119], a[92:95], v[100:103]\n"
"ds_write_addtid_b32 v105 offset:7200\n"
"v_add_f32 v127, v127, v135\n"
"v_mfma_f32_16x16x32_bf16 v[100:103], v[44:47], a[96:99], v[100:103]\n"
"ds_write_addtid_b32 v106 offset:11904\n"
"v_add_f32 v124, v124, v96\n"
"v_mfma_f32_16x16x32_bf16 v[100:103], v[48:51], a[100:103], v[100:103]\n"
"ds_write_addtid_b32 v107 offset:16544\n"
"v_add_f32 v125, v125, v97\n"
"v_mfma_f32_16x16x32_bf16 v[100:103], v[52:55], a[104:107], v[100:103]\n"
"v_add_f32 v126, v126, v98\n"
"v_add_f32 v127, v127, v99\n"
"v_mfma_f32_16x16x32_bf16 v[100:103], v[56:59], a[108:111], v[100:103]\n"
"v_cvt_pk_bf16_f32 v90, v124, v125\n"
"v_cvt_pk_bf16_f32 v91, v126, v127\n"
"v_mfma_f32_16x16x32_bf16 v[96:99], v[108:111], a[112:115], 0\n"
"buffer_store_dwordx4 v[88:91], v13, s[40:43], s95 offen sc1\n"
"v_mfma_f32_16x16x32_bf16 v[96:99], v[112:115], a[116:119], v[96:99]\n"
"s_waitcnt lgkmcnt(15)\n"
"ds_write_addtid_b32 v100 offset:2816\n"
"v_add_f32 v136, v136, v144\n"
"v_mfma_f32_16x16x32_bf16 v[96:99], v[116:119], a[120:123], v[96:99]\n"
"ds_write_addtid_b32 v101 offset:7456\n"
"v_add_f32 v137, v137, v145\n"
"v_mfma_f32_16x16x32_bf16 v[96:99], v[44:47], a[124:127], v[96:99]\n"
"ds_write_addtid_b32 v102 offset:12160\n"
"v_add_f32 v138, v138, v146\n"
"v_mfma_f32_16x16x32_bf16 v[96:99], v[48:51], a[128:131], v[96:99]\n"
"ds_write_addtid_b32 v103 offset:16800\n"
"v_add_f32 v139, v139, v147\n"
"v_mfma_f32_16x16x32_bf16 v[96:99], v[52:55], a[132:135], v[96:99]\n"
"v_add_f32 v136, v136, v148\n"
"v_add_f32 v137, v137, v149\n"
"v_mfma_f32_16x16x32_bf16 v[96:99], v[56:59], a[136:139], v[96:99]\n"
"v_add_f32 v138, v138, v150\n"
"v_add_f32 v139, v139, v151\n"
"v_mfma_f32_16x16x32_bf16 v[88:91], v[108:111], a[140:143], 0\n"
"v_add_f32 v136, v136, v92\n"
"v_add_f32 v137, v137, v93\n"
"v_mfma_f32_16x16x32_bf16 v[88:91], v[112:115], a[144:147], v[88:91]\n"
"ds_write_addtid_b32 v96 offset:3072\n"
"v_add_f32 v138, v138, v94\n"
"v_mfma_f32_16x16x32_bf16 v[88:91], v[116:119], a[148:151], v[88:91]\n"
"ds_write_addtid_b32 v97 offset:7712\n"
"v_add_f32 v139, v139, v95\n"
"v_mfma_f32_16x16x32_bf16 v[88:91], v[44:47], a[152:155], v[88:91]\n"
"ds_write_addtid_b32 v98 offset:12416\n"
"v_cvt_pk_bf16_f32 v100, v136, v137\n"
"v_mfma_f32_16x16x32_bf16 v[88:91], v[48:51], a[156:159], v[88:91]\n"
"ds_write_addtid_b32 v99 offset:17056\n"
"v_cvt_pk_bf16_f32 v101, v138, v139\n"
"v_mfma_f32_16x16x32_bf16 v[88:91], v[52:55], a[160:163], v[88:91]\n"
"s_waitcnt lgkmcnt(15)\n"
"v_add_f32 v152, v152, v156\n"
"v_add_f32 v153, v153, v157\n"
"v_mfma_f32_16x16x32_bf16 v[88:91], v[56:59], a[164:167], v[88:91]\n"
"v_add_f32 v154, v154, v158\n"
"v_add_f32 v155, v155, v159\n"
"v_mfma_f32_16x16x32_bf16 v[92:95], v[108:111], a[224:227], 0\n"
"v_add_f32 v152, v152, v160\n"
"v_add_f32 v153, v153, v161\n"
"v_mfma_f32_16x16x32_bf16 v[92:95], v[112:115], a[228:231], v[92:95]\n"
"ds_write_addtid_b32 v88 offset:3328\n"
"v_add_f32 v154, v154, v162\n"
"v_mfma_f32_16x16x32_bf16 v[92:95], v[116:119], a[232:235], v[92:95]\n"
"ds_write_addtid_b32 v89 offset:7968\n"
"v_add_f32 v155, v155, v163\n"
"v_mfma_f32_16x16x32_bf16 v[92:95], v[44:47], a[236:239], v[92:95]\n"
"ds_write_addtid_b32 v90 offset:12672\n"
"v_add_f32 v152, v152, v120\n"
"v_mfma_f32_16x16x32_bf16 v[92:95], v[48:51], a[240:243], v[92:95]\n"
"ds_write_addtid_b32 v91 offset:17312\n"
"v_add_f32 v153, v153, v121\n"
"v_mfma_f32_16x16x32_bf16 v[92:95], v[52:55], a[244:247], v[92:95]\n"
"v_add_f32 v154, v154, v122\n"
"v_add_f32 v155, v155, v123\n"
"v_mfma_f32_16x16x32_bf16 v[92:95], v[56:59], a[248:251], v[92:95]\n"
"v_cvt_pk_bf16_f32 v102, v152, v153\n"
"v_cvt_pk_bf16_f32 v103, v154, v155\n"
"v_mfma_f32_16x16x32_bf16 v[88:91], v[108:111], a[252:255], 0\n"
"buffer_store_dwordx4 v[100:103], v15, s[40:43], s95 offen sc1\n"
"v_mfma_f32_16x16x32_bf16 v[88:91], v[112:115], v[20:23], v[88:91]\n"
"buffer_store_dwordx2 v[0:1], v14, s[40:43], s94 offen offset:0 sc1\n"
"ds_write_addtid_b32 v92 offset:3584\n"
"v_mfma_f32_16x16x32_bf16 v[88:91], v[116:119], v[24:27], v[88:91]\n"
"ds_write_addtid_b32 v93 offset:8224\n"
"v_mfma_f32_16x16x32_bf16 v[88:91], v[44:47], v[28:31], v[88:91]\n"
"ds_write_addtid_b32 v94 offset:12928\n"
"v_mfma_f32_16x16x32_bf16 v[88:91], v[48:51], v[32:35], v[88:91]\n"
"ds_write_addtid_b32 v95 offset:17568\n"
"v_mfma_f32_16x16x32_bf16 v[88:91], v[52:55], v[36:39], v[88:91]\n"
"v_mfma_f32_16x16x32_bf16 v[88:91], v[56:59], v[40:43], v[88:91]\n"
"s_add_u32 s58, s50, 0x180000\n"
"v_mfma_f32_16x16x32_bf16 v[96:99], a[168:171], v[108:111], 0\n"
"s_and_b32 s58, s58, 0x3ffffff\n"
"v_mfma_f32_16x16x32_bf16 v[96:99], a[172:175], v[112:115], v[96:99]\n"
"s_add_u32 s56, s40, s58\n"
"ds_write_addtid_b32 v88 offset:3840\n"
"v_mfma_f32_16x16x32_bf16 v[96:99], a[176:179], v[116:119], v[96:99]\n"
"s_addc_u32 s57, s41, 0\n"
"ds_write_addtid_b32 v89 offset:8480\n"
"v_mfma_f32_16x16x32_bf16 v[96:99], a[180:183], v[44:47], v[96:99]\n"
"s_waitcnt vmcnt(6)\n"
"ds_write_addtid_b32 v90 offset:13184\n"
"v_cmp_ne_u64 vcc, s[48:49], v[168:169]\n"
"v_mfma_f32_16x16x32_bf16 v[96:99], a[184:187], v[48:51], v[96:99]\n"
"ds_write_addtid_b32 v91 offset:17824\n"
"v_mfma_f32_16x16x32_bf16 v[96:99], a[188:191], v[52:55], v[96:99]\n"
"s_cbranch_vccnz .LG_4s\n"
".LG_4r:\n"
"v_mfma_f32_16x16x32_bf16 v[96:99], a[192:195], v[56:59], v[96:99]\n"
"s_add_u32 s50, s50, 0x180000\n"
"global_load_dwordx4 v[88:91], v5, s[56:57] offset:0 sc1\n"
"v_mfma_f32_16x16x32_bf16 v[100:103], a[196:199], v[108:111], 0\n"
"v_mfma_f32_16x16x32_bf16 v[100:103], a[200:203], v[112:115], v[100:103]\n"
"s_waitcnt lgkmcnt(0)\n"
"global_load_dwordx4 v[92:95], v6, s[56:57] offset:0 sc1\n"
"v_mfma_f32_16x16x32_bf16 v[100:103], a[204:207], v[116:119], v[100:103]\n"
"s_barrier\n"
"v_mfma_f32_16x16x32_bf16 v[100:103], a[208:211], v[44:47], v[100:103]\n"
"ds_read_b128 v[104:107], v2 offset:0\n"
"ds_read_b128 v[108:111], v3 offset:2560\n"
"v_mfma_f32_16x16x32_bf16 v[100:103], a[212:215], v[48:51], v[100:103]\n"
"ds_read_b128 v[112:115], v4 offset:1024\n"
"ds_read_b128 v[116:119], v2 offset:256\n"
"v_mfma_f32_16x16x32_bf16 v[100:103], a[216:219], v[52:55], v[100:103]\n"
"ds_read_b128 v[120:123], v3 offset:2816\n"
"ds_read_b128 v[124:127], v4 offset:1280\n"
"v_mfma_f32_16x16x32_bf16 v[100:103], a[220:223], v[56:59], v[100:103]\n"
"v_mfma_f32_16x16x32_bf16 v[128:131], v[60:63], a[0:3], 0\n"
"v_mfma_f32_16x16x32_bf16 v[128:131], v[64:67], a[4:7], v[128:131]\n"
"global_load_dwordx4 v[44:47], v7, s[56:57] offset:0 sc1\n"
"v_mfma_f32_16x16x32_bf16 v[128:131], v[68:71], a[8:11], v[128:131]\n"
"v_mfma_f32_16x16x32_bf16 v[128:131], v[72:75], a[12:15], v[128:131]\n"
"s_waitcnt vmcnt(7)\n"
"v_mfma_f32_16x16x32_bf16 v[128:131], v[76:79], a[16:19], v[128:131]\n"
"s_waitcnt lgkmcnt(3)\n"
"v_add_f32 v104, v104, v108\n"
"v_add_f32 v105, v105, v109\n"
"v_mfma_f32_16x16x32_bf16 v[128:131], v[80:83], a[20:23], v[128:131]\n"
"s_waitcnt vmcnt(6)\n"
"global_load_dwordx4 v[48:51], v8, s[56:57] offset:0 sc1\n"
"v_add_f32 v106, v106, v110\n"
"v_mfma_f32_16x16x32_bf16 v[128:131], v[84:87], a[24:27], v[128:131]\n"
"buffer_load_dwordx2 v[136:137], v12, s[40:43], s50 offen sc1\n"
"v_mfma_f32_16x16x32_bf16 v[132:135], v[60:63], a[28:31], 0\n"
"v_add_f32 v107, v107, v111\n"
"v_add_f32 v104, v104, v112\n"
"v_mfma_f32_16x16x32_bf16 v[132:135], v[64:67], a[32:35], v[132:135]\n"
"ds_write_addtid_b32 v128 offset:0\n"
"v_add_f32 v105, v105, v113\n"
"v_mfma_f32_16x16x32_bf16 v[132:135], v[68:71], a[36:39], v[132:135]\n"
"ds_write_addtid_b32 v129 offset:4640\n"
"global_load_dwordx4 v[52:55], v9, s[56:57] offset:0 sc1\n"
"v_mfma_f32_16x16x32_bf16 v[132:135], v[72:75], a[40:43], v[132:135]\n"
"ds_write_addtid_b32 v130 offset:9344\n"
"v_add_f32 v106, v106, v114\n"
"v_mfma_f32_16x16x32_bf16 v[132:135], v[76:79], a[44:47], v[132:135]\n"
"ds_write_addtid_b32 v131 offset:13984\n"
"global_load_dwordx4 v[56:59], v10, s[56:57] offset:0 sc1\n"
"v_mfma_f32_16x16x32_bf16 v[132:135], v[80:83], a[48:51], v[132:135]\n"
"v_add_f32 v107, v107, v115\n"
"v_add_f32 v104, v104, v96\n"
"v_mfma_f32_16x16x32_bf16 v[132:135], v[84:87], a[52:55], v[132:135]\n"
"v_add_f32 v105, v105, v97\n"
"v_add_f32 v106, v106, v98\n"
"v_mfma_f32_16x16x32_bf16 v[108:111], v[60:63], a[56:59], 0\n"
"v_add_f32 v107, v107, v99\n"
"v_mfma_f32_16x16x32_bf16 v[108:111], v[64:67], a[60:63], v[108:111]\n"
"s_add_u32 s95, s50, 0xffd00000\n"
"s_and_b32 s94, s95, 0xfbffffff\n"
"ds_write_addtid_b32 v132 offset:256\n"
"v_mfma_f32_16x16x32_bf16 v[108:111], v[68:71], a[64:67], v[108:111]\n"
"ds_write_addtid_b32 v133 offset:4896\n"
"global_load_dwordx4 v[96:99], v11, s[56:57] offset:0 sc1\n"
"v_mfma_f32_16x16x32_bf16 v[108:111], v[72:75], a[68:71], v[108:111]\n"
"ds_write_addtid_b32 v134 offset:9600\n"
"v_cvt_pk_bf16_f32 v112, v104, v105\n"
"v_mfma_f32_16x16x32_bf16 v[108:111], v[76:79], a[72:75], v[108:111]\n"
"ds_write_addtid_b32 v135 offset:14240\n"
"v_cvt_pk_bf16_f32 v113, v106, v107\n"
"v_mfma_f32_16x16x32_bf16 v[108:111], v[80:83], a[76:79], v[108:111]\n"
"s_waitcnt lgkmcnt(8)\n"
"v_add_f32 v116, v116, v120\n"
"v_add_f32 v117, v117, v121\n"
"v_mfma_f32_16x16x32_bf16 v[108:111], v[84:87], a[80:83], v[108:111]\n"
"v_add_f32 v118, v118, v122\n"
"v_add_f32 v119, v119, v123\n"
"v_mfma_f32_16x16x32_bf16 v[104:107], v[60:63], a[84:87], 0\n"
"v_add_f32 v116, v116, v124\n"
"v_add_f32 v117, v117, v125\n"
"v_mfma_f32_16x16x32_bf16 v[104:107], v[64:67], a[88:91], v[104:107]\n"
"ds_write_addtid_b32 v108 offset:512\n"
"v_add_f32 v118, v118, v126\n"
"v_mfma_f32_16x16x32_bf16 v[104:107], v[68:71], a[92:95], v[104:107]\n"
"ds_write_addtid_b32 v109 offset:5152\n"
"v_add_f32 v119, v119, v127\n"
"v_mfma_f32_16x16x32_bf16 v[104:107], v[72:75], a[96:99], v[104:107]\n"
"ds_write_addtid_b32 v110 offset:9856\n"
"v_add_f32 v116, v116, v100\n"
"v_mfma_f32_16x16x32_bf16 v[104:107], v[76:79], a[100:103], v[104:107]\n"
"ds_write_addtid_b32 v111 offset:14496\n"
"v_add_f32 v117, v117, v101\n"
"v_mfma_f32_16x16x32_bf16 v[104:107], v[80:83], a[104:107], v[104:107]\n"
"v_add_f32 v118, v118, v102\n"
"v_add_f32 v119, v119, v103\n"
"v_mfma_f32_16x16x32_bf16 v[104:107], v[84:87], a[108:111], v[104:107]\n"
"s_waitcnt vmcnt(9)\n"
"buffer_store_dwordx2 v[0:1], v16, s[40:43], s93 offen offset:0 sc1\n"
"v_cvt_pk_bf16_f32 v114, v116, v117\n"
"v_mfma_f32_16x16x32_bf16 v[100:103], v[60:63], a[112:115], 0\n"
"v_cvt_pk_bf16_f32 v115, v118, v119\n"
"v_mfma_f32_16x16x32_bf16 v[100:103], v[64:67], a[116:119], v[100:103]\n"
"ds_write_addtid_b32 v104 offset:768\n"
"v_mfma_f32_16x16x32_bf16 v[100:103], v[68:71], a[120:123], v[100:103]\n"
"ds_write_addtid_b32 v105 offset:5408\n"
"v_mfma_f32_16x16x32_bf16 v[100:103], v[72:75], a[124:127], v[100:103]\n"
"ds_write_addtid_b32 v106 offset:10112\n"
"v_mfma_f32_16x16x32_bf16 v[100:103], v[76:79], a[128:131], v[100:103]\n"
"buffer_store_dwordx4 v[112:115], v13, s[40:43], s94 offen sc1\n"
"ds_write_addtid_b32 v107 offset:14752\n"
"v_mfma_f32_16x16x32_bf16 v[100:103], v[80:83], a[132:135], v[100:103]\n"
"v_mfma_f32_16x16x32_bf16 v[100:103], v[84:87], a[136:139], v[100:103]\n"
"v_mfma_f32_16x16x32_bf16 v[108:111], v[60:63], a[140:143], 0\n"
"v_mfma_f32_16x16x32_bf16 v[108:111], v[64:67], a[144:147], v[108:111]\n"
"ds_write_addtid_b32 v100 offset:1024\n"
"v_mfma_f32_16x16x32_bf16 v[108:111], v[68:71], a[148:151], v[108:111]\n"
"ds_write_addtid_b32 v101 offset:5664\n"
"v_mfma_f32_16x16x32_bf16 v[108:111], v[72:75], a[152:155], v[108:111]\n"
"ds_write_addtid_b32 v102 offset:10368\n"
"v_mfma_f32_16x16x32_bf16 v[108:111], v[76:79], a[156:159], v[108:111]\n"
"ds_write_addtid_b32 v103 offset:15008\n"
"v_mfma_f32_16x16x32_bf16 v[108:111], v[80:83], a[160:163], v[108:111]\n"
"v_mfma_f32_16x16x32_bf16 v[108:111], v[84:87], a[164:167], v[108:111]\n"
"v_mfma_f32_16x16x32_bf16 v[104:107], v[60:63], a[224:227], 0\n"
"v_mfma_f32_16x16x32_bf16 v[104:107], v[64:67], a[228:231], v[104:107]\n"
"ds_write_addtid_b32 v108 offset:1280\n"
"v_mfma_f32_16x16x32_bf16 v[104:107], v[68:71], a[232:235], v[104:107]\n"
"ds_write_addtid_b32 v109 offset:5920\n"
"v_mfma_f32_16x16x32_bf16 v[104:107], v[72:75], a[236:239], v[104:107]\n"
"ds_write_addtid_b32 v110 offset:10624\n"
"v_mfma_f32_16x16x32_bf16 v[104:107], v[76:79], a[240:243], v[104:107]\n"
"ds_write_addtid_b32 v111 offset:15264\n"
"v_mfma_f32_16x16x32_bf16 v[104:107], v[80:83], a[244:247], v[104:107]\n"
"v_mfma_f32_16x16x32_bf16 v[104:107], v[84:87], a[248:251], v[104:107]\n"
"v_mfma_f32_16x16x32_bf16 v[112:115], v[60:63], a[252:255], 0\n"
"v_mfma_f32_16x16x32_bf16 v[112:115], v[64:67], v[20:23], v[112:115]\n"
"s_add_u32 s58, s50, 0x180000\n"
"ds_write_addtid_b32 v104 offset:1536\n"
"v_mfma_f32_16x16x32_bf16 v[112:115], v[68:71], v[24:27], v[112:115]\n"
"s_and_b32 s58, s58, 0x3ffffff\n"
"ds_write_addtid_b32 v105 offset:6176\n"
"v_mfma_f32_16x16x32_bf16 v[112:115], v[72:75], v[28:31], v[112:115]\n"
"s_add_u32 s56, s40, s58\n"
"ds_write_addtid_b32 v106 offset:10880\n"
"v_mfma_f32_16x16x32_bf16 v[112:115], v[76:79], v[32:35], v[112:115]\n"
"s_addc_u32 s57, s41, 0\n"
"ds_write_addtid_b32 v107 offset:15520\n"
"v_mfma_f32_16x16x32_bf16 v[112:115], v[80:83], v[36:39], v[112:115]\n"
"s_waitcnt vmcnt(5)\n"
"v_cmp_ne_u64 vcc, s[48:49], v[136:137]\n"
"v_mfma_f32_16x16x32_bf16 v[112:115], v[84:87], v[40:43], v[112:115]\n"
"v_mfma_f32_16x16x32_bf16 v[108:111], a[168:171], v[60:63], 0\n"
"s_cbranch_vccnz .LG_1s\n"
".LG_1r:\n"
"v_mfma_f32_16x16x32_bf16 v[108:111], a[172:175], v[64:67], v[108:111]\n"
"s_add_u32 s50, s50, 0x180000\n"
"ds_write_addtid_b32 v112 offset:1792\n"
"v_mfma_f32_16x16x32_bf16 v[108:111], a[176:179], v[68:71], v[108:111]\n"
"ds_write_addtid_b32 v113 offset:6432\n"
"v_mfma_f32_16x16x32_bf16 v[108:111], a[180:183], v[72:75], v[108:111]\n"
"ds_write_addtid_b32 v114 offset:11136\n"
"v_mfma_f32_16x16x32_bf16 v[108:111], a[184:187], v[76:79], v[108:111]\n"
"ds_write_addtid_b32 v115 offset:15776\n"
"global_load_dwordx4 v[100:103], v5, s[56:57] offset:0 sc1\n"
"v_mfma_f32_16x16x32_bf16 v[108:111], a[188:191], v[80:83], v[108:111]\n"
"v_mfma_f32_16x16x32_bf16 v[108:111], a[192:195], v[84:87], v[108:111]\n"
"v_mfma_f32_16x16x32_bf16 v[112:115], a[196:199], v[60:63], 0\n"
"v_mfma_f32_16x16x32_bf16 v[112:115], a[200:203], v[64:67], v[112:115]\n"
"s_waitcnt lgkmcnt(0)\n"
"global_load_dwordx4 v[104:107], v6, s[56:57] offset:0 sc1\n"
"v_mfma_f32_16x16x32_bf16 v[112:115], a[204:207], v[68:71], v[112:115]\n"
"s_barrier\n"
"v_mfma_f32_16x16x32_bf16 v[112:115], a[208:211], v[72:75], v[112:115]\n"
"ds_read_b128 v[60:63], v2 offset:2048\n"
"ds_read_b128 v[116:119], v3 offset:512\n"
"v_mfma_f32_16x16x32_bf16 v[112:115], a[212:215], v[76:79], v[112:115]\n"
"ds_read_b128 v[120:123], v4 offset:3072\n"
"ds_read_b128 v[124:127], v2 offset:2304\n"
"v_mfma_f32_16x16x32_bf16 v[112:115], a[216:219], v[80:83], v[112:115]\n"
"global_load_dwordx4 v[64:67], v7, s[56:57] offset:0 sc1\n"
"ds_read_b128 v[128:131], v3 offset:768\n"
"v_mfma_f32_16x16x32_bf16 v[112:115], a[220:223], v[84:87], v[112:115]\n"
"ds_read_b128 v[132:135], v4 offset:3328\n"
"v_mfma_f32_16x16x32_bf16 v[80:83], v[88:91], a[0:3], 0\n"
"v_mfma_f32_16x16x32_bf16 v[80:83], v[92:95], a[4:7], v[80:83]\n"
"v_mfma_f32_16x16x32_bf16 v[80:83], v[44:47], a[8:11], v[80:83]\n"
"global_load_dwordx4 v[68:71], v8, s[56:57] offset:0 sc1\n"
"v_mfma_f32_16x16x32_bf16 v[80:83], v[48:51], a[12:15], v[80:83]\n"
"s_waitcnt vmcnt(7)\n"
"v_mfma_f32_16x16x32_bf16 v[80:83], v[52:55], a[16:19], v[80:83]\n"
"s_waitcnt lgkmcnt(3)\n"
"v_add_f32 v60, v60, v116\n"
"v_add_f32 v61, v61, v117\n"
"v_mfma_f32_16x16x32_bf16 v[80:83], v[56:59], a[20:23], v[80:83]\n"
"s_waitcnt vmcnt(6)\n"
"buffer_load_dwordx2 v[136:137], v12, s[40:43], s50 offen sc1\n"
"v_mfma_f32_16x16x32_bf16 v[80:83], v[96:99], a[24:27], v[80:83]\n"
"global_load_dwordx4 v[72:75], v9, s[56:57] offset:0 sc1\n"
"v_add_f32 v62, v62, v118\n"
"v_mfma_f32_16x16x32_bf16 v[84:87], v[88:91], a[28:31], 0\n"
"v_add_f32 v63, v63, v119\n"
"v_add_f32 v60, v60, v120\n"
"v_mfma_f32_16x16x32_bf16 v[84:87], v[92:95], a[32:35], v[84:87]\n"
"ds_write_addtid_b32 v80 offset:2048\n"
"v_add_f32 v61, v61, v121\n"
"v_mfma_f32_16x16x32_bf16 v[84:87], v[44:47], a[36:39], v[84:87]\n"
"ds_write_addtid_b32 v81 offset:6688\n"
"v_add_f32 v62, v62, v122\n"
"v_mfma_f32_16x16x32_bf16 v[84:87], v[48:51], a[40:43], v[84:87]\n"
"s_waitcnt vmcnt(6)\n"
"ds_write_addtid_b32 v82 offset:11392\n"
"global_load_dwordx4 v[76:79], v10, s[56:57] offset:0 sc1\n"
"v_mfma_f32_16x16x32_bf16 v[84:87], v[52:55], a[44:47], v[84:87]\n"
"buffer_store_dwordx2 v[0:1], v14, s[40:43], s95 offen offset:0 sc1\n"
"ds_write_addtid_b32 v83 offset:16032\n"
"v_mfma_f32_16x16x32_bf16 v[84:87], v[56:59], a[48:51], v[84:87]\n"
"v_add_f32 v63, v63, v123\n"
"v_add_f32 v60, v60, v108\n"
"v_mfma_f32_16x16x32_bf16 v[84:87], v[96:99], a[52:55], v[84:87]\n"
"v_add_f32 v61, v61, v109\n"
"v_add_f32 v62, v62, v110\n"
"v_mfma_f32_16x16x32_bf16 v[116:119], v[88:91], a[56:59], 0\n"
"global_load_dwordx4 v[80:83], v11, s[56:57] offset:0 sc1\n"
"v_add_f32 v63, v63, v111\n"
"v_mfma_f32_16x16x32_bf16 v[116:119], v[92:95], a[60:63], v[116:119]\n"
"s_add_u32 s95, s50, 0xffd00000\n"
"s_and_b32 s94, s95, 0xfbffffff\n"
"ds_write_addtid_b32 v84 offset:2304\n"
"v_mfma_f32_16x16x32_bf16 v[116:119], v[44:47], a[64:67], v[116:119]\n"
"ds_write_addtid_b32 v85 offset:6944\n"
"v_cvt_pk_bf16_f32 v108, v60, v61\n"
"v_mfma_f32_16x16x32_bf16 v[116:119], v[48:51], a[68:71], v[116:119]\n"
"ds_write_addtid_b32 v86 offset:11648\n"
"v_cvt_pk_bf16_f32 v109, v62, v63\n"
"v_mfma_f32_16x16x32_bf16 v[116:119], v[52:55], a[72:75], v[116:119]\n"
"s_waitcnt lgkmcnt(7)\n"
"ds_write_addtid_b32 v87 offset:16288\n"
"v_add_f32 v124, v124, v128\n"
"v_mfma_f32_16x16x32_bf16 v[116:119], v[56:59], a[76:79], v[116:119]\n"
"v_add_f32 v125, v125, v129\n"
"v_add_f32 v126, v126, v130\n"
"v_mfma_f32_16x16x32_bf16 v[116:119], v[96:99], a[80:83], v[116:119]\n"
"v_add_f32 v127, v127, v131\n"
"v_add_f32 v124, v124, v132\n"
"v_mfma_f32_16x16x32_bf16 v[60:63], v[88:91], a[84:87], 0\n"
"v_add_f32 v125, v125, v133\n"
"v_add_f32 v126, v126, v134\n"
"v_mfma_f32_16x16x32_bf16 v[60:63], v[92:95], a[88:91], v[60:63]\n"
"ds_write_addtid_b32 v116 offset:2560\n"
"v_add_f32 v127, v127, v135\n"
"v_mfma_f32_16x16x32_bf16 v[60:63], v[44:47], a[92:95], v[60:63]\n"
"ds_write_addtid_b32 v117 offset:7200\n"
"v_add_f32 v124, v124, v112\n"
"v_mfma_f32_16x16x32_bf16 v[60:63], v[48:51], a[96:99], v[60:63]\n"
"ds_write_addtid_b32 v118 offset:11904\n"
"v_add_f32 v125, v125, v113\n"
"v_mfma_f32_16x16x32_bf16 v[60:63], v[52:55], a[100:103], v[60:63]\n"
"ds_write_addtid_b32 v119 offset:16544\n"
"v_add_f32 v126, v126, v114\n"
"v_mfma_f32_16x16x32_bf16 v[60:63], v[56:59], a[104:107], v[60:63]\n"
"v_add_f32 v127, v127, v115\n"
"v_cvt_pk_bf16_f32 v110, v124, v125\n"
"v_mfma_f32_16x16x32_bf16 v[60:63], v[96:99], a[108:111], v[60:63]\n"
"v_cvt_pk_bf16_f32 v111, v126, v127\n"
"v_mfma_f32_16x16x32_bf16 v[84:87], v[88:91], a[112:115], 0\n"
"v_mfma_f32_16x16x32_bf16 v[84:87], v[92:95], a[116:119], v[84:87]\n"
"ds_write_addtid_b32 v60 offset:2816\n"
"v_mfma_f32_16x16x32_bf16 v[84:87], v[44:47], a[120:123], v[84:87]\n"
"ds_write_addtid_b32 v61 offset:7456\n"
"v_mfma_f32_16x16x32_bf16 v[84:87], v[48:51], a[124:127], v[84:87]\n"
"ds_write_addtid_b32 v62 offset:12160\n"
"v_mfma_f32_16x16x32_bf16 v[84:87], v[52:55], a[128:131], v[84:87]\n"
"ds_write_addtid_b32 v63 offset:16800\n"
"v_mfma_f32_16x16x32_bf16 v[84:87], v[56:59], a[132:135], v[84:87]\n"
"v_mfma_f32_16x16x32_bf16 v[84:87], v[96:99], a[136:139], v[84:87]\n"
"v_mfma_f32_16x16x32_bf16 v[112:115], v[88:91], a[140:143], 0\n"
"buffer_store_dwordx4 v[108:111], v13, s[40:43], s94 offen sc1\n"
"v_mfma_f32_16x16x32_bf16 v[112:115], v[92:95], a[144:147], v[112:115]\n"
"ds_write_addtid_b32 v84 offset:3072\n"
"v_mfma_f32_16x16x32_bf16 v[112:115], v[44:47], a[148:151], v[112:115]\n"
"ds_write_addtid_b32 v85 offset:7712\n"
"v_mfma_f32_16x16x32_bf16 v[112:115], v[48:51], a[152:155], v[112:115]\n"
"ds_write_addtid_b32 v86 offset:12416\n"
"v_mfma_f32_16x16x32_bf16 v[112:115], v[52:55], a[156:159], v[112:115]\n"
"ds_write_addtid_b32 v87 offset:17056\n"
"v_mfma_f32_16x16x32_bf16 v[112:115], v[56:59], a[160:163], v[112:115]\n"
"v_mfma_f32_16x16x32_bf16 v[112:115], v[96:99], a[164:167], v[112:115]\n"
"v_mfma_f32_16x16x32_bf16 v[60:63], v[88:91], a[224:227], 0\n"
"v_mfma_f32_16x16x32_bf16 v[60:63], v[92:95], a[228:231], v[60:63]\n"
"ds_write_addtid_b32 v112 offset:3328\n"
"v_mfma_f32_16x16x32_bf16 v[60:63], v[44:47], a[232:235], v[60:63]\n"
"ds_write_addtid_b32 v113 offset:7968\n"
"v_mfma_f32_16x16x32_bf16 v[60:63], v[48:51], a[236:239], v[60:63]\n"
"ds_write_addtid_b32 v114 offset:12672\n"
"v_mfma_f32_16x16x32_bf16 v[60:63], v[52:55], a[240:243], v[60:63]\n"
"ds_write_addtid_b32 v115 offset:17312\n"
"v_mfma_f32_16x16x32_bf16 v[60:63], v[56:59], a[244:247], v[60:63]\n"
"v_mfma_f32_16x16x32_bf16 v[60:63], v[96:99], a[248:251], v[60:63]\n"
"v_mfma_f32_16x16x32_bf16 v[84:87], v[88:91], a[252:255], 0\n"
"v_mfma_f32_16x16x32_bf16 v[84:87], v[92:95], v[20:23], v[84:87]\n"
"s_add_u32 s58, s50, 0x180000\n"
"ds_write_addtid_b32 v60 offset:3584\n"
"v_mfma_f32_16x16x32_bf16 v[84:87], v[44:47], v[24:27], v[84:87]\n"
"s_and_b32 s58, s58, 0x3ffffff\n"
"ds_write_addtid_b32 v61 offset:8224\n"
"v_mfma_f32_16x16x32_bf16 v[84:87], v[48:51], v[28:31], v[84:87]\n"
"s_add_u32 s56, s40, s58\n"
"ds_write_addtid_b32 v62 offset:12928\n"
"v_mfma_f32_16x16x32_bf16 v[84:87], v[52:55], v[32:35], v[84:87]\n"
"s_addc_u32 s57, s41, 0\n"
"ds_write_addtid_b32 v63 offset:17568\n"
"v_mfma_f32_16x16x32_bf16 v[84:87], v[56:59], v[36:39], v[84:87]\n"
"s_waitcnt vmcnt(5)\n"
"v_cmp_ne_u64 vcc, s[48:49], v[136:137]\n"
"v_mfma_f32_16x16x32_bf16 v[84:87], v[96:99], v[40:43], v[84:87]\n"
"v_mfma_f32_16x16x32_bf16 v[120:123], a[168:171], v[88:91], 0\n"
"s_cbranch_vccnz .LG_2s\n"
".LG_2r:\n"
"v_mfma_f32_16x16x32_bf16 v[120:123], a[172:175], v[92:95], v[120:123]\n"
"s_add_u32 s50, s50, 0x180000\n"
"ds_write_addtid_b32 v84 offset:3840\n"
"v_mfma_f32_16x16x32_bf16 v[120:123], a[176:179], v[44:47], v[120:123]\n"
"ds_write_addtid_b32 v85 offset:8480\n"
"v_mfma_f32_16x16x32_bf16 v[120:123], a[180:183], v[48:51], v[120:123]\n"
"ds_write_addtid_b32 v86 offset:13184\n"
"v_mfma_f32_16x16x32_bf16 v[120:123], a[184:187], v[52:55], v[120:123]\n"
"ds_write_addtid_b32 v87 offset:17824\n"
"v_mfma_f32_16x16x32_bf16 v[120:123], a[188:191], v[56:59], v[120:123]\n"
"global_load_dwordx4 v[108:111], v5, s[56:57] offset:0 sc1\n"
"v_mfma_f32_16x16x32_bf16 v[120:123], a[192:195], v[96:99], v[120:123]\n"
"v_mfma_f32_16x16x32_bf16 v[124:127], a[196:199], v[88:91], 0\n"
"v_mfma_f32_16x16x32_bf16 v[124:127], a[200:203], v[92:95], v[124:127]\n"
"s_waitcnt lgkmcnt(0)\n"
"v_mfma_f32_16x16x32_bf16 v[124:127], a[204:207], v[44:47], v[124:127]\n"
"s_barrier\n"
"v_mfma_f32_16x16x32_bf16 v[124:127], a[208:211], v[48:51], v[124:127]\n"
"global_load_dwordx4 v[112:115], v6, s[56:57] offset:0 sc1\n"
"ds_read_b128 v[92:95], v2 offset:0\n"
"v_mfma_f32_16x16x32_bf16 v[124:127], a[212:215], v[52:55], v[124:127]\n"
"ds_read_b128 v[48:51], v3 offset:2560\n"
"ds_read_b128 v[84:87], v4 offset:1024\n"
"v_mfma_f32_16x16x32_bf16 v[124:127], a[216:219], v[56:59], v[124:127]\n"
"ds_read_b128 v[128:131], v2 offset:256\n"
"ds_read_b128 v[132:135], v3 offset:2816\n"
"v_mfma_f32_16x16x32_bf16 v[124:127], a[220:223], v[96:99], v[124:127]\n"
"global_load_dwordx4 v[116:119], v7, s[56:57] offset:0 sc1\n"
"ds_read_b128 v[136:139], v4 offset:1280\n"
"s_branch .LG_top\n"
".LG_1s:\n"
"s_cmp_ge_u32 s50, s53\n"
"s_cbranch_scc1 .LG_t0\n"
"s_cmp_ge_u32 s50, s52\n"
"s_cbranch_scc1 .LG_1r\n"
"s_sub_u32 s60, s50, 0x0\n"
"s_mov_b32 s61, 200000\n"
"s_nop 7\n"
".LG_1p:\n"
"buffer_load_dwordx2 v[136:137], v12, s[40:43], s60 offen sc1\n"
"s_waitcnt vmcnt(0)\n"
"v_cmp_ne_u64 vcc, s[48:49], v[136:137]\n"
"s_cbranch_vccz .LG_1px\n"
"s_sleep 1\n"
"s_sub_u32 s61, s61, 1\n"
"s_cbranch_scc0 .LG_1p\n"
"s_branch .LG_end\n"
".LG_1px:\n"
"s_branch .LG_1r\n"
".LG_2s:\n"
"s_cmp_ge_u32 s50, s53\n"
"s_cbranch_scc1 .LG_t1\n"
"s_cmp_ge_u32 s50, s52\n"
"s_cbranch_scc1 .LG_2r\n"
"s_sub_u32 s60, s50, 0x0\n"
"s_mov_b32 s61, 200000\n"
"s_nop 7\n"
".LG_2p:\n"
"buffer_load_dwordx2 v[136:137], v12, s[40:43], s60 offen sc1\n"
"s_waitcnt vmcnt(0)\n"
"v_cmp_ne_u64 vcc, s[48:49], v[136:137]\n"
"s_cbranch_vccz .LG_2px\n"
"s_sleep 1\n"
"s_sub_u32 s61, s61, 1\n"
"s_cbranch_scc0 .LG_2p\n"
"s_branch .LG_end\n"
".LG_2px:\n"
"s_branch .LG_2r\n"
".LG_3s:\n"
"s_cmp_ge_u32 s50, s53\n"
"s_cbranch_scc1 .LG_t2\n"
"s_cmp_ge_u32 s50, s52\n"
"s_cbranch_scc1 .LG_3r\n"
"s_sub_u32 s60, s50, 0x0\n"
"s_mov_b32 s61, 200000\n"
"s_nop 7\n"
".LG_3p:\n"
"buffer_load_dwordx2 v[140:141], v12, s[40:43], s60 offen sc1\n"
"s_waitcnt vmcnt(0)\n"
"v_cmp_ne_u64 vcc, s[48:49], v[140:141]\n"
"s_cbranch_vccz .LG_3px\n"
"s_sleep 1\n"
"s_sub_u32 s61, s61, 1\n"
"s_cbranch_scc0 .LG_3p\n"
"s_branch .LG_end\n"
".LG_3px:\n"
"s_branch .LG_3r\n"
".LG_4s:\n"
"s_cmp_ge_u32 s50, s53\n"
"s_cbranch_scc1 .LG_t3\n"
"s_cmp_ge_u32 s50, s52\n"
"s_cbranch_scc1 .LG_4r\n"
"s_sub_u32 s60, s50, 0x0\n"
"s_mov_b32 s61, 200000\n"
"s_nop 7\n"
".LG_4p:\n"
"buffer_load_dwordx2 v[168:169], v12, s[40:43], s60 offen sc1\n"
"s_waitcnt vmcnt(0)\n"
"v_cmp_ne_u64 vcc, s[48:49], v[168:169]\n"
"s_cbranch_vccz .LG_4px\n"
"s_sleep 1\n"
"s_sub_u32 s61, s61, 1\n"
"s_cbranch_scc0 .LG_4p\n"
"s_branch .LG_end\n"
".LG_4px:\n"
"s_branch .LG_4r\n"
".LG_t0:\n"
"v_mfma_f32_16x16x32_bf16 v[108:111], a[172:175], v[64:67], v[108:111]\n"
"ds_write_addtid_b32 v112 offset:1792\n"
"v_mfma_f32_16x16x32_bf16 v[108:111], a[176:179], v[68:71], v[108:111]\n"
"ds_write_addtid_b32 v113 offset:6432\n"
"v_mfma_f32_16x16x32_bf16 v[108:111], a[180:183], v[72:75], v[108:111]\n"
"ds_write_addtid_b32 v114 offset:11136\n"
"v_mfma_f32_16x16x32_bf16 v[108:111], a[184:187], v[76:79], v[108:111]\n"
"ds_write_addtid_b32 v115 offset:15776\n"
"v_mfma_f32_16x16x32_bf16 v[108:111], a[188:191], v[80:83], v[108:111]\n"
"v_mfma_f32_16x16x32_bf16 v[108:111], a[192:195], v[84:87], v[108:111]\n"
"v_mfma_f32_16x16x32_bf16 v[112:115], a[196:199], v[60:63], 0\n"
"v_mfma_f32_16x16x32_bf16 v[112:115], a[200:203], v[64:67], v[112:115]\n"
"s_waitcnt lgkmcnt(0)\n"
"v_mfma_f32_16x16x32_bf16 v[112:115], a[204:207], v[68:71], v[112:115]\n"
"s_barrier\n"
"v_mfma_f32_16x16x32_bf16 v[112:115], a[208:211], v[72:75], v[112:115]\n"
"ds_read_b128 v[60:63], v2 offset:2048\n"
"ds_read_b128 v[116:119], v3 offset:512\n"
"v_mfma_f32_16x16x32_bf16 v[112:115], a[212:215], v[76:79], v[112:115]\n"
"ds_read_b128 v[120:123], v4 offset:3072\n"
"ds_read_b128 v[124:127], v2 offset:2304\n"
"v_mfma_f32_16x16x32_bf16 v[112:115], a[216:219], v[80:83], v[112:115]\n"
"ds_read_b128 v[128:131], v3 offset:768\n"
"v_mfma_f32_16x16x32_bf16 v[112:115], a[220:223], v[84:87], v[112:115]\n"
"s_waitcnt vmcnt(8)\n"
"ds_read_b128 v[132:135], v4 offset:3328\n"
"v_mfma_f32_16x16x32_bf16 v[80:83], v[88:91], a[0:3], 0\n"
"v_mfma_f32_16x16x32_bf16 v[80:83], v[92:95], a[4:7], v[80:83]\n"
"s_waitcnt vmcnt(6)\n"
"v_mfma_f32_16x16x32_bf16 v[80:83], v[44:47], a[8:11], v[80:83]\n"
"v_mfma_f32_16x16x32_bf16 v[80:83], v[48:51], a[12:15], v[80:83]\n"
"s_waitcnt vmcnt(3)\n"
"v_mfma_f32_16x16x32_bf16 v[80:83], v[52:55], a[16:19], v[80:83]\n"
"s_waitcnt lgkmcnt(3)\n"
"v_add_f32 v60, v60, v116\n"
"v_add_f32 v61, v61, v117\n"
"v_mfma_f32_16x16x32_bf16 v[80:83], v[56:59], a[20:23], v[80:83]\n"
"s_waitcnt vmcnt(2)\n"
"v_mfma_f32_16x16x32_bf16 v[80:83], v[96:99], a[24:27], v[80:83]\n"
"v_add_f32 v62, v62, v118\n"
"v_mfma_f32_16x16x32_bf16 v[84:87], v[88:91], a[28:31], 0\n"
"v_add_f32 v63, v63, v119\n"
"v_add_f32 v60, v60, v120\n"
"v_mfma_f32_16x16x32_bf16 v[84:87], v[92:95], a[32:35], v[84:87]\n"
"ds_write_addtid_b32 v80 offset:2048\n"
"v_add_f32 v61, v61, v121\n"
"v_mfma_f32_16x16x32_bf16 v[84:87], v[44:47], a[36:39], v[84:87]\n"
"ds_write_addtid_b32 v81 offset:6688\n"
"v_add_f32 v62, v62, v122\n"
"v_mfma_f32_16x16x32_bf16 v[84:87], v[48:51], a[40:43], v[84:87]\n"
"s_waitcnt vmcnt(0)\n"
"ds_write_addtid_b32 v82 offset:11392\n"
"v_mfma_f32_16x16x32_bf16 v[84:87], v[52:55], a[44:47], v[84:87]\n"
"buffer_store_dwordx2 v[0:1], v14, s[40:43], s95 offen offset:0 sc1\n"
"ds_write_addtid_b32 v83 offset:16032\n"
"v_mfma_f32_16x16x32_bf16 v[84:87], v[56:59], a[48:51], v[84:87]\n"
"v_add_f32 v63, v63, v123\n"
"v_add_f32 v60, v60, v108\n"
"v_mfma_f32_16x16x32_bf16 v[84:87], v[96:99], a[52:55], v[84:87]\n"
"v_add_f32 v61, v61, v109\n"
"v_add_f32 v62, v62, v110\n"
"v_mfma_f32_16x16x32_bf16 v[116:119], v[88:91], a[56:59], 0\n"
"v_add_f32 v63, v63, v111\n"
"v_mfma_f32_16x16x32_bf16 v[116:119], v[92:95], a[60:63], v[116:119]\n"
"s_add_u32 s95, s50, 0xffe80000\n"
"s_and_b32 s94, s95, 0xfbffffff\n"
"ds_write_addtid_b32 v84 offset:2304\n"
"v_mfma_f32_16x16x32_bf16 v[116:119], v[44:47], a[64:67], v[116:119]\n"
"ds_write_addtid_b32 v85 offset:6944\n"
"v_cvt_pk_bf16_f32 v108, v60, v61\n"
"v_mfma_f32_16x16x32_bf16 v[116:119], v[48:51], a[68:71], v[116:119]\n"
"ds_write_addtid_b32 v86 offset:11648\n"
"v_cvt_pk_bf16_f32 v109, v62, v63\n"
"v_mfma_f32_16x16x32_bf16 v[116:119], v[52:55], a[72:75], v[116:119]\n"
"s_waitcnt lgkmcnt(7)\n"
"ds_write_addtid_b32 v87 offset:16288\n"
"v_add_f32 v124, v124, v128\n"
"v_mfma_f32_16x16x32_bf16 v[116:119], v[56:59], a[76:79], v[116:119]\n"
"v_add_f32 v125, v125, v129\n"
"v_add_f32 v126, v126, v130\n"
"v_mfma_f32_16x16x32_bf16 v[116:119], v[96:99], a[80:83], v[116:119]\n"
"v_add_f32 v127, v127, v131\n"
"v_add_f32 v124, v124, v132\n"
"v_mfma_f32_16x16x32_bf16 v[60:63], v[88:91], a[84:87], 0\n"
"v_add_f32 v125, v125, v133\n"
"v_add_f32 v126, v126, v134\n"
"v_mfma_f32_16x16x32_bf16 v[60:63], v[92:95], a[88:91], v[60:63]\n"
"ds_write_addtid_b32 v116 offset:2560\n"
"v_add_f32 v127, v127, v135\n"
"v_mfma_f32_16x16x32_bf16 v[60:63], v[44:47], a[92:95], v[60:63]\n"
"ds_write_addtid_b32 v117 offset:7200\n"
"v_add_f32 v124, v124, v112\n"
"v_mfma_f32_16x16x32_bf16 v[60:63], v[48:51], a[96:99], v[60:63]\n"
"ds_write_addtid_b32 v118 offset:11904\n"
"v_add_f32 v125, v125, v113\n"
"v_mfma_f32_16x16x32_bf16 v[60:63], v[52:55], a[100:103], v[60:63]\n"
"ds_write_addtid_b32 v119 offset:16544\n"
"v_add_f32 v126, v126, v114\n"
"v_mfma_f32_16x16x32_bf16 v[60:63], v[56:59], a[104:107], v[60:63]\n"
"v_add_f32 v127, v127, v115\n"
"v_cvt_pk_bf16_f32 v110, v124, v125\n"
"v_mfma_f32_16x16x32_bf16 v[60:63], v[96:99], a[108:111], v[60:63]\n"
"v_cvt_pk_bf16_f32 v111, v126, v127\n"
"v_mfma_f32_16x16x32_bf16 v[84:87], v[88:91], a[112:115], 0\n"
"v_mfma_f32_16x16x32_bf16 v[84:87], v[92:95], a[116:119], v[84:87]\n"
"ds_write_addtid_b32 v60 offset:2816\n"
"v_mfma_f32_16x16x32_bf16 v[84:87], v[44:47], a[120:123], v[84:87]\n"
"ds_write_addtid_b32 v61 offset:7456\n"
"v_mfma_f32_16x16x32_bf16 v[84:87], v[48:51], a[124:127], v[84:87]\n"
"ds_write_addtid_b32 v62 offset:12160\n"
"v_mfma_f32_16x16x32_bf16 v[84:87], v[52:55], a[128:131], v[84:87]\n"
"ds_write_addtid_b32 v63 offset:16800\n"
"v_mfma_f32_16x16x32_bf16 v[84:87], v[56:59], a[132:135], v[84:87]\n"
"v_mfma_f32_16x16x32_bf16 v[84:87], v[96:99], a[136:139], v[84:87]\n"
"v_mfma_f32_16x16x32_bf16 v[112:115], v[88:91], a[140:143], 0\n"
"buffer_store_dwordx4 v[108:111], v13, s[40:43], s94 offen sc1\n"
"v_mfma_f32_16x16x32_bf16 v[112:115], v[92:95], a[144:147], v[112:115]\n"
"ds_write_addtid_b32 v84 offset:3072\n"
"v_mfma_f32_16x16x32_bf16 v[112:115], v[44:47], a[148:151], v[112:115]\n"
"ds_write_addtid_b32 v85 offset:7712\n"
"v_mfma_f32_16x16x32_bf16 v[112:115], v[48:51], a[152:155], v[112:115]\n"
"ds_write_addtid_b32 v86 offset:12416\n"
"v_mfma_f32_16x16x32_bf16 v[112:115], v[52:55], a[156:159], v[112:115]\n"
"ds_write_addtid_b32 v87 offset:17056\n"
"v_mfma_f32_16x16x32_bf16 v[112:115], v[56:59], a[160:163], v[112:115]\n"
"v_mfma_f32_16x16x32_bf16 v[112:115], v[96:99], a[164:167], v[112:115]\n"
"v_mfma_f32_16x16x32_bf16 v[60:63], v[88:91], a[224:227], 0\n"
"v_mfma_f32_16x16x32_bf16 v[60:63], v[92:95], a[228:231], v[60:63]\n"
"ds_write_addtid_b32 v112 offset:3328\n"
"v_mfma_f32_16x16x32_bf16 v[60:63], v[44:47], a[232:235], v[60:63]\n"
"ds_write_addtid_b32 v113 offset:7968\n"
"v_mfma_f32_16x16x32_bf16 v[60:63], v[48:51], a[236:239], v[60:63]\n"
"ds_write_addtid_b32 v114 offset:12672\n"
"v_mfma_f32_16x16x32_bf16 v[60:63], v[52:55], a[240:243], v[60:63]\n"
"ds_write_addtid_b32 v115 offset:17312\n"
"v_mfma_f32_16x16x32_bf16 v[60:63], v[56:59], a[244:247], v[60:63]\n"
"v_mfma_f32_16x16x32_bf16 v[60:63], v[96:99], a[248:251], v[60:63]\n"
"v_mfma_f32_16x16x32_bf16 v[84:87], v[88:91], a[252:255], 0\n"
"v_mfma_f32_16x16x32_bf16 v[84:87], v[92:95], v[20:23], v[84:87]\n"
"ds_write_addtid_b32 v60 offset:3584\n"
"v_mfma_f32_16x16x32_bf16 v[84:87], v[44:47], v[24:27], v[84:87]\n"
"ds_write_addtid_b32 v61 offset:8224\n"
"v_mfma_f32_16x16x32_bf16 v[84:87], v[48:51], v[28:31], v[84:87]\n"
"ds_write_addtid_b32 v62 offset:12928\n"
"v_mfma_f32_16x16x32_bf16 v[84:87], v[52:55], v[32:35], v[84:87]\n"
"ds_write_addtid_b32 v63 offset:17568\n"
"v_mfma_f32_16x16x32_bf16 v[84:87], v[56:59], v[36:39], v[84:87]\n"
"v_mfma_f32_16x16x32_bf16 v[84:87], v[96:99], v[40:43], v[84:87]\n"
"v_mfma_f32_16x16x32_bf16 v[120:123], a[168:171], v[88:91], 0\n"
"v_mfma_f32_16x16x32_bf16 v[120:123], a[172:175], v[92:95], v[120:123]\n"
"ds_write_addtid_b32 v84 offset:3840\n"
"v_mfma_f32_16x16x32_bf16 v[120:123], a[176:179], v[44:47], v[120:123]\n"
"ds_write_addtid_b32 v85 offset:8480\n"
"v_mfma_f32_16x16x32_bf16 v[120:123], a[180:183], v[48:51], v[120:123]\n"
"ds_write_addtid_b32 v86 offset:13184\n"
"v_mfma_f32_16x16x32_bf16 v[120:123], a[184:187], v[52:55], v[120:123]\n"
"ds_write_addtid_b32 v87 offset:17824\n"
"v_mfma_f32_16x16x32_bf16 v[120:123], a[188:191], v[56:59], v[120:123]\n"
"v_mfma_f32_16x16x32_bf16 v[120:123], a[192:195], v[96:99], v[120:123]\n"
"v_mfma_f32_16x16x32_bf16 v[124:127], a[196:199], v[88:91], 0\n"
"v_mfma_f32_16x16x32_bf16 v[124:127], a[200:203], v[92:95], v[124:127]\n"
"s_waitcnt lgkmcnt(0)\n"
"v_mfma_f32_16x16x32_bf16 v[124:127], a[204:207], v[44:47], v[124:127]\n"
"s_barrier\n"
"v_mfma_f32_16x16x32_bf16 v[124:127], a[208:211], v[48:51], v[124:127]\n"
"ds_read_b128 v[92:95], v2 offset:0\n"
"v_mfma_f32_16x16x32_bf16 v[124:127], a[212:215], v[52:55], v[124:127]\n"
"ds_read_b128 v[48:51], v3 offset:2560\n"
"ds_read_b128 v[84:87], v4 offset:1024\n"
"v_mfma_f32_16x16x32_bf16 v[124:127], a[216:219], v[56:59], v[124:127]\n"
"ds_read_b128 v[128:131], v2 offset:256\n"
"ds_read_b128 v[132:135], v3 offset:2816\n"
"v_mfma_f32_16x16x32_bf16 v[124:127], a[220:223], v[96:99], v[124:127]\n"
"ds_read_b128 v[136:139], v4 offset:1280\n"
"s_waitcnt lgkmcnt(3)\n"
"v_add_f32 v92, v92, v48\n"
"v_add_f32 v93, v93, v49\n"
"v_add_f32 v94, v94, v50\n"
"v_add_f32 v95, v95, v51\n"
"v_add_f32 v92, v92, v84\n"
"v_add_f32 v93, v93, v85\n"
"v_add_f32 v94, v94, v86\n"
"v_add_f32 v95, v95, v87\n"
"v_add_f32 v92, v92, v120\n"
"v_add_f32 v93, v93, v121\n"
"s_waitcnt vmcnt(0)\n"
"buffer_store_dwordx2 v[0:1], v14, s[40:43], s95 offen offset:0 sc1\n"
"v_add_f32 v94, v94, v122\n"
"v_add_f32 v95, v95, v123\n"
"s_add_u32 s94, s50, 0x0\n"
"s_and_b32 s95, s94, 0xfbffffff\n"
"v_cvt_pk_bf16_f32 v88, v92, v93\n"
"v_cvt_pk_bf16_f32 v89, v94, v95\n"
"s_waitcnt lgkmcnt(0)\n"
"v_add_f32 v128, v128, v132\n"
"v_add_f32 v129, v129, v133\n"
"v_add_f32 v130, v130, v134\n"
"v_add_f32 v131, v131, v135\n"
"v_add_f32 v128, v128, v136\n"
"v_add_f32 v129, v129, v137\n"
"v_add_f32 v130, v130, v138\n"
"v_add_f32 v131, v131, v139\n"
"v_add_f32 v128, v128, v124\n"
"v_add_f32 v129, v129, v125\n"
"v_add_f32 v130, v130, v126\n"
"v_add_f32 v131, v131, v127\n"
"v_cvt_pk_bf16_f32 v90, v128, v129\n"
"v_cvt_pk_bf16_f32 v91, v130, v131\n"
"buffer_store_dwordx4 v[88:91], v13, s[40:43], s95 offen sc1\n"
"s_waitcnt vmcnt(0)\n"
"buffer_store_dwordx2 v[0:1], v14, s[40:43], s94 offen offset:0 sc1\n"
"s_branch .LG_end\n"
".LG_t1:\n"
"v_mfma_f32_16x16x32_bf16 v[120:123], a[172:175], v[92:95], v[120:123]\n"
"ds_write_addtid_b32 v84 offset:3840\n"
"v_mfma_f32_16x16x32_bf16 v[120:123], a[176:179], v[44:47], v[120:123]\n"
"ds_write_addtid_b32 v85 offset:8480\n"
"v_mfma_f32_16x16x32_bf16 v[120:123], a[180:183], v[48:51], v[120:123]\n"
"ds_write_addtid_b32 v86 offset:13184\n"
"v_mfma_f32_16x16x32_bf16 v[120:123], a[184:187], v[52:55], v[120:123]\n"
"ds_write_addtid_b32 v87 offset:17824\n"
"v_mfma_f32_16x16x32_bf16 v[120:123], a[188:191], v[56:59], v[120:123]\n"
"v_mfma_f32_16x16x32_bf16 v[120:123], a[192:195], v[96:99], v[120:123]\n"
"v_mfma_f32_16x16x32_bf16 v[124:127], a[196:199], v[88:91], 0\n"
"v_mfma_f32_16x16x32_bf16 v[124:127], a[200:203], v[92:95], v[124:127]\n"
"s_waitcnt lgkmcnt(0)\n"
"v_mfma_f32_16x16x32_bf16 v[124:127], a[204:207], v[44:47], v[124:127]\n"
"s_barrier\n"
"v_mfma_f32_16x16x32_bf16 v[124:127], a[208:211], v[48:51], v[124:127]\n"
"ds_read_b128 v[92:95], v2 offset:0\n"
"v_mfma_f32_16x16x32_bf16 v[124:127], a[212:215], v[52:55], v[124:127]\n"
"ds_read_b128 v[48:51], v3 offset:2560\n"
"ds_read_b128 v[84:87], v4 offset:1024\n"
"v_mfma_f32_16x16x32_bf16 v[124:127], a[216:219], v[56:59], v[124:127]\n"
"ds_read_b128 v[128:131], v2 offset:256\n"
"ds_read_b128 v[132:135], v3 offset:2816\n"
"v_mfma_f32_16x16x32_bf16 v[124:127], a[220:223], v[96:99], v[124:127]\n"
"s_waitcnt vmcnt(8)\n"
"ds_read_b128 v[136:139], v4 offset:1280\n"
"v_mfma_f32_16x16x32_bf16 v[56:59], v[100:103], a[0:3], 0\n"
"v_mfma_f32_16x16x32_bf16 v[56:59], v[104:107], a[4:7], v[56:59]\n"
"s_waitcnt vmcnt(6)\n"
"v_mfma_f32_16x16x32_bf16 v[56:59], v[64:67], a[8:11], v[56:59]\n"
"v_mfma_f32_16x16x32_bf16 v[56:59], v[68:71], a[12:15], v[56:59]\n"
"s_waitcnt vmcnt(3)\n"
"v_mfma_f32_16x16x32_bf16 v[56:59], v[72:75], a[16:19], v[56:59]\n"
"s_waitcnt lgkmcnt(3)\n"
"v_add_f32 v92, v92, v48\n"
"v_add_f32 v93, v93, v49\n"
"v_mfma_f32_16x16x32_bf16 v[56:59], v[76:79], a[20:23], v[56:59]\n"
"s_waitcnt vmcnt(1)\n"
"v_add_f32 v94, v94, v50\n"
"v_add_f32 v95, v95, v51\n"
"v_mfma_f32_16x16x32_bf16 v[56:59], v[80:83], a[24:27], v[56:59]\n"
"v_mfma_f32_16x16x32_bf16 v[60:63], v[100:103], a[28:31], 0\n"
"v_add_f32 v92, v92, v84\n"
"v_mfma_f32_16x16x32_bf16 v[60:63], v[104:107], a[32:35], v[60:63]\n"
"ds_write_addtid_b32 v56 offset:0\n"
"v_add_f32 v93, v93, v85\n"
"v_mfma_f32_16x16x32_bf16 v[60:63], v[64:67], a[36:39], v[60:63]\n"
"ds_write_addtid_b32 v57 offset:4640\n"
"v_add_f32 v94, v94, v86\n"
"v_mfma_f32_16x16x32_bf16 v[60:63], v[68:71], a[40:43], v[60:63]\n"
"ds_write_addtid_b32 v58 offset:9344\n"
"v_add_f32 v95, v95, v87\n"
"v_mfma_f32_16x16x32_bf16 v[60:63], v[72:75], a[44:47], v[60:63]\n"
"ds_write_addtid_b32 v59 offset:13984\n"
"v_add_f32 v92, v92, v120\n"
"v_mfma_f32_16x16x32_bf16 v[60:63], v[76:79], a[48:51], v[60:63]\n"
"v_add_f32 v93, v93, v121\n"
"v_mfma_f32_16x16x32_bf16 v[60:63], v[80:83], a[52:55], v[60:63]\n"
"s_waitcnt vmcnt(0)\n"
"buffer_store_dwordx2 v[0:1], v14, s[40:43], s95 offen offset:0 sc1\n"
"v_add_f32 v94, v94, v122\n"
"v_mfma_f32_16x16x32_bf16 v[84:87], v[100:103], a[56:59], 0\n"
"v_add_f32 v95, v95, v123\n"
"v_mfma_f32_16x16x32_bf16 v[84:87], v[104:107], a[60:63], v[84:87]\n"
"ds_write_addtid_b32 v60 offset:256\n"
"v_mfma_f32_16x16x32_bf16 v[84:87], v[64:67], a[64:67], v[84:87]\n"
"s_add_u32 s94, s50, 0xffe80000\n"
"s_and_b32 s95, s94, 0xfbffffff\n"
"ds_write_addtid_b32 v61 offset:4896\n"
"v_mfma_f32_16x16x32_bf16 v[84:87], v[68:71], a[68:71], v[84:87]\n"
"ds_write_addtid_b32 v62 offset:9600\n"
"v_cvt_pk_bf16_f32 v88, v92, v93\n"
"v_mfma_f32_16x16x32_bf16 v[84:87], v[72:75], a[72:75], v[84:87]\n"
"ds_write_addtid_b32 v63 offset:14240\n"
"v_cvt_pk_bf16_f32 v89, v94, v95\n"
"v_mfma_f32_16x16x32_bf16 v[84:87], v[76:79], a[76:79], v[84:87]\n"
"s_waitcnt lgkmcnt(8)\n"
"v_add_f32 v128, v128, v132\n"
"v_add_f32 v129, v129, v133\n"
"v_mfma_f32_16x16x32_bf16 v[84:87], v[80:83], a[80:83], v[84:87]\n"
"v_add_f32 v130, v130, v134\n"
"v_add_f32 v131, v131, v135\n"
"v_mfma_f32_16x16x32_bf16 v[60:63], v[100:103], a[84:87], 0\n"
"v_add_f32 v128, v128, v136\n"
"v_add_f32 v129, v129, v137\n"
"v_mfma_f32_16x16x32_bf16 v[60:63], v[104:107], a[88:91], v[60:63]\n"
"ds_write_addtid_b32 v84 offset:512\n"
"v_add_f32 v130, v130, v138\n"
"v_mfma_f32_16x16x32_bf16 v[60:63], v[64:67], a[92:95], v[60:63]\n"
"ds_write_addtid_b32 v85 offset:5152\n"
"v_add_f32 v131, v131, v139\n"
"v_mfma_f32_16x16x32_bf16 v[60:63], v[68:71], a[96:99], v[60:63]\n"
"ds_write_addtid_b32 v86 offset:9856\n"
"v_add_f32 v128, v128, v124\n"
"v_mfma_f32_16x16x32_bf16 v[60:63], v[72:75], a[100:103], v[60:63]\n"
"ds_write_addtid_b32 v87 offset:14496\n"
"v_add_f32 v129, v129, v125\n"
"v_mfma_f32_16x16x32_bf16 v[60:63], v[76:79], a[104:107], v[60:63]\n"
"v_add_f32 v130, v130, v126\n"
"v_add_f32 v131, v131, v127\n"
"v_mfma_f32_16x16x32_bf16 v[60:63], v[80:83], a[108:111], v[60:63]\n"
"v_cvt_pk_bf16_f32 v90, v128, v129\n"
"v_cvt_pk_bf16_f32 v91, v130, v131\n"
"v_mfma_f32_16x16x32_bf16 v[84:87], v[100:103], a[112:115], 0\n"
"v_mfma_f32_16x16x32_bf16 v[84:87], v[104:107], a[116:119], v[84:87]\n"
"ds_write_addtid_b32 v60 offset:768\n"
"v_mfma_f32_16x16x32_bf16 v[84:87], v[64:67], a[120:123], v[84:87]\n"
"ds_write_addtid_b32 v61 offset:5408\n"
"v_mfma_f32_16x16x32_bf16 v[84:87], v[68:71], a[124:127], v[84:87]\n"
"ds_write_addtid_b32 v62 offset:10112\n"
"v_mfma_f32_16x16x32_bf16 v[84:87], v[72:75], a[128:131], v[84:87]\n"
"ds_write_addtid_b32 v63 offset:14752\n"
"v_mfma_f32_16x16x32_bf16 v[84:87], v[76:79], a[132:135], v[84:87]\n"
"v_mfma_f32_16x16x32_bf16 v[84:87], v[80:83], a[136:139], v[84:87]\n"
"v_mfma_f32_16x16x32_bf16 v[92:95], v[100:103], a[140:143], 0\n"
"v_mfma_f32_16x16x32_bf16 v[92:95], v[104:107], a[144:147], v[92:95]\n"
"ds_write_addtid_b32 v84 offset:1024\n"
"v_mfma_f32_16x16x32_bf16 v[92:95], v[64:67], a[148:151], v[92:95]\n"
"ds_write_addtid_b32 v85 offset:5664\n"
"v_mfma_f32_16x16x32_bf16 v[92:95], v[68:71], a[152:155], v[92:95]\n"
"ds_write_addtid_b32 v86 offset:10368\n"
"v_mfma_f32_16x16x32_bf16 v[92:95], v[72:75], a[156:159], v[92:95]\n"
"buffer_store_dwordx4 v[88:91], v13, s[40:43], s95 offen sc1\n"
"ds_write_addtid_b32 v87 offset:15008\n"
"v_mfma_f32_16x16x32_bf16 v[92:95], v[76:79], a[160:163], v[92:95]\n"
"v_mfma_f32_16x16x32_bf16 v[92:95], v[80:83], a[164:167], v[92:95]\n"
"v_mfma_f32_16x16x32_bf16 v[88:91], a[168:171], v[100:103], 0\n"
"v_mfma_f32_16x16x32_bf16 v[88:91], a[172:175], v[104:107], v[88:91]\n"
"ds_write_addtid_b32 v92 offset:1280\n"
"v_mfma_f32_16x16x32_bf16 v[88:91], a[176:179], v[64:67], v[88:91]\n"
"ds_write_addtid_b32 v93 offset:5920\n"
"v_mfma_f32_16x16x32_bf16 v[88:91], a[180:183], v[68:71], v[88:91]\n"
"ds_write_addtid_b32 v94 offset:10624\n"
"v_mfma_f32_16x16x32_bf16 v[88:91], a[184:187], v[72:75], v[88:91]\n"
"ds_write_addtid_b32 v95 offset:15264\n"
"v_mfma_f32_16x16x32_bf16 v[88:91], a[188:191], v[76:79], v[88:91]\n"
"v_mfma_f32_16x16x32_bf16 v[88:91], a[192:195], v[80:83], v[88:91]\n"
"v_mfma_f32_16x16x32_bf16 v[96:99], a[196:199], v[100:103], 0\n"
"v_mfma_f32_16x16x32_bf16 v[96:99], a[200:203], v[104:107], v[96:99]\n"
"v_mfma_f32_16x16x32_bf16 v[96:99], a[204:207], v[64:67], v[96:99]\n"
"v_mfma_f32_16x16x32_bf16 v[96:99], a[208:211], v[68:71], v[96:99]\n"
"v_mfma_f32_16x16x32_bf16 v[96:99], a[212:215], v[72:75], v[96:99]\n"
"v_mfma_f32_16x16x32_bf16 v[96:99], a[216:219], v[76:79], v[96:99]\n"
"v_mfma_f32_16x16x32_bf16 v[96:99], a[220:223], v[80:83], v[96:99]\n"
"v_mfma_f32_16x16x32_bf16 v[92:95], a[224:227], v[100:103], 0\n"
"v_mfma_f32_16x16x32_bf16 v[92:95], a[228:231], v[104:107], v[92:95]\n"
"v_mfma_f32_16x16x32_bf16 v[92:95], a[232:235], v[64:67], v[92:95]\n"
"v_mfma_f32_16x16x32_bf16 v[92:95], a[236:239], v[68:71], v[92:95]\n"
"v_mfma_f32_16x16x32_bf16 v[92:95], a[240:243], v[72:75], v[92:95]\n"
"v_mfma_f32_16x16x32_bf16 v[92:95], a[244:247], v[76:79], v[92:95]\n"
"v_mfma_f32_16x16x32_bf16 v[92:95], a[248:251], v[80:83], v[92:95]\n"
"v_mfma_f32_16x16x32_bf16 v[120:123], a[252:255], v[100:103], 0\n"
"v_mfma_f32_16x16x32_bf16 v[120:123], v[20:23], v[104:107], v[120:123]\n"
"s_waitcnt lgkmcnt(0)\n"
"v_mfma_f32_16x16x32_bf16 v[120:123], v[24:27], v[64:67], v[120:123]\n"
"s_barrier\n"
"v_mfma_f32_16x16x32_bf16 v[120:123], v[28:31], v[68:71], v[120:123]\n"
"ds_read_b128 v[100:103], v2 offset:2048\n"
"ds_read_b128 v[84:87], v3 offset:512\n"
"v_mfma_f32_16x16x32_bf16 v[120:123], v[32:35], v[72:75], v[120:123]\n"
"ds_read_b128 v[104:107], v4 offset:3072\n"
"v_mfma_f32_16x16x32_bf16 v[120:123], v[36:39], v[76:79], v[120:123]\n"
"ds_read_b128 v[124:127], v2 offset:2304\n"
"ds_read_b128 v[128:131], v3 offset:768\n"
"v_mfma_f32_16x16x32_bf16 v[120:123], v[40:43], v[80:83], v[120:123]\n"
"ds_read_b128 v[132:135], v4 offset:3328\n"
"ds_read_b128 v[136:139], v2 offset:3584\n"
"ds_read_b128 v[144:147], v3 offset:1536\n"
"ds_read_b128 v[148:151], v4 offset:3584\n"
"ds_read_b128 v[152:155], v2 offset:3840\n"
"ds_read_b128 v[156:159], v3 offset:1792\n"
"ds_read_b128 v[160:163], v4 offset:3840\n"
"s_waitcnt lgkmcnt(9)\n"
"v_add_f32 v100, v100, v84\n"
"v_add_f32 v101, v101, v85\n"
"v_add_f32 v102, v102, v86\n"
"v_add_f32 v103, v103, v87\n"
"v_add_f32 v100, v100, v104\n"
"v_add_f32 v101, v101, v105\n"
"v_add_f32 v102, v102, v106\n"
"v_add_f32 v103, v103, v107\n"
"v_add_f32 v100, v100, v88\n"
"v_add_f32 v101, v101, v89\n"
"v_add_f32 v102, v102, v90\n"
"v_add_f32 v103, v103, v91\n"
"s_add_u32 s93, s50, 0x0\n"
"s_and_b32 s95, s93, 0xfbffffff\n"
"v_cvt_pk_bf16_f32 v88, v100, v101\n"
"s_waitcnt vmcnt(0)\n"
"v_cvt_pk_bf16_f32 v89, v102, v103\n"
"s_waitcnt lgkmcnt(6)\n"
"v_add_f32 v124, v124, v128\n"
"v_add_f32 v125, v125, v129\n"
"v_add_f32 v126, v126, v130\n"
"v_add_f32 v127, v127, v131\n"
"v_add_f32 v124, v124, v132\n"
"v_add_f32 v125, v125, v133\n"
"v_add_f32 v126, v126, v134\n"
"v_add_f32 v127, v127, v135\n"
"v_add_f32 v124, v124, v96\n"
"v_add_f32 v125, v125, v97\n"
"v_add_f32 v126, v126, v98\n"
"v_add_f32 v127, v127, v99\n"
"v_cvt_pk_bf16_f32 v90, v124, v125\n"
"v_cvt_pk_bf16_f32 v91, v126, v127\n"
"buffer_store_dwordx4 v[88:91], v13, s[40:43], s95 offen sc1\n"
"s_waitcnt lgkmcnt(3)\n"
"v_add_f32 v136, v136, v144\n"
"v_add_f32 v137, v137, v145\n"
"v_add_f32 v138, v138, v146\n"
"v_add_f32 v139, v139, v147\n"
"v_add_f32 v136, v136, v148\n"
"v_add_f32 v137, v137, v149\n"
"v_add_f32 v138, v138, v150\n"
"v_add_f32 v139, v139, v151\n"
"v_add_f32 v136, v136, v92\n"
"v_add_f32 v137, v137, v93\n"
"v_add_f32 v138, v138, v94\n"
"v_add_f32 v139, v139, v95\n"
"v_cvt_pk_bf16_f32 v100, v136, v137\n"
"v_cvt_pk_bf16_f32 v101, v138, v139\n"
"s_waitcnt lgkmcnt(0)\n"
"v_add_f32 v152, v152, v156\n"
"v_add_f32 v153, v153, v157\n"
"v_add_f32 v154, v154, v158\n"
"v_add_f32 v155, v155, v159\n"
"v_add_f32 v152, v152, v160\n"
"v_add_f32 v153, v153, v161\n"
"v_add_f32 v154, v154, v162\n"
"v_add_f32 v155, v155, v163\n"
"v_add_f32 v152, v152, v120\n"
"v_add_f32 v153, v153, v121\n"
"v_add_f32 v154, v154, v122\n"
"v_add_f32 v155, v155, v123\n"
"v_cvt_pk_bf16_f32 v102, v152, v153\n"
"v_cvt_pk_bf16_f32 v103, v154, v155\n"
"buffer_store_dwordx4 v[100:103], v15, s[40:43], s95 offen sc1\n"
"buffer_store_dwordx2 v[0:1], v14, s[40:43], s94 offen offset:0 sc1\n"
"s_waitcnt vmcnt(1)\n"
"buffer_store_dwordx2 v[0:1], v16, s[40:43], s93 offen offset:0 sc1\n"
"s_branch .LG_end\n"
".LG_t2:\n"
"v_mfma_f32_16x16x32_bf16 v[92:95], a[236:239], v[68:71], v[92:95]\n"
"v_mfma_f32_16x16x32_bf16 v[92:95], a[240:243], v[72:75], v[92:95]\n"
"v_mfma_f32_16x16x32_bf16 v[92:95], a[244:247], v[76:79], v[92:95]\n"
"v_mfma_f32_16x16x32_bf16 v[92:95], a[248:251], v[80:83], v[92:95]\n"
"v_mfma_f32_16x16x32_bf16 v[120:123], a[252:255], v[100:103], 0\n"
"v_mfma_f32_16x16x32_bf16 v[120:123], v[20:23], v[104:107], v[120:123]\n"
"s_waitcnt lgkmcnt(0)\n"
"v_mfma_f32_16x16x32_bf16 v[120:123], v[24:27], v[64:67], v[120:123]\n"
"s_barrier\n"
"v_mfma_f32_16x16x32_bf16 v[120:123], v[28:31], v[68:71], v[120:123]\n"
"ds_read_b128 v[100:103], v2 offset:2048\n"
"ds_read_b128 v[84:87], v3 offset:512\n"
"v_mfma_f32_16x16x32_bf16 v[120:123], v[32:35], v[72:75], v[120:123]\n"
"ds_read_b128 v[104:107], v4 offset:3072\n"
"v_mfma_f32_16x16x32_bf16 v[120:123], v[36:39], v[76:79], v[120:123]\n"
"ds_read_b128 v[124:127], v2 offset:2304\n"
"ds_read_b128 v[128:131], v3 offset:768\n"
"v_mfma_f32_16x16x32_bf16 v[120:123], v[40:43], v[80:83], v[120:123]\n"
"s_waitcnt vmcnt(8)\n"
"ds_read_b128 v[132:135], v4 offset:3328\n"
"ds_read_b128 v[136:139], v2 offset:3584\n"
"v_mfma_f32_16x16x32_bf16 v[140:143], v[108:111], a[0:3], 0\n"
"ds_read_b128 v[144:147], v3 offset:1536\n"
"v_mfma_f32_16x16x32_bf16 v[140:143], v[112:115], a[4:7], v[140:143]\n"
"s_waitcnt vmcnt(6)\n"
"ds_read_b128 v[148:151], v4 offset:3584\n"
"ds_read_b128 v[152:155], v2 offset:3840\n"
"v_mfma_f32_16x16x32_bf16 v[140:143], v[116:119], a[8:11], v[140:143]\n"
"ds_read_b128 v[156:159], v3 offset:1792\n"
"ds_read_b128 v[160:163], v4 offset:3840\n"
"v_mfma_f32_16x16x32_bf16 v[140:143], v[44:47], a[12:15], v[140:143]\n"
"s_waitcnt vmcnt(3)\n"
"v_mfma_f32_16x16x32_bf16 v[140:143], v[48:51], a[16:19], v[140:143]\n"
"s_waitcnt lgkmcnt(9)\n"
"v_add_f32 v100, v100, v84\n"
"v_mfma_f32_16x16x32_bf16 v[140:143], v[52:55], a[20:23], v[140:143]\n"
"s_waitcnt vmcnt(1)\n"
"v_add_f32 v101, v101, v85\n"
"v_add_f32 v102, v102, v86\n"
"v_mfma_f32_16x16x32_bf16 v[140:143], v[56:59], a[24:27], v[140:143]\n"
"v_add_f32 v103, v103, v87\n"
"v_add_f32 v100, v100, v104\n"
"v_mfma_f32_16x16x32_bf16 v[164:167], v[108:111], a[28:31], 0\n"
"v_mfma_f32_16x16x32_bf16 v[164:167], v[112:115], a[32:35], v[164:167]\n"
"ds_write_addtid_b32 v140 offset:2048\n"
"v_mfma_f32_16x16x32_bf16 v[164:167], v[116:119], a[36:39], v[164:167]\n"
"ds_write_addtid_b32 v141 offset:6688\n"
"v_add_f32 v101, v101, v105\n"
"v_mfma_f32_16x16x32_bf16 v[164:167], v[44:47], a[40:43], v[164:167]\n"
"ds_write_addtid_b32 v142 offset:11392\n"
"v_mfma_f32_16x16x32_bf16 v[164:167], v[48:51], a[44:47], v[164:167]\n"
"ds_write_addtid_b32 v143 offset:16032\n"
"v_add_f32 v102, v102, v106\n"
"v_mfma_f32_16x16x32_bf16 v[164:167], v[52:55], a[48:51], v[164:167]\n"
"v_add_f32 v103, v103, v107\n"
"v_add_f32 v100, v100, v88\n"
"v_mfma_f32_16x16x32_bf16 v[164:167], v[56:59], a[52:55], v[164:167]\n"
"v_add_f32 v101, v101, v89\n"
"v_add_f32 v102, v102, v90\n"
"v_mfma_f32_16x16x32_bf16 v[104:107], v[108:111], a[56:59], 0\n"
"v_add_f32 v103, v103, v91\n"
"v_mfma_f32_16x16x32_bf16 v[104:107], v[112:115], a[60:63], v[104:107]\n"
"s_add_u32 s93, s50, 0xffe80000\n"
"s_and_b32 s95, s93, 0xfbffffff\n"
"ds_write_addtid_b32 v164 offset:2304\n"
"v_mfma_f32_16x16x32_bf16 v[104:107], v[116:119], a[64:67], v[104:107]\n"
"ds_write_addtid_b32 v165 offset:6944\n"
"v_cvt_pk_bf16_f32 v88, v100, v101\n"
"v_mfma_f32_16x16x32_bf16 v[104:107], v[44:47], a[68:71], v[104:107]\n"
"s_waitcnt vmcnt(0)\n"
"ds_write_addtid_b32 v166 offset:11648\n"
"v_mfma_f32_16x16x32_bf16 v[104:107], v[48:51], a[72:75], v[104:107]\n"
"ds_write_addtid_b32 v167 offset:16288\n"
"v_cvt_pk_bf16_f32 v89, v102, v103\n"
"v_mfma_f32_16x16x32_bf16 v[104:107], v[52:55], a[76:79], v[104:107]\n"
"s_waitcnt lgkmcnt(14)\n"
"v_add_f32 v124, v124, v128\n"
"v_add_f32 v125, v125, v129\n"
"v_mfma_f32_16x16x32_bf16 v[104:107], v[56:59], a[80:83], v[104:107]\n"
"v_add_f32 v126, v126, v130\n"
"v_add_f32 v127, v127, v131\n"
"v_mfma_f32_16x16x32_bf16 v[100:103], v[108:111], a[84:87], 0\n"
"v_add_f32 v124, v124, v132\n"
"v_add_f32 v125, v125, v133\n"
"v_mfma_f32_16x16x32_bf16 v[100:103], v[112:115], a[88:91], v[100:103]\n"
"ds_write_addtid_b32 v104 offset:2560\n"
"v_add_f32 v126, v126, v134\n"
"v_mfma_f32_16x16x32_bf16 v[100:103], v[116:119], a[92:95], v[100:103]\n"
"ds_write_addtid_b32 v105 offset:7200\n"
"v_add_f32 v127, v127, v135\n"
"v_mfma_f32_16x16x32_bf16 v[100:103], v[44:47], a[96:99], v[100:103]\n"
"ds_write_addtid_b32 v106 offset:11904\n"
"v_add_f32 v124, v124, v96\n"
"v_mfma_f32_16x16x32_bf16 v[100:103], v[48:51], a[100:103], v[100:103]\n"
"ds_write_addtid_b32 v107 offset:16544\n"
"v_add_f32 v125, v125, v97\n"
"v_mfma_f32_16x16x32_bf16 v[100:103], v[52:55], a[104:107], v[100:103]\n"
"v_add_f32 v126, v126, v98\n"
"v_add_f32 v127, v127, v99\n"
"v_mfma_f32_16x16x32_bf16 v[100:103], v[56:59], a[108:111], v[100:103]\n"
"v_cvt_pk_bf16_f32 v90, v124, v125\n"
"v_cvt_pk_bf16_f32 v91, v126, v127\n"
"v_mfma_f32_16x16x32_bf16 v[96:99], v[108:111], a[112:115], 0\n"
"buffer_store_dwordx4 v[88:91], v13, s[40:43], s95 offen sc1\n"
"v_mfma_f32_16x16x32_bf16 v[96:99], v[112:115], a[116:119], v[96:99]\n"
"s_waitcnt lgkmcnt(15)\n"
"ds_write_addtid_b32 v100 offset:2816\n"
"v_add_f32 v136, v136, v144\n"
"v_mfma_f32_16x16x32_bf16 v[96:99], v[116:119], a[120:123], v[96:99]\n"
"ds_write_addtid_b32 v101 offset:7456\n"
"v_add_f32 v137, v137, v145\n"
"v_mfma_f32_16x16x32_bf16 v[96:99], v[44:47], a[124:127], v[96:99]\n"
"ds_write_addtid_b32 v102 offset:12160\n"
"v_add_f32 v138, v138, v146\n"
"v_mfma_f32_16x16x32_bf16 v[96:99], v[48:51], a[128:131], v[96:99]\n"
"ds_write_addtid_b32 v103 offset:16800\n"
"v_add_f32 v139, v139, v147\n"
"v_mfma_f32_16x16x32_bf16 v[96:99], v[52:55], a[132:135], v[96:99]\n"
"v_add_f32 v136, v136, v148\n"
"v_add_f32 v137, v137, v149\n"
"v_mfma_f32_16x16x32_bf16 v[96:99], v[56:59], a[136:139], v[96:99]\n"
"v_add_f32 v138, v138, v150\n"
"v_add_f32 v139, v139, v151\n"
"v_mfma_f32_16x16x32_bf16 v[88:91], v[108:111], a[140:143], 0\n"
"v_add_f32 v136, v136, v92\n"
"v_add_f32 v137, v137, v93\n"
"v_mfma_f32_16x16x32_bf16 v[88:91], v[112:115], a[144:147], v[88:91]\n"
"ds_write_addtid_b32 v96 offset:3072\n"
"v_add_f32 v138, v138, v94\n"
"v_mfma_f32_16x16x32_bf16 v[88:91], v[116:119], a[148:151], v[88:91]\n"
"ds_write_addtid_b32 v97 offset:7712\n"
"v_add_f32 v139, v139, v95\n"
"v_mfma_f32_16x16x32_bf16 v[88:91], v[44:47], a[152:155], v[88:91]\n"
"ds_write_addtid_b32 v98 offset:12416\n"
"v_cvt_pk_bf16_f32 v100, v136, v137\n"
"v_mfma_f32_16x16x32_bf16 v[88:91], v[48:51], a[156:159], v[88:91]\n"
"ds_write_addtid_b32 v99 offset:17056\n"
"v_cvt_pk_bf16_f32 v101, v138, v139\n"
"v_mfma_f32_16x16x32_bf16 v[88:91], v[52:55], a[160:163], v[88:91]\n"
"s_waitcnt lgkmcnt(15)\n"
"v_add_f32 v152, v152, v156\n"
"v_add_f32 v153, v153, v157\n"
"v_mfma_f32_16x16x32_bf16 v[88:91], v[56:59], a[164:167], v[88:91]\n"
"v_add_f32 v154, v154, v158\n"
"v_add_f32 v155, v155, v159\n"
"v_mfma_f32_16x16x32_bf16 v[92:95], v[108:111], a[224:227], 0\n"
"v_add_f32 v152, v152, v160\n"
"v_add_f32 v153, v153, v161\n"
"v_mfma_f32_16x16x32_bf16 v[92:95], v[112:115], a[228:231], v[92:95]\n"
"ds_write_addtid_b32 v88 offset:3328\n"
"v_add_f32 v154, v154, v162\n"
"v_mfma_f32_16x16x32_bf16 v[92:95], v[116:119], a[232:235], v[92:95]\n"
"ds_write_addtid_b32 v89 offset:7968\n"
"v_add_f32 v155, v155, v163\n"
"v_mfma_f32_16x16x32_bf16 v[92:95], v[44:47], a[236:239], v[92:95]\n"
"ds_write_addtid_b32 v90 offset:12672\n"
"v_add_f32 v152, v152, v120\n"
"v_mfma_f32_16x16x32_bf16 v[92:95], v[48:51], a[240:243], v[92:95]\n"
"ds_write_addtid_b32 v91 offset:17312\n"
"v_add_f32 v153, v153, v121\n"
"v_mfma_f32_16x16x32_bf16 v[92:95], v[52:55], a[244:247], v[92:95]\n"
"v_add_f32 v154, v154, v122\n"
"v_add_f32 v155, v155, v123\n"
"v_mfma_f32_16x16x32_bf16 v[92:95], v[56:59], a[248:251], v[92:95]\n"
"v_cvt_pk_bf16_f32 v102, v152, v153\n"
"v_cvt_pk_bf16_f32 v103, v154, v155\n"
"v_mfma_f32_16x16x32_bf16 v[88:91], v[108:111], a[252:255], 0\n"
"buffer_store_dwordx4 v[100:103], v15, s[40:43], s95 offen sc1\n"
"v_mfma_f32_16x16x32_bf16 v[88:91], v[112:115], v[20:23], v[88:91]\n"
"buffer_store_dwordx2 v[0:1], v14, s[40:43], s94 offen offset:0 sc1\n"
"ds_write_addtid_b32 v92 offset:3584\n"
"v_mfma_f32_16x16x32_bf16 v[88:91], v[116:119], v[24:27], v[88:91]\n"
"ds_write_addtid_b32 v93 offset:8224\n"
"v_mfma_f32_16x16x32_bf16 v[88:91], v[44:47], v[28:31], v[88:91]\n"
"ds_write_addtid_b32 v94 offset:12928\n"
"v_mfma_f32_16x16x32_bf16 v[88:91], v[48:51], v[32:35], v[88:91]\n"
"ds_write_addtid_b32 v95 offset:17568\n"
"v_mfma_f32_16x16x32_bf16 v[88:91], v[52:55], v[36:39], v[88:91]\n"
"v_mfma_f32_16x16x32_bf16 v[88:91], v[56:59], v[40:43], v[88:91]\n"
"v_mfma_f32_16x16x32_bf16 v[96:99], a[168:171], v[108:111], 0\n"
"v_mfma_f32_16x16x32_bf16 v[96:99], a[172:175], v[112:115], v[96:99]\n"
"ds_write_addtid_b32 v88 offset:3840\n"
"v_mfma_f32_16x16x32_bf16 v[96:99], a[176:179], v[116:119], v[96:99]\n"
"ds_write_addtid_b32 v89 offset:8480\n"
"v_mfma_f32_16x16x32_bf16 v[96:99], a[180:183], v[44:47], v[96:99]\n"
"ds_write_addtid_b32 v90 offset:13184\n"
"v_mfma_f32_16x16x32_bf16 v[96:99], a[184:187], v[48:51], v[96:99]\n"
"ds_write_addtid_b32 v91 offset:17824\n"
"v_mfma_f32_16x16x32_bf16 v[96:99], a[188:191], v[52:55], v[96:99]\n"
"v_mfma_f32_16x16x32_bf16 v[96:99], a[192:195], v[56:59], v[96:99]\n"
"v_mfma_f32_16x16x32_bf16 v[100:103], a[196:199], v[108:111], 0\n"
"v_mfma_f32_16x16x32_bf16 v[100:103], a[200:203], v[112:115], v[100:103]\n"
"s_waitcnt lgkmcnt(0)\n"
"v_mfma_f32_16x16x32_bf16 v[100:103], a[204:207], v[116:119], v[100:103]\n"
"s_barrier\n"
"v_mfma_f32_16x16x32_bf16 v[100:103], a[208:211], v[44:47], v[100:103]\n"
"ds_read_b128 v[104:107], v2 offset:0\n"
"ds_read_b128 v[108:111], v3 offset:2560\n"
"v_mfma_f32_16x16x32_bf16 v[100:103], a[212:215], v[48:51], v[100:103]\n"
"ds_read_b128 v[112:115], v4 offset:1024\n"
"ds_read_b128 v[116:119], v2 offset:256\n"
"v_mfma_f32_16x16x32_bf16 v[100:103], a[216:219], v[52:55], v[100:103]\n"
"ds_read_b128 v[120:123], v3 offset:2816\n"
"ds_read_b128 v[124:127], v4 offset:1280\n"
"v_mfma_f32_16x16x32_bf16 v[100:103], a[220:223], v[56:59], v[100:103]\n"
"s_waitcnt lgkmcnt(3)\n"
"v_add_f32 v104, v104, v108\n"
"v_add_f32 v105, v105, v109\n"
"v_add_f32 v106, v106, v110\n"
"v_add_f32 v107, v107, v111\n"
"v_add_f32 v104, v104, v112\n"
"v_add_f32 v105, v105, v113\n"
"v_add_f32 v106, v106, v114\n"
"v_add_f32 v107, v107, v115\n"
"v_add_f32 v104, v104, v96\n"
"v_add_f32 v105, v105, v97\n"
"v_add_f32 v106, v106, v98\n"
"v_add_f32 v107, v107, v99\n"
"s_add_u32 s95, s50, 0x0\n"
"s_and_b32 s94, s95, 0xfbffffff\n"
"v_cvt_pk_bf16_f32 v112, v104, v105\n"
"v_cvt_pk_bf16_f32 v113, v106, v107\n"
"s_waitcnt lgkmcnt(0)\n"
"v_add_f32 v116, v116, v120\n"
"v_add_f32 v117, v117, v121\n"
"v_add_f32 v118, v118, v122\n"
"v_add_f32 v119, v119, v123\n"
"v_add_f32 v116, v116, v124\n"
"v_add_f32 v117, v117, v125\n"
"v_add_f32 v118, v118, v126\n"
"v_add_f32 v119, v119, v127\n"
"v_add_f32 v116, v116, v100\n"
"v_add_f32 v117, v117, v101\n"
"v_add_f32 v118, v118, v102\n"
"v_add_f32 v119, v119, v103\n"
"s_waitcnt vmcnt(1)\n"
"buffer_store_dwordx2 v[0:1], v16, s[40:43], s93 offen offset:0 sc1\n"
"v_cvt_pk_bf16_f32 v114, v116, v117\n"
"v_cvt_pk_bf16_f32 v115, v118, v119\n"
"buffer_store_dwordx4 v[112:115], v13, s[40:43], s94 offen sc1\n"
"s_waitcnt vmcnt(0)\n"
"buffer_store_dwordx2 v[0:1], v14, s[40:43], s95 offen offset:0 sc1\n"
"s_branch .LG_end\n"
".LG_t3:\n"
"v_mfma_f32_16x16x32_bf16 v[96:99], a[192:195], v[56:59], v[96:99]\n"
"v_mfma_f32_16x16x32_bf16 v[100:103], a[196:199], v[108:111], 0\n"
"v_mfma_f32_16x16x32_bf16 v[100:103], a[200:203], v[112:115], v[100:103]\n"
"s_waitcnt lgkmcnt(0)\n"
"v_mfma_f32_16x16x32_bf16 v[100:103], a[204:207], v[116:119], v[100:103]\n"
"s_barrier\n"
"v_mfma_f32_16x16x32_bf16 v[100:103], a[208:211], v[44:47], v[100:103]\n"
"ds_read_b128 v[104:107], v2 offset:0\n"
"ds_read_b128 v[108:111], v3 offset:2560\n"
"v_mfma_f32_16x16x32_bf16 v[100:103], a[212:215], v[48:51], v[100:103]\n"
"ds_read_b128 v[112:115], v4 offset:1024\n"
"ds_read_b128 v[116:119], v2 offset:256\n"
"v_mfma_f32_16x16x32_bf16 v[100:103], a[216:219], v[52:55], v[100:103]\n"
"ds_read_b128 v[120:123], v3 offset:2816\n"
"ds_read_b128 v[124:127], v4 offset:1280\n"
"v_mfma_f32_16x16x32_bf16 v[100:103], a[220:223], v[56:59], v[100:103]\n"
"s_waitcnt vmcnt(9)\n"
"v_mfma_f32_16x16x32_bf16 v[128:131], v[60:63], a[0:3], 0\n"
"v_mfma_f32_16x16x32_bf16 v[128:131], v[64:67], a[4:7], v[128:131]\n"
"s_waitcnt vmcnt(7)\n"
"v_mfma_f32_16x16x32_bf16 v[128:131], v[68:71], a[8:11], v[128:131]\n"
"v_mfma_f32_16x16x32_bf16 v[128:131], v[72:75], a[12:15], v[128:131]\n"
"s_waitcnt vmcnt(4)\n"
"v_mfma_f32_16x16x32_bf16 v[128:131], v[76:79], a[16:19], v[128:131]\n"
"s_waitcnt lgkmcnt(3)\n"
"v_add_f32 v104, v104, v108\n"
"v_add_f32 v105, v105, v109\n"
"v_mfma_f32_16x16x32_bf16 v[128:131], v[80:83], a[20:23], v[128:131]\n"
"s_waitcnt vmcnt(3)\n"
"v_add_f32 v106, v106, v110\n"
"v_mfma_f32_16x16x32_bf16 v[128:131], v[84:87], a[24:27], v[128:131]\n"
"v_mfma_f32_16x16x32_bf16 v[132:135], v[60:63], a[28:31], 0\n"
"v_add_f32 v107, v107, v111\n"
"v_add_f32 v104, v104, v112\n"
"v_mfma_f32_16x16x32_bf16 v[132:135], v[64:67], a[32:35], v[132:135]\n"
"ds_write_addtid_b32 v128 offset:0\n"
"v_add_f32 v105, v105, v113\n"
"v_mfma_f32_16x16x32_bf16 v[132:135], v[68:71], a[36:39], v[132:135]\n"
"ds_write_addtid_b32 v129 offset:4640\n"
"v_mfma_f32_16x16x32_bf16 v[132:135], v[72:75], a[40:43], v[132:135]\n"
"ds_write_addtid_b32 v130 offset:9344\n"
"v_add_f32 v106, v106, v114\n"
"v_mfma_f32_16x16x32_bf16 v[132:135], v[76:79], a[44:47], v[132:135]\n"
"ds_write_addtid_b32 v131 offset:13984\n"
"v_mfma_f32_16x16x32_bf16 v[132:135], v[80:83], a[48:51], v[132:135]\n"
"v_add_f32 v107, v107, v115\n"
"v_add_f32 v104, v104, v96\n"
"v_mfma_f32_16x16x32_bf16 v[132:135], v[84:87], a[52:55], v[132:135]\n"
"v_add_f32 v105, v105, v97\n"
"v_add_f32 v106, v106, v98\n"
"v_mfma_f32_16x16x32_bf16 v[108:111], v[60:63], a[56:59], 0\n"
"v_add_f32 v107, v107, v99\n"
"v_mfma_f32_16x16x32_bf16 v[108:111], v[64:67], a[60:63], v[108:111]\n"
"s_add_u32 s95, s50, 0xffe80000\n"
"s_and_b32 s94, s95, 0xfbffffff\n"
"ds_write_addtid_b32 v132 offset:256\n"
"v_mfma_f32_16x16x32_bf16 v[108:111], v[68:71], a[64:67], v[108:111]\n"
"ds_write_addtid_b32 v133 offset:4896\n"
"v_mfma_f32_16x16x32_bf16 v[108:111], v[72:75], a[68:71], v[108:111]\n"
"ds_write_addtid_b32 v134 offset:9600\n"
"v_cvt_pk_bf16_f32 v112, v104, v105\n"
"v_mfma_f32_16x16x32_bf16 v[108:111], v[76:79], a[72:75], v[108:111]\n"
"ds_write_addtid_b32 v135 offset:14240\n"
"v_cvt_pk_bf16_f32 v113, v106, v107\n"
"v_mfma_f32_16x16x32_bf16 v[108:111], v[80:83], a[76:79], v[108:111]\n"
"s_waitcnt lgkmcnt(8)\n"
"v_add_f32 v116, v116, v120\n"
"v_add_f32 v117, v117, v121\n"
"v_mfma_f32_16x16x32_bf16 v[108:111], v[84:87], a[80:83], v[108:111]\n"
"v_add_f32 v118, v118, v122\n"
"v_add_f32 v119, v119, v123\n"
"v_mfma_f32_16x16x32_bf16 v[104:107], v[60:63], a[84:87], 0\n"
"v_add_f32 v116, v116, v124\n"
"v_add_f32 v117, v117, v125\n"
"v_mfma_f32_16x16x32_bf16 v[104:107], v[64:67], a[88:91], v[104:107]\n"
"ds_write_addtid_b32 v108 offset:512\n"
"v_add_f32 v118, v118, v126\n"
"v_mfma_f32_16x16x32_bf16 v[104:107], v[68:71], a[92:95], v[104:107]\n"
"ds_write_addtid_b32 v109 offset:5152\n"
"v_add_f32 v119, v119, v127\n"
"v_mfma_f32_16x16x32_bf16 v[104:107], v[72:75], a[96:99], v[104:107]\n"
"ds_write_addtid_b32 v110 offset:9856\n"
"v_add_f32 v116, v116, v100\n"
"v_mfma_f32_16x16x32_bf16 v[104:107], v[76:79], a[100:103], v[104:107]\n"
"ds_write_addtid_b32 v111 offset:14496\n"
"v_add_f32 v117, v117, v101\n"
"v_mfma_f32_16x16x32_bf16 v[104:107], v[80:83], a[104:107], v[104:107]\n"
"v_add_f32 v118, v118, v102\n"
"v_add_f32 v119, v119, v103\n"
"v_mfma_f32_16x16x32_bf16 v[104:107], v[84:87], a[108:111], v[104:107]\n"
"s_waitcnt vmcnt(1)\n"
"buffer_store_dwordx2 v[0:1], v16, s[40:43], s93 offen offset:0 sc1\n"
"v_cvt_pk_bf16_f32 v114, v116, v117\n"
"v_mfma_f32_16x16x32_bf16 v[100:103], v[60:63], a[112:115], 0\n"
"v_cvt_pk_bf16_f32 v115, v118, v119\n"
"v_mfma_f32_16x16x32_bf16 v[100:103], v[64:67], a[116:119], v[100:103]\n"
"ds_write_addtid_b32 v104 offset:768\n"
"v_mfma_f32_16x16x32_bf16 v[100:103], v[68:71], a[120:123], v[100:103]\n"
"ds_write_addtid_b32 v105 offset:5408\n"
"v_mfma_f32_16x16x32_bf16 v[100:103], v[72:75], a[124:127], v[100:103]\n"
"ds_write_addtid_b32 v106 offset:10112\n"
"v_mfma_f32_16x16x32_bf16 v[100:103], v[76:79], a[128:131], v[100:103]\n"
"buffer_store_dwordx4 v[112:115], v13, s[40:43], s94 offen sc1\n"
"ds_write_addtid_b32 v107 offset:14752\n"
"v_mfma_f32_16x16x32_bf16 v[100:103], v[80:83], a[132:135], v[100:103]\n"
"v_mfma_f32_16x16x32_bf16 v[100:103], v[84:87], a[136:139], v[100:103]\n"
"v_mfma_f32_16x16x32_bf16 v[108:111], v[60:63], a[140:143], 0\n"
"v_mfma_f32_16x16x32_bf16 v[108:111], v[64:67], a[144:147], v[108:111]\n"
"ds_write_addtid_b32 v100 offset:1024\n"
"v_mfma_f32_16x16x32_bf16 v[108:111], v[68:71], a[148:151], v[108:111]\n"
"ds_write_addtid_b32 v101 offset:5664\n"
"v_mfma_f32_16x16x32_bf16 v[108:111], v[72:75], a[152:155], v[108:111]\n"
"ds_write_addtid_b32 v102 offset:10368\n"
"v_mfma_f32_16x16x32_bf16 v[108:111], v[76:79], a[156:159], v[108:111]\n"
"ds_write_addtid_b32 v103 offset:15008\n"
"v_mfma_f32_16x16x32_bf16 v[108:111], v[80:83], a[160:163], v[108:111]\n"
"v_mfma_f32_16x16x32_bf16 v[108:111], v[84:87], a[164:167], v[108:111]\n"
"v_mfma_f32_16x16x32_bf16 v[104:107], v[60:63], a[224:227], 0\n"
"v_mfma_f32_16x16x32_bf16 v[104:107], v[64:67], a[228:231], v[104:107]\n"
"ds_write_addtid_b32 v108 offset:1280\n"
"v_mfma_f32_16x16x32_bf16 v[104:107], v[68:71], a[232:235], v[104:107]\n"
"ds_write_addtid_b32 v109 offset:5920\n"
"v_mfma_f32_16x16x32_bf16 v[104:107], v[72:75], a[236:239], v[104:107]\n"
"ds_write_addtid_b32 v110 offset:10624\n"
"v_mfma_f32_16x16x32_bf16 v[104:107], v[76:79], a[240:243], v[104:107]\n"
"ds_write_addtid_b32 v111 offset:15264\n"
"v_mfma_f32_16x16x32_bf16 v[104:107], v[80:83], a[244:247], v[104:107]\n"
"v_mfma_f32_16x16x32_bf16 v[104:107], v[84:87], a[248:251], v[104:107]\n"
"v_mfma_f32_16x16x32_bf16 v[112:115], v[60:63], a[252:255], 0\n"
"v_mfma_f32_16x16x32_bf16 v[112:115], v[64:67], v[20:23], v[112:115]\n"
"ds_write_addtid_b32 v104 offset:1536\n"
"v_mfma_f32_16x16x32_bf16 v[112:115], v[68:71], v[24:27], v[112:115]\n"
"ds_write_addtid_b32 v105 offset:6176\n"
"v_mfma_f32_16x16x32_bf16 v[112:115], v[72:75], v[28:31], v[112:115]\n"
"ds_write_addtid_b32 v106 offset:10880\n"
"v_mfma_f32_16x16x32_bf16 v[112:115], v[76:79], v[32:35], v[112:115]\n"
"ds_write_addtid_b32 v107 offset:15520\n"
"v_mfma_f32_16x16x32_bf16 v[112:115], v[80:83], v[36:39], v[112:115]\n"
"v_mfma_f32_16x16x32_bf16 v[112:115], v[84:87], v[40:43], v[112:115]\n"
"v_mfma_f32_16x16x32_bf16 v[108:111], a[168:171], v[60:63], 0\n"
"v_mfma_f32_16x16x32_bf16 v[108:111], a[172:175], v[64:67], v[108:111]\n"
"ds_write_addtid_b32 v112 offset:1792\n"
"v_mfma_f32_16x16x32_bf16 v[108:111], a[176:179], v[68:71], v[108:111]\n"
"ds_write_addtid_b32 v113 offset:6432\n"
"v_mfma_f32_16x16x32_bf16 v[108:111], a[180:183], v[72:75], v[108:111]\n"
"ds_write_addtid_b32 v114 offset:11136\n"
"v_mfma_f32_16x16x32_bf16 v[108:111], a[184:187], v[76:79], v[108:111]\n"
"ds_write_addtid_b32 v115 offset:15776\n"
"v_mfma_f32_16x16x32_bf16 v[108:111], a[188:191], v[80:83], v[108:111]\n"
"v_mfma_f32_16x16x32_bf16 v[108:111], a[192:195], v[84:87], v[108:111]\n"
"v_mfma_f32_16x16x32_bf16 v[112:115], a[196:199], v[60:63], 0\n"
"v_mfma_f32_16x16x32_bf16 v[112:115], a[200:203], v[64:67], v[112:115]\n"
"s_waitcnt lgkmcnt(0)\n"
"v_mfma_f32_16x16x32_bf16 v[112:115], a[204:207], v[68:71], v[112:115]\n"
"s_barrier\n"
"v_mfma_f32_16x16x32_bf16 v[112:115], a[208:211], v[72:75], v[112:115]\n"
"ds_read_b128 v[60:63], v2 offset:2048\n"
"ds_read_b128 v[116:119], v3 offset:512\n"
"v_mfma_f32_16x16x32_bf16 v[112:115], a[212:215], v[76:79], v[112:115]\n"
"ds_read_b128 v[120:123], v4 offset:3072\n"
"ds_read_b128 v[124:127], v2 offset:2304\n"
"v_mfma_f32_16x16x32_bf16 v[112:115], a[216:219], v[80:83], v[112:115]\n"
"ds_read_b128 v[128:131], v3 offset:768\n"
"v_mfma_f32_16x16x32_bf16 v[112:115], a[220:223], v[84:87], v[112:115]\n"
"ds_read_b128 v[132:135], v4 offset:3328\n"
"s_waitcnt lgkmcnt(3)\n"
"v_add_f32 v60, v60, v116\n"
"v_add_f32 v61, v61, v117\n"
"v_add_f32 v62, v62, v118\n"
"v_add_f32 v63, v63, v119\n"
"v_add_f32 v60, v60, v120\n"
"v_add_f32 v61, v61, v121\n"
"v_add_f32 v62, v62, v122\n"
"s_waitcnt vmcnt(0)\n"
"buffer_store_dwordx2 v[0:1], v14, s[40:43], s95 offen offset:0 sc1\n"
"v_add_f32 v63, v63, v123\n"
"v_add_f32 v60, v60, v108\n"
"v_add_f32 v61, v61, v109\n"
"v_add_f32 v62, v62, v110\n"
"v_add_f32 v63, v63, v111\n"
"s_add_u32 s95, s50, 0x0\n"
"s_and_b32 s94, s95, 0xfbffffff\n"
"v_cvt_pk_bf16_f32 v108, v60, v61\n"
"v_cvt_pk_bf16_f32 v109, v62, v63\n"
"s_waitcnt lgkmcnt(0)\n"
"v_add_f32 v124, v124, v128\n"
"v_add_f32 v125, v125, v129\n"
"v_add_f32 v126, v126, v130\n"
"v_add_f32 v127, v127, v131\n"
"v_add_f32 v124, v124, v132\n"
"v_add_f32 v125, v125, v133\n"
"v_add_f32 v126, v126, v134\n"
"v_add_f32 v127, v127, v135\n"
"v_add_f32 v124, v124, v112\n"
"v_add_f32 v125, v125, v113\n"
"v_add_f32 v126, v126, v114\n"
"v_add_f32 v127, v127, v115\n"
"v_cvt_pk_bf16_f32 v110, v124, v125\n"
"v_cvt_pk_bf16_f32 v111, v126, v127\n"
"buffer_store_dwordx4 v[108:111], v13, s[40:43], s94 offen sc1\n"
"s_waitcnt vmcnt(0)\n"
"buffer_store_dwordx2 v[0:1], v14, s[40:43], s95 offen offset:0 sc1\n"
"s_branch .LG_end\n"
".LG_end:\n"
"s_waitcnt vmcnt(0) lgkmcnt(0)\n"
"s_endpgm\n"
      :: "{v2}"((unsigned)(lds0 + ((wid + 3) & 3) * 18592 + trp)), "{v3}"((unsigned)(lds0 + ((wid + 2) & 3) * 18592 + trp)), "{v4}"((unsigned)(lds0 + ((wid + 1) & 3) * 18592 + trp)), "{v5}"((unsigned)(lane * 16 + f0 * 1024 + ((0 + rot) % 7) * 1024 + 134316032)), "{v6}"((unsigned)(lane * 16 + f0 * 1024 + ((1 + rot) % 7) * 1024 + 134316032)), "{v7}"((unsigned)(lane * 16 + f0 * 1024 + ((2 + rot) % 7) * 1024 + 134316032)), "{v8}"((unsigned)(lane * 16 + f0 * 1024 + ((3 + rot) % 7) * 1024 + 134316032)), "{v9}"((unsigned)(lane * 16 + f0 * 1024 + ((4 + rot) % 7) * 1024 + 134316032)), "{v10}"((unsigned)(lane * 16 + f0 * 1024 + ((5 + rot) % 7) * 1024 + 134316032)), "{v11}"((unsigned)(lane * 16 + f0 * 1024 + ((6 + rot) % 7) * 1024 + 134316032)), "{v12}"((unsigned)((f0 + lane % 7) * 8 + kq * 32 + 353024 + G_GL * 1572864)), "{v13}"((unsigned)(lane * 16 + pair * 4096 + kq * 1024 + 212992)), "{v14}"((unsigned)(lane ? DROP : pair * 64 + kq * 8 + 354304)), "{v15}"((unsigned)(lane * 16 + (pg0 + 4) * 4096 + kq * 1024 + 212992)), "{v16}"((unsigned)(lane > 1 ? DROP : (lane ? pg0 + 4 : pair) * 64 + kq * 8 + 354304)), "{v44}"((unsigned)(vT + tsw)), "{v45}"((unsigned)(vT + (tsw ^ 8))), "{v46}"((unsigned)(vD + ((0 + rot) % 7) * 65536)), "{v47}"((unsigned)(vD + ((1 + rot) % 7) * 65536)), "{v48}"((unsigned)(vD + ((2 + rot) % 7) * 65536)), "{v49}"((unsigned)(vD + ((3 + rot) % 7) * 65536)), "{v50}"((unsigned)(vD + ((4 + rot) % 7) * 65536)), "{v51}"((unsigned)(vD + ((5 + rot) % 7) * 65536)), "{v52}"((unsigned)(vD + ((6 + rot) % 7) * 65536)), "{s[40:43]}"(rS), "{s[44:47]}"(rW), "{s[48:49]}"(wsp), "{s50}"((unsigned)(sA)), "{s51}"((unsigned)(sLo)), "{s52}"((unsigned)(sHi)), "{s53}"((unsigned)(sEx)), "{s54}"((unsigned)(sEnt)), "{s55}"((unsigned)(sLd)), "{s[56:57]}"(wsb + (sLo & 0x3ffffff)), "{s59}"((unsigned)(lds0 + wid * 18592)), "{s62}"((unsigned)(sW0 + 64 * ((wid + 1) & 3))), "{s63}"((unsigned)(sW0 + 64 * ((wid + 2) & 3))), "{s64}"((unsigned)(sW0 + 64 * ((wid + 3) & 3))), "{s65}"((unsigned)(sW0 + 64 * ((wid + 4) & 3))), "{s66}"((unsigned)(sW0 + 256))
      : "v255", "a255", "s95", "vcc", "scc", "memory");

    } else {
      const int v16 = kq * 4 + wid;
      const unsigned sEx = sHi + (J_EX + (set == 1 && !J_TL)) * 3 * RS, sEnt = grp - 4;
      const u32x4 rO = desc(a.out, 2048 * 2048), bia0 = *(const u32x4*)(a.b2 + v16 * (32 << JJ) + lg * 8), bia1 = *(const u32x4*)(a.b2 + (JJ ? 32 : 512) + v16 * (32 << JJ) + lg * 8);
      const unsigned se = lg == (li >> 2) ? 0x3f80u << (16 * (li & 1)) : 0u, sel0 = (li & 2) ? 0u : se, sel1 = (li & 2) ? se : 0u;
      const u64 ep = (set == 2 && idx == 27 && wid == 0) ? (u64)a.ws : 0ull;
    asm volatile(
"s_nop 4\n"
"v_mov_b32 v186, 0\n"
"global_load_dwordx2 v[0:1], v186, s[48:49] sc1\n"
"v_mov_b32 v188, 0x3f803f80\n"
"v_mov_b32 v189, 0x3f803f80\n"
"v_mov_b32 v190, 0x3f803f80\n"
"v_mov_b32 v191, 0x3f803f80\n"
"s_add_u32 m0, s55, 0\n"
"s_add_u32 s60, s86, 0\n"
"buffer_load_dwordx4 v46, s[44:47], s60 offen lds\n"
"s_add_u32 m0, s55, 1024\n"
"s_add_u32 s60, s86, 32768\n"
"buffer_load_dwordx4 v46, s[44:47], s60 offen lds\n"
"s_add_u32 m0, s55, 2048\n"
"s_add_u32 s60, s87, 0\n"
"buffer_load_dwordx4 v46, s[44:47], s60 offen lds\n"
"s_add_u32 m0, s55, 3072\n"
"s_add_u32 s60, s87, 32768\n"
"buffer_load_dwordx4 v46, s[44:47], s60 offen lds\n"
"s_add_u32 m0, s55, 4096\n"
"s_add_u32 s60, s88, 0\n"
"buffer_load_dwordx4 v46, s[44:47], s60 offen lds\n"
"s_add_u32 m0, s55, 5120\n"
"s_add_u32 s60, s88, 32768\n"
"buffer_load_dwordx4 v46, s[44:47], s60 offen lds\n"
"s_add_u32 m0, s55, 6144\n"
"s_add_u32 s60, s89, 0\n"
"buffer_load_dwordx4 v46, s[44:47], s60 offen lds\n"
"s_add_u32 m0, s55, 7168\n"
"s_add_u32 s60, s89, 32768\n"
"buffer_load_dwordx4 v46, s[44:47], s60 offen lds\n"
"s_add_u32 m0, s55, 8192\n"
"s_add_u32 s60, s86, 0\n"
"buffer_load_dwordx4 v47, s[44:47], s60 offen lds\n"
"s_add_u32 m0, s55, 9216\n"
"s_add_u32 s60, s86, 32768\n"
"buffer_load_dwordx4 v47, s[44:47], s60 offen lds\n"
"s_add_u32 m0, s55, 10240\n"
"s_add_u32 s60, s87, 0\n"
"buffer_load_dwordx4 v47, s[44:47], s60 offen lds\n"
"s_add_u32 m0, s55, 11264\n"
"s_add_u32 s60, s87, 32768\n"
"buffer_load_dwordx4 v47, s[44:47], s60 offen lds\n"
"s_add_u32 m0, s55, 12288\n"
"s_add_u32 s60, s88, 0\n"
"buffer_load_dwordx4 v47, s[44:47], s60 offen lds\n"
"s_add_u32 m0, s55, 13312\n"
"s_add_u32 s60, s88, 32768\n"
"buffer_load_dwordx4 v47, s[44:47], s60 offen lds\n"
"s_add_u32 m0, s55, 14336\n"
"s_add_u32 s60, s89, 0\n"
"buffer_load_dwordx4 v47, s[44:47], s60 offen lds\n"
"s_add_u32 m0, s55, 15360\n"
"s_add_u32 s60, s89, 32768\n"
"buffer_load_dwordx4 v47, s[44:47], s60 offen lds\n"
"s_add_u32 m0, s55, 16384\n"
"s_add_u32 s60, s86, 0\n"
"buffer_load_dwordx4 v48, s[44:47], s60 offen lds\n"
"s_add_u32 m0, s55, 17408\n"
"s_add_u32 s60, s86, 32768\n"
"buffer_load_dwordx4 v48, s[44:47], s60 offen lds\n"
"s_add_u32 m0, s55, 18432\n"
"s_add_u32 s60, s87, 0\n"
"buffer_load_dwordx4 v48, s[44:47], s60 offen lds\n"
"s_add_u32 m0, s55, 19456\n"
"s_add_u32 s60, s87, 32768\n"
"buffer_load_dwordx4 v48, s[44:47], s60 offen lds\n"
"s_add_u32 m0, s55, 20480\n"
"s_add_u32 s60, s88, 0\n"
"buffer_load_dwordx4 v48, s[44:47], s60 offen lds\n"
"s_add_u32 m0, s55, 21504\n"
"s_add_u32 s60, s88, 32768\n"
"buffer_load_dwordx4 v48, s[44:47], s60 offen lds\n"
"s_add_u32 m0, s55, 22528\n"
"s_add_u32 s60, s89, 0\n"
"buffer_load_dwordx4 v48, s[44:47], s60 offen lds\n"
"s_add_u32 m0, s55, 23552\n"
"s_add_u32 s60, s89, 32768\n"
"buffer_load_dwordx4 v48, s[44:47], s60 offen lds\n"
"s_add_u32 m0, s55, 24576\n"
"s_add_u32 s60, s86, 0\n"
"buffer_load_dwordx4 v49, s[44:47], s60 offen lds\n"
"s_add_u32 m0, s55, 25600\n"
"s_add_u32 s60, s86, 32768\n"
"buffer_load_dwordx4 v49, s[44:47], s60 offen lds\n"
"s_add_u32 m0, s55, 26624\n"
"s_add_u32 s60, s87, 0\n"
"buffer_load_dwordx4 v49, s[44:47], s60 offen lds\n"
"s_add_u32 m0, s55, 27648\n"
"s_add_u32 s60, s87, 32768\n"
"buffer_load_dwordx4 v49, s[44:47], s60 offen lds\n"
"s_add_u32 m0, s55, 28672\n"
"s_add_u32 s60, s88, 0\n"
"buffer_load_dwordx4 v49, s[44:47], s60 offen lds\n"
"s_add_u32 m0, s55, 29696\n"
"s_add_u32 s60, s88, 32768\n"
"buffer_load_dwordx4 v49, s[44:47], s60 offen lds\n"
"s_add_u32 m0, s55, 30720\n"
"s_add_u32 s60, s89, 0\n"
"buffer_load_dwordx4 v49, s[44:47], s60 offen lds\n"
"s_add_u32 m0, s55, 31744\n"
"s_add_u32 s60, s89, 32768\n"
"buffer_load_dwordx4 v49, s[44:47], s60 offen lds\n"
"s_add_u32 m0, s55, 32768\n"
"s_add_u32 s60, s86, 0\n"
"buffer_load_dwordx4 v50, s[44:47], s60 offen lds\n"
"s_add_u32 m0, s55, 33792\n"
"s_add_u32 s60, s86, 32768\n"
"buffer_load_dwordx4 v50, s[44:47], s60 offen lds\n"
"s_add_u32 m0, s55, 34816\n"
"s_add_u32 s60, s87, 0\n"
"buffer_load_dwordx4 v50, s[44:47], s60 offen lds\n"
"s_add_u32 m0, s55, 35840\n"
"s_add_u32 s60, s87, 32768\n"
"buffer_load_dwordx4 v50, s[44:47], s60 offen lds\n"
"s_add_u32 m0, s55, 36864\n"
"s_add_u32 s60, s88, 0\n"
"buffer_load_dwordx4 v50, s[44:47], s60 offen lds\n"
"s_add_u32 m0, s55, 37888\n"
"s_add_u32 s60, s88, 32768\n"
"buffer_load_dwordx4 v50, s[44:47], s60 offen lds\n"
"s_add_u32 m0, s55, 38912\n"
"s_add_u32 s60, s89, 0\n"
"buffer_load_dwordx4 v50, s[44:47], s60 offen lds\n"
"s_add_u32 m0, s55, 39936\n"
"s_add_u32 s60, s89, 32768\n"
"buffer_load_dwordx4 v50, s[44:47], s60 offen lds\n"
"s_waitcnt vmcnt(38)\n"
"ds_read_b64_tr_b16 a[0:1], v44 offset:0\n"
"ds_read_b64_tr_b16 a[2:3], v44 offset:256\n"
"ds_read_b64_tr_b16 a[28:29], v45 offset:0\n"
"ds_read_b64_tr_b16 a[30:31], v45 offset:256\n"
"v_add_u32 v0, 2, v0\n"
"v_add_u32 v1, 0x9e3779b9, v1\n"
"v_or_b32 v0, 1, v0\n"
"v_readfirstlane_b32 s49, v1\n"
"v_readfirstlane_b32 s48, v0\n"
"s_waitcnt vmcnt(36)\n"
"ds_read_b64_tr_b16 a[56:57], v44 offset:2048\n"
"ds_read_b64_tr_b16 a[58:59], v44 offset:2304\n"
"ds_read_b64_tr_b16 a[84:85], v45 offset:2048\n"
"ds_read_b64_tr_b16 a[86:87], v45 offset:2304\n"
"s_waitcnt lgkmcnt(4)\n"
"s_add_u32 m0, s55, 0\n"
"s_add_u32 s60, s86, 0\n"
"buffer_load_dwordx4 v51, s[44:47], s60 offen lds\n"
"s_add_u32 m0, s55, 1024\n"
"s_add_u32 s60, s86, 32768\n"
"buffer_load_dwordx4 v51, s[44:47], s60 offen lds\n"
"s_waitcnt vmcnt(36)\n"
"ds_read_b64_tr_b16 a[112:113], v44 offset:4096\n"
"ds_read_b64_tr_b16 a[114:115], v44 offset:4352\n"
"ds_read_b64_tr_b16 a[140:141], v45 offset:4096\n"
"ds_read_b64_tr_b16 a[142:143], v45 offset:4352\n"
"s_waitcnt lgkmcnt(4)\n"
"s_add_u32 m0, s55, 2048\n"
"s_add_u32 s60, s87, 0\n"
"buffer_load_dwordx4 v51, s[44:47], s60 offen lds\n"
"s_add_u32 m0, s55, 3072\n"
"s_add_u32 s60, s87, 32768\n"
"buffer_load_dwordx4 v51, s[44:47], s60 offen lds\n"
"s_waitcnt vmcnt(36)\n"
"ds_read_b64_tr_b16 a[168:169], v44 offset:6144\n"
"ds_read_b64_tr_b16 a[170:171], v44 offset:6400\n"
"ds_read_b64_tr_b16 a[196:197], v45 offset:6144\n"
"ds_read_b64_tr_b16 a[198:199], v45 offset:6400\n"
"s_waitcnt lgkmcnt(4)\n"
"s_add_u32 m0, s55, 4096\n"
"s_add_u32 s60, s88, 0\n"
"buffer_load_dwordx4 v51, s[44:47], s60 offen lds\n"
"s_add_u32 m0, s55, 5120\n"
"s_add_u32 s60, s88, 32768\n"
"buffer_load_dwordx4 v51, s[44:47], s60 offen lds\n"
"s_waitcnt vmcnt(36)\n"
"ds_read_b64_tr_b16 a[4:5], v44 offset:8192\n"
"ds_read_b64_tr_b16 a[6:7], v44 offset:8448\n"
"ds_read_b64_tr_b16 a[32:33], v45 offset:8192\n"
"ds_read_b64_tr_b16 a[34:35], v45 offset:8448\n"
"s_waitcnt lgkmcnt(4)\n"
"s_add_u32 m0, s55, 6144\n"
"s_add_u32 s60, s89, 0\n"
"buffer_load_dwordx4 v51, s[44:47], s60 offen lds\n"
"s_add_u32 m0, s55, 7168\n"
"s_add_u32 s60, s89, 32768\n"
"buffer_load_dwordx4 v51, s[44:47], s60 offen lds\n"
"s_waitcnt vmcnt(36)\n"
"ds_read_b64_tr_b16 a[60:61], v44 offset:10240\n"
"ds_read_b64_tr_b16 a[62:63], v44 offset:10496\n"
"ds_read_b64_tr_b16 a[88:89], v45 offset:10240\n"
"ds_read_b64_tr_b16 a[90:91], v45 offset:10496\n"
"s_waitcnt lgkmcnt(4)\n"
"s_add_u32 m0, s55, 8192\n"
"s_add_u32 s60, s86, 0\n"
"buffer_load_dwordx4 v52, s[44:47], s60 offen lds\n"
"s_add_u32 m0, s55, 9216\n"
"s_add_u32 s60, s86, 32768\n"
"buffer_load_dwordx4 v52, s[44:47], s60 offen lds\n"
"s_waitcnt vmcnt(36)\n"
"ds_read_b64_tr_b16 a[116:117], v44 offset:12288\n"
"ds_read_b64_tr_b16 a[118:119], v44 offset:12544\n"
"ds_read_b64_tr_b16 a[144:145], v45 offset:12288\n"
"ds_read_b64_tr_b16 a[146:147], v45 offset:12544\n"
"s_waitcnt lgkmcnt(4)\n"
"s_add_u32 m0, s55, 10240\n"
"s_add_u32 s60, s87, 0\n"
"buffer_load_dwordx4 v52, s[44:47], s60 offen lds\n"
"s_add_u32 m0, s55, 11264\n"
"s_add_u32 s60, s87, 32768\n"
"buffer_load_dwordx4 v52, s[44:47], s60 offen lds\n"
"s_waitcnt vmcnt(36)\n"
"ds_read_b64_tr_b16 a[172:173], v44 offset:14336\n"
"ds_read_b64_tr_b16 a[174:175], v44 offset:14592\n"
"ds_read_b64_tr_b16 a[200:201], v45 offset:14336\n"
"ds_read_b64_tr_b16 a[202:203], v45 offset:14592\n"
"s_waitcnt lgkmcnt(4)\n"
"s_add_u32 m0, s55, 12288\n"
"s_add_u32 s60, s88, 0\n"
"buffer_load_dwordx4 v52, s[44:47], s60 offen lds\n"
"s_add_u32 m0, s55, 13312\n"
"s_add_u32 s60, s88, 32768\n"
"buffer_load_dwordx4 v52, s[44:47], s60 offen lds\n"
"s_waitcnt vmcnt(36)\n"
"ds_read_b64_tr_b16 a[8:9], v44 offset:16384\n"
"ds_read_b64_tr_b16 a[10:11], v44 offset:16640\n"
"ds_read_b64_tr_b16 a[36:37], v45 offset:16384\n"
"ds_read_b64_tr_b16 a[38:39], v45 offset:16640\n"
"s_waitcnt lgkmcnt(4)\n"
"s_add_u32 m0, s55, 14336\n"
"s_add_u32 s60, s89, 0\n"
"buffer_load_dwordx4 v52, s[44:47], s60 offen lds\n"
"s_add_u32 m0, s55, 15360\n"
"s_add_u32 s60, s89, 32768\n"
"buffer_load_dwordx4 v52, s[44:47], s60 offen lds\n"
"s_waitcnt vmcnt(36)\n"
"ds_read_b64_tr_b16 a[64:65], v44 offset:18432\n"
"ds_read_b64_tr_b16 a[66:67], v44 offset:18688\n"
"ds_read_b64_tr_b16 a[92:93], v45 offset:18432\n"
"ds_read_b64_tr_b16 a[94:95], v45 offset:18688\n"
"s_waitcnt lgkmcnt(4)\n"
"s_waitcnt vmcnt(34)\n"
"ds_read_b64_tr_b16 a[120:121], v44 offset:20480\n"
"ds_read_b64_tr_b16 a[122:123], v44 offset:20736\n"
"ds_read_b64_tr_b16 a[148:149], v45 offset:20480\n"
"ds_read_b64_tr_b16 a[150:151], v45 offset:20736\n"
"s_waitcnt lgkmcnt(4)\n"
"s_waitcnt vmcnt(32)\n"
"ds_read_b64_tr_b16 a[176:177], v44 offset:22528\n"
"ds_read_b64_tr_b16 a[178:179], v44 offset:22784\n"
"ds_read_b64_tr_b16 a[204:205], v45 offset:22528\n"
"ds_read_b64_tr_b16 a[206:207], v45 offset:22784\n"
"s_waitcnt lgkmcnt(4)\n"
"s_waitcnt vmcnt(30)\n"
"ds_read_b64_tr_b16 a[12:13], v44 offset:24576\n"
"ds_read_b64_tr_b16 a[14:15], v44 offset:24832\n"
"ds_read_b64_tr_b16 a[40:41], v45 offset:24576\n"
"ds_read_b64_tr_b16 a[42:43], v45 offset:24832\n"
"s_waitcnt lgkmcnt(4)\n"
"s_waitcnt vmcnt(28)\n"
"ds_read_b64_tr_b16 a[68:69], v44 offset:26624\n"
"ds_read_b64_tr_b16 a[70:71], v44 offset:26880\n"
"ds_read_b64_tr_b16 a[96:97], v45 offset:26624\n"
"ds_read_b64_tr_b16 a[98:99], v45 offset:26880\n"
"s_waitcnt lgkmcnt(4)\n"
"s_waitcnt vmcnt(26)\n"
"ds_read_b64_tr_b16 a[124:125], v44 offset:28672\n"
"ds_read_b64_tr_b16 a[126:127], v44 offset:28928\n"
"ds_read_b64_tr_b16 a[152:153], v45 offset:28672\n"
"ds_read_b64_tr_b16 a[154:155], v45 offset:28928\n"
"s_waitcnt lgkmcnt(4)\n"
"s_waitcnt vmcnt(24)\n"
"ds_read_b64_tr_b16 a[180:181], v44 offset:30720\n"
"ds_read_b64_tr_b16 a[182:183], v44 offset:30976\n"
"ds_read_b64_tr_b16 a[208:209], v45 offset:30720\n"
"ds_read_b64_tr_b16 a[210:211], v45 offset:30976\n"
"s_waitcnt lgkmcnt(4)\n"
"s_waitcnt vmcnt(22)\n"
"ds_read_b64_tr_b16 a[16:17], v44 offset:32768\n"
"ds_read_b64_tr_b16 a[18:19], v44 offset:33024\n"
"ds_read_b64_tr_b16 a[44:45], v45 offset:32768\n"
"ds_read_b64_tr_b16 a[46:47], v45 offset:33024\n"
"s_waitcnt lgkmcnt(4)\n"
"s_waitcnt vmcnt(20)\n"
"ds_read_b64_tr_b16 a[72:73], v44 offset:34816\n"
"ds_read_b64_tr_b16 a[74:75], v44 offset:35072\n"
"ds_read_b64_tr_b16 a[100:101], v45 offset:34816\n"
"ds_read_b64_tr_b16 a[102:103], v45 offset:35072\n"
"s_waitcnt lgkmcnt(4)\n"
"s_waitcnt vmcnt(18)\n"
"ds_read_b64_tr_b16 a[128:129], v44 offset:36864\n"
"ds_read_b64_tr_b16 a[130:131], v44 offset:37120\n"
"ds_read_b64_tr_b16 a[156:157], v45 offset:36864\n"
"ds_read_b64_tr_b16 a[158:159], v45 offset:37120\n"
"s_waitcnt lgkmcnt(4)\n"
"s_waitcnt vmcnt(16)\n"
"ds_read_b64_tr_b16 a[184:185], v44 offset:38912\n"
"ds_read_b64_tr_b16 a[186:187], v44 offset:39168\n"
"ds_read_b64_tr_b16 a[212:213], v45 offset:38912\n"
"ds_read_b64_tr_b16 a[214:215], v45 offset:39168\n"
"s_waitcnt lgkmcnt(4)\n"
"s_waitcnt vmcnt(14)\n"
"ds_read_b64_tr_b16 a[20:21], v44 offset:0\n"
"ds_read_b64_tr_b16 a[22:23], v44 offset:256\n"
"ds_read_b64_tr_b16 a[48:49], v45 offset:0\n"
"ds_read_b64_tr_b16 a[50:51], v45 offset:256\n"
"s_waitcnt lgkmcnt(4)\n"
"s_waitcnt vmcnt(12)\n"
"ds_read_b64_tr_b16 a[76:77], v44 offset:2048\n"
"ds_read_b64_tr_b16 a[78:79], v44 offset:2304\n"
"ds_read_b64_tr_b16 a[104:105], v45 offset:2048\n"
"ds_read_b64_tr_b16 a[106:107], v45 offset:2304\n"
"s_waitcnt lgkmcnt(4)\n"
"s_waitcnt vmcnt(10)\n"
"ds_read_b64_tr_b16 a[132:133], v44 offset:4096\n"
"ds_read_b64_tr_b16 a[134:135], v44 offset:4352\n"
"ds_read_b64_tr_b16 a[160:161], v45 offset:4096\n"
"ds_read_b64_tr_b16 a[162:163], v45 offset:4352\n"
"s_waitcnt lgkmcnt(4)\n"
"s_waitcnt vmcnt(8)\n"
"ds_read_b64_tr_b16 a[188:189], v44 offset:6144\n"
"ds_read_b64_tr_b16 a[190:191], v44 offset:6400\n"
"ds_read_b64_tr_b16 a[216:217], v45 offset:6144\n"
"ds_read_b64_tr_b16 a[218:219], v45 offset:6400\n"
"s_waitcnt lgkmcnt(4)\n"
"s_waitcnt vmcnt(6)\n"
"ds_read_b64_tr_b16 a[24:25], v44 offset:8192\n"
"ds_read_b64_tr_b16 a[26:27], v44 offset:8448\n"
"ds_read_b64_tr_b16 a[52:53], v45 offset:8192\n"
"ds_read_b64_tr_b16 a[54:55], v45 offset:8448\n"
"s_waitcnt lgkmcnt(4)\n"
"s_waitcnt vmcnt(4)\n"
"ds_read_b64_tr_b16 a[80:81], v44 offset:10240\n"
"ds_read_b64_tr_b16 a[82:83], v44 offset:10496\n"
"ds_read_b64_tr_b16 a[108:109], v45 offset:10240\n"
"ds_read_b64_tr_b16 a[110:111], v45 offset:10496\n"
"s_waitcnt lgkmcnt(4)\n"
"s_waitcnt vmcnt(2)\n"
"ds_read_b64_tr_b16 a[136:137], v44 offset:12288\n"
"ds_read_b64_tr_b16 a[138:139], v44 offset:12544\n"
"ds_read_b64_tr_b16 a[164:165], v45 offset:12288\n"
"ds_read_b64_tr_b16 a[166:167], v45 offset:12544\n"
"s_waitcnt lgkmcnt(4)\n"
"s_waitcnt vmcnt(0)\n"
"ds_read_b64_tr_b16 a[192:193], v44 offset:14336\n"
"ds_read_b64_tr_b16 a[194:195], v44 offset:14592\n"
"ds_read_b64_tr_b16 a[220:221], v45 offset:14336\n"
"ds_read_b64_tr_b16 a[222:223], v45 offset:14592\n"
"s_waitcnt lgkmcnt(4)\n"
"s_waitcnt vmcnt(0)\n"
"s_waitcnt lgkmcnt(0)\n"
"s_mov_b32 m0, s59\n"
"s_barrier\n"
"s_mov_b64 s[84:85], s[48:49]\n"
"s_mov_b32 s28, 0x80000000\n"
"s_mov_b32 s29, 0x80000000\n"
"s_mov_b32 s91, 0x80000000\n"
"s_mov_b32 s30, 0x80000000\n"
"s_mov_b32 s30, 0x80000000\n"
"s_mov_b32 s29, 0x80000000\n"
"s_mov_b32 s31, 0x80000000\n"
"s_mov_b32 s28, 0x80000000\n"
"s_mov_b32 s31, 0x80000000\n"
"s_mov_b32 s29, 0x80000000\n"
"s_mov_b32 s90, 0x80000000\n"
"v_mov_b32 v168, 0\n"
"v_mov_b32 v169, 0\n"
"v_mov_b32 v84, 0\n"
"v_mov_b32 v85, 0\n"
"v_mov_b32 v176, 0\n"
"v_mov_b32 v177, 0\n"
"v_mov_b32 v144, 0\n"
"v_mov_b32 v145, 0\n"
"s_cmp_eq_u32 s54, 0\n"
"s_cbranch_scc1 .LJ_1s\n"
"s_cmp_eq_u32 s54, 1\n"
"s_cbranch_scc1 .LJ_2s\n"
"s_cmp_eq_u32 s54, 2\n"
"s_cbranch_scc1 .LJ_3s\n"
".p2align 6\n"
".LJ_top:\n"
"v_mfma_f32_16x16x32_bf16 v[44:47], a[252:255], a[0:3], 0\n"
"s_add_u32 s50, s50, 0x180000\n"
"global_load_dwordx4 a[224:227], v5, s[56:57] offset:0 sc1\n"
"ds_read_b128 v[48:51], v4 offset:4352\n"
"v_mfma_f32_16x16x32_bf16 v[44:47], v[92:95], a[4:7], v[44:47]\n"
"v_cvt_pk_bf16_f32 v174, v168, v169\n"
"v_cvt_pk_bf16_f32 v175, v170, v171\n"
"v_mfma_f32_16x16x32_bf16 v[44:47], v[96:99], a[8:11], v[44:47]\n"
"s_add_u32 s31, s50, 0xff800000\n"
"ds_bpermute_b32 v52, v21, v172\n"
"v_mfma_f32_16x16x32_bf16 v[44:47], v[100:103], a[12:15], v[44:47]\n"
"ds_bpermute_b32 v53, v21, v173\n"
"v_mfma_f32_16x16x32_bf16 v[44:47], v[104:107], a[16:19], v[44:47]\n"
"s_waitcnt lgkmcnt(5)\n"
"buffer_load_dwordx2 v[168:169], v12, s[40:43], s50 offen sc1\n"
"v_mfma_f32_16x16x32_bf16 v[44:47], v[108:111], a[20:23], v[44:47]\n"
"global_load_dwordx4 a[228:231], v6, s[56:57] offset:0 sc1\n"
"v_add_f32 v152, v152, v156\n"
"v_mfma_f32_16x16x32_bf16 v[44:47], v[112:115], a[24:27], v[44:47]\n"
"v_add_f32 v153, v153, v157\n"
"v_add_f32 v154, v154, v158\n"
"v_mfma_f32_16x16x32_bf16 v[56:59], a[252:255], a[28:31], 0\n"
"v_add_f32 v155, v155, v159\n"
"v_add_f32 v152, v152, v160\n"
"v_mfma_f32_16x16x32_bf16 v[56:59], v[92:95], a[32:35], v[56:59]\n"
"ds_write_addtid_b32 v44 offset:0\n"
"v_add_f32 v153, v153, v161\n"
"v_mfma_f32_16x16x32_bf16 v[56:59], v[96:99], a[36:39], v[56:59]\n"
"ds_write_addtid_b32 v45 offset:4640\n"
"v_add_f32 v154, v154, v162\n"
"v_mfma_f32_16x16x32_bf16 v[56:59], v[100:103], a[40:43], v[56:59]\n"
"ds_write_addtid_b32 v46 offset:9344\n"
"global_load_dwordx4 a[232:235], v7, s[56:57] offset:0 sc1\n"
"v_mfma_f32_16x16x32_bf16 v[56:59], v[104:107], a[44:47], v[56:59]\n"
"ds_write_addtid_b32 v47 offset:13984\n"
"v_add_f32 v155, v155, v163\n"
"v_mfma_f32_16x16x32_bf16 v[56:59], v[108:111], a[48:51], v[56:59]\n"
"v_add_f32 v152, v152, v148\n"
"v_add_f32 v153, v153, v149\n"
"v_mfma_f32_16x16x32_bf16 v[56:59], v[112:115], a[52:55], v[56:59]\n"
"v_add_f32 v154, v154, v150\n"
"v_add_f32 v155, v155, v151\n"
"v_mfma_f32_16x16x32_bf16 v[44:47], a[252:255], a[56:59], 0\n"
"s_add_u32 s30, s50, 0xffb80000\n"
"s_and_b32 s29, s30, 0xfbffffff\n"
"v_cvt_pk_bf16_f32 v60, v152, v153\n"
"v_mfma_f32_16x16x32_bf16 v[44:47], v[92:95], a[60:63], v[44:47]\n"
"ds_write_addtid_b32 v56 offset:256\n"
"global_load_dwordx4 a[236:239], v8, s[56:57] offset:0 sc1\n"
"v_mfma_f32_16x16x32_bf16 v[44:47], v[96:99], a[64:67], v[44:47]\n"
"ds_write_addtid_b32 v57 offset:4896\n"
"v_cvt_pk_bf16_f32 v61, v154, v155\n"
"v_mfma_f32_16x16x32_bf16 v[44:47], v[100:103], a[68:71], v[44:47]\n"
"s_waitcnt lgkmcnt(8)\n"
"ds_write_addtid_b32 v58 offset:9600\n"
"v_add_f32 v64, v64, v164\n"
"v_mfma_f32_16x16x32_bf16 v[44:47], v[104:107], a[72:75], v[44:47]\n"
"ds_write_addtid_b32 v59 offset:14240\n"
"v_add_f32 v65, v65, v165\n"
"v_mfma_f32_16x16x32_bf16 v[44:47], v[108:111], a[76:79], v[44:47]\n"
"v_add_f32 v66, v66, v166\n"
"v_add_f32 v67, v67, v167\n"
"v_mfma_f32_16x16x32_bf16 v[44:47], v[112:115], a[80:83], v[44:47]\n"
"global_load_dwordx4 a[240:243], v9, s[56:57] offset:0 sc1\n"
"v_add_f32 v64, v64, v48\n"
"v_mfma_f32_16x16x32_bf16 v[56:59], a[252:255], a[84:87], 0\n"
"s_waitcnt vmcnt(11)\n"
"buffer_store_dwordx2 v[0:1], v14, s[40:43], s91 offen offset:0 sc1\n"
"v_add_f32 v65, v65, v49\n"
"v_mfma_f32_16x16x32_bf16 v[56:59], v[92:95], a[88:91], v[56:59]\n"
"ds_write_addtid_b32 v44 offset:512\n"
"v_add_f32 v66, v66, v50\n"
"v_mfma_f32_16x16x32_bf16 v[56:59], v[96:99], a[92:95], v[56:59]\n"
"ds_write_addtid_b32 v45 offset:5152\n"
"v_add_f32 v67, v67, v51\n"
"v_mfma_f32_16x16x32_bf16 v[56:59], v[100:103], a[96:99], v[56:59]\n"
"ds_write_addtid_b32 v46 offset:9856\n"
"v_add_f32 v64, v64, v144\n"
"v_mfma_f32_16x16x32_bf16 v[56:59], v[104:107], a[100:103], v[56:59]\n"
"ds_write_addtid_b32 v47 offset:14496\n"
"global_load_dwordx4 a[244:247], v10, s[56:57] offset:0 sc1\n"
"v_mfma_f32_16x16x32_bf16 v[56:59], v[108:111], a[104:107], v[56:59]\n"
"v_add_f32 v65, v65, v145\n"
"v_add_f32 v66, v66, v146\n"
"v_mfma_f32_16x16x32_bf16 v[56:59], v[112:115], a[108:111], v[56:59]\n"
"v_add_f32 v67, v67, v147\n"
"v_cvt_pk_bf16_f32 v62, v64, v65\n"
"v_mfma_f32_16x16x32_bf16 v[44:47], a[252:255], a[112:115], 0\n"
"s_cmp_lt_u32 s31, 0x8000000\n"
"s_cselect_b32 s31, 0x80000000, s31\n"
"v_cvt_pk_bf16_f32 v63, v66, v67\n"
"v_mfma_f32_16x16x32_bf16 v[44:47], v[92:95], a[116:119], v[44:47]\n"
"buffer_store_dwordx4 v[60:63], v13, s[40:43], s29 offen sc1\n"
"ds_write_addtid_b32 v56 offset:768\n"
"ds_bpermute_b32 v54, v21, v174\n"
"v_mfma_f32_16x16x32_bf16 v[44:47], v[96:99], a[120:123], v[44:47]\n"
"ds_write_addtid_b32 v57 offset:5408\n"
"global_load_dwordx4 a[248:251], v11, s[56:57] offset:0 sc1\n"
"v_mfma_f32_16x16x32_bf16 v[44:47], v[100:103], a[124:127], v[44:47]\n"
"ds_write_addtid_b32 v58 offset:10112\n"
"v_mov_b32 v68, v30\n"
"v_mfma_f32_16x16x32_bf16 v[44:47], v[104:107], a[128:131], v[44:47]\n"
"ds_write_addtid_b32 v59 offset:14752\n"
"v_mov_b32 v69, v31\n"
"v_mfma_f32_16x16x32_bf16 v[44:47], v[108:111], a[132:135], v[44:47]\n"
"buffer_load_dwordx2 v[144:145], v15, s[40:43], s31 offen offset:0 sc1\n"
"v_mfma_f32_16x16x32_bf16 v[44:47], v[112:115], a[136:139], v[44:47]\n"
"ds_bpermute_b32 v55, v21, v175\n"
"v_mfma_f32_16x16x32_bf16 v[48:51], a[252:255], a[140:143], 0\n"
"v_mov_b32 v90, v32\n"
"v_mov_b32 v91, v33\n"
"v_mfma_f32_16x16x32_bf16 v[48:51], v[92:95], a[144:147], v[48:51]\n"
"s_waitcnt vmcnt(11)\n"
"ds_write_addtid_b32 v44 offset:1024\n"
"v_mfma_f32_16x16x32_bf16 v[56:59], v[38:41], v[68:71], 0\n"
"v_mfma_f32_16x16x32_bf16 v[56:59], v[38:41], v[76:79], v[56:59]\n"
"v_mfma_f32_16x16x32_bf16 v[56:59], v[38:41], v[84:87], v[56:59]\n"
"v_mfma_f32_16x16x32_bf16 v[60:63], v[38:41], v[72:75], 0\n"
"v_mfma_f32_16x16x32_bf16 v[60:63], v[38:41], v[80:83], v[60:63]\n"
"v_mfma_f32_16x16x32_bf16 v[60:63], v[38:41], v[88:91], v[60:63]\n"
"v_cvt_pk_bf16_f32 v64, v56, v57\n"
"v_cvt_pk_bf16_f32 v65, v58, v59\n"
"v_mfma_f32_16x16x32_bf16 v[48:51], v[96:99], a[148:151], v[48:51]\n"
"ds_write_addtid_b32 v45 offset:5664\n"
"v_mfma_f32_16x16x32_bf16 v[48:51], v[100:103], a[152:155], v[48:51]\n"
"ds_write_addtid_b32 v46 offset:10368\n"
"v_cvt_pk_bf16_f32 v66, v60, v61\n"
"v_mfma_f32_16x16x32_bf16 v[48:51], v[104:107], a[156:159], v[48:51]\n"
"ds_write_addtid_b32 v47 offset:15008\n"
"v_cvt_pk_bf16_f32 v67, v62, v63\n"
"v_mfma_f32_16x16x32_bf16 v[56:59], v[172:175], v[172:175], 0\n"
"v_mfma_f32_16x16x32_bf16 v[68:71], v[34:37], v[172:175], 0\n"
"v_mfma_f32_16x16x32_bf16 v[56:59], v[64:67], v[64:67], v[56:59]\n"
"v_mfma_f32_16x16x32_bf16 v[68:71], v[34:37], v[64:67], v[68:71]\n"
"v_mfma_f32_16x16x32_bf16 v[48:51], v[108:111], a[160:163], v[48:51]\n"
"s_waitcnt lgkmcnt(4)\n"
"buffer_store_dwordx4 v[52:55], v18, s[76:79], s28 offen sc1\n"
"ds_bpermute_b32 v44, v21, v64\n"
"v_cndmask_b32_e64 v56, v56, v57, s[64:65]\n"
"v_mfma_f32_16x16x32_bf16 v[48:51], v[112:115], a[164:167], v[48:51]\n"
"ds_bpermute_b32 v45, v21, v65\n"
"v_mfma_f32_16x16x32_bf16 v[52:55], a[168:171], a[252:255], 0\n"
"ds_bpermute_b32 v46, v21, v66\n"
"v_mfma_f32_16x16x32_bf16 v[52:55], a[172:175], v[92:95], v[52:55]\n"
"ds_write_addtid_b32 v48 offset:1280\n"
"v_cndmask_b32_e64 v58, v58, v59, s[64:65]\n"
"v_mfma_f32_16x16x32_bf16 v[52:55], a[176:179], v[96:99], v[52:55]\n"
"ds_write_addtid_b32 v49 offset:5920\n"
"v_cndmask_b32_e64 v69, v56, v58, s[66:67]\n"
"v_mfma_f32_16x16x32_bf16 v[52:55], a[180:183], v[100:103], v[52:55]\n"
"buffer_store_dwordx2 v[68:69], v23, s[40:43], s90 offen offset:0 sc1\n"
"ds_write_addtid_b32 v50 offset:10624\n"
"v_mfma_f32_16x16x32_bf16 v[52:55], a[184:187], v[104:107], v[52:55]\n"
"s_add_u32 s58, s50, 0x180000\n"
"ds_write_addtid_b32 v51 offset:15264\n"
"v_mfma_f32_16x16x32_bf16 v[52:55], a[188:191], v[108:111], v[52:55]\n"
"s_and_b32 s58, s58, 0x3ffffff\n"
"ds_bpermute_b32 v47, v21, v67\n"
"v_mfma_f32_16x16x32_bf16 v[52:55], a[192:195], v[112:115], v[52:55]\n"
"s_add_u32 s56, s40, s58\n"
"v_mfma_f32_16x16x32_bf16 v[48:51], a[196:199], a[252:255], 0\n"
"s_addc_u32 s57, s41, 0\n"
"v_mfma_f32_16x16x32_bf16 v[48:51], a[200:203], v[92:95], v[48:51]\n"
"s_waitcnt lgkmcnt(1)\n"
"v_mfma_f32_16x16x32_bf16 v[48:51], a[204:207], v[96:99], v[48:51]\n"
"s_barrier\n"
"v_mfma_f32_16x16x32_bf16 v[48:51], a[208:211], v[100:103], v[48:51]\n"
"s_waitcnt vmcnt(11)\n"
"ds_read_b128 v[56:59], v2 offset:0\n"
"v_cmp_ne_u64 vcc, s[48:49], v[168:169]\n"
"v_mfma_f32_16x16x32_bf16 v[48:51], a[212:215], v[104:107], v[48:51]\n"
"s_waitcnt lgkmcnt(1)\n"
"buffer_store_dwordx4 v[44:47], v19, s[76:79], s28 offen sc1\n"
"ds_read_b128 v[60:63], v3 offset:512\n"
"ds_read_b128 v[64:67], v4 offset:1024\n"
"v_mfma_f32_16x16x32_bf16 v[48:51], a[216:219], v[108:111], v[48:51]\n"
"s_cbranch_vccnz .LJ_1s\n"
".LJ_1r:\n"
"v_mfma_f32_16x16x32_bf16 v[48:51], a[220:223], v[112:115], v[48:51]\n"
"ds_read_b128 v[44:47], v2 offset:256\n"
"ds_read_b128 v[68:71], v3 offset:768\n"
"v_mfma_f32_16x16x32_bf16 v[72:75], v[116:119], a[0:3], 0\n"
"s_add_u32 s50, s50, 0x180000\n"
"global_load_dwordx4 a[252:255], v5, s[56:57] offset:0 sc1\n"
"ds_read_b128 v[76:79], v4 offset:1280\n"
"v_mfma_f32_16x16x32_bf16 v[72:75], v[120:123], a[4:7], v[72:75]\n"
"v_mfma_f32_16x16x32_bf16 v[72:75], v[124:127], a[8:11], v[72:75]\n"
"v_mfma_f32_16x16x32_bf16 v[72:75], v[128:131], a[12:15], v[72:75]\n"
"v_mfma_f32_16x16x32_bf16 v[72:75], v[132:135], a[16:19], v[72:75]\n"
"s_waitcnt lgkmcnt(3)\n"
"buffer_load_dwordx2 v[84:85], v12, s[40:43], s50 offen sc1\n"
"v_mfma_f32_16x16x32_bf16 v[72:75], v[136:139], a[20:23], v[72:75]\n"
"global_load_dwordx4 v[92:95], v6, s[56:57] offset:0 sc1\n"
"v_add_f32 v56, v56, v60\n"
"v_mfma_f32_16x16x32_bf16 v[72:75], v[140:143], a[24:27], v[72:75]\n"
"v_add_f32 v57, v57, v61\n"
"v_add_f32 v58, v58, v62\n"
"v_mfma_f32_16x16x32_bf16 v[80:83], v[116:119], a[28:31], 0\n"
"v_add_f32 v59, v59, v63\n"
"v_add_f32 v56, v56, v64\n"
"v_mfma_f32_16x16x32_bf16 v[80:83], v[120:123], a[32:35], v[80:83]\n"
"ds_write_addtid_b32 v72 offset:1536\n"
"v_add_f32 v57, v57, v65\n"
"v_mfma_f32_16x16x32_bf16 v[80:83], v[124:127], a[36:39], v[80:83]\n"
"ds_write_addtid_b32 v73 offset:6176\n"
"v_add_f32 v58, v58, v66\n"
"v_mfma_f32_16x16x32_bf16 v[80:83], v[128:131], a[40:43], v[80:83]\n"
"ds_write_addtid_b32 v74 offset:10880\n"
"global_load_dwordx4 v[96:99], v7, s[56:57] offset:0 sc1\n"
"v_mfma_f32_16x16x32_bf16 v[80:83], v[132:135], a[44:47], v[80:83]\n"
"ds_write_addtid_b32 v75 offset:15520\n"
"v_add_f32 v59, v59, v67\n"
"v_mfma_f32_16x16x32_bf16 v[80:83], v[136:139], a[48:51], v[80:83]\n"
"v_add_f32 v56, v56, v52\n"
"v_add_f32 v57, v57, v53\n"
"v_mfma_f32_16x16x32_bf16 v[80:83], v[140:143], a[52:55], v[80:83]\n"
"v_add_f32 v58, v58, v54\n"
"v_add_f32 v59, v59, v55\n"
"v_mfma_f32_16x16x32_bf16 v[60:63], v[116:119], a[56:59], 0\n"
"s_add_u32 s28, s50, 0xffb80000\n"
"s_and_b32 s29, s28, 0xfbffffff\n"
"v_cvt_pk_bf16_f32 v64, v56, v57\n"
"v_mfma_f32_16x16x32_bf16 v[60:63], v[120:123], a[60:63], v[60:63]\n"
"ds_write_addtid_b32 v80 offset:1792\n"
"global_load_dwordx4 v[100:103], v8, s[56:57] offset:0 sc1\n"
"v_mfma_f32_16x16x32_bf16 v[60:63], v[124:127], a[64:67], v[60:63]\n"
"ds_write_addtid_b32 v81 offset:6432\n"
"v_cvt_pk_bf16_f32 v65, v58, v59\n"
"v_mfma_f32_16x16x32_bf16 v[60:63], v[128:131], a[68:71], v[60:63]\n"
"s_waitcnt lgkmcnt(6)\n"
"ds_write_addtid_b32 v82 offset:11136\n"
"v_add_f32 v44, v44, v68\n"
"v_mfma_f32_16x16x32_bf16 v[60:63], v[132:135], a[72:75], v[60:63]\n"
"ds_write_addtid_b32 v83 offset:15776\n"
"v_add_f32 v45, v45, v69\n"
"v_mfma_f32_16x16x32_bf16 v[60:63], v[136:139], a[76:79], v[60:63]\n"
"v_add_f32 v46, v46, v70\n"
"v_add_f32 v47, v47, v71\n"
"v_mfma_f32_16x16x32_bf16 v[60:63], v[140:143], a[80:83], v[60:63]\n"
"global_load_dwordx4 v[104:107], v9, s[56:57] offset:0 sc1\n"
"v_add_f32 v44, v44, v76\n"
"v_mfma_f32_16x16x32_bf16 v[52:55], v[116:119], a[84:87], 0\n"
"s_waitcnt vmcnt(11)\n"
"v_add_f32 v45, v45, v77\n"
"v_add_f32 v46, v46, v78\n"
"v_mfma_f32_16x16x32_bf16 v[52:55], v[120:123], a[88:91], v[52:55]\n"
"ds_write_addtid_b32 v60 offset:2048\n"
"v_add_f32 v47, v47, v79\n"
"v_mfma_f32_16x16x32_bf16 v[52:55], v[124:127], a[92:95], v[52:55]\n"
"ds_write_addtid_b32 v61 offset:6688\n"
"v_add_f32 v44, v44, v48\n"
"v_mfma_f32_16x16x32_bf16 v[52:55], v[128:131], a[96:99], v[52:55]\n"
"ds_write_addtid_b32 v62 offset:11392\n"
"v_add_f32 v45, v45, v49\n"
"v_mfma_f32_16x16x32_bf16 v[52:55], v[132:135], a[100:103], v[52:55]\n"
"s_waitcnt vmcnt(9)\n"
"ds_write_addtid_b32 v63 offset:16032\n"
"global_load_dwordx4 v[108:111], v10, s[56:57] offset:0 sc1\n"
"v_mfma_f32_16x16x32_bf16 v[52:55], v[136:139], a[104:107], v[52:55]\n"
"v_add_f32 v46, v46, v50\n"
"v_add_f32 v47, v47, v51\n"
"v_mfma_f32_16x16x32_bf16 v[52:55], v[140:143], a[108:111], v[52:55]\n"
"v_cvt_pk_bf16_f32 v66, v44, v45\n"
"v_cvt_pk_bf16_f32 v67, v46, v47\n"
"v_mfma_f32_16x16x32_bf16 v[68:71], v[116:119], a[112:115], 0\n"
"buffer_store_dwordx4 v[64:67], v13, s[40:43], s29 offen sc1\n"
"v_mfma_f32_16x16x32_bf16 v[68:71], v[120:123], a[116:119], v[68:71]\n"
"buffer_store_dwordx2 v[0:1], v14, s[40:43], s30 offen offset:0 sc1\n"
"ds_write_addtid_b32 v52 offset:2304\n"
"v_mfma_f32_16x16x32_bf16 v[68:71], v[124:127], a[120:123], v[68:71]\n"
"s_and_b32 s29, s31, 0x3ffffff\n"
"ds_write_addtid_b32 v53 offset:6944\n"
"global_load_dwordx4 v[112:115], v11, s[56:57] offset:0 sc1\n"
"v_mfma_f32_16x16x32_bf16 v[68:71], v[128:131], a[124:127], v[68:71]\n"
"s_add_u32 s62, s40, s29\n"
"ds_write_addtid_b32 v54 offset:11648\n"
"v_cmp_ne_u64 vcc, s[84:85], v[144:145]\n"
"v_mfma_f32_16x16x32_bf16 v[68:71], v[132:135], a[128:131], v[68:71]\n"
"s_addc_u32 s63, s41, 0\n"
"ds_write_addtid_b32 v55 offset:16288\n"
"v_mfma_f32_16x16x32_bf16 v[68:71], v[136:139], a[132:135], v[68:71]\n"
"s_cbranch_vccnz .LJ_4s\n"
".LJ_4r:\n"
"v_mfma_f32_16x16x32_bf16 v[68:71], v[140:143], a[136:139], v[68:71]\n"
"global_load_dwordx4 v[46:49], v16, s[62:63] offset:-4096 sc1\n"
"v_mfma_f32_16x16x32_bf16 v[72:75], v[116:119], a[140:143], 0\n"
"v_mfma_f32_16x16x32_bf16 v[72:75], v[120:123], a[144:147], v[72:75]\n"
"ds_write_addtid_b32 v68 offset:2560\n"
"v_mfma_f32_16x16x32_bf16 v[72:75], v[124:127], a[148:151], v[72:75]\n"
"ds_write_addtid_b32 v69 offset:7200\n"
"v_mfma_f32_16x16x32_bf16 v[72:75], v[128:131], a[152:155], v[72:75]\n"
"ds_write_addtid_b32 v70 offset:11904\n"
"v_mfma_f32_16x16x32_bf16 v[72:75], v[132:135], a[156:159], v[72:75]\n"
"ds_write_addtid_b32 v71 offset:16544\n"
"global_load_dwordx4 v[50:53], v16, s[62:63] offset:-3072 sc1\n"
"v_mfma_f32_16x16x32_bf16 v[72:75], v[136:139], a[160:163], v[72:75]\n"
"v_mfma_f32_16x16x32_bf16 v[72:75], v[140:143], a[164:167], v[72:75]\n"
"v_mfma_f32_16x16x32_bf16 v[68:71], a[168:171], v[116:119], 0\n"
"v_mfma_f32_16x16x32_bf16 v[68:71], a[172:175], v[120:123], v[68:71]\n"
"ds_write_addtid_b32 v72 offset:2816\n"
"v_mfma_f32_16x16x32_bf16 v[68:71], a[176:179], v[124:127], v[68:71]\n"
"ds_write_addtid_b32 v73 offset:7456\n"
"global_load_dwordx4 v[54:57], v16, s[62:63] offset:-2048 sc1\n"
"v_mfma_f32_16x16x32_bf16 v[68:71], a[180:183], v[128:131], v[68:71]\n"
"s_waitcnt vmcnt(13)\n"
"buffer_store_dwordx2 v[0:1], v24, s[40:43], s90 offen offset:0 sc1\n"
"ds_write_addtid_b32 v74 offset:12160\n"
"v_mfma_f32_16x16x32_bf16 v[68:71], a[184:187], v[132:135], v[68:71]\n"
"s_add_u32 s58, s50, 0x180000\n"
"ds_write_addtid_b32 v75 offset:16800\n"
"v_mfma_f32_16x16x32_bf16 v[68:71], a[188:191], v[136:139], v[68:71]\n"
"s_and_b32 s58, s58, 0x3ffffff\n"
"v_mfma_f32_16x16x32_bf16 v[68:71], a[192:195], v[140:143], v[68:71]\n"
"s_add_u32 s56, s40, s58\n"
"v_mfma_f32_16x16x32_bf16 v[144:147], a[196:199], v[116:119], 0\n"
"s_addc_u32 s57, s41, 0\n"
"global_load_dwordx4 v[58:61], v16, s[62:63] offset:-1024 sc1\n"
"v_mfma_f32_16x16x32_bf16 v[144:147], a[200:203], v[120:123], v[144:147]\n"
"s_waitcnt lgkmcnt(0)\n"
"v_mfma_f32_16x16x32_bf16 v[144:147], a[204:207], v[124:127], v[144:147]\n"
"s_barrier\n"
"v_mfma_f32_16x16x32_bf16 v[144:147], a[208:211], v[128:131], v[144:147]\n"
"s_waitcnt vmcnt(13)\n"
"ds_read_b128 v[72:75], v2 offset:1536\n"
"v_cmp_ne_u64 vcc, s[48:49], v[84:85]\n"
"v_mfma_f32_16x16x32_bf16 v[144:147], a[212:215], v[132:135], v[144:147]\n"
"ds_read_b128 v[76:79], v3 offset:2048\n"
"ds_read_b128 v[80:83], v4 offset:2560\n"
"v_mfma_f32_16x16x32_bf16 v[144:147], a[216:219], v[136:139], v[144:147]\n"
"s_cbranch_vccnz .LJ_2s\n"
".LJ_2r:\n"
"v_mfma_f32_16x16x32_bf16 v[144:147], a[220:223], v[140:143], v[144:147]\n"
"ds_read_b128 v[148:151], v2 offset:1792\n"
"ds_read_b128 v[84:87], v3 offset:2304\n"
"v_mfma_f32_16x16x32_bf16 v[88:91], a[224:227], a[0:3], 0\n"
"s_add_u32 s50, s50, 0x180000\n"
"global_load_dwordx4 v[116:119], v5, s[56:57] offset:0 sc1\n"
"ds_read_b128 v[136:139], v4 offset:2816\n"
"v_mfma_f32_16x16x32_bf16 v[88:91], a[228:231], a[4:7], v[88:91]\n"
"global_load_dwordx4 v[62:65], v17, s[62:63] offset:0 sc1\n"
"v_mfma_f32_16x16x32_bf16 v[88:91], a[232:235], a[8:11], v[88:91]\n"
"v_mfma_f32_16x16x32_bf16 v[88:91], a[236:239], a[12:15], v[88:91]\n"
"v_mfma_f32_16x16x32_bf16 v[88:91], a[240:243], a[16:19], v[88:91]\n"
"s_waitcnt lgkmcnt(3)\n"
"buffer_load_dwordx2 v[176:177], v12, s[40:43], s50 offen sc1\n"
"v_mfma_f32_16x16x32_bf16 v[88:91], a[244:247], a[20:23], v[88:91]\n"
"global_load_dwordx4 v[120:123], v6, s[56:57] offset:0 sc1\n"
"v_add_f32 v72, v72, v76\n"
"v_mfma_f32_16x16x32_bf16 v[88:91], a[248:251], a[24:27], v[88:91]\n"
"v_add_f32 v73, v73, v77\n"
"v_add_f32 v74, v74, v78\n"
"v_mfma_f32_16x16x32_bf16 v[132:135], a[224:227], a[28:31], 0\n"
"v_add_f32 v75, v75, v79\n"
"v_add_f32 v72, v72, v80\n"
"v_mfma_f32_16x16x32_bf16 v[132:135], a[228:231], a[32:35], v[132:135]\n"
"ds_write_addtid_b32 v88 offset:3072\n"
"v_add_f32 v73, v73, v81\n"
"v_mfma_f32_16x16x32_bf16 v[132:135], a[232:235], a[36:39], v[132:135]\n"
"ds_write_addtid_b32 v89 offset:7712\n"
"v_add_f32 v74, v74, v82\n"
"v_mfma_f32_16x16x32_bf16 v[132:135], a[236:239], a[40:43], v[132:135]\n"
"ds_write_addtid_b32 v90 offset:12416\n"
"global_load_dwordx4 v[124:127], v7, s[56:57] offset:0 sc1\n"
"v_mfma_f32_16x16x32_bf16 v[132:135], a[240:243], a[44:47], v[132:135]\n"
"ds_write_addtid_b32 v91 offset:17056\n"
"v_add_f32 v75, v75, v83\n"
"v_mfma_f32_16x16x32_bf16 v[132:135], a[244:247], a[48:51], v[132:135]\n"
"v_add_f32 v72, v72, v68\n"
"v_add_f32 v73, v73, v69\n"
"v_mfma_f32_16x16x32_bf16 v[132:135], a[248:251], a[52:55], v[132:135]\n"
"v_add_f32 v74, v74, v70\n"
"v_add_f32 v75, v75, v71\n"
"v_mfma_f32_16x16x32_bf16 v[76:79], a[224:227], a[56:59], 0\n"
"s_add_u32 s91, s50, 0xffb80000\n"
"s_and_b32 s30, s91, 0xfbffffff\n"
"v_cvt_pk_bf16_f32 v140, v72, v73\n"
"v_mfma_f32_16x16x32_bf16 v[76:79], a[228:231], a[60:63], v[76:79]\n"
"ds_write_addtid_b32 v132 offset:3328\n"
"global_load_dwordx4 v[128:131], v8, s[56:57] offset:0 sc1\n"
"v_mfma_f32_16x16x32_bf16 v[76:79], a[232:235], a[64:67], v[76:79]\n"
"ds_write_addtid_b32 v133 offset:7968\n"
"v_cvt_pk_bf16_f32 v141, v74, v75\n"
"v_mfma_f32_16x16x32_bf16 v[76:79], a[236:239], a[68:71], v[76:79]\n"
"s_waitcnt lgkmcnt(6)\n"
"ds_write_addtid_b32 v134 offset:12672\n"
"v_add_f32 v148, v148, v84\n"
"v_mfma_f32_16x16x32_bf16 v[76:79], a[240:243], a[72:75], v[76:79]\n"
"ds_write_addtid_b32 v135 offset:17312\n"
"v_add_f32 v149, v149, v85\n"
"v_mfma_f32_16x16x32_bf16 v[76:79], a[244:247], a[76:79], v[76:79]\n"
"v_add_f32 v150, v150, v86\n"
"v_add_f32 v151, v151, v87\n"
"v_mfma_f32_16x16x32_bf16 v[76:79], a[248:251], a[80:83], v[76:79]\n"
"s_waitcnt vmcnt(13)\n"
"global_load_dwordx4 v[132:135], v9, s[56:57] offset:0 sc1\n"
"v_add_f32 v148, v148, v136\n"
"v_mfma_f32_16x16x32_bf16 v[152:155], a[224:227], a[84:87], 0\n"
"buffer_store_dwordx2 v[0:1], v14, s[40:43], s28 offen offset:0 sc1\n"
"v_add_f32 v149, v149, v137\n"
"v_mfma_f32_16x16x32_bf16 v[152:155], a[228:231], a[88:91], v[152:155]\n"
"ds_write_addtid_b32 v76 offset:3584\n"
"v_add_f32 v150, v150, v138\n"
"v_mfma_f32_16x16x32_bf16 v[152:155], a[232:235], a[92:95], v[152:155]\n"
"ds_write_addtid_b32 v77 offset:8224\n"
"v_add_f32 v151, v151, v139\n"
"v_mfma_f32_16x16x32_bf16 v[152:155], a[236:239], a[96:99], v[152:155]\n"
"ds_write_addtid_b32 v78 offset:12928\n"
"v_add_f32 v148, v148, v144\n"
"v_mfma_f32_16x16x32_bf16 v[152:155], a[240:243], a[100:103], v[152:155]\n"
"ds_write_addtid_b32 v79 offset:17568\n"
"global_load_dwordx4 v[136:139], v10, s[56:57] offset:0 sc1\n"
"v_mfma_f32_16x16x32_bf16 v[152:155], a[244:247], a[104:107], v[152:155]\n"
"v_add_f32 v149, v149, v145\n"
"v_add_f32 v150, v150, v146\n"
"v_mfma_f32_16x16x32_bf16 v[152:155], a[248:251], a[108:111], v[152:155]\n"
"v_add_f32 v151, v151, v147\n"
"v_cvt_pk_bf16_f32 v142, v148, v149\n"
"v_mfma_f32_16x16x32_bf16 v[156:159], a[224:227], a[112:115], 0\n"
"global_load_dwordx4 v[70:73], v16, s[62:63] offset:0 sc1\n"
"v_cvt_pk_bf16_f32 v143, v150, v151\n"
"v_mfma_f32_16x16x32_bf16 v[156:159], a[228:231], a[116:119], v[156:159]\n"
"buffer_store_dwordx4 v[140:143], v13, s[40:43], s30 offen sc1\n"
"ds_write_addtid_b32 v152 offset:3840\n"
"v_mfma_f32_16x16x32_bf16 v[156:159], a[232:235], a[120:123], v[156:159]\n"
"ds_write_addtid_b32 v153 offset:8480\n"
"global_load_dwordx4 v[140:143], v11, s[56:57] offset:0 sc1\n"
"v_mfma_f32_16x16x32_bf16 v[156:159], a[236:239], a[124:127], v[156:159]\n"
"ds_write_addtid_b32 v154 offset:13184\n"
"v_mfma_f32_16x16x32_bf16 v[156:159], a[240:243], a[128:131], v[156:159]\n"
"ds_write_addtid_b32 v155 offset:17824\n"
"v_mfma_f32_16x16x32_bf16 v[156:159], a[244:247], a[132:135], v[156:159]\n"
"s_mov_b32 s90, s31\n"
"global_load_dwordx4 v[74:77], v16, s[62:63] offset:1024 sc1\n"
"v_mov_b32 v44, v26\n"
"v_mfma_f32_16x16x32_bf16 v[156:159], a[248:251], a[136:139], v[156:159]\n"
"v_mov_b32 v45, v27\n"
"v_mov_b32 v66, v28\n"
"v_mfma_f32_16x16x32_bf16 v[144:147], a[224:227], a[140:143], 0\n"
"v_mov_b32 v67, v29\n"
"v_mfma_f32_16x16x32_bf16 v[144:147], a[228:231], a[144:147], v[144:147]\n"
"ds_write_addtid_b32 v156 offset:4096\n"
"v_mfma_f32_16x16x32_bf16 v[144:147], a[232:235], a[148:151], v[144:147]\n"
"ds_write_addtid_b32 v157 offset:8736\n"
"v_mfma_f32_16x16x32_bf16 v[144:147], a[236:239], a[152:155], v[144:147]\n"
"ds_write_addtid_b32 v158 offset:13440\n"
"global_load_dwordx4 v[78:81], v16, s[62:63] offset:2048 sc1\n"
"v_mfma_f32_16x16x32_bf16 v[144:147], a[240:243], a[156:159], v[144:147]\n"
"ds_write_addtid_b32 v159 offset:18080\n"
"v_mfma_f32_16x16x32_bf16 v[144:147], a[244:247], a[160:163], v[144:147]\n"
"v_mfma_f32_16x16x32_bf16 v[144:147], a[248:251], a[164:167], v[144:147]\n"
"v_mfma_f32_16x16x32_bf16 v[148:151], a[168:171], a[224:227], 0\n"
"v_mfma_f32_16x16x32_bf16 v[148:151], a[172:175], a[228:231], v[148:151]\n"
"ds_write_addtid_b32 v144 offset:4352\n"
"global_load_dwordx4 v[82:85], v16, s[62:63] offset:3072 sc1\n"
"v_mfma_f32_16x16x32_bf16 v[148:151], a[176:179], a[232:235], v[148:151]\n"
"ds_write_addtid_b32 v145 offset:8992\n"
"v_mfma_f32_16x16x32_bf16 v[148:151], a[180:183], a[236:239], v[148:151]\n"
"ds_write_addtid_b32 v146 offset:13696\n"
"v_mfma_f32_16x16x32_bf16 v[148:151], a[184:187], a[240:243], v[148:151]\n"
"s_add_u32 s58, s50, 0x180000\n"
"ds_write_addtid_b32 v147 offset:18336\n"
"v_mfma_f32_16x16x32_bf16 v[148:151], a[188:191], a[244:247], v[148:151]\n"
"s_and_b32 s58, s58, 0x3ffffff\n"
"v_mfma_f32_16x16x32_bf16 v[148:151], a[192:195], a[248:251], v[148:151]\n"
"s_add_u32 s56, s40, s58\n"
"global_load_dwordx4 v[86:89], v17, s[62:63] offset:1024 sc1\n"
"v_mfma_f32_16x16x32_bf16 v[144:147], a[196:199], a[224:227], 0\n"
"s_addc_u32 s57, s41, 0\n"
"v_mfma_f32_16x16x32_bf16 v[144:147], a[200:203], a[228:231], v[144:147]\n"
"s_waitcnt lgkmcnt(0)\n"
"v_mfma_f32_16x16x32_bf16 v[144:147], a[204:207], a[232:235], v[144:147]\n"
"s_barrier\n"
"v_mfma_f32_16x16x32_bf16 v[144:147], a[208:211], a[236:239], v[144:147]\n"
"s_waitcnt vmcnt(13)\n"
"ds_read_b128 v[152:155], v2 offset:3072\n"
"v_cmp_ne_u64 vcc, s[48:49], v[176:177]\n"
"v_mfma_f32_16x16x32_bf16 v[144:147], a[212:215], a[240:243], v[144:147]\n"
"ds_read_b128 v[156:159], v3 offset:3584\n"
"ds_read_b128 v[160:163], v4 offset:4096\n"
"v_mfma_f32_16x16x32_bf16 v[164:167], v[38:41], v[44:47], 0\n"
"s_cmp_ge_i32 s90, s80\n"
"s_cbranch_scc1 .LJ_ws\n"
".LJ_wr:\n"
"v_mfma_f32_16x16x32_bf16 v[164:167], v[38:41], v[52:55], v[164:167]\n"
"s_lshr_b32 s28, s90, s81\n"
"v_mfma_f32_16x16x32_bf16 v[164:167], v[38:41], v[60:63], v[164:167]\n"
"s_bitcmp1_b32 s28, 15\n"
"s_cselect_b32 s31, s83, s82\n"
"v_mfma_f32_16x16x32_bf16 v[168:171], v[38:41], v[48:51], 0\n"
"s_add_u32 s28, s28, s31\n"
"v_mfma_f32_16x16x32_bf16 v[168:171], v[38:41], v[56:59], v[168:171]\n"
"v_mfma_f32_16x16x32_bf16 v[168:171], v[38:41], v[64:67], v[168:171]\n"
"v_cvt_pk_bf16_f32 v172, v164, v165\n"
"v_cvt_pk_bf16_f32 v173, v166, v167\n"
"v_mfma_f32_16x16x32_bf16 v[144:147], a[216:219], a[244:247], v[144:147]\n"
"s_cbranch_vccnz .LJ_3s\n"
".LJ_3r:\n"
"v_mfma_f32_16x16x32_bf16 v[144:147], a[220:223], a[248:251], v[144:147]\n"
"ds_read_b128 v[64:67], v2 offset:3328\n"
"ds_read_b128 v[164:167], v3 offset:3840\n"
"s_branch .LJ_top\n"
".LJ_1s:\n"
"s_cmp_ge_u32 s50, s53\n"
"s_cbranch_scc1 .LJ_t0\n"
"s_cmp_ge_u32 s50, s52\n"
"s_cbranch_scc1 .LJ_1r\n"
"s_sub_u32 s60, s50, 0x0\n"
"s_mov_b32 s61, 200000\n"
"s_nop 7\n"
".LJ_1p:\n"
"buffer_load_dwordx2 v[168:169], v12, s[40:43], s60 offen sc1\n"
"s_waitcnt vmcnt(0)\n"
"v_cmp_ne_u64 vcc, s[48:49], v[168:169]\n"
"s_cbranch_vccz .LJ_1px\n"
"s_sleep 1\n"
"s_sub_u32 s61, s61, 1\n"
"s_cbranch_scc0 .LJ_1p\n"
"s_branch .LJ_end\n"
".LJ_1px:\n"
"s_branch .LJ_1r\n"
".LJ_2s:\n"
"s_cmp_ge_u32 s50, s53\n"
"s_cbranch_scc1 .LJ_t1\n"
"s_cmp_ge_u32 s50, s52\n"
"s_cbranch_scc1 .LJ_2r\n"
"s_sub_u32 s60, s50, 0x0\n"
"s_mov_b32 s61, 200000\n"
"s_nop 7\n"
".LJ_2p:\n"
"buffer_load_dwordx2 v[84:85], v12, s[40:43], s60 offen sc1\n"
"s_waitcnt vmcnt(0)\n"
"v_cmp_ne_u64 vcc, s[48:49], v[84:85]\n"
"s_cbranch_vccz .LJ_2px\n"
"s_sleep 1\n"
"s_sub_u32 s61, s61, 1\n"
"s_cbranch_scc0 .LJ_2p\n"
"s_branch .LJ_end\n"
".LJ_2px:\n"
"s_branch .LJ_2r\n"
".LJ_3s:\n"
"s_cmp_ge_u32 s50, s53\n"
"s_cbranch_scc1 .LJ_t2\n"
"s_cmp_ge_u32 s50, s52\n"
"s_cbranch_scc1 .LJ_3r\n"
"s_sub_u32 s60, s50, 0x0\n"
"s_mov_b32 s61, 200000\n"
"s_nop 7\n"
".LJ_3p:\n"
"buffer_load_dwordx2 v[176:177], v12, s[40:43], s60 offen sc1\n"
"s_waitcnt vmcnt(0)\n"
"v_cmp_ne_u64 vcc, s[48:49], v[176:177]\n"
"s_cbranch_vccz .LJ_3px\n"
"s_sleep 1\n"
"s_sub_u32 s61, s61, 1\n"
"s_cbranch_scc0 .LJ_3p\n"
"s_branch .LJ_end\n"
".LJ_3px:\n"
"s_branch .LJ_3r\n"
".LJ_4s:\n"
"s_cmp_ge_i32 s31, 0x10000000\n"
"s_cselect_b64 s[84:85], 0, s[84:85]\n"
"s_cmp_ge_u32 s31, 0x10000000\n"
"s_cbranch_scc1 .LJ_4r\n"
"s_mov_b32 s61, 200000\n"
"s_nop 7\n"
".LJ_4p:\n"
"buffer_load_dwordx2 v[144:145], v15, s[40:43], s31 offen offset:0 sc1\n"
"s_waitcnt vmcnt(0)\n"
"v_cmp_ne_u64 vcc, s[84:85], v[144:145]\n"
"s_cbranch_vccz .LJ_4px\n"
"s_sleep 1\n"
"s_sub_u32 s61, s61, 1\n"
"s_cbranch_scc0 .LJ_4p\n"
"s_branch .LJ_end\n"
".LJ_4px:\n"
"s_branch .LJ_4r\n"
".LJ_ws:\n"
"s_mov_b64 s[76:77], s[68:69]\n"
"s_mov_b32 s78, s70\n"
"s_nop 7\n"
"v_mov_b32 v18, v20\n"
"v_add_u32 v19, 64, v20\n"
"v_mov_b32 v21, v22\n"
"s_mov_b32 s80, 0x7fffffff\n"
"s_mov_b32 s81, 4\n"
"s_mov_b32 s82, 0xff400000\n"
"s_mov_b32 s83, 0xff3f8800\n"
"s_branch .LJ_wr\n"
".LJ_4t0k6s:\n"
"s_cmp_ge_i32 s31, 0x10000000\n"
"s_cselect_b64 s[84:85], 0, s[84:85]\n"
"s_cmp_ge_u32 s31, 0x10000000\n"
"s_cbranch_scc1 .LJ_4t0k6r\n"
"s_mov_b32 s61, 200000\n"
"s_nop 7\n"
".LJ_4t0k6p:\n"
"buffer_load_dwordx2 v[144:145], v15, s[40:43], s31 offen offset:0 sc1\n"
"s_waitcnt vmcnt(0)\n"
"v_cmp_ne_u64 vcc, s[84:85], v[144:145]\n"
"s_cbranch_vccz .LJ_4t0k6px\n"
"s_sleep 1\n"
"s_sub_u32 s61, s61, 1\n"
"s_cbranch_scc0 .LJ_4t0k6p\n"
"s_branch .LJ_end\n"
".LJ_4t0k6px:\n"
"s_branch .LJ_4t0k6r\n"
".LJ_wt0k6s:\n"
"s_mov_b64 s[76:77], s[68:69]\n"
"s_mov_b32 s78, s70\n"
"s_nop 7\n"
"v_mov_b32 v18, v20\n"
"v_add_u32 v19, 64, v20\n"
"v_mov_b32 v21, v22\n"
"s_mov_b32 s80, 0x7fffffff\n"
"s_mov_b32 s81, 4\n"
"s_mov_b32 s82, 0xff400000\n"
"s_mov_b32 s83, 0xff3f8800\n"
"s_branch .LJ_wt0k6r\n"
".LJ_4t0k7s:\n"
"s_cmp_ge_i32 s31, 0x10000000\n"
"s_cselect_b64 s[84:85], 0, s[84:85]\n"
"s_cmp_ge_u32 s31, 0x10000000\n"
"s_cbranch_scc1 .LJ_4t0k7r\n"
"s_mov_b32 s61, 200000\n"
"s_nop 7\n"
".LJ_4t0k7p:\n"
"buffer_load_dwordx2 v[144:145], v15, s[40:43], s31 offen offset:0 sc1\n"
"s_waitcnt vmcnt(0)\n"
"v_cmp_ne_u64 vcc, s[84:85], v[144:145]\n"
"s_cbranch_vccz .LJ_4t0k7px\n"
"s_sleep 1\n"
"s_sub_u32 s61, s61, 1\n"
"s_cbranch_scc0 .LJ_4t0k7p\n"
"s_branch .LJ_end\n"
".LJ_4t0k7px:\n"
"s_branch .LJ_4t0k7r\n"
".LJ_wt0k7s:\n"
"s_mov_b64 s[76:77], s[68:69]\n"
"s_mov_b32 s78, s70\n"
"s_nop 7\n"
"v_mov_b32 v18, v20\n"
"v_add_u32 v19, 64, v20\n"
"v_mov_b32 v21, v22\n"
"s_mov_b32 s80, 0x7fffffff\n"
"s_mov_b32 s81, 4\n"
"s_mov_b32 s82, 0xff400000\n"
"s_mov_b32 s83, 0xff3f8800\n"
"s_branch .LJ_wt0k7r\n"
".LJ_4t0k0s:\n"
"s_cmp_ge_i32 s31, 0x10000000\n"
"s_cbranch_scc1 .LJ_end\n"
"s_cselect_b64 s[84:85], 0, s[84:85]\n"
"s_cmp_ge_u32 s31, 0x10000000\n"
"s_cbranch_scc1 .LJ_4t0k0r\n"
"s_mov_b32 s61, 200000\n"
"s_nop 7\n"
".LJ_4t0k0p:\n"
"buffer_load_dwordx2 v[144:145], v15, s[40:43], s31 offen offset:0 sc1\n"
"s_waitcnt vmcnt(0)\n"
"v_cmp_ne_u64 vcc, s[84:85], v[144:145]\n"
"s_cbranch_vccz .LJ_4t0k0px\n"
"s_sleep 1\n"
"s_sub_u32 s61, s61, 1\n"
"s_cbranch_scc0 .LJ_4t0k0p\n"
"s_branch .LJ_end\n"
".LJ_4t0k0px:\n"
"s_branch .LJ_4t0k0r\n"
".LJ_wt0k0s:\n"
"s_mov_b64 s[76:77], s[68:69]\n"
"s_mov_b32 s78, s70\n"
"s_nop 7\n"
"v_mov_b32 v18, v20\n"
"v_add_u32 v19, 64, v20\n"
"v_mov_b32 v21, v22\n"
"s_mov_b32 s80, 0x7fffffff\n"
"s_mov_b32 s81, 4\n"
"s_mov_b32 s82, 0xff400000\n"
"s_mov_b32 s83, 0xff3f8800\n"
"s_branch .LJ_wt0k0r\n"
".LJ_wt1k6s:\n"
"s_mov_b64 s[76:77], s[68:69]\n"
"s_mov_b32 s78, s70\n"
"s_nop 7\n"
"v_mov_b32 v18, v20\n"
"v_add_u32 v19, 64, v20\n"
"v_mov_b32 v21, v22\n"
"s_mov_b32 s80, 0x7fffffff\n"
"s_mov_b32 s81, 4\n"
"s_mov_b32 s82, 0xff400000\n"
"s_mov_b32 s83, 0xff3f8800\n"
"s_branch .LJ_wt1k6r\n"
".LJ_4t1k7s:\n"
"s_cmp_ge_i32 s31, 0x10000000\n"
"s_cselect_b64 s[84:85], 0, s[84:85]\n"
"s_cmp_ge_u32 s31, 0x10000000\n"
"s_cbranch_scc1 .LJ_4t1k7r\n"
"s_mov_b32 s61, 200000\n"
"s_nop 7\n"
".LJ_4t1k7p:\n"
"buffer_load_dwordx2 v[144:145], v15, s[40:43], s31 offen offset:0 sc1\n"
"s_waitcnt vmcnt(0)\n"
"v_cmp_ne_u64 vcc, s[84:85], v[144:145]\n"
"s_cbranch_vccz .LJ_4t1k7px\n"
"s_sleep 1\n"
"s_sub_u32 s61, s61, 1\n"
"s_cbranch_scc0 .LJ_4t1k7p\n"
"s_branch .LJ_end\n"
".LJ_4t1k7px:\n"
"s_branch .LJ_4t1k7r\n"
".LJ_wt1k7s:\n"
"s_mov_b64 s[76:77], s[68:69]\n"
"s_mov_b32 s78, s70\n"
"s_nop 7\n"
"v_mov_b32 v18, v20\n"
"v_add_u32 v19, 64, v20\n"
"v_mov_b32 v21, v22\n"
"s_mov_b32 s80, 0x7fffffff\n"
"s_mov_b32 s81, 4\n"
"s_mov_b32 s82, 0xff400000\n"
"s_mov_b32 s83, 0xff3f8800\n"
"s_branch .LJ_wt1k7r\n"
".LJ_4t1k0s:\n"
"s_cmp_ge_i32 s31, 0x10000000\n"
"s_cbranch_scc1 .LJ_end\n"
"s_cselect_b64 s[84:85], 0, s[84:85]\n"
"s_cmp_ge_u32 s31, 0x10000000\n"
"s_cbranch_scc1 .LJ_4t1k0r\n"
"s_mov_b32 s61, 200000\n"
"s_nop 7\n"
".LJ_4t1k0p:\n"
"buffer_load_dwordx2 v[144:145], v15, s[40:43], s31 offen offset:0 sc1\n"
"s_waitcnt vmcnt(0)\n"
"v_cmp_ne_u64 vcc, s[84:85], v[144:145]\n"
"s_cbranch_vccz .LJ_4t1k0px\n"
"s_sleep 1\n"
"s_sub_u32 s61, s61, 1\n"
"s_cbranch_scc0 .LJ_4t1k0p\n"
"s_branch .LJ_end\n"
".LJ_4t1k0px:\n"
"s_branch .LJ_4t1k0r\n"
".LJ_wt1k0s:\n"
"s_mov_b64 s[76:77], s[68:69]\n"
"s_mov_b32 s78, s70\n"
"s_nop 7\n"
"v_mov_b32 v18, v20\n"
"v_add_u32 v19, 64, v20\n"
"v_mov_b32 v21, v22\n"
"s_mov_b32 s80, 0x7fffffff\n"
"s_mov_b32 s81, 4\n"
"s_mov_b32 s82, 0xff400000\n"
"s_mov_b32 s83, 0xff3f8800\n"
"s_branch .LJ_wt1k0r\n"
".LJ_4t2k7s:\n"
"s_cmp_ge_i32 s31, 0x10000000\n"
"s_cselect_b64 s[84:85], 0, s[84:85]\n"
"s_cmp_ge_u32 s31, 0x10000000\n"
"s_cbranch_scc1 .LJ_4t2k7r\n"
"s_mov_b32 s61, 200000\n"
"s_nop 7\n"
".LJ_4t2k7p:\n"
"buffer_load_dwordx2 v[144:145], v15, s[40:43], s31 offen offset:0 sc1\n"
"s_waitcnt vmcnt(0)\n"
"v_cmp_ne_u64 vcc, s[84:85], v[144:145]\n"
"s_cbranch_vccz .LJ_4t2k7px\n"
"s_sleep 1\n"
"s_sub_u32 s61, s61, 1\n"
"s_cbranch_scc0 .LJ_4t2k7p\n"
"s_branch .LJ_end\n"
".LJ_4t2k7px:\n"
"s_branch .LJ_4t2k7r\n"
".LJ_wt2k7s:\n"
"s_mov_b64 s[76:77], s[68:69]\n"
"s_mov_b32 s78, s70\n"
"s_nop 7\n"
"v_mov_b32 v18, v20\n"
"v_add_u32 v19, 64, v20\n"
"v_mov_b32 v21, v22\n"
"s_mov_b32 s80, 0x7fffffff\n"
"s_mov_b32 s81, 4\n"
"s_mov_b32 s82, 0xff400000\n"
"s_mov_b32 s83, 0xff3f8800\n"
"s_branch .LJ_wt2k7r\n"
".LJ_4t2k0s:\n"
"s_cmp_ge_i32 s31, 0x10000000\n"
"s_cbranch_scc1 .LJ_end\n"
"s_cselect_b64 s[84:85], 0, s[84:85]\n"
"s_cmp_ge_u32 s31, 0x10000000\n"
"s_cbranch_scc1 .LJ_4t2k0r\n"
"s_mov_b32 s61, 200000\n"
"s_nop 7\n"
".LJ_4t2k0p:\n"
"buffer_load_dwordx2 v[144:145], v15, s[40:43], s31 offen offset:0 sc1\n"
"s_waitcnt vmcnt(0)\n"
"v_cmp_ne_u64 vcc, s[84:85], v[144:145]\n"
"s_cbranch_vccz .LJ_4t2k0px\n"
"s_sleep 1\n"
"s_sub_u32 s61, s61, 1\n"
"s_cbranch_scc0 .LJ_4t2k0p\n"
"s_branch .LJ_end\n"
".LJ_4t2k0px:\n"
"s_branch .LJ_4t2k0r\n"
".LJ_wt2k0s:\n"
"s_mov_b64 s[76:77], s[68:69]\n"
"s_mov_b32 s78, s70\n"
"s_nop 7\n"
"v_mov_b32 v18, v20\n"
"v_add_u32 v19, 64, v20\n"
"v_mov_b32 v21, v22\n"
"s_mov_b32 s80, 0x7fffffff\n"
"s_mov_b32 s81, 4\n"
"s_mov_b32 s82, 0xff400000\n"
"s_mov_b32 s83, 0xff3f8800\n"
"s_branch .LJ_wt2k0r\n"
".LJ_t0:\n"
"v_mfma_f32_16x16x32_bf16 v[48:51], a[220:223], v[112:115], v[48:51]\n"
"s_waitcnt vmcnt(26)\n"
"ds_read_b128 v[44:47], v2 offset:256\n"
"ds_read_b128 v[68:71], v3 offset:768\n"
"v_mfma_f32_16x16x32_bf16 v[72:75], v[116:119], a[0:3], 0\n"
"ds_read_b128 v[76:79], v4 offset:1280\n"
"v_mfma_f32_16x16x32_bf16 v[72:75], v[120:123], a[4:7], v[72:75]\n"
"s_waitcnt vmcnt(24)\n"
"v_mfma_f32_16x16x32_bf16 v[72:75], v[124:127], a[8:11], v[72:75]\n"
"v_mfma_f32_16x16x32_bf16 v[72:75], v[128:131], a[12:15], v[72:75]\n"
"s_waitcnt vmcnt(21)\n"
"v_mfma_f32_16x16x32_bf16 v[72:75], v[132:135], a[16:19], v[72:75]\n"
"s_waitcnt lgkmcnt(3)\n"
"v_mfma_f32_16x16x32_bf16 v[72:75], v[136:139], a[20:23], v[72:75]\n"
"s_waitcnt vmcnt(18)\n"
"v_add_f32 v56, v56, v60\n"
"v_mfma_f32_16x16x32_bf16 v[72:75], v[140:143], a[24:27], v[72:75]\n"
"v_add_f32 v57, v57, v61\n"
"v_add_f32 v58, v58, v62\n"
"v_mfma_f32_16x16x32_bf16 v[80:83], v[116:119], a[28:31], 0\n"
"v_add_f32 v59, v59, v63\n"
"v_add_f32 v56, v56, v64\n"
"v_mfma_f32_16x16x32_bf16 v[80:83], v[120:123], a[32:35], v[80:83]\n"
"ds_write_addtid_b32 v72 offset:1536\n"
"v_add_f32 v57, v57, v65\n"
"v_mfma_f32_16x16x32_bf16 v[80:83], v[124:127], a[36:39], v[80:83]\n"
"ds_write_addtid_b32 v73 offset:6176\n"
"v_add_f32 v58, v58, v66\n"
"v_mfma_f32_16x16x32_bf16 v[80:83], v[128:131], a[40:43], v[80:83]\n"
"ds_write_addtid_b32 v74 offset:10880\n"
"v_mfma_f32_16x16x32_bf16 v[80:83], v[132:135], a[44:47], v[80:83]\n"
"ds_write_addtid_b32 v75 offset:15520\n"
"v_add_f32 v59, v59, v67\n"
"v_mfma_f32_16x16x32_bf16 v[80:83], v[136:139], a[48:51], v[80:83]\n"
"v_add_f32 v56, v56, v52\n"
"v_add_f32 v57, v57, v53\n"
"v_mfma_f32_16x16x32_bf16 v[80:83], v[140:143], a[52:55], v[80:83]\n"
"v_add_f32 v58, v58, v54\n"
"v_add_f32 v59, v59, v55\n"
"v_mfma_f32_16x16x32_bf16 v[60:63], v[116:119], a[56:59], 0\n"
"s_add_u32 s28, s50, 0xffd00000\n"
"s_and_b32 s29, s28, 0xfbffffff\n"
"v_cvt_pk_bf16_f32 v64, v56, v57\n"
"v_mfma_f32_16x16x32_bf16 v[60:63], v[120:123], a[60:63], v[60:63]\n"
"ds_write_addtid_b32 v80 offset:1792\n"
"v_mfma_f32_16x16x32_bf16 v[60:63], v[124:127], a[64:67], v[60:63]\n"
"ds_write_addtid_b32 v81 offset:6432\n"
"v_cvt_pk_bf16_f32 v65, v58, v59\n"
"v_mfma_f32_16x16x32_bf16 v[60:63], v[128:131], a[68:71], v[60:63]\n"
"s_waitcnt lgkmcnt(6)\n"
"ds_write_addtid_b32 v82 offset:11136\n"
"v_add_f32 v44, v44, v68\n"
"v_mfma_f32_16x16x32_bf16 v[60:63], v[132:135], a[72:75], v[60:63]\n"
"ds_write_addtid_b32 v83 offset:15776\n"
"v_add_f32 v45, v45, v69\n"
"v_mfma_f32_16x16x32_bf16 v[60:63], v[136:139], a[76:79], v[60:63]\n"
"v_add_f32 v46, v46, v70\n"
"v_add_f32 v47, v47, v71\n"
"v_mfma_f32_16x16x32_bf16 v[60:63], v[140:143], a[80:83], v[60:63]\n"
"v_add_f32 v44, v44, v76\n"
"v_mfma_f32_16x16x32_bf16 v[52:55], v[116:119], a[84:87], 0\n"
"s_waitcnt vmcnt(5)\n"
"v_add_f32 v45, v45, v77\n"
"v_add_f32 v46, v46, v78\n"
"v_mfma_f32_16x16x32_bf16 v[52:55], v[120:123], a[88:91], v[52:55]\n"
"ds_write_addtid_b32 v60 offset:2048\n"
"v_add_f32 v47, v47, v79\n"
"v_mfma_f32_16x16x32_bf16 v[52:55], v[124:127], a[92:95], v[52:55]\n"
"ds_write_addtid_b32 v61 offset:6688\n"
"v_add_f32 v44, v44, v48\n"
"v_mfma_f32_16x16x32_bf16 v[52:55], v[128:131], a[96:99], v[52:55]\n"
"ds_write_addtid_b32 v62 offset:11392\n"
"v_add_f32 v45, v45, v49\n"
"v_mfma_f32_16x16x32_bf16 v[52:55], v[132:135], a[100:103], v[52:55]\n"
"s_waitcnt vmcnt(3)\n"
"ds_write_addtid_b32 v63 offset:16032\n"
"v_mfma_f32_16x16x32_bf16 v[52:55], v[136:139], a[104:107], v[52:55]\n"
"v_add_f32 v46, v46, v50\n"
"v_add_f32 v47, v47, v51\n"
"v_mfma_f32_16x16x32_bf16 v[52:55], v[140:143], a[108:111], v[52:55]\n"
"v_cvt_pk_bf16_f32 v66, v44, v45\n"
"v_cvt_pk_bf16_f32 v67, v46, v47\n"
"v_mfma_f32_16x16x32_bf16 v[68:71], v[116:119], a[112:115], 0\n"
"buffer_store_dwordx4 v[64:67], v13, s[40:43], s29 offen sc1\n"
"v_mfma_f32_16x16x32_bf16 v[68:71], v[120:123], a[116:119], v[68:71]\n"
"buffer_store_dwordx2 v[0:1], v14, s[40:43], s30 offen offset:0 sc1\n"
"ds_write_addtid_b32 v52 offset:2304\n"
"v_mfma_f32_16x16x32_bf16 v[68:71], v[124:127], a[120:123], v[68:71]\n"
"s_and_b32 s29, s31, 0x3ffffff\n"
"ds_write_addtid_b32 v53 offset:6944\n"
"v_mfma_f32_16x16x32_bf16 v[68:71], v[128:131], a[124:127], v[68:71]\n"
"s_add_u32 s62, s40, s29\n"
"ds_write_addtid_b32 v54 offset:11648\n"
"v_cmp_ne_u64 vcc, s[84:85], v[144:145]\n"
"v_mfma_f32_16x16x32_bf16 v[68:71], v[132:135], a[128:131], v[68:71]\n"
"s_addc_u32 s63, s41, 0\n"
"ds_write_addtid_b32 v55 offset:16288\n"
"v_mfma_f32_16x16x32_bf16 v[68:71], v[136:139], a[132:135], v[68:71]\n"
"s_cbranch_vccnz .LJ_4t0k6s\n"
".LJ_4t0k6r:\n"
"v_mfma_f32_16x16x32_bf16 v[68:71], v[140:143], a[136:139], v[68:71]\n"
"global_load_dwordx4 v[46:49], v16, s[62:63] offset:-4096 sc1\n"
"v_mfma_f32_16x16x32_bf16 v[72:75], v[116:119], a[140:143], 0\n"
"v_mfma_f32_16x16x32_bf16 v[72:75], v[120:123], a[144:147], v[72:75]\n"
"ds_write_addtid_b32 v68 offset:2560\n"
"v_mfma_f32_16x16x32_bf16 v[72:75], v[124:127], a[148:151], v[72:75]\n"
"ds_write_addtid_b32 v69 offset:7200\n"
"v_mfma_f32_16x16x32_bf16 v[72:75], v[128:131], a[152:155], v[72:75]\n"
"ds_write_addtid_b32 v70 offset:11904\n"
"v_mfma_f32_16x16x32_bf16 v[72:75], v[132:135], a[156:159], v[72:75]\n"
"ds_write_addtid_b32 v71 offset:16544\n"
"global_load_dwordx4 v[50:53], v16, s[62:63] offset:-3072 sc1\n"
"v_mfma_f32_16x16x32_bf16 v[72:75], v[136:139], a[160:163], v[72:75]\n"
"v_mfma_f32_16x16x32_bf16 v[72:75], v[140:143], a[164:167], v[72:75]\n"
"v_mfma_f32_16x16x32_bf16 v[68:71], a[168:171], v[116:119], 0\n"
"v_mfma_f32_16x16x32_bf16 v[68:71], a[172:175], v[120:123], v[68:71]\n"
"ds_write_addtid_b32 v72 offset:2816\n"
"v_mfma_f32_16x16x32_bf16 v[68:71], a[176:179], v[124:127], v[68:71]\n"
"ds_write_addtid_b32 v73 offset:7456\n"
"global_load_dwordx4 v[54:57], v16, s[62:63] offset:-2048 sc1\n"
"v_mfma_f32_16x16x32_bf16 v[68:71], a[180:183], v[128:131], v[68:71]\n"
"ds_write_addtid_b32 v74 offset:12160\n"
"v_mfma_f32_16x16x32_bf16 v[68:71], a[184:187], v[132:135], v[68:71]\n"
"ds_write_addtid_b32 v75 offset:16800\n"
"v_mfma_f32_16x16x32_bf16 v[68:71], a[188:191], v[136:139], v[68:71]\n"
"v_mfma_f32_16x16x32_bf16 v[68:71], a[192:195], v[140:143], v[68:71]\n"
"v_mfma_f32_16x16x32_bf16 v[144:147], a[196:199], v[116:119], 0\n"
"global_load_dwordx4 v[58:61], v16, s[62:63] offset:-1024 sc1\n"
"v_mfma_f32_16x16x32_bf16 v[144:147], a[200:203], v[120:123], v[144:147]\n"
"s_waitcnt lgkmcnt(0)\n"
"v_mfma_f32_16x16x32_bf16 v[144:147], a[204:207], v[124:127], v[144:147]\n"
"s_barrier\n"
"v_mfma_f32_16x16x32_bf16 v[144:147], a[208:211], v[128:131], v[144:147]\n"
"ds_read_b128 v[72:75], v2 offset:1536\n"
"v_mfma_f32_16x16x32_bf16 v[144:147], a[212:215], v[132:135], v[144:147]\n"
"ds_read_b128 v[76:79], v3 offset:2048\n"
"ds_read_b128 v[80:83], v4 offset:2560\n"
"v_mfma_f32_16x16x32_bf16 v[144:147], a[216:219], v[136:139], v[144:147]\n"
"v_mfma_f32_16x16x32_bf16 v[144:147], a[220:223], v[140:143], v[144:147]\n"
"s_waitcnt vmcnt(17)\n"
"ds_read_b128 v[148:151], v2 offset:1792\n"
"ds_read_b128 v[84:87], v3 offset:2304\n"
"v_mfma_f32_16x16x32_bf16 v[88:91], a[224:227], a[0:3], 0\n"
"ds_read_b128 v[136:139], v4 offset:2816\n"
"v_mfma_f32_16x16x32_bf16 v[88:91], a[228:231], a[4:7], v[88:91]\n"
"s_waitcnt vmcnt(15)\n"
"global_load_dwordx4 v[62:65], v17, s[62:63] offset:0 sc1\n"
"v_mfma_f32_16x16x32_bf16 v[88:91], a[232:235], a[8:11], v[88:91]\n"
"v_mfma_f32_16x16x32_bf16 v[88:91], a[236:239], a[12:15], v[88:91]\n"
"s_waitcnt vmcnt(13)\n"
"v_mfma_f32_16x16x32_bf16 v[88:91], a[240:243], a[16:19], v[88:91]\n"
"s_waitcnt lgkmcnt(3)\n"
"v_mfma_f32_16x16x32_bf16 v[88:91], a[244:247], a[20:23], v[88:91]\n"
"s_waitcnt vmcnt(11)\n"
"v_add_f32 v72, v72, v76\n"
"v_mfma_f32_16x16x32_bf16 v[88:91], a[248:251], a[24:27], v[88:91]\n"
"v_add_f32 v73, v73, v77\n"
"v_add_f32 v74, v74, v78\n"
"v_mfma_f32_16x16x32_bf16 v[132:135], a[224:227], a[28:31], 0\n"
"v_add_f32 v75, v75, v79\n"
"v_add_f32 v72, v72, v80\n"
"v_mfma_f32_16x16x32_bf16 v[132:135], a[228:231], a[32:35], v[132:135]\n"
"ds_write_addtid_b32 v88 offset:3072\n"
"v_add_f32 v73, v73, v81\n"
"v_mfma_f32_16x16x32_bf16 v[132:135], a[232:235], a[36:39], v[132:135]\n"
"ds_write_addtid_b32 v89 offset:7712\n"
"v_add_f32 v74, v74, v82\n"
"v_mfma_f32_16x16x32_bf16 v[132:135], a[236:239], a[40:43], v[132:135]\n"
"ds_write_addtid_b32 v90 offset:12416\n"
"v_mfma_f32_16x16x32_bf16 v[132:135], a[240:243], a[44:47], v[132:135]\n"
"ds_write_addtid_b32 v91 offset:17056\n"
"v_add_f32 v75, v75, v83\n"
"v_mfma_f32_16x16x32_bf16 v[132:135], a[244:247], a[48:51], v[132:135]\n"
"v_add_f32 v72, v72, v68\n"
"v_add_f32 v73, v73, v69\n"
"v_mfma_f32_16x16x32_bf16 v[132:135], a[248:251], a[52:55], v[132:135]\n"
"v_add_f32 v74, v74, v70\n"
"v_add_f32 v75, v75, v71\n"
"v_mfma_f32_16x16x32_bf16 v[76:79], a[224:227], a[56:59], 0\n"
"s_add_u32 s91, s50, 0xffe80000\n"
"s_and_b32 s30, s91, 0xfbffffff\n"
"v_cvt_pk_bf16_f32 v140, v72, v73\n"
"v_mfma_f32_16x16x32_bf16 v[76:79], a[228:231], a[60:63], v[76:79]\n"
"ds_write_addtid_b32 v132 offset:3328\n"
"v_mfma_f32_16x16x32_bf16 v[76:79], a[232:235], a[64:67], v[76:79]\n"
"ds_write_addtid_b32 v133 offset:7968\n"
"v_cvt_pk_bf16_f32 v141, v74, v75\n"
"v_mfma_f32_16x16x32_bf16 v[76:79], a[236:239], a[68:71], v[76:79]\n"
"s_waitcnt lgkmcnt(6)\n"
"ds_write_addtid_b32 v134 offset:12672\n"
"v_add_f32 v148, v148, v84\n"
"v_mfma_f32_16x16x32_bf16 v[76:79], a[240:243], a[72:75], v[76:79]\n"
"ds_write_addtid_b32 v135 offset:17312\n"
"v_add_f32 v149, v149, v85\n"
"v_mfma_f32_16x16x32_bf16 v[76:79], a[244:247], a[76:79], v[76:79]\n"
"v_add_f32 v150, v150, v86\n"
"v_add_f32 v151, v151, v87\n"
"v_mfma_f32_16x16x32_bf16 v[76:79], a[248:251], a[80:83], v[76:79]\n"
"s_waitcnt vmcnt(6)\n"
"v_add_f32 v148, v148, v136\n"
"v_mfma_f32_16x16x32_bf16 v[152:155], a[224:227], a[84:87], 0\n"
"buffer_store_dwordx2 v[0:1], v14, s[40:43], s28 offen offset:0 sc1\n"
"v_add_f32 v149, v149, v137\n"
"v_mfma_f32_16x16x32_bf16 v[152:155], a[228:231], a[88:91], v[152:155]\n"
"ds_write_addtid_b32 v76 offset:3584\n"
"v_add_f32 v150, v150, v138\n"
"v_mfma_f32_16x16x32_bf16 v[152:155], a[232:235], a[92:95], v[152:155]\n"
"ds_write_addtid_b32 v77 offset:8224\n"
"v_add_f32 v151, v151, v139\n"
"v_mfma_f32_16x16x32_bf16 v[152:155], a[236:239], a[96:99], v[152:155]\n"
"ds_write_addtid_b32 v78 offset:12928\n"
"v_add_f32 v148, v148, v144\n"
"v_mfma_f32_16x16x32_bf16 v[152:155], a[240:243], a[100:103], v[152:155]\n"
"ds_write_addtid_b32 v79 offset:17568\n"
"v_mfma_f32_16x16x32_bf16 v[152:155], a[244:247], a[104:107], v[152:155]\n"
"v_add_f32 v149, v149, v145\n"
"v_add_f32 v150, v150, v146\n"
"v_mfma_f32_16x16x32_bf16 v[152:155], a[248:251], a[108:111], v[152:155]\n"
"v_add_f32 v151, v151, v147\n"
"v_cvt_pk_bf16_f32 v142, v148, v149\n"
"v_mfma_f32_16x16x32_bf16 v[156:159], a[224:227], a[112:115], 0\n"
"global_load_dwordx4 v[70:73], v16, s[62:63] offset:0 sc1\n"
"v_cvt_pk_bf16_f32 v143, v150, v151\n"
"v_mfma_f32_16x16x32_bf16 v[156:159], a[228:231], a[116:119], v[156:159]\n"
"buffer_store_dwordx4 v[140:143], v13, s[40:43], s30 offen sc1\n"
"ds_write_addtid_b32 v152 offset:3840\n"
"v_mfma_f32_16x16x32_bf16 v[156:159], a[232:235], a[120:123], v[156:159]\n"
"ds_write_addtid_b32 v153 offset:8480\n"
"v_mfma_f32_16x16x32_bf16 v[156:159], a[236:239], a[124:127], v[156:159]\n"
"ds_write_addtid_b32 v154 offset:13184\n"
"v_mfma_f32_16x16x32_bf16 v[156:159], a[240:243], a[128:131], v[156:159]\n"
"ds_write_addtid_b32 v155 offset:17824\n"
"v_mfma_f32_16x16x32_bf16 v[156:159], a[244:247], a[132:135], v[156:159]\n"
"s_mov_b32 s90, s31\n"
"global_load_dwordx4 v[74:77], v16, s[62:63] offset:1024 sc1\n"
"v_mov_b32 v44, v26\n"
"v_mfma_f32_16x16x32_bf16 v[156:159], a[248:251], a[136:139], v[156:159]\n"
"v_mov_b32 v45, v27\n"
"v_mov_b32 v66, v28\n"
"v_mfma_f32_16x16x32_bf16 v[144:147], a[224:227], a[140:143], 0\n"
"v_mov_b32 v67, v29\n"
"v_mfma_f32_16x16x32_bf16 v[144:147], a[228:231], a[144:147], v[144:147]\n"
"ds_write_addtid_b32 v156 offset:4096\n"
"v_mfma_f32_16x16x32_bf16 v[144:147], a[232:235], a[148:151], v[144:147]\n"
"ds_write_addtid_b32 v157 offset:8736\n"
"v_mfma_f32_16x16x32_bf16 v[144:147], a[236:239], a[152:155], v[144:147]\n"
"ds_write_addtid_b32 v158 offset:13440\n"
"global_load_dwordx4 v[78:81], v16, s[62:63] offset:2048 sc1\n"
"v_mfma_f32_16x16x32_bf16 v[144:147], a[240:243], a[156:159], v[144:147]\n"
"ds_write_addtid_b32 v159 offset:18080\n"
"v_mfma_f32_16x16x32_bf16 v[144:147], a[244:247], a[160:163], v[144:147]\n"
"v_mfma_f32_16x16x32_bf16 v[144:147], a[248:251], a[164:167], v[144:147]\n"
"v_mfma_f32_16x16x32_bf16 v[148:151], a[168:171], a[224:227], 0\n"
"v_mfma_f32_16x16x32_bf16 v[148:151], a[172:175], a[228:231], v[148:151]\n"
"ds_write_addtid_b32 v144 offset:4352\n"
"global_load_dwordx4 v[82:85], v16, s[62:63] offset:3072 sc1\n"
"v_mfma_f32_16x16x32_bf16 v[148:151], a[176:179], a[232:235], v[148:151]\n"
"ds_write_addtid_b32 v145 offset:8992\n"
"v_mfma_f32_16x16x32_bf16 v[148:151], a[180:183], a[236:239], v[148:151]\n"
"ds_write_addtid_b32 v146 offset:13696\n"
"v_mfma_f32_16x16x32_bf16 v[148:151], a[184:187], a[240:243], v[148:151]\n"
"ds_write_addtid_b32 v147 offset:18336\n"
"v_mfma_f32_16x16x32_bf16 v[148:151], a[188:191], a[244:247], v[148:151]\n"
"v_mfma_f32_16x16x32_bf16 v[148:151], a[192:195], a[248:251], v[148:151]\n"
"global_load_dwordx4 v[86:89], v17, s[62:63] offset:1024 sc1\n"
"v_mfma_f32_16x16x32_bf16 v[144:147], a[196:199], a[224:227], 0\n"
"v_mfma_f32_16x16x32_bf16 v[144:147], a[200:203], a[228:231], v[144:147]\n"
"s_waitcnt lgkmcnt(0)\n"
"v_mfma_f32_16x16x32_bf16 v[144:147], a[204:207], a[232:235], v[144:147]\n"
"s_barrier\n"
"v_mfma_f32_16x16x32_bf16 v[144:147], a[208:211], a[236:239], v[144:147]\n"
"ds_read_b128 v[152:155], v2 offset:3072\n"
"v_mfma_f32_16x16x32_bf16 v[144:147], a[212:215], a[240:243], v[144:147]\n"
"s_waitcnt vmcnt(7)\n"
"ds_read_b128 v[156:159], v3 offset:3584\n"
"ds_read_b128 v[160:163], v4 offset:4096\n"
"v_mfma_f32_16x16x32_bf16 v[164:167], v[38:41], v[44:47], 0\n"
"s_cmp_ge_i32 s90, s80\n"
"s_cbranch_scc1 .LJ_wt0k6s\n"
".LJ_wt0k6r:\n"
"v_mfma_f32_16x16x32_bf16 v[164:167], v[38:41], v[52:55], v[164:167]\n"
"s_lshr_b32 s28, s90, s81\n"
"v_mfma_f32_16x16x32_bf16 v[164:167], v[38:41], v[60:63], v[164:167]\n"
"s_bitcmp1_b32 s28, 15\n"
"s_cselect_b32 s31, s83, s82\n"
"v_mfma_f32_16x16x32_bf16 v[168:171], v[38:41], v[48:51], 0\n"
"s_add_u32 s28, s28, s31\n"
"v_mfma_f32_16x16x32_bf16 v[168:171], v[38:41], v[56:59], v[168:171]\n"
"v_mfma_f32_16x16x32_bf16 v[168:171], v[38:41], v[64:67], v[168:171]\n"
"v_cvt_pk_bf16_f32 v172, v164, v165\n"
"v_cvt_pk_bf16_f32 v173, v166, v167\n"
"v_mfma_f32_16x16x32_bf16 v[144:147], a[216:219], a[244:247], v[144:147]\n"
"v_mfma_f32_16x16x32_bf16 v[144:147], a[220:223], a[248:251], v[144:147]\n"
"ds_read_b128 v[64:67], v2 offset:3328\n"
"ds_read_b128 v[164:167], v3 offset:3840\n"
"ds_read_b128 v[48:51], v4 offset:4352\n"
"v_cvt_pk_bf16_f32 v174, v168, v169\n"
"v_cvt_pk_bf16_f32 v175, v170, v171\n"
"s_add_u32 s31, s50, 0xffc80000\n"
"ds_bpermute_b32 v52, v21, v172\n"
"ds_bpermute_b32 v53, v21, v173\n"
"s_waitcnt lgkmcnt(5)\n"
"v_add_f32 v152, v152, v156\n"
"v_add_f32 v153, v153, v157\n"
"v_add_f32 v154, v154, v158\n"
"v_add_f32 v155, v155, v159\n"
"v_add_f32 v152, v152, v160\n"
"v_add_f32 v153, v153, v161\n"
"v_add_f32 v154, v154, v162\n"
"v_add_f32 v155, v155, v163\n"
"v_add_f32 v152, v152, v148\n"
"v_add_f32 v153, v153, v149\n"
"v_add_f32 v154, v154, v150\n"
"v_add_f32 v155, v155, v151\n"
"s_add_u32 s30, s50, 0x0\n"
"s_and_b32 s29, s30, 0xfbffffff\n"
"v_cvt_pk_bf16_f32 v60, v152, v153\n"
"v_cvt_pk_bf16_f32 v61, v154, v155\n"
"s_waitcnt lgkmcnt(2)\n"
"v_add_f32 v64, v64, v164\n"
"v_add_f32 v65, v65, v165\n"
"v_add_f32 v66, v66, v166\n"
"v_add_f32 v67, v67, v167\n"
"v_add_f32 v64, v64, v48\n"
"s_waitcnt vmcnt(4)\n"
"buffer_store_dwordx2 v[0:1], v14, s[40:43], s91 offen offset:0 sc1\n"
"v_add_f32 v65, v65, v49\n"
"v_add_f32 v66, v66, v50\n"
"v_add_f32 v67, v67, v51\n"
"v_add_f32 v64, v64, v144\n"
"v_add_f32 v65, v65, v145\n"
"v_add_f32 v66, v66, v146\n"
"v_add_f32 v67, v67, v147\n"
"v_cvt_pk_bf16_f32 v62, v64, v65\n"
"s_cmp_lt_u32 s31, 0x8000000\n"
"s_cselect_b32 s31, 0x80000000, s31\n"
"v_cvt_pk_bf16_f32 v63, v66, v67\n"
"buffer_store_dwordx4 v[60:63], v13, s[40:43], s29 offen sc1\n"
"ds_bpermute_b32 v54, v21, v174\n"
"v_mov_b32 v68, v30\n"
"v_mov_b32 v69, v31\n"
"buffer_load_dwordx2 v[144:145], v15, s[40:43], s31 offen offset:0 sc1\n"
"ds_bpermute_b32 v55, v21, v175\n"
"v_mov_b32 v90, v32\n"
"v_mov_b32 v91, v33\n"
"s_waitcnt vmcnt(3)\n"
"v_mfma_f32_16x16x32_bf16 v[56:59], v[38:41], v[68:71], 0\n"
"v_mfma_f32_16x16x32_bf16 v[56:59], v[38:41], v[76:79], v[56:59]\n"
"v_mfma_f32_16x16x32_bf16 v[56:59], v[38:41], v[84:87], v[56:59]\n"
"v_mfma_f32_16x16x32_bf16 v[60:63], v[38:41], v[72:75], 0\n"
"v_mfma_f32_16x16x32_bf16 v[60:63], v[38:41], v[80:83], v[60:63]\n"
"v_mfma_f32_16x16x32_bf16 v[60:63], v[38:41], v[88:91], v[60:63]\n"
"v_cvt_pk_bf16_f32 v64, v56, v57\n"
"v_cvt_pk_bf16_f32 v65, v58, v59\n"
"s_nop 5\n"
"v_cvt_pk_bf16_f32 v66, v60, v61\n"
"v_cvt_pk_bf16_f32 v67, v62, v63\n"
"s_waitcnt lgkmcnt(0)\n"
"buffer_store_dwordx4 v[52:55], v18, s[76:79], s28 offen sc1\n"
"ds_bpermute_b32 v44, v21, v64\n"
"ds_bpermute_b32 v45, v21, v65\n"
"ds_bpermute_b32 v46, v21, v66\n"
"ds_bpermute_b32 v47, v21, v67\n"
"s_waitcnt lgkmcnt(0)\n"
"buffer_store_dwordx4 v[44:47], v19, s[76:79], s28 offen sc1\n"
"s_waitcnt vmcnt(3)\n"
"s_waitcnt vmcnt(2)\n"
"buffer_store_dwordx2 v[0:1], v14, s[40:43], s30 offen offset:0 sc1\n"
"s_and_b32 s29, s31, 0x3ffffff\n"
"s_add_u32 s62, s40, s29\n"
"v_cmp_ne_u64 vcc, s[84:85], v[144:145]\n"
"s_addc_u32 s63, s41, 0\n"
"s_cbranch_vccnz .LJ_4t0k7s\n"
".LJ_4t0k7r:\n"
"global_load_dwordx4 v[46:49], v16, s[62:63] offset:-4096 sc1\n"
"global_load_dwordx4 v[50:53], v16, s[62:63] offset:-3072 sc1\n"
"global_load_dwordx4 v[54:57], v16, s[62:63] offset:-2048 sc1\n"
"global_load_dwordx4 v[58:61], v16, s[62:63] offset:-1024 sc1\n"
"global_load_dwordx4 v[62:65], v17, s[62:63] offset:0 sc1\n"
"global_load_dwordx4 v[70:73], v16, s[62:63] offset:0 sc1\n"
"s_mov_b32 s90, s31\n"
"global_load_dwordx4 v[74:77], v16, s[62:63] offset:1024 sc1\n"
"v_mov_b32 v44, v26\n"
"v_mov_b32 v45, v27\n"
"v_mov_b32 v66, v28\n"
"v_mov_b32 v67, v29\n"
"global_load_dwordx4 v[78:81], v16, s[62:63] offset:2048 sc1\n"
"global_load_dwordx4 v[82:85], v16, s[62:63] offset:3072 sc1\n"
"global_load_dwordx4 v[86:89], v17, s[62:63] offset:1024 sc1\n"
"s_waitcnt vmcnt(5)\n"
"v_mfma_f32_16x16x32_bf16 v[164:167], v[38:41], v[44:47], 0\n"
"s_cmp_ge_i32 s90, s80\n"
"s_cbranch_scc1 .LJ_wt0k7s\n"
".LJ_wt0k7r:\n"
"v_mfma_f32_16x16x32_bf16 v[164:167], v[38:41], v[52:55], v[164:167]\n"
"s_lshr_b32 s28, s90, s81\n"
"v_mfma_f32_16x16x32_bf16 v[164:167], v[38:41], v[60:63], v[164:167]\n"
"s_bitcmp1_b32 s28, 15\n"
"s_cselect_b32 s31, s83, s82\n"
"v_mfma_f32_16x16x32_bf16 v[168:171], v[38:41], v[48:51], 0\n"
"s_add_u32 s28, s28, s31\n"
"v_mfma_f32_16x16x32_bf16 v[168:171], v[38:41], v[56:59], v[168:171]\n"
"v_mfma_f32_16x16x32_bf16 v[168:171], v[38:41], v[64:67], v[168:171]\n"
"v_cvt_pk_bf16_f32 v172, v164, v165\n"
"v_cvt_pk_bf16_f32 v173, v166, v167\n"
"s_nop 5\n"
"v_cvt_pk_bf16_f32 v174, v168, v169\n"
"v_cvt_pk_bf16_f32 v175, v170, v171\n"
"s_add_u32 s31, s50, 0x100000\n"
"ds_bpermute_b32 v52, v21, v172\n"
"ds_bpermute_b32 v53, v21, v173\n"
"s_cmp_lt_u32 s31, 0x8000000\n"
"s_cselect_b32 s31, 0x80000000, s31\n"
"ds_bpermute_b32 v54, v21, v174\n"
"v_mov_b32 v68, v30\n"
"v_mov_b32 v69, v31\n"
"buffer_load_dwordx2 v[144:145], v15, s[40:43], s31 offen offset:0 sc1\n"
"ds_bpermute_b32 v55, v21, v175\n"
"v_mov_b32 v90, v32\n"
"v_mov_b32 v91, v33\n"
"s_waitcnt vmcnt(1)\n"
"v_mfma_f32_16x16x32_bf16 v[56:59], v[38:41], v[68:71], 0\n"
"v_mfma_f32_16x16x32_bf16 v[56:59], v[38:41], v[76:79], v[56:59]\n"
"v_mfma_f32_16x16x32_bf16 v[56:59], v[38:41], v[84:87], v[56:59]\n"
"v_mfma_f32_16x16x32_bf16 v[60:63], v[38:41], v[72:75], 0\n"
"v_mfma_f32_16x16x32_bf16 v[60:63], v[38:41], v[80:83], v[60:63]\n"
"v_mfma_f32_16x16x32_bf16 v[60:63], v[38:41], v[88:91], v[60:63]\n"
"v_cvt_pk_bf16_f32 v64, v56, v57\n"
"v_cvt_pk_bf16_f32 v65, v58, v59\n"
"s_nop 5\n"
"v_cvt_pk_bf16_f32 v66, v60, v61\n"
"v_cvt_pk_bf16_f32 v67, v62, v63\n"
"s_waitcnt lgkmcnt(0)\n"
"buffer_store_dwordx4 v[52:55], v18, s[76:79], s28 offen sc1\n"
"ds_bpermute_b32 v44, v21, v64\n"
"ds_bpermute_b32 v45, v21, v65\n"
"ds_bpermute_b32 v46, v21, v66\n"
"ds_bpermute_b32 v47, v21, v67\n"
"s_waitcnt lgkmcnt(0)\n"
"buffer_store_dwordx4 v[44:47], v19, s[76:79], s28 offen sc1\n"
"s_waitcnt vmcnt(2)\n"
"s_and_b32 s29, s31, 0x3ffffff\n"
"s_add_u32 s62, s40, s29\n"
"v_cmp_ne_u64 vcc, s[84:85], v[144:145]\n"
"s_addc_u32 s63, s41, 0\n"
"s_cbranch_vccnz .LJ_4t0k0s\n"
".LJ_4t0k0r:\n"
"global_load_dwordx4 v[46:49], v16, s[62:63] offset:-4096 sc1\n"
"global_load_dwordx4 v[50:53], v16, s[62:63] offset:-3072 sc1\n"
"global_load_dwordx4 v[54:57], v16, s[62:63] offset:-2048 sc1\n"
"global_load_dwordx4 v[58:61], v16, s[62:63] offset:-1024 sc1\n"
"global_load_dwordx4 v[62:65], v17, s[62:63] offset:0 sc1\n"
"global_load_dwordx4 v[70:73], v16, s[62:63] offset:0 sc1\n"
"s_mov_b32 s90, s31\n"
"global_load_dwordx4 v[74:77], v16, s[62:63] offset:1024 sc1\n"
"v_mov_b32 v44, v26\n"
"v_mov_b32 v45, v27\n"
"v_mov_b32 v66, v28\n"
"v_mov_b32 v67, v29\n"
"global_load_dwordx4 v[78:81], v16, s[62:63] offset:2048 sc1\n"
"global_load_dwordx4 v[82:85], v16, s[62:63] offset:3072 sc1\n"
"global_load_dwordx4 v[86:89], v17, s[62:63] offset:1024 sc1\n"
"s_waitcnt vmcnt(5)\n"
"v_mfma_f32_16x16x32_bf16 v[164:167], v[38:41], v[44:47], 0\n"
"s_cmp_ge_i32 s90, s80\n"
"s_cbranch_scc1 .LJ_wt0k0s\n"
".LJ_wt0k0r:\n"
"v_mfma_f32_16x16x32_bf16 v[164:167], v[38:41], v[52:55], v[164:167]\n"
"s_lshr_b32 s28, s90, s81\n"
"v_mfma_f32_16x16x32_bf16 v[164:167], v[38:41], v[60:63], v[164:167]\n"
"s_bitcmp1_b32 s28, 15\n"
"s_cselect_b32 s31, s83, s82\n"
"v_mfma_f32_16x16x32_bf16 v[168:171], v[38:41], v[48:51], 0\n"
"s_add_u32 s28, s28, s31\n"
"v_mfma_f32_16x16x32_bf16 v[168:171], v[38:41], v[56:59], v[168:171]\n"
"v_mfma_f32_16x16x32_bf16 v[168:171], v[38:41], v[64:67], v[168:171]\n"
"v_cvt_pk_bf16_f32 v172, v164, v165\n"
"v_cvt_pk_bf16_f32 v173, v166, v167\n"
"s_nop 5\n"
"v_cvt_pk_bf16_f32 v174, v168, v169\n"
"v_cvt_pk_bf16_f32 v175, v170, v171\n"
"ds_bpermute_b32 v52, v21, v172\n"
"ds_bpermute_b32 v53, v21, v173\n"
"ds_bpermute_b32 v54, v21, v174\n"
"v_mov_b32 v68, v30\n"
"v_mov_b32 v69, v31\n"
"ds_bpermute_b32 v55, v21, v175\n"
"v_mov_b32 v90, v32\n"
"v_mov_b32 v91, v33\n"
"s_waitcnt vmcnt(0)\n"
"v_mfma_f32_16x16x32_bf16 v[56:59], v[38:41], v[68:71], 0\n"
"v_mfma_f32_16x16x32_bf16 v[56:59], v[38:41], v[76:79], v[56:59]\n"
"v_mfma_f32_16x16x32_bf16 v[56:59], v[38:41], v[84:87], v[56:59]\n"
"v_mfma_f32_16x16x32_bf16 v[60:63], v[38:41], v[72:75], 0\n"
"v_mfma_f32_16x16x32_bf16 v[60:63], v[38:41], v[80:83], v[60:63]\n"
"v_mfma_f32_16x16x32_bf16 v[60:63], v[38:41], v[88:91], v[60:63]\n"
"v_cvt_pk_bf16_f32 v64, v56, v57\n"
"v_cvt_pk_bf16_f32 v65, v58, v59\n"
"s_nop 5\n"
"v_cvt_pk_bf16_f32 v66, v60, v61\n"
"v_cvt_pk_bf16_f32 v67, v62, v63\n"
"s_waitcnt lgkmcnt(0)\n"
"buffer_store_dwordx4 v[52:55], v18, s[76:79], s28 offen sc1\n"
"ds_bpermute_b32 v44, v21, v64\n"
"ds_bpermute_b32 v45, v21, v65\n"
"ds_bpermute_b32 v46, v21, v66\n"
"ds_bpermute_b32 v47, v21, v67\n"
"s_waitcnt lgkmcnt(0)\n"
"buffer_store_dwordx4 v[44:47], v19, s[76:79], s28 offen sc1\n"
"s_branch .LJ_end\n"
".LJ_t1:\n"
"v_mfma_f32_16x16x32_bf16 v[144:147], a[220:223], v[140:143], v[144:147]\n"
"s_waitcnt vmcnt(26)\n"
"ds_read_b128 v[148:151], v2 offset:1792\n"
"ds_read_b128 v[84:87], v3 offset:2304\n"
"v_mfma_f32_16x16x32_bf16 v[88:91], a[224:227], a[0:3], 0\n"
"ds_read_b128 v[136:139], v4 offset:2816\n"
"v_mfma_f32_16x16x32_bf16 v[88:91], a[228:231], a[4:7], v[88:91]\n"
"s_waitcnt vmcnt(24)\n"
"global_load_dwordx4 v[62:65], v17, s[62:63] offset:0 sc1\n"
"v_mfma_f32_16x16x32_bf16 v[88:91], a[232:235], a[8:11], v[88:91]\n"
"v_mfma_f32_16x16x32_bf16 v[88:91], a[236:239], a[12:15], v[88:91]\n"
"s_waitcnt vmcnt(22)\n"
"v_mfma_f32_16x16x32_bf16 v[88:91], a[240:243], a[16:19], v[88:91]\n"
"s_waitcnt lgkmcnt(3)\n"
"v_mfma_f32_16x16x32_bf16 v[88:91], a[244:247], a[20:23], v[88:91]\n"
"s_waitcnt vmcnt(20)\n"
"v_add_f32 v72, v72, v76\n"
"v_mfma_f32_16x16x32_bf16 v[88:91], a[248:251], a[24:27], v[88:91]\n"
"v_add_f32 v73, v73, v77\n"
"v_add_f32 v74, v74, v78\n"
"v_mfma_f32_16x16x32_bf16 v[132:135], a[224:227], a[28:31], 0\n"
"v_add_f32 v75, v75, v79\n"
"v_add_f32 v72, v72, v80\n"
"v_mfma_f32_16x16x32_bf16 v[132:135], a[228:231], a[32:35], v[132:135]\n"
"ds_write_addtid_b32 v88 offset:3072\n"
"v_add_f32 v73, v73, v81\n"
"v_mfma_f32_16x16x32_bf16 v[132:135], a[232:235], a[36:39], v[132:135]\n"
"ds_write_addtid_b32 v89 offset:7712\n"
"v_add_f32 v74, v74, v82\n"
"v_mfma_f32_16x16x32_bf16 v[132:135], a[236:239], a[40:43], v[132:135]\n"
"ds_write_addtid_b32 v90 offset:12416\n"
"v_mfma_f32_16x16x32_bf16 v[132:135], a[240:243], a[44:47], v[132:135]\n"
"ds_write_addtid_b32 v91 offset:17056\n"
"v_add_f32 v75, v75, v83\n"
"v_mfma_f32_16x16x32_bf16 v[132:135], a[244:247], a[48:51], v[132:135]\n"
"v_add_f32 v72, v72, v68\n"
"v_add_f32 v73, v73, v69\n"
"v_mfma_f32_16x16x32_bf16 v[132:135], a[248:251], a[52:55], v[132:135]\n"
"v_add_f32 v74, v74, v70\n"
"v_add_f32 v75, v75, v71\n"
"v_mfma_f32_16x16x32_bf16 v[76:79], a[224:227], a[56:59], 0\n"
"s_add_u32 s91, s50, 0xffd00000\n"
"s_and_b32 s30, s91, 0xfbffffff\n"
"v_cvt_pk_bf16_f32 v140, v72, v73\n"
"v_mfma_f32_16x16x32_bf16 v[76:79], a[228:231], a[60:63], v[76:79]\n"
"ds_write_addtid_b32 v132 offset:3328\n"
"v_mfma_f32_16x16x32_bf16 v[76:79], a[232:235], a[64:67], v[76:79]\n"
"ds_write_addtid_b32 v133 offset:7968\n"
"v_cvt_pk_bf16_f32 v141, v74, v75\n"
"v_mfma_f32_16x16x32_bf16 v[76:79], a[236:239], a[68:71], v[76:79]\n"
"s_waitcnt lgkmcnt(6)\n"
"ds_write_addtid_b32 v134 offset:12672\n"
"v_add_f32 v148, v148, v84\n"
"v_mfma_f32_16x16x32_bf16 v[76:79], a[240:243], a[72:75], v[76:79]\n"
"ds_write_addtid_b32 v135 offset:17312\n"
"v_add_f32 v149, v149, v85\n"
"v_mfma_f32_16x16x32_bf16 v[76:79], a[244:247], a[76:79], v[76:79]\n"
"v_add_f32 v150, v150, v86\n"
"v_add_f32 v151, v151, v87\n"
"v_mfma_f32_16x16x32_bf16 v[76:79], a[248:251], a[80:83], v[76:79]\n"
"s_waitcnt vmcnt(8)\n"
"v_add_f32 v148, v148, v136\n"
"v_mfma_f32_16x16x32_bf16 v[152:155], a[224:227], a[84:87], 0\n"
"buffer_store_dwordx2 v[0:1], v14, s[40:43], s28 offen offset:0 sc1\n"
"v_add_f32 v149, v149, v137\n"
"v_mfma_f32_16x16x32_bf16 v[152:155], a[228:231], a[88:91], v[152:155]\n"
"ds_write_addtid_b32 v76 offset:3584\n"
"v_add_f32 v150, v150, v138\n"
"v_mfma_f32_16x16x32_bf16 v[152:155], a[232:235], a[92:95], v[152:155]\n"
"ds_write_addtid_b32 v77 offset:8224\n"
"v_add_f32 v151, v151, v139\n"
"v_mfma_f32_16x16x32_bf16 v[152:155], a[236:239], a[96:99], v[152:155]\n"
"ds_write_addtid_b32 v78 offset:12928\n"
"v_add_f32 v148, v148, v144\n"
"v_mfma_f32_16x16x32_bf16 v[152:155], a[240:243], a[100:103], v[152:155]\n"
"ds_write_addtid_b32 v79 offset:17568\n"
"v_mfma_f32_16x16x32_bf16 v[152:155], a[244:247], a[104:107], v[152:155]\n"
"v_add_f32 v149, v149, v145\n"
"v_add_f32 v150, v150, v146\n"
"v_mfma_f32_16x16x32_bf16 v[152:155], a[248:251], a[108:111], v[152:155]\n"
"v_add_f32 v151, v151, v147\n"
"v_cvt_pk_bf16_f32 v142, v148, v149\n"
"v_mfma_f32_16x16x32_bf16 v[156:159], a[224:227], a[112:115], 0\n"
"global_load_dwordx4 v[70:73], v16, s[62:63] offset:0 sc1\n"
"v_cvt_pk_bf16_f32 v143, v150, v151\n"
"v_mfma_f32_16x16x32_bf16 v[156:159], a[228:231], a[116:119], v[156:159]\n"
"buffer_store_dwordx4 v[140:143], v13, s[40:43], s30 offen sc1\n"
"ds_write_addtid_b32 v152 offset:3840\n"
"v_mfma_f32_16x16x32_bf16 v[156:159], a[232:235], a[120:123], v[156:159]\n"
"ds_write_addtid_b32 v153 offset:8480\n"
"v_mfma_f32_16x16x32_bf16 v[156:159], a[236:239], a[124:127], v[156:159]\n"
"ds_write_addtid_b32 v154 offset:13184\n"
"v_mfma_f32_16x16x32_bf16 v[156:159], a[240:243], a[128:131], v[156:159]\n"
"ds_write_addtid_b32 v155 offset:17824\n"
"v_mfma_f32_16x16x32_bf16 v[156:159], a[244:247], a[132:135], v[156:159]\n"
"s_mov_b32 s90, s31\n"
"global_load_dwordx4 v[74:77], v16, s[62:63] offset:1024 sc1\n"
"v_mov_b32 v44, v26\n"
"v_mfma_f32_16x16x32_bf16 v[156:159], a[248:251], a[136:139], v[156:159]\n"
"v_mov_b32 v45, v27\n"
"v_mov_b32 v66, v28\n"
"v_mfma_f32_16x16x32_bf16 v[144:147], a[224:227], a[140:143], 0\n"
"v_mov_b32 v67, v29\n"
"v_mfma_f32_16x16x32_bf16 v[144:147], a[228:231], a[144:147], v[144:147]\n"
"ds_write_addtid_b32 v156 offset:4096\n"
"v_mfma_f32_16x16x32_bf16 v[144:147], a[232:235], a[148:151], v[144:147]\n"
"ds_write_addtid_b32 v157 offset:8736\n"
"v_mfma_f32_16x16x32_bf16 v[144:147], a[236:239], a[152:155], v[144:147]\n"
"ds_write_addtid_b32 v158 offset:13440\n"
"global_load_dwordx4 v[78:81], v16, s[62:63] offset:2048 sc1\n"
"v_mfma_f32_16x16x32_bf16 v[144:147], a[240:243], a[156:159], v[144:147]\n"
"ds_write_addtid_b32 v159 offset:18080\n"
"v_mfma_f32_16x16x32_bf16 v[144:147], a[244:247], a[160:163], v[144:147]\n"
"v_mfma_f32_16x16x32_bf16 v[144:147], a[248:251], a[164:167], v[144:147]\n"
"v_mfma_f32_16x16x32_bf16 v[148:151], a[168:171], a[224:227], 0\n"
"v_mfma_f32_16x16x32_bf16 v[148:151], a[172:175], a[228:231], v[148:151]\n"
"ds_write_addtid_b32 v144 offset:4352\n"
"global_load_dwordx4 v[82:85], v16, s[62:63] offset:3072 sc1\n"
"v_mfma_f32_16x16x32_bf16 v[148:151], a[176:179], a[232:235], v[148:151]\n"
"ds_write_addtid_b32 v145 offset:8992\n"
"v_mfma_f32_16x16x32_bf16 v[148:151], a[180:183], a[236:239], v[148:151]\n"
"ds_write_addtid_b32 v146 offset:13696\n"
"v_mfma_f32_16x16x32_bf16 v[148:151], a[184:187], a[240:243], v[148:151]\n"
"ds_write_addtid_b32 v147 offset:18336\n"
"v_mfma_f32_16x16x32_bf16 v[148:151], a[188:191], a[244:247], v[148:151]\n"
"v_mfma_f32_16x16x32_bf16 v[148:151], a[192:195], a[248:251], v[148:151]\n"
"global_load_dwordx4 v[86:89], v17, s[62:63] offset:1024 sc1\n"
"v_mfma_f32_16x16x32_bf16 v[144:147], a[196:199], a[224:227], 0\n"
"v_mfma_f32_16x16x32_bf16 v[144:147], a[200:203], a[228:231], v[144:147]\n"
"s_waitcnt lgkmcnt(0)\n"
"v_mfma_f32_16x16x32_bf16 v[144:147], a[204:207], a[232:235], v[144:147]\n"
"s_barrier\n"
"v_mfma_f32_16x16x32_bf16 v[144:147], a[208:211], a[236:239], v[144:147]\n"
"ds_read_b128 v[152:155], v2 offset:3072\n"
"v_mfma_f32_16x16x32_bf16 v[144:147], a[212:215], a[240:243], v[144:147]\n"
"s_waitcnt vmcnt(7)\n"
"ds_read_b128 v[156:159], v3 offset:3584\n"
"ds_read_b128 v[160:163], v4 offset:4096\n"
"v_mfma_f32_16x16x32_bf16 v[164:167], v[38:41], v[44:47], 0\n"
"s_cmp_ge_i32 s90, s80\n"
"s_cbranch_scc1 .LJ_wt1k6s\n"
".LJ_wt1k6r:\n"
"v_mfma_f32_16x16x32_bf16 v[164:167], v[38:41], v[52:55], v[164:167]\n"
"s_lshr_b32 s28, s90, s81\n"
"v_mfma_f32_16x16x32_bf16 v[164:167], v[38:41], v[60:63], v[164:167]\n"
"s_bitcmp1_b32 s28, 15\n"
"s_cselect_b32 s31, s83, s82\n"
"v_mfma_f32_16x16x32_bf16 v[168:171], v[38:41], v[48:51], 0\n"
"s_add_u32 s28, s28, s31\n"
"v_mfma_f32_16x16x32_bf16 v[168:171], v[38:41], v[56:59], v[168:171]\n"
"v_mfma_f32_16x16x32_bf16 v[168:171], v[38:41], v[64:67], v[168:171]\n"
"v_cvt_pk_bf16_f32 v172, v164, v165\n"
"v_cvt_pk_bf16_f32 v173, v166, v167\n"
"v_mfma_f32_16x16x32_bf16 v[144:147], a[216:219], a[244:247], v[144:147]\n"
"v_mfma_f32_16x16x32_bf16 v[144:147], a[220:223], a[248:251], v[144:147]\n"
"s_waitcnt vmcnt(20)\n"
"ds_read_b128 v[64:67], v2 offset:3328\n"
"ds_read_b128 v[164:167], v3 offset:3840\n"
"v_mfma_f32_16x16x32_bf16 v[44:47], a[252:255], a[0:3], 0\n"
"ds_read_b128 v[48:51], v4 offset:4352\n"
"v_mfma_f32_16x16x32_bf16 v[44:47], v[92:95], a[4:7], v[44:47]\n"
"s_waitcnt vmcnt(18)\n"
"v_cvt_pk_bf16_f32 v174, v168, v169\n"
"v_cvt_pk_bf16_f32 v175, v170, v171\n"
"v_mfma_f32_16x16x32_bf16 v[44:47], v[96:99], a[8:11], v[44:47]\n"
"s_add_u32 s31, s50, 0xffb00000\n"
"ds_bpermute_b32 v52, v21, v172\n"
"v_mfma_f32_16x16x32_bf16 v[44:47], v[100:103], a[12:15], v[44:47]\n"
"s_waitcnt vmcnt(16)\n"
"ds_bpermute_b32 v53, v21, v173\n"
"v_mfma_f32_16x16x32_bf16 v[44:47], v[104:107], a[16:19], v[44:47]\n"
"s_waitcnt lgkmcnt(5)\n"
"v_mfma_f32_16x16x32_bf16 v[44:47], v[108:111], a[20:23], v[44:47]\n"
"s_waitcnt vmcnt(13)\n"
"v_add_f32 v152, v152, v156\n"
"v_mfma_f32_16x16x32_bf16 v[44:47], v[112:115], a[24:27], v[44:47]\n"
"v_add_f32 v153, v153, v157\n"
"v_add_f32 v154, v154, v158\n"
"v_mfma_f32_16x16x32_bf16 v[56:59], a[252:255], a[28:31], 0\n"
"v_add_f32 v155, v155, v159\n"
"v_add_f32 v152, v152, v160\n"
"v_mfma_f32_16x16x32_bf16 v[56:59], v[92:95], a[32:35], v[56:59]\n"
"ds_write_addtid_b32 v44 offset:0\n"
"v_add_f32 v153, v153, v161\n"
"v_mfma_f32_16x16x32_bf16 v[56:59], v[96:99], a[36:39], v[56:59]\n"
"ds_write_addtid_b32 v45 offset:4640\n"
"v_add_f32 v154, v154, v162\n"
"v_mfma_f32_16x16x32_bf16 v[56:59], v[100:103], a[40:43], v[56:59]\n"
"ds_write_addtid_b32 v46 offset:9344\n"
"v_mfma_f32_16x16x32_bf16 v[56:59], v[104:107], a[44:47], v[56:59]\n"
"ds_write_addtid_b32 v47 offset:13984\n"
"v_add_f32 v155, v155, v163\n"
"v_mfma_f32_16x16x32_bf16 v[56:59], v[108:111], a[48:51], v[56:59]\n"
"v_add_f32 v152, v152, v148\n"
"v_add_f32 v153, v153, v149\n"
"v_mfma_f32_16x16x32_bf16 v[56:59], v[112:115], a[52:55], v[56:59]\n"
"v_add_f32 v154, v154, v150\n"
"v_add_f32 v155, v155, v151\n"
"v_mfma_f32_16x16x32_bf16 v[44:47], a[252:255], a[56:59], 0\n"
"s_add_u32 s30, s50, 0xffe80000\n"
"s_and_b32 s29, s30, 0xfbffffff\n"
"v_cvt_pk_bf16_f32 v60, v152, v153\n"
"v_mfma_f32_16x16x32_bf16 v[44:47], v[92:95], a[60:63], v[44:47]\n"
"ds_write_addtid_b32 v56 offset:256\n"
"v_mfma_f32_16x16x32_bf16 v[44:47], v[96:99], a[64:67], v[44:47]\n"
"ds_write_addtid_b32 v57 offset:4896\n"
"v_cvt_pk_bf16_f32 v61, v154, v155\n"
"v_mfma_f32_16x16x32_bf16 v[44:47], v[100:103], a[68:71], v[44:47]\n"
"s_waitcnt lgkmcnt(8)\n"
"ds_write_addtid_b32 v58 offset:9600\n"
"v_add_f32 v64, v64, v164\n"
"v_mfma_f32_16x16x32_bf16 v[44:47], v[104:107], a[72:75], v[44:47]\n"
"ds_write_addtid_b32 v59 offset:14240\n"
"v_add_f32 v65, v65, v165\n"
"v_mfma_f32_16x16x32_bf16 v[44:47], v[108:111], a[76:79], v[44:47]\n"
"v_add_f32 v66, v66, v166\n"
"v_add_f32 v67, v67, v167\n"
"v_mfma_f32_16x16x32_bf16 v[44:47], v[112:115], a[80:83], v[44:47]\n"
"v_add_f32 v64, v64, v48\n"
"v_mfma_f32_16x16x32_bf16 v[56:59], a[252:255], a[84:87], 0\n"
"s_waitcnt vmcnt(4)\n"
"buffer_store_dwordx2 v[0:1], v14, s[40:43], s91 offen offset:0 sc1\n"
"v_add_f32 v65, v65, v49\n"
"v_mfma_f32_16x16x32_bf16 v[56:59], v[92:95], a[88:91], v[56:59]\n"
"ds_write_addtid_b32 v44 offset:512\n"
"v_add_f32 v66, v66, v50\n"
"v_mfma_f32_16x16x32_bf16 v[56:59], v[96:99], a[92:95], v[56:59]\n"
"ds_write_addtid_b32 v45 offset:5152\n"
"v_add_f32 v67, v67, v51\n"
"v_mfma_f32_16x16x32_bf16 v[56:59], v[100:103], a[96:99], v[56:59]\n"
"ds_write_addtid_b32 v46 offset:9856\n"
"v_add_f32 v64, v64, v144\n"
"v_mfma_f32_16x16x32_bf16 v[56:59], v[104:107], a[100:103], v[56:59]\n"
"ds_write_addtid_b32 v47 offset:14496\n"
"v_mfma_f32_16x16x32_bf16 v[56:59], v[108:111], a[104:107], v[56:59]\n"
"v_add_f32 v65, v65, v145\n"
"v_add_f32 v66, v66, v146\n"
"v_mfma_f32_16x16x32_bf16 v[56:59], v[112:115], a[108:111], v[56:59]\n"
"v_add_f32 v67, v67, v147\n"
"v_cvt_pk_bf16_f32 v62, v64, v65\n"
"v_mfma_f32_16x16x32_bf16 v[44:47], a[252:255], a[112:115], 0\n"
"s_cmp_lt_u32 s31, 0x8000000\n"
"s_cselect_b32 s31, 0x80000000, s31\n"
"v_cvt_pk_bf16_f32 v63, v66, v67\n"
"v_mfma_f32_16x16x32_bf16 v[44:47], v[92:95], a[116:119], v[44:47]\n"
"buffer_store_dwordx4 v[60:63], v13, s[40:43], s29 offen sc1\n"
"ds_write_addtid_b32 v56 offset:768\n"
"ds_bpermute_b32 v54, v21, v174\n"
"v_mfma_f32_16x16x32_bf16 v[44:47], v[96:99], a[120:123], v[44:47]\n"
"ds_write_addtid_b32 v57 offset:5408\n"
"v_mfma_f32_16x16x32_bf16 v[44:47], v[100:103], a[124:127], v[44:47]\n"
"ds_write_addtid_b32 v58 offset:10112\n"
"v_mov_b32 v68, v30\n"
"v_mfma_f32_16x16x32_bf16 v[44:47], v[104:107], a[128:131], v[44:47]\n"
"ds_write_addtid_b32 v59 offset:14752\n"
"v_mov_b32 v69, v31\n"
"v_mfma_f32_16x16x32_bf16 v[44:47], v[108:111], a[132:135], v[44:47]\n"
"buffer_load_dwordx2 v[144:145], v15, s[40:43], s31 offen offset:0 sc1\n"
"v_mfma_f32_16x16x32_bf16 v[44:47], v[112:115], a[136:139], v[44:47]\n"
"ds_bpermute_b32 v55, v21, v175\n"
"v_mfma_f32_16x16x32_bf16 v[48:51], a[252:255], a[140:143], 0\n"
"v_mov_b32 v90, v32\n"
"v_mov_b32 v91, v33\n"
"v_mfma_f32_16x16x32_bf16 v[48:51], v[92:95], a[144:147], v[48:51]\n"
"s_waitcnt vmcnt(3)\n"
"ds_write_addtid_b32 v44 offset:1024\n"
"v_mfma_f32_16x16x32_bf16 v[56:59], v[38:41], v[68:71], 0\n"
"v_mfma_f32_16x16x32_bf16 v[56:59], v[38:41], v[76:79], v[56:59]\n"
"v_mfma_f32_16x16x32_bf16 v[56:59], v[38:41], v[84:87], v[56:59]\n"
"v_mfma_f32_16x16x32_bf16 v[60:63], v[38:41], v[72:75], 0\n"
"v_mfma_f32_16x16x32_bf16 v[60:63], v[38:41], v[80:83], v[60:63]\n"
"v_mfma_f32_16x16x32_bf16 v[60:63], v[38:41], v[88:91], v[60:63]\n"
"v_cvt_pk_bf16_f32 v64, v56, v57\n"
"v_cvt_pk_bf16_f32 v65, v58, v59\n"
"v_mfma_f32_16x16x32_bf16 v[48:51], v[96:99], a[148:151], v[48:51]\n"
"ds_write_addtid_b32 v45 offset:5664\n"
"v_mfma_f32_16x16x32_bf16 v[48:51], v[100:103], a[152:155], v[48:51]\n"
"ds_write_addtid_b32 v46 offset:10368\n"
"v_cvt_pk_bf16_f32 v66, v60, v61\n"
"v_mfma_f32_16x16x32_bf16 v[48:51], v[104:107], a[156:159], v[48:51]\n"
"ds_write_addtid_b32 v47 offset:15008\n"
"v_cvt_pk_bf16_f32 v67, v62, v63\n"
"v_mfma_f32_16x16x32_bf16 v[48:51], v[108:111], a[160:163], v[48:51]\n"
"s_waitcnt lgkmcnt(4)\n"
"buffer_store_dwordx4 v[52:55], v18, s[76:79], s28 offen sc1\n"
"ds_bpermute_b32 v44, v21, v64\n"
"v_mfma_f32_16x16x32_bf16 v[48:51], v[112:115], a[164:167], v[48:51]\n"
"ds_bpermute_b32 v45, v21, v65\n"
"v_mfma_f32_16x16x32_bf16 v[52:55], a[168:171], a[252:255], 0\n"
"ds_bpermute_b32 v46, v21, v66\n"
"v_mfma_f32_16x16x32_bf16 v[52:55], a[172:175], v[92:95], v[52:55]\n"
"ds_write_addtid_b32 v48 offset:1280\n"
"v_mfma_f32_16x16x32_bf16 v[52:55], a[176:179], v[96:99], v[52:55]\n"
"ds_write_addtid_b32 v49 offset:5920\n"
"v_mfma_f32_16x16x32_bf16 v[52:55], a[180:183], v[100:103], v[52:55]\n"
"ds_write_addtid_b32 v50 offset:10624\n"
"v_mfma_f32_16x16x32_bf16 v[52:55], a[184:187], v[104:107], v[52:55]\n"
"ds_write_addtid_b32 v51 offset:15264\n"
"v_mfma_f32_16x16x32_bf16 v[52:55], a[188:191], v[108:111], v[52:55]\n"
"ds_bpermute_b32 v47, v21, v67\n"
"v_mfma_f32_16x16x32_bf16 v[52:55], a[192:195], v[112:115], v[52:55]\n"
"v_mfma_f32_16x16x32_bf16 v[48:51], a[196:199], a[252:255], 0\n"
"v_mfma_f32_16x16x32_bf16 v[48:51], a[200:203], v[92:95], v[48:51]\n"
"s_waitcnt lgkmcnt(1)\n"
"v_mfma_f32_16x16x32_bf16 v[48:51], a[204:207], v[96:99], v[48:51]\n"
"s_barrier\n"
"v_mfma_f32_16x16x32_bf16 v[48:51], a[208:211], v[100:103], v[48:51]\n"
"ds_read_b128 v[56:59], v2 offset:0\n"
"v_mfma_f32_16x16x32_bf16 v[48:51], a[212:215], v[104:107], v[48:51]\n"
"s_waitcnt lgkmcnt(1)\n"
"buffer_store_dwordx4 v[44:47], v19, s[76:79], s28 offen sc1\n"
"ds_read_b128 v[60:63], v3 offset:512\n"
"ds_read_b128 v[64:67], v4 offset:1024\n"
"v_mfma_f32_16x16x32_bf16 v[48:51], a[216:219], v[108:111], v[48:51]\n"
"v_mfma_f32_16x16x32_bf16 v[48:51], a[220:223], v[112:115], v[48:51]\n"
"ds_read_b128 v[44:47], v2 offset:256\n"
"ds_read_b128 v[68:71], v3 offset:768\n"
"ds_read_b128 v[76:79], v4 offset:1280\n"
"s_waitcnt lgkmcnt(3)\n"
"v_add_f32 v56, v56, v60\n"
"v_add_f32 v57, v57, v61\n"
"v_add_f32 v58, v58, v62\n"
"v_add_f32 v59, v59, v63\n"
"v_add_f32 v56, v56, v64\n"
"v_add_f32 v57, v57, v65\n"
"v_add_f32 v58, v58, v66\n"
"v_add_f32 v59, v59, v67\n"
"v_add_f32 v56, v56, v52\n"
"v_add_f32 v57, v57, v53\n"
"v_add_f32 v58, v58, v54\n"
"v_add_f32 v59, v59, v55\n"
"s_add_u32 s28, s50, 0x0\n"
"s_and_b32 s29, s28, 0xfbffffff\n"
"v_cvt_pk_bf16_f32 v64, v56, v57\n"
"v_cvt_pk_bf16_f32 v65, v58, v59\n"
"s_waitcnt lgkmcnt(0)\n"
"v_add_f32 v44, v44, v68\n"
"v_add_f32 v45, v45, v69\n"
"v_add_f32 v46, v46, v70\n"
"v_add_f32 v47, v47, v71\n"
"v_add_f32 v44, v44, v76\n"
"s_waitcnt vmcnt(3)\n"
"v_add_f32 v45, v45, v77\n"
"v_add_f32 v46, v46, v78\n"
"v_add_f32 v47, v47, v79\n"
"v_add_f32 v44, v44, v48\n"
"v_add_f32 v45, v45, v49\n"
"s_waitcnt vmcnt(2)\n"
"v_add_f32 v46, v46, v50\n"
"v_add_f32 v47, v47, v51\n"
"v_cvt_pk_bf16_f32 v66, v44, v45\n"
"v_cvt_pk_bf16_f32 v67, v46, v47\n"
"buffer_store_dwordx4 v[64:67], v13, s[40:43], s29 offen sc1\n"
"buffer_store_dwordx2 v[0:1], v14, s[40:43], s30 offen offset:0 sc1\n"
"s_and_b32 s29, s31, 0x3ffffff\n"
"s_add_u32 s62, s40, s29\n"
"v_cmp_ne_u64 vcc, s[84:85], v[144:145]\n"
"s_addc_u32 s63, s41, 0\n"
"s_cbranch_vccnz .LJ_4t1k7s\n"
".LJ_4t1k7r:\n"
"global_load_dwordx4 v[46:49], v16, s[62:63] offset:-4096 sc1\n"
"global_load_dwordx4 v[50:53], v16, s[62:63] offset:-3072 sc1\n"
"global_load_dwordx4 v[54:57], v16, s[62:63] offset:-2048 sc1\n"
"global_load_dwordx4 v[58:61], v16, s[62:63] offset:-1024 sc1\n"
"global_load_dwordx4 v[62:65], v17, s[62:63] offset:0 sc1\n"
"s_waitcnt vmcnt(6)\n"
"buffer_store_dwordx2 v[0:1], v14, s[40:43], s28 offen offset:0 sc1\n"
"global_load_dwordx4 v[70:73], v16, s[62:63] offset:0 sc1\n"
"s_mov_b32 s90, s31\n"
"global_load_dwordx4 v[74:77], v16, s[62:63] offset:1024 sc1\n"
"v_mov_b32 v44, v26\n"
"v_mov_b32 v45, v27\n"
"v_mov_b32 v66, v28\n"
"v_mov_b32 v67, v29\n"
"global_load_dwordx4 v[78:81], v16, s[62:63] offset:2048 sc1\n"
"global_load_dwordx4 v[82:85], v16, s[62:63] offset:3072 sc1\n"
"global_load_dwordx4 v[86:89], v17, s[62:63] offset:1024 sc1\n"
"s_waitcnt vmcnt(6)\n"
"v_mfma_f32_16x16x32_bf16 v[164:167], v[38:41], v[44:47], 0\n"
"s_cmp_ge_i32 s90, s80\n"
"s_cbranch_scc1 .LJ_wt1k7s\n"
".LJ_wt1k7r:\n"
"v_mfma_f32_16x16x32_bf16 v[164:167], v[38:41], v[52:55], v[164:167]\n"
"s_lshr_b32 s28, s90, s81\n"
"v_mfma_f32_16x16x32_bf16 v[164:167], v[38:41], v[60:63], v[164:167]\n"
"s_bitcmp1_b32 s28, 15\n"
"s_cselect_b32 s31, s83, s82\n"
"v_mfma_f32_16x16x32_bf16 v[168:171], v[38:41], v[48:51], 0\n"
"s_add_u32 s28, s28, s31\n"
"v_mfma_f32_16x16x32_bf16 v[168:171], v[38:41], v[56:59], v[168:171]\n"
"v_mfma_f32_16x16x32_bf16 v[168:171], v[38:41], v[64:67], v[168:171]\n"
"v_cvt_pk_bf16_f32 v172, v164, v165\n"
"v_cvt_pk_bf16_f32 v173, v166, v167\n"
"s_nop 5\n"
"v_cvt_pk_bf16_f32 v174, v168, v169\n"
"v_cvt_pk_bf16_f32 v175, v170, v171\n"
"s_add_u32 s31, s50, 0xfff80000\n"
"ds_bpermute_b32 v52, v21, v172\n"
"ds_bpermute_b32 v53, v21, v173\n"
"s_cmp_lt_u32 s31, 0x8000000\n"
"s_cselect_b32 s31, 0x80000000, s31\n"
"ds_bpermute_b32 v54, v21, v174\n"
"v_mov_b32 v68, v30\n"
"v_mov_b32 v69, v31\n"
"buffer_load_dwordx2 v[144:145], v15, s[40:43], s31 offen offset:0 sc1\n"
"ds_bpermute_b32 v55, v21, v175\n"
"v_mov_b32 v90, v32\n"
"v_mov_b32 v91, v33\n"
"s_waitcnt vmcnt(1)\n"
"v_mfma_f32_16x16x32_bf16 v[56:59], v[38:41], v[68:71], 0\n"
"v_mfma_f32_16x16x32_bf16 v[56:59], v[38:41], v[76:79], v[56:59]\n"
"v_mfma_f32_16x16x32_bf16 v[56:59], v[38:41], v[84:87], v[56:59]\n"
"v_mfma_f32_16x16x32_bf16 v[60:63], v[38:41], v[72:75], 0\n"
"v_mfma_f32_16x16x32_bf16 v[60:63], v[38:41], v[80:83], v[60:63]\n"
"v_mfma_f32_16x16x32_bf16 v[60:63], v[38:41], v[88:91], v[60:63]\n"
"v_cvt_pk_bf16_f32 v64, v56, v57\n"
"v_cvt_pk_bf16_f32 v65, v58, v59\n"
"s_nop 5\n"
"v_cvt_pk_bf16_f32 v66, v60, v61\n"
"v_cvt_pk_bf16_f32 v67, v62, v63\n"
"s_waitcnt lgkmcnt(0)\n"
"buffer_store_dwordx4 v[52:55], v18, s[76:79], s28 offen sc1\n"
"ds_bpermute_b32 v44, v21, v64\n"
"ds_bpermute_b32 v45, v21, v65\n"
"ds_bpermute_b32 v46, v21, v66\n"
"ds_bpermute_b32 v47, v21, v67\n"
"s_waitcnt lgkmcnt(0)\n"
"buffer_store_dwordx4 v[44:47], v19, s[76:79], s28 offen sc1\n"
"s_waitcnt vmcnt(2)\n"
"s_and_b32 s29, s31, 0x3ffffff\n"
"s_add_u32 s62, s40, s29\n"
"v_cmp_ne_u64 vcc, s[84:85], v[144:145]\n"
"s_addc_u32 s63, s41, 0\n"
"s_cbranch_vccnz .LJ_4t1k0s\n"
".LJ_4t1k0r:\n"
"global_load_dwordx4 v[46:49], v16, s[62:63] offset:-4096 sc1\n"
"global_load_dwordx4 v[50:53], v16, s[62:63] offset:-3072 sc1\n"
"global_load_dwordx4 v[54:57], v16, s[62:63] offset:-2048 sc1\n"
"global_load_dwordx4 v[58:61], v16, s[62:63] offset:-1024 sc1\n"
"global_load_dwordx4 v[62:65], v17, s[62:63] offset:0 sc1\n"
"global_load_dwordx4 v[70:73], v16, s[62:63] offset:0 sc1\n"
"s_mov_b32 s90, s31\n"
"global_load_dwordx4 v[74:77], v16, s[62:63] offset:1024 sc1\n"
"v_mov_b32 v44, v26\n"
"v_mov_b32 v45, v27\n"
"v_mov_b32 v66, v28\n"
"v_mov_b32 v67, v29\n"
"global_load_dwordx4 v[78:81], v16, s[62:63] offset:2048 sc1\n"
"global_load_dwordx4 v[82:85], v16, s[62:63] offset:3072 sc1\n"
"global_load_dwordx4 v[86:89], v17, s[62:63] offset:1024 sc1\n"
"s_waitcnt vmcnt(5)\n"
"v_mfma_f32_16x16x32_bf16 v[164:167], v[38:41], v[44:47], 0\n"
"s_cmp_ge_i32 s90, s80\n"
"s_cbranch_scc1 .LJ_wt1k0s\n"
".LJ_wt1k0r:\n"
"v_mfma_f32_16x16x32_bf16 v[164:167], v[38:41], v[52:55], v[164:167]\n"
"s_lshr_b32 s28, s90, s81\n"
"v_mfma_f32_16x16x32_bf16 v[164:167], v[38:41], v[60:63], v[164:167]\n"
"s_bitcmp1_b32 s28, 15\n"
"s_cselect_b32 s31, s83, s82\n"
"v_mfma_f32_16x16x32_bf16 v[168:171], v[38:41], v[48:51], 0\n"
"s_add_u32 s28, s28, s31\n"
"v_mfma_f32_16x16x32_bf16 v[168:171], v[38:41], v[56:59], v[168:171]\n"
"v_mfma_f32_16x16x32_bf16 v[168:171], v[38:41], v[64:67], v[168:171]\n"
"v_cvt_pk_bf16_f32 v172, v164, v165\n"
"v_cvt_pk_bf16_f32 v173, v166, v167\n"
"s_nop 5\n"
"v_cvt_pk_bf16_f32 v174, v168, v169\n"
"v_cvt_pk_bf16_f32 v175, v170, v171\n"
"ds_bpermute_b32 v52, v21, v172\n"
"ds_bpermute_b32 v53, v21, v173\n"
"ds_bpermute_b32 v54, v21, v174\n"
"v_mov_b32 v68, v30\n"
"v_mov_b32 v69, v31\n"
"ds_bpermute_b32 v55, v21, v175\n"
"v_mov_b32 v90, v32\n"
"v_mov_b32 v91, v33\n"
"s_waitcnt vmcnt(0)\n"
"v_mfma_f32_16x16x32_bf16 v[56:59], v[38:41], v[68:71], 0\n"
"v_mfma_f32_16x16x32_bf16 v[56:59], v[38:41], v[76:79], v[56:59]\n"
"v_mfma_f32_16x16x32_bf16 v[56:59], v[38:41], v[84:87], v[56:59]\n"
"v_mfma_f32_16x16x32_bf16 v[60:63], v[38:41], v[72:75], 0\n"
"v_mfma_f32_16x16x32_bf16 v[60:63], v[38:41], v[80:83], v[60:63]\n"
"v_mfma_f32_16x16x32_bf16 v[60:63], v[38:41], v[88:91], v[60:63]\n"
"v_cvt_pk_bf16_f32 v64, v56, v57\n"
"v_cvt_pk_bf16_f32 v65, v58, v59\n"
"s_nop 5\n"
"v_cvt_pk_bf16_f32 v66, v60, v61\n"
"v_cvt_pk_bf16_f32 v67, v62, v63\n"
"s_waitcnt lgkmcnt(0)\n"
"buffer_store_dwordx4 v[52:55], v18, s[76:79], s28 offen sc1\n"
"ds_bpermute_b32 v44, v21, v64\n"
"ds_bpermute_b32 v45, v21, v65\n"
"ds_bpermute_b32 v46, v21, v66\n"
"ds_bpermute_b32 v47, v21, v67\n"
"s_waitcnt lgkmcnt(0)\n"
"buffer_store_dwordx4 v[44:47], v19, s[76:79], s28 offen sc1\n"
"s_branch .LJ_end\n"
".LJ_t2:\n"
"v_mfma_f32_16x16x32_bf16 v[144:147], a[220:223], a[248:251], v[144:147]\n"
"s_waitcnt vmcnt(28)\n"
"ds_read_b128 v[64:67], v2 offset:3328\n"
"ds_read_b128 v[164:167], v3 offset:3840\n"
"v_mfma_f32_16x16x32_bf16 v[44:47], a[252:255], a[0:3], 0\n"
"ds_read_b128 v[48:51], v4 offset:4352\n"
"v_mfma_f32_16x16x32_bf16 v[44:47], v[92:95], a[4:7], v[44:47]\n"
"s_waitcnt vmcnt(26)\n"
"v_cvt_pk_bf16_f32 v174, v168, v169\n"
"v_cvt_pk_bf16_f32 v175, v170, v171\n"
"v_mfma_f32_16x16x32_bf16 v[44:47], v[96:99], a[8:11], v[44:47]\n"
"s_add_u32 s31, s50, 0xff980000\n"
"ds_bpermute_b32 v52, v21, v172\n"
"v_mfma_f32_16x16x32_bf16 v[44:47], v[100:103], a[12:15], v[44:47]\n"
"s_waitcnt vmcnt(24)\n"
"ds_bpermute_b32 v53, v21, v173\n"
"v_mfma_f32_16x16x32_bf16 v[44:47], v[104:107], a[16:19], v[44:47]\n"
"s_waitcnt lgkmcnt(5)\n"
"v_mfma_f32_16x16x32_bf16 v[44:47], v[108:111], a[20:23], v[44:47]\n"
"s_waitcnt vmcnt(21)\n"
"v_add_f32 v152, v152, v156\n"
"v_mfma_f32_16x16x32_bf16 v[44:47], v[112:115], a[24:27], v[44:47]\n"
"v_add_f32 v153, v153, v157\n"
"v_add_f32 v154, v154, v158\n"
"v_mfma_f32_16x16x32_bf16 v[56:59], a[252:255], a[28:31], 0\n"
"v_add_f32 v155, v155, v159\n"
"v_add_f32 v152, v152, v160\n"
"v_mfma_f32_16x16x32_bf16 v[56:59], v[92:95], a[32:35], v[56:59]\n"
"ds_write_addtid_b32 v44 offset:0\n"
"v_add_f32 v153, v153, v161\n"
"v_mfma_f32_16x16x32_bf16 v[56:59], v[96:99], a[36:39], v[56:59]\n"
"ds_write_addtid_b32 v45 offset:4640\n"
"v_add_f32 v154, v154, v162\n"
"v_mfma_f32_16x16x32_bf16 v[56:59], v[100:103], a[40:43], v[56:59]\n"
"ds_write_addtid_b32 v46 offset:9344\n"
"v_mfma_f32_16x16x32_bf16 v[56:59], v[104:107], a[44:47], v[56:59]\n"
"ds_write_addtid_b32 v47 offset:13984\n"
"v_add_f32 v155, v155, v163\n"
"v_mfma_f32_16x16x32_bf16 v[56:59], v[108:111], a[48:51], v[56:59]\n"
"v_add_f32 v152, v152, v148\n"
"v_add_f32 v153, v153, v149\n"
"v_mfma_f32_16x16x32_bf16 v[56:59], v[112:115], a[52:55], v[56:59]\n"
"v_add_f32 v154, v154, v150\n"
"v_add_f32 v155, v155, v151\n"
"v_mfma_f32_16x16x32_bf16 v[44:47], a[252:255], a[56:59], 0\n"
"s_add_u32 s30, s50, 0xffd00000\n"
"s_and_b32 s29, s30, 0xfbffffff\n"
"v_cvt_pk_bf16_f32 v60, v152, v153\n"
"v_mfma_f32_16x16x32_bf16 v[44:47], v[92:95], a[60:63], v[44:47]\n"
"ds_write_addtid_b32 v56 offset:256\n"
"v_mfma_f32_16x16x32_bf16 v[44:47], v[96:99], a[64:67], v[44:47]\n"
"ds_write_addtid_b32 v57 offset:4896\n"
"v_cvt_pk_bf16_f32 v61, v154, v155\n"
"v_mfma_f32_16x16x32_bf16 v[44:47], v[100:103], a[68:71], v[44:47]\n"
"s_waitcnt lgkmcnt(8)\n"
"ds_write_addtid_b32 v58 offset:9600\n"
"v_add_f32 v64, v64, v164\n"
"v_mfma_f32_16x16x32_bf16 v[44:47], v[104:107], a[72:75], v[44:47]\n"
"ds_write_addtid_b32 v59 offset:14240\n"
"v_add_f32 v65, v65, v165\n"
"v_mfma_f32_16x16x32_bf16 v[44:47], v[108:111], a[76:79], v[44:47]\n"
"v_add_f32 v66, v66, v166\n"
"v_add_f32 v67, v67, v167\n"
"v_mfma_f32_16x16x32_bf16 v[44:47], v[112:115], a[80:83], v[44:47]\n"
"v_add_f32 v64, v64, v48\n"
"v_mfma_f32_16x16x32_bf16 v[56:59], a[252:255], a[84:87], 0\n"
"s_waitcnt vmcnt(5)\n"
"buffer_store_dwordx2 v[0:1], v14, s[40:43], s91 offen offset:0 sc1\n"
"v_add_f32 v65, v65, v49\n"
"v_mfma_f32_16x16x32_bf16 v[56:59], v[92:95], a[88:91], v[56:59]\n"
"ds_write_addtid_b32 v44 offset:512\n"
"v_add_f32 v66, v66, v50\n"
"v_mfma_f32_16x16x32_bf16 v[56:59], v[96:99], a[92:95], v[56:59]\n"
"ds_write_addtid_b32 v45 offset:5152\n"
"v_add_f32 v67, v67, v51\n"
"v_mfma_f32_16x16x32_bf16 v[56:59], v[100:103], a[96:99], v[56:59]\n"
"ds_write_addtid_b32 v46 offset:9856\n"
"v_add_f32 v64, v64, v144\n"
"v_mfma_f32_16x16x32_bf16 v[56:59], v[104:107], a[100:103], v[56:59]\n"
"ds_write_addtid_b32 v47 offset:14496\n"
"v_mfma_f32_16x16x32_bf16 v[56:59], v[108:111], a[104:107], v[56:59]\n"
"v_add_f32 v65, v65, v145\n"
"v_add_f32 v66, v66, v146\n"
"v_mfma_f32_16x16x32_bf16 v[56:59], v[112:115], a[108:111], v[56:59]\n"
"v_add_f32 v67, v67, v147\n"
"v_cvt_pk_bf16_f32 v62, v64, v65\n"
"v_mfma_f32_16x16x32_bf16 v[44:47], a[252:255], a[112:115], 0\n"
"s_cmp_lt_u32 s31, 0x8000000\n"
"s_cselect_b32 s31, 0x80000000, s31\n"
"v_cvt_pk_bf16_f32 v63, v66, v67\n"
"v_mfma_f32_16x16x32_bf16 v[44:47], v[92:95], a[116:119], v[44:47]\n"
"buffer_store_dwordx4 v[60:63], v13, s[40:43], s29 offen sc1\n"
"ds_write_addtid_b32 v56 offset:768\n"
"ds_bpermute_b32 v54, v21, v174\n"
"v_mfma_f32_16x16x32_bf16 v[44:47], v[96:99], a[120:123], v[44:47]\n"
"ds_write_addtid_b32 v57 offset:5408\n"
"v_mfma_f32_16x16x32_bf16 v[44:47], v[100:103], a[124:127], v[44:47]\n"
"ds_write_addtid_b32 v58 offset:10112\n"
"v_mov_b32 v68, v30\n"
"v_mfma_f32_16x16x32_bf16 v[44:47], v[104:107], a[128:131], v[44:47]\n"
"ds_write_addtid_b32 v59 offset:14752\n"
"v_mov_b32 v69, v31\n"
"v_mfma_f32_16x16x32_bf16 v[44:47], v[108:111], a[132:135], v[44:47]\n"
"buffer_load_dwordx2 v[144:145], v15, s[40:43], s31 offen offset:0 sc1\n"
"v_mfma_f32_16x16x32_bf16 v[44:47], v[112:115], a[136:139], v[44:47]\n"
"ds_bpermute_b32 v55, v21, v175\n"
"v_mfma_f32_16x16x32_bf16 v[48:51], a[252:255], a[140:143], 0\n"
"v_mov_b32 v90, v32\n"
"v_mov_b32 v91, v33\n"
"v_mfma_f32_16x16x32_bf16 v[48:51], v[92:95], a[144:147], v[48:51]\n"
"s_waitcnt vmcnt(3)\n"
"ds_write_addtid_b32 v44 offset:1024\n"
"v_mfma_f32_16x16x32_bf16 v[56:59], v[38:41], v[68:71], 0\n"
"v_mfma_f32_16x16x32_bf16 v[56:59], v[38:41], v[76:79], v[56:59]\n"
"v_mfma_f32_16x16x32_bf16 v[56:59], v[38:41], v[84:87], v[56:59]\n"
"v_mfma_f32_16x16x32_bf16 v[60:63], v[38:41], v[72:75], 0\n"
"v_mfma_f32_16x16x32_bf16 v[60:63], v[38:41], v[80:83], v[60:63]\n"
"v_mfma_f32_16x16x32_bf16 v[60:63], v[38:41], v[88:91], v[60:63]\n"
"v_cvt_pk_bf16_f32 v64, v56, v57\n"
"v_cvt_pk_bf16_f32 v65, v58, v59\n"
"v_mfma_f32_16x16x32_bf16 v[48:51], v[96:99], a[148:151], v[48:51]\n"
"ds_write_addtid_b32 v45 offset:5664\n"
"v_mfma_f32_16x16x32_bf16 v[48:51], v[100:103], a[152:155], v[48:51]\n"
"ds_write_addtid_b32 v46 offset:10368\n"
"v_cvt_pk_bf16_f32 v66, v60, v61\n"
"v_mfma_f32_16x16x32_bf16 v[48:51], v[104:107], a[156:159], v[48:51]\n"
"ds_write_addtid_b32 v47 offset:15008\n"
"v_cvt_pk_bf16_f32 v67, v62, v63\n"
"v_mfma_f32_16x16x32_bf16 v[48:51], v[108:111], a[160:163], v[48:51]\n"
"s_waitcnt lgkmcnt(4)\n"
"buffer_store_dwordx4 v[52:55], v18, s[76:79], s28 offen sc1\n"
"ds_bpermute_b32 v44, v21, v64\n"
"v_mfma_f32_16x16x32_bf16 v[48:51], v[112:115], a[164:167], v[48:51]\n"
"ds_bpermute_b32 v45, v21, v65\n"
"v_mfma_f32_16x16x32_bf16 v[52:55], a[168:171], a[252:255], 0\n"
"ds_bpermute_b32 v46, v21, v66\n"
"v_mfma_f32_16x16x32_bf16 v[52:55], a[172:175], v[92:95], v[52:55]\n"
"ds_write_addtid_b32 v48 offset:1280\n"
"v_mfma_f32_16x16x32_bf16 v[52:55], a[176:179], v[96:99], v[52:55]\n"
"ds_write_addtid_b32 v49 offset:5920\n"
"v_mfma_f32_16x16x32_bf16 v[52:55], a[180:183], v[100:103], v[52:55]\n"
"ds_write_addtid_b32 v50 offset:10624\n"
"v_mfma_f32_16x16x32_bf16 v[52:55], a[184:187], v[104:107], v[52:55]\n"
"ds_write_addtid_b32 v51 offset:15264\n"
"v_mfma_f32_16x16x32_bf16 v[52:55], a[188:191], v[108:111], v[52:55]\n"
"ds_bpermute_b32 v47, v21, v67\n"
"v_mfma_f32_16x16x32_bf16 v[52:55], a[192:195], v[112:115], v[52:55]\n"
"v_mfma_f32_16x16x32_bf16 v[48:51], a[196:199], a[252:255], 0\n"
"v_mfma_f32_16x16x32_bf16 v[48:51], a[200:203], v[92:95], v[48:51]\n"
"s_waitcnt lgkmcnt(1)\n"
"v_mfma_f32_16x16x32_bf16 v[48:51], a[204:207], v[96:99], v[48:51]\n"
"s_barrier\n"
"v_mfma_f32_16x16x32_bf16 v[48:51], a[208:211], v[100:103], v[48:51]\n"
"ds_read_b128 v[56:59], v2 offset:0\n"
"v_mfma_f32_16x16x32_bf16 v[48:51], a[212:215], v[104:107], v[48:51]\n"
"s_waitcnt lgkmcnt(1)\n"
"buffer_store_dwordx4 v[44:47], v19, s[76:79], s28 offen sc1\n"
"ds_read_b128 v[60:63], v3 offset:512\n"
"ds_read_b128 v[64:67], v4 offset:1024\n"
"v_mfma_f32_16x16x32_bf16 v[48:51], a[216:219], v[108:111], v[48:51]\n"
"v_mfma_f32_16x16x32_bf16 v[48:51], a[220:223], v[112:115], v[48:51]\n"
"s_waitcnt vmcnt(17)\n"
"ds_read_b128 v[44:47], v2 offset:256\n"
"ds_read_b128 v[68:71], v3 offset:768\n"
"v_mfma_f32_16x16x32_bf16 v[72:75], v[116:119], a[0:3], 0\n"
"ds_read_b128 v[76:79], v4 offset:1280\n"
"v_mfma_f32_16x16x32_bf16 v[72:75], v[120:123], a[4:7], v[72:75]\n"
"s_waitcnt vmcnt(15)\n"
"v_mfma_f32_16x16x32_bf16 v[72:75], v[124:127], a[8:11], v[72:75]\n"
"v_mfma_f32_16x16x32_bf16 v[72:75], v[128:131], a[12:15], v[72:75]\n"
"s_waitcnt vmcnt(12)\n"
"v_mfma_f32_16x16x32_bf16 v[72:75], v[132:135], a[16:19], v[72:75]\n"
"s_waitcnt lgkmcnt(3)\n"
"v_mfma_f32_16x16x32_bf16 v[72:75], v[136:139], a[20:23], v[72:75]\n"
"s_waitcnt vmcnt(9)\n"
"v_add_f32 v56, v56, v60\n"
"v_mfma_f32_16x16x32_bf16 v[72:75], v[140:143], a[24:27], v[72:75]\n"
"v_add_f32 v57, v57, v61\n"
"v_add_f32 v58, v58, v62\n"
"v_mfma_f32_16x16x32_bf16 v[80:83], v[116:119], a[28:31], 0\n"
"v_add_f32 v59, v59, v63\n"
"v_add_f32 v56, v56, v64\n"
"v_mfma_f32_16x16x32_bf16 v[80:83], v[120:123], a[32:35], v[80:83]\n"
"ds_write_addtid_b32 v72 offset:1536\n"
"v_add_f32 v57, v57, v65\n"
"v_mfma_f32_16x16x32_bf16 v[80:83], v[124:127], a[36:39], v[80:83]\n"
"ds_write_addtid_b32 v73 offset:6176\n"
"v_add_f32 v58, v58, v66\n"
"v_mfma_f32_16x16x32_bf16 v[80:83], v[128:131], a[40:43], v[80:83]\n"
"ds_write_addtid_b32 v74 offset:10880\n"
"v_mfma_f32_16x16x32_bf16 v[80:83], v[132:135], a[44:47], v[80:83]\n"
"ds_write_addtid_b32 v75 offset:15520\n"
"v_add_f32 v59, v59, v67\n"
"v_mfma_f32_16x16x32_bf16 v[80:83], v[136:139], a[48:51], v[80:83]\n"
"v_add_f32 v56, v56, v52\n"
"v_add_f32 v57, v57, v53\n"
"v_mfma_f32_16x16x32_bf16 v[80:83], v[140:143], a[52:55], v[80:83]\n"
"v_add_f32 v58, v58, v54\n"
"v_add_f32 v59, v59, v55\n"
"v_mfma_f32_16x16x32_bf16 v[60:63], v[116:119], a[56:59], 0\n"
"s_add_u32 s28, s50, 0xffe80000\n"
"s_and_b32 s29, s28, 0xfbffffff\n"
"v_cvt_pk_bf16_f32 v64, v56, v57\n"
"v_mfma_f32_16x16x32_bf16 v[60:63], v[120:123], a[60:63], v[60:63]\n"
"ds_write_addtid_b32 v80 offset:1792\n"
"v_mfma_f32_16x16x32_bf16 v[60:63], v[124:127], a[64:67], v[60:63]\n"
"ds_write_addtid_b32 v81 offset:6432\n"
"v_cvt_pk_bf16_f32 v65, v58, v59\n"
"v_mfma_f32_16x16x32_bf16 v[60:63], v[128:131], a[68:71], v[60:63]\n"
"s_waitcnt lgkmcnt(6)\n"
"ds_write_addtid_b32 v82 offset:11136\n"
"v_add_f32 v44, v44, v68\n"
"v_mfma_f32_16x16x32_bf16 v[60:63], v[132:135], a[72:75], v[60:63]\n"
"ds_write_addtid_b32 v83 offset:15776\n"
"v_add_f32 v45, v45, v69\n"
"v_mfma_f32_16x16x32_bf16 v[60:63], v[136:139], a[76:79], v[60:63]\n"
"v_add_f32 v46, v46, v70\n"
"v_add_f32 v47, v47, v71\n"
"v_mfma_f32_16x16x32_bf16 v[60:63], v[140:143], a[80:83], v[60:63]\n"
"v_add_f32 v44, v44, v76\n"
"v_mfma_f32_16x16x32_bf16 v[52:55], v[116:119], a[84:87], 0\n"
"s_waitcnt vmcnt(3)\n"
"v_add_f32 v45, v45, v77\n"
"v_add_f32 v46, v46, v78\n"
"v_mfma_f32_16x16x32_bf16 v[52:55], v[120:123], a[88:91], v[52:55]\n"
"ds_write_addtid_b32 v60 offset:2048\n"
"v_add_f32 v47, v47, v79\n"
"v_mfma_f32_16x16x32_bf16 v[52:55], v[124:127], a[92:95], v[52:55]\n"
"ds_write_addtid_b32 v61 offset:6688\n"
"v_add_f32 v44, v44, v48\n"
"v_mfma_f32_16x16x32_bf16 v[52:55], v[128:131], a[96:99], v[52:55]\n"
"ds_write_addtid_b32 v62 offset:11392\n"
"v_add_f32 v45, v45, v49\n"
"v_mfma_f32_16x16x32_bf16 v[52:55], v[132:135], a[100:103], v[52:55]\n"
"s_waitcnt vmcnt(2)\n"
"ds_write_addtid_b32 v63 offset:16032\n"
"v_mfma_f32_16x16x32_bf16 v[52:55], v[136:139], a[104:107], v[52:55]\n"
"v_add_f32 v46, v46, v50\n"
"v_add_f32 v47, v47, v51\n"
"v_mfma_f32_16x16x32_bf16 v[52:55], v[140:143], a[108:111], v[52:55]\n"
"v_cvt_pk_bf16_f32 v66, v44, v45\n"
"v_cvt_pk_bf16_f32 v67, v46, v47\n"
"v_mfma_f32_16x16x32_bf16 v[68:71], v[116:119], a[112:115], 0\n"
"buffer_store_dwordx4 v[64:67], v13, s[40:43], s29 offen sc1\n"
"v_mfma_f32_16x16x32_bf16 v[68:71], v[120:123], a[116:119], v[68:71]\n"
"buffer_store_dwordx2 v[0:1], v14, s[40:43], s30 offen offset:0 sc1\n"
"ds_write_addtid_b32 v52 offset:2304\n"
"v_mfma_f32_16x16x32_bf16 v[68:71], v[124:127], a[120:123], v[68:71]\n"
"s_and_b32 s29, s31, 0x3ffffff\n"
"ds_write_addtid_b32 v53 offset:6944\n"
"v_mfma_f32_16x16x32_bf16 v[68:71], v[128:131], a[124:127], v[68:71]\n"
"s_add_u32 s62, s40, s29\n"
"ds_write_addtid_b32 v54 offset:11648\n"
"v_cmp_ne_u64 vcc, s[84:85], v[144:145]\n"
"v_mfma_f32_16x16x32_bf16 v[68:71], v[132:135], a[128:131], v[68:71]\n"
"s_addc_u32 s63, s41, 0\n"
"ds_write_addtid_b32 v55 offset:16288\n"
"v_mfma_f32_16x16x32_bf16 v[68:71], v[136:139], a[132:135], v[68:71]\n"
"s_cbranch_vccnz .LJ_4t2k7s\n"
".LJ_4t2k7r:\n"
"v_mfma_f32_16x16x32_bf16 v[68:71], v[140:143], a[136:139], v[68:71]\n"
"global_load_dwordx4 v[46:49], v16, s[62:63] offset:-4096 sc1\n"
"v_mfma_f32_16x16x32_bf16 v[72:75], v[116:119], a[140:143], 0\n"
"v_mfma_f32_16x16x32_bf16 v[72:75], v[120:123], a[144:147], v[72:75]\n"
"ds_write_addtid_b32 v68 offset:2560\n"
"v_mfma_f32_16x16x32_bf16 v[72:75], v[124:127], a[148:151], v[72:75]\n"
"ds_write_addtid_b32 v69 offset:7200\n"
"v_mfma_f32_16x16x32_bf16 v[72:75], v[128:131], a[152:155], v[72:75]\n"
"ds_write_addtid_b32 v70 offset:11904\n"
"v_mfma_f32_16x16x32_bf16 v[72:75], v[132:135], a[156:159], v[72:75]\n"
"ds_write_addtid_b32 v71 offset:16544\n"
"global_load_dwordx4 v[50:53], v16, s[62:63] offset:-3072 sc1\n"
"v_mfma_f32_16x16x32_bf16 v[72:75], v[136:139], a[160:163], v[72:75]\n"
"v_mfma_f32_16x16x32_bf16 v[72:75], v[140:143], a[164:167], v[72:75]\n"
"v_mfma_f32_16x16x32_bf16 v[68:71], a[168:171], v[116:119], 0\n"
"v_mfma_f32_16x16x32_bf16 v[68:71], a[172:175], v[120:123], v[68:71]\n"
"ds_write_addtid_b32 v72 offset:2816\n"
"v_mfma_f32_16x16x32_bf16 v[68:71], a[176:179], v[124:127], v[68:71]\n"
"ds_write_addtid_b32 v73 offset:7456\n"
"global_load_dwordx4 v[54:57], v16, s[62:63] offset:-2048 sc1\n"
"v_mfma_f32_16x16x32_bf16 v[68:71], a[180:183], v[128:131], v[68:71]\n"
"ds_write_addtid_b32 v74 offset:12160\n"
"v_mfma_f32_16x16x32_bf16 v[68:71], a[184:187], v[132:135], v[68:71]\n"
"ds_write_addtid_b32 v75 offset:16800\n"
"v_mfma_f32_16x16x32_bf16 v[68:71], a[188:191], v[136:139], v[68:71]\n"
"v_mfma_f32_16x16x32_bf16 v[68:71], a[192:195], v[140:143], v[68:71]\n"
"v_mfma_f32_16x16x32_bf16 v[144:147], a[196:199], v[116:119], 0\n"
"global_load_dwordx4 v[58:61], v16, s[62:63] offset:-1024 sc1\n"
"v_mfma_f32_16x16x32_bf16 v[144:147], a[200:203], v[120:123], v[144:147]\n"
"s_waitcnt lgkmcnt(0)\n"
"v_mfma_f32_16x16x32_bf16 v[144:147], a[204:207], v[124:127], v[144:147]\n"
"s_barrier\n"
"v_mfma_f32_16x16x32_bf16 v[144:147], a[208:211], v[128:131], v[144:147]\n"
"ds_read_b128 v[72:75], v2 offset:1536\n"
"v_mfma_f32_16x16x32_bf16 v[144:147], a[212:215], v[132:135], v[144:147]\n"
"ds_read_b128 v[76:79], v3 offset:2048\n"
"ds_read_b128 v[80:83], v4 offset:2560\n"
"v_mfma_f32_16x16x32_bf16 v[144:147], a[216:219], v[136:139], v[144:147]\n"
"v_mfma_f32_16x16x32_bf16 v[144:147], a[220:223], v[140:143], v[144:147]\n"
"ds_read_b128 v[148:151], v2 offset:1792\n"
"ds_read_b128 v[84:87], v3 offset:2304\n"
"ds_read_b128 v[136:139], v4 offset:2816\n"
"global_load_dwordx4 v[62:65], v17, s[62:63] offset:0 sc1\n"
"s_waitcnt lgkmcnt(3)\n"
"v_add_f32 v72, v72, v76\n"
"v_add_f32 v73, v73, v77\n"
"v_add_f32 v74, v74, v78\n"
"v_add_f32 v75, v75, v79\n"
"v_add_f32 v72, v72, v80\n"
"v_add_f32 v73, v73, v81\n"
"v_add_f32 v74, v74, v82\n"
"v_add_f32 v75, v75, v83\n"
"v_add_f32 v72, v72, v68\n"
"v_add_f32 v73, v73, v69\n"
"v_add_f32 v74, v74, v70\n"
"v_add_f32 v75, v75, v71\n"
"s_add_u32 s91, s50, 0x0\n"
"s_and_b32 s30, s91, 0xfbffffff\n"
"v_cvt_pk_bf16_f32 v140, v72, v73\n"
"v_cvt_pk_bf16_f32 v141, v74, v75\n"
"s_waitcnt lgkmcnt(0)\n"
"v_add_f32 v148, v148, v84\n"
"v_add_f32 v149, v149, v85\n"
"v_add_f32 v150, v150, v86\n"
"v_add_f32 v151, v151, v87\n"
"s_waitcnt vmcnt(6)\n"
"v_add_f32 v148, v148, v136\n"
"buffer_store_dwordx2 v[0:1], v14, s[40:43], s28 offen offset:0 sc1\n"
"v_add_f32 v149, v149, v137\n"
"v_add_f32 v150, v150, v138\n"
"v_add_f32 v151, v151, v139\n"
"v_add_f32 v148, v148, v144\n"
"v_add_f32 v149, v149, v145\n"
"v_add_f32 v150, v150, v146\n"
"v_add_f32 v151, v151, v147\n"
"v_cvt_pk_bf16_f32 v142, v148, v149\n"
"global_load_dwordx4 v[70:73], v16, s[62:63] offset:0 sc1\n"
"v_cvt_pk_bf16_f32 v143, v150, v151\n"
"buffer_store_dwordx4 v[140:143], v13, s[40:43], s30 offen sc1\n"
"s_mov_b32 s90, s31\n"
"global_load_dwordx4 v[74:77], v16, s[62:63] offset:1024 sc1\n"
"v_mov_b32 v44, v26\n"
"v_mov_b32 v45, v27\n"
"v_mov_b32 v66, v28\n"
"v_mov_b32 v67, v29\n"
"global_load_dwordx4 v[78:81], v16, s[62:63] offset:2048 sc1\n"
"global_load_dwordx4 v[82:85], v16, s[62:63] offset:3072 sc1\n"
"global_load_dwordx4 v[86:89], v17, s[62:63] offset:1024 sc1\n"
"s_waitcnt vmcnt(7)\n"
"v_mfma_f32_16x16x32_bf16 v[164:167], v[38:41], v[44:47], 0\n"
"s_cmp_ge_i32 s90, s80\n"
"s_cbranch_scc1 .LJ_wt2k7s\n"
".LJ_wt2k7r:\n"
"v_mfma_f32_16x16x32_bf16 v[164:167], v[38:41], v[52:55], v[164:167]\n"
"s_lshr_b32 s28, s90, s81\n"
"v_mfma_f32_16x16x32_bf16 v[164:167], v[38:41], v[60:63], v[164:167]\n"
"s_bitcmp1_b32 s28, 15\n"
"s_cselect_b32 s31, s83, s82\n"
"v_mfma_f32_16x16x32_bf16 v[168:171], v[38:41], v[48:51], 0\n"
"s_add_u32 s28, s28, s31\n"
"v_mfma_f32_16x16x32_bf16 v[168:171], v[38:41], v[56:59], v[168:171]\n"
"v_mfma_f32_16x16x32_bf16 v[168:171], v[38:41], v[64:67], v[168:171]\n"
"v_cvt_pk_bf16_f32 v172, v164, v165\n"
"v_cvt_pk_bf16_f32 v173, v166, v167\n"
"s_nop 5\n"
"v_cvt_pk_bf16_f32 v174, v168, v169\n"
"v_cvt_pk_bf16_f32 v175, v170, v171\n"
"s_add_u32 s31, s50, 0xffe00000\n"
"ds_bpermute_b32 v52, v21, v172\n"
"ds_bpermute_b32 v53, v21, v173\n"
"s_waitcnt vmcnt(4)\n"
"buffer_store_dwordx2 v[0:1], v14, s[40:43], s91 offen offset:0 sc1\n"
"s_cmp_lt_u32 s31, 0x8000000\n"
"s_cselect_b32 s31, 0x80000000, s31\n"
"ds_bpermute_b32 v54, v21, v174\n"
"v_mov_b32 v68, v30\n"
"v_mov_b32 v69, v31\n"
"buffer_load_dwordx2 v[144:145], v15, s[40:43], s31 offen offset:0 sc1\n"
"ds_bpermute_b32 v55, v21, v175\n"
"v_mov_b32 v90, v32\n"
"v_mov_b32 v91, v33\n"
"s_waitcnt vmcnt(2)\n"
"v_mfma_f32_16x16x32_bf16 v[56:59], v[38:41], v[68:71], 0\n"
"v_mfma_f32_16x16x32_bf16 v[56:59], v[38:41], v[76:79], v[56:59]\n"
"v_mfma_f32_16x16x32_bf16 v[56:59], v[38:41], v[84:87], v[56:59]\n"
"v_mfma_f32_16x16x32_bf16 v[60:63], v[38:41], v[72:75], 0\n"
"v_mfma_f32_16x16x32_bf16 v[60:63], v[38:41], v[80:83], v[60:63]\n"
"v_mfma_f32_16x16x32_bf16 v[60:63], v[38:41], v[88:91], v[60:63]\n"
"v_cvt_pk_bf16_f32 v64, v56, v57\n"
"v_cvt_pk_bf16_f32 v65, v58, v59\n"
"s_nop 5\n"
"v_cvt_pk_bf16_f32 v66, v60, v61\n"
"v_cvt_pk_bf16_f32 v67, v62, v63\n"
"s_waitcnt lgkmcnt(0)\n"
"buffer_store_dwordx4 v[52:55], v18, s[76:79], s28 offen sc1\n"
"ds_bpermute_b32 v44, v21, v64\n"
"ds_bpermute_b32 v45, v21, v65\n"
"ds_bpermute_b32 v46, v21, v66\n"
"ds_bpermute_b32 v47, v21, v67\n"
"s_waitcnt lgkmcnt(0)\n"
"buffer_store_dwordx4 v[44:47], v19, s[76:79], s28 offen sc1\n"
"s_waitcnt vmcnt(2)\n"
"s_and_b32 s29, s31, 0x3ffffff\n"
"s_add_u32 s62, s40, s29\n"
"v_cmp_ne_u64 vcc, s[84:85], v[144:145]\n"
"s_addc_u32 s63, s41, 0\n"
"s_cbranch_vccnz .LJ_4t2k0s\n"
".LJ_4t2k0r:\n"
"global_load_dwordx4 v[46:49], v16, s[62:63] offset:-4096 sc1\n"
"global_load_dwordx4 v[50:53], v16, s[62:63] offset:-3072 sc1\n"
"global_load_dwordx4 v[54:57], v16, s[62:63] offset:-2048 sc1\n"
"global_load_dwordx4 v[58:61], v16, s[62:63] offset:-1024 sc1\n"
"global_load_dwordx4 v[62:65], v17, s[62:63] offset:0 sc1\n"
"global_load_dwordx4 v[70:73], v16, s[62:63] offset:0 sc1\n"
"s_mov_b32 s90, s31\n"
"global_load_dwordx4 v[74:77], v16, s[62:63] offset:1024 sc1\n"
"v_mov_b32 v44, v26\n"
"v_mov_b32 v45, v27\n"
"v_mov_b32 v66, v28\n"
"v_mov_b32 v67, v29\n"
"global_load_dwordx4 v[78:81], v16, s[62:63] offset:2048 sc1\n"
"global_load_dwordx4 v[82:85], v16, s[62:63] offset:3072 sc1\n"
"global_load_dwordx4 v[86:89], v17, s[62:63] offset:1024 sc1\n"
"s_waitcnt vmcnt(5)\n"
"v_mfma_f32_16x16x32_bf16 v[164:167], v[38:41], v[44:47], 0\n"
"s_cmp_ge_i32 s90, s80\n"
"s_cbranch_scc1 .LJ_wt2k0s\n"
".LJ_wt2k0r:\n"
"v_mfma_f32_16x16x32_bf16 v[164:167], v[38:41], v[52:55], v[164:167]\n"
"s_lshr_b32 s28, s90, s81\n"
"v_mfma_f32_16x16x32_bf16 v[164:167], v[38:41], v[60:63], v[164:167]\n"
"s_bitcmp1_b32 s28, 15\n"
"s_cselect_b32 s31, s83, s82\n"
"v_mfma_f32_16x16x32_bf16 v[168:171], v[38:41], v[48:51], 0\n"
"s_add_u32 s28, s28, s31\n"
"v_mfma_f32_16x16x32_bf16 v[168:171], v[38:41], v[56:59], v[168:171]\n"
"v_mfma_f32_16x16x32_bf16 v[168:171], v[38:41], v[64:67], v[168:171]\n"
"v_cvt_pk_bf16_f32 v172, v164, v165\n"
"v_cvt_pk_bf16_f32 v173, v166, v167\n"
"s_nop 5\n"
"v_cvt_pk_bf16_f32 v174, v168, v169\n"
"v_cvt_pk_bf16_f32 v175, v170, v171\n"
"ds_bpermute_b32 v52, v21, v172\n"
"ds_bpermute_b32 v53, v21, v173\n"
"ds_bpermute_b32 v54, v21, v174\n"
"v_mov_b32 v68, v30\n"
"v_mov_b32 v69, v31\n"
"ds_bpermute_b32 v55, v21, v175\n"
"v_mov_b32 v90, v32\n"
"v_mov_b32 v91, v33\n"
"s_waitcnt vmcnt(0)\n"
"v_mfma_f32_16x16x32_bf16 v[56:59], v[38:41], v[68:71], 0\n"
"v_mfma_f32_16x16x32_bf16 v[56:59], v[38:41], v[76:79], v[56:59]\n"
"v_mfma_f32_16x16x32_bf16 v[56:59], v[38:41], v[84:87], v[56:59]\n"
"v_mfma_f32_16x16x32_bf16 v[60:63], v[38:41], v[72:75], 0\n"
"v_mfma_f32_16x16x32_bf16 v[60:63], v[38:41], v[80:83], v[60:63]\n"
"v_mfma_f32_16x16x32_bf16 v[60:63], v[38:41], v[88:91], v[60:63]\n"
"v_cvt_pk_bf16_f32 v64, v56, v57\n"
"v_cvt_pk_bf16_f32 v65, v58, v59\n"
"s_nop 5\n"
"v_cvt_pk_bf16_f32 v66, v60, v61\n"
"v_cvt_pk_bf16_f32 v67, v62, v63\n"
"s_waitcnt lgkmcnt(0)\n"
"buffer_store_dwordx4 v[52:55], v18, s[76:79], s28 offen sc1\n"
"ds_bpermute_b32 v44, v21, v64\n"
"ds_bpermute_b32 v45, v21, v65\n"
"ds_bpermute_b32 v46, v21, v66\n"
"ds_bpermute_b32 v47, v21, v67\n"
"s_waitcnt lgkmcnt(0)\n"
"buffer_store_dwordx4 v[44:47], v19, s[76:79], s28 offen sc1\n"
"s_branch .LJ_end\n"
".LJ_end:\n"
"s_cmp_lt_u32 s50, s53\n"
"s_cbranch_scc1 .LJ_ne\n"
"s_cmp_eq_u64 s[72:73], 0\n"
"s_cbranch_scc1 .LJ_ne\n"
"v_mov_b32 v12, 0\n"
"global_store_dwordx2 v12, v[0:1], s[72:73] sc1\n"
".LJ_ne:\n"
"s_waitcnt vmcnt(0) lgkmcnt(0)\n"
"s_endpgm\n"
      :: "{v2}"((unsigned)(lds0 + ((wid + 3) & 3) * 18592 + trp)), "{v3}"((unsigned)(lds0 + ((wid + 2) & 3) * 18592 + trp)), "{v4}"((unsigned)(lds0 + ((wid + 1) & 3) * 18592 + trp)), "{v5}"((unsigned)(lane * 16 + f0 * 1024 + ((0 + rot) % 7) * 1024 + 134316032)), "{v6}"((unsigned)(lane * 16 + f0 * 1024 + ((1 + rot) % 7) * 1024 + 134316032)), "{v7}"((unsigned)(lane * 16 + f0 * 1024 + ((2 + rot) % 7) * 1024 + 134316032)), "{v8}"((unsigned)(lane * 16 + f0 * 1024 + ((3 + rot) % 7) * 1024 + 134316032)), "{v9}"((unsigned)(lane * 16 + f0 * 1024 + ((4 + rot) % 7) * 1024 + 134316032)), "{v10}"((unsigned)(lane * 16 + f0 * 1024 + ((5 + rot) % 7) * 1024 + 134316032)), "{v11}"((unsigned)(lane * 16 + f0 * 1024 + ((6 + rot) % 7) * 1024 + 134316032)), "{v12}"((unsigned)((f0 + lane % 7) * 8 + kq * 32 + 353024 + J_GL * 1572864)), "{v13}"((unsigned)(lane * 16 + pair * 4096 + kq * 1024 + 212992)), "{v14}"((unsigned)(lane ? DROP : pair * 64 + kq * 8 + 354304)), "{v15}"((unsigned)((v16 * 2 + ((lane >> 2) & 1)) * 64 + (lane & 3) * 8 + 354304)), "{v16}"((unsigned)(lane * 16 + v16 * 8192 + 134434816)), "{v17}"((unsigned)(lane * 16 + v16 * 2048 + 134283264)), "{v18}"((unsigned)(lane * 16 + v16 * 2048 + 0)), "{v19}"((unsigned)(lane * 16 + v16 * 2048 + 1024)), "{v20}"((unsigned)(((lane >> 4) + (lane & 12)) * 4096 + (lane & 3) * 16 + v16 * 128)), "{v21}"((unsigned)(lane * 4)), "{v22}"((unsigned)((lane & 3) * 64 + (lane & 60))), "{v23}"((unsigned)(lg == (li >> 2) ? v16 * 128 + li * 8 + 67457024 : DROP)), "{v24}"((unsigned)(lane ? DROP : v16 * 8 + 67462912)), "{v26}"((unsigned)(bia0[0])), "{v27}"((unsigned)(bia0[1])), "{v28}"((unsigned)(bia0[2])), "{v29}"((unsigned)(bia0[3])), "{v30}"((unsigned)(bia1[0])), "{v31}"((unsigned)(bia1[1])), "{v32}"((unsigned)(bia1[2])), "{v33}"((unsigned)(bia1[3])), "{v34}"((unsigned)(0x3f803f80)), "{v35}"((unsigned)(0x3f803f80)), "{v36}"((unsigned)(0x3f803f80)), "{v37}"((unsigned)(0x3f803f80)), "{v38}"((unsigned)(sel0)), "{v39}"((unsigned)(sel1)), "{v40}"((unsigned)(sel0)), "{v41}"((unsigned)(sel1)), "{v44}"((unsigned)(vT + tsw)), "{v45}"((unsigned)(vT + (tsw ^ 8))), "{v46}"((unsigned)(vD + ((0 + rot) % 7) * 65536)), "{v47}"((unsigned)(vD + ((1 + rot) % 7) * 65536)), "{v48}"((unsigned)(vD + ((2 + rot) % 7) * 65536)), "{v49}"((unsigned)(vD + ((3 + rot) % 7) * 65536)), "{v50}"((unsigned)(vD + ((4 + rot) % 7) * 65536)), "{v51}"((unsigned)(vD + ((5 + rot) % 7) * 65536)), "{v52}"((unsigned)(vD + ((6 + rot) % 7) * 65536)), "{s[40:43]}"(rS), "{s[44:47]}"(rW), "{s[48:49]}"(wsp), "{s50}"((unsigned)(sA)), "{s51}"((unsigned)(sLo)), "{s52}"((unsigned)(sHi)), "{s53}"((unsigned)(sEx)), "{s54}"((unsigned)(sEnt)), "{s55}"((unsigned)(sLd)), "{s[56:57]}"(wsb + (sLo & 0x3ffffff)), "{s59}"((unsigned)(lds0 + wid * 18592)), "{s[62:63]}"(wsb), "{s[64:65]}"(0xAAAAAAAAAAAAAAAAull), "{s[66:67]}"(0xCCCCCCCCCCCCCCCCull), "{s[68:71]}"(rO), "{s[72:73]}"(ep), "{s[76:79]}"(rS), "{s80}"((unsigned)(201326592)), "{s81}"((unsigned)(0)), "{s82}"((unsigned)(0)), "{s83}"((unsigned)(0)), "{s86}"((unsigned)(sW0 + 64 * ((wid + 1) & 3))), "{s87}"((unsigned)(sW0 + 64 * ((wid + 2) & 3))), "{s88}"((unsigned)(sW0 + 64 * ((wid + 3) & 3))), "{s89}"((unsigned)(sW0 + 64 * ((wid + 4) & 3)))
      : "v255", "a255", "s95", "vcc", "scc", "memory");

    }
  }
}

extern "C" void kernel_launch(void* const* d_in, const int* in_sizes, int n_in, void* d_out, int out_size,
                              void* d_ws, size_t ws_size, hipStream_t stream) {
  Args m{};
  m.x = (const u16*)d_in[0]; m.wqkv = (const u16*)d_in[1]; m.wo = (const u16*)d_in[2]; m.w1 = (const u16*)d_in[4];
  m.b1 = (const u16*)d_in[5]; m.w2 = (const u16*)d_in[6]; m.b2 = (const u16*)d_in[7];
  m.out = (u16*)d_out; m.ws = (char*)d_ws;
  (void)hipFuncSetAttribute((const void*)stream_kernel, hipFuncAttributeMaxDynamicSharedMemorySize, LDS_BYTES);
  hipLaunchKernelGGL(stream_kernel, dim3(256), dim3(256), LDS_BYTES, stream, m);
}
```
